```python
import torch

def _softmax(x, axis=-1):
    return torch.softmax(x, dim=axis)

def _gelu(x):
    return torch.nn.functional.gelu(x, approximate='tanh')

def _silu(x):
    return torch.nn.functional.silu(x)

def _sigmoid(x):
    return torch.sigmoid(x)

def _layernorm(x, axis=-1, eps=1e-05):
    m = x.mean(dim=axis, keepdim=True)
    v = x.var(dim=axis, keepdim=True, unbiased=False)
    return (x - m) / torch.sqrt(v + eps)

def _scatter_add(tgt, idx, src, axis=0):
    return tgt.clone().index_add_(axis, idx, src)

def _segment_sum(src, seg, n):
    out = torch.zeros((n,) + src.shape[1:], dtype=src.dtype)
    return out.index_add_(0, seg, src)

def _pad(x, pw):
    flat = tuple((v for pair in reversed(pw) for v in pair))
    return torch.nn.functional.pad(x, flat)
kernel_name = 'concat_heads__memory_aug_recurrent_mem_tr__ssm_conv_s4d__prompt'

def setup_inputs():

    _w = lambda a: (lambda t: t.to(torch.bfloat16) if t.is_floating_point() else t)(torch.as_tensor(a))

    def _A_setup():
        B, S, Dm = (1, 2048, 576)
        H, Dh = (32, 128)
        M, seg = (8, 32)
        seg = min(seg, S)
        S = S // seg * seg
        return {'x': _w((lambda *s: torch.randn(*s, dtype=torch.bfloat16) * 0.05)(B, S, Dm)), 'mem0': _w((lambda *s: torch.randn(*s, dtype=torch.bfloat16) * 0.4)(B, M, Dm)), 'Wq': _w((lambda *s: torch.randn(*s, dtype=torch.bfloat16) * 6.4)(Dm, H, Dh) * Dm ** (-0.5)), 'Wk': _w((lambda *s: torch.randn(*s, dtype=torch.bfloat16) * 6.4)(Dm, H, Dh) * Dm ** (-0.5)), 'Wv': _w((lambda *s: torch.randn(*s, dtype=torch.bfloat16) * 0.4)(Dm, H, Dh) * Dm ** (-0.5)), 'Wo': _w((lambda *s: torch.randn(*s, dtype=torch.bfloat16) * 0.4)(H, Dh, Dm) * (H * Dh) ** (-0.5)), 'seg_len': seg}
    _A_in = _A_setup()

    def _B_setup():
        Bsz, L, D, N = (1, 2048, 576, 128)
        return {'u': _w((lambda *s: torch.randn(*s, dtype=torch.bfloat16) * 0.05)(Bsz, L, D)), 'log_A_real': _w((lambda *s: torch.randn(*s, dtype=torch.bfloat16) * 3.2)(D, N) * 0.5), 'A_imag': _w((lambda *s: torch.randn(*s, dtype=torch.bfloat16) * 0.05)(D, N) * 3.14), 'C_real': _w((lambda *s: torch.randn(*s, dtype=torch.bfloat16) * 0.4)(D, N) / N ** 0.5), 'C_imag': _w((lambda *s: torch.randn(*s, dtype=torch.bfloat16) * 0.4)(D, N) / N ** 0.5), 'log_dt': _w((lambda *s: torch.randn(*s, dtype=torch.bfloat16) * 0.05)(D) * 0.5 - 4.0), 'D_skip': _w((lambda *s: torch.randn(*s, dtype=torch.bfloat16) * 0.05)(D))}
    _B_in = _B_setup()
    return {'A_x': _A_in['x'], 'A_mem0': _A_in['mem0'], 'A_Wq': _A_in['Wq'], 'A_Wk': _A_in['Wk'], 'A_Wv': _A_in['Wv'], 'A_Wo': _A_in['Wo'], 'A_seg_len': _A_in['seg_len'], 'B_u': _B_in['u'], 'B_log_A_real': _B_in['log_A_real'], 'B_A_imag': _B_in['A_imag'], 'B_C_real': _B_in['C_real'], 'B_C_imag': _B_in['C_imag'], 'B_log_dt': _B_in['log_dt'], 'B_D_skip': _B_in['D_skip']}

def reference(A_x, A_mem0, A_Wq, A_Wk, A_Wv, A_Wo, A_seg_len, B_u, B_log_A_real, B_A_imag, B_C_real, B_C_imag, B_log_dt, B_D_skip):

    def _A_ref(x, mem0, Wq, Wk, Wv, Wo, seg_len):
        B, S, Dm = x.shape
        H, Dh = (Wq.shape[1], Wq.shape[2])
        M = mem0.shape[1]
        scale = Dh ** (-0.5)
        n_seg = S // seg_len
        L = M + seg_len
        idx = torch.arange(L)
        mask = (idx[:, None] < M) | (idx[:, None] >= idx[None, :])
        mem = mem0
        outs = []
        for s in range(n_seg):
            xs = x[:, s * seg_len:(s + 1) * seg_len, :].clone()
            h = torch.concatenate([mem, xs], axis=1)
            q = torch.einsum('btd,dhe->bhte', h, Wq)
            k = torch.einsum('btd,dhe->bhte', h, Wk)
            v = torch.einsum('btd,dhe->bhte', h, Wv)
            scores = torch.einsum('bhqe,bhke->bhqk', q, k) * scale
            scores = torch.where(mask[None, None], scores, -1000000000.0)
            p = _softmax(scores, axis=-1)
            o = torch.einsum('bhqk,bhke->bhqe', p, v)
            o = torch.einsum('bhte,hed->btd', o, Wo)
            mem = o[:, :M, :].clone()
            outs.append(o[:, M:, :])
        return torch.concatenate(outs, axis=1)

    def _B_ref(u, log_A_real, A_imag, C_real, C_imag, log_dt, D_skip):
        L = u.shape[1]
        dt = torch.exp(log_dt)
        A = -torch.exp(log_A_real) + 1j * A_imag
        C = (C_real + 1j * C_imag) * dt[:, None]
        pos = (lambda x, dt: x.to(dt))(torch.arange(L), log_dt.dtype)
        P = torch.exp((A * dt[:, None])[:, :, None] * pos[None, None, :])
        K = 2.0 * torch.einsum('dn,dnl->dl', C, P).real
        L2 = 2 * L
        u_t = (lambda x, *d: x.permute(*d))(u, 0, 2, 1)
        Kf = (lambda x, *a, **k: torch.fft.rfft(x.float(), *a, **k))(K, L2)
        Uf = (lambda x, *a, **k: torch.fft.rfft(x.float(), *a, **k))(u_t, L2)
        y = (lambda x, *a, **k: torch.fft.irfft(x, *a, **k).to(torch.bfloat16))(Uf * Kf[None, :, :], L2)[:, :, :L]
        y = (lambda x, *d: x.permute(*d))(y, 0, 2, 1)
        return y + u * D_skip[None, None, :]
    a = _A_ref(A_x, A_mem0, A_Wq, A_Wk, A_Wv, A_Wo, A_seg_len)
    b = _B_ref(B_u, B_log_A_real, B_A_imag, B_C_real, B_C_imag, B_log_dt, B_D_skip)
    return torch.concatenate([a, b], axis=-1)
```

```cpp
#include <hip/hip_runtime.h>
#include <hip/hip_bf16.h>
#include <stdint.h>
#include <stddef.h>

typedef __attribute__((ext_vector_type(8))) __bf16 bf16x8;
typedef __attribute__((ext_vector_type(4))) short s16x4;
typedef __attribute__((ext_vector_type(4))) float f32x4;
typedef __attribute__((ext_vector_type(4))) unsigned u32x4;
typedef __attribute__((ext_vector_type(2))) unsigned u32x2;
typedef __attribute__((ext_vector_type(2))) float f32x2;
typedef __attribute__((ext_vector_type(16))) unsigned u32x16;
typedef unsigned short u16;
typedef unsigned long long u64;

#define GAS __attribute__((address_space(1)))
typedef GAS unsigned gu32;
#define RLX_AGENT __ATOMIC_RELAXED, __HIP_MEMORY_SCOPE_AGENT

static constexpr int DM = 576, NH = 32, DH = 128, MM = 8, SEG = 32, NSEG = 64, SL = 2048;
static constexpr int HD = NH * DH;
static constexpr int OUTW = 2 * DM;
static constexpr int NST = 128;

__device__ __forceinline__ float bf2f(u16 h) { return __uint_as_float(((unsigned)h) << 16); }
typedef __attribute__((ext_vector_type(2))) __bf16 bf16x2_t;
__device__ __forceinline__ unsigned pk2bf(float a, float b) {
    bf16x2_t v; v[0] = (__bf16)a; v[1] = (__bf16)b;
    return __builtin_bit_cast(unsigned, v);
}
__device__ __forceinline__ float bfr(float f) { return __uint_as_float(pk2bf(f, f) << 16); }
__device__ __forceinline__ float lo2f(unsigned u) { return __uint_as_float(u << 16); }
__device__ __forceinline__ float hi2f(unsigned u) { return __uint_as_float(u & 0xffff0000u); }
__device__ __forceinline__ float rowmax4(float x) {
    auto a = __builtin_amdgcn_permlane16_swap(__float_as_uint(x), __float_as_uint(x), false, false);
    x = fmaxf(__uint_as_float(a[0]), __uint_as_float(a[1]));
    auto b = __builtin_amdgcn_permlane32_swap(__float_as_uint(x), __float_as_uint(x), false, false);
    return fmaxf(__uint_as_float(b[0]), __uint_as_float(b[1]));
}
__device__ __forceinline__ float rowsum4(float x) {
    auto a = __builtin_amdgcn_permlane16_swap(__float_as_uint(x), __float_as_uint(x), false, false);
    x = __uint_as_float(a[0]) + __uint_as_float(a[1]);
    auto b = __builtin_amdgcn_permlane32_swap(__float_as_uint(x), __float_as_uint(x), false, false);
    return __uint_as_float(b[0]) + __uint_as_float(b[1]);
}

struct MainP {
    int prm[8];
    const u16* mem0; const u16* W[4]; const u16* X; const u16* U;
    char* ws;
    const u16* logA; const u16* Aim; const u16* Cre; const u16* Cim; const u16* logdt; const u16* Dskip;
    u16* out;
};
static constexpr int LDS_TOTAL = 163840;
static constexpr unsigned kWaitCap = 1u << 18;
#define VMC0() asm volatile("s_waitcnt vmcnt(0)" ::: "memory")
#define PRM_DEFAULT 1150, 600, 144, 672, 800, 1619792095, 4194480, 683672257
#define ASM_QK \
    "s_load_dwordx2 s[42:43], %3, 48\n\t" \
    "s_load_dwordx2 s[44:45], %3, 40\n\t" \
    "s_load_dwordx2 s[36:37], %3, 0x58\n\t" \
    "s_load_dwordx2 s[50:51], %3, 0x48\n\t" \
    "s_load_dwordx8 s[84:91], %3, 0\n\t" \
    "s_mov_b32 s66, %4\n\t" \
    "s_mov_b32 s67, %5\n\t" \
    "s_mov_b32 s68, 262144\n\t" \
    "s_mov_b32 s72, 0\n\t" \
    "s_mov_b32 s83, -1\n\t" \
    "s_mov_b32 s65, 0\n\t" \
    "s_lshl_b32 s82, s66, 10\n\t" \
    "s_waitcnt lgkmcnt(0)\n\t" \
    "s_add_u32 s46, s42, 2097152\n\t" \
    "s_addc_u32 s47, s43, 0\n\t" \
    "s_add_u32 m0, s82, 65536\n\t" \
    "s_nop 0\n\t" \
    "global_load_lds_dwordx4 v200, s[46:47]\n\t" \
    "s_add_u32 s46, s42, 2228224\n\t" \
    "s_addc_u32 s47, s43, 0\n\t" \
    "s_add_u32 m0, s82, 69632\n\t" \
    "s_nop 0\n\t" \
    "global_load_lds_dwordx4 v200, s[46:47]\n\t" \
    "s_add_u32 s46, s42, 2359296\n\t" \
    "s_addc_u32 s47, s43, 0\n\t" \
    "s_add_u32 m0, s82, 73728\n\t" \
    "s_nop 0\n\t" \
    "global_load_lds_dwordx4 v200, s[46:47]\n\t" \
    "s_add_u32 s46, s42, 2490368\n\t" \
    "s_addc_u32 s47, s43, 0\n\t" \
    "s_add_u32 m0, s82, 77824\n\t" \
    "s_nop 0\n\t" \
    "global_load_lds_dwordx4 v200, s[46:47]\n\t" \
    "s_add_u32 s46, s42, 2621440\n\t" \
    "s_addc_u32 s47, s43, 0\n\t" \
    "s_add_u32 m0, s82, 81920\n\t" \
    "s_nop 0\n\t" \
    "global_load_lds_dwordx4 v200, s[46:47]\n\t" \
    "s_add_u32 s46, s42, 2752512\n\t" \
    "s_addc_u32 s47, s43, 0\n\t" \
    "s_add_u32 m0, s82, 86016\n\t" \
    "s_nop 0\n\t" \
    "global_load_lds_dwordx4 v200, s[46:47]\n\t" \
    "s_add_u32 s46, s42, 2883584\n\t" \
    "s_addc_u32 s47, s43, 0\n\t" \
    "s_add_u32 m0, s82, 90112\n\t" \
    "s_nop 0\n\t" \
    "global_load_lds_dwordx4 v200, s[46:47]\n\t" \
    "s_add_u32 s46, s42, 3014656\n\t" \
    "s_addc_u32 s47, s43, 0\n\t" \
    "s_add_u32 m0, s82, 94208\n\t" \
    "s_nop 0\n\t" \
    "global_load_lds_dwordx4 v200, s[46:47]\n\t" \
    "s_add_u32 s46, s42, 3145728\n\t" \
    "s_addc_u32 s47, s43, 0\n\t" \
    "s_add_u32 m0, s82, 98304\n\t" \
    "s_nop 0\n\t" \
    "global_load_lds_dwordx4 v200, s[46:47]\n\t" \
    "s_add_u32 s46, s42, 3276800\n\t" \
    "s_addc_u32 s47, s43, 0\n\t" \
    "s_add_u32 m0, s82, 102400\n\t" \
    "s_nop 0\n\t" \
    "global_load_lds_dwordx4 v200, s[46:47]\n\t" \
    "s_add_u32 s46, s42, 3407872\n\t" \
    "s_addc_u32 s47, s43, 0\n\t" \
    "s_add_u32 m0, s82, 106496\n\t" \
    "s_nop 0\n\t" \
    "global_load_lds_dwordx4 v200, s[46:47]\n\t" \
    "s_add_u32 s46, s42, 3538944\n\t" \
    "s_addc_u32 s47, s43, 0\n\t" \
    "s_add_u32 m0, s82, 110592\n\t" \
    "s_nop 0\n\t" \
    "global_load_lds_dwordx4 v200, s[46:47]\n\t" \
    "s_add_u32 s46, s42, 3670016\n\t" \
    "s_addc_u32 s47, s43, 0\n\t" \
    "s_add_u32 m0, s82, 114688\n\t" \
    "s_nop 0\n\t" \
    "global_load_lds_dwordx4 v200, s[46:47]\n\t" \
    "s_add_u32 s46, s42, 3801088\n\t" \
    "s_addc_u32 s47, s43, 0\n\t" \
    "s_add_u32 m0, s82, 118784\n\t" \
    "s_nop 0\n\t" \
    "global_load_lds_dwordx4 v200, s[46:47]\n\t" \
    "s_add_u32 s46, s42, 3932160\n\t" \
    "s_addc_u32 s47, s43, 0\n\t" \
    "s_add_u32 m0, s82, 122880\n\t" \
    "s_nop 0\n\t" \
    "global_load_lds_dwordx4 v200, s[46:47]\n\t" \
    "s_add_u32 s46, s42, 4063232\n\t" \
    "s_addc_u32 s47, s43, 0\n\t" \
    "s_add_u32 m0, s82, 126976\n\t" \
    "s_nop 0\n\t" \
    "global_load_lds_dwordx4 v200, s[46:47]\n\t" \
    "s_add_u32 s46, s42, 4194304\n\t" \
    "s_addc_u32 s47, s43, 0\n\t" \
    "s_add_u32 m0, s82, 131072\n\t" \
    "s_nop 0\n\t" \
    "global_load_lds_dwordx4 v200, s[46:47]\n\t" \
    "s_add_u32 s46, s42, 4325376\n\t" \
    "s_addc_u32 s47, s43, 0\n\t" \
    "s_add_u32 m0, s82, 135168\n\t" \
    "s_nop 0\n\t" \
    "global_load_lds_dwordx4 v200, s[46:47]\n\t" \
    "s_add_u32 s46, s42, 4456448\n\t" \
    "s_addc_u32 s47, s43, 0\n\t" \
    "s_add_u32 m0, s82, 139264\n\t" \
    "s_nop 0\n\t" \
    "global_load_lds_dwordx4 v200, s[46:47]\n\t" \
    "s_add_u32 s46, s42, 4587520\n\t" \
    "s_addc_u32 s47, s43, 0\n\t" \
    "s_add_u32 m0, s82, 143360\n\t" \
    "s_nop 0\n\t" \
    "global_load_lds_dwordx4 v200, s[46:47]\n\t" \
    "s_mov_b32 s77, 3019898880\n\t" \
    "s_mov_b32 s78, 3054198966\n\t" \
    "s_waitcnt lgkmcnt(0)\n\t" \
    "s_mov_b32 s70, 116\n\t" \
    "qk_E1_%=:\n\t" \
    "s_memtime s[56:57]\n\t" \
    "s_memrealtime s[2:3]\n\t" \
    "s_nop 0\n\t" \
    "s_memrealtime s[4:5]\n\t" \
    "s_nop 0\n\t" \
    "s_memrealtime s[6:7]\n\t" \
    "s_nop 0\n\t" \
    "s_memrealtime s[8:9]\n\t" \
    "s_nop 0\n\t" \
    "s_memrealtime s[10:11]\n\t" \
    "s_nop 0\n\t" \
    "s_memrealtime s[12:13]\n\t" \
    "s_nop 0\n\t" \
    "s_memrealtime s[14:15]\n\t" \
    "s_nop 0\n\t" \
    "s_memrealtime s[16:17]\n\t" \
    "s_nop 0\n\t" \
    "s_memrealtime s[18:19]\n\t" \
    "s_nop 0\n\t" \
    "s_memrealtime s[20:21]\n\t" \
    "s_nop 0\n\t" \
    "s_memrealtime s[22:23]\n\t" \
    "s_nop 0\n\t" \
    "s_memrealtime s[24:25]\n\t" \
    "s_nop 0\n\t" \
    "s_memrealtime s[26:27]\n\t" \
    "s_nop 0\n\t" \
    "s_memrealtime s[28:29]\n\t" \
    "s_memtime s[54:55]\n\t" \
    "s_add_u32 s70, s70, 8\n\t" \
    "s_waitcnt lgkmcnt(0)\n\t" \
    "s_sub_u32 s69, s54, s56\n\t" \
    "s_cmp_gt_u32 s69, s70\n\t" \
    "s_cbranch_scc1 qk_E1_%=\n\t" \
    "s_add_u32 s2, s2, s4\n\t" \
    "s_add_u32 s6, s6, s8\n\t" \
    "s_add_u32 s10, s10, s12\n\t" \
    "s_add_u32 s14, s14, s16\n\t" \
    "s_add_u32 s18, s18, s20\n\t" \
    "s_add_u32 s22, s22, s24\n\t" \
    "s_add_u32 s2, s2, s6\n\t" \
    "s_add_u32 s10, s10, s14\n\t" \
    "s_add_u32 s18, s18, s22\n\t" \
    "s_add_u32 s2, s2, s26\n\t" \
    "s_add_u32 s10, s10, s18\n\t" \
    "s_add_u32 s2, s2, s10\n\t" \
    "s_mul_i32 s70, s28, 13\n\t" \
    "s_sub_u32 s70, s70, s2\n\t" \
    "s_min_u32 s70, s70, 52\n\t" \
    "s_mul_i32 s69, s69, s70\n\t" \
    "s_mul_hi_u32 s69, s69, 76695844\n\t" \
    "s_mov_b32 s75, s73\n\t" \
    "s_mov_b32 s76, s74\n\t" \
    "s_add_u32 s73, s56, s69\n\t" \
    "s_lshl_b32 s74, s28, 4\n\t" \
    "s_waitcnt vmcnt(32) lgkmcnt(0)\n\t" \
    "s_barrier\n\t" \
    "ds_read_b64_tr_b16 a[144:145], v201 offset:0\n\t" \
    "ds_read_b64_tr_b16 a[146:147], v201 offset:1024\n\t" \
    "ds_read_b64_tr_b16 a[148:149], v201 offset:8192\n\t" \
    "ds_read_b64_tr_b16 a[150:151], v201 offset:9216\n\t" \
    "ds_read_b64_tr_b16 a[216:217], v202 offset:0\n\t" \
    "ds_read_b64_tr_b16 a[218:219], v202 offset:1024\n\t" \
    "ds_read_b64_tr_b16 a[220:221], v202 offset:8192\n\t" \
    "ds_read_b64_tr_b16 a[222:223], v202 offset:9216\n\t" \
    "s_waitcnt vmcnt(28) lgkmcnt(0)\n\t" \
    "s_barrier\n\t" \
    "ds_read_b64_tr_b16 a[152:153], v201 offset:16384\n\t" \
    "ds_read_b64_tr_b16 a[154:155], v201 offset:17408\n\t" \
    "ds_read_b64_tr_b16 a[156:157], v201 offset:24576\n\t" \
    "ds_read_b64_tr_b16 a[158:159], v201 offset:25600\n\t" \
    "ds_read_b64_tr_b16 a[224:225], v202 offset:16384\n\t" \
    "ds_read_b64_tr_b16 a[226:227], v202 offset:17408\n\t" \
    "ds_read_b64_tr_b16 a[228:229], v202 offset:24576\n\t" \
    "ds_read_b64_tr_b16 a[230:231], v202 offset:25600\n\t" \
    "s_add_u32 s46, s44, 0\n\t" \
    "s_addc_u32 s47, s45, 0\n\t" \
    "s_add_u32 m0, s82, 0\n\t" \
    "s_nop 0\n\t" \
    "global_load_lds_dwordx4 v200, s[46:47]\n\t" \
    "s_add_u32 s46, s44, 131072\n\t" \
    "s_addc_u32 s47, s45, 0\n\t" \
    "s_add_u32 m0, s82, 4096\n\t" \
    "s_nop 0\n\t" \
    "global_load_lds_dwordx4 v200, s[46:47]\n\t" \
    "s_add_u32 s46, s44, 262144\n\t" \
    "s_addc_u32 s47, s45, 0\n\t" \
    "s_add_u32 m0, s82, 8192\n\t" \
    "s_nop 0\n\t" \
    "global_load_lds_dwordx4 v200, s[46:47]\n\t" \
    "s_add_u32 s46, s44, 393216\n\t" \
    "s_addc_u32 s47, s45, 0\n\t" \
    "s_add_u32 m0, s82, 12288\n\t" \
    "s_nop 0\n\t" \
    "global_load_lds_dwordx4 v200, s[46:47]\n\t" \
    "s_waitcnt vmcnt(28) lgkmcnt(0)\n\t" \
    "s_barrier\n\t" \
    "ds_read_b64_tr_b16 a[160:161], v201 offset:32768\n\t" \
    "ds_read_b64_tr_b16 a[162:163], v201 offset:33792\n\t" \
    "ds_read_b64_tr_b16 a[164:165], v201 offset:40960\n\t" \
    "ds_read_b64_tr_b16 a[166:167], v201 offset:41984\n\t" \
    "ds_read_b64_tr_b16 a[232:233], v202 offset:32768\n\t" \
    "ds_read_b64_tr_b16 a[234:235], v202 offset:33792\n\t" \
    "ds_read_b64_tr_b16 a[236:237], v202 offset:40960\n\t" \
    "ds_read_b64_tr_b16 a[238:239], v202 offset:41984\n\t" \
    "s_add_u32 s46, s44, 524288\n\t" \
    "s_addc_u32 s47, s45, 0\n\t" \
    "s_add_u32 m0, s82, 16384\n\t" \
    "s_nop 0\n\t" \
    "global_load_lds_dwordx4 v200, s[46:47]\n\t" \
    "s_add_u32 s46, s44, 655360\n\t" \
    "s_addc_u32 s47, s45, 0\n\t" \
    "s_add_u32 m0, s82, 20480\n\t" \
    "s_nop 0\n\t" \
    "global_load_lds_dwordx4 v200, s[46:47]\n\t" \
    "s_add_u32 s46, s44, 786432\n\t" \
    "s_addc_u32 s47, s45, 0\n\t" \
    "s_add_u32 m0, s82, 24576\n\t" \
    "s_nop 0\n\t" \
    "global_load_lds_dwordx4 v200, s[46:47]\n\t" \
    "s_add_u32 s46, s44, 917504\n\t" \
    "s_addc_u32 s47, s45, 0\n\t" \
    "s_add_u32 m0, s82, 28672\n\t" \
    "s_nop 0\n\t" \
    "global_load_lds_dwordx4 v200, s[46:47]\n\t" \
    "s_waitcnt vmcnt(28) lgkmcnt(0)\n\t" \
    "s_barrier\n\t" \
    "ds_read_b64_tr_b16 a[168:169], v201 offset:49152\n\t" \
    "ds_read_b64_tr_b16 a[170:171], v201 offset:50176\n\t" \
    "ds_read_b64_tr_b16 a[172:173], v201 offset:57344\n\t" \
    "ds_read_b64_tr_b16 a[174:175], v201 offset:58368\n\t" \
    "ds_read_b64_tr_b16 a[240:241], v202 offset:49152\n\t" \
    "ds_read_b64_tr_b16 a[242:243], v202 offset:50176\n\t" \
    "ds_read_b64_tr_b16 a[244:245], v202 offset:57344\n\t" \
    "ds_read_b64_tr_b16 a[246:247], v202 offset:58368\n\t" \
    "s_add_u32 s46, s44, 1048576\n\t" \
    "s_addc_u32 s47, s45, 0\n\t" \
    "s_add_u32 m0, s82, 32768\n\t" \
    "s_nop 0\n\t" \
    "global_load_lds_dwordx4 v200, s[46:47]\n\t" \
    "s_add_u32 s46, s44, 1179648\n\t" \
    "s_addc_u32 s47, s45, 0\n\t" \
    "s_add_u32 m0, s82, 36864\n\t" \
    "s_nop 0\n\t" \
    "global_load_lds_dwordx4 v200, s[46:47]\n\t" \
    "s_add_u32 s46, s44, 1310720\n\t" \
    "s_addc_u32 s47, s45, 0\n\t" \
    "s_add_u32 m0, s82, 40960\n\t" \
    "s_nop 0\n\t" \
    "global_load_lds_dwordx4 v200, s[46:47]\n\t" \
    "s_add_u32 s46, s44, 1441792\n\t" \
    "s_addc_u32 s47, s45, 0\n\t" \
    "s_add_u32 m0, s82, 45056\n\t" \
    "s_nop 0\n\t" \
    "global_load_lds_dwordx4 v200, s[46:47]\n\t" \
    "s_waitcnt vmcnt(28) lgkmcnt(0)\n\t" \
    "s_barrier\n\t" \
    "v_add_u32 v201, 0x10000, v201\n\t" \
    "v_add_u32 v202, 0x10000, v202\n\t" \
    "ds_read_b64_tr_b16 a[176:177], v201 offset:0\n\t" \
    "ds_read_b64_tr_b16 a[178:179], v201 offset:1024\n\t" \
    "ds_read_b64_tr_b16 a[180:181], v201 offset:8192\n\t" \
    "ds_read_b64_tr_b16 a[182:183], v201 offset:9216\n\t" \
    "ds_read_b64_tr_b16 a[248:249], v202 offset:0\n\t" \
    "ds_read_b64_tr_b16 a[250:251], v202 offset:1024\n\t" \
    "ds_read_b64_tr_b16 a[252:253], v202 offset:8192\n\t" \
    "ds_read_b64_tr_b16 a[254:255], v202 offset:9216\n\t" \
    "s_add_u32 s46, s44, 1572864\n\t" \
    "s_addc_u32 s47, s45, 0\n\t" \
    "s_add_u32 m0, s82, 49152\n\t" \
    "s_nop 0\n\t" \
    "global_load_lds_dwordx4 v200, s[46:47]\n\t" \
    "s_add_u32 s46, s44, 1703936\n\t" \
    "s_addc_u32 s47, s45, 0\n\t" \
    "s_add_u32 m0, s82, 53248\n\t" \
    "s_nop 0\n\t" \
    "global_load_lds_dwordx4 v200, s[46:47]\n\t" \
    "s_add_u32 s46, s44, 1835008\n\t" \
    "s_addc_u32 s47, s45, 0\n\t" \
    "s_add_u32 m0, s82, 57344\n\t" \
    "s_nop 0\n\t" \
    "global_load_lds_dwordx4 v200, s[46:47]\n\t" \
    "s_add_u32 s46, s44, 1966080\n\t" \
    "s_addc_u32 s47, s45, 0\n\t" \
    "s_add_u32 m0, s82, 61440\n\t" \
    "s_nop 0\n\t" \
    "global_load_lds_dwordx4 v200, s[46:47]\n\t" \
    "s_waitcnt vmcnt(28) lgkmcnt(0)\n\t" \
    "s_barrier\n\t" \
    "ds_read_b64_tr_b16 a[184:185], v201 offset:16384\n\t" \
    "ds_read_b64_tr_b16 a[186:187], v201 offset:17408\n\t" \
    "ds_read_b64_tr_b16 a[188:189], v201 offset:24576\n\t" \
    "ds_read_b64_tr_b16 a[190:191], v201 offset:25600\n\t" \
    "ds_read_b64_tr_b16 v[224:225], v202 offset:16384\n\t" \
    "ds_read_b64_tr_b16 v[226:227], v202 offset:17408\n\t" \
    "ds_read_b64_tr_b16 v[228:229], v202 offset:24576\n\t" \
    "ds_read_b64_tr_b16 v[230:231], v202 offset:25600\n\t" \
    "s_add_u32 s46, s44, 2097152\n\t" \
    "s_addc_u32 s47, s45, 0\n\t" \
    "s_add_u32 m0, s82, 65536\n\t" \
    "s_nop 0\n\t" \
    "global_load_lds_dwordx4 v200, s[46:47]\n\t" \
    "s_add_u32 s46, s44, 2228224\n\t" \
    "s_addc_u32 s47, s45, 0\n\t" \
    "s_add_u32 m0, s82, 69632\n\t" \
    "s_nop 0\n\t" \
    "global_load_lds_dwordx4 v200, s[46:47]\n\t" \
    "s_add_u32 s46, s44, 2359296\n\t" \
    "s_addc_u32 s47, s45, 0\n\t" \
    "s_add_u32 m0, s82, 73728\n\t" \
    "s_nop 0\n\t" \
    "global_load_lds_dwordx4 v200, s[46:47]\n\t" \
    "s_add_u32 s46, s44, 2490368\n\t" \
    "s_addc_u32 s47, s45, 0\n\t" \
    "s_add_u32 m0, s82, 77824\n\t" \
    "s_nop 0\n\t" \
    "global_load_lds_dwordx4 v200, s[46:47]\n\t" \
    "s_waitcnt vmcnt(28) lgkmcnt(0)\n\t" \
    "s_barrier\n\t" \
    "ds_read_b64_tr_b16 a[192:193], v201 offset:32768\n\t" \
    "ds_read_b64_tr_b16 a[194:195], v201 offset:33792\n\t" \
    "ds_read_b64_tr_b16 a[196:197], v201 offset:40960\n\t" \
    "ds_read_b64_tr_b16 a[198:199], v201 offset:41984\n\t" \
    "ds_read_b64_tr_b16 v[232:233], v202 offset:32768\n\t" \
    "ds_read_b64_tr_b16 v[234:235], v202 offset:33792\n\t" \
    "ds_read_b64_tr_b16 v[236:237], v202 offset:40960\n\t" \
    "ds_read_b64_tr_b16 v[238:239], v202 offset:41984\n\t" \
    "s_add_u32 s46, s44, 2621440\n\t" \
    "s_addc_u32 s47, s45, 0\n\t" \
    "s_add_u32 m0, s82, 81920\n\t" \
    "s_nop 0\n\t" \
    "global_load_lds_dwordx4 v200, s[46:47]\n\t" \
    "s_add_u32 s46, s44, 2752512\n\t" \
    "s_addc_u32 s47, s45, 0\n\t" \
    "s_add_u32 m0, s82, 86016\n\t" \
    "s_nop 0\n\t" \
    "global_load_lds_dwordx4 v200, s[46:47]\n\t" \
    "s_add_u32 s46, s44, 2883584\n\t" \
    "s_addc_u32 s47, s45, 0\n\t" \
    "s_add_u32 m0, s82, 90112\n\t" \
    "s_nop 0\n\t" \
    "global_load_lds_dwordx4 v200, s[46:47]\n\t" \
    "s_add_u32 s46, s44, 3014656\n\t" \
    "s_addc_u32 s47, s45, 0\n\t" \
    "s_add_u32 m0, s82, 94208\n\t" \
    "s_nop 0\n\t" \
    "global_load_lds_dwordx4 v200, s[46:47]\n\t" \
    "s_waitcnt vmcnt(28) lgkmcnt(0)\n\t" \
    "s_barrier\n\t" \
    "ds_read_b64_tr_b16 a[200:201], v201 offset:49152\n\t" \
    "ds_read_b64_tr_b16 a[202:203], v201 offset:50176\n\t" \
    "ds_read_b64_tr_b16 a[204:205], v201 offset:57344\n\t" \
    "ds_read_b64_tr_b16 a[206:207], v201 offset:58368\n\t" \
    "ds_read_b64_tr_b16 v[240:241], v202 offset:49152\n\t" \
    "ds_read_b64_tr_b16 v[242:243], v202 offset:50176\n\t" \
    "ds_read_b64_tr_b16 v[244:245], v202 offset:57344\n\t" \
    "ds_read_b64_tr_b16 v[246:247], v202 offset:58368\n\t" \
    "s_add_u32 s46, s44, 3145728\n\t" \
    "s_addc_u32 s47, s45, 0\n\t" \
    "s_add_u32 m0, s82, 98304\n\t" \
    "s_nop 0\n\t" \
    "global_load_lds_dwordx4 v200, s[46:47]\n\t" \
    "s_add_u32 s46, s44, 3276800\n\t" \
    "s_addc_u32 s47, s45, 0\n\t" \
    "s_add_u32 m0, s82, 102400\n\t" \
    "s_nop 0\n\t" \
    "global_load_lds_dwordx4 v200, s[46:47]\n\t" \
    "s_add_u32 s46, s44, 3407872\n\t" \
    "s_addc_u32 s47, s45, 0\n\t" \
    "s_add_u32 m0, s82, 106496\n\t" \
    "s_nop 0\n\t" \
    "global_load_lds_dwordx4 v200, s[46:47]\n\t" \
    "s_add_u32 s46, s44, 3538944\n\t" \
    "s_addc_u32 s47, s45, 0\n\t" \
    "s_add_u32 m0, s82, 110592\n\t" \
    "s_nop 0\n\t" \
    "global_load_lds_dwordx4 v200, s[46:47]\n\t" \
    "v_mbcnt_lo_u32_b32 v160, -1, 0\n\t" \
    "v_mbcnt_hi_u32_b32 v160, -1, v160\n\t" \
    "v_lshlrev_b32 v160, 6, v160\n\t" \
    "v_add_u32 v160, 6225920, v160\n\t" \
    "v_mov_b32 v161, 6234112\n\t" \
    "global_load_dword v161, v161, s[36:37] sc1\n\t" \
    "global_load_dwordx2 v[162:163], v160, s[36:37] sc1\n\t" \
    "global_load_dword v164, v160, s[36:37] offset:2048 sc1\n\t" \
    "s_waitcnt vmcnt(31) lgkmcnt(0)\n\t" \
    "s_barrier\n\t" \
    "v_add_u32 v201, 0x10000, v201\n\t" \
    "v_add_u32 v202, 0x10000, v202\n\t" \
    "ds_read_b64_tr_b16 a[208:209], v201 offset:0\n\t" \
    "ds_read_b64_tr_b16 a[210:211], v201 offset:1024\n\t" \
    "ds_read_b64_tr_b16 a[212:213], v201 offset:8192\n\t" \
    "ds_read_b64_tr_b16 a[214:215], v201 offset:9216\n\t" \
    "ds_read_b64_tr_b16 v[248:249], v202 offset:0\n\t" \
    "ds_read_b64_tr_b16 v[250:251], v202 offset:1024\n\t" \
    "ds_read_b64_tr_b16 v[252:253], v202 offset:8192\n\t" \
    "ds_read_b64_tr_b16 v[254:255], v202 offset:9216\n\t" \
    "v_add_u32 v201, 0xfffe0000, v201\n\t" \
    "v_add_u32 v202, 0xfffe0000, v202\n\t" \
    "s_add_u32 s46, s44, 3670016\n\t" \
    "s_addc_u32 s47, s45, 0\n\t" \
    "s_add_u32 m0, s82, 114688\n\t" \
    "s_nop 0\n\t" \
    "global_load_lds_dwordx4 v200, s[46:47]\n\t" \
    "s_add_u32 s46, s44, 3801088\n\t" \
    "s_addc_u32 s47, s45, 0\n\t" \
    "s_add_u32 m0, s82, 118784\n\t" \
    "s_nop 0\n\t" \
    "global_load_lds_dwordx4 v200, s[46:47]\n\t" \
    "s_add_u32 s46, s44, 3932160\n\t" \
    "s_addc_u32 s47, s45, 0\n\t" \
    "s_add_u32 m0, s82, 122880\n\t" \
    "s_nop 0\n\t" \
    "global_load_lds_dwordx4 v200, s[46:47]\n\t" \
    "s_add_u32 s46, s44, 4063232\n\t" \
    "s_addc_u32 s47, s45, 0\n\t" \
    "s_add_u32 m0, s82, 126976\n\t" \
    "s_nop 0\n\t" \
    "global_load_lds_dwordx4 v200, s[46:47]\n\t" \
    "s_waitcnt lgkmcnt(0)\n\t" \
    "s_barrier\n\t" \
    "s_add_u32 s46, s44, 4194304\n\t" \
    "s_addc_u32 s47, s45, 0\n\t" \
    "s_add_u32 m0, s82, 131072\n\t" \
    "s_nop 0\n\t" \
    "global_load_lds_dwordx4 v200, s[46:47]\n\t" \
    "s_add_u32 s46, s44, 4325376\n\t" \
    "s_addc_u32 s47, s45, 0\n\t" \
    "s_add_u32 m0, s82, 135168\n\t" \
    "s_nop 0\n\t" \
    "global_load_lds_dwordx4 v200, s[46:47]\n\t" \
    "s_add_u32 s46, s44, 4456448\n\t" \
    "s_addc_u32 s47, s45, 0\n\t" \
    "s_add_u32 m0, s82, 139264\n\t" \
    "s_nop 0\n\t" \
    "global_load_lds_dwordx4 v200, s[46:47]\n\t" \
    "s_add_u32 s46, s44, 4587520\n\t" \
    "s_addc_u32 s47, s45, 0\n\t" \
    "s_add_u32 m0, s82, 143360\n\t" \
    "s_nop 0\n\t" \
    "global_load_lds_dwordx4 v200, s[46:47]\n\t" \
    "s_waitcnt vmcnt(35) lgkmcnt(0)\n\t" \
    "s_barrier\n\t" \
    "ds_read_b64_tr_b16 a[0:1], v201 offset:0\n\t" \
    "ds_read_b64_tr_b16 a[2:3], v201 offset:1024\n\t" \
    "ds_read_b64_tr_b16 a[4:5], v201 offset:8192\n\t" \
    "ds_read_b64_tr_b16 a[6:7], v201 offset:9216\n\t" \
    "ds_read_b64_tr_b16 a[72:73], v202 offset:0\n\t" \
    "ds_read_b64_tr_b16 a[74:75], v202 offset:1024\n\t" \
    "ds_read_b64_tr_b16 a[76:77], v202 offset:8192\n\t" \
    "ds_read_b64_tr_b16 a[78:79], v202 offset:9216\n\t" \
    "s_waitcnt vmcnt(31) lgkmcnt(0)\n\t" \
    "s_barrier\n\t" \
    "ds_read_b64_tr_b16 a[8:9], v201 offset:16384\n\t" \
    "ds_read_b64_tr_b16 a[10:11], v201 offset:17408\n\t" \
    "ds_read_b64_tr_b16 a[12:13], v201 offset:24576\n\t" \
    "ds_read_b64_tr_b16 a[14:15], v201 offset:25600\n\t" \
    "ds_read_b64_tr_b16 a[80:81], v202 offset:16384\n\t" \
    "ds_read_b64_tr_b16 a[82:83], v202 offset:17408\n\t" \
    "ds_read_b64_tr_b16 a[84:85], v202 offset:24576\n\t" \
    "ds_read_b64_tr_b16 a[86:87], v202 offset:25600\n\t" \
    "s_waitcnt vmcnt(27) lgkmcnt(0)\n\t" \
    "s_barrier\n\t" \
    "ds_read_b64_tr_b16 a[16:17], v201 offset:32768\n\t" \
    "ds_read_b64_tr_b16 a[18:19], v201 offset:33792\n\t" \
    "ds_read_b64_tr_b16 a[20:21], v201 offset:40960\n\t" \
    "ds_read_b64_tr_b16 a[22:23], v201 offset:41984\n\t" \
    "ds_read_b64_tr_b16 a[88:89], v202 offset:32768\n\t" \
    "ds_read_b64_tr_b16 a[90:91], v202 offset:33792\n\t" \
    "ds_read_b64_tr_b16 a[92:93], v202 offset:40960\n\t" \
    "ds_read_b64_tr_b16 a[94:95], v202 offset:41984\n\t" \
    "s_waitcnt vmcnt(23) lgkmcnt(0)\n\t" \
    "s_barrier\n\t" \
    "ds_read_b64_tr_b16 a[24:25], v201 offset:49152\n\t" \
    "ds_read_b64_tr_b16 a[26:27], v201 offset:50176\n\t" \
    "ds_read_b64_tr_b16 a[28:29], v201 offset:57344\n\t" \
    "ds_read_b64_tr_b16 a[30:31], v201 offset:58368\n\t" \
    "ds_read_b64_tr_b16 a[96:97], v202 offset:49152\n\t" \
    "ds_read_b64_tr_b16 a[98:99], v202 offset:50176\n\t" \
    "ds_read_b64_tr_b16 a[100:101], v202 offset:57344\n\t" \
    "ds_read_b64_tr_b16 a[102:103], v202 offset:58368\n\t" \
    "s_waitcnt vmcnt(19) lgkmcnt(0)\n\t" \
    "s_barrier\n\t" \
    "v_add_u32 v201, 0x10000, v201\n\t" \
    "v_add_u32 v202, 0x10000, v202\n\t" \
    "ds_read_b64_tr_b16 a[32:33], v201 offset:0\n\t" \
    "ds_read_b64_tr_b16 a[34:35], v201 offset:1024\n\t" \
    "ds_read_b64_tr_b16 a[36:37], v201 offset:8192\n\t" \
    "ds_read_b64_tr_b16 a[38:39], v201 offset:9216\n\t" \
    "ds_read_b64_tr_b16 a[104:105], v202 offset:0\n\t" \
    "ds_read_b64_tr_b16 a[106:107], v202 offset:1024\n\t" \
    "ds_read_b64_tr_b16 a[108:109], v202 offset:8192\n\t" \
    "ds_read_b64_tr_b16 a[110:111], v202 offset:9216\n\t" \
    "s_waitcnt vmcnt(15) lgkmcnt(0)\n\t" \
    "s_barrier\n\t" \
    "ds_read_b64_tr_b16 a[40:41], v201 offset:16384\n\t" \
    "ds_read_b64_tr_b16 a[42:43], v201 offset:17408\n\t" \
    "ds_read_b64_tr_b16 a[44:45], v201 offset:24576\n\t" \
    "ds_read_b64_tr_b16 a[46:47], v201 offset:25600\n\t" \
    "ds_read_b64_tr_b16 a[112:113], v202 offset:16384\n\t" \
    "ds_read_b64_tr_b16 a[114:115], v202 offset:17408\n\t" \
    "ds_read_b64_tr_b16 a[116:117], v202 offset:24576\n\t" \
    "ds_read_b64_tr_b16 a[118:119], v202 offset:25600\n\t" \
    "s_waitcnt vmcnt(11) lgkmcnt(0)\n\t" \
    "s_barrier\n\t" \
    "ds_read_b64_tr_b16 a[48:49], v201 offset:32768\n\t" \
    "ds_read_b64_tr_b16 a[50:51], v201 offset:33792\n\t" \
    "ds_read_b64_tr_b16 a[52:53], v201 offset:40960\n\t" \
    "ds_read_b64_tr_b16 a[54:55], v201 offset:41984\n\t" \
    "ds_read_b64_tr_b16 a[120:121], v202 offset:32768\n\t" \
    "ds_read_b64_tr_b16 a[122:123], v202 offset:33792\n\t" \
    "ds_read_b64_tr_b16 a[124:125], v202 offset:40960\n\t" \
    "ds_read_b64_tr_b16 a[126:127], v202 offset:41984\n\t" \
    "s_waitcnt vmcnt(8)\n\t" \
    "qk_C2_%=:\n\t" \
    "v_cmp_ne_u32 s[54:55], v162, v164\n\t" \
    "v_sub_u32 v162, v162, v161\n\t" \
    "v_cmp_ne_u32 vcc, 1, v162\n\t" \
    "s_nop 1\n\t" \
    "s_or_b64 vcc, vcc, s[54:55]\n\t" \
    "s_cbranch_scc0 qk_Cok3_%=\n\t" \
    "s_cmp_eq_u32 s68, 0\n\t" \
    "s_cbranch_scc1 qk_F5_%=\n\t" \
    "s_sub_u32 s68, s68, 1\n\t" \
    "s_sleep 2\n\t" \
    "s_branch qk_Cag4_%=\n\t" \
    "qk_F5_%=:\n\t" \
    "v_mov_b32 v164, 5767424\n\t" \
    "v_mov_b32 v165, 4\n\t" \
    "global_store_dword v164, v165, s[36:37] sc1\n\t" \
    "s_waitcnt vmcnt(0)\n\t" \
    "s_branch qk_Cok3_%=\n\t" \
    "qk_Cag4_%=:\n\t" \
    "global_load_dwordx2 v[162:163], v160, s[36:37] sc1\n\t" \
    "global_load_dword v164, v160, s[36:37] offset:2048 sc1\n\t" \
    "s_waitcnt vmcnt(0)\n\t" \
    "s_branch qk_C2_%=\n\t" \
    "qk_Cok3_%=:\n\t" \
    "s_cmp_lg_u32 s66, 0\n\t" \
    "s_cbranch_scc1 qk_Ca6_%=\n\t" \
    "v_mov_b32 v160, 5836800\n\t" \
    "s_mov_b64 exec, 1\n\t" \
    "global_atomic_add v160, v162, s[36:37] sc1\n\t" \
    "s_mov_b64 exec, -1\n\t" \
    "qk_Ca6_%=:\n\t" \
    "v_mov_b32_dpp v160, v163 quad_perm:[1,0,3,2] row_mask:0xf bank_mask:0xf\n\t" \
    "v_cmp_lt_u32 s[44:45], v160, v163\n\t" \
    "s_nop 1\n\t" \
    "s_bitcmp1_b32 s44, 0\n\t" \
    "s_cselect_b32 s62, 8192, 0\n\t" \
    "s_bitcmp1_b32 s44, 2\n\t" \
    "s_cselect_b32 s63, 8192, 0\n\t" \
    "s_or_b32 s63, s63, 0x200000\n\t" \
    "v_min_u32 v160, v160, v163\n\t" \
    "v_cmp_lt_u32 vcc, 660, v160\n\t" \
    "s_nop 1\n\t" \
    "s_cmp_lg_u64 vcc, 0\n\t" \
    "s_cselect_b32 s69, 1, 0\n\t" \
    "s_mul_i32 s70, s69, 160\n\t" \
    "s_add_u32 s86, s86, s70\n\t" \
    "s_mul_i32 s70, s69, 536\n\t" \
    "s_add_u32 s87, s87, s70\n\t" \
    "s_mul_i32 s70, s69, 256\n\t" \
    "s_add_u32 s88, s88, s70\n\t" \
    "global_load_dword v25, v24, s[36:37] sc1\n\t" \
    "s_waitcnt vmcnt(5) lgkmcnt(0)\n\t" \
    "s_barrier\n\t" \
    "ds_read_b64_tr_b16 a[56:57], v201 offset:49152\n\t" \
    "ds_read_b64_tr_b16 a[58:59], v201 offset:50176\n\t" \
    "ds_read_b64_tr_b16 a[60:61], v201 offset:57344\n\t" \
    "ds_read_b64_tr_b16 a[62:63], v201 offset:58368\n\t" \
    "ds_read_b64_tr_b16 a[128:129], v202 offset:49152\n\t" \
    "ds_read_b64_tr_b16 a[130:131], v202 offset:50176\n\t" \
    "ds_read_b64_tr_b16 a[132:133], v202 offset:57344\n\t" \
    "ds_read_b64_tr_b16 a[134:135], v202 offset:58368\n\t" \
    "s_waitcnt vmcnt(1) lgkmcnt(0)\n\t" \
    "s_barrier\n\t" \
    "v_add_u32 v201, 0x10000, v201\n\t" \
    "v_add_u32 v202, 0x10000, v202\n\t" \
    "ds_read_b64_tr_b16 a[64:65], v201 offset:0\n\t" \
    "ds_read_b64_tr_b16 a[66:67], v201 offset:1024\n\t" \
    "ds_read_b64_tr_b16 a[68:69], v201 offset:8192\n\t" \
    "ds_read_b64_tr_b16 a[70:71], v201 offset:9216\n\t" \
    "ds_read_b64_tr_b16 a[136:137], v202 offset:0\n\t" \
    "ds_read_b64_tr_b16 a[138:139], v202 offset:1024\n\t" \
    "ds_read_b64_tr_b16 a[140:141], v202 offset:8192\n\t" \
    "ds_read_b64_tr_b16 a[142:143], v202 offset:9216\n\t" \
    "v_add_u32 v201, 0xfffe0000, v201\n\t" \
    "v_add_u32 v202, 0xfffe0000, v202\n\t" \
    "s_waitcnt lgkmcnt(0)\n\t" \
    "s_barrier\n\t" \
    "s_mul_i32 s70, s67, 7\n\t" \
    "s_add_u32 s69, s65, 0\n\t" \
    "s_xor_b32 s69, s69, s70\n\t" \
    "s_and_b32 s69, s69, 63\n\t" \
    "s_lshl_b32 s69, s69, 13\n\t" \
    "s_mul_i32 s70, s67, 524288\n\t" \
    "s_add_u32 s69, s69, s70\n\t" \
    "s_add_u32 s69, s69, 23068672\n\t" \
    "s_add_u32 s38, s36, s69\n\t" \
    "s_addc_u32 s39, s37, 0\n\t" \
    "v_mov_b32 v26, 0x3db504f3\n\t" \
    "v_mov_b32 v28, 0x3fb8aa3b\n\t" \
    "v_mov_b32 v27, 0x3db504f3\n\t" \
    "v_mov_b32 v29, 0x3fb8aa3b\n\t" \
    "v_xor_b32 v180, 65536, v18\n\t" \
    "v_xor_b32 v181, 131072, v18\n\t" \
    "v_xor_b32 v182, 196608, v18\n\t" \
    "s_mov_b32 s48, 0xaaaaaaaa\n\t" \
    "s_mov_b32 s49, 0xaaaaaaaa\n\t" \
    "s_mov_b32 s58, 0x00ff00ff\n\t" \
    "s_mov_b32 s59, 0x00ff00ff\n\t" \
    "s_lshl_b32 s69, s66, 2\n\t" \
    "v_mov_b32 v30, s69\n\t" \
    "v_mov_b32 v31, 0\n\t" \
    "ds_write_b32 v30, v31 offset:20496\n\t" \
    "s_waitcnt lgkmcnt(0)\n\t" \
    "s_waitcnt vmcnt(0)\n\t" \
    "s_cmp_gt_u32 s66, 1\n\t" \
    "s_cbranch_scc1 qk_nk7_%=\n\t" \
    "v_readfirstlane_b32 s83, v25\n\t" \
    "s_nop 1\n\t" \
    "qk_WF8_%=:\n\t" \
    "s_cmp_ge_i32 s83, s65\n\t" \
    "s_cbranch_scc1 qk_WFok9_%=\n\t" \
    "global_load_dword v160, v24, s[36:37] sc1\n\t" \
    "s_waitcnt vmcnt(0)\n\t" \
    "v_readfirstlane_b32 s83, v160\n\t" \
    "s_nop 1\n\t" \
    "s_cmp_ge_i32 s83, s65\n\t" \
    "s_cbranch_scc1 qk_WFok9_%=\n\t" \
    "s_cmp_eq_u32 s68, 0\n\t" \
    "s_cbranch_scc1 qk_F10_%=\n\t" \
    "s_sub_u32 s68, s68, 1\n\t" \
    "s_sleep 2\n\t" \
    "s_branch qk_WF8_%=\n\t" \
    "qk_F10_%=:\n\t" \
    "v_mov_b32 v160, 5767424\n\t" \
    "v_mov_b32 v161, 8\n\t" \
    "global_store_dword v160, v161, s[36:37] sc1\n\t" \
    "s_waitcnt vmcnt(0)\n\t" \
    "s_branch qk_WFok9_%=\n\t" \
    "qk_WFok9_%=:\n\t" \
    "global_load_dwordx4 v[100:103], v0, s[38:39] offset:0 sc1\n\t" \
    "global_load_dwordx4 v[104:107], v0, s[38:39] offset:2048 sc1\n\t" \
    "global_load_dwordx4 v[108:111], v15, s[38:39] offset:0 sc1\n\t" \
    "global_load_dwordx4 v[112:115], v15, s[38:39] offset:2048 sc1\n\t" \
    "global_load_dwordx4 v[116:119], v0, s[38:39] offset:1024 sc1\n\t" \
    "global_load_dwordx4 v[120:123], v0, s[38:39] offset:3072 sc1\n\t" \
    "global_load_dwordx4 v[124:127], v15, s[38:39] offset:1024 sc1\n\t" \
    "global_load_dwordx4 v[128:131], v15, s[38:39] offset:3072 sc1\n\t" \
    "qk_nk7_%=:\n\t" \
    "v_mov_b32 v33, v21\n\t" \
    "v_readlane_b32 s69, v21, 63\n\t" \
    "s_nop 1\n\t" \
    "v_mov_b32 v21, s69\n\t" \
    "s_mov_b32 s79, s74\n\t" \
    "s_mov_b32 s80, s74\n\t" \
    "s_cmp_eq_u32 s66, 1\n\t" \
    "s_cbranch_scc1 qk_loop12_%=\n\t" \
    "s_cmp_eq_u32 s66, 2\n\t" \
    "s_cbranch_scc1 qk_loop13_%=\n\t" \
    "s_cmp_eq_u32 s66, 3\n\t" \
    "s_cbranch_scc1 qk_loop14_%=\n\t" \
    "qk_loop11_%=:\n\t" \
    "v_mov_b32 v72, 0\n\t" \
    "v_mov_b32 v73, 0\n\t" \
    "v_mov_b32 v74, 0\n\t" \
    "v_mov_b32 v75, 0\n\t" \
    "v_mov_b32 v76, 0\n\t" \
    "v_mov_b32 v77, 0\n\t" \
    "v_mov_b32 v78, 0\n\t" \
    "v_mov_b32 v79, 0\n\t" \
    "v_mov_b32 v80, 0\n\t" \
    "v_mov_b32 v81, 0\n\t" \
    "v_mov_b32 v82, 0\n\t" \
    "v_mov_b32 v83, 0\n\t" \
    "v_mov_b32 v84, 0\n\t" \
    "v_mov_b32 v85, 0\n\t" \
    "v_mov_b32 v86, 0\n\t" \
    "v_mov_b32 v87, 0\n\t" \
    "v_mov_b32 v88, 0\n\t" \
    "v_mov_b32 v89, 0\n\t" \
    "v_mov_b32 v90, 0\n\t" \
    "v_mov_b32 v91, 0\n\t" \
    "v_mov_b32 v92, 0\n\t" \
    "v_mov_b32 v93, 0\n\t" \
    "v_mov_b32 v94, 0\n\t" \
    "v_mov_b32 v95, 0\n\t" \
    "v_mov_b32 v96, 0\n\t" \
    "v_mov_b32 v97, 0\n\t" \
    "v_mov_b32 v98, 0\n\t" \
    "v_mov_b32 v99, 0\n\t" \
    "v_add_u32 v31, 1, v31\n\t" \
    "s_add_u32 s69, s65, 0\n\t" \
    "s_and_b32 s69, s69, 7\n\t" \
    "s_lshl_b32 s69, s69, 18\n\t" \
    "s_add_u32 s69, s69, s62\n\t" \
    "s_add_u32 s46, s36, s69\n\t" \
    "s_addc_u32 s47, s37, 0\n\t" \
    "s_sub_u32 s70, s79, s74\n\t" \
    "s_max_i32 s70, s70, 0\n\t" \
    "s_min_i32 s70, s70, 16000\n\t" \
    "s_lshl_b32 s70, s70, 1\n\t" \
    "s_mul_hi_u32 s70, s70, s77\n\t" \
    "s_add_u32 s70, s70, s73\n\t" \
    ".p2align 6\n\t" \
    "s_nop 0\n\t" \
    "s_nop 0\n\t" \
    "s_nop 0\n\t" \
    "s_nop 0\n\t" \
    "s_nop 0\n\t" \
    "s_nop 0\n\t" \
    "qk_go16_%=:\n\t" \
    "s_getpc_b64 s[30:31]\n\t" \
    "qk_Wp18_%=:\n\t" \
    "s_add_u32 s30, s30, qk_We19_%=-qk_Wp18_%=\n\t" \
    "s_addc_u32 s31, s31, 0\n\t" \
    "s_sub_u32 s70, s70, 96\n\t" \
    "qk_W17_%=:\n\t" \
    "s_memtime s[56:57]\n\t" \
    "s_waitcnt lgkmcnt(0)\n\t" \
    "s_sub_u32 s69, s70, s56\n\t" \
    "s_cmp_gt_i32 s69, 80\n\t" \
    "s_cbranch_scc1 qk_W17_%=\n\t" \
    "s_max_i32 s69, s69, 0\n\t" \
    "s_and_b32 s69, s69, -4\n\t" \
    "s_sub_u32 s30, s30, s69\n\t" \
    "s_subb_u32 s31, s31, 0\n\t" \
    "s_setpc_b64 s[30:31]\n\t" \
    "s_nop 0\n\t" \
    "s_nop 0\n\t" \
    "s_nop 0\n\t" \
    "s_nop 0\n\t" \
    "s_nop 0\n\t" \
    "s_nop 0\n\t" \
    "s_nop 0\n\t" \
    "s_nop 0\n\t" \
    "s_nop 0\n\t" \
    "s_nop 0\n\t" \
    "s_nop 0\n\t" \
    "s_nop 0\n\t" \
    "s_nop 0\n\t" \
    "s_nop 0\n\t" \
    "s_nop 0\n\t" \
    "s_nop 0\n\t" \
    "s_nop 0\n\t" \
    "s_nop 0\n\t" \
    "s_nop 0\n\t" \
    "s_nop 0\n\t" \
    "qk_We19_%=:\n\t" \
    "qk_G20_%=:\n\t" \
    "global_load_dwordx4 v[36:39], v1, s[46:47] sc1\n\t" \
    "global_load_dwordx4 v[40:43], v23, s[46:47] sc1\n\t" \
    "s_nop 7\n\t" \
    "global_load_dwordx4 v[44:47], v2, s[46:47] sc1\n\t" \
    "s_waitcnt vmcnt(2)\n\t" \
    "v_smfmac_f32_16x16x64_bf16 v[72:75], v[36:39], a[0:7], v10\n\t" \
    "ds_write_b128 v3, v[36:39]\n\t" \
    "v_smfmac_f32_16x16x64_bf16 v[76:79], v[36:39], a[72:79], v10\n\t" \
    "v_smfmac_f32_16x16x64_bf16 v[80:83], v[36:39], a[144:151], v10\n\t" \
    "v_smfmac_f32_16x16x64_bf16 v[84:87], v[36:39], a[216:223], v10\n\t" \
    "s_waitcnt vmcnt(1)\n\t" \
    "v_cmp_u_f32 vcc, v36, v40\n\t" \
    "ds_write_b128 v3, v[40:43] offset:1024\n\t" \
    "v_smfmac_f32_16x16x64_bf16 v[72:75], v[40:43], a[8:15], v10\n\t" \
    "v_smfmac_f32_16x16x64_bf16 v[76:79], v[40:43], a[80:87], v10\n\t" \
    "s_cbranch_vccnz qk_Gbad21_%=\n\t" \
    "qk_Gc22_%=:\n\t" \
    "s_waitcnt lgkmcnt(0)\n\t" \
    "s_barrier\n\t" \
    "ds_read_b128 v[48:51], v4\n\t" \
    "ds_read_b128 v[52:55], v5\n\t" \
    "v_smfmac_f32_16x16x64_bf16 v[80:83], v[40:43], a[152:159], v10\n\t" \
    "ds_read_b128 v[56:59], v6\n\t" \
    "ds_read_b128 v[60:63], v7\n\t" \
    "v_smfmac_f32_16x16x64_bf16 v[84:87], v[40:43], a[224:231], v10\n\t" \
    "s_waitcnt vmcnt(0)\n\t" \
    "v_cmp_eq_u32 vcc, -1, v44\n\t" \
    "ds_read_b128 v[64:67], v8\n\t" \
    "ds_read_b128 v[68:71], v9\n\t" \
    "s_cbranch_vccnz qk_Gbad226_%=\n\t" \
    "qk_Gc227_%=:\n\t" \
    "v_smfmac_f32_16x16x64_bf16 v[72:75], v[44:47], a[64:71], v10\n\t" \
    "v_smfmac_f32_16x16x64_bf16 v[76:79], v[44:47], a[136:143], v10\n\t" \
    "v_smfmac_f32_16x16x64_bf16 v[80:83], v[44:47], a[208:215], v10\n\t" \
    "v_smfmac_f32_16x16x64_bf16 v[84:87], v[44:47], v[248:255], v10\n\t" \
    "s_waitcnt lgkmcnt(5)\n\t" \
    "v_smfmac_f32_16x16x64_bf16 v[72:75], v[48:51], a[16:23], v10\n\t" \
    "s_waitcnt lgkmcnt(4)\n\t" \
    "v_smfmac_f32_16x16x64_bf16 v[72:75], v[52:55], a[24:31], v10\n\t" \
    "s_waitcnt lgkmcnt(3)\n\t" \
    "v_smfmac_f32_16x16x64_bf16 v[72:75], v[56:59], a[32:39], v10\n\t" \
    "s_waitcnt lgkmcnt(2)\n\t" \
    "v_smfmac_f32_16x16x64_bf16 v[72:75], v[60:63], a[40:47], v10\n\t" \
    "s_waitcnt lgkmcnt(1)\n\t" \
    "v_smfmac_f32_16x16x64_bf16 v[72:75], v[64:67], a[48:55], v10\n\t" \
    "s_waitcnt lgkmcnt(0)\n\t" \
    "v_smfmac_f32_16x16x64_bf16 v[72:75], v[68:71], a[56:63], v10\n\t" \
    "v_smfmac_f32_16x16x64_bf16 v[76:79], v[48:51], a[88:95], v10\n\t" \
    "s_nop 1\n\t" \
    "s_nop 0\n\t" \
    "v_smfmac_f32_16x16x64_bf16 v[76:79], v[52:55], a[96:103], v10\n\t" \
    "s_nop 1\n\t" \
    "s_nop 0\n\t" \
    "v_smfmac_f32_16x16x64_bf16 v[76:79], v[56:59], a[104:111], v10\n\t" \
    "v_add_f32 v72, v72, v74\n\t" \
    "v_smfmac_f32_16x16x64_bf16 v[76:79], v[60:63], a[112:119], v10\n\t" \
    "v_add_f32 v73, v73, v75\n\t" \
    "v_smfmac_f32_16x16x64_bf16 v[76:79], v[64:67], a[120:127], v10\n\t" \
    "v_cvt_pk_bf16_f32 v156, v72, v73\n\t" \
    "v_smfmac_f32_16x16x64_bf16 v[76:79], v[68:71], a[128:135], v10\n\t" \
    "v_cmp_u_f32 vcc, v72, v73\n\t" \
    "ds_write_b32 v12, v156 offset:0\n\t" \
    "v_smfmac_f32_16x16x64_bf16 v[80:83], v[48:51], a[160:167], v10\n\t" \
    "s_nop 1\n\t" \
    "s_cbranch_vccnz qk_Gslow23_%=\n\t" \
    "qk_Gback24_%=:\n\t" \
    "v_smfmac_f32_16x16x64_bf16 v[80:83], v[52:55], a[168:175], v10\n\t" \
    "s_nop 1\n\t" \
    "s_nop 0\n\t" \
    "v_smfmac_f32_16x16x64_bf16 v[80:83], v[56:59], a[176:183], v10\n\t" \
    "v_add_f32 v76, v76, v78\n\t" \
    "v_smfmac_f32_16x16x64_bf16 v[80:83], v[60:63], a[184:191], v10\n\t" \
    "v_add_f32 v77, v77, v79\n\t" \
    "v_smfmac_f32_16x16x64_bf16 v[80:83], v[64:67], a[192:199], v10\n\t" \
    "v_cvt_pk_bf16_f32 v157, v76, v77\n\t" \
    "v_smfmac_f32_16x16x64_bf16 v[80:83], v[68:71], a[200:207], v10\n\t" \
    "ds_write_b32 v12, v157 offset:64\n\t" \
    "v_smfmac_f32_16x16x64_bf16 v[84:87], v[48:51], a[232:239], v10\n\t" \
    "s_nop 1\n\t" \
    "s_nop 0\n\t" \
    "v_smfmac_f32_16x16x64_bf16 v[84:87], v[52:55], a[240:247], v10\n\t" \
    "s_nop 1\n\t" \
    "s_nop 0\n\t" \
    "v_smfmac_f32_16x16x64_bf16 v[84:87], v[56:59], a[248:255], v10\n\t" \
    "v_add_f32 v80, v80, v82\n\t" \
    "v_smfmac_f32_16x16x64_bf16 v[84:87], v[60:63], v[224:231], v10\n\t" \
    "v_add_f32 v81, v81, v83\n\t" \
    "v_smfmac_f32_16x16x64_bf16 v[84:87], v[64:67], v[232:239], v10\n\t" \
    "v_cvt_pk_bf16_f32 v158, v80, v81\n\t" \
    "v_smfmac_f32_16x16x64_bf16 v[84:87], v[68:71], v[240:247], v10\n\t" \
    "ds_write_b32 v16, v158 offset:0\n\t" \
    "qk_Gdone25_%=:\n\t" \
    "s_nop 6\n\t" \
    "v_add_f32 v84, v84, v86\n\t" \
    "v_add_f32 v85, v85, v87\n\t" \
    "v_cvt_pk_bf16_f32 v159, v84, v85\n\t" \
    "ds_write_b32 v16, v159 offset:256\n\t" \
    "s_waitcnt lgkmcnt(0)\n\t" \
    "s_barrier\n\t" \
    "ds_read_b64_tr_b16 v[132:133], v13 offset:0\n\t" \
    "ds_read_b64_tr_b16 v[134:135], v13 offset:128\n\t" \
    "ds_read_b64_tr_b16 v[136:137], v13 offset:1024\n\t" \
    "ds_read_b64_tr_b16 v[138:139], v13 offset:1152\n\t" \
    "ds_read_b64_tr_b16 v[140:141], v17 offset:0\n\t" \
    "ds_read_b64_tr_b16 v[142:143], v17 offset:256\n\t" \
    "ds_read_b64_tr_b16 v[144:145], v17 offset:512\n\t" \
    "ds_read_b64_tr_b16 v[146:147], v17 offset:768\n\t" \
    "ds_read_b64_tr_b16 v[148:149], v17 offset:1024\n\t" \
    "ds_read_b64_tr_b16 v[150:151], v17 offset:1280\n\t" \
    "ds_read_b64_tr_b16 v[152:153], v17 offset:1536\n\t" \
    "ds_read_b64_tr_b16 v[154:155], v17 offset:1792\n\t" \
    "s_waitcnt lgkmcnt(10)\n\t" \
    "v_smfmac_f32_16x16x64_bf16 v[88:91], v[132:135], v[100:107], v10\n\t" \
    "v_smfmac_f32_16x16x64_bf16 v[92:95], v[132:135], v[116:123], v10\n\t" \
    "s_waitcnt lgkmcnt(8)\n\t" \
    "v_smfmac_f32_16x16x64_bf16 v[88:91], v[136:139], v[108:115], v10\n\t" \
    "v_smfmac_f32_16x16x64_bf16 v[92:95], v[136:139], v[124:131], v10\n\t" \
    "s_waitcnt lgkmcnt(4)\n\t" \
    "v_smfmac_f32_16x16x64_bf16 v[96:99], v[132:135], v[140:147], v10\n\t" \
    "s_waitcnt lgkmcnt(0)\n\t" \
    "s_nop 1\n\t" \
    "v_smfmac_f32_16x16x64_bf16 v[96:99], v[136:139], v[148:155], v10\n\t" \
    "s_nop 0\n\t" \
    "v_add_f32 v168, v88, v90\n\t" \
    "v_add_f32 v169, v92, v94\n\t" \
    "v_cvt_pk_bf16_f32 v168, 0, v168\n\t" \
    "v_cvt_pk_bf16_f32 v169, 0, v169\n\t" \
    "v_mul_f32 v168, v168, v26\n\t" \
    "v_mul_f32 v169, v169, v26\n\t" \
    "v_cvt_pk_bf16_f32 v168, 0, v168\n\t" \
    "v_add_f32 v172, v96, v98\n\t" \
    "v_cvt_pk_bf16_f32 v169, 0, v169\n\t" \
    "v_cvt_pk_bf16_f32 v172, 0, v172\n\t" \
    "v_max_f32 v178, v168, v169\n\t" \
    "v_mul_f32 v172, v172, v26\n\t" \
    "v_mul_f32 v168, v168, v28\n\t" \
    "v_cvt_pk_bf16_f32 v172, 0, v172\n\t" \
    "v_max_f32_dpp v178, v178, v178 row_mirror row_mask:0xf bank_mask:0xf\n\t" \
    "v_max_f32 v173, v178, v172\n\t" \
    "v_mul_f32 v172, v172, v28\n\t" \
    "v_mul_f32 v169, v169, v28\n\t" \
    "v_max_f32_dpp v173, v173, v173 quad_perm:[1,0,3,2] row_mask:0xf bank_mask:0xf\n\t" \
    "s_nop 1\n\t" \
    "v_max_f32_dpp v173, v173, v173 quad_perm:[2,3,0,1] row_mask:0xf bank_mask:0xf\n\t" \
    "s_nop 1\n\t" \
    "v_max_f32_dpp v173, v173, v173 row_half_mirror row_mask:0xf bank_mask:0xf\n\t" \
    "v_fma_f32 v172, -v173, v28, v172\n\t" \
    "v_fma_f32 v168, -v173, v28, v168\n\t" \
    "v_fma_f32 v169, -v173, v28, v169\n\t" \
    "v_exp_f32 v172, v172\n\t" \
    "v_exp_f32 v168, v168\n\t" \
    "v_exp_f32 v169, v169\n\t" \
    "v_fma_f32 v173, v172, 0.5, v168\n\t" \
    "v_add_f32 v173, v173, v169\n\t" \
    "v_mov_b32_dpp v174, v172 quad_perm:[0,0,2,2] row_mask:0xf bank_mask:0xf\n\t" \
    "v_mov_b32_dpp v175, v172 quad_perm:[1,1,3,3] row_mask:0xf bank_mask:0xf\n\t" \
    "v_add_f32_dpp v173, v173, v173 quad_perm:[1,0,3,2] row_mask:0xf bank_mask:0xf\n\t" \
    "v_mov_b32_dpp v176, v168 quad_perm:[1,0,3,2] row_mask:0xf bank_mask:0xf\n\t" \
    "v_mov_b32_dpp v177, v169 quad_perm:[1,0,3,2] row_mask:0xf bank_mask:0xf\n\t" \
    "v_add_f32_dpp v173, v173, v173 quad_perm:[2,3,0,1] row_mask:0xf bank_mask:0xf\n\t" \
    "v_cndmask_b32 v168, v168, v177, s[48:49]\n\t" \
    "v_cndmask_b32 v169, v176, v169, s[48:49]\n\t" \
    "v_add_f32_dpp v173, v173, v173 row_half_mirror row_mask:0xf bank_mask:0xf\n\t" \
    "s_nop 1\n\t" \
    "v_add_f32_dpp v173, v173, v173 row_mirror row_mask:0xf bank_mask:0xf\n\t" \
    "v_rcp_f32 v173, v173\n\t" \
    "s_nop 0\n\t" \
    "v_pk_mul_f32 v[168:169], v[168:169], v[172:173] op_sel:[0,1] op_sel_hi:[1,1]\n\t" \
    "v_pk_mul_f32 v[174:175], v[174:175], v[172:173] op_sel:[0,1] op_sel_hi:[1,1]\n\t" \
    "v_cvt_pk_bf16_f32 v176, v168, v169\n\t" \
    "global_store_dword v18, v176, s[46:47] sc1\n\t" \
    "v_cvt_pk_bf16_f32 v177, v174, v175\n\t" \
    "global_store_dword v180, v176, s[46:47] sc1\n\t" \
    "global_store_dword v181, v176, s[46:47] sc1\n\t" \
    "global_store_dword v182, v176, s[46:47] sc1\n\t" \
    "global_store_dword v19, v177, s[46:47] sc1\n\t" \
    "s_cmp_eq_u32 s65, 63\n\t" \
    "s_cbranch_scc1 qk_last15_%=\n\t" \
    "v_readfirstlane_b32 s69, v25\n\t" \
    "s_add_u32 s65, s65, 1\n\t" \
    "s_max_i32 s83, s83, s69\n\t" \
    "s_cmp_gt_u32 s65, 6\n\t" \
    "s_cselect_b32 s83, 99, s83\n\t" \
    "v_mov_b32 v72, 0\n\t" \
    "v_mov_b32 v73, 0\n\t" \
    "v_mov_b32 v74, 0\n\t" \
    "v_mov_b32 v75, 0\n\t" \
    "v_mov_b32 v76, 0\n\t" \
    "v_mov_b32 v77, 0\n\t" \
    "v_mov_b32 v78, 0\n\t" \
    "v_mov_b32 v79, 0\n\t" \
    "v_mov_b32 v80, 0\n\t" \
    "v_mov_b32 v81, 0\n\t" \
    "v_mov_b32 v82, 0\n\t" \
    "v_mov_b32 v83, 0\n\t" \
    "v_mov_b32 v84, 0\n\t" \
    "v_mov_b32 v85, 0\n\t" \
    "v_mov_b32 v86, 0\n\t" \
    "v_mov_b32 v87, 0\n\t" \
    "v_mov_b32 v88, 0\n\t" \
    "v_mov_b32 v89, 0\n\t" \
    "v_mov_b32 v90, 0\n\t" \
    "v_mov_b32 v91, 0\n\t" \
    "v_mov_b32 v92, 0\n\t" \
    "v_mov_b32 v93, 0\n\t" \
    "v_mov_b32 v94, 0\n\t" \
    "v_mov_b32 v95, 0\n\t" \
    "v_mov_b32 v96, 0\n\t" \
    "v_mov_b32 v97, 0\n\t" \
    "v_mov_b32 v98, 0\n\t" \
    "v_mov_b32 v99, 0\n\t" \
    "v_add_u32 v31, 1, v31\n\t" \
    "s_add_u32 s69, s65, 0\n\t" \
    "s_and_b32 s69, s69, 7\n\t" \
    "s_lshl_b32 s69, s69, 18\n\t" \
    "s_add_u32 s69, s69, s62\n\t" \
    "s_add_u32 s46, s36, s69\n\t" \
    "s_addc_u32 s47, s37, 0\n\t" \
    "s_mul_i32 s70, s67, 7\n\t" \
    "s_add_u32 s69, s65, 0\n\t" \
    "s_xor_b32 s69, s69, s70\n\t" \
    "s_and_b32 s69, s69, 63\n\t" \
    "s_lshl_b32 s69, s69, 13\n\t" \
    "s_mul_i32 s70, s67, 524288\n\t" \
    "s_add_u32 s69, s69, s70\n\t" \
    "s_add_u32 s69, s69, 23068672\n\t" \
    "s_add_u32 s38, s36, s69\n\t" \
    "s_addc_u32 s39, s37, 0\n\t" \
    "qk_WF28_%=:\n\t" \
    "s_cmp_ge_i32 s83, s65\n\t" \
    "s_cbranch_scc1 qk_WFok29_%=\n\t" \
    "global_load_dword v160, v24, s[36:37] sc1\n\t" \
    "s_waitcnt vmcnt(0)\n\t" \
    "v_readfirstlane_b32 s83, v160\n\t" \
    "s_nop 1\n\t" \
    "s_cmp_ge_i32 s83, s65\n\t" \
    "s_cbranch_scc1 qk_WFok29_%=\n\t" \
    "s_cmp_eq_u32 s68, 0\n\t" \
    "s_cbranch_scc1 qk_F30_%=\n\t" \
    "s_sub_u32 s68, s68, 1\n\t" \
    "s_sleep 2\n\t" \
    "s_branch qk_WF28_%=\n\t" \
    "qk_F30_%=:\n\t" \
    "v_mov_b32 v160, 5767424\n\t" \
    "v_mov_b32 v161, 9\n\t" \
    "global_store_dword v160, v161, s[36:37] sc1\n\t" \
    "s_waitcnt vmcnt(0)\n\t" \
    "s_branch qk_WFok29_%=\n\t" \
    "qk_WFok29_%=:\n\t" \
    "global_load_dwordx4 v[100:103], v0, s[38:39] offset:0 sc1\n\t" \
    "global_load_dwordx4 v[104:107], v0, s[38:39] offset:2048 sc1\n\t" \
    "global_load_dwordx4 v[108:111], v15, s[38:39] offset:0 sc1\n\t" \
    "global_load_dwordx4 v[112:115], v15, s[38:39] offset:2048 sc1\n\t" \
    "global_load_dwordx4 v[116:119], v0, s[38:39] offset:1024 sc1\n\t" \
    "global_load_dwordx4 v[120:123], v0, s[38:39] offset:3072 sc1\n\t" \
    "global_load_dwordx4 v[124:127], v15, s[38:39] offset:1024 sc1\n\t" \
    "global_load_dwordx4 v[128:131], v15, s[38:39] offset:3072 sc1\n\t" \
    "s_cmp_gt_i32 s83, s65\n\t" \
    "s_cbranch_scc1 qk_nf31_%=\n\t" \
    "global_load_dword v25, v24, s[36:37] sc1\n\t" \
    "qk_nf31_%=:\n\t" \
    "s_barrier\n\t" \
    "ds_read_b32 v168, v11 offset:20480\n\t" \
    "s_waitcnt lgkmcnt(0)\n\t" \
    "v_readfirstlane_b32 s70, v168\n\t" \
    "s_branch qk_go16_%=\n\t" \
    "qk_Gbad21_%=:\n\t" \
    "v_max_u32 v160, v36, v40\n\t" \
    "v_cmp_eq_u32 vcc, -1, v160\n\t" \
    "s_cbranch_vccz qk_Gc22_%=\n\t" \
    "s_waitcnt vmcnt(0) lgkmcnt(0)\n\t" \
    "s_nop 7\n\t" \
    "v_mov_b32 v72, 0\n\t" \
    "v_mov_b32 v73, 0\n\t" \
    "v_mov_b32 v74, 0\n\t" \
    "v_mov_b32 v75, 0\n\t" \
    "v_mov_b32 v76, 0\n\t" \
    "v_mov_b32 v77, 0\n\t" \
    "v_mov_b32 v78, 0\n\t" \
    "v_mov_b32 v79, 0\n\t" \
    "v_mov_b32 v80, 0\n\t" \
    "v_mov_b32 v81, 0\n\t" \
    "v_mov_b32 v82, 0\n\t" \
    "v_mov_b32 v83, 0\n\t" \
    "v_mov_b32 v84, 0\n\t" \
    "v_mov_b32 v85, 0\n\t" \
    "v_mov_b32 v86, 0\n\t" \
    "v_mov_b32 v87, 0\n\t" \
    "s_cmp_eq_u32 s68, 0\n\t" \
    "s_cbranch_scc1 qk_F32_%=\n\t" \
    "s_sub_u32 s68, s68, 1\n\t" \
    "s_branch qk_G20_%=\n\t" \
    "qk_F32_%=:\n\t" \
    "v_mov_b32 v160, 5767424\n\t" \
    "v_mov_b32 v161, 1\n\t" \
    "global_store_dword v160, v161, s[36:37] sc1\n\t" \
    "s_waitcnt vmcnt(0)\n\t" \
    "s_branch qk_Gc22_%=\n\t" \
    "qk_Gbad226_%=:\n\t" \
    "global_load_dwordx4 v[44:47], v2, s[46:47] sc1\n\t" \
    "s_waitcnt vmcnt(0)\n\t" \
    "v_cmp_eq_u32 vcc, -1, v44\n\t" \
    "s_cbranch_vccz qk_Gc227_%=\n\t" \
    "s_cmp_eq_u32 s68, 0\n\t" \
    "s_cbranch_scc1 qk_F33_%=\n\t" \
    "s_sub_u32 s68, s68, 1\n\t" \
    "s_sleep 1\n\t" \
    "s_branch qk_Gbad226_%=\n\t" \
    "qk_F33_%=:\n\t" \
    "v_mov_b32 v160, 5767424\n\t" \
    "v_mov_b32 v161, 2\n\t" \
    "global_store_dword v160, v161, s[36:37] sc1\n\t" \
    "s_waitcnt vmcnt(0)\n\t" \
    "s_branch qk_Gc227_%=\n\t" \
    "qk_Gslow23_%=:\n\t" \
    "v_max_u32 v160, v36, v37\n\t" \
    "v_max3_u32 v160, v160, v38, v39\n\t" \
    "v_max3_u32 v160, v160, v40, v41\n\t" \
    "v_max3_u32 v160, v160, v42, v43\n\t" \
    "v_max3_u32 v160, v160, v44, v45\n\t" \
    "v_max3_u32 v160, v160, v46, v47\n\t" \
    "v_max3_u32 v160, v160, v48, v49\n\t" \
    "v_max3_u32 v160, v160, v50, v51\n\t" \
    "v_max3_u32 v160, v160, v52, v53\n\t" \
    "v_max3_u32 v160, v160, v54, v55\n\t" \
    "v_max3_u32 v160, v160, v56, v57\n\t" \
    "v_max3_u32 v160, v160, v58, v59\n\t" \
    "v_max3_u32 v160, v160, v60, v61\n\t" \
    "v_max3_u32 v160, v160, v62, v63\n\t" \
    "v_max3_u32 v160, v160, v64, v65\n\t" \
    "v_max3_u32 v160, v160, v66, v67\n\t" \
    "v_max3_u32 v160, v160, v68, v69\n\t" \
    "v_max3_u32 v160, v160, v70, v71\n\t" \
    "v_cmp_eq_u32 vcc, -1, v160\n\t" \
    "s_cbranch_vccz qk_Gback24_%=\n\t" \
    "v_sub_u32 v160, v1, v3\n\t" \
    "s_nop 0\n\t" \
    "v_readfirstlane_b32 s69, v160\n\t" \
    "s_nop 7\n\t" \
    "s_add_u32 s30, s46, s69\n\t" \
    "s_addc_u32 s31, s47, 0\n\t" \
    "qk_Gag34_%=:\n\t" \
    "global_load_dwordx4 v[36:39], v1, s[46:47] sc1\n\t" \
    "global_load_dwordx4 v[40:43], v23, s[46:47] sc1\n\t" \
    "global_load_dwordx4 v[44:47], v2, s[46:47] sc1\n\t" \
    "global_load_dwordx4 v[48:51], v4, s[30:31] sc1\n\t" \
    "global_load_dwordx4 v[52:55], v5, s[30:31] sc1\n\t" \
    "global_load_dwordx4 v[56:59], v6, s[30:31] sc1\n\t" \
    "global_load_dwordx4 v[60:63], v7, s[30:31] sc1\n\t" \
    "global_load_dwordx4 v[64:67], v8, s[30:31] sc1\n\t" \
    "global_load_dwordx4 v[68:71], v9, s[30:31] sc1\n\t" \
    "s_waitcnt vmcnt(0) lgkmcnt(0)\n\t" \
    "v_max_u32 v160, v36, v37\n\t" \
    "v_max3_u32 v160, v160, v38, v39\n\t" \
    "v_max3_u32 v160, v160, v40, v41\n\t" \
    "v_max3_u32 v160, v160, v42, v43\n\t" \
    "v_max3_u32 v160, v160, v44, v45\n\t" \
    "v_max3_u32 v160, v160, v46, v47\n\t" \
    "v_max3_u32 v160, v160, v48, v49\n\t" \
    "v_max3_u32 v160, v160, v50, v51\n\t" \
    "v_max3_u32 v160, v160, v52, v53\n\t" \
    "v_max3_u32 v160, v160, v54, v55\n\t" \
    "v_max3_u32 v160, v160, v56, v57\n\t" \
    "v_max3_u32 v160, v160, v58, v59\n\t" \
    "v_max3_u32 v160, v160, v60, v61\n\t" \
    "v_max3_u32 v160, v160, v62, v63\n\t" \
    "v_max3_u32 v160, v160, v64, v65\n\t" \
    "v_max3_u32 v160, v160, v66, v67\n\t" \
    "v_max3_u32 v160, v160, v68, v69\n\t" \
    "v_max3_u32 v160, v160, v70, v71\n\t" \
    "v_cmp_eq_u32 vcc, -1, v160\n\t" \
    "s_cbranch_vccz qk_Gredo35_%=\n\t" \
    "s_cmp_eq_u32 s68, 0\n\t" \
    "s_cbranch_scc1 qk_F36_%=\n\t" \
    "s_sub_u32 s68, s68, 1\n\t" \
    "s_sleep 1\n\t" \
    "s_branch qk_Gag34_%=\n\t" \
    "qk_F36_%=:\n\t" \
    "v_mov_b32 v160, 5767424\n\t" \
    "v_mov_b32 v161, 2\n\t" \
    "global_store_dword v160, v161, s[36:37] sc1\n\t" \
    "s_waitcnt vmcnt(0)\n\t" \
    "s_branch qk_Gredo35_%=\n\t" \
    "qk_Gredo35_%=:\n\t" \
    "v_mov_b32 v72, 0\n\t" \
    "v_mov_b32 v73, 0\n\t" \
    "v_mov_b32 v74, 0\n\t" \
    "v_mov_b32 v75, 0\n\t" \
    "v_mov_b32 v76, 0\n\t" \
    "v_mov_b32 v77, 0\n\t" \
    "v_mov_b32 v78, 0\n\t" \
    "v_mov_b32 v79, 0\n\t" \
    "v_mov_b32 v80, 0\n\t" \
    "v_mov_b32 v81, 0\n\t" \
    "v_mov_b32 v82, 0\n\t" \
    "v_mov_b32 v83, 0\n\t" \
    "v_mov_b32 v84, 0\n\t" \
    "v_mov_b32 v85, 0\n\t" \
    "v_mov_b32 v86, 0\n\t" \
    "v_mov_b32 v87, 0\n\t" \
    "v_smfmac_f32_16x16x64_bf16 v[72:75], v[36:39], a[0:7], v10\n\t" \
    "v_smfmac_f32_16x16x64_bf16 v[72:75], v[40:43], a[8:15], v10\n\t" \
    "v_smfmac_f32_16x16x64_bf16 v[72:75], v[44:47], a[64:71], v10\n\t" \
    "v_smfmac_f32_16x16x64_bf16 v[72:75], v[48:51], a[16:23], v10\n\t" \
    "v_smfmac_f32_16x16x64_bf16 v[72:75], v[52:55], a[24:31], v10\n\t" \
    "v_smfmac_f32_16x16x64_bf16 v[72:75], v[56:59], a[32:39], v10\n\t" \
    "v_smfmac_f32_16x16x64_bf16 v[72:75], v[60:63], a[40:47], v10\n\t" \
    "v_smfmac_f32_16x16x64_bf16 v[72:75], v[64:67], a[48:55], v10\n\t" \
    "v_smfmac_f32_16x16x64_bf16 v[72:75], v[68:71], a[56:63], v10\n\t" \
    "v_smfmac_f32_16x16x64_bf16 v[76:79], v[36:39], a[72:79], v10\n\t" \
    "v_smfmac_f32_16x16x64_bf16 v[76:79], v[40:43], a[80:87], v10\n\t" \
    "v_smfmac_f32_16x16x64_bf16 v[76:79], v[44:47], a[136:143], v10\n\t" \
    "v_smfmac_f32_16x16x64_bf16 v[76:79], v[48:51], a[88:95], v10\n\t" \
    "v_smfmac_f32_16x16x64_bf16 v[76:79], v[52:55], a[96:103], v10\n\t" \
    "v_smfmac_f32_16x16x64_bf16 v[76:79], v[56:59], a[104:111], v10\n\t" \
    "v_smfmac_f32_16x16x64_bf16 v[76:79], v[60:63], a[112:119], v10\n\t" \
    "v_smfmac_f32_16x16x64_bf16 v[76:79], v[64:67], a[120:127], v10\n\t" \
    "v_smfmac_f32_16x16x64_bf16 v[76:79], v[68:71], a[128:135], v10\n\t" \
    "v_smfmac_f32_16x16x64_bf16 v[80:83], v[36:39], a[144:151], v10\n\t" \
    "v_smfmac_f32_16x16x64_bf16 v[80:83], v[40:43], a[152:159], v10\n\t" \
    "v_smfmac_f32_16x16x64_bf16 v[80:83], v[44:47], a[208:215], v10\n\t" \
    "v_smfmac_f32_16x16x64_bf16 v[80:83], v[48:51], a[160:167], v10\n\t" \
    "v_smfmac_f32_16x16x64_bf16 v[80:83], v[52:55], a[168:175], v10\n\t" \
    "v_smfmac_f32_16x16x64_bf16 v[80:83], v[56:59], a[176:183], v10\n\t" \
    "v_smfmac_f32_16x16x64_bf16 v[80:83], v[60:63], a[184:191], v10\n\t" \
    "v_smfmac_f32_16x16x64_bf16 v[80:83], v[64:67], a[192:199], v10\n\t" \
    "v_smfmac_f32_16x16x64_bf16 v[80:83], v[68:71], a[200:207], v10\n\t" \
    "v_smfmac_f32_16x16x64_bf16 v[84:87], v[36:39], a[216:223], v10\n\t" \
    "v_smfmac_f32_16x16x64_bf16 v[84:87], v[40:43], a[224:231], v10\n\t" \
    "v_smfmac_f32_16x16x64_bf16 v[84:87], v[44:47], v[248:255], v10\n\t" \
    "v_smfmac_f32_16x16x64_bf16 v[84:87], v[48:51], a[232:239], v10\n\t" \
    "v_smfmac_f32_16x16x64_bf16 v[84:87], v[52:55], a[240:247], v10\n\t" \
    "v_smfmac_f32_16x16x64_bf16 v[84:87], v[56:59], a[248:255], v10\n\t" \
    "v_smfmac_f32_16x16x64_bf16 v[84:87], v[60:63], v[224:231], v10\n\t" \
    "v_smfmac_f32_16x16x64_bf16 v[84:87], v[64:67], v[232:239], v10\n\t" \
    "v_smfmac_f32_16x16x64_bf16 v[84:87], v[68:71], v[240:247], v10\n\t" \
    "v_add_f32 v72, v72, v74\n\t" \
    "v_add_f32 v73, v73, v75\n\t" \
    "v_cvt_pk_bf16_f32 v156, v72, v73\n\t" \
    "ds_write_b32 v12, v156 offset:0\n\t" \
    "v_add_f32 v76, v76, v78\n\t" \
    "v_add_f32 v77, v77, v79\n\t" \
    "v_cvt_pk_bf16_f32 v157, v76, v77\n\t" \
    "ds_write_b32 v12, v157 offset:64\n\t" \
    "v_add_f32 v80, v80, v82\n\t" \
    "v_add_f32 v81, v81, v83\n\t" \
    "v_cvt_pk_bf16_f32 v158, v80, v81\n\t" \
    "ds_write_b32 v16, v158 offset:0\n\t" \
    "s_nop 7\n\t" \
    "s_branch qk_Gdone25_%=\n\t" \
    "qk_last15_%=:\n\t" \
    "s_endpgm\n\t" \
    "qk_loop12_%=:\n\t" \
    "v_mov_b32 v72, 0\n\t" \
    "v_mov_b32 v73, 0\n\t" \
    "v_mov_b32 v74, 0\n\t" \
    "v_mov_b32 v75, 0\n\t" \
    "v_mov_b32 v76, 0\n\t" \
    "v_mov_b32 v77, 0\n\t" \
    "v_mov_b32 v78, 0\n\t" \
    "v_mov_b32 v79, 0\n\t" \
    "v_mov_b32 v80, 0\n\t" \
    "v_mov_b32 v81, 0\n\t" \
    "v_mov_b32 v82, 0\n\t" \
    "v_mov_b32 v83, 0\n\t" \
    "v_mov_b32 v84, 0\n\t" \
    "v_mov_b32 v85, 0\n\t" \
    "v_mov_b32 v86, 0\n\t" \
    "v_mov_b32 v87, 0\n\t" \
    "v_mov_b32 v88, 0\n\t" \
    "v_mov_b32 v89, 0\n\t" \
    "v_mov_b32 v90, 0\n\t" \
    "v_mov_b32 v91, 0\n\t" \
    "v_mov_b32 v92, 0\n\t" \
    "v_mov_b32 v93, 0\n\t" \
    "v_mov_b32 v94, 0\n\t" \
    "v_mov_b32 v95, 0\n\t" \
    "v_mov_b32 v96, 0\n\t" \
    "v_mov_b32 v97, 0\n\t" \
    "v_mov_b32 v98, 0\n\t" \
    "v_mov_b32 v99, 0\n\t" \
    "v_add_u32 v31, 1, v31\n\t" \
    "s_add_u32 s69, s65, 0\n\t" \
    "s_and_b32 s69, s69, 7\n\t" \
    "s_lshl_b32 s69, s69, 18\n\t" \
    "s_add_u32 s69, s69, s62\n\t" \
    "s_add_u32 s46, s36, s69\n\t" \
    "s_addc_u32 s47, s37, 0\n\t" \
    "s_sub_u32 s70, s79, s74\n\t" \
    "s_max_i32 s70, s70, 0\n\t" \
    "s_min_i32 s70, s70, 16000\n\t" \
    "s_lshl_b32 s70, s70, 1\n\t" \
    "s_mul_hi_u32 s70, s70, s77\n\t" \
    "s_add_u32 s70, s70, s73\n\t" \
    ".p2align 6\n\t" \
    "s_nop 0\n\t" \
    "s_nop 0\n\t" \
    "s_nop 0\n\t" \
    "s_nop 0\n\t" \
    "s_nop 0\n\t" \
    "s_nop 0\n\t" \
    "qk_go38_%=:\n\t" \
    "s_getpc_b64 s[30:31]\n\t" \
    "qk_Wp40_%=:\n\t" \
    "s_add_u32 s30, s30, qk_We41_%=-qk_Wp40_%=\n\t" \
    "s_addc_u32 s31, s31, 0\n\t" \
    "s_sub_u32 s70, s70, 96\n\t" \
    "qk_W39_%=:\n\t" \
    "s_memtime s[56:57]\n\t" \
    "s_waitcnt lgkmcnt(0)\n\t" \
    "s_sub_u32 s69, s70, s56\n\t" \
    "s_cmp_gt_i32 s69, 80\n\t" \
    "s_cbranch_scc1 qk_W39_%=\n\t" \
    "s_max_i32 s69, s69, 0\n\t" \
    "s_and_b32 s69, s69, -4\n\t" \
    "s_sub_u32 s30, s30, s69\n\t" \
    "s_subb_u32 s31, s31, 0\n\t" \
    "s_setpc_b64 s[30:31]\n\t" \
    "s_nop 0\n\t" \
    "s_nop 0\n\t" \
    "s_nop 0\n\t" \
    "s_nop 0\n\t" \
    "s_nop 0\n\t" \
    "s_nop 0\n\t" \
    "s_nop 0\n\t" \
    "s_nop 0\n\t" \
    "s_nop 0\n\t" \
    "s_nop 0\n\t" \
    "s_nop 0\n\t" \
    "s_nop 0\n\t" \
    "s_nop 0\n\t" \
    "s_nop 0\n\t" \
    "s_nop 0\n\t" \
    "s_nop 0\n\t" \
    "s_nop 0\n\t" \
    "s_nop 0\n\t" \
    "s_nop 0\n\t" \
    "s_nop 0\n\t" \
    "qk_We41_%=:\n\t" \
    "qk_G42_%=:\n\t" \
    "global_load_dwordx4 v[36:39], v1, s[46:47] sc1\n\t" \
    "global_load_dwordx4 v[40:43], v23, s[46:47] sc1\n\t" \
    "s_nop 7\n\t" \
    "global_load_dwordx4 v[44:47], v2, s[46:47] sc1\n\t" \
    "s_waitcnt vmcnt(2)\n\t" \
    "v_smfmac_f32_16x16x64_bf16 v[72:75], v[36:39], a[16:23], v10\n\t" \
    "ds_write_b128 v3, v[36:39]\n\t" \
    "v_smfmac_f32_16x16x64_bf16 v[76:79], v[36:39], a[88:95], v10\n\t" \
    "v_smfmac_f32_16x16x64_bf16 v[80:83], v[36:39], a[160:167], v10\n\t" \
    "v_smfmac_f32_16x16x64_bf16 v[84:87], v[36:39], a[232:239], v10\n\t" \
    "s_waitcnt vmcnt(1)\n\t" \
    "v_cmp_u_f32 vcc, v36, v40\n\t" \
    "ds_write_b128 v3, v[40:43] offset:1024\n\t" \
    "v_smfmac_f32_16x16x64_bf16 v[72:75], v[40:43], a[24:31], v10\n\t" \
    "v_smfmac_f32_16x16x64_bf16 v[76:79], v[40:43], a[96:103], v10\n\t" \
    "s_cbranch_vccnz qk_Gbad43_%=\n\t" \
    "qk_Gc44_%=:\n\t" \
    "s_waitcnt lgkmcnt(0)\n\t" \
    "s_barrier\n\t" \
    "ds_read_b128 v[48:51], v4\n\t" \
    "ds_read_b128 v[52:55], v5\n\t" \
    "v_smfmac_f32_16x16x64_bf16 v[80:83], v[40:43], a[168:175], v10\n\t" \
    "ds_read_b128 v[56:59], v6\n\t" \
    "ds_read_b128 v[60:63], v7\n\t" \
    "v_smfmac_f32_16x16x64_bf16 v[84:87], v[40:43], a[240:247], v10\n\t" \
    "s_waitcnt vmcnt(0)\n\t" \
    "v_cmp_eq_u32 vcc, -1, v44\n\t" \
    "ds_read_b128 v[64:67], v8\n\t" \
    "ds_read_b128 v[68:71], v9\n\t" \
    "s_cbranch_vccnz qk_Gbad248_%=\n\t" \
    "qk_Gc249_%=:\n\t" \
    "v_smfmac_f32_16x16x64_bf16 v[72:75], v[44:47], a[64:71], v10\n\t" \
    "v_smfmac_f32_16x16x64_bf16 v[76:79], v[44:47], a[136:143], v10\n\t" \
    "v_smfmac_f32_16x16x64_bf16 v[80:83], v[44:47], a[208:215], v10\n\t" \
    "v_smfmac_f32_16x16x64_bf16 v[84:87], v[44:47], v[248:255], v10\n\t" \
    "s_waitcnt lgkmcnt(5)\n\t" \
    "v_smfmac_f32_16x16x64_bf16 v[72:75], v[48:51], a[32:39], v10\n\t" \
    "s_waitcnt lgkmcnt(4)\n\t" \
    "v_smfmac_f32_16x16x64_bf16 v[72:75], v[52:55], a[40:47], v10\n\t" \
    "s_waitcnt lgkmcnt(3)\n\t" \
    "v_smfmac_f32_16x16x64_bf16 v[72:75], v[56:59], a[48:55], v10\n\t" \
    "s_waitcnt lgkmcnt(2)\n\t" \
    "v_smfmac_f32_16x16x64_bf16 v[72:75], v[60:63], a[56:63], v10\n\t" \
    "s_waitcnt lgkmcnt(1)\n\t" \
    "v_smfmac_f32_16x16x64_bf16 v[72:75], v[64:67], a[0:7], v10\n\t" \
    "s_waitcnt lgkmcnt(0)\n\t" \
    "v_smfmac_f32_16x16x64_bf16 v[72:75], v[68:71], a[8:15], v10\n\t" \
    "v_smfmac_f32_16x16x64_bf16 v[76:79], v[48:51], a[104:111], v10\n\t" \
    "s_nop 1\n\t" \
    "s_nop 0\n\t" \
    "v_smfmac_f32_16x16x64_bf16 v[76:79], v[52:55], a[112:119], v10\n\t" \
    "s_nop 1\n\t" \
    "s_nop 0\n\t" \
    "v_smfmac_f32_16x16x64_bf16 v[76:79], v[56:59], a[120:127], v10\n\t" \
    "v_add_f32 v72, v72, v74\n\t" \
    "v_smfmac_f32_16x16x64_bf16 v[76:79], v[60:63], a[128:135], v10\n\t" \
    "v_add_f32 v73, v73, v75\n\t" \
    "v_smfmac_f32_16x16x64_bf16 v[76:79], v[64:67], a[72:79], v10\n\t" \
    "v_cvt_pk_bf16_f32 v156, v72, v73\n\t" \
    "v_smfmac_f32_16x16x64_bf16 v[76:79], v[68:71], a[80:87], v10\n\t" \
    "v_cmp_u_f32 vcc, v72, v73\n\t" \
    "ds_write_b32 v12, v156 offset:0\n\t" \
    "v_smfmac_f32_16x16x64_bf16 v[80:83], v[48:51], a[176:183], v10\n\t" \
    "s_nop 1\n\t" \
    "s_cbranch_vccnz qk_Gslow45_%=\n\t" \
    "qk_Gback46_%=:\n\t" \
    "v_smfmac_f32_16x16x64_bf16 v[80:83], v[52:55], a[184:191], v10\n\t" \
    "s_nop 1\n\t" \
    "s_nop 0\n\t" \
    "v_smfmac_f32_16x16x64_bf16 v[80:83], v[56:59], a[192:199], v10\n\t" \
    "v_add_f32 v76, v76, v78\n\t" \
    "v_smfmac_f32_16x16x64_bf16 v[80:83], v[60:63], a[200:207], v10\n\t" \
    "v_add_f32 v77, v77, v79\n\t" \
    "v_smfmac_f32_16x16x64_bf16 v[80:83], v[64:67], a[144:151], v10\n\t" \
    "v_cvt_pk_bf16_f32 v157, v76, v77\n\t" \
    "v_smfmac_f32_16x16x64_bf16 v[80:83], v[68:71], a[152:159], v10\n\t" \
    "ds_write_b32 v12, v157 offset:64\n\t" \
    "v_smfmac_f32_16x16x64_bf16 v[84:87], v[48:51], a[248:255], v10\n\t" \
    "s_nop 1\n\t" \
    "s_nop 0\n\t" \
    "v_smfmac_f32_16x16x64_bf16 v[84:87], v[52:55], v[224:231], v10\n\t" \
    "s_nop 1\n\t" \
    "s_nop 0\n\t" \
    "v_smfmac_f32_16x16x64_bf16 v[84:87], v[56:59], v[232:239], v10\n\t" \
    "v_add_f32 v80, v80, v82\n\t" \
    "v_smfmac_f32_16x16x64_bf16 v[84:87], v[60:63], v[240:247], v10\n\t" \
    "v_add_f32 v81, v81, v83\n\t" \
    "v_smfmac_f32_16x16x64_bf16 v[84:87], v[64:67], a[216:223], v10\n\t" \
    "v_cvt_pk_bf16_f32 v158, v80, v81\n\t" \
    "v_smfmac_f32_16x16x64_bf16 v[84:87], v[68:71], a[224:231], v10\n\t" \
    "ds_write_b32 v16, v158 offset:0\n\t" \
    "qk_Gdone47_%=:\n\t" \
    "s_nop 6\n\t" \
    "v_add_f32 v84, v84, v86\n\t" \
    "v_add_f32 v85, v85, v87\n\t" \
    "v_cvt_pk_bf16_f32 v159, v84, v85\n\t" \
    "ds_write_b32 v16, v159 offset:256\n\t" \
    "s_waitcnt lgkmcnt(0)\n\t" \
    "s_barrier\n\t" \
    "ds_read_b64_tr_b16 v[132:133], v13 offset:0\n\t" \
    "ds_read_b64_tr_b16 v[134:135], v13 offset:128\n\t" \
    "ds_read_b64_tr_b16 v[136:137], v13 offset:1024\n\t" \
    "ds_read_b64_tr_b16 v[138:139], v13 offset:1152\n\t" \
    "ds_read_b64_tr_b16 v[140:141], v17 offset:0\n\t" \
    "ds_read_b64_tr_b16 v[142:143], v17 offset:256\n\t" \
    "ds_read_b64_tr_b16 v[144:145], v17 offset:512\n\t" \
    "ds_read_b64_tr_b16 v[146:147], v17 offset:768\n\t" \
    "ds_read_b64_tr_b16 v[148:149], v17 offset:1024\n\t" \
    "ds_read_b64_tr_b16 v[150:151], v17 offset:1280\n\t" \
    "ds_read_b64_tr_b16 v[152:153], v17 offset:1536\n\t" \
    "ds_read_b64_tr_b16 v[154:155], v17 offset:1792\n\t" \
    "s_waitcnt lgkmcnt(10)\n\t" \
    "v_smfmac_f32_16x16x64_bf16 v[88:91], v[132:135], v[100:107], v10\n\t" \
    "v_smfmac_f32_16x16x64_bf16 v[92:95], v[132:135], v[116:123], v10\n\t" \
    "s_waitcnt lgkmcnt(8)\n\t" \
    "v_smfmac_f32_16x16x64_bf16 v[88:91], v[136:139], v[108:115], v10\n\t" \
    "v_smfmac_f32_16x16x64_bf16 v[92:95], v[136:139], v[124:131], v10\n\t" \
    "s_waitcnt lgkmcnt(4)\n\t" \
    "v_smfmac_f32_16x16x64_bf16 v[96:99], v[132:135], v[140:147], v10\n\t" \
    "s_waitcnt lgkmcnt(0)\n\t" \
    "s_nop 1\n\t" \
    "v_smfmac_f32_16x16x64_bf16 v[96:99], v[136:139], v[148:155], v10\n\t" \
    "s_nop 0\n\t" \
    "v_add_f32 v168, v89, v91\n\t" \
    "v_add_f32 v169, v93, v95\n\t" \
    "v_cvt_pk_bf16_f32 v168, 0, v168\n\t" \
    "v_cvt_pk_bf16_f32 v169, 0, v169\n\t" \
    "v_mul_f32 v168, v168, v26\n\t" \
    "v_mul_f32 v169, v169, v26\n\t" \
    "v_cvt_pk_bf16_f32 v168, 0, v168\n\t" \
    "v_add_f32 v172, v97, v99\n\t" \
    "v_cvt_pk_bf16_f32 v169, 0, v169\n\t" \
    "v_cvt_pk_bf16_f32 v172, 0, v172\n\t" \
    "v_max_f32 v178, v168, v169\n\t" \
    "v_mul_f32 v172, v172, v26\n\t" \
    "v_mul_f32 v168, v168, v28\n\t" \
    "v_cvt_pk_bf16_f32 v172, 0, v172\n\t" \
    "v_max_f32_dpp v178, v178, v178 row_mirror row_mask:0xf bank_mask:0xf\n\t" \
    "v_max_f32 v173, v178, v172\n\t" \
    "v_mul_f32 v172, v172, v28\n\t" \
    "v_mul_f32 v169, v169, v28\n\t" \
    "v_max_f32_dpp v173, v173, v173 quad_perm:[1,0,3,2] row_mask:0xf bank_mask:0xf\n\t" \
    "s_nop 1\n\t" \
    "v_max_f32_dpp v173, v173, v173 quad_perm:[2,3,0,1] row_mask:0xf bank_mask:0xf\n\t" \
    "s_nop 1\n\t" \
    "v_max_f32_dpp v173, v173, v173 row_half_mirror row_mask:0xf bank_mask:0xf\n\t" \
    "v_fma_f32 v172, -v173, v28, v172\n\t" \
    "v_fma_f32 v168, -v173, v28, v168\n\t" \
    "v_fma_f32 v169, -v173, v28, v169\n\t" \
    "v_exp_f32 v172, v172\n\t" \
    "v_exp_f32 v168, v168\n\t" \
    "v_exp_f32 v169, v169\n\t" \
    "v_fma_f32 v173, v172, 0.5, v168\n\t" \
    "v_add_f32 v173, v173, v169\n\t" \
    "v_mov_b32_dpp v174, v172 quad_perm:[0,0,2,2] row_mask:0xf bank_mask:0xf\n\t" \
    "v_mov_b32_dpp v175, v172 quad_perm:[1,1,3,3] row_mask:0xf bank_mask:0xf\n\t" \
    "v_add_f32_dpp v173, v173, v173 quad_perm:[1,0,3,2] row_mask:0xf bank_mask:0xf\n\t" \
    "v_mov_b32_dpp v176, v168 quad_perm:[1,0,3,2] row_mask:0xf bank_mask:0xf\n\t" \
    "v_mov_b32_dpp v177, v169 quad_perm:[1,0,3,2] row_mask:0xf bank_mask:0xf\n\t" \
    "v_add_f32_dpp v173, v173, v173 quad_perm:[2,3,0,1] row_mask:0xf bank_mask:0xf\n\t" \
    "v_cndmask_b32 v168, v168, v177, s[48:49]\n\t" \
    "v_cndmask_b32 v169, v176, v169, s[48:49]\n\t" \
    "v_add_f32_dpp v173, v173, v173 row_half_mirror row_mask:0xf bank_mask:0xf\n\t" \
    "s_nop 1\n\t" \
    "v_add_f32_dpp v173, v173, v173 row_mirror row_mask:0xf bank_mask:0xf\n\t" \
    "v_rcp_f32 v173, v173\n\t" \
    "s_nop 0\n\t" \
    "v_pk_mul_f32 v[168:169], v[168:169], v[172:173] op_sel:[0,1] op_sel_hi:[1,1]\n\t" \
    "v_pk_mul_f32 v[174:175], v[174:175], v[172:173] op_sel:[0,1] op_sel_hi:[1,1]\n\t" \
    "v_cvt_pk_bf16_f32 v176, v168, v169\n\t" \
    "global_store_dword v18, v176, s[46:47] sc1\n\t" \
    "v_cvt_pk_bf16_f32 v177, v174, v175\n\t" \
    "global_store_dword v180, v176, s[46:47] sc1\n\t" \
    "global_store_dword v181, v176, s[46:47] sc1\n\t" \
    "global_store_dword v182, v176, s[46:47] sc1\n\t" \
    "global_store_dword v19, v177, s[46:47] sc1\n\t" \
    "s_cmp_eq_u32 s65, 63\n\t" \
    "s_cbranch_scc1 qk_last37_%=\n\t" \
    "v_readfirstlane_b32 s69, v25\n\t" \
    "s_add_u32 s65, s65, 1\n\t" \
    "s_max_i32 s83, s83, s69\n\t" \
    "s_cmp_gt_u32 s65, 6\n\t" \
    "s_cselect_b32 s83, 99, s83\n\t" \
    "v_mov_b32 v72, 0\n\t" \
    "v_mov_b32 v73, 0\n\t" \
    "v_mov_b32 v74, 0\n\t" \
    "v_mov_b32 v75, 0\n\t" \
    "v_mov_b32 v76, 0\n\t" \
    "v_mov_b32 v77, 0\n\t" \
    "v_mov_b32 v78, 0\n\t" \
    "v_mov_b32 v79, 0\n\t" \
    "v_mov_b32 v80, 0\n\t" \
    "v_mov_b32 v81, 0\n\t" \
    "v_mov_b32 v82, 0\n\t" \
    "v_mov_b32 v83, 0\n\t" \
    "v_mov_b32 v84, 0\n\t" \
    "v_mov_b32 v85, 0\n\t" \
    "v_mov_b32 v86, 0\n\t" \
    "v_mov_b32 v87, 0\n\t" \
    "v_mov_b32 v88, 0\n\t" \
    "v_mov_b32 v89, 0\n\t" \
    "v_mov_b32 v90, 0\n\t" \
    "v_mov_b32 v91, 0\n\t" \
    "v_mov_b32 v92, 0\n\t" \
    "v_mov_b32 v93, 0\n\t" \
    "v_mov_b32 v94, 0\n\t" \
    "v_mov_b32 v95, 0\n\t" \
    "v_mov_b32 v96, 0\n\t" \
    "v_mov_b32 v97, 0\n\t" \
    "v_mov_b32 v98, 0\n\t" \
    "v_mov_b32 v99, 0\n\t" \
    "v_add_u32 v31, 1, v31\n\t" \
    "s_add_u32 s69, s65, 0\n\t" \
    "s_and_b32 s69, s69, 7\n\t" \
    "s_lshl_b32 s69, s69, 18\n\t" \
    "s_add_u32 s69, s69, s62\n\t" \
    "s_add_u32 s46, s36, s69\n\t" \
    "s_addc_u32 s47, s37, 0\n\t" \
    "s_mul_i32 s70, s67, 7\n\t" \
    "s_add_u32 s69, s65, 0\n\t" \
    "s_xor_b32 s69, s69, s70\n\t" \
    "s_and_b32 s69, s69, 63\n\t" \
    "s_lshl_b32 s69, s69, 13\n\t" \
    "s_mul_i32 s70, s67, 524288\n\t" \
    "s_add_u32 s69, s69, s70\n\t" \
    "s_add_u32 s69, s69, 23068672\n\t" \
    "s_add_u32 s38, s36, s69\n\t" \
    "s_addc_u32 s39, s37, 0\n\t" \
    "qk_WF50_%=:\n\t" \
    "s_cmp_ge_i32 s83, s65\n\t" \
    "s_cbranch_scc1 qk_WFok51_%=\n\t" \
    "global_load_dword v160, v24, s[36:37] sc1\n\t" \
    "s_waitcnt vmcnt(0)\n\t" \
    "v_readfirstlane_b32 s83, v160\n\t" \
    "s_nop 1\n\t" \
    "s_cmp_ge_i32 s83, s65\n\t" \
    "s_cbranch_scc1 qk_WFok51_%=\n\t" \
    "s_cmp_eq_u32 s68, 0\n\t" \
    "s_cbranch_scc1 qk_F52_%=\n\t" \
    "s_sub_u32 s68, s68, 1\n\t" \
    "s_sleep 2\n\t" \
    "s_branch qk_WF50_%=\n\t" \
    "qk_F52_%=:\n\t" \
    "v_mov_b32 v160, 5767424\n\t" \
    "v_mov_b32 v161, 9\n\t" \
    "global_store_dword v160, v161, s[36:37] sc1\n\t" \
    "s_waitcnt vmcnt(0)\n\t" \
    "s_branch qk_WFok51_%=\n\t" \
    "qk_WFok51_%=:\n\t" \
    "global_load_dwordx4 v[100:103], v0, s[38:39] offset:0 sc1\n\t" \
    "global_load_dwordx4 v[104:107], v0, s[38:39] offset:2048 sc1\n\t" \
    "global_load_dwordx4 v[108:111], v15, s[38:39] offset:0 sc1\n\t" \
    "global_load_dwordx4 v[112:115], v15, s[38:39] offset:2048 sc1\n\t" \
    "global_load_dwordx4 v[116:119], v0, s[38:39] offset:1024 sc1\n\t" \
    "global_load_dwordx4 v[120:123], v0, s[38:39] offset:3072 sc1\n\t" \
    "global_load_dwordx4 v[124:127], v15, s[38:39] offset:1024 sc1\n\t" \
    "global_load_dwordx4 v[128:131], v15, s[38:39] offset:3072 sc1\n\t" \
    "s_cmp_gt_i32 s83, s65\n\t" \
    "s_cbranch_scc1 qk_nf53_%=\n\t" \
    "global_load_dword v25, v24, s[36:37] sc1\n\t" \
    "qk_nf53_%=:\n\t" \
    "s_barrier\n\t" \
    "ds_read_b32 v168, v11 offset:20480\n\t" \
    "s_waitcnt lgkmcnt(0)\n\t" \
    "v_readfirstlane_b32 s70, v168\n\t" \
    "s_branch qk_go38_%=\n\t" \
    "qk_Gbad43_%=:\n\t" \
    "v_max_u32 v160, v36, v40\n\t" \
    "v_cmp_eq_u32 vcc, -1, v160\n\t" \
    "s_cbranch_vccz qk_Gc44_%=\n\t" \
    "s_waitcnt vmcnt(0) lgkmcnt(0)\n\t" \
    "s_nop 7\n\t" \
    "v_mov_b32 v72, 0\n\t" \
    "v_mov_b32 v73, 0\n\t" \
    "v_mov_b32 v74, 0\n\t" \
    "v_mov_b32 v75, 0\n\t" \
    "v_mov_b32 v76, 0\n\t" \
    "v_mov_b32 v77, 0\n\t" \
    "v_mov_b32 v78, 0\n\t" \
    "v_mov_b32 v79, 0\n\t" \
    "v_mov_b32 v80, 0\n\t" \
    "v_mov_b32 v81, 0\n\t" \
    "v_mov_b32 v82, 0\n\t" \
    "v_mov_b32 v83, 0\n\t" \
    "v_mov_b32 v84, 0\n\t" \
    "v_mov_b32 v85, 0\n\t" \
    "v_mov_b32 v86, 0\n\t" \
    "v_mov_b32 v87, 0\n\t" \
    "s_cmp_eq_u32 s68, 0\n\t" \
    "s_cbranch_scc1 qk_F54_%=\n\t" \
    "s_sub_u32 s68, s68, 1\n\t" \
    "s_branch qk_G42_%=\n\t" \
    "qk_F54_%=:\n\t" \
    "v_mov_b32 v160, 5767424\n\t" \
    "v_mov_b32 v161, 1\n\t" \
    "global_store_dword v160, v161, s[36:37] sc1\n\t" \
    "s_waitcnt vmcnt(0)\n\t" \
    "s_branch qk_Gc44_%=\n\t" \
    "qk_Gbad248_%=:\n\t" \
    "global_load_dwordx4 v[44:47], v2, s[46:47] sc1\n\t" \
    "s_waitcnt vmcnt(0)\n\t" \
    "v_cmp_eq_u32 vcc, -1, v44\n\t" \
    "s_cbranch_vccz qk_Gc249_%=\n\t" \
    "s_cmp_eq_u32 s68, 0\n\t" \
    "s_cbranch_scc1 qk_F55_%=\n\t" \
    "s_sub_u32 s68, s68, 1\n\t" \
    "s_sleep 1\n\t" \
    "s_branch qk_Gbad248_%=\n\t" \
    "qk_F55_%=:\n\t" \
    "v_mov_b32 v160, 5767424\n\t" \
    "v_mov_b32 v161, 2\n\t" \
    "global_store_dword v160, v161, s[36:37] sc1\n\t" \
    "s_waitcnt vmcnt(0)\n\t" \
    "s_branch qk_Gc249_%=\n\t" \
    "qk_Gslow45_%=:\n\t" \
    "v_max_u32 v160, v36, v37\n\t" \
    "v_max3_u32 v160, v160, v38, v39\n\t" \
    "v_max3_u32 v160, v160, v40, v41\n\t" \
    "v_max3_u32 v160, v160, v42, v43\n\t" \
    "v_max3_u32 v160, v160, v44, v45\n\t" \
    "v_max3_u32 v160, v160, v46, v47\n\t" \
    "v_max3_u32 v160, v160, v48, v49\n\t" \
    "v_max3_u32 v160, v160, v50, v51\n\t" \
    "v_max3_u32 v160, v160, v52, v53\n\t" \
    "v_max3_u32 v160, v160, v54, v55\n\t" \
    "v_max3_u32 v160, v160, v56, v57\n\t" \
    "v_max3_u32 v160, v160, v58, v59\n\t" \
    "v_max3_u32 v160, v160, v60, v61\n\t" \
    "v_max3_u32 v160, v160, v62, v63\n\t" \
    "v_max3_u32 v160, v160, v64, v65\n\t" \
    "v_max3_u32 v160, v160, v66, v67\n\t" \
    "v_max3_u32 v160, v160, v68, v69\n\t" \
    "v_max3_u32 v160, v160, v70, v71\n\t" \
    "v_cmp_eq_u32 vcc, -1, v160\n\t" \
    "s_cbranch_vccz qk_Gback46_%=\n\t" \
    "v_sub_u32 v160, v1, v3\n\t" \
    "s_nop 0\n\t" \
    "v_readfirstlane_b32 s69, v160\n\t" \
    "s_nop 7\n\t" \
    "s_add_u32 s30, s46, s69\n\t" \
    "s_addc_u32 s31, s47, 0\n\t" \
    "qk_Gag56_%=:\n\t" \
    "global_load_dwordx4 v[36:39], v1, s[46:47] sc1\n\t" \
    "global_load_dwordx4 v[40:43], v23, s[46:47] sc1\n\t" \
    "global_load_dwordx4 v[44:47], v2, s[46:47] sc1\n\t" \
    "global_load_dwordx4 v[48:51], v4, s[30:31] sc1\n\t" \
    "global_load_dwordx4 v[52:55], v5, s[30:31] sc1\n\t" \
    "global_load_dwordx4 v[56:59], v6, s[30:31] sc1\n\t" \
    "global_load_dwordx4 v[60:63], v7, s[30:31] sc1\n\t" \
    "global_load_dwordx4 v[64:67], v8, s[30:31] sc1\n\t" \
    "global_load_dwordx4 v[68:71], v9, s[30:31] sc1\n\t" \
    "s_waitcnt vmcnt(0) lgkmcnt(0)\n\t" \
    "v_max_u32 v160, v36, v37\n\t" \
    "v_max3_u32 v160, v160, v38, v39\n\t" \
    "v_max3_u32 v160, v160, v40, v41\n\t" \
    "v_max3_u32 v160, v160, v42, v43\n\t" \
    "v_max3_u32 v160, v160, v44, v45\n\t" \
    "v_max3_u32 v160, v160, v46, v47\n\t" \
    "v_max3_u32 v160, v160, v48, v49\n\t" \
    "v_max3_u32 v160, v160, v50, v51\n\t" \
    "v_max3_u32 v160, v160, v52, v53\n\t" \
    "v_max3_u32 v160, v160, v54, v55\n\t" \
    "v_max3_u32 v160, v160, v56, v57\n\t" \
    "v_max3_u32 v160, v160, v58, v59\n\t" \
    "v_max3_u32 v160, v160, v60, v61\n\t" \
    "v_max3_u32 v160, v160, v62, v63\n\t" \
    "v_max3_u32 v160, v160, v64, v65\n\t" \
    "v_max3_u32 v160, v160, v66, v67\n\t" \
    "v_max3_u32 v160, v160, v68, v69\n\t" \
    "v_max3_u32 v160, v160, v70, v71\n\t" \
    "v_cmp_eq_u32 vcc, -1, v160\n\t" \
    "s_cbranch_vccz qk_Gredo57_%=\n\t" \
    "s_cmp_eq_u32 s68, 0\n\t" \
    "s_cbranch_scc1 qk_F58_%=\n\t" \
    "s_sub_u32 s68, s68, 1\n\t" \
    "s_sleep 1\n\t" \
    "s_branch qk_Gag56_%=\n\t" \
    "qk_F58_%=:\n\t" \
    "v_mov_b32 v160, 5767424\n\t" \
    "v_mov_b32 v161, 2\n\t" \
    "global_store_dword v160, v161, s[36:37] sc1\n\t" \
    "s_waitcnt vmcnt(0)\n\t" \
    "s_branch qk_Gredo57_%=\n\t" \
    "qk_Gredo57_%=:\n\t" \
    "v_mov_b32 v72, 0\n\t" \
    "v_mov_b32 v73, 0\n\t" \
    "v_mov_b32 v74, 0\n\t" \
    "v_mov_b32 v75, 0\n\t" \
    "v_mov_b32 v76, 0\n\t" \
    "v_mov_b32 v77, 0\n\t" \
    "v_mov_b32 v78, 0\n\t" \
    "v_mov_b32 v79, 0\n\t" \
    "v_mov_b32 v80, 0\n\t" \
    "v_mov_b32 v81, 0\n\t" \
    "v_mov_b32 v82, 0\n\t" \
    "v_mov_b32 v83, 0\n\t" \
    "v_mov_b32 v84, 0\n\t" \
    "v_mov_b32 v85, 0\n\t" \
    "v_mov_b32 v86, 0\n\t" \
    "v_mov_b32 v87, 0\n\t" \
    "v_smfmac_f32_16x16x64_bf16 v[72:75], v[36:39], a[16:23], v10\n\t" \
    "v_smfmac_f32_16x16x64_bf16 v[72:75], v[40:43], a[24:31], v10\n\t" \
    "v_smfmac_f32_16x16x64_bf16 v[72:75], v[44:47], a[64:71], v10\n\t" \
    "v_smfmac_f32_16x16x64_bf16 v[72:75], v[48:51], a[32:39], v10\n\t" \
    "v_smfmac_f32_16x16x64_bf16 v[72:75], v[52:55], a[40:47], v10\n\t" \
    "v_smfmac_f32_16x16x64_bf16 v[72:75], v[56:59], a[48:55], v10\n\t" \
    "v_smfmac_f32_16x16x64_bf16 v[72:75], v[60:63], a[56:63], v10\n\t" \
    "v_smfmac_f32_16x16x64_bf16 v[72:75], v[64:67], a[0:7], v10\n\t" \
    "v_smfmac_f32_16x16x64_bf16 v[72:75], v[68:71], a[8:15], v10\n\t" \
    "v_smfmac_f32_16x16x64_bf16 v[76:79], v[36:39], a[88:95], v10\n\t" \
    "v_smfmac_f32_16x16x64_bf16 v[76:79], v[40:43], a[96:103], v10\n\t" \
    "v_smfmac_f32_16x16x64_bf16 v[76:79], v[44:47], a[136:143], v10\n\t" \
    "v_smfmac_f32_16x16x64_bf16 v[76:79], v[48:51], a[104:111], v10\n\t" \
    "v_smfmac_f32_16x16x64_bf16 v[76:79], v[52:55], a[112:119], v10\n\t" \
    "v_smfmac_f32_16x16x64_bf16 v[76:79], v[56:59], a[120:127], v10\n\t" \
    "v_smfmac_f32_16x16x64_bf16 v[76:79], v[60:63], a[128:135], v10\n\t" \
    "v_smfmac_f32_16x16x64_bf16 v[76:79], v[64:67], a[72:79], v10\n\t" \
    "v_smfmac_f32_16x16x64_bf16 v[76:79], v[68:71], a[80:87], v10\n\t" \
    "v_smfmac_f32_16x16x64_bf16 v[80:83], v[36:39], a[160:167], v10\n\t" \
    "v_smfmac_f32_16x16x64_bf16 v[80:83], v[40:43], a[168:175], v10\n\t" \
    "v_smfmac_f32_16x16x64_bf16 v[80:83], v[44:47], a[208:215], v10\n\t" \
    "v_smfmac_f32_16x16x64_bf16 v[80:83], v[48:51], a[176:183], v10\n\t" \
    "v_smfmac_f32_16x16x64_bf16 v[80:83], v[52:55], a[184:191], v10\n\t" \
    "v_smfmac_f32_16x16x64_bf16 v[80:83], v[56:59], a[192:199], v10\n\t" \
    "v_smfmac_f32_16x16x64_bf16 v[80:83], v[60:63], a[200:207], v10\n\t" \
    "v_smfmac_f32_16x16x64_bf16 v[80:83], v[64:67], a[144:151], v10\n\t" \
    "v_smfmac_f32_16x16x64_bf16 v[80:83], v[68:71], a[152:159], v10\n\t" \
    "v_smfmac_f32_16x16x64_bf16 v[84:87], v[36:39], a[232:239], v10\n\t" \
    "v_smfmac_f32_16x16x64_bf16 v[84:87], v[40:43], a[240:247], v10\n\t" \
    "v_smfmac_f32_16x16x64_bf16 v[84:87], v[44:47], v[248:255], v10\n\t" \
    "v_smfmac_f32_16x16x64_bf16 v[84:87], v[48:51], a[248:255], v10\n\t" \
    "v_smfmac_f32_16x16x64_bf16 v[84:87], v[52:55], v[224:231], v10\n\t" \
    "v_smfmac_f32_16x16x64_bf16 v[84:87], v[56:59], v[232:239], v10\n\t" \
    "v_smfmac_f32_16x16x64_bf16 v[84:87], v[60:63], v[240:247], v10\n\t" \
    "v_smfmac_f32_16x16x64_bf16 v[84:87], v[64:67], a[216:223], v10\n\t" \
    "v_smfmac_f32_16x16x64_bf16 v[84:87], v[68:71], a[224:231], v10\n\t" \
    "v_add_f32 v72, v72, v74\n\t" \
    "v_add_f32 v73, v73, v75\n\t" \
    "v_cvt_pk_bf16_f32 v156, v72, v73\n\t" \
    "ds_write_b32 v12, v156 offset:0\n\t" \
    "v_add_f32 v76, v76, v78\n\t" \
    "v_add_f32 v77, v77, v79\n\t" \
    "v_cvt_pk_bf16_f32 v157, v76, v77\n\t" \
    "ds_write_b32 v12, v157 offset:64\n\t" \
    "v_add_f32 v80, v80, v82\n\t" \
    "v_add_f32 v81, v81, v83\n\t" \
    "v_cvt_pk_bf16_f32 v158, v80, v81\n\t" \
    "ds_write_b32 v16, v158 offset:0\n\t" \
    "s_nop 7\n\t" \
    "s_branch qk_Gdone47_%=\n\t" \
    "qk_last37_%=:\n\t" \
    "s_endpgm\n\t" \
    "qk_loop13_%=:\n\t" \
    "v_mov_b32 v72, 0\n\t" \
    "v_mov_b32 v73, 0\n\t" \
    "v_mov_b32 v74, 0\n\t" \
    "v_mov_b32 v75, 0\n\t" \
    "v_mov_b32 v76, 0\n\t" \
    "v_mov_b32 v77, 0\n\t" \
    "v_mov_b32 v78, 0\n\t" \
    "v_mov_b32 v79, 0\n\t" \
    "v_mov_b32 v80, 0\n\t" \
    "v_mov_b32 v81, 0\n\t" \
    "v_mov_b32 v82, 0\n\t" \
    "v_mov_b32 v83, 0\n\t" \
    "v_mov_b32 v84, 0\n\t" \
    "v_mov_b32 v85, 0\n\t" \
    "v_mov_b32 v86, 0\n\t" \
    "v_mov_b32 v87, 0\n\t" \
    "v_add_u32 v31, 1, v31\n\t" \
    "s_add_u32 s69, s65, 0\n\t" \
    "s_and_b32 s69, s69, 7\n\t" \
    "s_lshl_b32 s69, s69, 18\n\t" \
    "s_add_u32 s69, s69, s62\n\t" \
    "s_add_u32 s46, s36, s69\n\t" \
    "s_addc_u32 s47, s37, 0\n\t" \
    "s_mul_i32 s70, s67, 7\n\t" \
    "s_add_u32 s69, s65, 0\n\t" \
    "s_xor_b32 s69, s69, s70\n\t" \
    "s_and_b32 s69, s69, 63\n\t" \
    "s_lshl_b32 s69, s69, 12\n\t" \
    "s_mul_i32 s70, s67, 262144\n\t" \
    "s_add_u32 s69, s69, s70\n\t" \
    "s_add_u32 s69, s69, 39845888\n\t" \
    "s_add_u32 s40, s36, s69\n\t" \
    "s_addc_u32 s41, s37, 0\n\t" \
    "s_sub_u32 s69, s65, 1\n\t" \
    "v_mov_b32 v25, s69\n\t" \
    "s_sub_u32 s70, s79, s74\n\t" \
    "s_max_i32 s70, s70, 0\n\t" \
    "s_min_i32 s70, s70, 16000\n\t" \
    "s_lshl_b32 s70, s70, 1\n\t" \
    "s_mul_hi_u32 s70, s70, s77\n\t" \
    "s_add_u32 s70, s70, s73\n\t" \
    ".p2align 6\n\t" \
    "s_nop 0\n\t" \
    "s_nop 0\n\t" \
    "s_nop 0\n\t" \
    "s_nop 0\n\t" \
    "s_nop 0\n\t" \
    "s_nop 0\n\t" \
    "qk_go60_%=:\n\t" \
    "s_getpc_b64 s[30:31]\n\t" \
    "qk_Wp63_%=:\n\t" \
    "s_add_u32 s30, s30, qk_We64_%=-qk_Wp63_%=\n\t" \
    "s_addc_u32 s31, s31, 0\n\t" \
    "s_sub_u32 s70, s70, 96\n\t" \
    "qk_W62_%=:\n\t" \
    "s_memtime s[56:57]\n\t" \
    "s_waitcnt lgkmcnt(0)\n\t" \
    "s_sub_u32 s69, s70, s56\n\t" \
    "s_cmp_gt_i32 s69, 80\n\t" \
    "s_cbranch_scc1 qk_W62_%=\n\t" \
    "s_max_i32 s69, s69, 0\n\t" \
    "s_and_b32 s69, s69, -4\n\t" \
    "s_sub_u32 s30, s30, s69\n\t" \
    "s_subb_u32 s31, s31, 0\n\t" \
    "s_setpc_b64 s[30:31]\n\t" \
    "s_nop 0\n\t" \
    "s_nop 0\n\t" \
    "s_nop 0\n\t" \
    "s_nop 0\n\t" \
    "s_nop 0\n\t" \
    "s_nop 0\n\t" \
    "s_nop 0\n\t" \
    "s_nop 0\n\t" \
    "s_nop 0\n\t" \
    "s_nop 0\n\t" \
    "s_nop 0\n\t" \
    "s_nop 0\n\t" \
    "s_nop 0\n\t" \
    "s_nop 0\n\t" \
    "s_nop 0\n\t" \
    "s_nop 0\n\t" \
    "s_nop 0\n\t" \
    "s_nop 0\n\t" \
    "s_nop 0\n\t" \
    "s_nop 0\n\t" \
    "qk_We64_%=:\n\t" \
    "qk_G65_%=:\n\t" \
    "global_load_dwordx4 v[36:39], v1, s[46:47] sc1\n\t" \
    "global_load_dwordx4 v[40:43], v23, s[46:47] sc1\n\t" \
    "s_nop 7\n\t" \
    "global_load_dwordx4 v[44:47], v2, s[46:47] sc1\n\t" \
    "s_waitcnt vmcnt(2)\n\t" \
    "v_smfmac_f32_16x16x64_bf16 v[72:75], v[36:39], a[32:39], v10\n\t" \
    "ds_write_b128 v3, v[36:39]\n\t" \
    "v_smfmac_f32_16x16x64_bf16 v[76:79], v[36:39], a[104:111], v10\n\t" \
    "v_smfmac_f32_16x16x64_bf16 v[80:83], v[36:39], a[176:183], v10\n\t" \
    "v_smfmac_f32_16x16x64_bf16 v[84:87], v[36:39], a[248:255], v10\n\t" \
    "s_waitcnt vmcnt(1)\n\t" \
    "v_cmp_u_f32 vcc, v36, v40\n\t" \
    "ds_write_b128 v3, v[40:43] offset:1024\n\t" \
    "v_smfmac_f32_16x16x64_bf16 v[72:75], v[40:43], a[40:47], v10\n\t" \
    "v_smfmac_f32_16x16x64_bf16 v[76:79], v[40:43], a[112:119], v10\n\t" \
    "s_cbranch_vccnz qk_Gbad66_%=\n\t" \
    "qk_Gc67_%=:\n\t" \
    "s_waitcnt lgkmcnt(0)\n\t" \
    "s_barrier\n\t" \
    "ds_read_b128 v[48:51], v4\n\t" \
    "ds_read_b128 v[52:55], v5\n\t" \
    "v_smfmac_f32_16x16x64_bf16 v[80:83], v[40:43], a[184:191], v10\n\t" \
    "ds_read_b128 v[56:59], v6\n\t" \
    "ds_read_b128 v[60:63], v7\n\t" \
    "v_smfmac_f32_16x16x64_bf16 v[84:87], v[40:43], v[224:231], v10\n\t" \
    "s_waitcnt vmcnt(0)\n\t" \
    "v_cmp_eq_u32 vcc, -1, v44\n\t" \
    "ds_read_b128 v[64:67], v8\n\t" \
    "ds_read_b128 v[68:71], v9\n\t" \
    "s_cbranch_vccnz qk_Gbad271_%=\n\t" \
    "qk_Gc272_%=:\n\t" \
    "v_smfmac_f32_16x16x64_bf16 v[72:75], v[44:47], a[64:71], v10\n\t" \
    "v_smfmac_f32_16x16x64_bf16 v[76:79], v[44:47], a[136:143], v10\n\t" \
    "v_smfmac_f32_16x16x64_bf16 v[80:83], v[44:47], a[208:215], v10\n\t" \
    "v_smfmac_f32_16x16x64_bf16 v[84:87], v[44:47], v[248:255], v10\n\t" \
    "s_waitcnt lgkmcnt(5)\n\t" \
    "v_smfmac_f32_16x16x64_bf16 v[72:75], v[48:51], a[48:55], v10\n\t" \
    "s_cmp_lt_u32 s65, 58\n\t" \
    "s_cbranch_scc1 qk_kf61_%=\n\t" \
    "global_store_dword v14, v25, s[36:37] sc1\n\t" \
    "qk_kf61_%=:\n\t" \
    "s_waitcnt lgkmcnt(4)\n\t" \
    "v_smfmac_f32_16x16x64_bf16 v[72:75], v[52:55], a[56:63], v10\n\t" \
    "s_waitcnt lgkmcnt(3)\n\t" \
    "v_smfmac_f32_16x16x64_bf16 v[72:75], v[56:59], a[0:7], v10\n\t" \
    "s_waitcnt lgkmcnt(2)\n\t" \
    "v_smfmac_f32_16x16x64_bf16 v[72:75], v[60:63], a[8:15], v10\n\t" \
    "s_waitcnt lgkmcnt(1)\n\t" \
    "v_smfmac_f32_16x16x64_bf16 v[72:75], v[64:67], a[16:23], v10\n\t" \
    "s_waitcnt lgkmcnt(0)\n\t" \
    "v_smfmac_f32_16x16x64_bf16 v[72:75], v[68:71], a[24:31], v10\n\t" \
    "v_smfmac_f32_16x16x64_bf16 v[76:79], v[48:51], a[120:127], v10\n\t" \
    "s_nop 1\n\t" \
    "s_nop 0\n\t" \
    "v_smfmac_f32_16x16x64_bf16 v[76:79], v[52:55], a[128:135], v10\n\t" \
    "s_nop 1\n\t" \
    "s_nop 0\n\t" \
    "v_smfmac_f32_16x16x64_bf16 v[76:79], v[56:59], a[72:79], v10\n\t" \
    "v_add_f32 v72, v72, v74\n\t" \
    "v_smfmac_f32_16x16x64_bf16 v[76:79], v[60:63], a[80:87], v10\n\t" \
    "v_add_f32 v73, v73, v75\n\t" \
    "v_smfmac_f32_16x16x64_bf16 v[76:79], v[64:67], a[88:95], v10\n\t" \
    "v_cvt_pk_bf16_f32 v156, v72, v73\n\t" \
    "v_smfmac_f32_16x16x64_bf16 v[76:79], v[68:71], a[96:103], v10\n\t" \
    "v_cmp_u_f32 vcc, v72, v73\n\t" \
    "ds_write_b32 v12, v156 offset:0\n\t" \
    "v_smfmac_f32_16x16x64_bf16 v[80:83], v[48:51], a[192:199], v10\n\t" \
    "s_nop 1\n\t" \
    "s_cbranch_vccnz qk_Gslow68_%=\n\t" \
    "qk_Gback69_%=:\n\t" \
    "v_smfmac_f32_16x16x64_bf16 v[80:83], v[52:55], a[200:207], v10\n\t" \
    "s_nop 1\n\t" \
    "s_nop 0\n\t" \
    "v_smfmac_f32_16x16x64_bf16 v[80:83], v[56:59], a[144:151], v10\n\t" \
    "v_add_f32 v76, v76, v78\n\t" \
    "v_smfmac_f32_16x16x64_bf16 v[80:83], v[60:63], a[152:159], v10\n\t" \
    "v_add_f32 v77, v77, v79\n\t" \
    "v_smfmac_f32_16x16x64_bf16 v[80:83], v[64:67], a[160:167], v10\n\t" \
    "v_cvt_pk_bf16_f32 v157, v76, v77\n\t" \
    "v_smfmac_f32_16x16x64_bf16 v[80:83], v[68:71], a[168:175], v10\n\t" \
    "ds_write_b32 v12, v157 offset:64\n\t" \
    "v_smfmac_f32_16x16x64_bf16 v[84:87], v[48:51], v[232:239], v10\n\t" \
    "s_nop 1\n\t" \
    "s_nop 0\n\t" \
    "v_smfmac_f32_16x16x64_bf16 v[84:87], v[52:55], v[240:247], v10\n\t" \
    "s_nop 1\n\t" \
    "s_nop 0\n\t" \
    "v_smfmac_f32_16x16x64_bf16 v[84:87], v[56:59], a[216:223], v10\n\t" \
    "v_add_f32 v80, v80, v82\n\t" \
    "v_smfmac_f32_16x16x64_bf16 v[84:87], v[60:63], a[224:231], v10\n\t" \
    "v_add_f32 v81, v81, v83\n\t" \
    "v_smfmac_f32_16x16x64_bf16 v[84:87], v[64:67], a[232:239], v10\n\t" \
    "v_cvt_pk_bf16_f32 v158, v80, v81\n\t" \
    "v_smfmac_f32_16x16x64_bf16 v[84:87], v[68:71], a[240:247], v10\n\t" \
    "ds_write_b32 v16, v158 offset:0\n\t" \
    "qk_Gdone70_%=:\n\t" \
    "s_nop 6\n\t" \
    "v_add_f32 v84, v84, v86\n\t" \
    "v_add_f32 v85, v85, v87\n\t" \
    "v_cvt_pk_bf16_f32 v159, v84, v85\n\t" \
    "ds_write_b32 v16, v159 offset:256\n\t" \
    "s_waitcnt lgkmcnt(0)\n\t" \
    "s_barrier\n\t" \
    "s_sleep 1\n\t" \
    "ds_read_b64_tr_b16 v[140:141], v17 offset:0\n\t" \
    "ds_read_b64_tr_b16 v[142:143], v17 offset:256\n\t" \
    "ds_read_b64_tr_b16 v[144:145], v17 offset:512\n\t" \
    "ds_read_b64_tr_b16 v[146:147], v17 offset:768\n\t" \
    "ds_read_b64_tr_b16 v[148:149], v17 offset:1024\n\t" \
    "ds_read_b64_tr_b16 v[150:151], v17 offset:1280\n\t" \
    "ds_read_b64_tr_b16 v[152:153], v17 offset:1536\n\t" \
    "ds_read_b64_tr_b16 v[154:155], v17 offset:1792\n\t" \
    "s_waitcnt lgkmcnt(0)\n\t" \
    "s_cmp_eq_u32 s65, 63\n\t" \
    "s_cbranch_scc1 qk_last59_%=\n\t" \
    "s_cmp_ge_u32 s65, 58\n\t" \
    "s_cbranch_scc1 qk_nsl73_%=\n\t" \
    "s_sleep 6\n\t" \
    "global_store_dword v14, v25, s[36:37] sc1\n\t" \
    "qk_nsl73_%=:\n\t" \
    "s_mov_b64 exec, s[58:59]\n\t" \
    "global_store_dwordx4 v0, v[140:143], s[40:41] offset:0 sc1\n\t" \
    "global_store_dwordx4 v0, v[144:147], s[40:41] offset:1024 sc1\n\t" \
    "global_store_dwordx4 v0, v[148:151], s[40:41] offset:2048 sc1\n\t" \
    "global_store_dwordx4 v0, v[152:155], s[40:41] offset:3072 sc1\n\t" \
    "s_mov_b64 exec, -1\n\t" \
    "s_add_u32 s65, s65, 1\n\t" \
    "v_mov_b32 v72, 0\n\t" \
    "v_mov_b32 v73, 0\n\t" \
    "v_mov_b32 v74, 0\n\t" \
    "v_mov_b32 v75, 0\n\t" \
    "v_mov_b32 v76, 0\n\t" \
    "v_mov_b32 v77, 0\n\t" \
    "v_mov_b32 v78, 0\n\t" \
    "v_mov_b32 v79, 0\n\t" \
    "v_mov_b32 v80, 0\n\t" \
    "v_mov_b32 v81, 0\n\t" \
    "v_mov_b32 v82, 0\n\t" \
    "v_mov_b32 v83, 0\n\t" \
    "v_mov_b32 v84, 0\n\t" \
    "v_mov_b32 v85, 0\n\t" \
    "v_mov_b32 v86, 0\n\t" \
    "v_mov_b32 v87, 0\n\t" \
    "v_add_u32 v31, 1, v31\n\t" \
    "s_add_u32 s69, s65, 0\n\t" \
    "s_and_b32 s69, s69, 7\n\t" \
    "s_lshl_b32 s69, s69, 18\n\t" \
    "s_add_u32 s69, s69, s62\n\t" \
    "s_add_u32 s46, s36, s69\n\t" \
    "s_addc_u32 s47, s37, 0\n\t" \
    "s_mul_i32 s70, s67, 7\n\t" \
    "s_add_u32 s69, s65, 0\n\t" \
    "s_xor_b32 s69, s69, s70\n\t" \
    "s_and_b32 s69, s69, 63\n\t" \
    "s_lshl_b32 s69, s69, 12\n\t" \
    "s_mul_i32 s70, s67, 262144\n\t" \
    "s_add_u32 s69, s69, s70\n\t" \
    "s_add_u32 s69, s69, 39845888\n\t" \
    "s_add_u32 s40, s36, s69\n\t" \
    "s_addc_u32 s41, s37, 0\n\t" \
    "s_sub_u32 s69, s65, 1\n\t" \
    "v_mov_b32 v25, s69\n\t" \
    "s_barrier\n\t" \
    "ds_read_b32 v168, v11 offset:20480\n\t" \
    "s_waitcnt lgkmcnt(0)\n\t" \
    "v_readfirstlane_b32 s70, v168\n\t" \
    "s_branch qk_go60_%=\n\t" \
    "qk_Gbad66_%=:\n\t" \
    "v_max_u32 v160, v36, v40\n\t" \
    "v_cmp_eq_u32 vcc, -1, v160\n\t" \
    "s_cbranch_vccz qk_Gc67_%=\n\t" \
    "s_waitcnt vmcnt(0) lgkmcnt(0)\n\t" \
    "s_nop 7\n\t" \
    "v_mov_b32 v72, 0\n\t" \
    "v_mov_b32 v73, 0\n\t" \
    "v_mov_b32 v74, 0\n\t" \
    "v_mov_b32 v75, 0\n\t" \
    "v_mov_b32 v76, 0\n\t" \
    "v_mov_b32 v77, 0\n\t" \
    "v_mov_b32 v78, 0\n\t" \
    "v_mov_b32 v79, 0\n\t" \
    "v_mov_b32 v80, 0\n\t" \
    "v_mov_b32 v81, 0\n\t" \
    "v_mov_b32 v82, 0\n\t" \
    "v_mov_b32 v83, 0\n\t" \
    "v_mov_b32 v84, 0\n\t" \
    "v_mov_b32 v85, 0\n\t" \
    "v_mov_b32 v86, 0\n\t" \
    "v_mov_b32 v87, 0\n\t" \
    "s_cmp_eq_u32 s68, 0\n\t" \
    "s_cbranch_scc1 qk_F74_%=\n\t" \
    "s_sub_u32 s68, s68, 1\n\t" \
    "s_branch qk_G65_%=\n\t" \
    "qk_F74_%=:\n\t" \
    "v_mov_b32 v160, 5767424\n\t" \
    "v_mov_b32 v161, 1\n\t" \
    "global_store_dword v160, v161, s[36:37] sc1\n\t" \
    "s_waitcnt vmcnt(0)\n\t" \
    "s_branch qk_Gc67_%=\n\t" \
    "qk_Gbad271_%=:\n\t" \
    "global_load_dwordx4 v[44:47], v2, s[46:47] sc1\n\t" \
    "s_waitcnt vmcnt(0)\n\t" \
    "v_cmp_eq_u32 vcc, -1, v44\n\t" \
    "s_cbranch_vccz qk_Gc272_%=\n\t" \
    "s_cmp_eq_u32 s68, 0\n\t" \
    "s_cbranch_scc1 qk_F75_%=\n\t" \
    "s_sub_u32 s68, s68, 1\n\t" \
    "s_sleep 1\n\t" \
    "s_branch qk_Gbad271_%=\n\t" \
    "qk_F75_%=:\n\t" \
    "v_mov_b32 v160, 5767424\n\t" \
    "v_mov_b32 v161, 2\n\t" \
    "global_store_dword v160, v161, s[36:37] sc1\n\t" \
    "s_waitcnt vmcnt(0)\n\t" \
    "s_branch qk_Gc272_%=\n\t" \
    "qk_Gslow68_%=:\n\t" \
    "v_max_u32 v160, v36, v37\n\t" \
    "v_max3_u32 v160, v160, v38, v39\n\t" \
    "v_max3_u32 v160, v160, v40, v41\n\t" \
    "v_max3_u32 v160, v160, v42, v43\n\t" \
    "v_max3_u32 v160, v160, v44, v45\n\t" \
    "v_max3_u32 v160, v160, v46, v47\n\t" \
    "v_max3_u32 v160, v160, v48, v49\n\t" \
    "v_max3_u32 v160, v160, v50, v51\n\t" \
    "v_max3_u32 v160, v160, v52, v53\n\t" \
    "v_max3_u32 v160, v160, v54, v55\n\t" \
    "v_max3_u32 v160, v160, v56, v57\n\t" \
    "v_max3_u32 v160, v160, v58, v59\n\t" \
    "v_max3_u32 v160, v160, v60, v61\n\t" \
    "v_max3_u32 v160, v160, v62, v63\n\t" \
    "v_max3_u32 v160, v160, v64, v65\n\t" \
    "v_max3_u32 v160, v160, v66, v67\n\t" \
    "v_max3_u32 v160, v160, v68, v69\n\t" \
    "v_max3_u32 v160, v160, v70, v71\n\t" \
    "v_cmp_eq_u32 vcc, -1, v160\n\t" \
    "s_cbranch_vccz qk_Gback69_%=\n\t" \
    "v_sub_u32 v160, v1, v3\n\t" \
    "s_nop 0\n\t" \
    "v_readfirstlane_b32 s69, v160\n\t" \
    "s_nop 7\n\t" \
    "s_add_u32 s30, s46, s69\n\t" \
    "s_addc_u32 s31, s47, 0\n\t" \
    "qk_Gag76_%=:\n\t" \
    "global_load_dwordx4 v[36:39], v1, s[46:47] sc1\n\t" \
    "global_load_dwordx4 v[40:43], v23, s[46:47] sc1\n\t" \
    "global_load_dwordx4 v[44:47], v2, s[46:47] sc1\n\t" \
    "global_load_dwordx4 v[48:51], v4, s[30:31] sc1\n\t" \
    "global_load_dwordx4 v[52:55], v5, s[30:31] sc1\n\t" \
    "global_load_dwordx4 v[56:59], v6, s[30:31] sc1\n\t" \
    "global_load_dwordx4 v[60:63], v7, s[30:31] sc1\n\t" \
    "global_load_dwordx4 v[64:67], v8, s[30:31] sc1\n\t" \
    "global_load_dwordx4 v[68:71], v9, s[30:31] sc1\n\t" \
    "s_waitcnt vmcnt(0) lgkmcnt(0)\n\t" \
    "v_max_u32 v160, v36, v37\n\t" \
    "v_max3_u32 v160, v160, v38, v39\n\t" \
    "v_max3_u32 v160, v160, v40, v41\n\t" \
    "v_max3_u32 v160, v160, v42, v43\n\t" \
    "v_max3_u32 v160, v160, v44, v45\n\t" \
    "v_max3_u32 v160, v160, v46, v47\n\t" \
    "v_max3_u32 v160, v160, v48, v49\n\t" \
    "v_max3_u32 v160, v160, v50, v51\n\t" \
    "v_max3_u32 v160, v160, v52, v53\n\t" \
    "v_max3_u32 v160, v160, v54, v55\n\t" \
    "v_max3_u32 v160, v160, v56, v57\n\t" \
    "v_max3_u32 v160, v160, v58, v59\n\t" \
    "v_max3_u32 v160, v160, v60, v61\n\t" \
    "v_max3_u32 v160, v160, v62, v63\n\t" \
    "v_max3_u32 v160, v160, v64, v65\n\t" \
    "v_max3_u32 v160, v160, v66, v67\n\t" \
    "v_max3_u32 v160, v160, v68, v69\n\t" \
    "v_max3_u32 v160, v160, v70, v71\n\t" \
    "v_cmp_eq_u32 vcc, -1, v160\n\t" \
    "s_cbranch_vccz qk_Gredo77_%=\n\t" \
    "s_cmp_eq_u32 s68, 0\n\t" \
    "s_cbranch_scc1 qk_F78_%=\n\t" \
    "s_sub_u32 s68, s68, 1\n\t" \
    "s_sleep 1\n\t" \
    "s_branch qk_Gag76_%=\n\t" \
    "qk_F78_%=:\n\t" \
    "v_mov_b32 v160, 5767424\n\t" \
    "v_mov_b32 v161, 2\n\t" \
    "global_store_dword v160, v161, s[36:37] sc1\n\t" \
    "s_waitcnt vmcnt(0)\n\t" \
    "s_branch qk_Gredo77_%=\n\t" \
    "qk_Gredo77_%=:\n\t" \
    "v_mov_b32 v72, 0\n\t" \
    "v_mov_b32 v73, 0\n\t" \
    "v_mov_b32 v74, 0\n\t" \
    "v_mov_b32 v75, 0\n\t" \
    "v_mov_b32 v76, 0\n\t" \
    "v_mov_b32 v77, 0\n\t" \
    "v_mov_b32 v78, 0\n\t" \
    "v_mov_b32 v79, 0\n\t" \
    "v_mov_b32 v80, 0\n\t" \
    "v_mov_b32 v81, 0\n\t" \
    "v_mov_b32 v82, 0\n\t" \
    "v_mov_b32 v83, 0\n\t" \
    "v_mov_b32 v84, 0\n\t" \
    "v_mov_b32 v85, 0\n\t" \
    "v_mov_b32 v86, 0\n\t" \
    "v_mov_b32 v87, 0\n\t" \
    "v_smfmac_f32_16x16x64_bf16 v[72:75], v[36:39], a[32:39], v10\n\t" \
    "v_smfmac_f32_16x16x64_bf16 v[72:75], v[40:43], a[40:47], v10\n\t" \
    "v_smfmac_f32_16x16x64_bf16 v[72:75], v[44:47], a[64:71], v10\n\t" \
    "v_smfmac_f32_16x16x64_bf16 v[72:75], v[48:51], a[48:55], v10\n\t" \
    "v_smfmac_f32_16x16x64_bf16 v[72:75], v[52:55], a[56:63], v10\n\t" \
    "v_smfmac_f32_16x16x64_bf16 v[72:75], v[56:59], a[0:7], v10\n\t" \
    "v_smfmac_f32_16x16x64_bf16 v[72:75], v[60:63], a[8:15], v10\n\t" \
    "v_smfmac_f32_16x16x64_bf16 v[72:75], v[64:67], a[16:23], v10\n\t" \
    "v_smfmac_f32_16x16x64_bf16 v[72:75], v[68:71], a[24:31], v10\n\t" \
    "v_smfmac_f32_16x16x64_bf16 v[76:79], v[36:39], a[104:111], v10\n\t" \
    "v_smfmac_f32_16x16x64_bf16 v[76:79], v[40:43], a[112:119], v10\n\t" \
    "v_smfmac_f32_16x16x64_bf16 v[76:79], v[44:47], a[136:143], v10\n\t" \
    "v_smfmac_f32_16x16x64_bf16 v[76:79], v[48:51], a[120:127], v10\n\t" \
    "v_smfmac_f32_16x16x64_bf16 v[76:79], v[52:55], a[128:135], v10\n\t" \
    "v_smfmac_f32_16x16x64_bf16 v[76:79], v[56:59], a[72:79], v10\n\t" \
    "v_smfmac_f32_16x16x64_bf16 v[76:79], v[60:63], a[80:87], v10\n\t" \
    "v_smfmac_f32_16x16x64_bf16 v[76:79], v[64:67], a[88:95], v10\n\t" \
    "v_smfmac_f32_16x16x64_bf16 v[76:79], v[68:71], a[96:103], v10\n\t" \
    "v_smfmac_f32_16x16x64_bf16 v[80:83], v[36:39], a[176:183], v10\n\t" \
    "v_smfmac_f32_16x16x64_bf16 v[80:83], v[40:43], a[184:191], v10\n\t" \
    "v_smfmac_f32_16x16x64_bf16 v[80:83], v[44:47], a[208:215], v10\n\t" \
    "v_smfmac_f32_16x16x64_bf16 v[80:83], v[48:51], a[192:199], v10\n\t" \
    "v_smfmac_f32_16x16x64_bf16 v[80:83], v[52:55], a[200:207], v10\n\t" \
    "v_smfmac_f32_16x16x64_bf16 v[80:83], v[56:59], a[144:151], v10\n\t" \
    "v_smfmac_f32_16x16x64_bf16 v[80:83], v[60:63], a[152:159], v10\n\t" \
    "v_smfmac_f32_16x16x64_bf16 v[80:83], v[64:67], a[160:167], v10\n\t" \
    "v_smfmac_f32_16x16x64_bf16 v[80:83], v[68:71], a[168:175], v10\n\t" \
    "v_smfmac_f32_16x16x64_bf16 v[84:87], v[36:39], a[248:255], v10\n\t" \
    "v_smfmac_f32_16x16x64_bf16 v[84:87], v[40:43], v[224:231], v10\n\t" \
    "v_smfmac_f32_16x16x64_bf16 v[84:87], v[44:47], v[248:255], v10\n\t" \
    "v_smfmac_f32_16x16x64_bf16 v[84:87], v[48:51], v[232:239], v10\n\t" \
    "v_smfmac_f32_16x16x64_bf16 v[84:87], v[52:55], v[240:247], v10\n\t" \
    "v_smfmac_f32_16x16x64_bf16 v[84:87], v[56:59], a[216:223], v10\n\t" \
    "v_smfmac_f32_16x16x64_bf16 v[84:87], v[60:63], a[224:231], v10\n\t" \
    "v_smfmac_f32_16x16x64_bf16 v[84:87], v[64:67], a[232:239], v10\n\t" \
    "v_smfmac_f32_16x16x64_bf16 v[84:87], v[68:71], a[240:247], v10\n\t" \
    "v_add_f32 v72, v72, v74\n\t" \
    "v_add_f32 v73, v73, v75\n\t" \
    "v_cvt_pk_bf16_f32 v156, v72, v73\n\t" \
    "ds_write_b32 v12, v156 offset:0\n\t" \
    "v_add_f32 v76, v76, v78\n\t" \
    "v_add_f32 v77, v77, v79\n\t" \
    "v_cvt_pk_bf16_f32 v157, v76, v77\n\t" \
    "ds_write_b32 v12, v157 offset:64\n\t" \
    "v_add_f32 v80, v80, v82\n\t" \
    "v_add_f32 v81, v81, v83\n\t" \
    "v_cvt_pk_bf16_f32 v158, v80, v81\n\t" \
    "ds_write_b32 v16, v158 offset:0\n\t" \
    "s_nop 7\n\t" \
    "s_branch qk_Gdone70_%=\n\t" \
    "qk_last59_%=:\n\t" \
    "s_mov_b64 exec, s[58:59]\n\t" \
    "global_store_dwordx4 v0, v[140:143], s[40:41] offset:0 sc1\n\t" \
    "global_store_dwordx4 v0, v[144:147], s[40:41] offset:1024 sc1\n\t" \
    "global_store_dwordx4 v0, v[148:151], s[40:41] offset:2048 sc1\n\t" \
    "global_store_dwordx4 v0, v[152:155], s[40:41] offset:3072 sc1\n\t" \
    "s_mov_b64 exec, -1\n\t" \
    "s_endpgm\n\t" \
    "qk_loop14_%=:\n\t" \
    "v_mov_b32 v72, 0\n\t" \
    "v_mov_b32 v73, 0\n\t" \
    "v_mov_b32 v74, 0\n\t" \
    "v_mov_b32 v75, 0\n\t" \
    "v_mov_b32 v76, 0\n\t" \
    "v_mov_b32 v77, 0\n\t" \
    "v_mov_b32 v78, 0\n\t" \
    "v_mov_b32 v79, 0\n\t" \
    "v_mov_b32 v80, 0\n\t" \
    "v_mov_b32 v81, 0\n\t" \
    "v_mov_b32 v82, 0\n\t" \
    "v_mov_b32 v83, 0\n\t" \
    "v_mov_b32 v84, 0\n\t" \
    "v_mov_b32 v85, 0\n\t" \
    "v_mov_b32 v86, 0\n\t" \
    "v_mov_b32 v87, 0\n\t" \
    "v_add_u32 v31, 1, v31\n\t" \
    "s_add_u32 s69, s65, 0\n\t" \
    "s_and_b32 s69, s69, 7\n\t" \
    "s_lshl_b32 s69, s69, 18\n\t" \
    "s_add_u32 s69, s69, s62\n\t" \
    "s_add_u32 s46, s36, s69\n\t" \
    "s_addc_u32 s47, s37, 0\n\t" \
    "s_and_b32 s69, s65, 7\n\t" \
    "s_lshl_b32 s69, s69, 18\n\t" \
    "s_add_u32 s69, s69, s63\n\t" \
    "s_add_u32 s52, s36, s69\n\t" \
    "s_addc_u32 s53, s37, 0\n\t" \
    "s_sub_u32 s70, s79, s74\n\t" \
    "s_max_i32 s70, s70, 0\n\t" \
    "s_min_i32 s70, s70, 16000\n\t" \
    "s_lshl_b32 s70, s70, 1\n\t" \
    "s_mul_hi_u32 s70, s70, s77\n\t" \
    "s_add_u32 s70, s70, s73\n\t" \
    ".p2align 6\n\t" \
    "s_nop 0\n\t" \
    "s_nop 0\n\t" \
    "s_nop 0\n\t" \
    "s_nop 0\n\t" \
    "s_nop 0\n\t" \
    "s_nop 0\n\t" \
    "qk_go80_%=:\n\t" \
    "s_getpc_b64 s[30:31]\n\t" \
    "qk_Wp82_%=:\n\t" \
    "s_add_u32 s30, s30, qk_We83_%=-qk_Wp82_%=\n\t" \
    "s_addc_u32 s31, s31, 0\n\t" \
    "s_sub_u32 s70, s70, 96\n\t" \
    "qk_W81_%=:\n\t" \
    "s_memtime s[56:57]\n\t" \
    "s_waitcnt lgkmcnt(0)\n\t" \
    "s_sub_u32 s69, s70, s56\n\t" \
    "s_cmp_gt_i32 s69, 80\n\t" \
    "s_cbranch_scc1 qk_W81_%=\n\t" \
    "s_max_i32 s69, s69, 0\n\t" \
    "s_and_b32 s69, s69, -4\n\t" \
    "s_sub_u32 s30, s30, s69\n\t" \
    "s_subb_u32 s31, s31, 0\n\t" \
    "s_setpc_b64 s[30:31]\n\t" \
    "s_nop 0\n\t" \
    "s_nop 0\n\t" \
    "s_nop 0\n\t" \
    "s_nop 0\n\t" \
    "s_nop 0\n\t" \
    "s_nop 0\n\t" \
    "s_nop 0\n\t" \
    "s_nop 0\n\t" \
    "s_nop 0\n\t" \
    "s_nop 0\n\t" \
    "s_nop 0\n\t" \
    "s_nop 0\n\t" \
    "s_nop 0\n\t" \
    "s_nop 0\n\t" \
    "s_nop 0\n\t" \
    "s_nop 0\n\t" \
    "s_nop 0\n\t" \
    "s_nop 0\n\t" \
    "s_nop 0\n\t" \
    "s_nop 0\n\t" \
    "qk_We83_%=:\n\t" \
    "qk_G84_%=:\n\t" \
    "global_load_dwordx4 v[36:39], v1, s[46:47] sc1\n\t" \
    "global_load_dwordx4 v[40:43], v23, s[46:47] sc1\n\t" \
    "s_mul_hi_u32 s69, s84, s78\n\t" \
    "s_add_u32 s33, s74, s69\n\t" \
    "s_cmp_lt_u32 s65, 9\n\t" \
    "s_cselect_b32 s69, 160, 0\n\t" \
    "s_add_u32 s33, s33, s69\n\t" \
    "s_lshr_b32 s69, s90, 16\n\t" \
    "s_add_u32 s34, s33, s69\n\t" \
    "s_and_b32 s69, s90, 0xffff\n\t" \
    "s_sub_u32 s35, s80, s74\n\t" \
    "s_sub_u32 s35, s35, s69\n\t" \
    "s_add_u32 s99, s35, 240\n\t" \
    "s_add_u32 s98, s33, 400\n\t" \
    "global_load_dwordx4 v[44:47], v2, s[46:47] sc1\n\t" \
    "s_waitcnt vmcnt(2)\n\t" \
    "v_smfmac_f32_16x16x64_bf16 v[72:75], v[36:39], a[48:55], v10\n\t" \
    "ds_write_b128 v3, v[36:39]\n\t" \
    "v_smfmac_f32_16x16x64_bf16 v[76:79], v[36:39], a[120:127], v10\n\t" \
    "v_smfmac_f32_16x16x64_bf16 v[80:83], v[36:39], a[192:199], v10\n\t" \
    "v_smfmac_f32_16x16x64_bf16 v[84:87], v[36:39], v[232:239], v10\n\t" \
    "s_waitcnt vmcnt(1)\n\t" \
    "v_cmp_u_f32 vcc, v36, v40\n\t" \
    "ds_write_b128 v3, v[40:43] offset:1024\n\t" \
    "v_smfmac_f32_16x16x64_bf16 v[72:75], v[40:43], a[56:63], v10\n\t" \
    "v_smfmac_f32_16x16x64_bf16 v[76:79], v[40:43], a[128:135], v10\n\t" \
    "s_cbranch_vccnz qk_Gbad85_%=\n\t" \
    "qk_Gc86_%=:\n\t" \
    "s_waitcnt lgkmcnt(0)\n\t" \
    "s_barrier\n\t" \
    "s_memtime s[56:57]\n\t" \
    "ds_read_b128 v[48:51], v4\n\t" \
    "ds_read_b128 v[52:55], v5\n\t" \
    "v_smfmac_f32_16x16x64_bf16 v[80:83], v[40:43], a[200:207], v10\n\t" \
    "ds_read_b128 v[56:59], v6\n\t" \
    "ds_read_b128 v[60:63], v7\n\t" \
    "v_smfmac_f32_16x16x64_bf16 v[84:87], v[40:43], v[240:247], v10\n\t" \
    "s_waitcnt vmcnt(0)\n\t" \
    "v_cmp_eq_u32 vcc, -1, v44\n\t" \
    "ds_read_b128 v[64:67], v8\n\t" \
    "ds_read_b128 v[68:71], v9\n\t" \
    "s_cbranch_vccnz qk_Gbad290_%=\n\t" \
    "qk_Gc291_%=:\n\t" \
    "v_smfmac_f32_16x16x64_bf16 v[72:75], v[44:47], a[64:71], v10\n\t" \
    "v_smfmac_f32_16x16x64_bf16 v[76:79], v[44:47], a[136:143], v10\n\t" \
    "s_sub_u32 s69, s56, s73\n\t" \
    "s_mul_hi_u32 s69, s69, s78\n\t" \
    "s_cmp_gt_i32 s69, s35\n\t" \
    "v_smfmac_f32_16x16x64_bf16 v[80:83], v[44:47], a[208:215], v10\n\t" \
    "s_cselect_b32 s70, s34, s33\n\t" \
    "s_cmp_gt_i32 s69, s99\n\t" \
    "s_cselect_b32 s70, s98, s70\n\t" \
    "v_smfmac_f32_16x16x64_bf16 v[84:87], v[44:47], v[248:255], v10\n\t" \
    "s_add_u32 s70, s69, s70\n\t" \
    "v_and_b32_e64 v162, -2, s70\n\t" \
    "s_waitcnt lgkmcnt(5)\n\t" \
    "v_smfmac_f32_16x16x64_bf16 v[72:75], v[48:51], a[0:7], v10\n\t" \
    "global_store_dword v21, v162, s[52:53] sc1\n\t" \
    "s_waitcnt lgkmcnt(4)\n\t" \
    "v_smfmac_f32_16x16x64_bf16 v[72:75], v[52:55], a[8:15], v10\n\t" \
    "s_waitcnt lgkmcnt(3)\n\t" \
    "v_smfmac_f32_16x16x64_bf16 v[72:75], v[56:59], a[16:23], v10\n\t" \
    "s_waitcnt lgkmcnt(2)\n\t" \
    "v_smfmac_f32_16x16x64_bf16 v[72:75], v[60:63], a[24:31], v10\n\t" \
    "s_waitcnt lgkmcnt(1)\n\t" \
    "v_smfmac_f32_16x16x64_bf16 v[72:75], v[64:67], a[32:39], v10\n\t" \
    "s_waitcnt lgkmcnt(0)\n\t" \
    "v_smfmac_f32_16x16x64_bf16 v[72:75], v[68:71], a[40:47], v10\n\t" \
    "v_smfmac_f32_16x16x64_bf16 v[76:79], v[48:51], a[72:79], v10\n\t" \
    "s_nop 1\n\t" \
    "s_nop 0\n\t" \
    "v_smfmac_f32_16x16x64_bf16 v[76:79], v[52:55], a[80:87], v10\n\t" \
    "s_nop 1\n\t" \
    "s_nop 0\n\t" \
    "v_smfmac_f32_16x16x64_bf16 v[76:79], v[56:59], a[88:95], v10\n\t" \
    "v_add_f32 v72, v72, v74\n\t" \
    "v_smfmac_f32_16x16x64_bf16 v[76:79], v[60:63], a[96:103], v10\n\t" \
    "v_add_f32 v73, v73, v75\n\t" \
    "v_smfmac_f32_16x16x64_bf16 v[76:79], v[64:67], a[104:111], v10\n\t" \
    "v_cvt_pk_bf16_f32 v156, v72, v73\n\t" \
    "v_smfmac_f32_16x16x64_bf16 v[76:79], v[68:71], a[112:119], v10\n\t" \
    "v_cmp_u_f32 vcc, v72, v73\n\t" \
    "ds_write_b32 v12, v156 offset:0\n\t" \
    "v_smfmac_f32_16x16x64_bf16 v[80:83], v[48:51], a[144:151], v10\n\t" \
    "s_nop 1\n\t" \
    "s_cbranch_vccnz qk_Gslow87_%=\n\t" \
    "qk_Gback88_%=:\n\t" \
    "v_smfmac_f32_16x16x64_bf16 v[80:83], v[52:55], a[152:159], v10\n\t" \
    "s_nop 1\n\t" \
    "s_nop 0\n\t" \
    "v_smfmac_f32_16x16x64_bf16 v[80:83], v[56:59], a[160:167], v10\n\t" \
    "v_add_f32 v76, v76, v78\n\t" \
    "v_smfmac_f32_16x16x64_bf16 v[80:83], v[60:63], a[168:175], v10\n\t" \
    "v_add_f32 v77, v77, v79\n\t" \
    "v_smfmac_f32_16x16x64_bf16 v[80:83], v[64:67], a[176:183], v10\n\t" \
    "v_cvt_pk_bf16_f32 v157, v76, v77\n\t" \
    "v_smfmac_f32_16x16x64_bf16 v[80:83], v[68:71], a[184:191], v10\n\t" \
    "ds_write_b32 v12, v157 offset:64\n\t" \
    "v_smfmac_f32_16x16x64_bf16 v[84:87], v[48:51], a[216:223], v10\n\t" \
    "s_nop 1\n\t" \
    "s_nop 0\n\t" \
    "v_smfmac_f32_16x16x64_bf16 v[84:87], v[52:55], a[224:231], v10\n\t" \
    "s_nop 1\n\t" \
    "s_nop 0\n\t" \
    "v_smfmac_f32_16x16x64_bf16 v[84:87], v[56:59], a[232:239], v10\n\t" \
    "v_add_f32 v80, v80, v82\n\t" \
    "v_smfmac_f32_16x16x64_bf16 v[84:87], v[60:63], a[240:247], v10\n\t" \
    "v_add_f32 v81, v81, v83\n\t" \
    "v_smfmac_f32_16x16x64_bf16 v[84:87], v[64:67], a[248:255], v10\n\t" \
    "v_cvt_pk_bf16_f32 v158, v80, v81\n\t" \
    "v_smfmac_f32_16x16x64_bf16 v[84:87], v[68:71], v[224:231], v10\n\t" \
    "ds_write_b32 v16, v158 offset:0\n\t" \
    "qk_Gdone89_%=:\n\t" \
    "s_nop 6\n\t" \
    "v_add_f32 v84, v84, v86\n\t" \
    "v_add_f32 v85, v85, v87\n\t" \
    "v_cvt_pk_bf16_f32 v159, v84, v85\n\t" \
    "ds_write_b32 v16, v159 offset:256\n\t" \
    "s_waitcnt lgkmcnt(0)\n\t" \
    "s_barrier\n\t" \
    "s_cmp_eq_u32 s65, 63\n\t" \
    "s_cbranch_scc1 qk_last79_%=\n\t" \
    "s_cmp_lg_u32 s65, 0\n\t" \
    "s_cbranch_scc1 qk_ng92_%=\n\t" \
    "v_mov_b32 v168, 5838848\n\t" \
    "v_mov_b32 v169, 1\n\t" \
    "s_mov_b64 exec, 1\n\t" \
    "global_atomic_add v170, v168, v169, s[36:37] sc0 sc1\n\t" \
    "s_mov_b64 exec, -1\n\t" \
    "v_mbcnt_lo_u32_b32 v168, -1, 0\n\t" \
    "v_and_b32 v169, 3, v168\n\t" \
    "v_lshlrev_b32 v169, 16, v169\n\t" \
    "v_bfe_u32 v171, v168, 2, 2\n\t" \
    "v_lshl_add_u32 v169, v171, 11, v169\n\t" \
    "v_bfe_u32 v171, v168, 4, 1\n\t" \
    "v_lshl_add_u32 v169, v171, 14, v169\n\t" \
    "v_add_u32 v169, s63, v169\n\t" \
    "s_waitcnt vmcnt(0)\n\t" \
    "v_readfirstlane_b32 s69, v170\n\t" \
    "s_nop 1\n\t" \
    "s_cmp_lg_u32 s69, 126\n\t" \
    "s_cbranch_scc1 qk_go93_%=\n\t" \
    "s_memrealtime s[56:57]\n\t" \
    "s_waitcnt lgkmcnt(0)\n\t" \
    "s_lshl_b32 s69, s56, 4\n\t" \
    "s_add_u32 s69, s69, 2400\n\t" \
    "v_mov_b32 v168, s69\n\t" \
    "global_store_dword v169, v168, s[36:37] sc1\n\t" \
    "qk_go93_%=:\n\t" \
    "v_mov_b32 v21, v33\n\t" \
    "qk_ng92_%=:\n\t" \
    "s_sub_u32 s70, s80, s74\n\t" \
    "s_max_i32 s70, s70, 0\n\t" \
    "s_min_i32 s70, s70, 16000\n\t" \
    "s_lshl_b32 s70, s70, 1\n\t" \
    "s_mul_hi_u32 s70, s70, s77\n\t" \
    "s_add_u32 s70, s70, s73\n\t" \
    "qk_W94_%=:\n\t" \
    "s_memtime s[56:57]\n\t" \
    "s_waitcnt lgkmcnt(0)\n\t" \
    "s_sub_u32 s69, s70, s56\n\t" \
    "s_cmp_gt_i32 s69, 24\n\t" \
    "s_cbranch_scc1 qk_W94_%=\n\t" \
    "global_load_dword v160, v20, s[52:53] sc1\n\t" \
    "s_waitcnt lgkmcnt(0)\n\t" \
    "s_mov_b32 s70, 116\n\t" \
    "qk_E98_%=:\n\t" \
    "s_memtime s[56:57]\n\t" \
    "s_memrealtime s[2:3]\n\t" \
    "s_nop 0\n\t" \
    "s_memrealtime s[4:5]\n\t" \
    "s_nop 0\n\t" \
    "s_memrealtime s[6:7]\n\t" \
    "s_nop 0\n\t" \
    "s_memrealtime s[8:9]\n\t" \
    "s_nop 0\n\t" \
    "s_memrealtime s[10:11]\n\t" \
    "s_nop 0\n\t" \
    "s_memrealtime s[12:13]\n\t" \
    "s_nop 0\n\t" \
    "s_memrealtime s[14:15]\n\t" \
    "s_nop 0\n\t" \
    "s_memrealtime s[16:17]\n\t" \
    "s_nop 0\n\t" \
    "s_memrealtime s[18:19]\n\t" \
    "s_nop 0\n\t" \
    "s_memrealtime s[20:21]\n\t" \
    "s_nop 0\n\t" \
    "s_memrealtime s[22:23]\n\t" \
    "s_nop 0\n\t" \
    "s_memrealtime s[24:25]\n\t" \
    "s_nop 0\n\t" \
    "s_memrealtime s[26:27]\n\t" \
    "s_nop 0\n\t" \
    "s_memrealtime s[28:29]\n\t" \
    "s_memtime s[54:55]\n\t" \
    "s_add_u32 s70, s70, 8\n\t" \
    "s_waitcnt lgkmcnt(0)\n\t" \
    "s_sub_u32 s69, s54, s56\n\t" \
    "s_cmp_gt_u32 s69, s70\n\t" \
    "s_cbranch_scc1 qk_E98_%=\n\t" \
    "s_add_u32 s2, s2, s4\n\t" \
    "s_add_u32 s6, s6, s8\n\t" \
    "s_add_u32 s10, s10, s12\n\t" \
    "s_add_u32 s14, s14, s16\n\t" \
    "s_add_u32 s18, s18, s20\n\t" \
    "s_add_u32 s22, s22, s24\n\t" \
    "s_add_u32 s2, s2, s6\n\t" \
    "s_add_u32 s10, s10, s14\n\t" \
    "s_add_u32 s18, s18, s22\n\t" \
    "s_add_u32 s2, s2, s26\n\t" \
    "s_add_u32 s10, s10, s18\n\t" \
    "s_add_u32 s2, s2, s10\n\t" \
    "s_mul_i32 s70, s28, 13\n\t" \
    "s_sub_u32 s70, s70, s2\n\t" \
    "s_min_u32 s70, s70, 52\n\t" \
    "s_mul_i32 s69, s69, s70\n\t" \
    "s_mul_hi_u32 s69, s69, 76695844\n\t" \
    "s_mov_b32 s75, s73\n\t" \
    "s_mov_b32 s76, s74\n\t" \
    "s_add_u32 s73, s56, s69\n\t" \
    "s_lshl_b32 s74, s28, 4\n\t" \
    "s_sub_u32 s69, s73, s75\n\t" \
    "s_sub_u32 s70, s74, s76\n\t" \
    "s_sub_u32 s71, s70, 1600\n\t" \
    "s_cmp_lt_u32 s71, 400000\n\t" \
    "s_cbranch_scc0 qk_K99_%=\n\t" \
    "v_cvt_f32_u32 v168, s69\n\t" \
    "v_cvt_f32_u32 v169, s70\n\t" \
    "v_rcp_f32 v170, v168\n\t" \
    "v_rcp_f32 v171, v169\n\t" \
    "v_mul_f32 v170, v170, v169\n\t" \
    "v_mul_f32 v171, v171, v168\n\t" \
    "v_mul_f32 v170, 0x4f800000, v170\n\t" \
    "v_mul_f32 v171, 0x4f000000, v171\n\t" \
    "v_cvt_u32_f32 v170, v170\n\t" \
    "v_cvt_u32_f32 v171, v171\n\t" \
    "v_readfirstlane_b32 s69, v170\n\t" \
    "v_readfirstlane_b32 s70, v171\n\t" \
    "s_nop 1\n\t" \
    "s_min_u32 s71, s65, 2\n\t" \
    "s_sub_u32 s69, s69, s78\n\t" \
    "s_ashr_i32 s69, s69, s71\n\t" \
    "s_add_u32 s78, s78, s69\n\t" \
    "s_sub_u32 s70, s70, s77\n\t" \
    "s_ashr_i32 s70, s70, s71\n\t" \
    "s_add_u32 s77, s77, s70\n\t" \
    "qk_K99_%=:\n\t" \
    "s_add_u32 s65, s65, 1\n\t" \
    "v_mov_b32 v72, 0\n\t" \
    "v_mov_b32 v73, 0\n\t" \
    "v_mov_b32 v74, 0\n\t" \
    "v_mov_b32 v75, 0\n\t" \
    "v_mov_b32 v76, 0\n\t" \
    "v_mov_b32 v77, 0\n\t" \
    "v_mov_b32 v78, 0\n\t" \
    "v_mov_b32 v79, 0\n\t" \
    "v_mov_b32 v80, 0\n\t" \
    "v_mov_b32 v81, 0\n\t" \
    "v_mov_b32 v82, 0\n\t" \
    "v_mov_b32 v83, 0\n\t" \
    "v_mov_b32 v84, 0\n\t" \
    "v_mov_b32 v85, 0\n\t" \
    "v_mov_b32 v86, 0\n\t" \
    "v_mov_b32 v87, 0\n\t" \
    "v_add_u32 v31, 1, v31\n\t" \
    "s_add_u32 s69, s65, 0\n\t" \
    "s_and_b32 s69, s69, 7\n\t" \
    "s_lshl_b32 s69, s69, 18\n\t" \
    "s_add_u32 s69, s69, s62\n\t" \
    "s_add_u32 s46, s36, s69\n\t" \
    "s_addc_u32 s47, s37, 0\n\t" \
    "s_waitcnt vmcnt(0)\n\t" \
    "v_cmp_eq_u32 vcc, -1, v160\n\t" \
    "s_cbranch_vccz qk_Hok96_%=\n\t" \
    "qk_H95_%=:\n\t" \
    "global_load_dword v160, v20, s[52:53] sc1\n\t" \
    "s_waitcnt vmcnt(0)\n\t" \
    "v_cmp_eq_u32 vcc, -1, v160\n\t" \
    "s_cbranch_vccz qk_Hc97_%=\n\t" \
    "s_cmp_eq_u32 s68, 0\n\t" \
    "s_cbranch_scc1 qk_F100_%=\n\t" \
    "s_sub_u32 s68, s68, 1\n\t" \
    "s_sleep 2\n\t" \
    "s_branch qk_H95_%=\n\t" \
    "qk_F100_%=:\n\t" \
    "v_mov_b32 v161, 5767424\n\t" \
    "v_mov_b32 v162, 5\n\t" \
    "global_store_dword v161, v162, s[36:37] sc1\n\t" \
    "s_waitcnt vmcnt(0)\n\t" \
    "s_branch qk_Hc97_%=\n\t" \
    "qk_Hc97_%=:\n\t" \
    "s_memtime s[56:57]\n\t" \
    "s_waitcnt lgkmcnt(0)\n\t" \
    "s_sub_u32 s69, s56, s73\n\t" \
    "s_cmp_lt_u32 s69, 3000\n\t" \
    "s_cbranch_scc1 qk_St101_%=\n\t" \
    "s_waitcnt lgkmcnt(0)\n\t" \
    "s_mov_b32 s70, 116\n\t" \
    "qk_E102_%=:\n\t" \
    "s_memtime s[56:57]\n\t" \
    "s_memrealtime s[2:3]\n\t" \
    "s_nop 0\n\t" \
    "s_memrealtime s[4:5]\n\t" \
    "s_nop 0\n\t" \
    "s_memrealtime s[6:7]\n\t" \
    "s_nop 0\n\t" \
    "s_memrealtime s[8:9]\n\t" \
    "s_nop 0\n\t" \
    "s_memrealtime s[10:11]\n\t" \
    "s_nop 0\n\t" \
    "s_memrealtime s[12:13]\n\t" \
    "s_nop 0\n\t" \
    "s_memrealtime s[14:15]\n\t" \
    "s_nop 0\n\t" \
    "s_memrealtime s[16:17]\n\t" \
    "s_nop 0\n\t" \
    "s_memrealtime s[18:19]\n\t" \
    "s_nop 0\n\t" \
    "s_memrealtime s[20:21]\n\t" \
    "s_nop 0\n\t" \
    "s_memrealtime s[22:23]\n\t" \
    "s_nop 0\n\t" \
    "s_memrealtime s[24:25]\n\t" \
    "s_nop 0\n\t" \
    "s_memrealtime s[26:27]\n\t" \
    "s_nop 0\n\t" \
    "s_memrealtime s[28:29]\n\t" \
    "s_memtime s[54:55]\n\t" \
    "s_add_u32 s70, s70, 8\n\t" \
    "s_waitcnt lgkmcnt(0)\n\t" \
    "s_sub_u32 s69, s54, s56\n\t" \
    "s_cmp_gt_u32 s69, s70\n\t" \
    "s_cbranch_scc1 qk_E102_%=\n\t" \
    "s_add_u32 s2, s2, s4\n\t" \
    "s_add_u32 s6, s6, s8\n\t" \
    "s_add_u32 s10, s10, s12\n\t" \
    "s_add_u32 s14, s14, s16\n\t" \
    "s_add_u32 s18, s18, s20\n\t" \
    "s_add_u32 s22, s22, s24\n\t" \
    "s_add_u32 s2, s2, s6\n\t" \
    "s_add_u32 s10, s10, s14\n\t" \
    "s_add_u32 s18, s18, s22\n\t" \
    "s_add_u32 s2, s2, s26\n\t" \
    "s_add_u32 s10, s10, s18\n\t" \
    "s_add_u32 s2, s2, s10\n\t" \
    "s_mul_i32 s70, s28, 13\n\t" \
    "s_sub_u32 s70, s70, s2\n\t" \
    "s_min_u32 s70, s70, 52\n\t" \
    "s_mul_i32 s69, s69, s70\n\t" \
    "s_mul_hi_u32 s69, s69, 76695844\n\t" \
    "s_mov_b32 s75, s73\n\t" \
    "s_mov_b32 s76, s74\n\t" \
    "s_add_u32 s73, s56, s69\n\t" \
    "s_lshl_b32 s74, s28, 4\n\t" \
    "qk_St101_%=:\n\t" \
    "qk_Hok96_%=:\n\t" \
    "v_max_u32_dpp v160, v160, v160 row_ror:1 row_mask:0xf bank_mask:0xf\n\t" \
    "s_nop 1\n\t" \
    "v_max_u32_dpp v160, v160, v160 row_ror:2 row_mask:0xf bank_mask:0xf\n\t" \
    "s_nop 1\n\t" \
    "v_max_u32_dpp v160, v160, v160 row_ror:4 row_mask:0xf bank_mask:0xf\n\t" \
    "s_nop 1\n\t" \
    "v_max_u32_dpp v160, v160, v160 row_ror:8 row_mask:0xf bank_mask:0xf\n\t" \
    "s_nop 0\n\t" \
    "v_readlane_b32 s69, v160, 0\n\t" \
    "v_readlane_b32 s71, v160, 16\n\t" \
    "s_nop 1\n\t" \
    "s_max_u32 s81, s69, s71\n\t" \
    "s_mul_hi_u32 s69, s85, s78\n\t" \
    "s_add_u32 s79, s81, s69\n\t" \
    "s_add_u32 s79, s79, s87\n\t" \
    "s_cmp_ge_u32 s65, 37\n\t" \
    "s_cselect_b32 s69, 32, 0\n\t" \
    "s_sub_u32 s79, s79, s69\n\t" \
    "s_cmp_lt_u32 s65, 10\n\t" \
    "s_cselect_b32 s69, 64, 0\n\t" \
    "s_add_u32 s79, s79, s69\n\t" \
    "s_add_u32 s80, s79, s88\n\t" \
    "s_sub_u32 s70, s79, s74\n\t" \
    "s_max_i32 s70, s70, 0\n\t" \
    "s_min_i32 s70, s70, 16000\n\t" \
    "s_lshl_b32 s70, s70, 1\n\t" \
    "s_mul_hi_u32 s70, s70, s77\n\t" \
    "s_add_u32 s70, s70, s73\n\t" \
    "v_mov_b32 v168, s70\n\t" \
    "ds_write_b32 v11, v168 offset:20480\n\t" \
    "s_and_b32 s69, s65, 7\n\t" \
    "s_lshl_b32 s69, s69, 18\n\t" \
    "s_add_u32 s69, s69, s63\n\t" \
    "s_add_u32 s52, s36, s69\n\t" \
    "s_addc_u32 s53, s37, 0\n\t" \
    "s_waitcnt lgkmcnt(0)\n\t" \
    "s_barrier\n\t" \
    "s_branch qk_go80_%=\n\t" \
    "qk_Gbad85_%=:\n\t" \
    "v_max_u32 v160, v36, v40\n\t" \
    "v_cmp_eq_u32 vcc, -1, v160\n\t" \
    "s_cbranch_vccz qk_Gc86_%=\n\t" \
    "s_waitcnt vmcnt(0) lgkmcnt(0)\n\t" \
    "s_nop 7\n\t" \
    "v_mov_b32 v72, 0\n\t" \
    "v_mov_b32 v73, 0\n\t" \
    "v_mov_b32 v74, 0\n\t" \
    "v_mov_b32 v75, 0\n\t" \
    "v_mov_b32 v76, 0\n\t" \
    "v_mov_b32 v77, 0\n\t" \
    "v_mov_b32 v78, 0\n\t" \
    "v_mov_b32 v79, 0\n\t" \
    "v_mov_b32 v80, 0\n\t" \
    "v_mov_b32 v81, 0\n\t" \
    "v_mov_b32 v82, 0\n\t" \
    "v_mov_b32 v83, 0\n\t" \
    "v_mov_b32 v84, 0\n\t" \
    "v_mov_b32 v85, 0\n\t" \
    "v_mov_b32 v86, 0\n\t" \
    "v_mov_b32 v87, 0\n\t" \
    "s_cmp_eq_u32 s68, 0\n\t" \
    "s_cbranch_scc1 qk_F103_%=\n\t" \
    "s_sub_u32 s68, s68, 1\n\t" \
    "s_branch qk_G84_%=\n\t" \
    "qk_F103_%=:\n\t" \
    "v_mov_b32 v160, 5767424\n\t" \
    "v_mov_b32 v161, 1\n\t" \
    "global_store_dword v160, v161, s[36:37] sc1\n\t" \
    "s_waitcnt vmcnt(0)\n\t" \
    "s_branch qk_Gc86_%=\n\t" \
    "qk_Gbad290_%=:\n\t" \
    "global_load_dwordx4 v[44:47], v2, s[46:47] sc1\n\t" \
    "s_waitcnt vmcnt(0)\n\t" \
    "v_cmp_eq_u32 vcc, -1, v44\n\t" \
    "s_waitcnt lgkmcnt(0)\n\t" \
    "s_memtime s[56:57]\n\t" \
    "s_waitcnt lgkmcnt(0)\n\t" \
    "s_cbranch_vccz qk_Gc291_%=\n\t" \
    "s_cmp_eq_u32 s68, 0\n\t" \
    "s_cbranch_scc1 qk_F104_%=\n\t" \
    "s_sub_u32 s68, s68, 1\n\t" \
    "s_sleep 1\n\t" \
    "s_branch qk_Gbad290_%=\n\t" \
    "qk_F104_%=:\n\t" \
    "v_mov_b32 v160, 5767424\n\t" \
    "v_mov_b32 v161, 2\n\t" \
    "global_store_dword v160, v161, s[36:37] sc1\n\t" \
    "s_waitcnt vmcnt(0)\n\t" \
    "s_branch qk_Gc291_%=\n\t" \
    "qk_Gslow87_%=:\n\t" \
    "v_max_u32 v160, v36, v37\n\t" \
    "v_max3_u32 v160, v160, v38, v39\n\t" \
    "v_max3_u32 v160, v160, v40, v41\n\t" \
    "v_max3_u32 v160, v160, v42, v43\n\t" \
    "v_max3_u32 v160, v160, v44, v45\n\t" \
    "v_max3_u32 v160, v160, v46, v47\n\t" \
    "v_max3_u32 v160, v160, v48, v49\n\t" \
    "v_max3_u32 v160, v160, v50, v51\n\t" \
    "v_max3_u32 v160, v160, v52, v53\n\t" \
    "v_max3_u32 v160, v160, v54, v55\n\t" \
    "v_max3_u32 v160, v160, v56, v57\n\t" \
    "v_max3_u32 v160, v160, v58, v59\n\t" \
    "v_max3_u32 v160, v160, v60, v61\n\t" \
    "v_max3_u32 v160, v160, v62, v63\n\t" \
    "v_max3_u32 v160, v160, v64, v65\n\t" \
    "v_max3_u32 v160, v160, v66, v67\n\t" \
    "v_max3_u32 v160, v160, v68, v69\n\t" \
    "v_max3_u32 v160, v160, v70, v71\n\t" \
    "v_cmp_eq_u32 vcc, -1, v160\n\t" \
    "s_cbranch_vccz qk_Gback88_%=\n\t" \
    "v_sub_u32 v160, v1, v3\n\t" \
    "s_nop 0\n\t" \
    "v_readfirstlane_b32 s69, v160\n\t" \
    "s_nop 7\n\t" \
    "s_add_u32 s30, s46, s69\n\t" \
    "s_addc_u32 s31, s47, 0\n\t" \
    "qk_Gag105_%=:\n\t" \
    "global_load_dwordx4 v[36:39], v1, s[46:47] sc1\n\t" \
    "global_load_dwordx4 v[40:43], v23, s[46:47] sc1\n\t" \
    "global_load_dwordx4 v[44:47], v2, s[46:47] sc1\n\t" \
    "global_load_dwordx4 v[48:51], v4, s[30:31] sc1\n\t" \
    "global_load_dwordx4 v[52:55], v5, s[30:31] sc1\n\t" \
    "global_load_dwordx4 v[56:59], v6, s[30:31] sc1\n\t" \
    "global_load_dwordx4 v[60:63], v7, s[30:31] sc1\n\t" \
    "global_load_dwordx4 v[64:67], v8, s[30:31] sc1\n\t" \
    "global_load_dwordx4 v[68:71], v9, s[30:31] sc1\n\t" \
    "s_waitcnt vmcnt(0) lgkmcnt(0)\n\t" \
    "v_max_u32 v160, v36, v37\n\t" \
    "v_max3_u32 v160, v160, v38, v39\n\t" \
    "v_max3_u32 v160, v160, v40, v41\n\t" \
    "v_max3_u32 v160, v160, v42, v43\n\t" \
    "v_max3_u32 v160, v160, v44, v45\n\t" \
    "v_max3_u32 v160, v160, v46, v47\n\t" \
    "v_max3_u32 v160, v160, v48, v49\n\t" \
    "v_max3_u32 v160, v160, v50, v51\n\t" \
    "v_max3_u32 v160, v160, v52, v53\n\t" \
    "v_max3_u32 v160, v160, v54, v55\n\t" \
    "v_max3_u32 v160, v160, v56, v57\n\t" \
    "v_max3_u32 v160, v160, v58, v59\n\t" \
    "v_max3_u32 v160, v160, v60, v61\n\t" \
    "v_max3_u32 v160, v160, v62, v63\n\t" \
    "v_max3_u32 v160, v160, v64, v65\n\t" \
    "v_max3_u32 v160, v160, v66, v67\n\t" \
    "v_max3_u32 v160, v160, v68, v69\n\t" \
    "v_max3_u32 v160, v160, v70, v71\n\t" \
    "v_cmp_eq_u32 vcc, -1, v160\n\t" \
    "s_cbranch_vccz qk_Gredo106_%=\n\t" \
    "s_cmp_eq_u32 s68, 0\n\t" \
    "s_cbranch_scc1 qk_F107_%=\n\t" \
    "s_sub_u32 s68, s68, 1\n\t" \
    "s_sleep 1\n\t" \
    "s_branch qk_Gag105_%=\n\t" \
    "qk_F107_%=:\n\t" \
    "v_mov_b32 v160, 5767424\n\t" \
    "v_mov_b32 v161, 2\n\t" \
    "global_store_dword v160, v161, s[36:37] sc1\n\t" \
    "s_waitcnt vmcnt(0)\n\t" \
    "s_branch qk_Gredo106_%=\n\t" \
    "qk_Gredo106_%=:\n\t" \
    "v_mov_b32 v72, 0\n\t" \
    "v_mov_b32 v73, 0\n\t" \
    "v_mov_b32 v74, 0\n\t" \
    "v_mov_b32 v75, 0\n\t" \
    "v_mov_b32 v76, 0\n\t" \
    "v_mov_b32 v77, 0\n\t" \
    "v_mov_b32 v78, 0\n\t" \
    "v_mov_b32 v79, 0\n\t" \
    "v_mov_b32 v80, 0\n\t" \
    "v_mov_b32 v81, 0\n\t" \
    "v_mov_b32 v82, 0\n\t" \
    "v_mov_b32 v83, 0\n\t" \
    "v_mov_b32 v84, 0\n\t" \
    "v_mov_b32 v85, 0\n\t" \
    "v_mov_b32 v86, 0\n\t" \
    "v_mov_b32 v87, 0\n\t" \
    "v_smfmac_f32_16x16x64_bf16 v[72:75], v[36:39], a[48:55], v10\n\t" \
    "v_smfmac_f32_16x16x64_bf16 v[72:75], v[40:43], a[56:63], v10\n\t" \
    "v_smfmac_f32_16x16x64_bf16 v[72:75], v[44:47], a[64:71], v10\n\t" \
    "v_smfmac_f32_16x16x64_bf16 v[72:75], v[48:51], a[0:7], v10\n\t" \
    "v_smfmac_f32_16x16x64_bf16 v[72:75], v[52:55], a[8:15], v10\n\t" \
    "v_smfmac_f32_16x16x64_bf16 v[72:75], v[56:59], a[16:23], v10\n\t" \
    "v_smfmac_f32_16x16x64_bf16 v[72:75], v[60:63], a[24:31], v10\n\t" \
    "v_smfmac_f32_16x16x64_bf16 v[72:75], v[64:67], a[32:39], v10\n\t" \
    "v_smfmac_f32_16x16x64_bf16 v[72:75], v[68:71], a[40:47], v10\n\t" \
    "v_smfmac_f32_16x16x64_bf16 v[76:79], v[36:39], a[120:127], v10\n\t" \
    "v_smfmac_f32_16x16x64_bf16 v[76:79], v[40:43], a[128:135], v10\n\t" \
    "v_smfmac_f32_16x16x64_bf16 v[76:79], v[44:47], a[136:143], v10\n\t" \
    "v_smfmac_f32_16x16x64_bf16 v[76:79], v[48:51], a[72:79], v10\n\t" \
    "v_smfmac_f32_16x16x64_bf16 v[76:79], v[52:55], a[80:87], v10\n\t" \
    "v_smfmac_f32_16x16x64_bf16 v[76:79], v[56:59], a[88:95], v10\n\t" \
    "v_smfmac_f32_16x16x64_bf16 v[76:79], v[60:63], a[96:103], v10\n\t" \
    "v_smfmac_f32_16x16x64_bf16 v[76:79], v[64:67], a[104:111], v10\n\t" \
    "v_smfmac_f32_16x16x64_bf16 v[76:79], v[68:71], a[112:119], v10\n\t" \
    "v_smfmac_f32_16x16x64_bf16 v[80:83], v[36:39], a[192:199], v10\n\t" \
    "v_smfmac_f32_16x16x64_bf16 v[80:83], v[40:43], a[200:207], v10\n\t" \
    "v_smfmac_f32_16x16x64_bf16 v[80:83], v[44:47], a[208:215], v10\n\t" \
    "v_smfmac_f32_16x16x64_bf16 v[80:83], v[48:51], a[144:151], v10\n\t" \
    "v_smfmac_f32_16x16x64_bf16 v[80:83], v[52:55], a[152:159], v10\n\t" \
    "v_smfmac_f32_16x16x64_bf16 v[80:83], v[56:59], a[160:167], v10\n\t" \
    "v_smfmac_f32_16x16x64_bf16 v[80:83], v[60:63], a[168:175], v10\n\t" \
    "v_smfmac_f32_16x16x64_bf16 v[80:83], v[64:67], a[176:183], v10\n\t" \
    "v_smfmac_f32_16x16x64_bf16 v[80:83], v[68:71], a[184:191], v10\n\t" \
    "v_smfmac_f32_16x16x64_bf16 v[84:87], v[36:39], v[232:239], v10\n\t" \
    "v_smfmac_f32_16x16x64_bf16 v[84:87], v[40:43], v[240:247], v10\n\t" \
    "v_smfmac_f32_16x16x64_bf16 v[84:87], v[44:47], v[248:255], v10\n\t" \
    "v_smfmac_f32_16x16x64_bf16 v[84:87], v[48:51], a[216:223], v10\n\t" \
    "v_smfmac_f32_16x16x64_bf16 v[84:87], v[52:55], a[224:231], v10\n\t" \
    "v_smfmac_f32_16x16x64_bf16 v[84:87], v[56:59], a[232:239], v10\n\t" \
    "v_smfmac_f32_16x16x64_bf16 v[84:87], v[60:63], a[240:247], v10\n\t" \
    "v_smfmac_f32_16x16x64_bf16 v[84:87], v[64:67], a[248:255], v10\n\t" \
    "v_smfmac_f32_16x16x64_bf16 v[84:87], v[68:71], v[224:231], v10\n\t" \
    "v_add_f32 v72, v72, v74\n\t" \
    "v_add_f32 v73, v73, v75\n\t" \
    "v_cvt_pk_bf16_f32 v156, v72, v73\n\t" \
    "ds_write_b32 v12, v156 offset:0\n\t" \
    "v_add_f32 v76, v76, v78\n\t" \
    "v_add_f32 v77, v77, v79\n\t" \
    "v_cvt_pk_bf16_f32 v157, v76, v77\n\t" \
    "ds_write_b32 v12, v157 offset:64\n\t" \
    "v_add_f32 v80, v80, v82\n\t" \
    "v_add_f32 v81, v81, v83\n\t" \
    "v_cvt_pk_bf16_f32 v158, v80, v81\n\t" \
    "ds_write_b32 v16, v158 offset:0\n\t" \
    "s_nop 7\n\t" \
    "s_branch qk_Gdone89_%=\n\t" \
    "qk_last79_%=:\n\t" \
    "s_endpgm\n\t" \
    "qk_last_%=:\n\t" \
    ""
#define ASM_V \
    "s_load_dwordx2 s[42:43], %3, 56\n\t" \
    "s_load_dwordx2 s[44:45], %3, 64\n\t" \
    "s_load_dwordx2 s[36:37], %3, 0x58\n\t" \
    "s_load_dwordx2 s[50:51], %3, 0x48\n\t" \
    "s_load_dwordx8 s[84:91], %3, 0\n\t" \
    "s_mov_b32 s66, %4\n\t" \
    "s_mov_b32 s67, %5\n\t" \
    "s_mov_b32 s68, 262144\n\t" \
    "s_mov_b32 s72, 0\n\t" \
    "s_mov_b32 s83, -1\n\t" \
    "s_mov_b32 s65, 0\n\t" \
    "s_lshl_b32 s82, s66, 10\n\t" \
    "s_waitcnt lgkmcnt(0)\n\t" \
    "s_add_u32 s46, s42, 2097152\n\t" \
    "s_addc_u32 s47, s43, 0\n\t" \
    "s_add_u32 m0, s82, 65536\n\t" \
    "s_nop 0\n\t" \
    "global_load_lds_dwordx4 v200, s[46:47]\n\t" \
    "s_add_u32 s46, s42, 2228224\n\t" \
    "s_addc_u32 s47, s43, 0\n\t" \
    "s_add_u32 m0, s82, 69632\n\t" \
    "s_nop 0\n\t" \
    "global_load_lds_dwordx4 v200, s[46:47]\n\t" \
    "s_add_u32 s46, s42, 2359296\n\t" \
    "s_addc_u32 s47, s43, 0\n\t" \
    "s_add_u32 m0, s82, 73728\n\t" \
    "s_nop 0\n\t" \
    "global_load_lds_dwordx4 v200, s[46:47]\n\t" \
    "s_add_u32 s46, s42, 2490368\n\t" \
    "s_addc_u32 s47, s43, 0\n\t" \
    "s_add_u32 m0, s82, 77824\n\t" \
    "s_nop 0\n\t" \
    "global_load_lds_dwordx4 v200, s[46:47]\n\t" \
    "s_add_u32 s46, s42, 2621440\n\t" \
    "s_addc_u32 s47, s43, 0\n\t" \
    "s_add_u32 m0, s82, 81920\n\t" \
    "s_nop 0\n\t" \
    "global_load_lds_dwordx4 v200, s[46:47]\n\t" \
    "s_add_u32 s46, s42, 2752512\n\t" \
    "s_addc_u32 s47, s43, 0\n\t" \
    "s_add_u32 m0, s82, 86016\n\t" \
    "s_nop 0\n\t" \
    "global_load_lds_dwordx4 v200, s[46:47]\n\t" \
    "s_add_u32 s46, s42, 2883584\n\t" \
    "s_addc_u32 s47, s43, 0\n\t" \
    "s_add_u32 m0, s82, 90112\n\t" \
    "s_nop 0\n\t" \
    "global_load_lds_dwordx4 v200, s[46:47]\n\t" \
    "s_add_u32 s46, s42, 3014656\n\t" \
    "s_addc_u32 s47, s43, 0\n\t" \
    "s_add_u32 m0, s82, 94208\n\t" \
    "s_nop 0\n\t" \
    "global_load_lds_dwordx4 v200, s[46:47]\n\t" \
    "s_add_u32 s46, s42, 3145728\n\t" \
    "s_addc_u32 s47, s43, 0\n\t" \
    "s_add_u32 m0, s82, 98304\n\t" \
    "s_nop 0\n\t" \
    "global_load_lds_dwordx4 v200, s[46:47]\n\t" \
    "s_add_u32 s46, s42, 3276800\n\t" \
    "s_addc_u32 s47, s43, 0\n\t" \
    "s_add_u32 m0, s82, 102400\n\t" \
    "s_nop 0\n\t" \
    "global_load_lds_dwordx4 v200, s[46:47]\n\t" \
    "s_add_u32 s46, s42, 3407872\n\t" \
    "s_addc_u32 s47, s43, 0\n\t" \
    "s_add_u32 m0, s82, 106496\n\t" \
    "s_nop 0\n\t" \
    "global_load_lds_dwordx4 v200, s[46:47]\n\t" \
    "s_add_u32 s46, s42, 3538944\n\t" \
    "s_addc_u32 s47, s43, 0\n\t" \
    "s_add_u32 m0, s82, 110592\n\t" \
    "s_nop 0\n\t" \
    "global_load_lds_dwordx4 v200, s[46:47]\n\t" \
    "s_add_u32 s46, s42, 3670016\n\t" \
    "s_addc_u32 s47, s43, 0\n\t" \
    "s_add_u32 m0, s82, 114688\n\t" \
    "s_nop 0\n\t" \
    "global_load_lds_dwordx4 v200, s[46:47]\n\t" \
    "s_add_u32 s46, s42, 3801088\n\t" \
    "s_addc_u32 s47, s43, 0\n\t" \
    "s_add_u32 m0, s82, 118784\n\t" \
    "s_nop 0\n\t" \
    "global_load_lds_dwordx4 v200, s[46:47]\n\t" \
    "s_add_u32 s46, s42, 3932160\n\t" \
    "s_addc_u32 s47, s43, 0\n\t" \
    "s_add_u32 m0, s82, 122880\n\t" \
    "s_nop 0\n\t" \
    "global_load_lds_dwordx4 v200, s[46:47]\n\t" \
    "s_add_u32 s46, s42, 4063232\n\t" \
    "s_addc_u32 s47, s43, 0\n\t" \
    "s_add_u32 m0, s82, 126976\n\t" \
    "s_nop 0\n\t" \
    "global_load_lds_dwordx4 v200, s[46:47]\n\t" \
    "s_add_u32 s46, s42, 4194304\n\t" \
    "s_addc_u32 s47, s43, 0\n\t" \
    "s_add_u32 m0, s82, 131072\n\t" \
    "s_nop 0\n\t" \
    "global_load_lds_dwordx4 v200, s[46:47]\n\t" \
    "s_add_u32 s46, s42, 4325376\n\t" \
    "s_addc_u32 s47, s43, 0\n\t" \
    "s_add_u32 m0, s82, 135168\n\t" \
    "s_nop 0\n\t" \
    "global_load_lds_dwordx4 v200, s[46:47]\n\t" \
    "s_add_u32 s46, s42, 4456448\n\t" \
    "s_addc_u32 s47, s43, 0\n\t" \
    "s_add_u32 m0, s82, 139264\n\t" \
    "s_nop 0\n\t" \
    "global_load_lds_dwordx4 v200, s[46:47]\n\t" \
    "s_add_u32 s46, s42, 4587520\n\t" \
    "s_addc_u32 s47, s43, 0\n\t" \
    "s_add_u32 m0, s82, 143360\n\t" \
    "s_nop 0\n\t" \
    "global_load_lds_dwordx4 v200, s[46:47]\n\t" \
    "s_mov_b32 s77, 3019898880\n\t" \
    "s_mov_b32 s78, 3054198966\n\t" \
    "s_waitcnt lgkmcnt(0)\n\t" \
    "s_mov_b32 s70, 116\n\t" \
    "vv_E1_%=:\n\t" \
    "s_memtime s[56:57]\n\t" \
    "s_memrealtime s[2:3]\n\t" \
    "s_nop 0\n\t" \
    "s_memrealtime s[4:5]\n\t" \
    "s_nop 0\n\t" \
    "s_memrealtime s[6:7]\n\t" \
    "s_nop 0\n\t" \
    "s_memrealtime s[8:9]\n\t" \
    "s_nop 0\n\t" \
    "s_memrealtime s[10:11]\n\t" \
    "s_nop 0\n\t" \
    "s_memrealtime s[12:13]\n\t" \
    "s_nop 0\n\t" \
    "s_memrealtime s[14:15]\n\t" \
    "s_nop 0\n\t" \
    "s_memrealtime s[16:17]\n\t" \
    "s_nop 0\n\t" \
    "s_memrealtime s[18:19]\n\t" \
    "s_nop 0\n\t" \
    "s_memrealtime s[20:21]\n\t" \
    "s_nop 0\n\t" \
    "s_memrealtime s[22:23]\n\t" \
    "s_nop 0\n\t" \
    "s_memrealtime s[24:25]\n\t" \
    "s_nop 0\n\t" \
    "s_memrealtime s[26:27]\n\t" \
    "s_nop 0\n\t" \
    "s_memrealtime s[28:29]\n\t" \
    "s_memtime s[54:55]\n\t" \
    "s_add_u32 s70, s70, 8\n\t" \
    "s_waitcnt lgkmcnt(0)\n\t" \
    "s_sub_u32 s69, s54, s56\n\t" \
    "s_cmp_gt_u32 s69, s70\n\t" \
    "s_cbranch_scc1 vv_E1_%=\n\t" \
    "s_add_u32 s2, s2, s4\n\t" \
    "s_add_u32 s6, s6, s8\n\t" \
    "s_add_u32 s10, s10, s12\n\t" \
    "s_add_u32 s14, s14, s16\n\t" \
    "s_add_u32 s18, s18, s20\n\t" \
    "s_add_u32 s22, s22, s24\n\t" \
    "s_add_u32 s2, s2, s6\n\t" \
    "s_add_u32 s10, s10, s14\n\t" \
    "s_add_u32 s18, s18, s22\n\t" \
    "s_add_u32 s2, s2, s26\n\t" \
    "s_add_u32 s10, s10, s18\n\t" \
    "s_add_u32 s2, s2, s10\n\t" \
    "s_mul_i32 s70, s28, 13\n\t" \
    "s_sub_u32 s70, s70, s2\n\t" \
    "s_min_u32 s70, s70, 52\n\t" \
    "s_mul_i32 s69, s69, s70\n\t" \
    "s_mul_hi_u32 s69, s69, 76695844\n\t" \
    "s_mov_b32 s75, s73\n\t" \
    "s_mov_b32 s76, s74\n\t" \
    "s_add_u32 s73, s56, s69\n\t" \
    "s_lshl_b32 s74, s28, 4\n\t" \
    "s_waitcnt vmcnt(32) lgkmcnt(0)\n\t" \
    "s_barrier\n\t" \
    "ds_read_b64_tr_b16 a[0:1], v201 offset:0\n\t" \
    "ds_read_b64_tr_b16 a[2:3], v201 offset:1024\n\t" \
    "ds_read_b64_tr_b16 a[4:5], v201 offset:8192\n\t" \
    "ds_read_b64_tr_b16 a[6:7], v201 offset:9216\n\t" \
    "ds_read_b64_tr_b16 a[72:73], v202 offset:0\n\t" \
    "ds_read_b64_tr_b16 a[74:75], v202 offset:1024\n\t" \
    "ds_read_b64_tr_b16 a[76:77], v202 offset:8192\n\t" \
    "ds_read_b64_tr_b16 a[78:79], v202 offset:9216\n\t" \
    "s_waitcnt vmcnt(28) lgkmcnt(0)\n\t" \
    "s_barrier\n\t" \
    "ds_read_b64_tr_b16 a[8:9], v201 offset:16384\n\t" \
    "ds_read_b64_tr_b16 a[10:11], v201 offset:17408\n\t" \
    "ds_read_b64_tr_b16 a[12:13], v201 offset:24576\n\t" \
    "ds_read_b64_tr_b16 a[14:15], v201 offset:25600\n\t" \
    "ds_read_b64_tr_b16 a[80:81], v202 offset:16384\n\t" \
    "ds_read_b64_tr_b16 a[82:83], v202 offset:17408\n\t" \
    "ds_read_b64_tr_b16 a[84:85], v202 offset:24576\n\t" \
    "ds_read_b64_tr_b16 a[86:87], v202 offset:25600\n\t" \
    "s_waitcnt vmcnt(24) lgkmcnt(0)\n\t" \
    "s_barrier\n\t" \
    "ds_read_b64_tr_b16 a[16:17], v201 offset:32768\n\t" \
    "ds_read_b64_tr_b16 a[18:19], v201 offset:33792\n\t" \
    "ds_read_b64_tr_b16 a[20:21], v201 offset:40960\n\t" \
    "ds_read_b64_tr_b16 a[22:23], v201 offset:41984\n\t" \
    "ds_read_b64_tr_b16 a[88:89], v202 offset:32768\n\t" \
    "ds_read_b64_tr_b16 a[90:91], v202 offset:33792\n\t" \
    "ds_read_b64_tr_b16 a[92:93], v202 offset:40960\n\t" \
    "ds_read_b64_tr_b16 a[94:95], v202 offset:41984\n\t" \
    "s_waitcnt vmcnt(20) lgkmcnt(0)\n\t" \
    "s_barrier\n\t" \
    "ds_read_b64_tr_b16 a[24:25], v201 offset:49152\n\t" \
    "ds_read_b64_tr_b16 a[26:27], v201 offset:50176\n\t" \
    "ds_read_b64_tr_b16 a[28:29], v201 offset:57344\n\t" \
    "ds_read_b64_tr_b16 a[30:31], v201 offset:58368\n\t" \
    "ds_read_b64_tr_b16 a[96:97], v202 offset:49152\n\t" \
    "ds_read_b64_tr_b16 a[98:99], v202 offset:50176\n\t" \
    "ds_read_b64_tr_b16 a[100:101], v202 offset:57344\n\t" \
    "ds_read_b64_tr_b16 a[102:103], v202 offset:58368\n\t" \
    "s_add_u32 s46, s44, 0\n\t" \
    "s_addc_u32 s47, s45, 0\n\t" \
    "s_mul_i32 s69, s82, 9\n\t" \
    "s_add_u32 m0, s69, 0\n\t" \
    "s_nop 0\n\t" \
    "global_load_lds_dwordx4 v203, s[46:47]\n\t" \
    "s_add_u32 m0, s69, 1024\n\t" \
    "s_nop 0\n\t" \
    "global_load_lds_dwordx4 v204, s[46:47]\n\t" \
    "s_add_u32 m0, s69, 2048\n\t" \
    "s_nop 0\n\t" \
    "global_load_lds_dwordx4 v205, s[46:47]\n\t" \
    "s_add_u32 m0, s69, 3072\n\t" \
    "s_nop 0\n\t" \
    "global_load_lds_dwordx4 v206, s[46:47]\n\t" \
    "s_add_u32 m0, s69, 4096\n\t" \
    "s_nop 0\n\t" \
    "global_load_lds_dwordx4 v207, s[46:47]\n\t" \
    "s_add_u32 m0, s69, 5120\n\t" \
    "s_nop 0\n\t" \
    "global_load_lds_dwordx4 v208, s[46:47]\n\t" \
    "s_add_u32 m0, s69, 6144\n\t" \
    "s_nop 0\n\t" \
    "global_load_lds_dwordx4 v209, s[46:47]\n\t" \
    "s_add_u32 m0, s69, 7168\n\t" \
    "s_nop 0\n\t" \
    "global_load_lds_dwordx4 v210, s[46:47]\n\t" \
    "s_add_u32 m0, s69, 8192\n\t" \
    "s_nop 0\n\t" \
    "global_load_lds_dwordx4 v211, s[46:47]\n\t" \
    "s_waitcnt vmcnt(25) lgkmcnt(0)\n\t" \
    "s_barrier\n\t" \
    "v_add_u32 v201, 0x10000, v201\n\t" \
    "v_add_u32 v202, 0x10000, v202\n\t" \
    "ds_read_b64_tr_b16 a[32:33], v201 offset:0\n\t" \
    "ds_read_b64_tr_b16 a[34:35], v201 offset:1024\n\t" \
    "ds_read_b64_tr_b16 a[36:37], v201 offset:8192\n\t" \
    "ds_read_b64_tr_b16 a[38:39], v201 offset:9216\n\t" \
    "ds_read_b64_tr_b16 a[104:105], v202 offset:0\n\t" \
    "ds_read_b64_tr_b16 a[106:107], v202 offset:1024\n\t" \
    "ds_read_b64_tr_b16 a[108:109], v202 offset:8192\n\t" \
    "ds_read_b64_tr_b16 a[110:111], v202 offset:9216\n\t" \
    "s_waitcnt vmcnt(21) lgkmcnt(0)\n\t" \
    "s_barrier\n\t" \
    "ds_read_b64_tr_b16 a[40:41], v201 offset:16384\n\t" \
    "ds_read_b64_tr_b16 a[42:43], v201 offset:17408\n\t" \
    "ds_read_b64_tr_b16 a[44:45], v201 offset:24576\n\t" \
    "ds_read_b64_tr_b16 a[46:47], v201 offset:25600\n\t" \
    "ds_read_b64_tr_b16 a[112:113], v202 offset:16384\n\t" \
    "ds_read_b64_tr_b16 a[114:115], v202 offset:17408\n\t" \
    "ds_read_b64_tr_b16 a[116:117], v202 offset:24576\n\t" \
    "ds_read_b64_tr_b16 a[118:119], v202 offset:25600\n\t" \
    "s_add_u32 s46, s44, 36864\n\t" \
    "s_addc_u32 s47, s45, 0\n\t" \
    "s_mul_i32 s69, s82, 9\n\t" \
    "s_add_u32 m0, s69, 36864\n\t" \
    "s_nop 0\n\t" \
    "global_load_lds_dwordx4 v203, s[46:47]\n\t" \
    "s_add_u32 m0, s69, 37888\n\t" \
    "s_nop 0\n\t" \
    "global_load_lds_dwordx4 v204, s[46:47]\n\t" \
    "s_add_u32 m0, s69, 38912\n\t" \
    "s_nop 0\n\t" \
    "global_load_lds_dwordx4 v205, s[46:47]\n\t" \
    "s_add_u32 m0, s69, 39936\n\t" \
    "s_nop 0\n\t" \
    "global_load_lds_dwordx4 v206, s[46:47]\n\t" \
    "s_add_u32 m0, s69, 40960\n\t" \
    "s_nop 0\n\t" \
    "global_load_lds_dwordx4 v207, s[46:47]\n\t" \
    "s_add_u32 m0, s69, 41984\n\t" \
    "s_nop 0\n\t" \
    "global_load_lds_dwordx4 v208, s[46:47]\n\t" \
    "s_add_u32 m0, s69, 43008\n\t" \
    "s_nop 0\n\t" \
    "global_load_lds_dwordx4 v209, s[46:47]\n\t" \
    "s_add_u32 m0, s69, 44032\n\t" \
    "s_nop 0\n\t" \
    "global_load_lds_dwordx4 v210, s[46:47]\n\t" \
    "s_add_u32 m0, s69, 45056\n\t" \
    "s_nop 0\n\t" \
    "global_load_lds_dwordx4 v211, s[46:47]\n\t" \
    "s_waitcnt vmcnt(26) lgkmcnt(0)\n\t" \
    "s_barrier\n\t" \
    "ds_read_b64_tr_b16 a[48:49], v201 offset:32768\n\t" \
    "ds_read_b64_tr_b16 a[50:51], v201 offset:33792\n\t" \
    "ds_read_b64_tr_b16 a[52:53], v201 offset:40960\n\t" \
    "ds_read_b64_tr_b16 a[54:55], v201 offset:41984\n\t" \
    "ds_read_b64_tr_b16 a[120:121], v202 offset:32768\n\t" \
    "ds_read_b64_tr_b16 a[122:123], v202 offset:33792\n\t" \
    "ds_read_b64_tr_b16 a[124:125], v202 offset:40960\n\t" \
    "ds_read_b64_tr_b16 a[126:127], v202 offset:41984\n\t" \
    "s_waitcnt vmcnt(22) lgkmcnt(0)\n\t" \
    "s_barrier\n\t" \
    "ds_read_b64_tr_b16 a[56:57], v201 offset:49152\n\t" \
    "ds_read_b64_tr_b16 a[58:59], v201 offset:50176\n\t" \
    "ds_read_b64_tr_b16 a[60:61], v201 offset:57344\n\t" \
    "ds_read_b64_tr_b16 a[62:63], v201 offset:58368\n\t" \
    "ds_read_b64_tr_b16 a[128:129], v202 offset:49152\n\t" \
    "ds_read_b64_tr_b16 a[130:131], v202 offset:50176\n\t" \
    "ds_read_b64_tr_b16 a[132:133], v202 offset:57344\n\t" \
    "ds_read_b64_tr_b16 a[134:135], v202 offset:58368\n\t" \
    "s_add_u32 s46, s44, 73728\n\t" \
    "s_addc_u32 s47, s45, 0\n\t" \
    "s_mul_i32 s69, s82, 9\n\t" \
    "s_add_u32 m0, s69, 73728\n\t" \
    "s_nop 0\n\t" \
    "global_load_lds_dwordx4 v203, s[46:47]\n\t" \
    "s_add_u32 m0, s69, 74752\n\t" \
    "s_nop 0\n\t" \
    "global_load_lds_dwordx4 v204, s[46:47]\n\t" \
    "s_add_u32 m0, s69, 75776\n\t" \
    "s_nop 0\n\t" \
    "global_load_lds_dwordx4 v205, s[46:47]\n\t" \
    "s_add_u32 m0, s69, 76800\n\t" \
    "s_nop 0\n\t" \
    "global_load_lds_dwordx4 v206, s[46:47]\n\t" \
    "s_add_u32 m0, s69, 77824\n\t" \
    "s_nop 0\n\t" \
    "global_load_lds_dwordx4 v207, s[46:47]\n\t" \
    "s_add_u32 m0, s69, 78848\n\t" \
    "s_nop 0\n\t" \
    "global_load_lds_dwordx4 v208, s[46:47]\n\t" \
    "s_add_u32 m0, s69, 79872\n\t" \
    "s_nop 0\n\t" \
    "global_load_lds_dwordx4 v209, s[46:47]\n\t" \
    "s_add_u32 m0, s69, 80896\n\t" \
    "s_nop 0\n\t" \
    "global_load_lds_dwordx4 v210, s[46:47]\n\t" \
    "s_add_u32 m0, s69, 81920\n\t" \
    "s_nop 0\n\t" \
    "global_load_lds_dwordx4 v211, s[46:47]\n\t" \
    "v_mbcnt_lo_u32_b32 v140, -1, 0\n\t" \
    "v_mbcnt_hi_u32_b32 v140, -1, v140\n\t" \
    "v_lshlrev_b32 v140, 6, v140\n\t" \
    "v_add_u32 v140, 6225920, v140\n\t" \
    "v_mov_b32 v141, 6234112\n\t" \
    "global_load_dword v141, v141, s[36:37] sc1\n\t" \
    "global_load_dwordx2 v[142:143], v140, s[36:37] sc1\n\t" \
    "global_load_dword v144, v140, s[36:37] offset:2048 sc1\n\t" \
    "s_waitcnt vmcnt(30) lgkmcnt(0)\n\t" \
    "s_barrier\n\t" \
    "v_add_u32 v201, 0x10000, v201\n\t" \
    "v_add_u32 v202, 0x10000, v202\n\t" \
    "ds_read_b64_tr_b16 a[64:65], v201 offset:0\n\t" \
    "ds_read_b64_tr_b16 a[66:67], v201 offset:1024\n\t" \
    "ds_read_b64_tr_b16 a[68:69], v201 offset:8192\n\t" \
    "ds_read_b64_tr_b16 a[70:71], v201 offset:9216\n\t" \
    "ds_read_b64_tr_b16 a[136:137], v202 offset:0\n\t" \
    "ds_read_b64_tr_b16 a[138:139], v202 offset:1024\n\t" \
    "ds_read_b64_tr_b16 a[140:141], v202 offset:8192\n\t" \
    "ds_read_b64_tr_b16 a[142:143], v202 offset:9216\n\t" \
    "v_add_u32 v201, 0xfffe0000, v201\n\t" \
    "v_add_u32 v202, 0xfffe0000, v202\n\t" \
    "s_waitcnt lgkmcnt(0)\n\t" \
    "s_barrier\n\t" \
    "s_add_u32 s46, s44, 110592\n\t" \
    "s_addc_u32 s47, s45, 0\n\t" \
    "s_mul_i32 s69, s82, 9\n\t" \
    "s_add_u32 m0, s69, 110592\n\t" \
    "s_nop 0\n\t" \
    "global_load_lds_dwordx4 v203, s[46:47]\n\t" \
    "s_add_u32 m0, s69, 111616\n\t" \
    "s_nop 0\n\t" \
    "global_load_lds_dwordx4 v204, s[46:47]\n\t" \
    "s_add_u32 m0, s69, 112640\n\t" \
    "s_nop 0\n\t" \
    "global_load_lds_dwordx4 v205, s[46:47]\n\t" \
    "s_add_u32 m0, s69, 113664\n\t" \
    "s_nop 0\n\t" \
    "global_load_lds_dwordx4 v206, s[46:47]\n\t" \
    "s_add_u32 m0, s69, 114688\n\t" \
    "s_nop 0\n\t" \
    "global_load_lds_dwordx4 v207, s[46:47]\n\t" \
    "s_add_u32 m0, s69, 115712\n\t" \
    "s_nop 0\n\t" \
    "global_load_lds_dwordx4 v208, s[46:47]\n\t" \
    "s_add_u32 m0, s69, 116736\n\t" \
    "s_nop 0\n\t" \
    "global_load_lds_dwordx4 v209, s[46:47]\n\t" \
    "s_add_u32 m0, s69, 117760\n\t" \
    "s_nop 0\n\t" \
    "global_load_lds_dwordx4 v210, s[46:47]\n\t" \
    "s_add_u32 m0, s69, 118784\n\t" \
    "s_nop 0\n\t" \
    "global_load_lds_dwordx4 v211, s[46:47]\n\t" \
    "s_waitcnt vmcnt(21) lgkmcnt(0)\n\t" \
    "s_barrier\n\t" \
    "ds_read_b64_tr_b16 a[144:145], v212 offset:0\n\t" \
    "ds_read_b64_tr_b16 a[146:147], v212 offset:2304\n\t" \
    "ds_read_b64_tr_b16 a[148:149], v212 offset:18432\n\t" \
    "ds_read_b64_tr_b16 a[150:151], v212 offset:20736\n\t" \
    "s_waitcnt lgkmcnt(6)\n\t" \
    "ds_read_b64_tr_b16 a[176:177], v213 offset:0\n\t" \
    "ds_read_b64_tr_b16 a[178:179], v213 offset:2304\n\t" \
    "ds_read_b64_tr_b16 a[180:181], v213 offset:18432\n\t" \
    "ds_read_b64_tr_b16 a[182:183], v213 offset:20736\n\t" \
    "s_waitcnt lgkmcnt(6)\n\t" \
    "ds_read_b64_tr_b16 a[208:209], v212 offset:128\n\t" \
    "ds_read_b64_tr_b16 a[210:211], v212 offset:2432\n\t" \
    "ds_read_b64_tr_b16 a[212:213], v212 offset:18560\n\t" \
    "ds_read_b64_tr_b16 a[214:215], v212 offset:20864\n\t" \
    "s_waitcnt lgkmcnt(6)\n\t" \
    "ds_read_b64_tr_b16 a[240:241], v213 offset:128\n\t" \
    "ds_read_b64_tr_b16 a[242:243], v213 offset:2432\n\t" \
    "ds_read_b64_tr_b16 a[244:245], v213 offset:18560\n\t" \
    "ds_read_b64_tr_b16 a[246:247], v213 offset:20864\n\t" \
    "s_waitcnt lgkmcnt(6)\n\t" \
    "ds_read_b64_tr_b16 a[152:153], v214 offset:0\n\t" \
    "ds_read_b64_tr_b16 a[154:155], v214 offset:2304\n\t" \
    "ds_read_b64_tr_b16 a[156:157], v214 offset:18432\n\t" \
    "ds_read_b64_tr_b16 a[158:159], v214 offset:20736\n\t" \
    "s_waitcnt lgkmcnt(6)\n\t" \
    "ds_read_b64_tr_b16 a[184:185], v215 offset:0\n\t" \
    "ds_read_b64_tr_b16 a[186:187], v215 offset:2304\n\t" \
    "ds_read_b64_tr_b16 a[188:189], v215 offset:18432\n\t" \
    "ds_read_b64_tr_b16 a[190:191], v215 offset:20736\n\t" \
    "s_waitcnt lgkmcnt(6)\n\t" \
    "ds_read_b64_tr_b16 a[216:217], v214 offset:128\n\t" \
    "ds_read_b64_tr_b16 a[218:219], v214 offset:2432\n\t" \
    "ds_read_b64_tr_b16 a[220:221], v214 offset:18560\n\t" \
    "ds_read_b64_tr_b16 a[222:223], v214 offset:20864\n\t" \
    "s_waitcnt lgkmcnt(6)\n\t" \
    "ds_read_b64_tr_b16 a[248:249], v215 offset:128\n\t" \
    "ds_read_b64_tr_b16 a[250:251], v215 offset:2432\n\t" \
    "ds_read_b64_tr_b16 a[252:253], v215 offset:18560\n\t" \
    "ds_read_b64_tr_b16 a[254:255], v215 offset:20864\n\t" \
    "s_waitcnt lgkmcnt(6)\n\t" \
    "ds_read_b64_tr_b16 v[240:241], v24 offset:0\n\t" \
    "ds_read_b64_tr_b16 v[242:243], v24 offset:2304\n\t" \
    "ds_read_b64_tr_b16 v[244:245], v24 offset:18432\n\t" \
    "ds_read_b64_tr_b16 v[246:247], v24 offset:20736\n\t" \
    "v_add_u32 v212, 0x12000, v212\n\t" \
    "v_add_u32 v213, 0x12000, v213\n\t" \
    "v_add_u32 v214, 0x12000, v214\n\t" \
    "v_add_u32 v215, 0x12000, v215\n\t" \
    "v_add_u32 v24, 0x12000, v24\n\t" \
    "s_waitcnt vmcnt(9)\n\t" \
    "vv_C2_%=:\n\t" \
    "v_cmp_ne_u32 s[54:55], v142, v144\n\t" \
    "v_sub_u32 v142, v142, v141\n\t" \
    "v_cmp_ne_u32 vcc, 1, v142\n\t" \
    "s_nop 1\n\t" \
    "s_or_b64 vcc, vcc, s[54:55]\n\t" \
    "s_cbranch_scc0 vv_Cok3_%=\n\t" \
    "s_cmp_eq_u32 s68, 0\n\t" \
    "s_cbranch_scc1 vv_F5_%=\n\t" \
    "s_sub_u32 s68, s68, 1\n\t" \
    "s_sleep 2\n\t" \
    "s_branch vv_Cag4_%=\n\t" \
    "vv_F5_%=:\n\t" \
    "v_mov_b32 v144, 5767424\n\t" \
    "v_mov_b32 v145, 4\n\t" \
    "global_store_dword v144, v145, s[36:37] sc1\n\t" \
    "s_waitcnt vmcnt(0)\n\t" \
    "s_branch vv_Cok3_%=\n\t" \
    "vv_Cag4_%=:\n\t" \
    "global_load_dwordx2 v[142:143], v140, s[36:37] sc1\n\t" \
    "global_load_dword v144, v140, s[36:37] offset:2048 sc1\n\t" \
    "s_waitcnt vmcnt(0)\n\t" \
    "s_branch vv_C2_%=\n\t" \
    "vv_Cok3_%=:\n\t" \
    "s_cmp_lg_u32 s66, 0\n\t" \
    "s_cbranch_scc1 vv_Ca6_%=\n\t" \
    "v_mov_b32 v140, 5836800\n\t" \
    "s_mov_b64 exec, 1\n\t" \
    "global_atomic_add v140, v142, s[36:37] sc1\n\t" \
    "s_mov_b64 exec, -1\n\t" \
    "vv_Ca6_%=:\n\t" \
    "v_mov_b32_dpp v140, v143 quad_perm:[1,0,3,2] row_mask:0xf bank_mask:0xf\n\t" \
    "v_cmp_lt_u32 s[44:45], v140, v143\n\t" \
    "s_nop 1\n\t" \
    "s_bitcmp1_b32 s44, 0\n\t" \
    "s_cselect_b32 s62, 8192, 0\n\t" \
    "s_bitcmp1_b32 s44, 2\n\t" \
    "s_cselect_b32 s63, 8192, 0\n\t" \
    "s_or_b32 s63, s63, 0x200000\n\t" \
    "v_min_u32 v140, v140, v143\n\t" \
    "v_cmp_lt_u32 vcc, 660, v140\n\t" \
    "s_nop 1\n\t" \
    "s_cmp_lg_u64 vcc, 0\n\t" \
    "s_cselect_b32 s69, 1, 0\n\t" \
    "s_mul_i32 s70, s69, 160\n\t" \
    "s_add_u32 s86, s86, s70\n\t" \
    "s_mul_i32 s70, s69, 536\n\t" \
    "s_add_u32 s87, s87, s70\n\t" \
    "s_mul_i32 s70, s69, 256\n\t" \
    "s_add_u32 s88, s88, s70\n\t" \
    "s_waitcnt vmcnt(0) lgkmcnt(0)\n\t" \
    "s_barrier\n\t" \
    "ds_read_b64_tr_b16 a[160:161], v212 offset:0\n\t" \
    "ds_read_b64_tr_b16 a[162:163], v212 offset:2304\n\t" \
    "ds_read_b64_tr_b16 a[164:165], v212 offset:18432\n\t" \
    "ds_read_b64_tr_b16 a[166:167], v212 offset:20736\n\t" \
    "s_waitcnt lgkmcnt(6)\n\t" \
    "ds_read_b64_tr_b16 a[192:193], v213 offset:0\n\t" \
    "ds_read_b64_tr_b16 a[194:195], v213 offset:2304\n\t" \
    "ds_read_b64_tr_b16 a[196:197], v213 offset:18432\n\t" \
    "ds_read_b64_tr_b16 a[198:199], v213 offset:20736\n\t" \
    "s_waitcnt lgkmcnt(6)\n\t" \
    "ds_read_b64_tr_b16 a[224:225], v212 offset:128\n\t" \
    "ds_read_b64_tr_b16 a[226:227], v212 offset:2432\n\t" \
    "ds_read_b64_tr_b16 a[228:229], v212 offset:18560\n\t" \
    "ds_read_b64_tr_b16 a[230:231], v212 offset:20864\n\t" \
    "s_waitcnt lgkmcnt(6)\n\t" \
    "ds_read_b64_tr_b16 v[224:225], v213 offset:128\n\t" \
    "ds_read_b64_tr_b16 v[226:227], v213 offset:2432\n\t" \
    "ds_read_b64_tr_b16 v[228:229], v213 offset:18560\n\t" \
    "ds_read_b64_tr_b16 v[230:231], v213 offset:20864\n\t" \
    "s_waitcnt lgkmcnt(6)\n\t" \
    "ds_read_b64_tr_b16 a[168:169], v214 offset:0\n\t" \
    "ds_read_b64_tr_b16 a[170:171], v214 offset:2304\n\t" \
    "ds_read_b64_tr_b16 a[172:173], v214 offset:18432\n\t" \
    "ds_read_b64_tr_b16 a[174:175], v214 offset:20736\n\t" \
    "s_waitcnt lgkmcnt(6)\n\t" \
    "ds_read_b64_tr_b16 a[200:201], v215 offset:0\n\t" \
    "ds_read_b64_tr_b16 a[202:203], v215 offset:2304\n\t" \
    "ds_read_b64_tr_b16 a[204:205], v215 offset:18432\n\t" \
    "ds_read_b64_tr_b16 a[206:207], v215 offset:20736\n\t" \
    "s_waitcnt lgkmcnt(6)\n\t" \
    "ds_read_b64_tr_b16 a[232:233], v214 offset:128\n\t" \
    "ds_read_b64_tr_b16 a[234:235], v214 offset:2432\n\t" \
    "ds_read_b64_tr_b16 a[236:237], v214 offset:18560\n\t" \
    "ds_read_b64_tr_b16 a[238:239], v214 offset:20864\n\t" \
    "s_waitcnt lgkmcnt(6)\n\t" \
    "ds_read_b64_tr_b16 v[232:233], v215 offset:128\n\t" \
    "ds_read_b64_tr_b16 v[234:235], v215 offset:2432\n\t" \
    "ds_read_b64_tr_b16 v[236:237], v215 offset:18560\n\t" \
    "ds_read_b64_tr_b16 v[238:239], v215 offset:20864\n\t" \
    "s_waitcnt lgkmcnt(6)\n\t" \
    "ds_read_b64_tr_b16 v[248:249], v24 offset:0\n\t" \
    "ds_read_b64_tr_b16 v[250:251], v24 offset:2304\n\t" \
    "ds_read_b64_tr_b16 v[252:253], v24 offset:18432\n\t" \
    "ds_read_b64_tr_b16 v[254:255], v24 offset:20736\n\t" \
    "v_add_u32 v212, 0x12000, v212\n\t" \
    "v_add_u32 v213, 0x12000, v213\n\t" \
    "v_add_u32 v214, 0x12000, v214\n\t" \
    "v_add_u32 v215, 0x12000, v215\n\t" \
    "v_add_u32 v24, 0x12000, v24\n\t" \
    "s_waitcnt lgkmcnt(0)\n\t" \
    "s_barrier\n\t" \
    "s_mov_b32 s79, s74\n\t" \
    "s_sub_u32 s80, s74, 32000\n\t" \
    "v_mov_b32 v72, 0\n\t" \
    "v_mov_b32 v73, 0\n\t" \
    "v_mov_b32 v74, 0\n\t" \
    "v_mov_b32 v75, 0\n\t" \
    "v_mov_b32 v76, 0\n\t" \
    "v_mov_b32 v77, 0\n\t" \
    "v_mov_b32 v78, 0\n\t" \
    "v_mov_b32 v79, 0\n\t" \
    "v_mov_b32 v108, 0\n\t" \
    "v_mov_b32 v109, 0\n\t" \
    "v_mov_b32 v110, 0\n\t" \
    "v_mov_b32 v111, 0\n\t" \
    "v_mov_b32 v112, 0\n\t" \
    "v_mov_b32 v113, 0\n\t" \
    "v_mov_b32 v114, 0\n\t" \
    "v_mov_b32 v115, 0\n\t" \
    "v_mov_b32 v116, 0\n\t" \
    "v_mov_b32 v117, 0\n\t" \
    "v_mov_b32 v118, 0\n\t" \
    "v_mov_b32 v119, 0\n\t" \
    "v_mov_b32 v120, 0\n\t" \
    "v_mov_b32 v121, 0\n\t" \
    "v_mov_b32 v122, 0\n\t" \
    "v_mov_b32 v123, 0\n\t" \
    "v_mov_b32 v124, 0\n\t" \
    "v_mov_b32 v125, 0\n\t" \
    "v_mov_b32 v126, 0\n\t" \
    "v_mov_b32 v127, 0\n\t" \
    "s_add_u32 s69, s65, 0\n\t" \
    "s_and_b32 s69, s69, 7\n\t" \
    "s_lshl_b32 s69, s69, 18\n\t" \
    "s_add_u32 s69, s69, s62\n\t" \
    "s_add_u32 s46, s36, s69\n\t" \
    "s_addc_u32 s47, s37, 0\n\t" \
    "s_add_u32 s70, s65, 0\n\t" \
    "s_mul_i32 s69, s70, 171\n\t" \
    "s_lshr_b32 s69, s69, 9\n\t" \
    "s_mul_i32 s70, s70, 589824\n\t" \
    "s_mul_i32 s71, s69, 327680\n\t" \
    "s_add_u32 s70, s70, s71\n\t" \
    "s_lshl1_add_u32 s69, s69, 4\n\t" \
    "s_bitcmp1_b64 s[44:45], s69\n\t" \
    "s_cselect_b32 s69, 8192, 0\n\t" \
    "s_add_u32 s69, s69, s70\n\t" \
    "s_add_u32 s69, s69, 123731968\n\t" \
    "s_add_u32 s38, s36, s69\n\t" \
    "s_addc_u32 s39, s37, 0\n\t" \
    "s_and_b32 s69, s69, 0xffe00000\n\t" \
    "s_add_u32 s69, s69, 1823744\n\t" \
    "v_mov_b32 v17, s69\n\t" \
    "global_load_dword v17, v17, s[36:37]\n\t" \
    "s_and_b32 s69, s65, 7\n\t" \
    "s_lshl_b32 s69, s69, 18\n\t" \
    "s_add_u32 s69, s69, s63\n\t" \
    "s_add_u32 s52, s36, s69\n\t" \
    "s_addc_u32 s53, s37, 0\n\t" \
    "v_mov_b32 v100, 0\n\t" \
    "v_mov_b32 v101, 0\n\t" \
    "v_mov_b32 v102, 0\n\t" \
    "v_mov_b32 v103, 0\n\t" \
    "s_sub_u32 s70, s79, s74\n\t" \
    "s_max_i32 s70, s70, 0\n\t" \
    "s_min_i32 s70, s70, 16000\n\t" \
    "s_lshl_b32 s70, s70, 1\n\t" \
    "s_mul_hi_u32 s70, s70, s77\n\t" \
    "s_add_u32 s70, s70, s73\n\t" \
    "s_cmp_eq_u32 s66, 1\n\t" \
    "s_cbranch_scc1 vv_loop10_%=\n\t" \
    "s_cmp_eq_u32 s66, 2\n\t" \
    "s_cbranch_scc1 vv_loop11_%=\n\t" \
    "s_cmp_eq_u32 s66, 3\n\t" \
    "s_cbranch_scc1 vv_loop12_%=\n\t" \
    ".p2align 6\n\t" \
    "s_nop 0\n\t" \
    "s_nop 0\n\t" \
    "s_nop 0\n\t" \
    "s_nop 0\n\t" \
    "s_nop 0\n\t" \
    "s_nop 0\n\t" \
    "vv_loop9_%=:\n\t" \
    "s_getpc_b64 s[30:31]\n\t" \
    "vv_Wp15_%=:\n\t" \
    "s_add_u32 s30, s30, vv_We16_%=-vv_Wp15_%=\n\t" \
    "s_addc_u32 s31, s31, 0\n\t" \
    "s_sub_u32 s70, s70, 96\n\t" \
    "vv_W14_%=:\n\t" \
    "s_memtime s[56:57]\n\t" \
    "s_waitcnt lgkmcnt(0)\n\t" \
    "s_sub_u32 s69, s70, s56\n\t" \
    "s_cmp_gt_i32 s69, 80\n\t" \
    "s_cbranch_scc1 vv_W14_%=\n\t" \
    "s_max_i32 s69, s69, 0\n\t" \
    "s_and_b32 s69, s69, -4\n\t" \
    "s_sub_u32 s30, s30, s69\n\t" \
    "s_subb_u32 s31, s31, 0\n\t" \
    "s_setpc_b64 s[30:31]\n\t" \
    "s_nop 0\n\t" \
    "s_nop 0\n\t" \
    "s_nop 0\n\t" \
    "s_nop 0\n\t" \
    "s_nop 0\n\t" \
    "s_nop 0\n\t" \
    "s_nop 0\n\t" \
    "s_nop 0\n\t" \
    "s_nop 0\n\t" \
    "s_nop 0\n\t" \
    "s_nop 0\n\t" \
    "s_nop 0\n\t" \
    "s_nop 0\n\t" \
    "s_nop 0\n\t" \
    "s_nop 0\n\t" \
    "s_nop 0\n\t" \
    "s_nop 0\n\t" \
    "s_nop 0\n\t" \
    "s_nop 0\n\t" \
    "s_nop 0\n\t" \
    "vv_We16_%=:\n\t" \
    "vv_G17_%=:\n\t" \
    "global_load_dwordx4 v[36:39], v1, s[46:47] sc1\n\t" \
    "global_load_dwordx4 v[40:43], v23, s[46:47] sc1\n\t" \
    "s_nop 7\n\t" \
    "global_load_dwordx4 v[44:47], v2, s[46:47] sc1\n\t" \
    "s_waitcnt vmcnt(2)\n\t" \
    "v_smfmac_f32_16x16x64_bf16 v[72:75], v[36:39], a[0:7], v10\n\t" \
    "ds_write_b128 v3, v[36:39]\n\t" \
    "v_smfmac_f32_16x16x64_bf16 v[76:79], v[36:39], a[72:79], v10\n\t" \
    "s_waitcnt vmcnt(1)\n\t" \
    "v_cmp_u_f32 vcc, v36, v40\n\t" \
    "ds_write_b128 v3, v[40:43] offset:1024\n\t" \
    "v_smfmac_f32_16x16x64_bf16 v[72:75], v[40:43], a[8:15], v10\n\t" \
    "s_cbranch_vccnz vv_Gbad18_%=\n\t" \
    "vv_Gc19_%=:\n\t" \
    "s_waitcnt lgkmcnt(0)\n\t" \
    "s_barrier\n\t" \
    "ds_read_b128 v[48:51], v4\n\t" \
    "ds_read_b128 v[52:55], v5\n\t" \
    "v_smfmac_f32_16x16x64_bf16 v[76:79], v[40:43], a[80:87], v10\n\t" \
    "s_waitcnt vmcnt(0)\n\t" \
    "v_cmp_eq_u32 vcc, -1, v44\n\t" \
    "ds_read_b128 v[56:59], v6\n\t" \
    "ds_read_b128 v[60:63], v7\n\t" \
    "ds_read_b128 v[64:67], v8\n\t" \
    "ds_read_b128 v[68:71], v9\n\t" \
    "s_cbranch_vccnz vv_Gbad223_%=\n\t" \
    "vv_Gc224_%=:\n\t" \
    "v_smfmac_f32_16x16x64_bf16 v[72:75], v[44:47], a[64:71], v10\n\t" \
    "v_smfmac_f32_16x16x64_bf16 v[76:79], v[44:47], a[136:143], v10\n\t" \
    "s_waitcnt lgkmcnt(5)\n\t" \
    "v_smfmac_f32_16x16x64_bf16 v[72:75], v[48:51], a[16:23], v10\n\t" \
    "s_waitcnt lgkmcnt(4)\n\t" \
    "v_smfmac_f32_16x16x64_bf16 v[72:75], v[52:55], a[24:31], v10\n\t" \
    "s_waitcnt lgkmcnt(3)\n\t" \
    "v_smfmac_f32_16x16x64_bf16 v[72:75], v[56:59], a[32:39], v10\n\t" \
    "s_waitcnt lgkmcnt(2)\n\t" \
    "v_smfmac_f32_16x16x64_bf16 v[72:75], v[60:63], a[40:47], v10\n\t" \
    "s_waitcnt lgkmcnt(1)\n\t" \
    "v_smfmac_f32_16x16x64_bf16 v[72:75], v[64:67], a[48:55], v10\n\t" \
    "s_waitcnt lgkmcnt(0)\n\t" \
    "v_smfmac_f32_16x16x64_bf16 v[72:75], v[68:71], a[56:63], v10\n\t" \
    "v_smfmac_f32_16x16x64_bf16 v[76:79], v[48:51], a[88:95], v10\n\t" \
    "s_nop 1\n\t" \
    "s_nop 0\n\t" \
    "v_smfmac_f32_16x16x64_bf16 v[76:79], v[52:55], a[96:103], v10\n\t" \
    "s_nop 1\n\t" \
    "s_nop 0\n\t" \
    "v_smfmac_f32_16x16x64_bf16 v[76:79], v[56:59], a[104:111], v10\n\t" \
    "v_add_f32 v72, v72, v74\n\t" \
    "v_add_f32 v73, v73, v75\n\t" \
    "v_smfmac_f32_16x16x64_bf16 v[76:79], v[60:63], a[112:119], v10\n\t" \
    "v_cvt_pk_bf16_f32 v80, v72, v73\n\t" \
    "v_smfmac_f32_16x16x64_bf16 v[76:79], v[64:67], a[120:127], v10\n\t" \
    "v_cmp_u_f32 vcc, v72, v73\n\t" \
    "ds_write_b32 v12, v80 offset:0\n\t" \
    "v_smfmac_f32_16x16x64_bf16 v[76:79], v[68:71], a[128:135], v10\n\t" \
    "s_cbranch_vccnz vv_Gslow20_%=\n\t" \
    "vv_Gback21_%=:\n\t" \
    "vv_Gdone22_%=:\n\t" \
    "s_nop 6\n\t" \
    "v_add_f32 v76, v76, v78\n\t" \
    "v_add_f32 v77, v77, v79\n\t" \
    "v_cvt_pk_bf16_f32 v81, v76, v77\n\t" \
    "ds_write_b32 v12, v81 offset:256\n\t" \
    "s_waitcnt lgkmcnt(0)\n\t" \
    "s_barrier\n\t" \
    "ds_read_b64_tr_b16 v[84:85], v13 offset:0\n\t" \
    "v_smfmac_f32_16x16x64_bf16 v[100:103], v[36:39], a[0:7], v10\n\t" \
    "ds_read_b64_tr_b16 v[86:87], v13 offset:64\n\t" \
    "ds_read_b64_tr_b16 v[92:93], v13 offset:1152\n\t" \
    "v_smfmac_f32_16x16x64_bf16 v[100:103], v[36:39], a[0:7], v10\n\t" \
    "ds_read_b64_tr_b16 v[94:95], v13 offset:1216\n\t" \
    "ds_read_b64_tr_b16 v[88:89], v25 offset:0\n\t" \
    "v_smfmac_f32_16x16x64_bf16 v[100:103], v[36:39], a[0:7], v10\n\t" \
    "ds_read_b64_tr_b16 v[90:91], v25 offset:64\n\t" \
    "s_waitcnt lgkmcnt(4)\n\t" \
    "v_smfmac_f32_16x16x64_bf16 v[124:127], v[84:87], v[240:247], v19\n\t" \
    "ds_read_b64_tr_b16 v[96:97], v25 offset:1152\n\t" \
    "ds_read_b64_tr_b16 v[98:99], v25 offset:1216\n\t" \
    "s_waitcnt lgkmcnt(4)\n\t" \
    "v_smfmac_f32_16x16x64_bf16 v[124:127], v[92:95], v[248:255], v19\n\t" \
    "s_nop 2\n\t" \
    "v_smfmac_f32_16x16x64_bf16 v[108:111], v[84:87], a[144:151], v19\n\t" \
    "s_nop 2\n\t" \
    "v_smfmac_f32_16x16x64_bf16 v[108:111], v[92:95], a[160:167], v19\n\t" \
    "s_nop 1\n\t" \
    "v_permlane32_swap_b32 v124, v126\n\t" \
    "v_smfmac_f32_16x16x64_bf16 v[112:115], v[84:87], a[176:183], v19\n\t" \
    "s_nop 1\n\t" \
    "v_permlane32_swap_b32 v125, v127\n\t" \
    "v_smfmac_f32_16x16x64_bf16 v[112:115], v[92:95], a[192:199], v19\n\t" \
    "s_nop 0\n\t" \
    "v_add_f32 v124, v124, v126\n\t" \
    "s_waitcnt lgkmcnt(2)\n\t" \
    "v_smfmac_f32_16x16x64_bf16 v[108:111], v[88:91], a[152:159], v19\n\t" \
    "s_nop 1\n\t" \
    "v_add_f32 v125, v125, v127\n\t" \
    "v_smfmac_f32_16x16x64_bf16 v[112:115], v[88:91], a[184:191], v19\n\t" \
    "s_nop 0\n\t" \
    "v_cvt_pk_bf16_f32 v136, v124, v125\n\t" \
    "s_waitcnt lgkmcnt(0)\n\t" \
    "v_smfmac_f32_16x16x64_bf16 v[108:111], v[96:99], a[168:175], v19\n\t" \
    "s_nop 1\n\t" \
    "v_cvt_pk_bf16_f32 v137, v124, v125\n\t" \
    "v_smfmac_f32_16x16x64_bf16 v[112:115], v[96:99], a[200:207], v19\n\t" \
    "s_nop 1\n\t" \
    "v_permlane32_swap_b32 v136, v137\n\t" \
    "v_smfmac_f32_16x16x64_bf16 v[116:119], v[84:87], a[208:215], v19\n\t" \
    "s_mov_b32 exec_hi, 0\n\t" \
    "global_store_dwordx2 v16, v[136:137], s[38:39] sc1\n\t" \
    "s_mov_b32 exec_hi, -1\n\t" \
    "v_cvt_pk_bf16_f32 v128, v108, v109\n\t" \
    "v_smfmac_f32_16x16x64_bf16 v[116:119], v[88:91], a[216:223], v19\n\t" \
    "s_nop 1\n\t" \
    "v_cvt_pk_bf16_f32 v129, v110, v111\n\t" \
    "v_smfmac_f32_16x16x64_bf16 v[116:119], v[92:95], a[224:231], v19\n\t" \
    "s_nop 1\n\t" \
    "v_cvt_pk_bf16_f32 v130, v112, v113\n\t" \
    "v_smfmac_f32_16x16x64_bf16 v[116:119], v[96:99], a[232:239], v19\n\t" \
    "s_nop 1\n\t" \
    "v_cvt_pk_bf16_f32 v131, v114, v115\n\t" \
    "v_smfmac_f32_16x16x64_bf16 v[120:123], v[84:87], a[240:247], v19\n\t" \
    "s_nop 1\n\t" \
    "v_permlane16_swap_b32 v128, v130\n\t" \
    "v_smfmac_f32_16x16x64_bf16 v[120:123], v[88:91], a[248:255], v19\n\t" \
    "s_nop 1\n\t" \
    "v_permlane16_swap_b32 v129, v131\n\t" \
    "v_smfmac_f32_16x16x64_bf16 v[120:123], v[92:95], v[224:231], v19\n\t" \
    "s_nop 0\n\t" \
    "global_store_dwordx4 v14, v[128:131], s[38:39] sc1\n\t" \
    "v_cvt_pk_bf16_f32 v132, v116, v117\n\t" \
    "v_smfmac_f32_16x16x64_bf16 v[120:123], v[96:99], v[232:239], v19\n\t" \
    "s_nop 1\n\t" \
    "v_cvt_pk_bf16_f32 v133, v118, v119\n\t" \
    "s_nop 4\n\t" \
    "v_cvt_pk_bf16_f32 v134, v120, v121\n\t" \
    "v_cvt_pk_bf16_f32 v135, v122, v123\n\t" \
    "s_nop 0\n\t" \
    "v_permlane16_swap_b32 v132, v134\n\t" \
    "v_permlane16_swap_b32 v133, v135\n\t" \
    "global_store_dwordx4 v15, v[132:135], s[38:39] sc1\n\t" \
    "s_cmp_eq_u32 s65, 63\n\t" \
    "s_cbranch_scc1 vv_end13_%=\n\t" \
    "s_cmp_lg_u32 s66, 0\n\t" \
    "s_cbranch_scc1 vv_SF25_%=\n\t" \
    "s_lshl_b32 s69, s67, 7\n\t" \
    "s_add_u32 s69, s69, 5804032\n\t" \
    "s_add_u32 s70, s65, -1\n\t" \
    "v_mov_b32 v142, s69\n\t" \
    "v_mov_b32 v143, s70\n\t" \
    "s_mov_b64 exec, 1\n\t" \
    "global_store_dword v142, v143, s[36:37] sc1\n\t" \
    "s_mov_b64 exec, -1\n\t" \
    "vv_SF25_%=:\n\t" \
    "s_add_u32 s65, s65, 1\n\t" \
    "v_mov_b32 v72, 0\n\t" \
    "v_mov_b32 v73, 0\n\t" \
    "v_mov_b32 v74, 0\n\t" \
    "v_mov_b32 v75, 0\n\t" \
    "v_mov_b32 v76, 0\n\t" \
    "v_mov_b32 v77, 0\n\t" \
    "v_mov_b32 v78, 0\n\t" \
    "v_mov_b32 v79, 0\n\t" \
    "v_mov_b32 v108, 0\n\t" \
    "v_mov_b32 v109, 0\n\t" \
    "v_mov_b32 v110, 0\n\t" \
    "v_mov_b32 v111, 0\n\t" \
    "v_mov_b32 v112, 0\n\t" \
    "v_mov_b32 v113, 0\n\t" \
    "v_mov_b32 v114, 0\n\t" \
    "v_mov_b32 v115, 0\n\t" \
    "v_mov_b32 v116, 0\n\t" \
    "v_mov_b32 v117, 0\n\t" \
    "v_mov_b32 v118, 0\n\t" \
    "v_mov_b32 v119, 0\n\t" \
    "v_mov_b32 v120, 0\n\t" \
    "v_mov_b32 v121, 0\n\t" \
    "v_mov_b32 v122, 0\n\t" \
    "v_mov_b32 v123, 0\n\t" \
    "v_mov_b32 v124, 0\n\t" \
    "v_mov_b32 v125, 0\n\t" \
    "v_mov_b32 v126, 0\n\t" \
    "v_mov_b32 v127, 0\n\t" \
    "s_add_u32 s69, s65, 0\n\t" \
    "s_and_b32 s69, s69, 7\n\t" \
    "s_lshl_b32 s69, s69, 18\n\t" \
    "s_add_u32 s69, s69, s62\n\t" \
    "s_add_u32 s46, s36, s69\n\t" \
    "s_addc_u32 s47, s37, 0\n\t" \
    "s_add_u32 s70, s65, 0\n\t" \
    "s_mul_i32 s69, s70, 171\n\t" \
    "s_lshr_b32 s69, s69, 9\n\t" \
    "s_mul_i32 s70, s70, 589824\n\t" \
    "s_mul_i32 s71, s69, 327680\n\t" \
    "s_add_u32 s70, s70, s71\n\t" \
    "s_lshl1_add_u32 s69, s69, 4\n\t" \
    "s_bitcmp1_b64 s[44:45], s69\n\t" \
    "s_cselect_b32 s69, 8192, 0\n\t" \
    "s_add_u32 s69, s69, s70\n\t" \
    "s_add_u32 s69, s69, 123731968\n\t" \
    "s_add_u32 s38, s36, s69\n\t" \
    "s_addc_u32 s39, s37, 0\n\t" \
    "s_and_b32 s69, s69, 0xffe00000\n\t" \
    "s_add_u32 s69, s69, 1823744\n\t" \
    "v_mov_b32 v17, s69\n\t" \
    "global_load_dword v17, v17, s[36:37]\n\t" \
    "s_barrier\n\t" \
    "ds_read_b32 v160, v11 offset:20480\n\t" \
    "s_waitcnt lgkmcnt(0)\n\t" \
    "v_readfirstlane_b32 s70, v160\n\t" \
    "s_branch vv_loop9_%=\n\t" \
    "vv_Gbad18_%=:\n\t" \
    "v_max_u32 v140, v36, v40\n\t" \
    "v_cmp_eq_u32 vcc, -1, v140\n\t" \
    "s_cbranch_vccz vv_Gc19_%=\n\t" \
    "s_waitcnt vmcnt(0) lgkmcnt(0)\n\t" \
    "s_nop 7\n\t" \
    "v_mov_b32 v72, 0\n\t" \
    "v_mov_b32 v73, 0\n\t" \
    "v_mov_b32 v74, 0\n\t" \
    "v_mov_b32 v75, 0\n\t" \
    "v_mov_b32 v76, 0\n\t" \
    "v_mov_b32 v77, 0\n\t" \
    "v_mov_b32 v78, 0\n\t" \
    "v_mov_b32 v79, 0\n\t" \
    "s_cmp_eq_u32 s68, 0\n\t" \
    "s_cbranch_scc1 vv_F26_%=\n\t" \
    "s_sub_u32 s68, s68, 1\n\t" \
    "s_branch vv_G17_%=\n\t" \
    "vv_F26_%=:\n\t" \
    "v_mov_b32 v140, 5767424\n\t" \
    "v_mov_b32 v141, 1\n\t" \
    "global_store_dword v140, v141, s[36:37] sc1\n\t" \
    "s_waitcnt vmcnt(0)\n\t" \
    "s_branch vv_Gc19_%=\n\t" \
    "vv_Gbad223_%=:\n\t" \
    "global_load_dwordx4 v[44:47], v2, s[46:47] sc1\n\t" \
    "s_waitcnt vmcnt(0)\n\t" \
    "v_cmp_eq_u32 vcc, -1, v44\n\t" \
    "s_cbranch_vccz vv_Gc224_%=\n\t" \
    "s_cmp_eq_u32 s68, 0\n\t" \
    "s_cbranch_scc1 vv_F27_%=\n\t" \
    "s_sub_u32 s68, s68, 1\n\t" \
    "s_sleep 1\n\t" \
    "s_branch vv_Gbad223_%=\n\t" \
    "vv_F27_%=:\n\t" \
    "v_mov_b32 v140, 5767424\n\t" \
    "v_mov_b32 v141, 2\n\t" \
    "global_store_dword v140, v141, s[36:37] sc1\n\t" \
    "s_waitcnt vmcnt(0)\n\t" \
    "s_branch vv_Gc224_%=\n\t" \
    "vv_Gslow20_%=:\n\t" \
    "v_max_u32 v140, v36, v37\n\t" \
    "v_max3_u32 v140, v140, v38, v39\n\t" \
    "v_max3_u32 v140, v140, v40, v41\n\t" \
    "v_max3_u32 v140, v140, v42, v43\n\t" \
    "v_max3_u32 v140, v140, v44, v45\n\t" \
    "v_max3_u32 v140, v140, v46, v47\n\t" \
    "v_max3_u32 v140, v140, v48, v49\n\t" \
    "v_max3_u32 v140, v140, v50, v51\n\t" \
    "v_max3_u32 v140, v140, v52, v53\n\t" \
    "v_max3_u32 v140, v140, v54, v55\n\t" \
    "v_max3_u32 v140, v140, v56, v57\n\t" \
    "v_max3_u32 v140, v140, v58, v59\n\t" \
    "v_max3_u32 v140, v140, v60, v61\n\t" \
    "v_max3_u32 v140, v140, v62, v63\n\t" \
    "v_max3_u32 v140, v140, v64, v65\n\t" \
    "v_max3_u32 v140, v140, v66, v67\n\t" \
    "v_max3_u32 v140, v140, v68, v69\n\t" \
    "v_max3_u32 v140, v140, v70, v71\n\t" \
    "v_cmp_eq_u32 vcc, -1, v140\n\t" \
    "s_cbranch_vccz vv_Gback21_%=\n\t" \
    "v_sub_u32 v140, v1, v3\n\t" \
    "s_nop 0\n\t" \
    "v_readfirstlane_b32 s69, v140\n\t" \
    "s_nop 7\n\t" \
    "s_add_u32 s30, s46, s69\n\t" \
    "s_addc_u32 s31, s47, 0\n\t" \
    "vv_Gag28_%=:\n\t" \
    "global_load_dwordx4 v[36:39], v1, s[46:47] sc1\n\t" \
    "global_load_dwordx4 v[40:43], v23, s[46:47] sc1\n\t" \
    "global_load_dwordx4 v[44:47], v2, s[46:47] sc1\n\t" \
    "global_load_dwordx4 v[48:51], v4, s[30:31] sc1\n\t" \
    "global_load_dwordx4 v[52:55], v5, s[30:31] sc1\n\t" \
    "global_load_dwordx4 v[56:59], v6, s[30:31] sc1\n\t" \
    "global_load_dwordx4 v[60:63], v7, s[30:31] sc1\n\t" \
    "global_load_dwordx4 v[64:67], v8, s[30:31] sc1\n\t" \
    "global_load_dwordx4 v[68:71], v9, s[30:31] sc1\n\t" \
    "s_waitcnt vmcnt(0) lgkmcnt(0)\n\t" \
    "v_max_u32 v140, v36, v37\n\t" \
    "v_max3_u32 v140, v140, v38, v39\n\t" \
    "v_max3_u32 v140, v140, v40, v41\n\t" \
    "v_max3_u32 v140, v140, v42, v43\n\t" \
    "v_max3_u32 v140, v140, v44, v45\n\t" \
    "v_max3_u32 v140, v140, v46, v47\n\t" \
    "v_max3_u32 v140, v140, v48, v49\n\t" \
    "v_max3_u32 v140, v140, v50, v51\n\t" \
    "v_max3_u32 v140, v140, v52, v53\n\t" \
    "v_max3_u32 v140, v140, v54, v55\n\t" \
    "v_max3_u32 v140, v140, v56, v57\n\t" \
    "v_max3_u32 v140, v140, v58, v59\n\t" \
    "v_max3_u32 v140, v140, v60, v61\n\t" \
    "v_max3_u32 v140, v140, v62, v63\n\t" \
    "v_max3_u32 v140, v140, v64, v65\n\t" \
    "v_max3_u32 v140, v140, v66, v67\n\t" \
    "v_max3_u32 v140, v140, v68, v69\n\t" \
    "v_max3_u32 v140, v140, v70, v71\n\t" \
    "v_cmp_eq_u32 vcc, -1, v140\n\t" \
    "s_cbranch_vccz vv_Gredo29_%=\n\t" \
    "s_cmp_eq_u32 s68, 0\n\t" \
    "s_cbranch_scc1 vv_F30_%=\n\t" \
    "s_sub_u32 s68, s68, 1\n\t" \
    "s_sleep 1\n\t" \
    "s_branch vv_Gag28_%=\n\t" \
    "vv_F30_%=:\n\t" \
    "v_mov_b32 v140, 5767424\n\t" \
    "v_mov_b32 v141, 2\n\t" \
    "global_store_dword v140, v141, s[36:37] sc1\n\t" \
    "s_waitcnt vmcnt(0)\n\t" \
    "s_branch vv_Gredo29_%=\n\t" \
    "vv_Gredo29_%=:\n\t" \
    "v_mov_b32 v72, 0\n\t" \
    "v_mov_b32 v73, 0\n\t" \
    "v_mov_b32 v74, 0\n\t" \
    "v_mov_b32 v75, 0\n\t" \
    "v_mov_b32 v76, 0\n\t" \
    "v_mov_b32 v77, 0\n\t" \
    "v_mov_b32 v78, 0\n\t" \
    "v_mov_b32 v79, 0\n\t" \
    "v_smfmac_f32_16x16x64_bf16 v[72:75], v[36:39], a[0:7], v10\n\t" \
    "v_smfmac_f32_16x16x64_bf16 v[72:75], v[40:43], a[8:15], v10\n\t" \
    "v_smfmac_f32_16x16x64_bf16 v[72:75], v[44:47], a[64:71], v10\n\t" \
    "v_smfmac_f32_16x16x64_bf16 v[72:75], v[48:51], a[16:23], v10\n\t" \
    "v_smfmac_f32_16x16x64_bf16 v[72:75], v[52:55], a[24:31], v10\n\t" \
    "v_smfmac_f32_16x16x64_bf16 v[72:75], v[56:59], a[32:39], v10\n\t" \
    "v_smfmac_f32_16x16x64_bf16 v[72:75], v[60:63], a[40:47], v10\n\t" \
    "v_smfmac_f32_16x16x64_bf16 v[72:75], v[64:67], a[48:55], v10\n\t" \
    "v_smfmac_f32_16x16x64_bf16 v[72:75], v[68:71], a[56:63], v10\n\t" \
    "v_smfmac_f32_16x16x64_bf16 v[76:79], v[36:39], a[72:79], v10\n\t" \
    "v_smfmac_f32_16x16x64_bf16 v[76:79], v[40:43], a[80:87], v10\n\t" \
    "v_smfmac_f32_16x16x64_bf16 v[76:79], v[44:47], a[136:143], v10\n\t" \
    "v_smfmac_f32_16x16x64_bf16 v[76:79], v[48:51], a[88:95], v10\n\t" \
    "v_smfmac_f32_16x16x64_bf16 v[76:79], v[52:55], a[96:103], v10\n\t" \
    "v_smfmac_f32_16x16x64_bf16 v[76:79], v[56:59], a[104:111], v10\n\t" \
    "v_smfmac_f32_16x16x64_bf16 v[76:79], v[60:63], a[112:119], v10\n\t" \
    "v_smfmac_f32_16x16x64_bf16 v[76:79], v[64:67], a[120:127], v10\n\t" \
    "v_smfmac_f32_16x16x64_bf16 v[76:79], v[68:71], a[128:135], v10\n\t" \
    "v_add_f32 v72, v72, v74\n\t" \
    "v_add_f32 v73, v73, v75\n\t" \
    "v_cvt_pk_bf16_f32 v80, v72, v73\n\t" \
    "ds_write_b32 v12, v80 offset:0\n\t" \
    "s_nop 7\n\t" \
    "s_branch vv_Gdone22_%=\n\t" \
    "vv_end13_%=:\n\t" \
    "s_endpgm\n\t" \
    ".p2align 6\n\t" \
    "s_nop 0\n\t" \
    "s_nop 0\n\t" \
    "s_nop 0\n\t" \
    "s_nop 0\n\t" \
    "s_nop 0\n\t" \
    "s_nop 0\n\t" \
    "vv_loop10_%=:\n\t" \
    "s_getpc_b64 s[30:31]\n\t" \
    "vv_Wp33_%=:\n\t" \
    "s_add_u32 s30, s30, vv_We34_%=-vv_Wp33_%=\n\t" \
    "s_addc_u32 s31, s31, 0\n\t" \
    "s_sub_u32 s70, s70, 96\n\t" \
    "vv_W32_%=:\n\t" \
    "s_memtime s[56:57]\n\t" \
    "s_waitcnt lgkmcnt(0)\n\t" \
    "s_sub_u32 s69, s70, s56\n\t" \
    "s_cmp_gt_i32 s69, 80\n\t" \
    "s_cbranch_scc1 vv_W32_%=\n\t" \
    "s_max_i32 s69, s69, 0\n\t" \
    "s_and_b32 s69, s69, -4\n\t" \
    "s_sub_u32 s30, s30, s69\n\t" \
    "s_subb_u32 s31, s31, 0\n\t" \
    "s_setpc_b64 s[30:31]\n\t" \
    "s_nop 0\n\t" \
    "s_nop 0\n\t" \
    "s_nop 0\n\t" \
    "s_nop 0\n\t" \
    "s_nop 0\n\t" \
    "s_nop 0\n\t" \
    "s_nop 0\n\t" \
    "s_nop 0\n\t" \
    "s_nop 0\n\t" \
    "s_nop 0\n\t" \
    "s_nop 0\n\t" \
    "s_nop 0\n\t" \
    "s_nop 0\n\t" \
    "s_nop 0\n\t" \
    "s_nop 0\n\t" \
    "s_nop 0\n\t" \
    "s_nop 0\n\t" \
    "s_nop 0\n\t" \
    "s_nop 0\n\t" \
    "s_nop 0\n\t" \
    "vv_We34_%=:\n\t" \
    "vv_G35_%=:\n\t" \
    "global_load_dwordx4 v[36:39], v1, s[46:47] sc1\n\t" \
    "global_load_dwordx4 v[40:43], v23, s[46:47] sc1\n\t" \
    "s_nop 7\n\t" \
    "global_load_dwordx4 v[44:47], v2, s[46:47] sc1\n\t" \
    "s_waitcnt vmcnt(2)\n\t" \
    "v_smfmac_f32_16x16x64_bf16 v[72:75], v[36:39], a[16:23], v10\n\t" \
    "ds_write_b128 v3, v[36:39]\n\t" \
    "v_smfmac_f32_16x16x64_bf16 v[76:79], v[36:39], a[88:95], v10\n\t" \
    "s_waitcnt vmcnt(1)\n\t" \
    "v_cmp_u_f32 vcc, v36, v40\n\t" \
    "ds_write_b128 v3, v[40:43] offset:1024\n\t" \
    "v_smfmac_f32_16x16x64_bf16 v[72:75], v[40:43], a[24:31], v10\n\t" \
    "s_cbranch_vccnz vv_Gbad36_%=\n\t" \
    "vv_Gc37_%=:\n\t" \
    "s_waitcnt lgkmcnt(0)\n\t" \
    "s_barrier\n\t" \
    "ds_read_b128 v[48:51], v4\n\t" \
    "ds_read_b128 v[52:55], v5\n\t" \
    "v_smfmac_f32_16x16x64_bf16 v[76:79], v[40:43], a[96:103], v10\n\t" \
    "s_waitcnt vmcnt(0)\n\t" \
    "v_cmp_eq_u32 vcc, -1, v44\n\t" \
    "ds_read_b128 v[56:59], v6\n\t" \
    "ds_read_b128 v[60:63], v7\n\t" \
    "ds_read_b128 v[64:67], v8\n\t" \
    "ds_read_b128 v[68:71], v9\n\t" \
    "s_cbranch_vccnz vv_Gbad241_%=\n\t" \
    "vv_Gc242_%=:\n\t" \
    "v_smfmac_f32_16x16x64_bf16 v[72:75], v[44:47], a[64:71], v10\n\t" \
    "v_smfmac_f32_16x16x64_bf16 v[76:79], v[44:47], a[136:143], v10\n\t" \
    "s_waitcnt lgkmcnt(5)\n\t" \
    "v_smfmac_f32_16x16x64_bf16 v[72:75], v[48:51], a[32:39], v10\n\t" \
    "s_waitcnt lgkmcnt(4)\n\t" \
    "v_smfmac_f32_16x16x64_bf16 v[72:75], v[52:55], a[40:47], v10\n\t" \
    "s_waitcnt lgkmcnt(3)\n\t" \
    "v_smfmac_f32_16x16x64_bf16 v[72:75], v[56:59], a[48:55], v10\n\t" \
    "s_waitcnt lgkmcnt(2)\n\t" \
    "v_smfmac_f32_16x16x64_bf16 v[72:75], v[60:63], a[56:63], v10\n\t" \
    "s_waitcnt lgkmcnt(1)\n\t" \
    "v_smfmac_f32_16x16x64_bf16 v[72:75], v[64:67], a[0:7], v10\n\t" \
    "s_waitcnt lgkmcnt(0)\n\t" \
    "v_smfmac_f32_16x16x64_bf16 v[72:75], v[68:71], a[8:15], v10\n\t" \
    "v_smfmac_f32_16x16x64_bf16 v[76:79], v[48:51], a[104:111], v10\n\t" \
    "s_nop 1\n\t" \
    "s_nop 0\n\t" \
    "v_smfmac_f32_16x16x64_bf16 v[76:79], v[52:55], a[112:119], v10\n\t" \
    "s_nop 1\n\t" \
    "s_nop 0\n\t" \
    "v_smfmac_f32_16x16x64_bf16 v[76:79], v[56:59], a[120:127], v10\n\t" \
    "v_add_f32 v72, v72, v74\n\t" \
    "v_add_f32 v73, v73, v75\n\t" \
    "v_smfmac_f32_16x16x64_bf16 v[76:79], v[60:63], a[128:135], v10\n\t" \
    "v_cvt_pk_bf16_f32 v80, v72, v73\n\t" \
    "v_smfmac_f32_16x16x64_bf16 v[76:79], v[64:67], a[72:79], v10\n\t" \
    "v_cmp_u_f32 vcc, v72, v73\n\t" \
    "ds_write_b32 v12, v80 offset:0\n\t" \
    "v_smfmac_f32_16x16x64_bf16 v[76:79], v[68:71], a[80:87], v10\n\t" \
    "s_cbranch_vccnz vv_Gslow38_%=\n\t" \
    "vv_Gback39_%=:\n\t" \
    "vv_Gdone40_%=:\n\t" \
    "s_nop 6\n\t" \
    "v_add_f32 v76, v76, v78\n\t" \
    "v_add_f32 v77, v77, v79\n\t" \
    "v_cvt_pk_bf16_f32 v81, v76, v77\n\t" \
    "ds_write_b32 v12, v81 offset:256\n\t" \
    "s_waitcnt lgkmcnt(0)\n\t" \
    "s_barrier\n\t" \
    "ds_read_b64_tr_b16 v[84:85], v13 offset:0\n\t" \
    "v_smfmac_f32_16x16x64_bf16 v[100:103], v[36:39], a[0:7], v10\n\t" \
    "ds_read_b64_tr_b16 v[86:87], v13 offset:64\n\t" \
    "ds_read_b64_tr_b16 v[92:93], v13 offset:1152\n\t" \
    "v_smfmac_f32_16x16x64_bf16 v[100:103], v[36:39], a[0:7], v10\n\t" \
    "ds_read_b64_tr_b16 v[94:95], v13 offset:1216\n\t" \
    "ds_read_b64_tr_b16 v[88:89], v25 offset:0\n\t" \
    "v_smfmac_f32_16x16x64_bf16 v[100:103], v[36:39], a[0:7], v10\n\t" \
    "ds_read_b64_tr_b16 v[90:91], v25 offset:64\n\t" \
    "s_waitcnt lgkmcnt(4)\n\t" \
    "v_smfmac_f32_16x16x64_bf16 v[124:127], v[84:87], v[240:247], v19\n\t" \
    "ds_read_b64_tr_b16 v[96:97], v25 offset:1152\n\t" \
    "ds_read_b64_tr_b16 v[98:99], v25 offset:1216\n\t" \
    "s_waitcnt lgkmcnt(4)\n\t" \
    "v_smfmac_f32_16x16x64_bf16 v[124:127], v[92:95], v[248:255], v19\n\t" \
    "s_nop 2\n\t" \
    "v_smfmac_f32_16x16x64_bf16 v[108:111], v[84:87], a[144:151], v19\n\t" \
    "s_nop 2\n\t" \
    "v_smfmac_f32_16x16x64_bf16 v[108:111], v[92:95], a[160:167], v19\n\t" \
    "s_nop 1\n\t" \
    "v_permlane32_swap_b32 v124, v126\n\t" \
    "v_smfmac_f32_16x16x64_bf16 v[112:115], v[84:87], a[176:183], v19\n\t" \
    "s_nop 1\n\t" \
    "v_permlane32_swap_b32 v125, v127\n\t" \
    "v_smfmac_f32_16x16x64_bf16 v[112:115], v[92:95], a[192:199], v19\n\t" \
    "s_nop 0\n\t" \
    "v_add_f32 v124, v124, v126\n\t" \
    "s_waitcnt lgkmcnt(2)\n\t" \
    "v_smfmac_f32_16x16x64_bf16 v[108:111], v[88:91], a[152:159], v19\n\t" \
    "s_nop 1\n\t" \
    "v_add_f32 v125, v125, v127\n\t" \
    "v_smfmac_f32_16x16x64_bf16 v[112:115], v[88:91], a[184:191], v19\n\t" \
    "s_nop 0\n\t" \
    "v_cvt_pk_bf16_f32 v136, v124, v125\n\t" \
    "s_waitcnt lgkmcnt(0)\n\t" \
    "v_smfmac_f32_16x16x64_bf16 v[108:111], v[96:99], a[168:175], v19\n\t" \
    "s_nop 1\n\t" \
    "v_cvt_pk_bf16_f32 v137, v124, v125\n\t" \
    "v_smfmac_f32_16x16x64_bf16 v[112:115], v[96:99], a[200:207], v19\n\t" \
    "s_nop 1\n\t" \
    "v_permlane32_swap_b32 v136, v137\n\t" \
    "v_smfmac_f32_16x16x64_bf16 v[116:119], v[84:87], a[208:215], v19\n\t" \
    "s_mov_b32 exec_hi, 0\n\t" \
    "global_store_dwordx2 v16, v[136:137], s[38:39] sc1\n\t" \
    "s_mov_b32 exec_hi, -1\n\t" \
    "v_cvt_pk_bf16_f32 v128, v108, v109\n\t" \
    "v_smfmac_f32_16x16x64_bf16 v[116:119], v[88:91], a[216:223], v19\n\t" \
    "s_nop 1\n\t" \
    "v_cvt_pk_bf16_f32 v129, v110, v111\n\t" \
    "v_smfmac_f32_16x16x64_bf16 v[116:119], v[92:95], a[224:231], v19\n\t" \
    "s_nop 1\n\t" \
    "v_cvt_pk_bf16_f32 v130, v112, v113\n\t" \
    "v_smfmac_f32_16x16x64_bf16 v[116:119], v[96:99], a[232:239], v19\n\t" \
    "s_nop 1\n\t" \
    "v_cvt_pk_bf16_f32 v131, v114, v115\n\t" \
    "v_smfmac_f32_16x16x64_bf16 v[120:123], v[84:87], a[240:247], v19\n\t" \
    "s_nop 1\n\t" \
    "v_permlane16_swap_b32 v128, v130\n\t" \
    "v_smfmac_f32_16x16x64_bf16 v[120:123], v[88:91], a[248:255], v19\n\t" \
    "s_nop 1\n\t" \
    "v_permlane16_swap_b32 v129, v131\n\t" \
    "v_smfmac_f32_16x16x64_bf16 v[120:123], v[92:95], v[224:231], v19\n\t" \
    "s_nop 0\n\t" \
    "global_store_dwordx4 v14, v[128:131], s[38:39] sc1\n\t" \
    "v_cvt_pk_bf16_f32 v132, v116, v117\n\t" \
    "v_smfmac_f32_16x16x64_bf16 v[120:123], v[96:99], v[232:239], v19\n\t" \
    "s_nop 1\n\t" \
    "v_cvt_pk_bf16_f32 v133, v118, v119\n\t" \
    "s_nop 4\n\t" \
    "v_cvt_pk_bf16_f32 v134, v120, v121\n\t" \
    "v_cvt_pk_bf16_f32 v135, v122, v123\n\t" \
    "s_nop 0\n\t" \
    "v_permlane16_swap_b32 v132, v134\n\t" \
    "v_permlane16_swap_b32 v133, v135\n\t" \
    "global_store_dwordx4 v15, v[132:135], s[38:39] sc1\n\t" \
    "s_cmp_eq_u32 s65, 63\n\t" \
    "s_cbranch_scc1 vv_end31_%=\n\t" \
    "s_add_u32 s65, s65, 1\n\t" \
    "v_mov_b32 v72, 0\n\t" \
    "v_mov_b32 v73, 0\n\t" \
    "v_mov_b32 v74, 0\n\t" \
    "v_mov_b32 v75, 0\n\t" \
    "v_mov_b32 v76, 0\n\t" \
    "v_mov_b32 v77, 0\n\t" \
    "v_mov_b32 v78, 0\n\t" \
    "v_mov_b32 v79, 0\n\t" \
    "v_mov_b32 v108, 0\n\t" \
    "v_mov_b32 v109, 0\n\t" \
    "v_mov_b32 v110, 0\n\t" \
    "v_mov_b32 v111, 0\n\t" \
    "v_mov_b32 v112, 0\n\t" \
    "v_mov_b32 v113, 0\n\t" \
    "v_mov_b32 v114, 0\n\t" \
    "v_mov_b32 v115, 0\n\t" \
    "v_mov_b32 v116, 0\n\t" \
    "v_mov_b32 v117, 0\n\t" \
    "v_mov_b32 v118, 0\n\t" \
    "v_mov_b32 v119, 0\n\t" \
    "v_mov_b32 v120, 0\n\t" \
    "v_mov_b32 v121, 0\n\t" \
    "v_mov_b32 v122, 0\n\t" \
    "v_mov_b32 v123, 0\n\t" \
    "v_mov_b32 v124, 0\n\t" \
    "v_mov_b32 v125, 0\n\t" \
    "v_mov_b32 v126, 0\n\t" \
    "v_mov_b32 v127, 0\n\t" \
    "s_add_u32 s69, s65, 0\n\t" \
    "s_and_b32 s69, s69, 7\n\t" \
    "s_lshl_b32 s69, s69, 18\n\t" \
    "s_add_u32 s69, s69, s62\n\t" \
    "s_add_u32 s46, s36, s69\n\t" \
    "s_addc_u32 s47, s37, 0\n\t" \
    "s_add_u32 s70, s65, 0\n\t" \
    "s_mul_i32 s69, s70, 171\n\t" \
    "s_lshr_b32 s69, s69, 9\n\t" \
    "s_mul_i32 s70, s70, 589824\n\t" \
    "s_mul_i32 s71, s69, 327680\n\t" \
    "s_add_u32 s70, s70, s71\n\t" \
    "s_lshl1_add_u32 s69, s69, 4\n\t" \
    "s_bitcmp1_b64 s[44:45], s69\n\t" \
    "s_cselect_b32 s69, 8192, 0\n\t" \
    "s_add_u32 s69, s69, s70\n\t" \
    "s_add_u32 s69, s69, 123731968\n\t" \
    "s_add_u32 s38, s36, s69\n\t" \
    "s_addc_u32 s39, s37, 0\n\t" \
    "s_and_b32 s69, s69, 0xffe00000\n\t" \
    "s_add_u32 s69, s69, 1823744\n\t" \
    "v_mov_b32 v17, s69\n\t" \
    "global_load_dword v17, v17, s[36:37]\n\t" \
    "s_barrier\n\t" \
    "ds_read_b32 v160, v11 offset:20480\n\t" \
    "s_waitcnt lgkmcnt(0)\n\t" \
    "v_readfirstlane_b32 s70, v160\n\t" \
    "s_branch vv_loop10_%=\n\t" \
    "vv_Gbad36_%=:\n\t" \
    "v_max_u32 v140, v36, v40\n\t" \
    "v_cmp_eq_u32 vcc, -1, v140\n\t" \
    "s_cbranch_vccz vv_Gc37_%=\n\t" \
    "s_waitcnt vmcnt(0) lgkmcnt(0)\n\t" \
    "s_nop 7\n\t" \
    "v_mov_b32 v72, 0\n\t" \
    "v_mov_b32 v73, 0\n\t" \
    "v_mov_b32 v74, 0\n\t" \
    "v_mov_b32 v75, 0\n\t" \
    "v_mov_b32 v76, 0\n\t" \
    "v_mov_b32 v77, 0\n\t" \
    "v_mov_b32 v78, 0\n\t" \
    "v_mov_b32 v79, 0\n\t" \
    "s_cmp_eq_u32 s68, 0\n\t" \
    "s_cbranch_scc1 vv_F43_%=\n\t" \
    "s_sub_u32 s68, s68, 1\n\t" \
    "s_branch vv_G35_%=\n\t" \
    "vv_F43_%=:\n\t" \
    "v_mov_b32 v140, 5767424\n\t" \
    "v_mov_b32 v141, 1\n\t" \
    "global_store_dword v140, v141, s[36:37] sc1\n\t" \
    "s_waitcnt vmcnt(0)\n\t" \
    "s_branch vv_Gc37_%=\n\t" \
    "vv_Gbad241_%=:\n\t" \
    "global_load_dwordx4 v[44:47], v2, s[46:47] sc1\n\t" \
    "s_waitcnt vmcnt(0)\n\t" \
    "v_cmp_eq_u32 vcc, -1, v44\n\t" \
    "s_cbranch_vccz vv_Gc242_%=\n\t" \
    "s_cmp_eq_u32 s68, 0\n\t" \
    "s_cbranch_scc1 vv_F44_%=\n\t" \
    "s_sub_u32 s68, s68, 1\n\t" \
    "s_sleep 1\n\t" \
    "s_branch vv_Gbad241_%=\n\t" \
    "vv_F44_%=:\n\t" \
    "v_mov_b32 v140, 5767424\n\t" \
    "v_mov_b32 v141, 2\n\t" \
    "global_store_dword v140, v141, s[36:37] sc1\n\t" \
    "s_waitcnt vmcnt(0)\n\t" \
    "s_branch vv_Gc242_%=\n\t" \
    "vv_Gslow38_%=:\n\t" \
    "v_max_u32 v140, v36, v37\n\t" \
    "v_max3_u32 v140, v140, v38, v39\n\t" \
    "v_max3_u32 v140, v140, v40, v41\n\t" \
    "v_max3_u32 v140, v140, v42, v43\n\t" \
    "v_max3_u32 v140, v140, v44, v45\n\t" \
    "v_max3_u32 v140, v140, v46, v47\n\t" \
    "v_max3_u32 v140, v140, v48, v49\n\t" \
    "v_max3_u32 v140, v140, v50, v51\n\t" \
    "v_max3_u32 v140, v140, v52, v53\n\t" \
    "v_max3_u32 v140, v140, v54, v55\n\t" \
    "v_max3_u32 v140, v140, v56, v57\n\t" \
    "v_max3_u32 v140, v140, v58, v59\n\t" \
    "v_max3_u32 v140, v140, v60, v61\n\t" \
    "v_max3_u32 v140, v140, v62, v63\n\t" \
    "v_max3_u32 v140, v140, v64, v65\n\t" \
    "v_max3_u32 v140, v140, v66, v67\n\t" \
    "v_max3_u32 v140, v140, v68, v69\n\t" \
    "v_max3_u32 v140, v140, v70, v71\n\t" \
    "v_cmp_eq_u32 vcc, -1, v140\n\t" \
    "s_cbranch_vccz vv_Gback39_%=\n\t" \
    "v_sub_u32 v140, v1, v3\n\t" \
    "s_nop 0\n\t" \
    "v_readfirstlane_b32 s69, v140\n\t" \
    "s_nop 7\n\t" \
    "s_add_u32 s30, s46, s69\n\t" \
    "s_addc_u32 s31, s47, 0\n\t" \
    "vv_Gag45_%=:\n\t" \
    "global_load_dwordx4 v[36:39], v1, s[46:47] sc1\n\t" \
    "global_load_dwordx4 v[40:43], v23, s[46:47] sc1\n\t" \
    "global_load_dwordx4 v[44:47], v2, s[46:47] sc1\n\t" \
    "global_load_dwordx4 v[48:51], v4, s[30:31] sc1\n\t" \
    "global_load_dwordx4 v[52:55], v5, s[30:31] sc1\n\t" \
    "global_load_dwordx4 v[56:59], v6, s[30:31] sc1\n\t" \
    "global_load_dwordx4 v[60:63], v7, s[30:31] sc1\n\t" \
    "global_load_dwordx4 v[64:67], v8, s[30:31] sc1\n\t" \
    "global_load_dwordx4 v[68:71], v9, s[30:31] sc1\n\t" \
    "s_waitcnt vmcnt(0) lgkmcnt(0)\n\t" \
    "v_max_u32 v140, v36, v37\n\t" \
    "v_max3_u32 v140, v140, v38, v39\n\t" \
    "v_max3_u32 v140, v140, v40, v41\n\t" \
    "v_max3_u32 v140, v140, v42, v43\n\t" \
    "v_max3_u32 v140, v140, v44, v45\n\t" \
    "v_max3_u32 v140, v140, v46, v47\n\t" \
    "v_max3_u32 v140, v140, v48, v49\n\t" \
    "v_max3_u32 v140, v140, v50, v51\n\t" \
    "v_max3_u32 v140, v140, v52, v53\n\t" \
    "v_max3_u32 v140, v140, v54, v55\n\t" \
    "v_max3_u32 v140, v140, v56, v57\n\t" \
    "v_max3_u32 v140, v140, v58, v59\n\t" \
    "v_max3_u32 v140, v140, v60, v61\n\t" \
    "v_max3_u32 v140, v140, v62, v63\n\t" \
    "v_max3_u32 v140, v140, v64, v65\n\t" \
    "v_max3_u32 v140, v140, v66, v67\n\t" \
    "v_max3_u32 v140, v140, v68, v69\n\t" \
    "v_max3_u32 v140, v140, v70, v71\n\t" \
    "v_cmp_eq_u32 vcc, -1, v140\n\t" \
    "s_cbranch_vccz vv_Gredo46_%=\n\t" \
    "s_cmp_eq_u32 s68, 0\n\t" \
    "s_cbranch_scc1 vv_F47_%=\n\t" \
    "s_sub_u32 s68, s68, 1\n\t" \
    "s_sleep 1\n\t" \
    "s_branch vv_Gag45_%=\n\t" \
    "vv_F47_%=:\n\t" \
    "v_mov_b32 v140, 5767424\n\t" \
    "v_mov_b32 v141, 2\n\t" \
    "global_store_dword v140, v141, s[36:37] sc1\n\t" \
    "s_waitcnt vmcnt(0)\n\t" \
    "s_branch vv_Gredo46_%=\n\t" \
    "vv_Gredo46_%=:\n\t" \
    "v_mov_b32 v72, 0\n\t" \
    "v_mov_b32 v73, 0\n\t" \
    "v_mov_b32 v74, 0\n\t" \
    "v_mov_b32 v75, 0\n\t" \
    "v_mov_b32 v76, 0\n\t" \
    "v_mov_b32 v77, 0\n\t" \
    "v_mov_b32 v78, 0\n\t" \
    "v_mov_b32 v79, 0\n\t" \
    "v_smfmac_f32_16x16x64_bf16 v[72:75], v[36:39], a[16:23], v10\n\t" \
    "v_smfmac_f32_16x16x64_bf16 v[72:75], v[40:43], a[24:31], v10\n\t" \
    "v_smfmac_f32_16x16x64_bf16 v[72:75], v[44:47], a[64:71], v10\n\t" \
    "v_smfmac_f32_16x16x64_bf16 v[72:75], v[48:51], a[32:39], v10\n\t" \
    "v_smfmac_f32_16x16x64_bf16 v[72:75], v[52:55], a[40:47], v10\n\t" \
    "v_smfmac_f32_16x16x64_bf16 v[72:75], v[56:59], a[48:55], v10\n\t" \
    "v_smfmac_f32_16x16x64_bf16 v[72:75], v[60:63], a[56:63], v10\n\t" \
    "v_smfmac_f32_16x16x64_bf16 v[72:75], v[64:67], a[0:7], v10\n\t" \
    "v_smfmac_f32_16x16x64_bf16 v[72:75], v[68:71], a[8:15], v10\n\t" \
    "v_smfmac_f32_16x16x64_bf16 v[76:79], v[36:39], a[88:95], v10\n\t" \
    "v_smfmac_f32_16x16x64_bf16 v[76:79], v[40:43], a[96:103], v10\n\t" \
    "v_smfmac_f32_16x16x64_bf16 v[76:79], v[44:47], a[136:143], v10\n\t" \
    "v_smfmac_f32_16x16x64_bf16 v[76:79], v[48:51], a[104:111], v10\n\t" \
    "v_smfmac_f32_16x16x64_bf16 v[76:79], v[52:55], a[112:119], v10\n\t" \
    "v_smfmac_f32_16x16x64_bf16 v[76:79], v[56:59], a[120:127], v10\n\t" \
    "v_smfmac_f32_16x16x64_bf16 v[76:79], v[60:63], a[128:135], v10\n\t" \
    "v_smfmac_f32_16x16x64_bf16 v[76:79], v[64:67], a[72:79], v10\n\t" \
    "v_smfmac_f32_16x16x64_bf16 v[76:79], v[68:71], a[80:87], v10\n\t" \
    "v_add_f32 v72, v72, v74\n\t" \
    "v_add_f32 v73, v73, v75\n\t" \
    "v_cvt_pk_bf16_f32 v80, v72, v73\n\t" \
    "ds_write_b32 v12, v80 offset:0\n\t" \
    "s_nop 7\n\t" \
    "s_branch vv_Gdone40_%=\n\t" \
    "vv_end31_%=:\n\t" \
    "s_endpgm\n\t" \
    ".p2align 6\n\t" \
    "s_nop 0\n\t" \
    "s_nop 0\n\t" \
    "s_nop 0\n\t" \
    "s_nop 0\n\t" \
    "s_nop 0\n\t" \
    "s_nop 0\n\t" \
    "vv_loop11_%=:\n\t" \
    "s_getpc_b64 s[30:31]\n\t" \
    "vv_Wp50_%=:\n\t" \
    "s_add_u32 s30, s30, vv_We51_%=-vv_Wp50_%=\n\t" \
    "s_addc_u32 s31, s31, 0\n\t" \
    "s_sub_u32 s70, s70, 96\n\t" \
    "vv_W49_%=:\n\t" \
    "s_memtime s[56:57]\n\t" \
    "s_waitcnt lgkmcnt(0)\n\t" \
    "s_sub_u32 s69, s70, s56\n\t" \
    "s_cmp_gt_i32 s69, 80\n\t" \
    "s_cbranch_scc1 vv_W49_%=\n\t" \
    "s_max_i32 s69, s69, 0\n\t" \
    "s_and_b32 s69, s69, -4\n\t" \
    "s_sub_u32 s30, s30, s69\n\t" \
    "s_subb_u32 s31, s31, 0\n\t" \
    "s_setpc_b64 s[30:31]\n\t" \
    "s_nop 0\n\t" \
    "s_nop 0\n\t" \
    "s_nop 0\n\t" \
    "s_nop 0\n\t" \
    "s_nop 0\n\t" \
    "s_nop 0\n\t" \
    "s_nop 0\n\t" \
    "s_nop 0\n\t" \
    "s_nop 0\n\t" \
    "s_nop 0\n\t" \
    "s_nop 0\n\t" \
    "s_nop 0\n\t" \
    "s_nop 0\n\t" \
    "s_nop 0\n\t" \
    "s_nop 0\n\t" \
    "s_nop 0\n\t" \
    "s_nop 0\n\t" \
    "s_nop 0\n\t" \
    "s_nop 0\n\t" \
    "s_nop 0\n\t" \
    "vv_We51_%=:\n\t" \
    "vv_G52_%=:\n\t" \
    "global_load_dwordx4 v[36:39], v1, s[46:47] sc1\n\t" \
    "global_load_dwordx4 v[40:43], v23, s[46:47] sc1\n\t" \
    "s_nop 7\n\t" \
    "global_load_dwordx4 v[44:47], v2, s[46:47] sc1\n\t" \
    "s_waitcnt vmcnt(2)\n\t" \
    "v_smfmac_f32_16x16x64_bf16 v[72:75], v[36:39], a[32:39], v10\n\t" \
    "ds_write_b128 v3, v[36:39]\n\t" \
    "v_smfmac_f32_16x16x64_bf16 v[76:79], v[36:39], a[104:111], v10\n\t" \
    "s_waitcnt vmcnt(1)\n\t" \
    "v_cmp_u_f32 vcc, v36, v40\n\t" \
    "ds_write_b128 v3, v[40:43] offset:1024\n\t" \
    "v_smfmac_f32_16x16x64_bf16 v[72:75], v[40:43], a[40:47], v10\n\t" \
    "s_cbranch_vccnz vv_Gbad53_%=\n\t" \
    "vv_Gc54_%=:\n\t" \
    "s_waitcnt lgkmcnt(0)\n\t" \
    "s_barrier\n\t" \
    "ds_read_b128 v[48:51], v4\n\t" \
    "ds_read_b128 v[52:55], v5\n\t" \
    "v_smfmac_f32_16x16x64_bf16 v[76:79], v[40:43], a[112:119], v10\n\t" \
    "s_waitcnt vmcnt(0)\n\t" \
    "v_cmp_eq_u32 vcc, -1, v44\n\t" \
    "ds_read_b128 v[56:59], v6\n\t" \
    "ds_read_b128 v[60:63], v7\n\t" \
    "ds_read_b128 v[64:67], v8\n\t" \
    "ds_read_b128 v[68:71], v9\n\t" \
    "s_cbranch_vccnz vv_Gbad258_%=\n\t" \
    "vv_Gc259_%=:\n\t" \
    "v_smfmac_f32_16x16x64_bf16 v[72:75], v[44:47], a[64:71], v10\n\t" \
    "v_smfmac_f32_16x16x64_bf16 v[76:79], v[44:47], a[136:143], v10\n\t" \
    "s_waitcnt lgkmcnt(5)\n\t" \
    "v_smfmac_f32_16x16x64_bf16 v[72:75], v[48:51], a[48:55], v10\n\t" \
    "s_waitcnt lgkmcnt(4)\n\t" \
    "v_smfmac_f32_16x16x64_bf16 v[72:75], v[52:55], a[56:63], v10\n\t" \
    "s_waitcnt lgkmcnt(3)\n\t" \
    "v_smfmac_f32_16x16x64_bf16 v[72:75], v[56:59], a[0:7], v10\n\t" \
    "s_waitcnt lgkmcnt(2)\n\t" \
    "v_smfmac_f32_16x16x64_bf16 v[72:75], v[60:63], a[8:15], v10\n\t" \
    "s_waitcnt lgkmcnt(1)\n\t" \
    "v_smfmac_f32_16x16x64_bf16 v[72:75], v[64:67], a[16:23], v10\n\t" \
    "s_waitcnt lgkmcnt(0)\n\t" \
    "v_smfmac_f32_16x16x64_bf16 v[72:75], v[68:71], a[24:31], v10\n\t" \
    "v_smfmac_f32_16x16x64_bf16 v[76:79], v[48:51], a[120:127], v10\n\t" \
    "s_nop 1\n\t" \
    "s_nop 0\n\t" \
    "v_smfmac_f32_16x16x64_bf16 v[76:79], v[52:55], a[128:135], v10\n\t" \
    "s_nop 1\n\t" \
    "s_nop 0\n\t" \
    "v_smfmac_f32_16x16x64_bf16 v[76:79], v[56:59], a[72:79], v10\n\t" \
    "v_add_f32 v72, v72, v74\n\t" \
    "v_add_f32 v73, v73, v75\n\t" \
    "v_smfmac_f32_16x16x64_bf16 v[76:79], v[60:63], a[80:87], v10\n\t" \
    "v_cvt_pk_bf16_f32 v80, v72, v73\n\t" \
    "v_smfmac_f32_16x16x64_bf16 v[76:79], v[64:67], a[88:95], v10\n\t" \
    "v_cmp_u_f32 vcc, v72, v73\n\t" \
    "ds_write_b32 v12, v80 offset:0\n\t" \
    "v_smfmac_f32_16x16x64_bf16 v[76:79], v[68:71], a[96:103], v10\n\t" \
    "s_cbranch_vccnz vv_Gslow55_%=\n\t" \
    "vv_Gback56_%=:\n\t" \
    "vv_Gdone57_%=:\n\t" \
    "s_nop 6\n\t" \
    "v_add_f32 v76, v76, v78\n\t" \
    "v_add_f32 v77, v77, v79\n\t" \
    "v_cvt_pk_bf16_f32 v81, v76, v77\n\t" \
    "ds_write_b32 v12, v81 offset:256\n\t" \
    "s_waitcnt lgkmcnt(0)\n\t" \
    "s_barrier\n\t" \
    "ds_read_b64_tr_b16 v[84:85], v13 offset:0\n\t" \
    "v_smfmac_f32_16x16x64_bf16 v[100:103], v[36:39], a[0:7], v10\n\t" \
    "ds_read_b64_tr_b16 v[86:87], v13 offset:64\n\t" \
    "ds_read_b64_tr_b16 v[92:93], v13 offset:1152\n\t" \
    "v_smfmac_f32_16x16x64_bf16 v[100:103], v[36:39], a[0:7], v10\n\t" \
    "ds_read_b64_tr_b16 v[94:95], v13 offset:1216\n\t" \
    "ds_read_b64_tr_b16 v[88:89], v25 offset:0\n\t" \
    "v_smfmac_f32_16x16x64_bf16 v[100:103], v[36:39], a[0:7], v10\n\t" \
    "ds_read_b64_tr_b16 v[90:91], v25 offset:64\n\t" \
    "s_waitcnt lgkmcnt(4)\n\t" \
    "v_smfmac_f32_16x16x64_bf16 v[124:127], v[84:87], v[240:247], v19\n\t" \
    "ds_read_b64_tr_b16 v[96:97], v25 offset:1152\n\t" \
    "ds_read_b64_tr_b16 v[98:99], v25 offset:1216\n\t" \
    "s_waitcnt lgkmcnt(4)\n\t" \
    "v_smfmac_f32_16x16x64_bf16 v[124:127], v[92:95], v[248:255], v19\n\t" \
    "s_nop 2\n\t" \
    "v_smfmac_f32_16x16x64_bf16 v[108:111], v[84:87], a[144:151], v19\n\t" \
    "s_nop 2\n\t" \
    "v_smfmac_f32_16x16x64_bf16 v[108:111], v[92:95], a[160:167], v19\n\t" \
    "s_nop 1\n\t" \
    "v_permlane32_swap_b32 v124, v126\n\t" \
    "v_smfmac_f32_16x16x64_bf16 v[112:115], v[84:87], a[176:183], v19\n\t" \
    "s_nop 1\n\t" \
    "v_permlane32_swap_b32 v125, v127\n\t" \
    "v_smfmac_f32_16x16x64_bf16 v[112:115], v[92:95], a[192:199], v19\n\t" \
    "s_nop 0\n\t" \
    "v_add_f32 v124, v124, v126\n\t" \
    "s_waitcnt lgkmcnt(2)\n\t" \
    "v_smfmac_f32_16x16x64_bf16 v[108:111], v[88:91], a[152:159], v19\n\t" \
    "s_nop 1\n\t" \
    "v_add_f32 v125, v125, v127\n\t" \
    "v_smfmac_f32_16x16x64_bf16 v[112:115], v[88:91], a[184:191], v19\n\t" \
    "s_nop 0\n\t" \
    "v_cvt_pk_bf16_f32 v136, v124, v125\n\t" \
    "s_waitcnt lgkmcnt(0)\n\t" \
    "v_smfmac_f32_16x16x64_bf16 v[108:111], v[96:99], a[168:175], v19\n\t" \
    "s_nop 1\n\t" \
    "v_cvt_pk_bf16_f32 v137, v124, v125\n\t" \
    "v_smfmac_f32_16x16x64_bf16 v[112:115], v[96:99], a[200:207], v19\n\t" \
    "s_nop 1\n\t" \
    "v_permlane32_swap_b32 v136, v137\n\t" \
    "v_smfmac_f32_16x16x64_bf16 v[116:119], v[84:87], a[208:215], v19\n\t" \
    "s_mov_b32 exec_hi, 0\n\t" \
    "global_store_dwordx2 v16, v[136:137], s[38:39] sc1\n\t" \
    "s_mov_b32 exec_hi, -1\n\t" \
    "v_cvt_pk_bf16_f32 v128, v108, v109\n\t" \
    "v_smfmac_f32_16x16x64_bf16 v[116:119], v[88:91], a[216:223], v19\n\t" \
    "s_nop 1\n\t" \
    "v_cvt_pk_bf16_f32 v129, v110, v111\n\t" \
    "v_smfmac_f32_16x16x64_bf16 v[116:119], v[92:95], a[224:231], v19\n\t" \
    "s_nop 1\n\t" \
    "v_cvt_pk_bf16_f32 v130, v112, v113\n\t" \
    "v_smfmac_f32_16x16x64_bf16 v[116:119], v[96:99], a[232:239], v19\n\t" \
    "s_nop 1\n\t" \
    "v_cvt_pk_bf16_f32 v131, v114, v115\n\t" \
    "v_smfmac_f32_16x16x64_bf16 v[120:123], v[84:87], a[240:247], v19\n\t" \
    "s_nop 1\n\t" \
    "v_permlane16_swap_b32 v128, v130\n\t" \
    "v_smfmac_f32_16x16x64_bf16 v[120:123], v[88:91], a[248:255], v19\n\t" \
    "s_nop 1\n\t" \
    "v_permlane16_swap_b32 v129, v131\n\t" \
    "v_smfmac_f32_16x16x64_bf16 v[120:123], v[92:95], v[224:231], v19\n\t" \
    "s_nop 0\n\t" \
    "global_store_dwordx4 v14, v[128:131], s[38:39] sc1\n\t" \
    "v_cvt_pk_bf16_f32 v132, v116, v117\n\t" \
    "v_smfmac_f32_16x16x64_bf16 v[120:123], v[96:99], v[232:239], v19\n\t" \
    "s_nop 1\n\t" \
    "v_cvt_pk_bf16_f32 v133, v118, v119\n\t" \
    "s_nop 4\n\t" \
    "v_cvt_pk_bf16_f32 v134, v120, v121\n\t" \
    "v_cvt_pk_bf16_f32 v135, v122, v123\n\t" \
    "s_nop 0\n\t" \
    "v_permlane16_swap_b32 v132, v134\n\t" \
    "v_permlane16_swap_b32 v133, v135\n\t" \
    "global_store_dwordx4 v15, v[132:135], s[38:39] sc1\n\t" \
    "s_cmp_eq_u32 s65, 63\n\t" \
    "s_cbranch_scc1 vv_end48_%=\n\t" \
    "s_add_u32 s65, s65, 1\n\t" \
    "v_mov_b32 v72, 0\n\t" \
    "v_mov_b32 v73, 0\n\t" \
    "v_mov_b32 v74, 0\n\t" \
    "v_mov_b32 v75, 0\n\t" \
    "v_mov_b32 v76, 0\n\t" \
    "v_mov_b32 v77, 0\n\t" \
    "v_mov_b32 v78, 0\n\t" \
    "v_mov_b32 v79, 0\n\t" \
    "v_mov_b32 v108, 0\n\t" \
    "v_mov_b32 v109, 0\n\t" \
    "v_mov_b32 v110, 0\n\t" \
    "v_mov_b32 v111, 0\n\t" \
    "v_mov_b32 v112, 0\n\t" \
    "v_mov_b32 v113, 0\n\t" \
    "v_mov_b32 v114, 0\n\t" \
    "v_mov_b32 v115, 0\n\t" \
    "v_mov_b32 v116, 0\n\t" \
    "v_mov_b32 v117, 0\n\t" \
    "v_mov_b32 v118, 0\n\t" \
    "v_mov_b32 v119, 0\n\t" \
    "v_mov_b32 v120, 0\n\t" \
    "v_mov_b32 v121, 0\n\t" \
    "v_mov_b32 v122, 0\n\t" \
    "v_mov_b32 v123, 0\n\t" \
    "v_mov_b32 v124, 0\n\t" \
    "v_mov_b32 v125, 0\n\t" \
    "v_mov_b32 v126, 0\n\t" \
    "v_mov_b32 v127, 0\n\t" \
    "s_add_u32 s69, s65, 0\n\t" \
    "s_and_b32 s69, s69, 7\n\t" \
    "s_lshl_b32 s69, s69, 18\n\t" \
    "s_add_u32 s69, s69, s62\n\t" \
    "s_add_u32 s46, s36, s69\n\t" \
    "s_addc_u32 s47, s37, 0\n\t" \
    "s_add_u32 s70, s65, 0\n\t" \
    "s_mul_i32 s69, s70, 171\n\t" \
    "s_lshr_b32 s69, s69, 9\n\t" \
    "s_mul_i32 s70, s70, 589824\n\t" \
    "s_mul_i32 s71, s69, 327680\n\t" \
    "s_add_u32 s70, s70, s71\n\t" \
    "s_lshl1_add_u32 s69, s69, 4\n\t" \
    "s_bitcmp1_b64 s[44:45], s69\n\t" \
    "s_cselect_b32 s69, 8192, 0\n\t" \
    "s_add_u32 s69, s69, s70\n\t" \
    "s_add_u32 s69, s69, 123731968\n\t" \
    "s_add_u32 s38, s36, s69\n\t" \
    "s_addc_u32 s39, s37, 0\n\t" \
    "s_and_b32 s69, s69, 0xffe00000\n\t" \
    "s_add_u32 s69, s69, 1823744\n\t" \
    "v_mov_b32 v17, s69\n\t" \
    "global_load_dword v17, v17, s[36:37]\n\t" \
    "s_barrier\n\t" \
    "ds_read_b32 v160, v11 offset:20480\n\t" \
    "s_waitcnt lgkmcnt(0)\n\t" \
    "v_readfirstlane_b32 s70, v160\n\t" \
    "s_branch vv_loop11_%=\n\t" \
    "vv_Gbad53_%=:\n\t" \
    "v_max_u32 v140, v36, v40\n\t" \
    "v_cmp_eq_u32 vcc, -1, v140\n\t" \
    "s_cbranch_vccz vv_Gc54_%=\n\t" \
    "s_waitcnt vmcnt(0) lgkmcnt(0)\n\t" \
    "s_nop 7\n\t" \
    "v_mov_b32 v72, 0\n\t" \
    "v_mov_b32 v73, 0\n\t" \
    "v_mov_b32 v74, 0\n\t" \
    "v_mov_b32 v75, 0\n\t" \
    "v_mov_b32 v76, 0\n\t" \
    "v_mov_b32 v77, 0\n\t" \
    "v_mov_b32 v78, 0\n\t" \
    "v_mov_b32 v79, 0\n\t" \
    "s_cmp_eq_u32 s68, 0\n\t" \
    "s_cbranch_scc1 vv_F60_%=\n\t" \
    "s_sub_u32 s68, s68, 1\n\t" \
    "s_branch vv_G52_%=\n\t" \
    "vv_F60_%=:\n\t" \
    "v_mov_b32 v140, 5767424\n\t" \
    "v_mov_b32 v141, 1\n\t" \
    "global_store_dword v140, v141, s[36:37] sc1\n\t" \
    "s_waitcnt vmcnt(0)\n\t" \
    "s_branch vv_Gc54_%=\n\t" \
    "vv_Gbad258_%=:\n\t" \
    "global_load_dwordx4 v[44:47], v2, s[46:47] sc1\n\t" \
    "s_waitcnt vmcnt(0)\n\t" \
    "v_cmp_eq_u32 vcc, -1, v44\n\t" \
    "s_cbranch_vccz vv_Gc259_%=\n\t" \
    "s_cmp_eq_u32 s68, 0\n\t" \
    "s_cbranch_scc1 vv_F61_%=\n\t" \
    "s_sub_u32 s68, s68, 1\n\t" \
    "s_sleep 1\n\t" \
    "s_branch vv_Gbad258_%=\n\t" \
    "vv_F61_%=:\n\t" \
    "v_mov_b32 v140, 5767424\n\t" \
    "v_mov_b32 v141, 2\n\t" \
    "global_store_dword v140, v141, s[36:37] sc1\n\t" \
    "s_waitcnt vmcnt(0)\n\t" \
    "s_branch vv_Gc259_%=\n\t" \
    "vv_Gslow55_%=:\n\t" \
    "v_max_u32 v140, v36, v37\n\t" \
    "v_max3_u32 v140, v140, v38, v39\n\t" \
    "v_max3_u32 v140, v140, v40, v41\n\t" \
    "v_max3_u32 v140, v140, v42, v43\n\t" \
    "v_max3_u32 v140, v140, v44, v45\n\t" \
    "v_max3_u32 v140, v140, v46, v47\n\t" \
    "v_max3_u32 v140, v140, v48, v49\n\t" \
    "v_max3_u32 v140, v140, v50, v51\n\t" \
    "v_max3_u32 v140, v140, v52, v53\n\t" \
    "v_max3_u32 v140, v140, v54, v55\n\t" \
    "v_max3_u32 v140, v140, v56, v57\n\t" \
    "v_max3_u32 v140, v140, v58, v59\n\t" \
    "v_max3_u32 v140, v140, v60, v61\n\t" \
    "v_max3_u32 v140, v140, v62, v63\n\t" \
    "v_max3_u32 v140, v140, v64, v65\n\t" \
    "v_max3_u32 v140, v140, v66, v67\n\t" \
    "v_max3_u32 v140, v140, v68, v69\n\t" \
    "v_max3_u32 v140, v140, v70, v71\n\t" \
    "v_cmp_eq_u32 vcc, -1, v140\n\t" \
    "s_cbranch_vccz vv_Gback56_%=\n\t" \
    "v_sub_u32 v140, v1, v3\n\t" \
    "s_nop 0\n\t" \
    "v_readfirstlane_b32 s69, v140\n\t" \
    "s_nop 7\n\t" \
    "s_add_u32 s30, s46, s69\n\t" \
    "s_addc_u32 s31, s47, 0\n\t" \
    "vv_Gag62_%=:\n\t" \
    "global_load_dwordx4 v[36:39], v1, s[46:47] sc1\n\t" \
    "global_load_dwordx4 v[40:43], v23, s[46:47] sc1\n\t" \
    "global_load_dwordx4 v[44:47], v2, s[46:47] sc1\n\t" \
    "global_load_dwordx4 v[48:51], v4, s[30:31] sc1\n\t" \
    "global_load_dwordx4 v[52:55], v5, s[30:31] sc1\n\t" \
    "global_load_dwordx4 v[56:59], v6, s[30:31] sc1\n\t" \
    "global_load_dwordx4 v[60:63], v7, s[30:31] sc1\n\t" \
    "global_load_dwordx4 v[64:67], v8, s[30:31] sc1\n\t" \
    "global_load_dwordx4 v[68:71], v9, s[30:31] sc1\n\t" \
    "s_waitcnt vmcnt(0) lgkmcnt(0)\n\t" \
    "v_max_u32 v140, v36, v37\n\t" \
    "v_max3_u32 v140, v140, v38, v39\n\t" \
    "v_max3_u32 v140, v140, v40, v41\n\t" \
    "v_max3_u32 v140, v140, v42, v43\n\t" \
    "v_max3_u32 v140, v140, v44, v45\n\t" \
    "v_max3_u32 v140, v140, v46, v47\n\t" \
    "v_max3_u32 v140, v140, v48, v49\n\t" \
    "v_max3_u32 v140, v140, v50, v51\n\t" \
    "v_max3_u32 v140, v140, v52, v53\n\t" \
    "v_max3_u32 v140, v140, v54, v55\n\t" \
    "v_max3_u32 v140, v140, v56, v57\n\t" \
    "v_max3_u32 v140, v140, v58, v59\n\t" \
    "v_max3_u32 v140, v140, v60, v61\n\t" \
    "v_max3_u32 v140, v140, v62, v63\n\t" \
    "v_max3_u32 v140, v140, v64, v65\n\t" \
    "v_max3_u32 v140, v140, v66, v67\n\t" \
    "v_max3_u32 v140, v140, v68, v69\n\t" \
    "v_max3_u32 v140, v140, v70, v71\n\t" \
    "v_cmp_eq_u32 vcc, -1, v140\n\t" \
    "s_cbranch_vccz vv_Gredo63_%=\n\t" \
    "s_cmp_eq_u32 s68, 0\n\t" \
    "s_cbranch_scc1 vv_F64_%=\n\t" \
    "s_sub_u32 s68, s68, 1\n\t" \
    "s_sleep 1\n\t" \
    "s_branch vv_Gag62_%=\n\t" \
    "vv_F64_%=:\n\t" \
    "v_mov_b32 v140, 5767424\n\t" \
    "v_mov_b32 v141, 2\n\t" \
    "global_store_dword v140, v141, s[36:37] sc1\n\t" \
    "s_waitcnt vmcnt(0)\n\t" \
    "s_branch vv_Gredo63_%=\n\t" \
    "vv_Gredo63_%=:\n\t" \
    "v_mov_b32 v72, 0\n\t" \
    "v_mov_b32 v73, 0\n\t" \
    "v_mov_b32 v74, 0\n\t" \
    "v_mov_b32 v75, 0\n\t" \
    "v_mov_b32 v76, 0\n\t" \
    "v_mov_b32 v77, 0\n\t" \
    "v_mov_b32 v78, 0\n\t" \
    "v_mov_b32 v79, 0\n\t" \
    "v_smfmac_f32_16x16x64_bf16 v[72:75], v[36:39], a[32:39], v10\n\t" \
    "v_smfmac_f32_16x16x64_bf16 v[72:75], v[40:43], a[40:47], v10\n\t" \
    "v_smfmac_f32_16x16x64_bf16 v[72:75], v[44:47], a[64:71], v10\n\t" \
    "v_smfmac_f32_16x16x64_bf16 v[72:75], v[48:51], a[48:55], v10\n\t" \
    "v_smfmac_f32_16x16x64_bf16 v[72:75], v[52:55], a[56:63], v10\n\t" \
    "v_smfmac_f32_16x16x64_bf16 v[72:75], v[56:59], a[0:7], v10\n\t" \
    "v_smfmac_f32_16x16x64_bf16 v[72:75], v[60:63], a[8:15], v10\n\t" \
    "v_smfmac_f32_16x16x64_bf16 v[72:75], v[64:67], a[16:23], v10\n\t" \
    "v_smfmac_f32_16x16x64_bf16 v[72:75], v[68:71], a[24:31], v10\n\t" \
    "v_smfmac_f32_16x16x64_bf16 v[76:79], v[36:39], a[104:111], v10\n\t" \
    "v_smfmac_f32_16x16x64_bf16 v[76:79], v[40:43], a[112:119], v10\n\t" \
    "v_smfmac_f32_16x16x64_bf16 v[76:79], v[44:47], a[136:143], v10\n\t" \
    "v_smfmac_f32_16x16x64_bf16 v[76:79], v[48:51], a[120:127], v10\n\t" \
    "v_smfmac_f32_16x16x64_bf16 v[76:79], v[52:55], a[128:135], v10\n\t" \
    "v_smfmac_f32_16x16x64_bf16 v[76:79], v[56:59], a[72:79], v10\n\t" \
    "v_smfmac_f32_16x16x64_bf16 v[76:79], v[60:63], a[80:87], v10\n\t" \
    "v_smfmac_f32_16x16x64_bf16 v[76:79], v[64:67], a[88:95], v10\n\t" \
    "v_smfmac_f32_16x16x64_bf16 v[76:79], v[68:71], a[96:103], v10\n\t" \
    "v_add_f32 v72, v72, v74\n\t" \
    "v_add_f32 v73, v73, v75\n\t" \
    "v_cvt_pk_bf16_f32 v80, v72, v73\n\t" \
    "ds_write_b32 v12, v80 offset:0\n\t" \
    "s_nop 7\n\t" \
    "s_branch vv_Gdone57_%=\n\t" \
    "vv_end48_%=:\n\t" \
    "s_endpgm\n\t" \
    ".p2align 6\n\t" \
    "s_nop 0\n\t" \
    "s_nop 0\n\t" \
    "s_nop 0\n\t" \
    "s_nop 0\n\t" \
    "s_nop 0\n\t" \
    "s_nop 0\n\t" \
    "vv_loop12_%=:\n\t" \
    "s_getpc_b64 s[30:31]\n\t" \
    "vv_Wp67_%=:\n\t" \
    "s_add_u32 s30, s30, vv_We68_%=-vv_Wp67_%=\n\t" \
    "s_addc_u32 s31, s31, 0\n\t" \
    "s_sub_u32 s70, s70, 96\n\t" \
    "vv_W66_%=:\n\t" \
    "s_memtime s[56:57]\n\t" \
    "s_waitcnt lgkmcnt(0)\n\t" \
    "s_sub_u32 s69, s70, s56\n\t" \
    "s_cmp_gt_i32 s69, 80\n\t" \
    "s_cbranch_scc1 vv_W66_%=\n\t" \
    "s_max_i32 s69, s69, 0\n\t" \
    "s_and_b32 s69, s69, -4\n\t" \
    "s_sub_u32 s30, s30, s69\n\t" \
    "s_subb_u32 s31, s31, 0\n\t" \
    "s_setpc_b64 s[30:31]\n\t" \
    "s_nop 0\n\t" \
    "s_nop 0\n\t" \
    "s_nop 0\n\t" \
    "s_nop 0\n\t" \
    "s_nop 0\n\t" \
    "s_nop 0\n\t" \
    "s_nop 0\n\t" \
    "s_nop 0\n\t" \
    "s_nop 0\n\t" \
    "s_nop 0\n\t" \
    "s_nop 0\n\t" \
    "s_nop 0\n\t" \
    "s_nop 0\n\t" \
    "s_nop 0\n\t" \
    "s_nop 0\n\t" \
    "s_nop 0\n\t" \
    "s_nop 0\n\t" \
    "s_nop 0\n\t" \
    "s_nop 0\n\t" \
    "s_nop 0\n\t" \
    "vv_We68_%=:\n\t" \
    "vv_G69_%=:\n\t" \
    "global_load_dwordx4 v[36:39], v1, s[46:47] sc1\n\t" \
    "global_load_dwordx4 v[40:43], v23, s[46:47] sc1\n\t" \
    "s_nop 7\n\t" \
    "global_load_dwordx4 v[44:47], v2, s[46:47] sc1\n\t" \
    "s_waitcnt vmcnt(2)\n\t" \
    "v_smfmac_f32_16x16x64_bf16 v[72:75], v[36:39], a[48:55], v10\n\t" \
    "ds_write_b128 v3, v[36:39]\n\t" \
    "v_smfmac_f32_16x16x64_bf16 v[76:79], v[36:39], a[120:127], v10\n\t" \
    "s_waitcnt vmcnt(1)\n\t" \
    "v_cmp_u_f32 vcc, v36, v40\n\t" \
    "ds_write_b128 v3, v[40:43] offset:1024\n\t" \
    "v_smfmac_f32_16x16x64_bf16 v[72:75], v[40:43], a[56:63], v10\n\t" \
    "s_cbranch_vccnz vv_Gbad70_%=\n\t" \
    "vv_Gc71_%=:\n\t" \
    "s_waitcnt lgkmcnt(0)\n\t" \
    "s_barrier\n\t" \
    "ds_read_b128 v[48:51], v4\n\t" \
    "ds_read_b128 v[52:55], v5\n\t" \
    "v_smfmac_f32_16x16x64_bf16 v[76:79], v[40:43], a[128:135], v10\n\t" \
    "s_waitcnt vmcnt(0)\n\t" \
    "v_cmp_eq_u32 vcc, -1, v44\n\t" \
    "ds_read_b128 v[56:59], v6\n\t" \
    "ds_read_b128 v[60:63], v7\n\t" \
    "ds_read_b128 v[64:67], v8\n\t" \
    "ds_read_b128 v[68:71], v9\n\t" \
    "s_cbranch_vccnz vv_Gbad275_%=\n\t" \
    "vv_Gc276_%=:\n\t" \
    "v_smfmac_f32_16x16x64_bf16 v[72:75], v[44:47], a[64:71], v10\n\t" \
    "v_smfmac_f32_16x16x64_bf16 v[76:79], v[44:47], a[136:143], v10\n\t" \
    "s_waitcnt lgkmcnt(5)\n\t" \
    "v_smfmac_f32_16x16x64_bf16 v[72:75], v[48:51], a[0:7], v10\n\t" \
    "s_waitcnt lgkmcnt(4)\n\t" \
    "v_smfmac_f32_16x16x64_bf16 v[72:75], v[52:55], a[8:15], v10\n\t" \
    "s_waitcnt lgkmcnt(3)\n\t" \
    "v_smfmac_f32_16x16x64_bf16 v[72:75], v[56:59], a[16:23], v10\n\t" \
    "s_waitcnt lgkmcnt(2)\n\t" \
    "v_smfmac_f32_16x16x64_bf16 v[72:75], v[60:63], a[24:31], v10\n\t" \
    "s_waitcnt lgkmcnt(1)\n\t" \
    "v_smfmac_f32_16x16x64_bf16 v[72:75], v[64:67], a[32:39], v10\n\t" \
    "s_waitcnt lgkmcnt(0)\n\t" \
    "v_smfmac_f32_16x16x64_bf16 v[72:75], v[68:71], a[40:47], v10\n\t" \
    "v_smfmac_f32_16x16x64_bf16 v[76:79], v[48:51], a[72:79], v10\n\t" \
    "s_nop 1\n\t" \
    "s_nop 0\n\t" \
    "v_smfmac_f32_16x16x64_bf16 v[76:79], v[52:55], a[80:87], v10\n\t" \
    "s_nop 1\n\t" \
    "s_nop 0\n\t" \
    "v_smfmac_f32_16x16x64_bf16 v[76:79], v[56:59], a[88:95], v10\n\t" \
    "v_add_f32 v72, v72, v74\n\t" \
    "v_add_f32 v73, v73, v75\n\t" \
    "v_smfmac_f32_16x16x64_bf16 v[76:79], v[60:63], a[96:103], v10\n\t" \
    "v_cvt_pk_bf16_f32 v80, v72, v73\n\t" \
    "v_smfmac_f32_16x16x64_bf16 v[76:79], v[64:67], a[104:111], v10\n\t" \
    "v_cmp_u_f32 vcc, v72, v73\n\t" \
    "ds_write_b32 v12, v80 offset:0\n\t" \
    "v_smfmac_f32_16x16x64_bf16 v[76:79], v[68:71], a[112:119], v10\n\t" \
    "s_cbranch_vccnz vv_Gslow72_%=\n\t" \
    "vv_Gback73_%=:\n\t" \
    "vv_Gdone74_%=:\n\t" \
    "s_nop 6\n\t" \
    "v_add_f32 v76, v76, v78\n\t" \
    "v_add_f32 v77, v77, v79\n\t" \
    "v_cvt_pk_bf16_f32 v81, v76, v77\n\t" \
    "ds_write_b32 v12, v81 offset:256\n\t" \
    "s_waitcnt lgkmcnt(0)\n\t" \
    "s_barrier\n\t" \
    "ds_read_b64_tr_b16 v[84:85], v13 offset:0\n\t" \
    "v_smfmac_f32_16x16x64_bf16 v[100:103], v[36:39], a[0:7], v10\n\t" \
    "ds_read_b64_tr_b16 v[86:87], v13 offset:64\n\t" \
    "ds_read_b64_tr_b16 v[92:93], v13 offset:1152\n\t" \
    "v_smfmac_f32_16x16x64_bf16 v[100:103], v[36:39], a[0:7], v10\n\t" \
    "ds_read_b64_tr_b16 v[94:95], v13 offset:1216\n\t" \
    "ds_read_b64_tr_b16 v[88:89], v25 offset:0\n\t" \
    "v_smfmac_f32_16x16x64_bf16 v[100:103], v[36:39], a[0:7], v10\n\t" \
    "ds_read_b64_tr_b16 v[90:91], v25 offset:64\n\t" \
    "s_waitcnt lgkmcnt(4)\n\t" \
    "v_smfmac_f32_16x16x64_bf16 v[124:127], v[84:87], v[240:247], v19\n\t" \
    "ds_read_b64_tr_b16 v[96:97], v25 offset:1152\n\t" \
    "ds_read_b64_tr_b16 v[98:99], v25 offset:1216\n\t" \
    "s_waitcnt lgkmcnt(4)\n\t" \
    "v_smfmac_f32_16x16x64_bf16 v[124:127], v[92:95], v[248:255], v19\n\t" \
    "s_nop 2\n\t" \
    "v_smfmac_f32_16x16x64_bf16 v[108:111], v[84:87], a[144:151], v19\n\t" \
    "s_nop 2\n\t" \
    "v_smfmac_f32_16x16x64_bf16 v[108:111], v[92:95], a[160:167], v19\n\t" \
    "s_nop 1\n\t" \
    "v_permlane32_swap_b32 v124, v126\n\t" \
    "v_smfmac_f32_16x16x64_bf16 v[112:115], v[84:87], a[176:183], v19\n\t" \
    "s_nop 1\n\t" \
    "v_permlane32_swap_b32 v125, v127\n\t" \
    "v_smfmac_f32_16x16x64_bf16 v[112:115], v[92:95], a[192:199], v19\n\t" \
    "s_nop 0\n\t" \
    "v_add_f32 v124, v124, v126\n\t" \
    "s_waitcnt lgkmcnt(2)\n\t" \
    "v_smfmac_f32_16x16x64_bf16 v[108:111], v[88:91], a[152:159], v19\n\t" \
    "s_nop 1\n\t" \
    "v_add_f32 v125, v125, v127\n\t" \
    "v_smfmac_f32_16x16x64_bf16 v[112:115], v[88:91], a[184:191], v19\n\t" \
    "s_nop 0\n\t" \
    "v_cvt_pk_bf16_f32 v136, v124, v125\n\t" \
    "s_waitcnt lgkmcnt(0)\n\t" \
    "v_smfmac_f32_16x16x64_bf16 v[108:111], v[96:99], a[168:175], v19\n\t" \
    "s_nop 1\n\t" \
    "v_cvt_pk_bf16_f32 v137, v124, v125\n\t" \
    "v_smfmac_f32_16x16x64_bf16 v[112:115], v[96:99], a[200:207], v19\n\t" \
    "s_nop 1\n\t" \
    "v_permlane32_swap_b32 v136, v137\n\t" \
    "v_smfmac_f32_16x16x64_bf16 v[116:119], v[84:87], a[208:215], v19\n\t" \
    "s_mov_b32 exec_hi, 0\n\t" \
    "global_store_dwordx2 v16, v[136:137], s[38:39] sc1\n\t" \
    "s_mov_b32 exec_hi, -1\n\t" \
    "v_cvt_pk_bf16_f32 v128, v108, v109\n\t" \
    "v_smfmac_f32_16x16x64_bf16 v[116:119], v[88:91], a[216:223], v19\n\t" \
    "s_nop 1\n\t" \
    "v_cvt_pk_bf16_f32 v129, v110, v111\n\t" \
    "v_smfmac_f32_16x16x64_bf16 v[116:119], v[92:95], a[224:231], v19\n\t" \
    "s_nop 1\n\t" \
    "v_cvt_pk_bf16_f32 v130, v112, v113\n\t" \
    "v_smfmac_f32_16x16x64_bf16 v[116:119], v[96:99], a[232:239], v19\n\t" \
    "s_nop 1\n\t" \
    "v_cvt_pk_bf16_f32 v131, v114, v115\n\t" \
    "v_smfmac_f32_16x16x64_bf16 v[120:123], v[84:87], a[240:247], v19\n\t" \
    "s_nop 1\n\t" \
    "v_permlane16_swap_b32 v128, v130\n\t" \
    "v_smfmac_f32_16x16x64_bf16 v[120:123], v[88:91], a[248:255], v19\n\t" \
    "s_nop 1\n\t" \
    "v_permlane16_swap_b32 v129, v131\n\t" \
    "v_smfmac_f32_16x16x64_bf16 v[120:123], v[92:95], v[224:231], v19\n\t" \
    "s_nop 0\n\t" \
    "global_store_dwordx4 v14, v[128:131], s[38:39] sc1\n\t" \
    "v_cvt_pk_bf16_f32 v132, v116, v117\n\t" \
    "v_smfmac_f32_16x16x64_bf16 v[120:123], v[96:99], v[232:239], v19\n\t" \
    "s_nop 1\n\t" \
    "v_cvt_pk_bf16_f32 v133, v118, v119\n\t" \
    "s_nop 4\n\t" \
    "v_cvt_pk_bf16_f32 v134, v120, v121\n\t" \
    "v_cvt_pk_bf16_f32 v135, v122, v123\n\t" \
    "s_nop 0\n\t" \
    "v_permlane16_swap_b32 v132, v134\n\t" \
    "v_permlane16_swap_b32 v133, v135\n\t" \
    "global_store_dwordx4 v15, v[132:135], s[38:39] sc1\n\t" \
    "global_load_dword v140, v20, s[52:53] sc1\n\t" \
    "s_cmp_eq_u32 s65, 63\n\t" \
    "s_cbranch_scc1 vv_end65_%=\n\t" \
    "s_cmp_lg_u32 s65, 0\n\t" \
    "s_cbranch_scc1 vv_ng77_%=\n\t" \
    "v_mov_b32 v160, 5838848\n\t" \
    "v_mov_b32 v161, 1\n\t" \
    "s_mov_b64 exec, 1\n\t" \
    "global_atomic_add v162, v160, v161, s[36:37] sc0 sc1\n\t" \
    "s_mov_b64 exec, -1\n\t" \
    "v_mbcnt_lo_u32_b32 v160, -1, 0\n\t" \
    "v_and_b32 v161, 3, v160\n\t" \
    "v_lshlrev_b32 v161, 16, v161\n\t" \
    "v_bfe_u32 v163, v160, 2, 2\n\t" \
    "v_lshl_add_u32 v161, v163, 11, v161\n\t" \
    "v_bfe_u32 v163, v160, 4, 1\n\t" \
    "v_lshl_add_u32 v161, v163, 14, v161\n\t" \
    "v_add_u32 v161, s63, v161\n\t" \
    "s_waitcnt vmcnt(0)\n\t" \
    "v_readfirstlane_b32 s69, v162\n\t" \
    "s_nop 1\n\t" \
    "s_cmp_lg_u32 s69, 126\n\t" \
    "s_cbranch_scc1 vv_go78_%=\n\t" \
    "s_memrealtime s[56:57]\n\t" \
    "s_waitcnt lgkmcnt(0)\n\t" \
    "s_lshl_b32 s69, s56, 4\n\t" \
    "s_add_u32 s69, s69, 2400\n\t" \
    "v_mov_b32 v160, s69\n\t" \
    "global_store_dword v161, v160, s[36:37] sc1\n\t" \
    "vv_go78_%=:\n\t" \
    "vv_ng77_%=:\n\t" \
    "s_waitcnt lgkmcnt(0)\n\t" \
    "s_mov_b32 s70, 116\n\t" \
    "vv_E82_%=:\n\t" \
    "s_memtime s[56:57]\n\t" \
    "s_memrealtime s[2:3]\n\t" \
    "s_nop 0\n\t" \
    "s_memrealtime s[4:5]\n\t" \
    "s_nop 0\n\t" \
    "s_memrealtime s[6:7]\n\t" \
    "s_nop 0\n\t" \
    "s_memrealtime s[8:9]\n\t" \
    "s_nop 0\n\t" \
    "s_memrealtime s[10:11]\n\t" \
    "s_nop 0\n\t" \
    "s_memrealtime s[12:13]\n\t" \
    "s_nop 0\n\t" \
    "s_memrealtime s[14:15]\n\t" \
    "s_nop 0\n\t" \
    "s_memrealtime s[16:17]\n\t" \
    "s_nop 0\n\t" \
    "s_memrealtime s[18:19]\n\t" \
    "s_nop 0\n\t" \
    "s_memrealtime s[20:21]\n\t" \
    "s_nop 0\n\t" \
    "s_memrealtime s[22:23]\n\t" \
    "s_nop 0\n\t" \
    "s_memrealtime s[24:25]\n\t" \
    "s_nop 0\n\t" \
    "s_memrealtime s[26:27]\n\t" \
    "s_nop 0\n\t" \
    "s_memrealtime s[28:29]\n\t" \
    "s_memtime s[54:55]\n\t" \
    "s_add_u32 s70, s70, 8\n\t" \
    "s_waitcnt lgkmcnt(0)\n\t" \
    "s_sub_u32 s69, s54, s56\n\t" \
    "s_cmp_gt_u32 s69, s70\n\t" \
    "s_cbranch_scc1 vv_E82_%=\n\t" \
    "s_add_u32 s2, s2, s4\n\t" \
    "s_add_u32 s6, s6, s8\n\t" \
    "s_add_u32 s10, s10, s12\n\t" \
    "s_add_u32 s14, s14, s16\n\t" \
    "s_add_u32 s18, s18, s20\n\t" \
    "s_add_u32 s22, s22, s24\n\t" \
    "s_add_u32 s2, s2, s6\n\t" \
    "s_add_u32 s10, s10, s14\n\t" \
    "s_add_u32 s18, s18, s22\n\t" \
    "s_add_u32 s2, s2, s26\n\t" \
    "s_add_u32 s10, s10, s18\n\t" \
    "s_add_u32 s2, s2, s10\n\t" \
    "s_mul_i32 s70, s28, 13\n\t" \
    "s_sub_u32 s70, s70, s2\n\t" \
    "s_min_u32 s70, s70, 52\n\t" \
    "s_mul_i32 s69, s69, s70\n\t" \
    "s_mul_hi_u32 s69, s69, 76695844\n\t" \
    "s_mov_b32 s75, s73\n\t" \
    "s_mov_b32 s76, s74\n\t" \
    "s_add_u32 s73, s56, s69\n\t" \
    "s_lshl_b32 s74, s28, 4\n\t" \
    "s_sub_u32 s69, s73, s75\n\t" \
    "s_sub_u32 s70, s74, s76\n\t" \
    "s_sub_u32 s71, s70, 1600\n\t" \
    "s_cmp_lt_u32 s71, 400000\n\t" \
    "s_cbranch_scc0 vv_K83_%=\n\t" \
    "v_cvt_f32_u32 v160, s69\n\t" \
    "v_cvt_f32_u32 v161, s70\n\t" \
    "v_rcp_f32 v162, v160\n\t" \
    "v_rcp_f32 v163, v161\n\t" \
    "v_mul_f32 v162, v162, v161\n\t" \
    "v_mul_f32 v163, v163, v160\n\t" \
    "v_mul_f32 v162, 0x4f800000, v162\n\t" \
    "v_mul_f32 v163, 0x4f000000, v163\n\t" \
    "v_cvt_u32_f32 v162, v162\n\t" \
    "v_cvt_u32_f32 v163, v163\n\t" \
    "v_readfirstlane_b32 s69, v162\n\t" \
    "v_readfirstlane_b32 s70, v163\n\t" \
    "s_nop 1\n\t" \
    "s_min_u32 s71, s65, 2\n\t" \
    "s_sub_u32 s69, s69, s78\n\t" \
    "s_ashr_i32 s69, s69, s71\n\t" \
    "s_add_u32 s78, s78, s69\n\t" \
    "s_sub_u32 s70, s70, s77\n\t" \
    "s_ashr_i32 s70, s70, s71\n\t" \
    "s_add_u32 s77, s77, s70\n\t" \
    "vv_K83_%=:\n\t" \
    "s_add_u32 s65, s65, 1\n\t" \
    "v_mov_b32 v72, 0\n\t" \
    "v_mov_b32 v73, 0\n\t" \
    "v_mov_b32 v74, 0\n\t" \
    "v_mov_b32 v75, 0\n\t" \
    "v_mov_b32 v76, 0\n\t" \
    "v_mov_b32 v77, 0\n\t" \
    "v_mov_b32 v78, 0\n\t" \
    "v_mov_b32 v79, 0\n\t" \
    "v_mov_b32 v108, 0\n\t" \
    "v_mov_b32 v109, 0\n\t" \
    "v_mov_b32 v110, 0\n\t" \
    "v_mov_b32 v111, 0\n\t" \
    "v_mov_b32 v112, 0\n\t" \
    "v_mov_b32 v113, 0\n\t" \
    "v_mov_b32 v114, 0\n\t" \
    "v_mov_b32 v115, 0\n\t" \
    "v_mov_b32 v116, 0\n\t" \
    "v_mov_b32 v117, 0\n\t" \
    "v_mov_b32 v118, 0\n\t" \
    "v_mov_b32 v119, 0\n\t" \
    "v_mov_b32 v120, 0\n\t" \
    "v_mov_b32 v121, 0\n\t" \
    "v_mov_b32 v122, 0\n\t" \
    "v_mov_b32 v123, 0\n\t" \
    "v_mov_b32 v124, 0\n\t" \
    "v_mov_b32 v125, 0\n\t" \
    "v_mov_b32 v126, 0\n\t" \
    "v_mov_b32 v127, 0\n\t" \
    "s_add_u32 s69, s65, 0\n\t" \
    "s_and_b32 s69, s69, 7\n\t" \
    "s_lshl_b32 s69, s69, 18\n\t" \
    "s_add_u32 s69, s69, s62\n\t" \
    "s_add_u32 s46, s36, s69\n\t" \
    "s_addc_u32 s47, s37, 0\n\t" \
    "s_add_u32 s70, s65, 0\n\t" \
    "s_mul_i32 s69, s70, 171\n\t" \
    "s_lshr_b32 s69, s69, 9\n\t" \
    "s_mul_i32 s70, s70, 589824\n\t" \
    "s_mul_i32 s71, s69, 327680\n\t" \
    "s_add_u32 s70, s70, s71\n\t" \
    "s_lshl1_add_u32 s69, s69, 4\n\t" \
    "s_bitcmp1_b64 s[44:45], s69\n\t" \
    "s_cselect_b32 s69, 8192, 0\n\t" \
    "s_add_u32 s69, s69, s70\n\t" \
    "s_add_u32 s69, s69, 123731968\n\t" \
    "s_add_u32 s38, s36, s69\n\t" \
    "s_addc_u32 s39, s37, 0\n\t" \
    "s_and_b32 s69, s69, 0xffe00000\n\t" \
    "s_add_u32 s69, s69, 1823744\n\t" \
    "v_mov_b32 v17, s69\n\t" \
    "global_load_dword v17, v17, s[36:37]\n\t" \
    "s_waitcnt vmcnt(0)\n\t" \
    "v_cmp_eq_u32 vcc, -1, v140\n\t" \
    "s_cbranch_vccz vv_Hok80_%=\n\t" \
    "vv_H79_%=:\n\t" \
    "global_load_dword v140, v20, s[52:53] sc1\n\t" \
    "s_waitcnt vmcnt(0)\n\t" \
    "v_cmp_eq_u32 vcc, -1, v140\n\t" \
    "s_cbranch_vccz vv_Hc81_%=\n\t" \
    "s_cmp_eq_u32 s68, 0\n\t" \
    "s_cbranch_scc1 vv_F84_%=\n\t" \
    "s_sub_u32 s68, s68, 1\n\t" \
    "s_sleep 2\n\t" \
    "s_branch vv_H79_%=\n\t" \
    "vv_F84_%=:\n\t" \
    "v_mov_b32 v141, 5767424\n\t" \
    "v_mov_b32 v142, 6\n\t" \
    "global_store_dword v141, v142, s[36:37] sc1\n\t" \
    "s_waitcnt vmcnt(0)\n\t" \
    "s_branch vv_Hc81_%=\n\t" \
    "vv_Hc81_%=:\n\t" \
    "s_memtime s[56:57]\n\t" \
    "s_waitcnt lgkmcnt(0)\n\t" \
    "s_sub_u32 s69, s56, s73\n\t" \
    "s_cmp_lt_u32 s69, 3000\n\t" \
    "s_cbranch_scc1 vv_St85_%=\n\t" \
    "s_waitcnt lgkmcnt(0)\n\t" \
    "s_mov_b32 s70, 116\n\t" \
    "vv_E86_%=:\n\t" \
    "s_memtime s[56:57]\n\t" \
    "s_memrealtime s[2:3]\n\t" \
    "s_nop 0\n\t" \
    "s_memrealtime s[4:5]\n\t" \
    "s_nop 0\n\t" \
    "s_memrealtime s[6:7]\n\t" \
    "s_nop 0\n\t" \
    "s_memrealtime s[8:9]\n\t" \
    "s_nop 0\n\t" \
    "s_memrealtime s[10:11]\n\t" \
    "s_nop 0\n\t" \
    "s_memrealtime s[12:13]\n\t" \
    "s_nop 0\n\t" \
    "s_memrealtime s[14:15]\n\t" \
    "s_nop 0\n\t" \
    "s_memrealtime s[16:17]\n\t" \
    "s_nop 0\n\t" \
    "s_memrealtime s[18:19]\n\t" \
    "s_nop 0\n\t" \
    "s_memrealtime s[20:21]\n\t" \
    "s_nop 0\n\t" \
    "s_memrealtime s[22:23]\n\t" \
    "s_nop 0\n\t" \
    "s_memrealtime s[24:25]\n\t" \
    "s_nop 0\n\t" \
    "s_memrealtime s[26:27]\n\t" \
    "s_nop 0\n\t" \
    "s_memrealtime s[28:29]\n\t" \
    "s_memtime s[54:55]\n\t" \
    "s_add_u32 s70, s70, 8\n\t" \
    "s_waitcnt lgkmcnt(0)\n\t" \
    "s_sub_u32 s69, s54, s56\n\t" \
    "s_cmp_gt_u32 s69, s70\n\t" \
    "s_cbranch_scc1 vv_E86_%=\n\t" \
    "s_add_u32 s2, s2, s4\n\t" \
    "s_add_u32 s6, s6, s8\n\t" \
    "s_add_u32 s10, s10, s12\n\t" \
    "s_add_u32 s14, s14, s16\n\t" \
    "s_add_u32 s18, s18, s20\n\t" \
    "s_add_u32 s22, s22, s24\n\t" \
    "s_add_u32 s2, s2, s6\n\t" \
    "s_add_u32 s10, s10, s14\n\t" \
    "s_add_u32 s18, s18, s22\n\t" \
    "s_add_u32 s2, s2, s26\n\t" \
    "s_add_u32 s10, s10, s18\n\t" \
    "s_add_u32 s2, s2, s10\n\t" \
    "s_mul_i32 s70, s28, 13\n\t" \
    "s_sub_u32 s70, s70, s2\n\t" \
    "s_min_u32 s70, s70, 52\n\t" \
    "s_mul_i32 s69, s69, s70\n\t" \
    "s_mul_hi_u32 s69, s69, 76695844\n\t" \
    "s_mov_b32 s75, s73\n\t" \
    "s_mov_b32 s76, s74\n\t" \
    "s_add_u32 s73, s56, s69\n\t" \
    "s_lshl_b32 s74, s28, 4\n\t" \
    "vv_St85_%=:\n\t" \
    "vv_Hok80_%=:\n\t" \
    "v_max_u32_dpp v140, v140, v140 row_ror:1 row_mask:0xf bank_mask:0xf\n\t" \
    "s_nop 1\n\t" \
    "v_max_u32_dpp v140, v140, v140 row_ror:2 row_mask:0xf bank_mask:0xf\n\t" \
    "s_nop 1\n\t" \
    "v_max_u32_dpp v140, v140, v140 row_ror:4 row_mask:0xf bank_mask:0xf\n\t" \
    "s_nop 1\n\t" \
    "v_max_u32_dpp v140, v140, v140 row_ror:8 row_mask:0xf bank_mask:0xf\n\t" \
    "s_nop 0\n\t" \
    "v_readlane_b32 s69, v140, 0\n\t" \
    "v_readlane_b32 s71, v140, 16\n\t" \
    "s_nop 1\n\t" \
    "s_max_u32 s81, s69, s71\n\t" \
    "s_mul_hi_u32 s69, s85, s78\n\t" \
    "s_add_u32 s79, s81, s69\n\t" \
    "s_add_u32 s79, s79, s87\n\t" \
    "s_cmp_ge_u32 s65, 37\n\t" \
    "s_cselect_b32 s69, 32, 0\n\t" \
    "s_sub_u32 s79, s79, s69\n\t" \
    "s_cmp_lt_u32 s65, 10\n\t" \
    "s_cselect_b32 s69, 64, 0\n\t" \
    "s_add_u32 s79, s79, s69\n\t" \
    "s_add_u32 s80, s79, s88\n\t" \
    "s_sub_u32 s70, s79, s74\n\t" \
    "s_max_i32 s70, s70, 0\n\t" \
    "s_min_i32 s70, s70, 16000\n\t" \
    "s_lshl_b32 s70, s70, 1\n\t" \
    "s_mul_hi_u32 s70, s70, s77\n\t" \
    "s_add_u32 s70, s70, s73\n\t" \
    "v_mov_b32 v160, s70\n\t" \
    "ds_write_b32 v11, v160 offset:20480\n\t" \
    "s_and_b32 s69, s65, 7\n\t" \
    "s_lshl_b32 s69, s69, 18\n\t" \
    "s_add_u32 s69, s69, s63\n\t" \
    "s_add_u32 s52, s36, s69\n\t" \
    "s_addc_u32 s53, s37, 0\n\t" \
    "s_waitcnt lgkmcnt(0)\n\t" \
    "s_barrier\n\t" \
    "s_branch vv_loop12_%=\n\t" \
    "vv_Gbad70_%=:\n\t" \
    "v_max_u32 v140, v36, v40\n\t" \
    "v_cmp_eq_u32 vcc, -1, v140\n\t" \
    "s_cbranch_vccz vv_Gc71_%=\n\t" \
    "s_waitcnt vmcnt(0) lgkmcnt(0)\n\t" \
    "s_nop 7\n\t" \
    "v_mov_b32 v72, 0\n\t" \
    "v_mov_b32 v73, 0\n\t" \
    "v_mov_b32 v74, 0\n\t" \
    "v_mov_b32 v75, 0\n\t" \
    "v_mov_b32 v76, 0\n\t" \
    "v_mov_b32 v77, 0\n\t" \
    "v_mov_b32 v78, 0\n\t" \
    "v_mov_b32 v79, 0\n\t" \
    "s_cmp_eq_u32 s68, 0\n\t" \
    "s_cbranch_scc1 vv_F87_%=\n\t" \
    "s_sub_u32 s68, s68, 1\n\t" \
    "s_branch vv_G69_%=\n\t" \
    "vv_F87_%=:\n\t" \
    "v_mov_b32 v140, 5767424\n\t" \
    "v_mov_b32 v141, 1\n\t" \
    "global_store_dword v140, v141, s[36:37] sc1\n\t" \
    "s_waitcnt vmcnt(0)\n\t" \
    "s_branch vv_Gc71_%=\n\t" \
    "vv_Gbad275_%=:\n\t" \
    "global_load_dwordx4 v[44:47], v2, s[46:47] sc1\n\t" \
    "s_waitcnt vmcnt(0)\n\t" \
    "v_cmp_eq_u32 vcc, -1, v44\n\t" \
    "s_cbranch_vccz vv_Gc276_%=\n\t" \
    "s_cmp_eq_u32 s68, 0\n\t" \
    "s_cbranch_scc1 vv_F88_%=\n\t" \
    "s_sub_u32 s68, s68, 1\n\t" \
    "s_sleep 1\n\t" \
    "s_branch vv_Gbad275_%=\n\t" \
    "vv_F88_%=:\n\t" \
    "v_mov_b32 v140, 5767424\n\t" \
    "v_mov_b32 v141, 2\n\t" \
    "global_store_dword v140, v141, s[36:37] sc1\n\t" \
    "s_waitcnt vmcnt(0)\n\t" \
    "s_branch vv_Gc276_%=\n\t" \
    "vv_Gslow72_%=:\n\t" \
    "v_max_u32 v140, v36, v37\n\t" \
    "v_max3_u32 v140, v140, v38, v39\n\t" \
    "v_max3_u32 v140, v140, v40, v41\n\t" \
    "v_max3_u32 v140, v140, v42, v43\n\t" \
    "v_max3_u32 v140, v140, v44, v45\n\t" \
    "v_max3_u32 v140, v140, v46, v47\n\t" \
    "v_max3_u32 v140, v140, v48, v49\n\t" \
    "v_max3_u32 v140, v140, v50, v51\n\t" \
    "v_max3_u32 v140, v140, v52, v53\n\t" \
    "v_max3_u32 v140, v140, v54, v55\n\t" \
    "v_max3_u32 v140, v140, v56, v57\n\t" \
    "v_max3_u32 v140, v140, v58, v59\n\t" \
    "v_max3_u32 v140, v140, v60, v61\n\t" \
    "v_max3_u32 v140, v140, v62, v63\n\t" \
    "v_max3_u32 v140, v140, v64, v65\n\t" \
    "v_max3_u32 v140, v140, v66, v67\n\t" \
    "v_max3_u32 v140, v140, v68, v69\n\t" \
    "v_max3_u32 v140, v140, v70, v71\n\t" \
    "v_cmp_eq_u32 vcc, -1, v140\n\t" \
    "s_cbranch_vccz vv_Gback73_%=\n\t" \
    "v_sub_u32 v140, v1, v3\n\t" \
    "s_nop 0\n\t" \
    "v_readfirstlane_b32 s69, v140\n\t" \
    "s_nop 7\n\t" \
    "s_add_u32 s30, s46, s69\n\t" \
    "s_addc_u32 s31, s47, 0\n\t" \
    "vv_Gag89_%=:\n\t" \
    "global_load_dwordx4 v[36:39], v1, s[46:47] sc1\n\t" \
    "global_load_dwordx4 v[40:43], v23, s[46:47] sc1\n\t" \
    "global_load_dwordx4 v[44:47], v2, s[46:47] sc1\n\t" \
    "global_load_dwordx4 v[48:51], v4, s[30:31] sc1\n\t" \
    "global_load_dwordx4 v[52:55], v5, s[30:31] sc1\n\t" \
    "global_load_dwordx4 v[56:59], v6, s[30:31] sc1\n\t" \
    "global_load_dwordx4 v[60:63], v7, s[30:31] sc1\n\t" \
    "global_load_dwordx4 v[64:67], v8, s[30:31] sc1\n\t" \
    "global_load_dwordx4 v[68:71], v9, s[30:31] sc1\n\t" \
    "s_waitcnt vmcnt(0) lgkmcnt(0)\n\t" \
    "v_max_u32 v140, v36, v37\n\t" \
    "v_max3_u32 v140, v140, v38, v39\n\t" \
    "v_max3_u32 v140, v140, v40, v41\n\t" \
    "v_max3_u32 v140, v140, v42, v43\n\t" \
    "v_max3_u32 v140, v140, v44, v45\n\t" \
    "v_max3_u32 v140, v140, v46, v47\n\t" \
    "v_max3_u32 v140, v140, v48, v49\n\t" \
    "v_max3_u32 v140, v140, v50, v51\n\t" \
    "v_max3_u32 v140, v140, v52, v53\n\t" \
    "v_max3_u32 v140, v140, v54, v55\n\t" \
    "v_max3_u32 v140, v140, v56, v57\n\t" \
    "v_max3_u32 v140, v140, v58, v59\n\t" \
    "v_max3_u32 v140, v140, v60, v61\n\t" \
    "v_max3_u32 v140, v140, v62, v63\n\t" \
    "v_max3_u32 v140, v140, v64, v65\n\t" \
    "v_max3_u32 v140, v140, v66, v67\n\t" \
    "v_max3_u32 v140, v140, v68, v69\n\t" \
    "v_max3_u32 v140, v140, v70, v71\n\t" \
    "v_cmp_eq_u32 vcc, -1, v140\n\t" \
    "s_cbranch_vccz vv_Gredo90_%=\n\t" \
    "s_cmp_eq_u32 s68, 0\n\t" \
    "s_cbranch_scc1 vv_F91_%=\n\t" \
    "s_sub_u32 s68, s68, 1\n\t" \
    "s_sleep 1\n\t" \
    "s_branch vv_Gag89_%=\n\t" \
    "vv_F91_%=:\n\t" \
    "v_mov_b32 v140, 5767424\n\t" \
    "v_mov_b32 v141, 2\n\t" \
    "global_store_dword v140, v141, s[36:37] sc1\n\t" \
    "s_waitcnt vmcnt(0)\n\t" \
    "s_branch vv_Gredo90_%=\n\t" \
    "vv_Gredo90_%=:\n\t" \
    "v_mov_b32 v72, 0\n\t" \
    "v_mov_b32 v73, 0\n\t" \
    "v_mov_b32 v74, 0\n\t" \
    "v_mov_b32 v75, 0\n\t" \
    "v_mov_b32 v76, 0\n\t" \
    "v_mov_b32 v77, 0\n\t" \
    "v_mov_b32 v78, 0\n\t" \
    "v_mov_b32 v79, 0\n\t" \
    "v_smfmac_f32_16x16x64_bf16 v[72:75], v[36:39], a[48:55], v10\n\t" \
    "v_smfmac_f32_16x16x64_bf16 v[72:75], v[40:43], a[56:63], v10\n\t" \
    "v_smfmac_f32_16x16x64_bf16 v[72:75], v[44:47], a[64:71], v10\n\t" \
    "v_smfmac_f32_16x16x64_bf16 v[72:75], v[48:51], a[0:7], v10\n\t" \
    "v_smfmac_f32_16x16x64_bf16 v[72:75], v[52:55], a[8:15], v10\n\t" \
    "v_smfmac_f32_16x16x64_bf16 v[72:75], v[56:59], a[16:23], v10\n\t" \
    "v_smfmac_f32_16x16x64_bf16 v[72:75], v[60:63], a[24:31], v10\n\t" \
    "v_smfmac_f32_16x16x64_bf16 v[72:75], v[64:67], a[32:39], v10\n\t" \
    "v_smfmac_f32_16x16x64_bf16 v[72:75], v[68:71], a[40:47], v10\n\t" \
    "v_smfmac_f32_16x16x64_bf16 v[76:79], v[36:39], a[120:127], v10\n\t" \
    "v_smfmac_f32_16x16x64_bf16 v[76:79], v[40:43], a[128:135], v10\n\t" \
    "v_smfmac_f32_16x16x64_bf16 v[76:79], v[44:47], a[136:143], v10\n\t" \
    "v_smfmac_f32_16x16x64_bf16 v[76:79], v[48:51], a[72:79], v10\n\t" \
    "v_smfmac_f32_16x16x64_bf16 v[76:79], v[52:55], a[80:87], v10\n\t" \
    "v_smfmac_f32_16x16x64_bf16 v[76:79], v[56:59], a[88:95], v10\n\t" \
    "v_smfmac_f32_16x16x64_bf16 v[76:79], v[60:63], a[96:103], v10\n\t" \
    "v_smfmac_f32_16x16x64_bf16 v[76:79], v[64:67], a[104:111], v10\n\t" \
    "v_smfmac_f32_16x16x64_bf16 v[76:79], v[68:71], a[112:119], v10\n\t" \
    "v_add_f32 v72, v72, v74\n\t" \
    "v_add_f32 v73, v73, v75\n\t" \
    "v_cvt_pk_bf16_f32 v80, v72, v73\n\t" \
    "ds_write_b32 v12, v80 offset:0\n\t" \
    "s_nop 7\n\t" \
    "s_branch vv_Gdone74_%=\n\t" \
    "vv_end65_%=:\n\t" \
    "s_endpgm\n\t" \
    "vv_last_%=:\n\t" \
    ""
#define ASM_R \
    "s_mov_b64 s[36:37], %0\n\t" \
    "s_mov_b64 s[42:43], %1\n\t" \
    "s_mov_b64 s[44:45], %2\n\t" \
    "s_mov_b32 s66, %3\n\t" \
    "s_mov_b32 s67, %4\n\t" \
    "s_mov_b64 s[50:51], %5\n\t" \
    "s_mov_b32 s82, %7\n\t" \
    "s_load_dwordx8 s[84:91], %6, 0\n\t" \
    "v_mbcnt_lo_u32_b32 v223, -1, 0\n\t" \
    "v_mbcnt_hi_u32_b32 v223, -1, v223\n\t" \
    "s_getpc_b64 s[46:47]\n\t" \
    "rr_pc_%=:\n\t" \
    "s_lshl_b32 s69, s66, 13\n\t" \
    "v_lshl_add_u32 v222, v223, 7, s69\n\t" \
    "v_min_u32 v222, rr_last_%=-rr_pc_%=-4, v222\n\t" \
    "global_load_dword v222, v222, s[46:47]\n\t" \
    "v_lshlrev_b32 v223, 2, v223\n\t" \
    "v_add_u32 v223, s82, v223\n\t" \
    "ds_read_b32 v0, v223 offset:0\n\t" \
    "ds_read_b32 v1, v223 offset:256\n\t" \
    "ds_read_b32 v2, v223 offset:512\n\t" \
    "ds_read_b32 v3, v223 offset:768\n\t" \
    "ds_read_b32 v4, v223 offset:1024\n\t" \
    "ds_read_b32 v5, v223 offset:1280\n\t" \
    "ds_read_b32 v6, v223 offset:1536\n\t" \
    "ds_read_b32 v7, v223 offset:1792\n\t" \
    "ds_read_b32 v8, v223 offset:2048\n\t" \
    "ds_read_b32 v9, v223 offset:2304\n\t" \
    "ds_read_b32 v10, v223 offset:2560\n\t" \
    "ds_read_b32 v11, v223 offset:2816\n\t" \
    "s_waitcnt lgkmcnt(0)\n\t" \
    "ds_read_b32 v12, v223 offset:3072\n\t" \
    "ds_read_b32 v13, v223 offset:3328\n\t" \
    "ds_read_b32 v14, v223 offset:3584\n\t" \
    "ds_read_b32 v15, v223 offset:3840\n\t" \
    "ds_read_b32 v16, v223 offset:4096\n\t" \
    "ds_read_b32 v17, v223 offset:4352\n\t" \
    "ds_read_b32 v18, v223 offset:4608\n\t" \
    "ds_read_b32 v19, v223 offset:4864\n\t" \
    "ds_read_b32 v20, v223 offset:5120\n\t" \
    "ds_read_b32 v21, v223 offset:5376\n\t" \
    "ds_read_b32 v22, v223 offset:5632\n\t" \
    "ds_read_b32 v23, v223 offset:5888\n\t" \
    "s_waitcnt lgkmcnt(0)\n\t" \
    "ds_read_b32 v24, v223 offset:6144\n\t" \
    "ds_read_b32 v25, v223 offset:6400\n\t" \
    "ds_read_b32 v26, v223 offset:6656\n\t" \
    "ds_read_b32 v27, v223 offset:6912\n\t" \
    "ds_read_b32 v28, v223 offset:7168\n\t" \
    "ds_read_b32 v29, v223 offset:7424\n\t" \
    "ds_read_b32 v30, v223 offset:7680\n\t" \
    "ds_read_b32 v31, v223 offset:7936\n\t" \
    "ds_read_b32 v32, v223 offset:8192\n\t" \
    "ds_read_b32 v33, v223 offset:8448\n\t" \
    "ds_read_b32 v34, v223 offset:8704\n\t" \
    "ds_read_b32 v35, v223 offset:8960\n\t" \
    "s_waitcnt lgkmcnt(0)\n\t" \
    "ds_read_b32 v36, v223 offset:9216\n\t" \
    "ds_read_b32 v37, v223 offset:9472\n\t" \
    "ds_read_b32 v38, v223 offset:9728\n\t" \
    "ds_read_b32 v39, v223 offset:9984\n\t" \
    "ds_read_b32 v40, v223 offset:10240\n\t" \
    "ds_read_b32 v41, v223 offset:10496\n\t" \
    "ds_read_b32 v42, v223 offset:10752\n\t" \
    "ds_read_b32 v43, v223 offset:11008\n\t" \
    "ds_read_b32 v44, v223 offset:11264\n\t" \
    "ds_read_b32 v45, v223 offset:11520\n\t" \
    "ds_read_b32 v184, v223 offset:11776\n\t" \
    "ds_read_b32 v185, v223 offset:12032\n\t" \
    "s_waitcnt lgkmcnt(0)\n\t" \
    "ds_read_b32 v186, v223 offset:12288\n\t" \
    "ds_read_b32 v187, v223 offset:12544\n\t" \
    "ds_read_b32 v188, v223 offset:12800\n\t" \
    "s_waitcnt lgkmcnt(0)\n\t" \
    "s_mov_b32 s68, 262144\n\t" \
    "s_mov_b32 s72, 0\n\t" \
    "s_mov_b32 s83, -1\n\t" \
    "s_mov_b32 s65, 0\n\t" \
    "v_xor_b32 v180, 65536, v2\n\t" \
    "v_xor_b32 v181, 131072, v2\n\t" \
    "v_xor_b32 v182, 196608, v2\n\t" \
    "v_add_u32 v189, 4096, v7\n\t" \
    "v_add_u32 v190, 16384, v7\n\t" \
    "v_add_u32 v191, 20480, v7\n\t" \
    "s_mul_i32 s69, s67, 57\n\t" \
    "s_lshr_b32 s69, s69, 9\n\t" \
    "s_mul_i32 s69, s69, 9\n\t" \
    "s_sub_u32 s69, s67, s69\n\t" \
    "s_cmp_lt_u32 s69, 4\n\t" \
    "s_cselect_b32 s70, 21, 16\n\t" \
    "s_cmp_eq_u32 s69, 8\n\t" \
    "s_cselect_b32 s70, 26, s70\n\t" \
    "s_lshr_b32 s97, s91, s70\n\t" \
    "s_and_b32 s97, s97, 31\n\t" \
    "s_min_u32 s97, s97, 24\n\t" \
    "s_lshl_b32 s97, s97, 4\n\t" \
    "s_mov_b32 s77, 3019898880\n\t" \
    "s_mov_b32 s78, 3054198966\n\t" \
    "s_waitcnt lgkmcnt(0)\n\t" \
    "s_mov_b32 s70, 116\n\t" \
    "rr_E1_%=:\n\t" \
    "s_memtime s[56:57]\n\t" \
    "s_memrealtime s[2:3]\n\t" \
    "s_nop 0\n\t" \
    "s_memrealtime s[4:5]\n\t" \
    "s_nop 0\n\t" \
    "s_memrealtime s[6:7]\n\t" \
    "s_nop 0\n\t" \
    "s_memrealtime s[8:9]\n\t" \
    "s_nop 0\n\t" \
    "s_memrealtime s[10:11]\n\t" \
    "s_nop 0\n\t" \
    "s_memrealtime s[12:13]\n\t" \
    "s_nop 0\n\t" \
    "s_memrealtime s[14:15]\n\t" \
    "s_nop 0\n\t" \
    "s_memrealtime s[16:17]\n\t" \
    "s_nop 0\n\t" \
    "s_memrealtime s[18:19]\n\t" \
    "s_nop 0\n\t" \
    "s_memrealtime s[20:21]\n\t" \
    "s_nop 0\n\t" \
    "s_memrealtime s[22:23]\n\t" \
    "s_nop 0\n\t" \
    "s_memrealtime s[24:25]\n\t" \
    "s_nop 0\n\t" \
    "s_memrealtime s[26:27]\n\t" \
    "s_nop 0\n\t" \
    "s_memrealtime s[28:29]\n\t" \
    "s_memtime s[54:55]\n\t" \
    "s_add_u32 s70, s70, 8\n\t" \
    "s_waitcnt lgkmcnt(0)\n\t" \
    "s_sub_u32 s69, s54, s56\n\t" \
    "s_cmp_gt_u32 s69, s70\n\t" \
    "s_cbranch_scc1 rr_E1_%=\n\t" \
    "s_add_u32 s2, s2, s4\n\t" \
    "s_add_u32 s6, s6, s8\n\t" \
    "s_add_u32 s10, s10, s12\n\t" \
    "s_add_u32 s14, s14, s16\n\t" \
    "s_add_u32 s18, s18, s20\n\t" \
    "s_add_u32 s22, s22, s24\n\t" \
    "s_add_u32 s2, s2, s6\n\t" \
    "s_add_u32 s10, s10, s14\n\t" \
    "s_add_u32 s18, s18, s22\n\t" \
    "s_add_u32 s2, s2, s26\n\t" \
    "s_add_u32 s10, s10, s18\n\t" \
    "s_add_u32 s2, s2, s10\n\t" \
    "s_mul_i32 s70, s28, 13\n\t" \
    "s_sub_u32 s70, s70, s2\n\t" \
    "s_min_u32 s70, s70, 52\n\t" \
    "s_mul_i32 s69, s69, s70\n\t" \
    "s_mul_hi_u32 s69, s69, 76695844\n\t" \
    "s_mov_b32 s75, s73\n\t" \
    "s_mov_b32 s76, s74\n\t" \
    "s_add_u32 s73, s56, s69\n\t" \
    "s_lshl_b32 s74, s28, 4\n\t" \
    "v_mov_b32 v172, 0xd800\n\t" \
    "v_mov_b32 v173, 0x20d800\n\t" \
    "global_load_dword v172, v172, s[36:37]\n\t" \
    "global_load_dword v173, v173, s[36:37]\n\t" \
    "v_mbcnt_lo_u32_b32 v164, -1, 0\n\t" \
    "v_mbcnt_hi_u32_b32 v164, -1, v164\n\t" \
    "v_lshlrev_b32 v164, 6, v164\n\t" \
    "v_add_u32 v164, 6225920, v164\n\t" \
    "v_mov_b32 v165, 6234112\n\t" \
    "global_load_dword v165, v165, s[36:37] sc1\n\t" \
    "global_load_dwordx2 v[166:167], v164, s[36:37] sc1\n\t" \
    "global_load_dword v168, v164, s[36:37] offset:2048 sc1\n\t" \
    "s_waitcnt vmcnt(0)\n\t" \
    "rr_C2_%=:\n\t" \
    "v_cmp_ne_u32 s[54:55], v166, v168\n\t" \
    "v_sub_u32 v166, v166, v165\n\t" \
    "v_cmp_ne_u32 vcc, 1, v166\n\t" \
    "s_nop 1\n\t" \
    "s_or_b64 vcc, vcc, s[54:55]\n\t" \
    "s_cbranch_scc0 rr_Cok3_%=\n\t" \
    "s_cmp_eq_u32 s68, 0\n\t" \
    "s_cbranch_scc1 rr_F5_%=\n\t" \
    "s_sub_u32 s68, s68, 1\n\t" \
    "s_sleep 2\n\t" \
    "s_branch rr_Cag4_%=\n\t" \
    "rr_F5_%=:\n\t" \
    "v_mov_b32 v168, 5767424\n\t" \
    "v_mov_b32 v169, 4\n\t" \
    "global_store_dword v168, v169, s[36:37] sc1\n\t" \
    "s_waitcnt vmcnt(0)\n\t" \
    "s_branch rr_Cok3_%=\n\t" \
    "rr_Cag4_%=:\n\t" \
    "global_load_dwordx2 v[166:167], v164, s[36:37] sc1\n\t" \
    "global_load_dword v168, v164, s[36:37] offset:2048 sc1\n\t" \
    "s_waitcnt vmcnt(0)\n\t" \
    "s_branch rr_C2_%=\n\t" \
    "rr_Cok3_%=:\n\t" \
    "v_mov_b32_dpp v164, v167 quad_perm:[1,0,3,2] row_mask:0xf bank_mask:0xf\n\t" \
    "v_cmp_lt_u32 s[44:45], v164, v167\n\t" \
    "s_nop 1\n\t" \
    "s_bitcmp1_b32 s44, 0\n\t" \
    "s_cselect_b32 s62, 8192, 0\n\t" \
    "s_bitcmp1_b32 s44, 2\n\t" \
    "s_cselect_b32 s63, 8192, 0\n\t" \
    "s_or_b32 s63, s63, 0x200000\n\t" \
    "v_min_u32 v164, v164, v167\n\t" \
    "v_cmp_lt_u32 vcc, 660, v164\n\t" \
    "s_nop 1\n\t" \
    "s_cmp_lg_u64 vcc, 0\n\t" \
    "s_cselect_b32 s69, 1, 0\n\t" \
    "s_mul_i32 s70, s69, 160\n\t" \
    "s_add_u32 s86, s86, s70\n\t" \
    "s_mul_i32 s70, s69, 536\n\t" \
    "s_add_u32 s87, s87, s70\n\t" \
    "s_mul_i32 s70, s69, 256\n\t" \
    "s_add_u32 s88, s88, s70\n\t" \
    "v_mov_b32 v172, -1\n\t" \
    "v_mov_b32 v173, -1\n\t" \
    "v_mov_b32 v174, -1\n\t" \
    "v_mov_b32 v175, -1\n\t" \
    "v_mov_b32 v176, 32000\n\t" \
    "s_sub_u32 s69, s63, s62\n\t" \
    "s_sub_u32 s69, s69, 0x200000\n\t" \
    "v_bfe_u32 v164, v187, 21, 1\n\t" \
    "v_mad_i32_i24 v187, v164, s69, v187\n\t" \
    "v_bfe_u32 v164, v188, 21, 1\n\t" \
    "v_mad_i32_i24 v188, v164, s69, v188\n\t" \
    "v_mov_b32 v170, 0\n\t" \
    "s_mov_b32 s65, s66\n\t" \
    "s_lshl_b32 s58, s67, 14\n\t" \
    "v_mov_b32 v200, -1\n\t" \
    "v_mov_b32 v201, -1\n\t" \
    "v_lshlrev_b32 v202, 3, v0\n\t" \
    "v_and_b32 v203, 0xfff, v202\n\t" \
    "v_lshrrev_b32 v204, 9, v0\n\t" \
    "v_add_u32 v202, 5783552, v202\n\t" \
    "v_add_u32 v203, 5771264, v203\n\t" \
    "s_sub_u32 s80, s74, 32000\n\t" \
    "s_mov_b32 s64, 1\n\t" \
    "s_mov_b32 s65, 5\n\t" \
    "s_mov_b32 s83, 99\n\t" \
    "s_add_u32 s70, s65, 59\n\t" \
    "s_mul_i32 s69, s70, 171\n\t" \
    "s_lshr_b32 s69, s69, 9\n\t" \
    "s_mul_i32 s70, s70, 589824\n\t" \
    "s_mul_i32 s71, s69, 327680\n\t" \
    "s_add_u32 s70, s70, s71\n\t" \
    "s_lshl1_add_u32 s69, s69, 4\n\t" \
    "s_bitcmp1_b64 s[44:45], s69\n\t" \
    "s_cselect_b32 s69, 8192, 0\n\t" \
    "s_add_u32 s69, s69, s70\n\t" \
    "s_add_u32 s69, s69, 123731968\n\t" \
    "s_add_u32 s69, s69, s58\n\t" \
    "s_add_u32 s40, s36, s69\n\t" \
    "s_addc_u32 s41, s37, 0\n\t" \
    "s_branch rr_act7_%=\n\t" \
    "rr_g218_%=:\n\t" \
    "s_waitcnt vmcnt(0)\n\t" \
    "s_mov_b32 s64, 0\n\t" \
    "s_mov_b32 s65, s66\n\t" \
    "s_mov_b32 s83, 1\n\t" \
    "s_mov_b32 s77, 3154116608\n\t" \
    "rr_pf13_%=:\n\t" \
    "s_cmp_gt_u32 s65, 7\n\t" \
    "s_cselect_b32 s83, 99, s83\n\t" \
    "rr_WF19_%=:\n\t" \
    "s_cmp_ge_i32 s83, s65\n\t" \
    "s_cbranch_scc1 rr_WFok20_%=\n\t" \
    "global_load_dword v164, v6, s[36:37] sc1\n\t" \
    "s_waitcnt vmcnt(0)\n\t" \
    "v_min_i32_dpp v164, v164, v164 row_ror:1 row_mask:0xf bank_mask:0xf\n\t" \
    "s_nop 1\n\t" \
    "v_min_i32_dpp v164, v164, v164 row_ror:2 row_mask:0xf bank_mask:0xf\n\t" \
    "s_nop 1\n\t" \
    "v_min_i32_dpp v164, v164, v164 row_ror:4 row_mask:0xf bank_mask:0xf\n\t" \
    "s_nop 1\n\t" \
    "v_min_i32_dpp v164, v164, v164 row_ror:8 row_mask:0xf bank_mask:0xf\n\t" \
    "s_nop 0\n\t" \
    "v_readlane_b32 s69, v164, 0\n\t" \
    "v_readlane_b32 s70, v164, 16\n\t" \
    "s_nop 1\n\t" \
    "s_min_i32 s83, s69, s70\n\t" \
    "s_cmp_ge_i32 s83, s65\n\t" \
    "s_cbranch_scc1 rr_WFok20_%=\n\t" \
    "s_cmp_eq_u32 s68, 0\n\t" \
    "s_cbranch_scc1 rr_F21_%=\n\t" \
    "s_sub_u32 s68, s68, 1\n\t" \
    "s_sleep 2\n\t" \
    "s_branch rr_WF19_%=\n\t" \
    "rr_F21_%=:\n\t" \
    "v_mov_b32 v164, 5767424\n\t" \
    "v_mov_b32 v165, 10\n\t" \
    "global_store_dword v164, v165, s[36:37] sc1\n\t" \
    "s_waitcnt vmcnt(0)\n\t" \
    "s_branch rr_WFok20_%=\n\t" \
    "rr_WFok20_%=:\n\t" \
    "s_mul_i32 s70, s67, 7\n\t" \
    "s_add_u32 s69, s65, 0\n\t" \
    "s_xor_b32 s69, s69, s70\n\t" \
    "s_and_b32 s69, s69, 63\n\t" \
    "s_lshl_b32 s69, s69, 15\n\t" \
    "s_mul_i32 s70, s67, 2097152\n\t" \
    "s_add_u32 s69, s69, s70\n\t" \
    "s_add_u32 s69, s69, 48234496\n\t" \
    "s_add_u32 s38, s36, s69\n\t" \
    "s_addc_u32 s39, s37, 0\n\t" \
    "s_add_u32 s70, s65, 0\n\t" \
    "s_mul_i32 s69, s70, 171\n\t" \
    "s_lshr_b32 s69, s69, 9\n\t" \
    "s_mul_i32 s70, s70, 589824\n\t" \
    "s_mul_i32 s71, s69, 327680\n\t" \
    "s_add_u32 s70, s70, s71\n\t" \
    "s_lshl1_add_u32 s69, s69, 4\n\t" \
    "s_bitcmp1_b64 s[44:45], s69\n\t" \
    "s_cselect_b32 s69, 8192, 0\n\t" \
    "s_add_u32 s69, s69, s70\n\t" \
    "s_add_u32 s69, s69, 123731968\n\t" \
    "s_add_u32 s69, s69, s58\n\t" \
    "s_add_u32 s40, s36, s69\n\t" \
    "s_addc_u32 s41, s37, 0\n\t" \
    "s_and_b32 s69, s69, 0xffe00000\n\t" \
    "s_add_u32 s69, s69, 1823744\n\t" \
    "v_mov_b32 v168, s69\n\t" \
    "global_load_dword v168, v168, s[36:37]\n\t" \
    "v_mov_b32 v164, s83\n\t" \
    "s_add_u32 s69, s65, 2\n\t" \
    "s_cmp_ge_i32 s83, s69\n\t" \
    "s_cbranch_scc1 rr_nq22_%=\n\t" \
    "global_load_dword v164, v6, s[36:37] sc1\n\t" \
    "rr_nq22_%=:\n\t" \
    "s_cmp_gt_u32 s65, 61\n\t" \
    "s_cbranch_scc1 rr_idle6_%=\n\t" \
    "s_add_u32 s69, s65, 2\n\t" \
    "s_and_b32 s69, s69, 7\n\t" \
    "s_lshl_b32 s69, s69, 18\n\t" \
    "s_add_u32 s69, s69, s62\n\t" \
    "s_add_u32 s46, s36, s69\n\t" \
    "s_addc_u32 s47, s37, 0\n\t" \
    "global_store_dwordx4 v184, v[172:175], s[46:47] sc1\n\t" \
    "global_store_dwordx4 v185, v[172:175], s[46:47] sc1\n\t" \
    "global_store_dwordx4 v186, v[172:175], s[46:47] sc1\n\t" \
    "global_store_dwordx4 v187, v[172:175], s[46:47] sc1\n\t" \
    "global_store_dwordx4 v188, v[172:175], s[46:47] sc1\n\t" \
    "rr_idle6_%=:\n\t" \
    "s_cmp_lg_u32 s67, 35\n\t" \
    "s_cbranch_scc1 rr_cke23_%=\n\t" \
    "s_cmp_lt_u32 s65, 6\n\t" \
    "s_cbranch_scc1 rr_cke23_%=\n\t" \
    "s_add_u32 s69, s65, 3\n\t" \
    "s_min_u32 s69, s69, 63\n\t" \
    "s_add_u32 s70, s65, 2\n\t" \
    "s_min_u32 s70, s70, 63\n\t" \
    "s_cmp_lt_u32 s69, 24\n\t" \
    "s_cselect_b32 s71, 1, 0\n\t" \
    "v_xor_b32 v165, s69, v204\n\t" \
    "v_and_b32 v165, s71, v165\n\t" \
    "v_add_u32 v165, v165, v200\n\t" \
    "v_cmp_gt_i32 s[30:31], s69, v165\n\t" \
    "v_cmp_gt_i32 vcc, s70, v201\n\t" \
    "s_nop 1\n\t" \
    "s_or_b64 vcc, vcc, s[30:31]\n\t" \
    "s_cbranch_scc0 rr_cke23_%=\n\t" \
    "global_load_dword v200, v202, s[36:37] sc1\n\t" \
    "global_load_dword v201, v203, s[36:37] sc1\n\t" \
    "rr_cke23_%=:\n\t" \
    "s_cmp_gt_u32 s65, 62\n\t" \
    "s_cbranch_scc1 rr_n224_%=\n\t" \
    "s_cmp_lt_u32 s65, 2\n\t" \
    "s_cbranch_scc1 rr_nw225_%=\n\t" \
    "s_mul_hi_u32 s69, s85, s78\n\t" \
    "s_add_u32 s79, s81, s69\n\t" \
    "s_add_u32 s79, s79, s87\n\t" \
    "s_cmp_ge_u32 s65, 38\n\t" \
    "s_cselect_b32 s69, 32, 0\n\t" \
    "s_sub_u32 s79, s79, s69\n\t" \
    "s_cmp_lt_u32 s65, 11\n\t" \
    "s_cselect_b32 s69, 64, 0\n\t" \
    "s_add_u32 s79, s79, s69\n\t" \
    "s_add_u32 s80, s79, s88\n\t" \
    "s_add_u32 s79, s79, 240\n\t" \
    "s_cmp_gt_u32 s65, 8\n\t" \
    "s_cselect_b32 s69, 0, 176\n\t" \
    "s_sub_u32 s79, s79, s69\n\t" \
    "s_sub_u32 s70, s79, s74\n\t" \
    "s_max_i32 s70, s70, 0\n\t" \
    "s_min_i32 s70, s70, 16000\n\t" \
    "s_lshl_b32 s70, s70, 1\n\t" \
    "s_mul_hi_u32 s70, s70, s77\n\t" \
    "s_add_u32 s70, s70, s73\n\t" \
    "rr_W26_%=:\n\t" \
    "s_memtime s[56:57]\n\t" \
    "s_waitcnt lgkmcnt(0)\n\t" \
    "s_sub_u32 s69, s70, s56\n\t" \
    "s_cmp_gt_i32 s69, 24\n\t" \
    "s_cbranch_scc1 rr_W26_%=\n\t" \
    "s_branch rr_g117_%=\n\t" \
    "rr_nw225_%=:\n\t" \
    "v_and_b32 v165, 0xfffff83f, v191\n\t" \
    "v_add_u32 v165, s63, v165\n\t" \
    "rr_g016_%=:\n\t" \
    "global_load_dword v166, v165, s[36:37] sc1\n\t" \
    "s_waitcnt vmcnt(0)\n\t" \
    "v_cmp_eq_u32 vcc, -1, v166\n\t" \
    "s_cbranch_vccz rr_gs27_%=\n\t" \
    "s_cmp_eq_u32 s68, 0\n\t" \
    "s_cbranch_scc1 rr_F28_%=\n\t" \
    "s_sub_u32 s68, s68, 1\n\t" \
    "s_sleep 1\n\t" \
    "s_branch rr_g016_%=\n\t" \
    "rr_F28_%=:\n\t" \
    "v_mov_b32 v167, 5767424\n\t" \
    "v_mov_b32 v168, 16\n\t" \
    "global_store_dword v167, v168, s[36:37] sc1\n\t" \
    "s_waitcnt vmcnt(0)\n\t" \
    "s_branch rr_gs27_%=\n\t" \
    "rr_gs27_%=:\n\t" \
    "s_cmp_eq_u32 s66, 0\n\t" \
    "s_cbranch_scc1 rr_g117_%=\n\t" \
    "s_sleep 8\n\t" \
    "rr_g117_%=:\n\t" \
    "global_load_dwordx4 a[0:3], v26, s[38:39] offset:0 sc1\n\t" \
    "global_load_dwordx4 a[4:7], v26, s[38:39] offset:1024 sc1\n\t" \
    "global_load_dwordx4 a[8:11], v27, s[38:39] offset:0 sc1\n\t" \
    "global_load_dwordx4 a[12:15], v27, s[38:39] offset:1024 sc1\n\t" \
    "global_load_dwordx4 a[16:19], v28, s[38:39] offset:0 sc1\n\t" \
    "global_load_dwordx4 a[20:23], v28, s[38:39] offset:1024 sc1\n\t" \
    "global_load_dwordx4 a[24:27], v29, s[38:39] offset:0 sc1\n\t" \
    "global_load_dwordx4 a[28:31], v29, s[38:39] offset:1024 sc1\n\t" \
    "global_load_dwordx4 a[32:35], v30, s[38:39] offset:0 sc1\n\t" \
    "global_load_dwordx4 a[36:39], v30, s[38:39] offset:1024 sc1\n\t" \
    "global_load_dwordx4 a[40:43], v31, s[38:39] offset:0 sc1\n\t" \
    "global_load_dwordx4 a[44:47], v31, s[38:39] offset:1024 sc1\n\t" \
    "global_load_dwordx4 a[48:51], v32, s[38:39] offset:0 sc1\n\t" \
    "global_load_dwordx4 a[52:55], v32, s[38:39] offset:1024 sc1\n\t" \
    "global_load_dwordx4 a[56:59], v33, s[38:39] offset:0 sc1\n\t" \
    "global_load_dwordx4 a[60:63], v33, s[38:39] offset:1024 sc1\n\t" \
    "global_load_dwordx4 a[64:67], v34, s[38:39] offset:0 sc1\n\t" \
    "global_load_dwordx4 a[68:71], v34, s[38:39] offset:1024 sc1\n\t" \
    "global_load_dwordx4 a[72:75], v35, s[38:39] offset:0 sc1\n\t" \
    "global_load_dwordx4 a[76:79], v35, s[38:39] offset:1024 sc1\n\t" \
    "global_load_dwordx4 a[80:83], v36, s[38:39] offset:0 sc1\n\t" \
    "global_load_dwordx4 a[84:87], v36, s[38:39] offset:1024 sc1\n\t" \
    "global_load_dwordx4 a[88:91], v37, s[38:39] offset:0 sc1\n\t" \
    "global_load_dwordx4 a[92:95], v37, s[38:39] offset:1024 sc1\n\t" \
    "global_load_dwordx4 a[96:99], v38, s[38:39] offset:0 sc1\n\t" \
    "global_load_dwordx4 a[100:103], v38, s[38:39] offset:1024 sc1\n\t" \
    "global_load_dwordx4 a[104:107], v39, s[38:39] offset:0 sc1\n\t" \
    "global_load_dwordx4 a[108:111], v39, s[38:39] offset:1024 sc1\n\t" \
    "global_load_dwordx4 a[112:115], v40, s[38:39] offset:0 sc1\n\t" \
    "global_load_dwordx4 a[116:119], v40, s[38:39] offset:1024 sc1\n\t" \
    "global_load_dwordx4 a[120:123], v41, s[38:39] offset:0 sc1\n\t" \
    "global_load_dwordx4 a[124:127], v41, s[38:39] offset:1024 sc1\n\t" \
    "rr_n224_%=:\n\t" \
    "s_cmp_eq_u32 s65, 0\n\t" \
    "s_cbranch_scc1 rr_act7_%=\n\t" \
    "s_waitcnt vmcnt(0)\n\t" \
    "s_cmp_lg_u32 s67, 35\n\t" \
    "s_cbranch_scc1 rr_ck29_%=\n\t" \
    "s_cmp_lt_u32 s65, 6\n\t" \
    "s_cbranch_scc1 rr_ck29_%=\n\t" \
    "rr_ckl30_%=:\n\t" \
    "s_add_u32 s69, s65, 3\n\t" \
    "s_min_u32 s69, s69, 63\n\t" \
    "s_add_u32 s70, s65, 2\n\t" \
    "s_min_u32 s70, s70, 63\n\t" \
    "s_cmp_lt_u32 s69, 24\n\t" \
    "s_cselect_b32 s71, 1, 0\n\t" \
    "v_xor_b32 v165, s69, v204\n\t" \
    "v_and_b32 v165, s71, v165\n\t" \
    "v_add_u32 v165, v165, v200\n\t" \
    "v_cmp_gt_i32 s[30:31], s69, v165\n\t" \
    "v_cmp_gt_i32 vcc, s70, v201\n\t" \
    "s_nop 1\n\t" \
    "s_or_b64 vcc, vcc, s[30:31]\n\t" \
    "s_cbranch_scc0 rr_ck29_%=\n\t" \
    "global_load_dword v200, v202, s[36:37] sc1\n\t" \
    "global_load_dword v201, v203, s[36:37] sc1\n\t" \
    "s_waitcnt vmcnt(0)\n\t" \
    "s_cmp_eq_u32 s68, 0\n\t" \
    "s_cbranch_scc1 rr_F32_%=\n\t" \
    "s_sub_u32 s68, s68, 1\n\t" \
    "s_sleep 1\n\t" \
    "s_branch rr_ckl30_%=\n\t" \
    "rr_F32_%=:\n\t" \
    "v_mov_b32 v166, 5767424\n\t" \
    "v_mov_b32 v167, 15\n\t" \
    "global_store_dword v166, v167, s[36:37] sc1\n\t" \
    "s_waitcnt vmcnt(0)\n\t" \
    "s_branch rr_ck29_%=\n\t" \
    "rr_ck29_%=:\n\t" \
    "s_cmp_lg_u32 s65, 40\n\t" \
    "s_cbranch_scc1 rr_f233_%=\n\t" \
    "s_cmp_lg_u32 s67, 35\n\t" \
    "s_cbranch_scc1 rr_f233_%=\n\t" \
    "v_add_u32 v165, 36864, v6\n\t" \
    "rr_f2l34_%=:\n\t" \
    "global_load_dword v166, v165, s[36:37] sc1\n\t" \
    "s_waitcnt vmcnt(0)\n\t" \
    "v_cmp_ne_u32 vcc, 2, v166\n\t" \
    "s_cbranch_vccz rr_f233_%=\n\t" \
    "s_cmp_eq_u32 s68, 0\n\t" \
    "s_cbranch_scc1 rr_F35_%=\n\t" \
    "s_sub_u32 s68, s68, 1\n\t" \
    "s_sleep 2\n\t" \
    "s_branch rr_f2l34_%=\n\t" \
    "rr_F35_%=:\n\t" \
    "v_mov_b32 v167, 5767424\n\t" \
    "v_mov_b32 v168, 11\n\t" \
    "global_store_dword v167, v168, s[36:37] sc1\n\t" \
    "s_waitcnt vmcnt(0)\n\t" \
    "s_branch rr_f233_%=\n\t" \
    "rr_f233_%=:\n\t" \
    "v_min_i32_dpp v164, v164, v164 row_ror:1 row_mask:0xf bank_mask:0xf\n\t" \
    "s_nop 1\n\t" \
    "v_min_i32_dpp v164, v164, v164 row_ror:2 row_mask:0xf bank_mask:0xf\n\t" \
    "s_nop 1\n\t" \
    "v_min_i32_dpp v164, v164, v164 row_ror:4 row_mask:0xf bank_mask:0xf\n\t" \
    "s_nop 1\n\t" \
    "v_min_i32_dpp v164, v164, v164 row_ror:8 row_mask:0xf bank_mask:0xf\n\t" \
    "s_nop 0\n\t" \
    "v_readlane_b32 s69, v164, 0\n\t" \
    "v_readlane_b32 s70, v164, 16\n\t" \
    "s_nop 1\n\t" \
    "s_min_i32 s69, s69, s70\n\t" \
    "s_max_i32 s83, s83, s69\n\t" \
    "s_cmp_eq_u32 s65, 0\n\t" \
    "s_cbranch_scc1 rr_act7_%=\n\t" \
    "s_barrier\n\t" \
    "s_cmp_gt_u32 s65, 62\n\t" \
    "s_cbranch_scc1 rr_end8_%=\n\t" \
    "ds_read_b32 v164, v9\n\t" \
    "s_waitcnt lgkmcnt(0)\n\t" \
    "v_readfirstlane_b32 s81, v164\n\t" \
    "s_nop 1\n\t" \
    "s_cmp_lg_u32 s65, 1\n\t" \
    "s_cbranch_scc1 rr_n136_%=\n\t" \
    "s_waitcnt lgkmcnt(0)\n\t" \
    "s_mov_b32 s70, 116\n\t" \
    "rr_E37_%=:\n\t" \
    "s_memtime s[56:57]\n\t" \
    "s_memrealtime s[2:3]\n\t" \
    "s_nop 0\n\t" \
    "s_memrealtime s[4:5]\n\t" \
    "s_nop 0\n\t" \
    "s_memrealtime s[6:7]\n\t" \
    "s_nop 0\n\t" \
    "s_memrealtime s[8:9]\n\t" \
    "s_nop 0\n\t" \
    "s_memrealtime s[10:11]\n\t" \
    "s_nop 0\n\t" \
    "s_memrealtime s[12:13]\n\t" \
    "s_nop 0\n\t" \
    "s_memrealtime s[14:15]\n\t" \
    "s_nop 0\n\t" \
    "s_memrealtime s[16:17]\n\t" \
    "s_nop 0\n\t" \
    "s_memrealtime s[18:19]\n\t" \
    "s_nop 0\n\t" \
    "s_memrealtime s[20:21]\n\t" \
    "s_nop 0\n\t" \
    "s_memrealtime s[22:23]\n\t" \
    "s_nop 0\n\t" \
    "s_memrealtime s[24:25]\n\t" \
    "s_nop 0\n\t" \
    "s_memrealtime s[26:27]\n\t" \
    "s_nop 0\n\t" \
    "s_memrealtime s[28:29]\n\t" \
    "s_memtime s[54:55]\n\t" \
    "s_add_u32 s70, s70, 8\n\t" \
    "s_waitcnt lgkmcnt(0)\n\t" \
    "s_sub_u32 s69, s54, s56\n\t" \
    "s_cmp_gt_u32 s69, s70\n\t" \
    "s_cbranch_scc1 rr_E37_%=\n\t" \
    "s_add_u32 s2, s2, s4\n\t" \
    "s_add_u32 s6, s6, s8\n\t" \
    "s_add_u32 s10, s10, s12\n\t" \
    "s_add_u32 s14, s14, s16\n\t" \
    "s_add_u32 s18, s18, s20\n\t" \
    "s_add_u32 s22, s22, s24\n\t" \
    "s_add_u32 s2, s2, s6\n\t" \
    "s_add_u32 s10, s10, s14\n\t" \
    "s_add_u32 s18, s18, s22\n\t" \
    "s_add_u32 s2, s2, s26\n\t" \
    "s_add_u32 s10, s10, s18\n\t" \
    "s_add_u32 s2, s2, s10\n\t" \
    "s_mul_i32 s70, s28, 13\n\t" \
    "s_sub_u32 s70, s70, s2\n\t" \
    "s_min_u32 s70, s70, 52\n\t" \
    "s_mul_i32 s69, s69, s70\n\t" \
    "s_mul_hi_u32 s69, s69, 76695844\n\t" \
    "s_mov_b32 s75, s73\n\t" \
    "s_mov_b32 s76, s74\n\t" \
    "s_add_u32 s73, s56, s69\n\t" \
    "s_lshl_b32 s74, s28, 4\n\t" \
    "rr_n136_%=:\n\t" \
    "s_mul_hi_u32 s69, s85, s78\n\t" \
    "s_add_u32 s79, s81, s69\n\t" \
    "s_add_u32 s79, s79, s87\n\t" \
    "s_cmp_ge_u32 s65, 37\n\t" \
    "s_cselect_b32 s69, 32, 0\n\t" \
    "s_sub_u32 s79, s79, s69\n\t" \
    "s_cmp_lt_u32 s65, 10\n\t" \
    "s_cselect_b32 s69, 64, 0\n\t" \
    "s_add_u32 s79, s79, s69\n\t" \
    "s_add_u32 s80, s79, s88\n\t" \
    "s_sub_u32 s70, s79, s74\n\t" \
    "s_max_i32 s70, s70, 0\n\t" \
    "s_min_i32 s70, s70, 16000\n\t" \
    "s_lshl_b32 s70, s70, 1\n\t" \
    "s_mul_hi_u32 s70, s70, s77\n\t" \
    "s_add_u32 s70, s70, s73\n\t" \
    "rr_W38_%=:\n\t" \
    "s_memtime s[56:57]\n\t" \
    "s_waitcnt lgkmcnt(0)\n\t" \
    "s_sub_u32 s69, s70, s56\n\t" \
    "s_cmp_gt_i32 s69, 24\n\t" \
    "s_cbranch_scc1 rr_W38_%=\n\t" \
    "rr_act7_%=:\n\t" \
    "s_add_u32 s69, s65, 0\n\t" \
    "s_and_b32 s69, s69, 7\n\t" \
    "s_lshl_b32 s69, s69, 18\n\t" \
    "s_add_u32 s69, s69, s62\n\t" \
    "s_add_u32 s46, s36, s69\n\t" \
    "s_addc_u32 s47, s37, 0\n\t" \
    "s_add_u32 s69, s65, 1\n\t" \
    "s_and_b32 s69, s69, 7\n\t" \
    "s_lshl_b32 s69, s69, 18\n\t" \
    "s_add_u32 s69, s69, s62\n\t" \
    "s_add_u32 s48, s36, s69\n\t" \
    "s_addc_u32 s49, s37, 0\n\t" \
    "s_and_b32 s69, s65, 7\n\t" \
    "s_lshl_b32 s69, s69, 18\n\t" \
    "s_add_u32 s69, s69, s63\n\t" \
    "s_add_u32 s52, s36, s69\n\t" \
    "s_addc_u32 s53, s37, 0\n\t" \
    "s_cmp_eq_u32 s65, 0\n\t" \
    "s_cbranch_scc1 rr_nf39_%=\n\t" \
    "s_add_u32 s69, s65, 2\n\t" \
    "s_cmp_ge_i32 s83, s69\n\t" \
    "s_cbranch_scc1 rr_nf39_%=\n\t" \
    "global_load_dword v164, v6, s[36:37] sc1\n\t" \
    "s_waitcnt vmcnt(0)\n\t" \
    "v_min_i32_dpp v164, v164, v164 row_ror:1 row_mask:0xf bank_mask:0xf\n\t" \
    "s_nop 1\n\t" \
    "v_min_i32_dpp v164, v164, v164 row_ror:2 row_mask:0xf bank_mask:0xf\n\t" \
    "s_nop 1\n\t" \
    "v_min_i32_dpp v164, v164, v164 row_ror:4 row_mask:0xf bank_mask:0xf\n\t" \
    "s_nop 1\n\t" \
    "v_min_i32_dpp v164, v164, v164 row_ror:8 row_mask:0xf bank_mask:0xf\n\t" \
    "s_nop 0\n\t" \
    "v_readlane_b32 s69, v164, 0\n\t" \
    "v_readlane_b32 s70, v164, 16\n\t" \
    "s_nop 1\n\t" \
    "s_min_i32 s83, s69, s70\n\t" \
    "rr_nf39_%=:\n\t" \
    "s_waitcnt lgkmcnt(0)\n\t" \
    "s_mov_b32 s70, 116\n\t" \
    "rr_E40_%=:\n\t" \
    "s_memtime s[56:57]\n\t" \
    "s_memrealtime s[2:3]\n\t" \
    "s_nop 0\n\t" \
    "s_memrealtime s[4:5]\n\t" \
    "s_nop 0\n\t" \
    "s_memrealtime s[6:7]\n\t" \
    "s_nop 0\n\t" \
    "s_memrealtime s[8:9]\n\t" \
    "s_nop 0\n\t" \
    "s_memrealtime s[10:11]\n\t" \
    "s_nop 0\n\t" \
    "s_memrealtime s[12:13]\n\t" \
    "s_nop 0\n\t" \
    "s_memrealtime s[14:15]\n\t" \
    "s_nop 0\n\t" \
    "s_memrealtime s[16:17]\n\t" \
    "s_nop 0\n\t" \
    "s_memrealtime s[18:19]\n\t" \
    "s_nop 0\n\t" \
    "s_memrealtime s[20:21]\n\t" \
    "s_nop 0\n\t" \
    "s_memrealtime s[22:23]\n\t" \
    "s_nop 0\n\t" \
    "s_memrealtime s[24:25]\n\t" \
    "s_nop 0\n\t" \
    "s_memrealtime s[26:27]\n\t" \
    "s_nop 0\n\t" \
    "s_memrealtime s[28:29]\n\t" \
    "s_memtime s[54:55]\n\t" \
    "s_add_u32 s70, s70, 8\n\t" \
    "s_waitcnt lgkmcnt(0)\n\t" \
    "s_sub_u32 s69, s54, s56\n\t" \
    "s_cmp_gt_u32 s69, s70\n\t" \
    "s_cbranch_scc1 rr_E40_%=\n\t" \
    "s_add_u32 s2, s2, s4\n\t" \
    "s_add_u32 s6, s6, s8\n\t" \
    "s_add_u32 s10, s10, s12\n\t" \
    "s_add_u32 s14, s14, s16\n\t" \
    "s_add_u32 s18, s18, s20\n\t" \
    "s_add_u32 s22, s22, s24\n\t" \
    "s_add_u32 s2, s2, s6\n\t" \
    "s_add_u32 s10, s10, s14\n\t" \
    "s_add_u32 s18, s18, s22\n\t" \
    "s_add_u32 s2, s2, s26\n\t" \
    "s_add_u32 s10, s10, s18\n\t" \
    "s_add_u32 s2, s2, s10\n\t" \
    "s_mul_i32 s70, s28, 13\n\t" \
    "s_sub_u32 s70, s70, s2\n\t" \
    "s_min_u32 s70, s70, 52\n\t" \
    "s_mul_i32 s69, s69, s70\n\t" \
    "s_mul_hi_u32 s69, s69, 76695844\n\t" \
    "s_mov_b32 s75, s73\n\t" \
    "s_mov_b32 s76, s74\n\t" \
    "s_add_u32 s73, s56, s69\n\t" \
    "s_lshl_b32 s74, s28, 4\n\t" \
    "s_sub_u32 s69, s73, s75\n\t" \
    "s_sub_u32 s70, s74, s76\n\t" \
    "s_sub_u32 s71, s70, 1600\n\t" \
    "s_cmp_lt_u32 s71, 400000\n\t" \
    "s_cbranch_scc0 rr_K41_%=\n\t" \
    "v_cvt_f32_u32 v164, s69\n\t" \
    "v_cvt_f32_u32 v165, s70\n\t" \
    "v_rcp_f32 v166, v164\n\t" \
    "v_rcp_f32 v167, v165\n\t" \
    "v_mul_f32 v166, v166, v165\n\t" \
    "v_mul_f32 v167, v167, v164\n\t" \
    "v_mul_f32 v166, 0x4f800000, v166\n\t" \
    "v_mul_f32 v167, 0x4f000000, v167\n\t" \
    "v_cvt_u32_f32 v166, v166\n\t" \
    "v_cvt_u32_f32 v167, v167\n\t" \
    "v_readfirstlane_b32 s69, v166\n\t" \
    "v_readfirstlane_b32 s70, v167\n\t" \
    "s_nop 1\n\t" \
    "s_lshr_b32 s71, s65, 1\n\t" \
    "s_min_u32 s71, s71, 2\n\t" \
    "s_sub_u32 s69, s69, s78\n\t" \
    "s_ashr_i32 s69, s69, s71\n\t" \
    "s_add_u32 s78, s78, s69\n\t" \
    "s_sub_u32 s70, s70, s77\n\t" \
    "s_ashr_i32 s70, s70, s71\n\t" \
    "s_add_u32 s77, s77, s70\n\t" \
    "rr_K41_%=:\n\t" \
    "s_mov_b32 s91, s64\n\t" \
    "s_cmp_lg_u32 s64, 0\n\t" \
    "s_cselect_b32 s77, 0, s77\n\t" \
    "v_mov_b32 v160, 0\n\t" \
    "v_mov_b32 v161, 0\n\t" \
    "v_mov_b32 v162, 0\n\t" \
    "v_mov_b32 v163, 0\n\t" \
    ".p2align 6\n\t" \
    "s_nop 0\n\t" \
    "s_nop 0\n\t" \
    "s_sub_u32 s70, s80, s74\n\t" \
    "s_max_i32 s70, s70, 0\n\t" \
    "s_min_i32 s70, s70, 16000\n\t" \
    "s_lshl_b32 s70, s70, 1\n\t" \
    "s_mul_hi_u32 s70, s70, s77\n\t" \
    "s_add_u32 s70, s70, s73\n\t" \
    "s_getpc_b64 s[30:31]\n\t" \
    "rr_Wp44_%=:\n\t" \
    "s_add_u32 s30, s30, rr_We45_%=-rr_Wp44_%=\n\t" \
    "s_addc_u32 s31, s31, 0\n\t" \
    "s_sub_u32 s70, s70, 96\n\t" \
    "rr_W43_%=:\n\t" \
    "s_memtime s[56:57]\n\t" \
    "s_waitcnt lgkmcnt(0)\n\t" \
    "s_sub_u32 s69, s70, s56\n\t" \
    "s_cmp_gt_i32 s69, 80\n\t" \
    "s_cbranch_scc1 rr_W43_%=\n\t" \
    "s_max_i32 s69, s69, 0\n\t" \
    "s_and_b32 s69, s69, -4\n\t" \
    "s_sub_u32 s30, s30, s69\n\t" \
    "s_subb_u32 s31, s31, 0\n\t" \
    "s_setpc_b64 s[30:31]\n\t" \
    "s_nop 0\n\t" \
    "s_nop 0\n\t" \
    "s_nop 0\n\t" \
    "s_nop 0\n\t" \
    "s_nop 0\n\t" \
    "s_nop 0\n\t" \
    "s_nop 0\n\t" \
    "s_nop 0\n\t" \
    "s_nop 0\n\t" \
    "s_nop 0\n\t" \
    "s_nop 0\n\t" \
    "s_nop 0\n\t" \
    "s_nop 0\n\t" \
    "s_nop 0\n\t" \
    "s_nop 0\n\t" \
    "s_nop 0\n\t" \
    "s_nop 0\n\t" \
    "s_nop 0\n\t" \
    "s_nop 0\n\t" \
    "s_nop 0\n\t" \
    "rr_We45_%=:\n\t" \
    "global_load_dword v192, v7, s[52:53] offset:0 sc1\n\t" \
    "s_sub_u32 s71, s86, s74\n\t" \
    "s_getpc_b64 s[30:31]\n\t" \
    "rr_rpc49_%=:\n\t" \
    "s_add_u32 s50, s30, rr_pgo52_%=-rr_rpc49_%=\n\t" \
    "s_addc_u32 s51, s31, 0\n\t" \
    "s_add_u32 s30, s30, rr_again9_%=-rr_rpc49_%=\n\t" \
    "s_addc_u32 s31, s31, 0\n\t" \
    "s_nop 7\n\t" \
    "s_nop 7\n\t" \
    "s_nop 7\n\t" \
    "s_nop 7\n\t" \
    "s_nop 7\n\t" \
    "global_load_dword v193, v7, s[52:53] offset:2048 sc1\n\t" \
    "s_nop 7\n\t" \
    "s_nop 7\n\t" \
    "s_nop 7\n\t" \
    "s_nop 7\n\t" \
    "s_nop 7\n\t" \
    "s_nop 7\n\t" \
    "global_load_dword v194, v189, s[52:53] offset:0 sc1\n\t" \
    "s_waitcnt vmcnt(2)\n\t" \
    "rr_hok46_%=:\n\t" \
    "s_memtime s[54:55]\n\t" \
    "v_max_u32_dpp v192, v192, v192 row_ror:1 row_mask:0xf bank_mask:0xf\n\t" \
    "s_nop 1\n\t" \
    "v_max_u32_dpp v192, v192, v192 row_ror:2 row_mask:0xf bank_mask:0xf\n\t" \
    "s_nop 1\n\t" \
    "v_max_u32_dpp v192, v192, v192 row_ror:4 row_mask:0xf bank_mask:0xf\n\t" \
    "s_nop 1\n\t" \
    "v_max_u32_dpp v192, v192, v192 row_ror:8 row_mask:0xf bank_mask:0xf\n\t" \
    "s_nop 0\n\t" \
    "v_readlane_b32 s69, v192, 0\n\t" \
    "v_readlane_b32 s70, v192, 16\n\t" \
    "s_waitcnt lgkmcnt(0)\n\t" \
    "s_sub_u32 s56, s73, s54\n\t" \
    "s_max_u32 s81, s69, s70\n\t" \
    "s_cmp_eq_u32 s81, -1\n\t" \
    "s_cbranch_scc1 rr_hlate47_%=\n\t" \
    "s_add_u32 s70, s81, s71\n\t" \
    "s_max_i32 s70, s70, 0\n\t" \
    "s_min_i32 s70, s70, 16000\n\t" \
    "s_lshl_b32 s70, s70, 1\n\t" \
    "s_mul_hi_u32 s70, s70, s77\n\t" \
    "s_sub_u32 s56, s56, 292\n\t" \
    "s_add_u32 s69, s70, s56\n\t" \
    "s_max_i32 s69, s69, 0\n\t" \
    "s_min_i32 s96, s69, s97\n\t" \
    "s_and_b32 s96, s96, -16\n\t" \
    "s_sub_u32 s69, s69, s96\n\t" \
    "s_cmp_gt_i32 s69, 80\n\t" \
    "s_cbranch_scc1 rr_rpoll50_%=\n\t" \
    "rr_rback51_%=:\n\t" \
    "s_and_b32 s69, s69, -4\n\t" \
    "s_sub_u32 s30, s30, s69\n\t" \
    "s_subb_u32 s31, s31, 0\n\t" \
    "s_setpc_b64 s[30:31]\n\t" \
    "rr_rpoll50_%=:\n\t" \
    "s_add_u32 s70, s70, s73\n\t" \
    "s_sub_u32 s70, s70, 216\n\t" \
    "s_sub_u32 s70, s70, s96\n\t" \
    "rr_wl48_%=:\n\t" \
    "s_memtime s[56:57]\n\t" \
    "s_waitcnt lgkmcnt(0)\n\t" \
    "s_sub_u32 s69, s70, s56\n\t" \
    "s_cmp_gt_i32 s69, 80\n\t" \
    "s_cbranch_scc1 rr_wl48_%=\n\t" \
    "s_max_i32 s69, s69, 0\n\t" \
    "s_branch rr_rback51_%=\n\t" \
    "s_nop 0\n\t" \
    "s_nop 0\n\t" \
    "s_nop 0\n\t" \
    "s_nop 0\n\t" \
    "s_nop 0\n\t" \
    "s_nop 0\n\t" \
    "s_nop 0\n\t" \
    "s_nop 0\n\t" \
    "s_nop 0\n\t" \
    "s_nop 0\n\t" \
    "s_nop 0\n\t" \
    "s_nop 0\n\t" \
    "s_nop 0\n\t" \
    "s_nop 0\n\t" \
    "s_nop 0\n\t" \
    "s_nop 0\n\t" \
    "s_nop 0\n\t" \
    "s_nop 0\n\t" \
    "s_nop 0\n\t" \
    "s_nop 0\n\t" \
    "rr_again9_%=:\n\t" \
    "global_load_dwordx4 v[128:131], v0, s[40:41] offset:0 sc1\n\t" \
    "s_lshr_b32 s96, s96, 2\n\t" \
    "global_load_dwordx4 v[132:135], v0, s[40:41] offset:1024 sc1\n\t" \
    "s_sub_u32 s30, s50, s96\n\t" \
    "global_load_dwordx4 v[136:139], v0, s[40:41] offset:2048 sc1\n\t" \
    "s_subb_u32 s31, s51, 0\n\t" \
    "global_load_dwordx4 v[140:143], v0, s[40:41] offset:3072 sc1\n\t" \
    "global_load_dwordx4 v[144:147], v1, s[40:41] offset:0 sc1\n\t" \
    "global_load_dwordx4 v[148:151], v1, s[40:41] offset:1024 sc1\n\t" \
    "global_load_dwordx4 v[152:155], v1, s[40:41] offset:2048 sc1\n\t" \
    "global_load_dwordx4 v[156:159], v1, s[40:41] offset:3072 sc1\n\t" \
    "s_setpc_b64 s[30:31]\n\t" \
    "s_nop 3\n\t" \
    "s_nop 3\n\t" \
    "s_nop 3\n\t" \
    "s_nop 3\n\t" \
    "s_nop 3\n\t" \
    "s_nop 3\n\t" \
    "s_nop 3\n\t" \
    "s_nop 3\n\t" \
    "s_nop 3\n\t" \
    "s_nop 3\n\t" \
    "s_nop 3\n\t" \
    "s_nop 3\n\t" \
    "s_nop 3\n\t" \
    "s_nop 3\n\t" \
    "s_nop 3\n\t" \
    "s_nop 3\n\t" \
    "s_nop 3\n\t" \
    "s_nop 3\n\t" \
    "s_nop 3\n\t" \
    "s_nop 3\n\t" \
    "s_nop 3\n\t" \
    "s_nop 3\n\t" \
    "s_nop 3\n\t" \
    "s_nop 3\n\t" \
    "rr_pgo52_%=:\n\t" \
    "global_load_dwordx4 v[48:51], v10, s[46:47] sc1\n\t" \
    "global_load_dwordx4 v[52:55], v11, s[46:47] sc1\n\t" \
    "global_load_dwordx4 v[56:59], v12, s[46:47] sc1\n\t" \
    "global_load_dwordx4 v[60:63], v13, s[46:47] sc1\n\t" \
    "global_load_dwordx4 v[64:67], v14, s[46:47] sc1\n\t" \
    "global_load_dwordx4 v[68:71], v15, s[46:47] sc1\n\t" \
    "global_load_dwordx4 v[72:75], v16, s[46:47] sc1\n\t" \
    "global_load_dwordx4 v[76:79], v17, s[46:47] sc1\n\t" \
    "global_load_dwordx4 v[80:83], v18, s[46:47] sc1\n\t" \
    "global_load_dwordx4 v[84:87], v19, s[46:47] sc1\n\t" \
    "global_load_dwordx4 v[88:91], v20, s[46:47] sc1\n\t" \
    "global_load_dwordx4 v[92:95], v21, s[46:47] sc1\n\t" \
    "global_load_dwordx4 v[96:99], v22, s[46:47] sc1\n\t" \
    "global_load_dwordx4 v[100:103], v23, s[46:47] sc1\n\t" \
    "global_load_dwordx4 v[104:107], v24, s[46:47] sc1\n\t" \
    "global_load_dwordx4 v[108:111], v25, s[46:47] sc1\n\t" \
    "global_load_dwordx4 v[112:115], v42, s[46:47] sc1\n\t" \
    "global_load_dwordx4 v[116:119], v43, s[46:47] sc1\n\t" \
    "global_load_dwordx4 v[120:123], v44, s[46:47] sc1\n\t" \
    "global_load_dwordx4 v[124:127], v45, s[46:47] sc1\n\t" \
    "s_cmp_lg_u32 s91, 0\n\t" \
    "s_cbranch_scc1 rr_nob12_%=\n\t" \
    "v_mov_b32 v164, s81\n\t" \
    "ds_write_b32 v9, v164\n\t" \
    "s_waitcnt lgkmcnt(0)\n\t" \
    "s_barrier\n\t" \
    "rr_nob12_%=:\n\t" \
    "s_waitcnt vmcnt(19)\n\t" \
    "v_smfmac_f32_16x16x64_bf16 v[160:163], v[48:51], a[0:7], v4\n\t" \
    "s_waitcnt vmcnt(18)\n\t" \
    "v_smfmac_f32_16x16x64_bf16 v[160:163], v[52:55], a[8:15], v4\n\t" \
    "s_waitcnt vmcnt(17)\n\t" \
    "v_smfmac_f32_16x16x64_bf16 v[160:163], v[56:59], a[16:23], v4\n\t" \
    "s_waitcnt vmcnt(16)\n\t" \
    "v_smfmac_f32_16x16x64_bf16 v[160:163], v[60:63], a[24:31], v4\n\t" \
    "s_waitcnt vmcnt(15)\n\t" \
    "v_smfmac_f32_16x16x64_bf16 v[160:163], v[64:67], a[32:39], v4\n\t" \
    "s_waitcnt vmcnt(14)\n\t" \
    "v_smfmac_f32_16x16x64_bf16 v[160:163], v[68:71], a[40:47], v4\n\t" \
    "s_waitcnt vmcnt(13)\n\t" \
    "v_smfmac_f32_16x16x64_bf16 v[160:163], v[72:75], a[48:55], v4\n\t" \
    "s_waitcnt vmcnt(12)\n\t" \
    "v_smfmac_f32_16x16x64_bf16 v[160:163], v[76:79], a[56:63], v4\n\t" \
    "s_waitcnt vmcnt(11)\n\t" \
    "v_smfmac_f32_16x16x64_bf16 v[160:163], v[80:83], a[64:71], v4\n\t" \
    "s_waitcnt vmcnt(10)\n\t" \
    "v_smfmac_f32_16x16x64_bf16 v[160:163], v[84:87], a[72:79], v4\n\t" \
    "s_waitcnt vmcnt(9)\n\t" \
    "v_smfmac_f32_16x16x64_bf16 v[160:163], v[88:91], a[80:87], v4\n\t" \
    "s_waitcnt vmcnt(8)\n\t" \
    "v_smfmac_f32_16x16x64_bf16 v[160:163], v[92:95], a[88:95], v4\n\t" \
    "s_waitcnt vmcnt(7)\n\t" \
    "v_smfmac_f32_16x16x64_bf16 v[160:163], v[96:99], a[96:103], v4\n\t" \
    "s_waitcnt vmcnt(6)\n\t" \
    "v_smfmac_f32_16x16x64_bf16 v[160:163], v[100:103], a[104:111], v4\n\t" \
    "s_waitcnt vmcnt(5)\n\t" \
    "v_smfmac_f32_16x16x64_bf16 v[160:163], v[104:107], a[112:119], v4\n\t" \
    "s_waitcnt vmcnt(4)\n\t" \
    "v_smfmac_f32_16x16x64_bf16 v[160:163], v[108:111], a[120:127], v4\n\t" \
    "s_waitcnt vmcnt(3)\n\t" \
    "v_smfmac_f32_16x16x64_bf16 v[160:163], v[112:115], v[128:135], v4\n\t" \
    "s_waitcnt vmcnt(2)\n\t" \
    "v_smfmac_f32_16x16x64_bf16 v[160:163], v[116:119], v[136:143], v4\n\t" \
    "s_waitcnt vmcnt(1)\n\t" \
    "v_smfmac_f32_16x16x64_bf16 v[160:163], v[120:123], v[144:151], v4\n\t" \
    "s_waitcnt vmcnt(0)\n\t" \
    "v_smfmac_f32_16x16x64_bf16 v[160:163], v[124:127], v[152:159], v4\n\t" \
    "s_nop 7\n\t" \
    "v_pk_add_f32 v[160:161], v[160:161], v[162:163]\n\t" \
    "v_cvt_pk_bf16_f32 v164, v160, v161\n\t" \
    "v_cmp_u_f32 vcc, v160, v161\n\t" \
    "s_cbranch_vccnz rr_bad10_%=\n\t" \
    "rr_cont11_%=:\n\t" \
    "v_mov_b32_dpp v165, v164 quad_perm:[1,0,3,2] row_mask:0xf bank_mask:0xf\n\t" \
    "v_perm_b32 v164, v165, v164, v5\n\t" \
    "global_store_dword v180, v164, s[48:49] sc1\n\t" \
    "global_store_dword v181, v164, s[48:49] sc1\n\t" \
    "global_store_dword v182, v164, s[48:49] sc1\n\t" \
    "global_store_dword v2, v164, s[48:49] sc1\n\t" \
    "s_cmp_lg_u32 s64, 0\n\t" \
    "s_cbranch_scc1 rr_g218_%=\n\t" \
    "s_cmp_eq_u32 s65, 62\n\t" \
    "s_cbranch_scc1 rr_end8_%=\n\t" \
    "s_add_u32 s65, s65, 2\n\t" \
    "s_branch rr_pf13_%=\n\t" \
    "rr_bad10_%=:\n\t" \
    "s_cmp_lg_u32 s64, 0\n\t" \
    "s_cbranch_scc1 rr_cont11_%=\n\t" \
    "s_waitcnt lgkmcnt(0)\n\t" \
    "v_max_u32 v165, v48, v49\n\t" \
    "v_max3_u32 v165, v165, v50, v51\n\t" \
    "v_max3_u32 v165, v165, v52, v53\n\t" \
    "v_max3_u32 v165, v165, v54, v55\n\t" \
    "v_max3_u32 v165, v165, v56, v57\n\t" \
    "v_max3_u32 v165, v165, v58, v59\n\t" \
    "v_max3_u32 v165, v165, v60, v61\n\t" \
    "v_max3_u32 v165, v165, v62, v63\n\t" \
    "v_max3_u32 v165, v165, v64, v65\n\t" \
    "v_max3_u32 v165, v165, v66, v67\n\t" \
    "v_max3_u32 v165, v165, v68, v69\n\t" \
    "v_max3_u32 v165, v165, v70, v71\n\t" \
    "v_max3_u32 v165, v165, v72, v73\n\t" \
    "v_max3_u32 v165, v165, v74, v75\n\t" \
    "v_max3_u32 v165, v165, v76, v77\n\t" \
    "v_max3_u32 v165, v165, v78, v79\n\t" \
    "v_max3_u32 v165, v165, v80, v81\n\t" \
    "v_max3_u32 v165, v165, v82, v83\n\t" \
    "v_max3_u32 v165, v165, v84, v85\n\t" \
    "v_max3_u32 v165, v165, v86, v87\n\t" \
    "v_max3_u32 v165, v165, v88, v89\n\t" \
    "v_max3_u32 v165, v165, v90, v91\n\t" \
    "v_max3_u32 v165, v165, v92, v93\n\t" \
    "v_max3_u32 v165, v165, v94, v95\n\t" \
    "v_max3_u32 v165, v165, v96, v97\n\t" \
    "v_max3_u32 v165, v165, v98, v99\n\t" \
    "v_max3_u32 v165, v165, v100, v101\n\t" \
    "v_max3_u32 v165, v165, v102, v103\n\t" \
    "v_max3_u32 v165, v165, v104, v105\n\t" \
    "v_max3_u32 v165, v165, v106, v107\n\t" \
    "v_max3_u32 v165, v165, v108, v109\n\t" \
    "v_max3_u32 v165, v165, v110, v111\n\t" \
    "v_max3_u32 v165, v165, v112, v113\n\t" \
    "v_max3_u32 v165, v165, v114, v115\n\t" \
    "v_max3_u32 v165, v165, v116, v117\n\t" \
    "v_max3_u32 v165, v165, v118, v119\n\t" \
    "v_max3_u32 v165, v165, v120, v121\n\t" \
    "v_max3_u32 v165, v165, v122, v123\n\t" \
    "v_max3_u32 v165, v165, v124, v125\n\t" \
    "v_max3_u32 v165, v165, v126, v127\n\t" \
    "v_max_u32 v166, v128, v129\n\t" \
    "v_max3_u32 v166, v166, v130, v131\n\t" \
    "v_max3_u32 v166, v166, v132, v133\n\t" \
    "v_max3_u32 v166, v166, v134, v135\n\t" \
    "v_max3_u32 v166, v166, v136, v137\n\t" \
    "v_max3_u32 v166, v166, v138, v139\n\t" \
    "v_max3_u32 v166, v166, v140, v141\n\t" \
    "v_max3_u32 v166, v166, v142, v143\n\t" \
    "v_max3_u32 v166, v166, v144, v145\n\t" \
    "v_max3_u32 v166, v166, v146, v147\n\t" \
    "v_max3_u32 v166, v166, v148, v149\n\t" \
    "v_max3_u32 v166, v166, v150, v151\n\t" \
    "v_max3_u32 v166, v166, v152, v153\n\t" \
    "v_max3_u32 v166, v166, v154, v155\n\t" \
    "v_max3_u32 v166, v166, v156, v157\n\t" \
    "v_max3_u32 v166, v166, v158, v159\n\t" \
    "v_max_u32 v165, v165, v166\n\t" \
    "v_cmp_eq_u32 vcc, -1, v165\n\t" \
    "s_cbranch_vccz rr_cont11_%=\n\t" \
    "s_mov_b32 s91, 1\n\t" \
    "s_mov_b32 s96, 0\n\t" \
    "v_mov_b32 v160, 0\n\t" \
    "v_mov_b32 v161, 0\n\t" \
    "v_mov_b32 v162, 0\n\t" \
    "v_mov_b32 v163, 0\n\t" \
    "s_cmp_eq_u32 s68, 0\n\t" \
    "s_cbranch_scc1 rr_F53_%=\n\t" \
    "s_sub_u32 s68, s68, 1\n\t" \
    "s_branch rr_again9_%=\n\t" \
    "rr_F53_%=:\n\t" \
    "v_mov_b32 v166, 5767424\n\t" \
    "v_mov_b32 v167, 3\n\t" \
    "global_store_dword v166, v167, s[36:37] sc1\n\t" \
    "s_waitcnt vmcnt(0)\n\t" \
    "s_branch rr_cont11_%=\n\t" \
    "rr_hlate47_%=:\n\t" \
    "s_cmp_lg_u32 s64, 0\n\t" \
    "s_cbranch_scc1 rr_hpd55_%=\n\t" \
    "global_load_dword v195, v189, s[52:53] offset:2048 sc1\n\t" \
    "s_waitcnt vmcnt(2)\n\t" \
    "v_mov_b32 v192, v193\n\t" \
    "v_cmp_eq_u32 vcc, -1, v193\n\t" \
    "s_cbranch_vccz rr_hok46_%=\n\t" \
    "global_load_dword v196, v190, s[52:53] offset:0 sc1\n\t" \
    "s_waitcnt vmcnt(2)\n\t" \
    "v_mov_b32 v192, v194\n\t" \
    "v_cmp_eq_u32 vcc, -1, v194\n\t" \
    "s_cbranch_vccz rr_hok46_%=\n\t" \
    "global_load_dword v197, v190, s[52:53] offset:2048 sc1\n\t" \
    "s_waitcnt vmcnt(2)\n\t" \
    "v_mov_b32 v192, v195\n\t" \
    "v_cmp_eq_u32 vcc, -1, v195\n\t" \
    "s_cbranch_vccz rr_hok46_%=\n\t" \
    "s_waitcnt vmcnt(1)\n\t" \
    "v_mov_b32 v192, v196\n\t" \
    "v_cmp_eq_u32 vcc, -1, v196\n\t" \
    "s_cbranch_vccz rr_hok46_%=\n\t" \
    "s_waitcnt vmcnt(0)\n\t" \
    "v_mov_b32 v192, v197\n\t" \
    "v_cmp_eq_u32 vcc, -1, v197\n\t" \
    "s_cbranch_vccz rr_hok46_%=\n\t" \
    "rr_hp54_%=:\n\t" \
    "global_load_dword v192, v191, s[52:53] offset:0 sc1\n\t" \
    "s_waitcnt vmcnt(0)\n\t" \
    "v_cmp_eq_u32 vcc, -1, v192\n\t" \
    "s_cbranch_vccz rr_hpd55_%=\n\t" \
    "s_cmp_eq_u32 s68, 0\n\t" \
    "s_cbranch_scc1 rr_F56_%=\n\t" \
    "s_sub_u32 s68, s68, 1\n\t" \
    "s_sleep 1\n\t" \
    "s_branch rr_hp54_%=\n\t" \
    "rr_F56_%=:\n\t" \
    "v_mov_b32 v166, 5767424\n\t" \
    "v_mov_b32 v167, 7\n\t" \
    "global_store_dword v166, v167, s[36:37] sc1\n\t" \
    "s_waitcnt vmcnt(0)\n\t" \
    "s_branch rr_hpd55_%=\n\t" \
    "rr_hpd55_%=:\n\t" \
    "v_and_b32 v192, -2, v192\n\t" \
    "s_waitcnt lgkmcnt(0)\n\t" \
    "s_mov_b32 s70, 116\n\t" \
    "rr_E57_%=:\n\t" \
    "s_memtime s[56:57]\n\t" \
    "s_memrealtime s[2:3]\n\t" \
    "s_nop 0\n\t" \
    "s_memrealtime s[4:5]\n\t" \
    "s_nop 0\n\t" \
    "s_memrealtime s[6:7]\n\t" \
    "s_nop 0\n\t" \
    "s_memrealtime s[8:9]\n\t" \
    "s_nop 0\n\t" \
    "s_memrealtime s[10:11]\n\t" \
    "s_nop 0\n\t" \
    "s_memrealtime s[12:13]\n\t" \
    "s_nop 0\n\t" \
    "s_memrealtime s[14:15]\n\t" \
    "s_nop 0\n\t" \
    "s_memrealtime s[16:17]\n\t" \
    "s_nop 0\n\t" \
    "s_memrealtime s[18:19]\n\t" \
    "s_nop 0\n\t" \
    "s_memrealtime s[20:21]\n\t" \
    "s_nop 0\n\t" \
    "s_memrealtime s[22:23]\n\t" \
    "s_nop 0\n\t" \
    "s_memrealtime s[24:25]\n\t" \
    "s_nop 0\n\t" \
    "s_memrealtime s[26:27]\n\t" \
    "s_nop 0\n\t" \
    "s_memrealtime s[28:29]\n\t" \
    "s_memtime s[54:55]\n\t" \
    "s_add_u32 s70, s70, 8\n\t" \
    "s_waitcnt lgkmcnt(0)\n\t" \
    "s_sub_u32 s69, s54, s56\n\t" \
    "s_cmp_gt_u32 s69, s70\n\t" \
    "s_cbranch_scc1 rr_E57_%=\n\t" \
    "s_add_u32 s2, s2, s4\n\t" \
    "s_add_u32 s6, s6, s8\n\t" \
    "s_add_u32 s10, s10, s12\n\t" \
    "s_add_u32 s14, s14, s16\n\t" \
    "s_add_u32 s18, s18, s20\n\t" \
    "s_add_u32 s22, s22, s24\n\t" \
    "s_add_u32 s2, s2, s6\n\t" \
    "s_add_u32 s10, s10, s14\n\t" \
    "s_add_u32 s18, s18, s22\n\t" \
    "s_add_u32 s2, s2, s26\n\t" \
    "s_add_u32 s10, s10, s18\n\t" \
    "s_add_u32 s2, s2, s10\n\t" \
    "s_mul_i32 s70, s28, 13\n\t" \
    "s_sub_u32 s70, s70, s2\n\t" \
    "s_min_u32 s70, s70, 52\n\t" \
    "s_mul_i32 s69, s69, s70\n\t" \
    "s_mul_hi_u32 s69, s69, 76695844\n\t" \
    "s_mov_b32 s75, s73\n\t" \
    "s_mov_b32 s76, s74\n\t" \
    "s_add_u32 s73, s56, s69\n\t" \
    "s_lshl_b32 s74, s28, 4\n\t" \
    "s_sub_u32 s71, s86, s74\n\t" \
    "s_branch rr_hok46_%=\n\t" \
    "rr_end8_%=:\n\t" \
    "s_endpgm\n\t" \
    "rr_last_%=:\n\t" \
    ""
#define ASM_H \
    "s_mov_b64 s[36:37], %0\n\t" \
    "s_mov_b64 s[42:43], %1\n\t" \
    "s_mov_b64 s[44:45], %2\n\t" \
    "s_mov_b32 s66, %3\n\t" \
    "s_mov_b32 s67, %4\n\t" \
    "s_mov_b64 s[50:51], %5\n\t" \
    "s_mov_b32 s82, %7\n\t" \
    "s_load_dwordx8 s[84:91], %6, 0\n\t" \
    "v_mbcnt_lo_u32_b32 v223, -1, 0\n\t" \
    "v_mbcnt_hi_u32_b32 v223, -1, v223\n\t" \
    "s_getpc_b64 s[46:47]\n\t" \
    "hh_pc_%=:\n\t" \
    "s_lshl_b32 s69, s66, 13\n\t" \
    "v_lshl_add_u32 v222, v223, 7, s69\n\t" \
    "v_min_u32 v222, hh_last_%=-hh_pc_%=-4, v222\n\t" \
    "global_load_dword v222, v222, s[46:47]\n\t" \
    "v_lshlrev_b32 v223, 2, v223\n\t" \
    "v_add_u32 v223, s82, v223\n\t" \
    "ds_read_b32 v0, v223 offset:0\n\t" \
    "ds_read_b32 v1, v223 offset:256\n\t" \
    "ds_read_b32 v2, v223 offset:512\n\t" \
    "ds_read_b32 v3, v223 offset:768\n\t" \
    "ds_read_b32 v4, v223 offset:1024\n\t" \
    "ds_read_b32 v5, v223 offset:1280\n\t" \
    "ds_read_b32 v6, v223 offset:1536\n\t" \
    "ds_read_b32 v7, v223 offset:1792\n\t" \
    "ds_read_b32 v8, v223 offset:2048\n\t" \
    "ds_read_b32 v9, v223 offset:2304\n\t" \
    "ds_read_b32 v10, v223 offset:2560\n\t" \
    "ds_read_b32 v11, v223 offset:2816\n\t" \
    "s_waitcnt lgkmcnt(0)\n\t" \
    "ds_read_b32 v12, v223 offset:3072\n\t" \
    "ds_read_b32 v13, v223 offset:3328\n\t" \
    "ds_read_b32 v14, v223 offset:3584\n\t" \
    "ds_read_b32 v15, v223 offset:3840\n\t" \
    "ds_read_b32 v16, v223 offset:4096\n\t" \
    "ds_read_b32 v17, v223 offset:4352\n\t" \
    "s_waitcnt lgkmcnt(0)\n\t" \
    "s_mov_b32 s68, 262144\n\t" \
    "s_mov_b32 s72, 0\n\t" \
    "s_mov_b32 s83, -1\n\t" \
    "s_mov_b32 s65, 0\n\t" \
    "s_mov_b32 s77, 3019898880\n\t" \
    "s_mov_b32 s78, 3054198966\n\t" \
    "s_waitcnt lgkmcnt(0)\n\t" \
    "s_mov_b32 s70, 116\n\t" \
    "hh_E1_%=:\n\t" \
    "s_memtime s[56:57]\n\t" \
    "s_memrealtime s[2:3]\n\t" \
    "s_nop 0\n\t" \
    "s_memrealtime s[4:5]\n\t" \
    "s_nop 0\n\t" \
    "s_memrealtime s[6:7]\n\t" \
    "s_nop 0\n\t" \
    "s_memrealtime s[8:9]\n\t" \
    "s_nop 0\n\t" \
    "s_memrealtime s[10:11]\n\t" \
    "s_nop 0\n\t" \
    "s_memrealtime s[12:13]\n\t" \
    "s_nop 0\n\t" \
    "s_memrealtime s[14:15]\n\t" \
    "s_nop 0\n\t" \
    "s_memrealtime s[16:17]\n\t" \
    "s_nop 0\n\t" \
    "s_memrealtime s[18:19]\n\t" \
    "s_nop 0\n\t" \
    "s_memrealtime s[20:21]\n\t" \
    "s_nop 0\n\t" \
    "s_memrealtime s[22:23]\n\t" \
    "s_nop 0\n\t" \
    "s_memrealtime s[24:25]\n\t" \
    "s_nop 0\n\t" \
    "s_memrealtime s[26:27]\n\t" \
    "s_nop 0\n\t" \
    "s_memrealtime s[28:29]\n\t" \
    "s_memtime s[54:55]\n\t" \
    "s_add_u32 s70, s70, 8\n\t" \
    "s_waitcnt lgkmcnt(0)\n\t" \
    "s_sub_u32 s69, s54, s56\n\t" \
    "s_cmp_gt_u32 s69, s70\n\t" \
    "s_cbranch_scc1 hh_E1_%=\n\t" \
    "s_add_u32 s2, s2, s4\n\t" \
    "s_add_u32 s6, s6, s8\n\t" \
    "s_add_u32 s10, s10, s12\n\t" \
    "s_add_u32 s14, s14, s16\n\t" \
    "s_add_u32 s18, s18, s20\n\t" \
    "s_add_u32 s22, s22, s24\n\t" \
    "s_add_u32 s2, s2, s6\n\t" \
    "s_add_u32 s10, s10, s14\n\t" \
    "s_add_u32 s18, s18, s22\n\t" \
    "s_add_u32 s2, s2, s26\n\t" \
    "s_add_u32 s10, s10, s18\n\t" \
    "s_add_u32 s2, s2, s10\n\t" \
    "s_mul_i32 s70, s28, 13\n\t" \
    "s_sub_u32 s70, s70, s2\n\t" \
    "s_min_u32 s70, s70, 52\n\t" \
    "s_mul_i32 s69, s69, s70\n\t" \
    "s_mul_hi_u32 s69, s69, 76695844\n\t" \
    "s_mov_b32 s75, s73\n\t" \
    "s_mov_b32 s76, s74\n\t" \
    "s_add_u32 s73, s56, s69\n\t" \
    "s_lshl_b32 s74, s28, 4\n\t" \
    "v_mbcnt_lo_u32_b32 v30, -1, 0\n\t" \
    "v_mbcnt_hi_u32_b32 v30, -1, v30\n\t" \
    "v_lshlrev_b32 v30, 6, v30\n\t" \
    "v_add_u32 v30, 6225920, v30\n\t" \
    "v_mov_b32 v31, 6234112\n\t" \
    "global_load_dword v31, v31, s[36:37] sc1\n\t" \
    "global_load_dwordx2 v[32:33], v30, s[36:37] sc1\n\t" \
    "global_load_dword v34, v30, s[36:37] offset:2048 sc1\n\t" \
    "s_waitcnt vmcnt(0)\n\t" \
    "hh_C2_%=:\n\t" \
    "v_cmp_ne_u32 s[54:55], v32, v34\n\t" \
    "v_sub_u32 v32, v32, v31\n\t" \
    "v_cmp_ne_u32 vcc, 1, v32\n\t" \
    "s_nop 1\n\t" \
    "s_or_b64 vcc, vcc, s[54:55]\n\t" \
    "s_cbranch_scc0 hh_Cok3_%=\n\t" \
    "s_cmp_eq_u32 s68, 0\n\t" \
    "s_cbranch_scc1 hh_F5_%=\n\t" \
    "s_sub_u32 s68, s68, 1\n\t" \
    "s_sleep 2\n\t" \
    "s_branch hh_Cag4_%=\n\t" \
    "hh_F5_%=:\n\t" \
    "v_mov_b32 v34, 5767424\n\t" \
    "v_mov_b32 v35, 4\n\t" \
    "global_store_dword v34, v35, s[36:37] sc1\n\t" \
    "s_waitcnt vmcnt(0)\n\t" \
    "s_branch hh_Cok3_%=\n\t" \
    "hh_Cag4_%=:\n\t" \
    "global_load_dwordx2 v[32:33], v30, s[36:37] sc1\n\t" \
    "global_load_dword v34, v30, s[36:37] offset:2048 sc1\n\t" \
    "s_waitcnt vmcnt(0)\n\t" \
    "s_branch hh_C2_%=\n\t" \
    "hh_Cok3_%=:\n\t" \
    "v_mov_b32_dpp v30, v33 quad_perm:[1,0,3,2] row_mask:0xf bank_mask:0xf\n\t" \
    "v_cmp_lt_u32 s[44:45], v30, v33\n\t" \
    "s_nop 1\n\t" \
    "s_bitcmp1_b32 s44, 0\n\t" \
    "s_cselect_b32 s62, 8192, 0\n\t" \
    "s_bitcmp1_b32 s44, 2\n\t" \
    "s_cselect_b32 s63, 8192, 0\n\t" \
    "s_or_b32 s63, s63, 0x200000\n\t" \
    "v_min_u32 v30, v30, v33\n\t" \
    "v_cmp_lt_u32 vcc, 660, v30\n\t" \
    "s_nop 1\n\t" \
    "s_cmp_lg_u64 vcc, 0\n\t" \
    "s_cselect_b32 s69, 1, 0\n\t" \
    "s_mul_i32 s70, s69, 160\n\t" \
    "s_add_u32 s86, s86, s70\n\t" \
    "s_mul_i32 s70, s69, 536\n\t" \
    "s_add_u32 s87, s87, s70\n\t" \
    "s_mul_i32 s70, s69, 256\n\t" \
    "s_add_u32 s88, s88, s70\n\t" \
    "s_waitcnt lgkmcnt(0)\n\t" \
    "s_mov_b32 s70, 116\n\t" \
    "hh_E6_%=:\n\t" \
    "s_memtime s[56:57]\n\t" \
    "s_memrealtime s[2:3]\n\t" \
    "s_nop 0\n\t" \
    "s_memrealtime s[4:5]\n\t" \
    "s_nop 0\n\t" \
    "s_memrealtime s[6:7]\n\t" \
    "s_nop 0\n\t" \
    "s_memrealtime s[8:9]\n\t" \
    "s_nop 0\n\t" \
    "s_memrealtime s[10:11]\n\t" \
    "s_nop 0\n\t" \
    "s_memrealtime s[12:13]\n\t" \
    "s_nop 0\n\t" \
    "s_memrealtime s[14:15]\n\t" \
    "s_nop 0\n\t" \
    "s_memrealtime s[16:17]\n\t" \
    "s_nop 0\n\t" \
    "s_memrealtime s[18:19]\n\t" \
    "s_nop 0\n\t" \
    "s_memrealtime s[20:21]\n\t" \
    "s_nop 0\n\t" \
    "s_memrealtime s[22:23]\n\t" \
    "s_nop 0\n\t" \
    "s_memrealtime s[24:25]\n\t" \
    "s_nop 0\n\t" \
    "s_memrealtime s[26:27]\n\t" \
    "s_nop 0\n\t" \
    "s_memrealtime s[28:29]\n\t" \
    "s_memtime s[54:55]\n\t" \
    "s_add_u32 s70, s70, 8\n\t" \
    "s_waitcnt lgkmcnt(0)\n\t" \
    "s_sub_u32 s69, s54, s56\n\t" \
    "s_cmp_gt_u32 s69, s70\n\t" \
    "s_cbranch_scc1 hh_E6_%=\n\t" \
    "s_add_u32 s2, s2, s4\n\t" \
    "s_add_u32 s6, s6, s8\n\t" \
    "s_add_u32 s10, s10, s12\n\t" \
    "s_add_u32 s14, s14, s16\n\t" \
    "s_add_u32 s18, s18, s20\n\t" \
    "s_add_u32 s22, s22, s24\n\t" \
    "s_add_u32 s2, s2, s6\n\t" \
    "s_add_u32 s10, s10, s14\n\t" \
    "s_add_u32 s18, s18, s22\n\t" \
    "s_add_u32 s2, s2, s26\n\t" \
    "s_add_u32 s10, s10, s18\n\t" \
    "s_add_u32 s2, s2, s10\n\t" \
    "s_mul_i32 s70, s28, 13\n\t" \
    "s_sub_u32 s70, s70, s2\n\t" \
    "s_min_u32 s70, s70, 52\n\t" \
    "s_mul_i32 s69, s69, s70\n\t" \
    "s_mul_hi_u32 s69, s69, 76695844\n\t" \
    "s_mov_b32 s75, s73\n\t" \
    "s_mov_b32 s76, s74\n\t" \
    "s_add_u32 s73, s56, s69\n\t" \
    "s_lshl_b32 s74, s28, 4\n\t" \
    "s_sub_u32 s69, s73, s75\n\t" \
    "s_sub_u32 s70, s74, s76\n\t" \
    "s_sub_u32 s71, s70, 1600\n\t" \
    "s_cmp_lt_u32 s71, 400000\n\t" \
    "s_cbranch_scc0 hh_K7_%=\n\t" \
    "v_cvt_f32_u32 v30, s69\n\t" \
    "v_cvt_f32_u32 v31, s70\n\t" \
    "v_rcp_f32 v32, v30\n\t" \
    "v_rcp_f32 v33, v31\n\t" \
    "v_mul_f32 v32, v32, v31\n\t" \
    "v_mul_f32 v33, v33, v30\n\t" \
    "v_mul_f32 v32, 0x4f800000, v32\n\t" \
    "v_mul_f32 v33, 0x4f000000, v33\n\t" \
    "v_cvt_u32_f32 v32, v32\n\t" \
    "v_cvt_u32_f32 v33, v33\n\t" \
    "v_readfirstlane_b32 s69, v32\n\t" \
    "v_readfirstlane_b32 s70, v33\n\t" \
    "s_nop 1\n\t" \
    "s_mov_b32 s78, s69\n\t" \
    "s_mov_b32 s77, s70\n\t" \
    "hh_K7_%=:\n\t" \
    "s_sub_u32 s80, s74, 32000\n\t" \
    "v_mov_b32 v38, -1\n\t" \
    "v_mov_b32 v39, -1\n\t" \
    "s_mov_b32 m0, 155648\n\t" \
    "s_mul_i32 s96, s67, 16\n\t" \
    "s_add_u32 s96, s96, 64\n\t" \
    "hh_loop8_%=:\n\t" \
    "s_and_b32 s69, s65, 7\n\t" \
    "s_lshl_b32 s69, s69, 18\n\t" \
    "s_add_u32 s69, s69, s63\n\t" \
    "s_add_u32 s52, s36, s69\n\t" \
    "s_addc_u32 s53, s37, 0\n\t" \
    "s_add_u32 s71, s80, 608\n\t" \
    "s_sub_u32 s70, s71, s74\n\t" \
    "s_max_i32 s70, s70, 0\n\t" \
    "s_min_i32 s70, s70, 16000\n\t" \
    "s_lshl_b32 s70, s70, 1\n\t" \
    "s_mul_hi_u32 s70, s70, s77\n\t" \
    "s_add_u32 s70, s70, s73\n\t" \
    "hh_W11_%=:\n\t" \
    "s_memtime s[56:57]\n\t" \
    "s_waitcnt lgkmcnt(0)\n\t" \
    "s_sub_u32 s69, s70, s56\n\t" \
    "s_cmp_gt_i32 s69, 24\n\t" \
    "s_cbranch_scc1 hh_W11_%=\n\t" \
    "global_load_dword v30, v1, s[52:53] sc1\n\t" \
    "s_waitcnt lgkmcnt(0)\n\t" \
    "s_mov_b32 s70, 116\n\t" \
    "hh_E15_%=:\n\t" \
    "s_memtime s[56:57]\n\t" \
    "s_memrealtime s[2:3]\n\t" \
    "s_nop 0\n\t" \
    "s_memrealtime s[4:5]\n\t" \
    "s_nop 0\n\t" \
    "s_memrealtime s[6:7]\n\t" \
    "s_nop 0\n\t" \
    "s_memrealtime s[8:9]\n\t" \
    "s_nop 0\n\t" \
    "s_memrealtime s[10:11]\n\t" \
    "s_nop 0\n\t" \
    "s_memrealtime s[12:13]\n\t" \
    "s_nop 0\n\t" \
    "s_memrealtime s[14:15]\n\t" \
    "s_nop 0\n\t" \
    "s_memrealtime s[16:17]\n\t" \
    "s_nop 0\n\t" \
    "s_memrealtime s[18:19]\n\t" \
    "s_nop 0\n\t" \
    "s_memrealtime s[20:21]\n\t" \
    "s_nop 0\n\t" \
    "s_memrealtime s[22:23]\n\t" \
    "s_nop 0\n\t" \
    "s_memrealtime s[24:25]\n\t" \
    "s_nop 0\n\t" \
    "s_memrealtime s[26:27]\n\t" \
    "s_nop 0\n\t" \
    "s_memrealtime s[28:29]\n\t" \
    "s_memtime s[54:55]\n\t" \
    "s_add_u32 s70, s70, 8\n\t" \
    "s_waitcnt lgkmcnt(0)\n\t" \
    "s_sub_u32 s69, s54, s56\n\t" \
    "s_cmp_gt_u32 s69, s70\n\t" \
    "s_cbranch_scc1 hh_E15_%=\n\t" \
    "s_add_u32 s2, s2, s4\n\t" \
    "s_add_u32 s6, s6, s8\n\t" \
    "s_add_u32 s10, s10, s12\n\t" \
    "s_add_u32 s14, s14, s16\n\t" \
    "s_add_u32 s18, s18, s20\n\t" \
    "s_add_u32 s22, s22, s24\n\t" \
    "s_add_u32 s2, s2, s6\n\t" \
    "s_add_u32 s10, s10, s14\n\t" \
    "s_add_u32 s18, s18, s22\n\t" \
    "s_add_u32 s2, s2, s26\n\t" \
    "s_add_u32 s10, s10, s18\n\t" \
    "s_add_u32 s2, s2, s10\n\t" \
    "s_mul_i32 s70, s28, 13\n\t" \
    "s_sub_u32 s70, s70, s2\n\t" \
    "s_min_u32 s70, s70, 52\n\t" \
    "s_mul_i32 s69, s69, s70\n\t" \
    "s_mul_hi_u32 s69, s69, 76695844\n\t" \
    "s_mov_b32 s75, s73\n\t" \
    "s_mov_b32 s76, s74\n\t" \
    "s_add_u32 s73, s56, s69\n\t" \
    "s_lshl_b32 s74, s28, 4\n\t" \
    "s_sub_u32 s69, s73, s75\n\t" \
    "s_sub_u32 s70, s74, s76\n\t" \
    "s_sub_u32 s71, s70, 1600\n\t" \
    "s_cmp_lt_u32 s71, 400000\n\t" \
    "s_cbranch_scc0 hh_K16_%=\n\t" \
    "v_cvt_f32_u32 v34, s69\n\t" \
    "v_cvt_f32_u32 v35, s70\n\t" \
    "v_rcp_f32 v36, v34\n\t" \
    "v_rcp_f32 v37, v35\n\t" \
    "v_mul_f32 v36, v36, v35\n\t" \
    "v_mul_f32 v37, v37, v34\n\t" \
    "v_mul_f32 v36, 0x4f800000, v36\n\t" \
    "v_mul_f32 v37, 0x4f000000, v37\n\t" \
    "v_cvt_u32_f32 v36, v36\n\t" \
    "v_cvt_u32_f32 v37, v37\n\t" \
    "v_readfirstlane_b32 s69, v36\n\t" \
    "v_readfirstlane_b32 s70, v37\n\t" \
    "s_nop 1\n\t" \
    "s_sub_u32 s69, s69, s78\n\t" \
    "s_ashr_i32 s69, s69, 2\n\t" \
    "s_add_u32 s78, s78, s69\n\t" \
    "s_sub_u32 s70, s70, s77\n\t" \
    "s_ashr_i32 s70, s70, 2\n\t" \
    "s_add_u32 s77, s77, s70\n\t" \
    "hh_K16_%=:\n\t" \
    "s_waitcnt vmcnt(0)\n\t" \
    "v_cmp_eq_u32 vcc, -1, v30\n\t" \
    "s_cbranch_vccz hh_Hok13_%=\n\t" \
    "hh_H12_%=:\n\t" \
    "global_load_dword v30, v1, s[52:53] sc1\n\t" \
    "s_waitcnt vmcnt(0)\n\t" \
    "v_cmp_eq_u32 vcc, -1, v30\n\t" \
    "s_cbranch_vccz hh_Hc14_%=\n\t" \
    "s_cmp_eq_u32 s68, 0\n\t" \
    "s_cbranch_scc1 hh_F17_%=\n\t" \
    "s_sub_u32 s68, s68, 1\n\t" \
    "s_sleep 2\n\t" \
    "s_branch hh_H12_%=\n\t" \
    "hh_F17_%=:\n\t" \
    "v_mov_b32 v31, 5767424\n\t" \
    "v_mov_b32 v32, 14\n\t" \
    "global_store_dword v31, v32, s[36:37] sc1\n\t" \
    "s_waitcnt vmcnt(0)\n\t" \
    "s_branch hh_Hc14_%=\n\t" \
    "hh_Hc14_%=:\n\t" \
    "s_waitcnt lgkmcnt(0)\n\t" \
    "s_mov_b32 s70, 116\n\t" \
    "hh_E18_%=:\n\t" \
    "s_memtime s[56:57]\n\t" \
    "s_memrealtime s[2:3]\n\t" \
    "s_nop 0\n\t" \
    "s_memrealtime s[4:5]\n\t" \
    "s_nop 0\n\t" \
    "s_memrealtime s[6:7]\n\t" \
    "s_nop 0\n\t" \
    "s_memrealtime s[8:9]\n\t" \
    "s_nop 0\n\t" \
    "s_memrealtime s[10:11]\n\t" \
    "s_nop 0\n\t" \
    "s_memrealtime s[12:13]\n\t" \
    "s_nop 0\n\t" \
    "s_memrealtime s[14:15]\n\t" \
    "s_nop 0\n\t" \
    "s_memrealtime s[16:17]\n\t" \
    "s_nop 0\n\t" \
    "s_memrealtime s[18:19]\n\t" \
    "s_nop 0\n\t" \
    "s_memrealtime s[20:21]\n\t" \
    "s_nop 0\n\t" \
    "s_memrealtime s[22:23]\n\t" \
    "s_nop 0\n\t" \
    "s_memrealtime s[24:25]\n\t" \
    "s_nop 0\n\t" \
    "s_memrealtime s[26:27]\n\t" \
    "s_nop 0\n\t" \
    "s_memrealtime s[28:29]\n\t" \
    "s_memtime s[54:55]\n\t" \
    "s_add_u32 s70, s70, 8\n\t" \
    "s_waitcnt lgkmcnt(0)\n\t" \
    "s_sub_u32 s69, s54, s56\n\t" \
    "s_cmp_gt_u32 s69, s70\n\t" \
    "s_cbranch_scc1 hh_E18_%=\n\t" \
    "s_add_u32 s2, s2, s4\n\t" \
    "s_add_u32 s6, s6, s8\n\t" \
    "s_add_u32 s10, s10, s12\n\t" \
    "s_add_u32 s14, s14, s16\n\t" \
    "s_add_u32 s18, s18, s20\n\t" \
    "s_add_u32 s22, s22, s24\n\t" \
    "s_add_u32 s2, s2, s6\n\t" \
    "s_add_u32 s10, s10, s14\n\t" \
    "s_add_u32 s18, s18, s22\n\t" \
    "s_add_u32 s2, s2, s26\n\t" \
    "s_add_u32 s10, s10, s18\n\t" \
    "s_add_u32 s2, s2, s10\n\t" \
    "s_mul_i32 s70, s28, 13\n\t" \
    "s_sub_u32 s70, s70, s2\n\t" \
    "s_min_u32 s70, s70, 52\n\t" \
    "s_mul_i32 s69, s69, s70\n\t" \
    "s_mul_hi_u32 s69, s69, 76695844\n\t" \
    "s_mov_b32 s75, s73\n\t" \
    "s_mov_b32 s76, s74\n\t" \
    "s_add_u32 s73, s56, s69\n\t" \
    "s_lshl_b32 s74, s28, 4\n\t" \
    "s_sub_u32 s69, s73, s75\n\t" \
    "s_sub_u32 s70, s74, s76\n\t" \
    "s_sub_u32 s71, s70, 1600\n\t" \
    "s_cmp_lt_u32 s71, 400000\n\t" \
    "s_cbranch_scc0 hh_K19_%=\n\t" \
    "v_cvt_f32_u32 v34, s69\n\t" \
    "v_cvt_f32_u32 v35, s70\n\t" \
    "v_rcp_f32 v36, v34\n\t" \
    "v_rcp_f32 v37, v35\n\t" \
    "v_mul_f32 v36, v36, v35\n\t" \
    "v_mul_f32 v37, v37, v34\n\t" \
    "v_mul_f32 v36, 0x4f800000, v36\n\t" \
    "v_mul_f32 v37, 0x4f000000, v37\n\t" \
    "v_cvt_u32_f32 v36, v36\n\t" \
    "v_cvt_u32_f32 v37, v37\n\t" \
    "v_readfirstlane_b32 s69, v36\n\t" \
    "v_readfirstlane_b32 s70, v37\n\t" \
    "s_nop 1\n\t" \
    "s_sub_u32 s69, s69, s78\n\t" \
    "s_ashr_i32 s69, s69, 2\n\t" \
    "s_add_u32 s78, s78, s69\n\t" \
    "s_sub_u32 s70, s70, s77\n\t" \
    "s_ashr_i32 s70, s70, 2\n\t" \
    "s_add_u32 s77, s77, s70\n\t" \
    "hh_K19_%=:\n\t" \
    "hh_Hok13_%=:\n\t" \
    "v_max_u32_dpp v30, v30, v30 row_ror:1 row_mask:0xf bank_mask:0xf\n\t" \
    "s_nop 1\n\t" \
    "v_max_u32_dpp v30, v30, v30 row_ror:2 row_mask:0xf bank_mask:0xf\n\t" \
    "s_nop 1\n\t" \
    "v_max_u32_dpp v30, v30, v30 row_ror:4 row_mask:0xf bank_mask:0xf\n\t" \
    "s_nop 1\n\t" \
    "v_max_u32_dpp v30, v30, v30 row_ror:8 row_mask:0xf bank_mask:0xf\n\t" \
    "s_nop 0\n\t" \
    "v_readlane_b32 s69, v30, 0\n\t" \
    "v_readlane_b32 s71, v30, 16\n\t" \
    "s_nop 1\n\t" \
    "s_max_u32 s81, s69, s71\n\t" \
    "s_mul_hi_u32 s69, s85, s78\n\t" \
    "s_add_u32 s79, s81, s69\n\t" \
    "s_add_u32 s79, s79, s87\n\t" \
    "s_cmp_ge_u32 s65, 36\n\t" \
    "s_cselect_b32 s69, 32, 0\n\t" \
    "s_sub_u32 s79, s79, s69\n\t" \
    "s_cmp_lt_u32 s65, 9\n\t" \
    "s_cselect_b32 s69, 64, 0\n\t" \
    "s_add_u32 s79, s79, s69\n\t" \
    "s_add_u32 s80, s79, s88\n\t" \
    "s_add_u32 s65, s65, 1\n\t" \
    "s_cmp_gt_u32 s65, 62\n\t" \
    "s_cbranch_scc1 hh_end10_%=\n\t" \
    "s_cmp_le_u32 s65, 6\n\t" \
    "s_cbranch_scc1 hh_loop8_%=\n\t" \
    "v_lshrrev_b32 v20, 15, v11\n\t" \
    "v_add3_u32 v20, v20, s65, 1\n\t" \
    "v_min_u32 v20, 63, v20\n\t" \
    "v_mul_u32_u24 v20, v20, v11\n\t" \
    "v_xor_b32 v20, v20, v4\n\t" \
    "v_lshrrev_b32 v21, 15, v12\n\t" \
    "v_add3_u32 v21, v21, s65, 1\n\t" \
    "v_min_u32 v21, 63, v21\n\t" \
    "v_mul_u32_u24 v21, v21, v12\n\t" \
    "v_xor_b32 v21, v21, v5\n\t" \
    "v_lshrrev_b32 v22, 15, v13\n\t" \
    "v_add3_u32 v22, v22, s65, 1\n\t" \
    "v_min_u32 v22, 63, v22\n\t" \
    "v_mul_u32_u24 v22, v22, v13\n\t" \
    "v_xor_b32 v22, v22, v6\n\t" \
    "v_lshrrev_b32 v23, 15, v14\n\t" \
    "v_add3_u32 v23, v23, s65, 1\n\t" \
    "v_min_u32 v23, 63, v23\n\t" \
    "v_mul_u32_u24 v23, v23, v14\n\t" \
    "v_xor_b32 v23, v23, v7\n\t" \
    "v_lshrrev_b32 v24, 15, v15\n\t" \
    "v_add3_u32 v24, v24, s65, 1\n\t" \
    "v_min_u32 v24, 63, v24\n\t" \
    "v_mul_u32_u24 v24, v24, v15\n\t" \
    "v_xor_b32 v24, v24, v8\n\t" \
    "v_lshrrev_b32 v25, 15, v16\n\t" \
    "v_add3_u32 v25, v25, s65, 1\n\t" \
    "v_min_u32 v25, 63, v25\n\t" \
    "v_mul_u32_u24 v25, v25, v16\n\t" \
    "v_xor_b32 v25, v25, v9\n\t" \
    "v_lshrrev_b32 v26, 15, v17\n\t" \
    "v_add3_u32 v26, v26, s65, 1\n\t" \
    "v_min_u32 v26, 63, v26\n\t" \
    "v_mul_u32_u24 v26, v26, v17\n\t" \
    "v_xor_b32 v26, v26, v10\n\t" \
    "s_add_u32 s71, s79, s96\n\t" \
    "s_sub_u32 s70, s71, s74\n\t" \
    "s_max_i32 s70, s70, 0\n\t" \
    "s_min_i32 s70, s70, 16000\n\t" \
    "s_lshl_b32 s70, s70, 1\n\t" \
    "s_mul_hi_u32 s70, s70, s77\n\t" \
    "s_add_u32 s70, s70, s73\n\t" \
    "s_getpc_b64 s[30:31]\n\t" \
    "hh_Wp22_%=:\n\t" \
    "s_add_u32 s30, s30, hh_We23_%=-hh_Wp22_%=\n\t" \
    "s_addc_u32 s31, s31, 0\n\t" \
    "s_sub_u32 s70, s70, 96\n\t" \
    "hh_W21_%=:\n\t" \
    "s_memtime s[56:57]\n\t" \
    "s_waitcnt lgkmcnt(0)\n\t" \
    "s_sub_u32 s69, s70, s56\n\t" \
    "s_cmp_gt_i32 s69, 80\n\t" \
    "s_cbranch_scc1 hh_W21_%=\n\t" \
    "s_max_i32 s69, s69, 0\n\t" \
    "s_and_b32 s69, s69, -4\n\t" \
    "s_sub_u32 s30, s30, s69\n\t" \
    "s_subb_u32 s31, s31, 0\n\t" \
    "s_setpc_b64 s[30:31]\n\t" \
    "s_nop 0\n\t" \
    "s_nop 0\n\t" \
    "s_nop 0\n\t" \
    "s_nop 0\n\t" \
    "s_nop 0\n\t" \
    "s_nop 0\n\t" \
    "s_nop 0\n\t" \
    "s_nop 0\n\t" \
    "s_nop 0\n\t" \
    "s_nop 0\n\t" \
    "s_nop 0\n\t" \
    "s_nop 0\n\t" \
    "s_nop 0\n\t" \
    "s_nop 0\n\t" \
    "s_nop 0\n\t" \
    "s_nop 0\n\t" \
    "s_nop 0\n\t" \
    "s_nop 0\n\t" \
    "s_nop 0\n\t" \
    "s_nop 0\n\t" \
    "hh_We23_%=:\n\t" \
    "global_load_lds_dword v26, s[36:37] sc1\n\t" \
    "s_sleep 2\n\t" \
    "s_nop 5\n\t" \
    "global_load_lds_dword v25, s[36:37] sc1\n\t" \
    "s_sleep 2\n\t" \
    "s_nop 5\n\t" \
    "global_load_lds_dword v24, s[36:37] sc1\n\t" \
    "s_sleep 2\n\t" \
    "s_nop 5\n\t" \
    "global_load_lds_dword v23, s[36:37] sc1\n\t" \
    "s_sleep 2\n\t" \
    "s_nop 5\n\t" \
    "global_load_lds_dword v22, s[36:37] sc1\n\t" \
    "s_sleep 2\n\t" \
    "s_nop 5\n\t" \
    "global_load_lds_dword v21, s[36:37] sc1\n\t" \
    "s_sleep 2\n\t" \
    "s_nop 5\n\t" \
    "global_load_lds_dword v20, s[36:37] sc1\n\t" \
    "s_branch hh_loop8_%=\n\t" \
    "hh_end10_%=:\n\t" \
    "s_waitcnt vmcnt(0)\n\t" \
    "s_endpgm\n\t" \
    "hh_last_%=:\n\t" \
    ""
#define ASM_GX \
    "s_load_dwordx2 s[42:43], %3, 48\n\t" \
    "s_load_dwordx2 s[44:45], %3, 40\n\t" \
    "s_load_dwordx2 s[36:37], %3, 0x58\n\t" \
    "s_load_dwordx2 s[50:51], %3, 0x48\n\t" \
    "s_mov_b32 s66, %4\n\t" \
    "s_mov_b32 s67, %5\n\t" \
    "s_mov_b32 s68, 262144\n\t" \
    "s_mov_b32 s72, 0\n\t" \
    "s_mov_b32 s83, -1\n\t" \
    "s_mov_b32 s65, 0\n\t" \
    "s_lshl_b32 s82, s66, 10\n\t" \
    "s_waitcnt lgkmcnt(0)\n\t" \
    "s_mul_i32 s89, s66, 9216\n\t" \
    "s_mov_b32 s90, 0\n\t" \
    "s_add_u32 s46, s42, 2097152\n\t" \
    "s_addc_u32 s47, s43, 0\n\t" \
    "s_add_u32 m0, s82, 65536\n\t" \
    "s_nop 0\n\t" \
    "global_load_lds_dwordx4 v200, s[46:47]\n\t" \
    "s_add_u32 s46, s42, 2228224\n\t" \
    "s_addc_u32 s47, s43, 0\n\t" \
    "s_add_u32 m0, s82, 69632\n\t" \
    "s_nop 0\n\t" \
    "global_load_lds_dwordx4 v200, s[46:47]\n\t" \
    "s_add_u32 s46, s42, 2359296\n\t" \
    "s_addc_u32 s47, s43, 0\n\t" \
    "s_add_u32 m0, s82, 73728\n\t" \
    "s_nop 0\n\t" \
    "global_load_lds_dwordx4 v200, s[46:47]\n\t" \
    "s_add_u32 s46, s42, 2490368\n\t" \
    "s_addc_u32 s47, s43, 0\n\t" \
    "s_add_u32 m0, s82, 77824\n\t" \
    "s_nop 0\n\t" \
    "global_load_lds_dwordx4 v200, s[46:47]\n\t" \
    "s_add_u32 s46, s42, 2621440\n\t" \
    "s_addc_u32 s47, s43, 0\n\t" \
    "s_add_u32 m0, s82, 81920\n\t" \
    "s_nop 0\n\t" \
    "global_load_lds_dwordx4 v200, s[46:47]\n\t" \
    "s_add_u32 s46, s42, 2752512\n\t" \
    "s_addc_u32 s47, s43, 0\n\t" \
    "s_add_u32 m0, s82, 86016\n\t" \
    "s_nop 0\n\t" \
    "global_load_lds_dwordx4 v200, s[46:47]\n\t" \
    "s_add_u32 s46, s42, 2883584\n\t" \
    "s_addc_u32 s47, s43, 0\n\t" \
    "s_add_u32 m0, s82, 90112\n\t" \
    "s_nop 0\n\t" \
    "global_load_lds_dwordx4 v200, s[46:47]\n\t" \
    "s_add_u32 s46, s42, 3014656\n\t" \
    "s_addc_u32 s47, s43, 0\n\t" \
    "s_add_u32 m0, s82, 94208\n\t" \
    "s_nop 0\n\t" \
    "global_load_lds_dwordx4 v200, s[46:47]\n\t" \
    "s_add_u32 s46, s42, 3145728\n\t" \
    "s_addc_u32 s47, s43, 0\n\t" \
    "s_add_u32 m0, s82, 98304\n\t" \
    "s_nop 0\n\t" \
    "global_load_lds_dwordx4 v200, s[46:47]\n\t" \
    "s_add_u32 s46, s42, 3276800\n\t" \
    "s_addc_u32 s47, s43, 0\n\t" \
    "s_add_u32 m0, s82, 102400\n\t" \
    "s_nop 0\n\t" \
    "global_load_lds_dwordx4 v200, s[46:47]\n\t" \
    "s_add_u32 s46, s42, 3407872\n\t" \
    "s_addc_u32 s47, s43, 0\n\t" \
    "s_add_u32 m0, s82, 106496\n\t" \
    "s_nop 0\n\t" \
    "global_load_lds_dwordx4 v200, s[46:47]\n\t" \
    "s_add_u32 s46, s42, 3538944\n\t" \
    "s_addc_u32 s47, s43, 0\n\t" \
    "s_add_u32 m0, s82, 110592\n\t" \
    "s_nop 0\n\t" \
    "global_load_lds_dwordx4 v200, s[46:47]\n\t" \
    "s_add_u32 s46, s42, 3670016\n\t" \
    "s_addc_u32 s47, s43, 0\n\t" \
    "s_add_u32 m0, s82, 114688\n\t" \
    "s_nop 0\n\t" \
    "global_load_lds_dwordx4 v200, s[46:47]\n\t" \
    "s_add_u32 s46, s42, 3801088\n\t" \
    "s_addc_u32 s47, s43, 0\n\t" \
    "s_add_u32 m0, s82, 118784\n\t" \
    "s_nop 0\n\t" \
    "global_load_lds_dwordx4 v200, s[46:47]\n\t" \
    "s_add_u32 s46, s42, 3932160\n\t" \
    "s_addc_u32 s47, s43, 0\n\t" \
    "s_add_u32 m0, s82, 122880\n\t" \
    "s_nop 0\n\t" \
    "global_load_lds_dwordx4 v200, s[46:47]\n\t" \
    "s_add_u32 s46, s42, 4063232\n\t" \
    "s_addc_u32 s47, s43, 0\n\t" \
    "s_add_u32 m0, s82, 126976\n\t" \
    "s_nop 0\n\t" \
    "global_load_lds_dwordx4 v200, s[46:47]\n\t" \
    "s_add_u32 s46, s42, 4194304\n\t" \
    "s_addc_u32 s47, s43, 0\n\t" \
    "s_add_u32 m0, s82, 131072\n\t" \
    "s_nop 0\n\t" \
    "global_load_lds_dwordx4 v200, s[46:47]\n\t" \
    "s_add_u32 s46, s42, 4325376\n\t" \
    "s_addc_u32 s47, s43, 0\n\t" \
    "s_add_u32 m0, s82, 135168\n\t" \
    "s_nop 0\n\t" \
    "global_load_lds_dwordx4 v200, s[46:47]\n\t" \
    "s_add_u32 s46, s42, 4456448\n\t" \
    "s_addc_u32 s47, s43, 0\n\t" \
    "s_add_u32 m0, s82, 139264\n\t" \
    "s_nop 0\n\t" \
    "global_load_lds_dwordx4 v200, s[46:47]\n\t" \
    "s_add_u32 s46, s42, 4587520\n\t" \
    "s_addc_u32 s47, s43, 0\n\t" \
    "s_add_u32 m0, s82, 143360\n\t" \
    "s_nop 0\n\t" \
    "global_load_lds_dwordx4 v200, s[46:47]\n\t" \
    "global_load_dwordx4 v[120:123], v203, s[50:51]\n\t" \
    "global_load_dwordx4 v[124:127], v204, s[50:51]\n\t" \
    "global_load_dwordx4 v[128:131], v205, s[50:51]\n\t" \
    "global_load_dwordx4 v[132:135], v206, s[50:51]\n\t" \
    "global_load_dwordx4 v[136:139], v207, s[50:51]\n\t" \
    "global_load_dwordx4 v[140:143], v208, s[50:51]\n\t" \
    "global_load_dwordx4 v[144:147], v209, s[50:51]\n\t" \
    "global_load_dwordx4 v[148:151], v210, s[50:51]\n\t" \
    "global_load_dwordx4 v[152:155], v211, s[50:51]\n\t" \
    "s_add_u32 s52, s50, 36864\n\t" \
    "s_addc_u32 s53, s51, 0\n\t" \
    "global_load_dwordx4 v[72:75], v203, s[52:53]\n\t" \
    "global_load_dwordx4 v[76:79], v204, s[52:53]\n\t" \
    "global_load_dwordx4 v[80:83], v205, s[52:53]\n\t" \
    "global_load_dwordx4 v[84:87], v206, s[52:53]\n\t" \
    "global_load_dwordx4 v[88:91], v207, s[52:53]\n\t" \
    "global_load_dwordx4 v[92:95], v208, s[52:53]\n\t" \
    "global_load_dwordx4 v[96:99], v209, s[52:53]\n\t" \
    "global_load_dwordx4 v[100:103], v210, s[52:53]\n\t" \
    "global_load_dwordx4 v[104:107], v211, s[52:53]\n\t" \
    "s_waitcnt vmcnt(50) lgkmcnt(0)\n\t" \
    "s_barrier\n\t" \
    "ds_read_b64_tr_b16 a[144:145], v201 offset:0\n\t" \
    "ds_read_b64_tr_b16 a[146:147], v201 offset:1024\n\t" \
    "ds_read_b64_tr_b16 a[148:149], v201 offset:8192\n\t" \
    "ds_read_b64_tr_b16 a[150:151], v201 offset:9216\n\t" \
    "ds_read_b64_tr_b16 a[216:217], v202 offset:0\n\t" \
    "ds_read_b64_tr_b16 a[218:219], v202 offset:1024\n\t" \
    "ds_read_b64_tr_b16 a[220:221], v202 offset:8192\n\t" \
    "ds_read_b64_tr_b16 a[222:223], v202 offset:9216\n\t" \
    "s_waitcnt vmcnt(46) lgkmcnt(0)\n\t" \
    "s_barrier\n\t" \
    "ds_read_b64_tr_b16 a[152:153], v201 offset:16384\n\t" \
    "ds_read_b64_tr_b16 a[154:155], v201 offset:17408\n\t" \
    "ds_read_b64_tr_b16 a[156:157], v201 offset:24576\n\t" \
    "ds_read_b64_tr_b16 a[158:159], v201 offset:25600\n\t" \
    "ds_read_b64_tr_b16 a[224:225], v202 offset:16384\n\t" \
    "ds_read_b64_tr_b16 a[226:227], v202 offset:17408\n\t" \
    "ds_read_b64_tr_b16 a[228:229], v202 offset:24576\n\t" \
    "ds_read_b64_tr_b16 a[230:231], v202 offset:25600\n\t" \
    "s_waitcnt vmcnt(42) lgkmcnt(0)\n\t" \
    "s_barrier\n\t" \
    "ds_read_b64_tr_b16 a[160:161], v201 offset:32768\n\t" \
    "ds_read_b64_tr_b16 a[162:163], v201 offset:33792\n\t" \
    "ds_read_b64_tr_b16 a[164:165], v201 offset:40960\n\t" \
    "ds_read_b64_tr_b16 a[166:167], v201 offset:41984\n\t" \
    "ds_read_b64_tr_b16 a[232:233], v202 offset:32768\n\t" \
    "ds_read_b64_tr_b16 a[234:235], v202 offset:33792\n\t" \
    "ds_read_b64_tr_b16 a[236:237], v202 offset:40960\n\t" \
    "ds_read_b64_tr_b16 a[238:239], v202 offset:41984\n\t" \
    "s_waitcnt vmcnt(38) lgkmcnt(0)\n\t" \
    "s_barrier\n\t" \
    "ds_read_b64_tr_b16 a[168:169], v201 offset:49152\n\t" \
    "ds_read_b64_tr_b16 a[170:171], v201 offset:50176\n\t" \
    "ds_read_b64_tr_b16 a[172:173], v201 offset:57344\n\t" \
    "ds_read_b64_tr_b16 a[174:175], v201 offset:58368\n\t" \
    "ds_read_b64_tr_b16 a[240:241], v202 offset:49152\n\t" \
    "ds_read_b64_tr_b16 a[242:243], v202 offset:50176\n\t" \
    "ds_read_b64_tr_b16 a[244:245], v202 offset:57344\n\t" \
    "ds_read_b64_tr_b16 a[246:247], v202 offset:58368\n\t" \
    "v_mbcnt_lo_u32_b32 v164, -1, 0\n\t" \
    "v_mbcnt_hi_u32_b32 v164, -1, v164\n\t" \
    "v_lshlrev_b32 v164, 6, v164\n\t" \
    "v_add_u32 v164, 6225920, v164\n\t" \
    "v_mov_b32 v165, 6234112\n\t" \
    "global_load_dword v165, v165, s[36:37] sc1\n\t" \
    "global_load_dwordx2 v[166:167], v164, s[36:37] sc1\n\t" \
    "global_load_dword v168, v164, s[36:37] offset:2048 sc1\n\t" \
    "s_waitcnt vmcnt(37) lgkmcnt(0)\n\t" \
    "s_barrier\n\t" \
    "v_add_u32 v201, 0x10000, v201\n\t" \
    "v_add_u32 v202, 0x10000, v202\n\t" \
    "ds_read_b64_tr_b16 a[176:177], v201 offset:0\n\t" \
    "ds_read_b64_tr_b16 a[178:179], v201 offset:1024\n\t" \
    "ds_read_b64_tr_b16 a[180:181], v201 offset:8192\n\t" \
    "ds_read_b64_tr_b16 a[182:183], v201 offset:9216\n\t" \
    "ds_read_b64_tr_b16 a[248:249], v202 offset:0\n\t" \
    "ds_read_b64_tr_b16 a[250:251], v202 offset:1024\n\t" \
    "ds_read_b64_tr_b16 a[252:253], v202 offset:8192\n\t" \
    "ds_read_b64_tr_b16 a[254:255], v202 offset:9216\n\t" \
    "s_waitcnt vmcnt(33) lgkmcnt(0)\n\t" \
    "s_barrier\n\t" \
    "ds_read_b64_tr_b16 a[184:185], v201 offset:16384\n\t" \
    "ds_read_b64_tr_b16 a[186:187], v201 offset:17408\n\t" \
    "ds_read_b64_tr_b16 a[188:189], v201 offset:24576\n\t" \
    "ds_read_b64_tr_b16 a[190:191], v201 offset:25600\n\t" \
    "ds_read_b64_tr_b16 v[224:225], v202 offset:16384\n\t" \
    "ds_read_b64_tr_b16 v[226:227], v202 offset:17408\n\t" \
    "ds_read_b64_tr_b16 v[228:229], v202 offset:24576\n\t" \
    "ds_read_b64_tr_b16 v[230:231], v202 offset:25600\n\t" \
    "s_waitcnt vmcnt(29) lgkmcnt(0)\n\t" \
    "s_barrier\n\t" \
    "ds_read_b64_tr_b16 a[192:193], v201 offset:32768\n\t" \
    "ds_read_b64_tr_b16 a[194:195], v201 offset:33792\n\t" \
    "ds_read_b64_tr_b16 a[196:197], v201 offset:40960\n\t" \
    "ds_read_b64_tr_b16 a[198:199], v201 offset:41984\n\t" \
    "ds_read_b64_tr_b16 v[232:233], v202 offset:32768\n\t" \
    "ds_read_b64_tr_b16 v[234:235], v202 offset:33792\n\t" \
    "ds_read_b64_tr_b16 v[236:237], v202 offset:40960\n\t" \
    "ds_read_b64_tr_b16 v[238:239], v202 offset:41984\n\t" \
    "s_waitcnt vmcnt(25) lgkmcnt(0)\n\t" \
    "s_barrier\n\t" \
    "ds_read_b64_tr_b16 a[200:201], v201 offset:49152\n\t" \
    "ds_read_b64_tr_b16 a[202:203], v201 offset:50176\n\t" \
    "ds_read_b64_tr_b16 a[204:205], v201 offset:57344\n\t" \
    "ds_read_b64_tr_b16 a[206:207], v201 offset:58368\n\t" \
    "ds_read_b64_tr_b16 v[240:241], v202 offset:49152\n\t" \
    "ds_read_b64_tr_b16 v[242:243], v202 offset:50176\n\t" \
    "ds_read_b64_tr_b16 v[244:245], v202 offset:57344\n\t" \
    "ds_read_b64_tr_b16 v[246:247], v202 offset:58368\n\t" \
    "s_waitcnt vmcnt(21) lgkmcnt(0)\n\t" \
    "s_barrier\n\t" \
    "v_add_u32 v201, 0x10000, v201\n\t" \
    "v_add_u32 v202, 0x10000, v202\n\t" \
    "ds_read_b64_tr_b16 a[208:209], v201 offset:0\n\t" \
    "ds_read_b64_tr_b16 a[210:211], v201 offset:1024\n\t" \
    "ds_read_b64_tr_b16 a[212:213], v201 offset:8192\n\t" \
    "ds_read_b64_tr_b16 a[214:215], v201 offset:9216\n\t" \
    "ds_read_b64_tr_b16 v[248:249], v202 offset:0\n\t" \
    "ds_read_b64_tr_b16 v[250:251], v202 offset:1024\n\t" \
    "ds_read_b64_tr_b16 v[252:253], v202 offset:8192\n\t" \
    "ds_read_b64_tr_b16 v[254:255], v202 offset:9216\n\t" \
    "v_add_u32 v201, 0xfffe0000, v201\n\t" \
    "v_add_u32 v202, 0xfffe0000, v202\n\t" \
    "s_waitcnt lgkmcnt(0)\n\t" \
    "s_barrier\n\t" \
    "s_waitcnt vmcnt(0)\n\t" \
    "ds_write_b128 v3, v[120:123] offset:0\n\t" \
    "ds_write_b128 v3, v[124:127] offset:1024\n\t" \
    "ds_write_b128 v3, v[128:131] offset:2048\n\t" \
    "ds_write_b128 v3, v[132:135] offset:3072\n\t" \
    "ds_write_b128 v3, v[136:139] offset:4096\n\t" \
    "ds_write_b128 v3, v[140:143] offset:5120\n\t" \
    "ds_write_b128 v3, v[144:147] offset:6144\n\t" \
    "ds_write_b128 v3, v[148:151] offset:7168\n\t" \
    "ds_write_b128 v3, v[152:155] offset:8192\n\t" \
    "ds_write_b128 v3, v[72:75] offset:36864\n\t" \
    "ds_write_b128 v3, v[76:79] offset:37888\n\t" \
    "ds_write_b128 v3, v[80:83] offset:38912\n\t" \
    "ds_write_b128 v3, v[84:87] offset:39936\n\t" \
    "ds_write_b128 v3, v[88:91] offset:40960\n\t" \
    "ds_write_b128 v3, v[92:95] offset:41984\n\t" \
    "ds_write_b128 v3, v[96:99] offset:43008\n\t" \
    "ds_write_b128 v3, v[100:103] offset:44032\n\t" \
    "ds_write_b128 v3, v[104:107] offset:45056\n\t" \
    "gx_C1_%=:\n\t" \
    "v_cmp_ne_u32 s[54:55], v166, v168\n\t" \
    "v_sub_u32 v166, v166, v165\n\t" \
    "v_cmp_ne_u32 vcc, 1, v166\n\t" \
    "s_nop 1\n\t" \
    "s_or_b64 vcc, vcc, s[54:55]\n\t" \
    "s_cbranch_scc0 gx_Cok2_%=\n\t" \
    "s_cmp_eq_u32 s68, 0\n\t" \
    "s_cbranch_scc1 gx_F4_%=\n\t" \
    "s_sub_u32 s68, s68, 1\n\t" \
    "s_sleep 2\n\t" \
    "s_branch gx_Cag3_%=\n\t" \
    "gx_F4_%=:\n\t" \
    "v_mov_b32 v168, 5767424\n\t" \
    "v_mov_b32 v169, 4\n\t" \
    "global_store_dword v168, v169, s[36:37] sc1\n\t" \
    "s_waitcnt vmcnt(0)\n\t" \
    "s_branch gx_Cok2_%=\n\t" \
    "gx_Cag3_%=:\n\t" \
    "global_load_dwordx2 v[166:167], v164, s[36:37] sc1\n\t" \
    "global_load_dword v168, v164, s[36:37] offset:2048 sc1\n\t" \
    "s_waitcnt vmcnt(0)\n\t" \
    "s_branch gx_C1_%=\n\t" \
    "gx_Cok2_%=:\n\t" \
    "s_cmp_lg_u32 s66, 0\n\t" \
    "s_cbranch_scc1 gx_Ca5_%=\n\t" \
    "v_mov_b32 v164, 5836800\n\t" \
    "s_mov_b64 exec, 1\n\t" \
    "global_atomic_add v164, v166, s[36:37] sc1\n\t" \
    "s_mov_b64 exec, -1\n\t" \
    "gx_Ca5_%=:\n\t" \
    "s_waitcnt lgkmcnt(0)\n\t" \
    "s_barrier\n\t" \
    "ds_read_b128 v[24:27], v1 offset:0\n\t" \
    "ds_read_b128 v[28:31], v1 offset:18432\n\t" \
    "ds_read_b128 v[32:35], v2 offset:0\n\t" \
    "ds_read_b128 v[36:39], v2 offset:18432\n\t" \
    "ds_read_b128 v[40:43], v1 offset:128\n\t" \
    "ds_read_b128 v[44:47], v1 offset:18560\n\t" \
    "gx_l16_%=:\n\t" \
    "s_add_u32 s84, s65, 2\n\t" \
    "s_min_u32 s69, s84, 63\n\t" \
    "s_mul_i32 s69, s69, 36864\n\t" \
    "s_add_u32 s52, s50, s69\n\t" \
    "s_addc_u32 s53, s51, 0\n\t" \
    "s_add_u32 s90, s90, 36864\n\t" \
    "s_cmp_eq_u32 s90, 147456\n\t" \
    "s_cselect_b32 s90, 0, s90\n\t" \
    "s_add_u32 s91, s90, 36864\n\t" \
    "s_cmp_eq_u32 s91, 147456\n\t" \
    "s_cselect_b32 s91, 0, s91\n\t" \
    "s_add_u32 s91, s91, s89\n\t" \
    "v_add_u32 v22, s90, v0\n\t" \
    "v_xor_b32 v23, 64, v22\n\t" \
    "s_mul_i32 s70, s67, 7\n\t" \
    "s_add_u32 s69, s65, 0\n\t" \
    "s_xor_b32 s69, s69, s70\n\t" \
    "s_and_b32 s69, s69, 63\n\t" \
    "s_lshl_b32 s69, s69, 13\n\t" \
    "s_mul_i32 s70, s67, 524288\n\t" \
    "s_add_u32 s69, s69, s70\n\t" \
    "s_add_u32 s69, s69, 23068672\n\t" \
    "s_add_u32 s40, s36, s69\n\t" \
    "s_addc_u32 s41, s37, 0\n\t" \
    "s_waitcnt lgkmcnt(5)\n\t" \
    "v_mfma_f32_16x16x32_bf16 v[72:75], a[144:147], v[24:27], 0\n\t" \
    "ds_read_b128 v[48:51], v2 offset:128\n\t" \
    "s_waitcnt lgkmcnt(5)\n\t" \
    "v_mfma_f32_16x16x32_bf16 v[76:79], a[144:147], v[28:31], 0\n\t" \
    "ds_read_b128 v[52:55], v2 offset:18560\n\t" \
    "v_mfma_f32_16x16x32_bf16 v[80:83], a[216:219], v[24:27], 0\n\t" \
    "s_add_u32 m0, s91, 0\n\t" \
    "v_mfma_f32_16x16x32_bf16 v[84:87], a[216:219], v[28:31], 0\n\t" \
    "global_load_lds_dwordx4 v203, s[52:53]\n\t" \
    "s_waitcnt lgkmcnt(5)\n\t" \
    "v_mfma_f32_16x16x32_bf16 v[72:75], a[148:151], v[32:35], v[72:75]\n\t" \
    "ds_read_b128 v[56:59], v1 offset:256\n\t" \
    "s_waitcnt lgkmcnt(5)\n\t" \
    "v_mfma_f32_16x16x32_bf16 v[76:79], a[148:151], v[36:39], v[76:79]\n\t" \
    "ds_read_b128 v[60:63], v1 offset:18688\n\t" \
    "v_mfma_f32_16x16x32_bf16 v[80:83], a[220:223], v[32:35], v[80:83]\n\t" \
    "s_add_u32 m0, s91, 1024\n\t" \
    "v_mfma_f32_16x16x32_bf16 v[84:87], a[220:223], v[36:39], v[84:87]\n\t" \
    "global_load_lds_dwordx4 v204, s[52:53]\n\t" \
    "s_waitcnt lgkmcnt(5)\n\t" \
    "v_mfma_f32_16x16x32_bf16 v[72:75], a[152:155], v[40:43], v[72:75]\n\t" \
    "ds_read_b128 v[64:67], v2 offset:256\n\t" \
    "s_waitcnt lgkmcnt(5)\n\t" \
    "v_mfma_f32_16x16x32_bf16 v[76:79], a[152:155], v[44:47], v[76:79]\n\t" \
    "ds_read_b128 v[68:71], v2 offset:18688\n\t" \
    "v_mfma_f32_16x16x32_bf16 v[80:83], a[224:227], v[40:43], v[80:83]\n\t" \
    "s_add_u32 m0, s91, 2048\n\t" \
    "v_mfma_f32_16x16x32_bf16 v[84:87], a[224:227], v[44:47], v[84:87]\n\t" \
    "global_load_lds_dwordx4 v205, s[52:53]\n\t" \
    "s_waitcnt lgkmcnt(5)\n\t" \
    "v_mfma_f32_16x16x32_bf16 v[72:75], a[156:159], v[48:51], v[72:75]\n\t" \
    "ds_read_b128 v[24:27], v1 offset:384\n\t" \
    "s_waitcnt lgkmcnt(5)\n\t" \
    "v_mfma_f32_16x16x32_bf16 v[76:79], a[156:159], v[52:55], v[76:79]\n\t" \
    "ds_read_b128 v[28:31], v1 offset:18816\n\t" \
    "v_mfma_f32_16x16x32_bf16 v[80:83], a[228:231], v[48:51], v[80:83]\n\t" \
    "s_add_u32 m0, s91, 3072\n\t" \
    "v_mfma_f32_16x16x32_bf16 v[84:87], a[228:231], v[52:55], v[84:87]\n\t" \
    "global_load_lds_dwordx4 v206, s[52:53]\n\t" \
    "s_waitcnt lgkmcnt(5)\n\t" \
    "v_mfma_f32_16x16x32_bf16 v[72:75], a[160:163], v[56:59], v[72:75]\n\t" \
    "ds_read_b128 v[32:35], v2 offset:384\n\t" \
    "s_waitcnt lgkmcnt(5)\n\t" \
    "v_mfma_f32_16x16x32_bf16 v[76:79], a[160:163], v[60:63], v[76:79]\n\t" \
    "ds_read_b128 v[36:39], v2 offset:18816\n\t" \
    "v_mfma_f32_16x16x32_bf16 v[80:83], a[232:235], v[56:59], v[80:83]\n\t" \
    "s_add_u32 m0, s91, 4096\n\t" \
    "v_mfma_f32_16x16x32_bf16 v[84:87], a[232:235], v[60:63], v[84:87]\n\t" \
    "global_load_lds_dwordx4 v207, s[52:53]\n\t" \
    "s_waitcnt lgkmcnt(5)\n\t" \
    "v_mfma_f32_16x16x32_bf16 v[72:75], a[164:167], v[64:67], v[72:75]\n\t" \
    "ds_read_b128 v[40:43], v1 offset:512\n\t" \
    "s_waitcnt lgkmcnt(5)\n\t" \
    "v_mfma_f32_16x16x32_bf16 v[76:79], a[164:167], v[68:71], v[76:79]\n\t" \
    "ds_read_b128 v[44:47], v1 offset:18944\n\t" \
    "v_mfma_f32_16x16x32_bf16 v[80:83], a[236:239], v[64:67], v[80:83]\n\t" \
    "s_add_u32 m0, s91, 5120\n\t" \
    "v_mfma_f32_16x16x32_bf16 v[84:87], a[236:239], v[68:71], v[84:87]\n\t" \
    "global_load_lds_dwordx4 v208, s[52:53]\n\t" \
    "s_waitcnt lgkmcnt(5)\n\t" \
    "v_mfma_f32_16x16x32_bf16 v[72:75], a[168:171], v[24:27], v[72:75]\n\t" \
    "ds_read_b128 v[48:51], v2 offset:512\n\t" \
    "s_waitcnt lgkmcnt(5)\n\t" \
    "v_mfma_f32_16x16x32_bf16 v[76:79], a[168:171], v[28:31], v[76:79]\n\t" \
    "ds_read_b128 v[52:55], v2 offset:18944\n\t" \
    "v_mfma_f32_16x16x32_bf16 v[80:83], a[240:243], v[24:27], v[80:83]\n\t" \
    "s_add_u32 m0, s91, 6144\n\t" \
    "v_mfma_f32_16x16x32_bf16 v[84:87], a[240:243], v[28:31], v[84:87]\n\t" \
    "global_load_lds_dwordx4 v209, s[52:53]\n\t" \
    "s_waitcnt lgkmcnt(5)\n\t" \
    "v_mfma_f32_16x16x32_bf16 v[72:75], a[172:175], v[32:35], v[72:75]\n\t" \
    "ds_read_b128 v[56:59], v1 offset:640\n\t" \
    "s_waitcnt lgkmcnt(5)\n\t" \
    "v_mfma_f32_16x16x32_bf16 v[76:79], a[172:175], v[36:39], v[76:79]\n\t" \
    "ds_read_b128 v[60:63], v1 offset:19072\n\t" \
    "v_mfma_f32_16x16x32_bf16 v[80:83], a[244:247], v[32:35], v[80:83]\n\t" \
    "s_add_u32 m0, s91, 7168\n\t" \
    "v_mfma_f32_16x16x32_bf16 v[84:87], a[244:247], v[36:39], v[84:87]\n\t" \
    "global_load_lds_dwordx4 v210, s[52:53]\n\t" \
    "s_waitcnt lgkmcnt(5)\n\t" \
    "v_mfma_f32_16x16x32_bf16 v[72:75], a[176:179], v[40:43], v[72:75]\n\t" \
    "ds_read_b128 v[64:67], v2 offset:640\n\t" \
    "s_waitcnt lgkmcnt(5)\n\t" \
    "v_mfma_f32_16x16x32_bf16 v[76:79], a[176:179], v[44:47], v[76:79]\n\t" \
    "ds_read_b128 v[68:71], v2 offset:19072\n\t" \
    "v_mfma_f32_16x16x32_bf16 v[80:83], a[248:251], v[40:43], v[80:83]\n\t" \
    "s_add_u32 m0, s91, 8192\n\t" \
    "v_mfma_f32_16x16x32_bf16 v[84:87], a[248:251], v[44:47], v[84:87]\n\t" \
    "global_load_lds_dwordx4 v211, s[52:53]\n\t" \
    "s_waitcnt lgkmcnt(5)\n\t" \
    "v_mfma_f32_16x16x32_bf16 v[72:75], a[180:183], v[48:51], v[72:75]\n\t" \
    "ds_read_b128 v[24:27], v1 offset:768\n\t" \
    "s_waitcnt lgkmcnt(5)\n\t" \
    "v_mfma_f32_16x16x32_bf16 v[76:79], a[180:183], v[52:55], v[76:79]\n\t" \
    "ds_read_b128 v[28:31], v1 offset:19200\n\t" \
    "v_mfma_f32_16x16x32_bf16 v[80:83], a[252:255], v[48:51], v[80:83]\n\t" \
    "v_mfma_f32_16x16x32_bf16 v[84:87], a[252:255], v[52:55], v[84:87]\n\t" \
    "s_waitcnt lgkmcnt(5)\n\t" \
    "v_mfma_f32_16x16x32_bf16 v[72:75], a[184:187], v[56:59], v[72:75]\n\t" \
    "ds_read_b128 v[32:35], v2 offset:768\n\t" \
    "s_waitcnt lgkmcnt(5)\n\t" \
    "v_mfma_f32_16x16x32_bf16 v[76:79], a[184:187], v[60:63], v[76:79]\n\t" \
    "ds_read_b128 v[36:39], v2 offset:19200\n\t" \
    "v_mfma_f32_16x16x32_bf16 v[80:83], v[224:227], v[56:59], v[80:83]\n\t" \
    "s_waitcnt vmcnt(9)\n\t" \
    "s_barrier\n\t" \
    "v_mfma_f32_16x16x32_bf16 v[84:87], v[224:227], v[60:63], v[84:87]\n\t" \
    "s_cmp_lg_u32 s66, 0\n\t" \
    "s_cbranch_scc1 gx_SF9_%=\n\t" \
    "s_lshl_b32 s69, s67, 7\n\t" \
    "s_add_u32 s69, s69, 5771264\n\t" \
    "s_add_u32 s70, s65, -1\n\t" \
    "v_mov_b32 v156, s69\n\t" \
    "v_mov_b32 v157, s70\n\t" \
    "s_mov_b64 exec, 1\n\t" \
    "global_store_dword v156, v157, s[36:37] sc1\n\t" \
    "s_mov_b64 exec, -1\n\t" \
    "gx_SF9_%=:\n\t" \
    "s_waitcnt lgkmcnt(5)\n\t" \
    "v_mfma_f32_16x16x32_bf16 v[72:75], a[188:191], v[64:67], v[72:75]\n\t" \
    "ds_read_b128 v[40:43], v1 offset:896\n\t" \
    "s_waitcnt lgkmcnt(5)\n\t" \
    "v_mfma_f32_16x16x32_bf16 v[76:79], a[188:191], v[68:71], v[76:79]\n\t" \
    "ds_read_b128 v[44:47], v1 offset:19328\n\t" \
    "v_mfma_f32_16x16x32_bf16 v[80:83], v[228:231], v[64:67], v[80:83]\n\t" \
    "v_mfma_f32_16x16x32_bf16 v[84:87], v[228:231], v[68:71], v[84:87]\n\t" \
    "s_waitcnt lgkmcnt(5)\n\t" \
    "v_mfma_f32_16x16x32_bf16 v[72:75], a[192:195], v[24:27], v[72:75]\n\t" \
    "ds_read_b128 v[48:51], v2 offset:896\n\t" \
    "s_waitcnt lgkmcnt(5)\n\t" \
    "v_mfma_f32_16x16x32_bf16 v[76:79], a[192:195], v[28:31], v[76:79]\n\t" \
    "ds_read_b128 v[52:55], v2 offset:19328\n\t" \
    "v_mfma_f32_16x16x32_bf16 v[80:83], v[232:235], v[24:27], v[80:83]\n\t" \
    "v_mfma_f32_16x16x32_bf16 v[84:87], v[232:235], v[28:31], v[84:87]\n\t" \
    "s_waitcnt lgkmcnt(5)\n\t" \
    "v_mfma_f32_16x16x32_bf16 v[72:75], a[196:199], v[32:35], v[72:75]\n\t" \
    "ds_read_b128 v[56:59], v1 offset:1024\n\t" \
    "s_waitcnt lgkmcnt(5)\n\t" \
    "v_mfma_f32_16x16x32_bf16 v[76:79], a[196:199], v[36:39], v[76:79]\n\t" \
    "ds_read_b128 v[60:63], v1 offset:19456\n\t" \
    "v_mfma_f32_16x16x32_bf16 v[80:83], v[236:239], v[32:35], v[80:83]\n\t" \
    "v_mfma_f32_16x16x32_bf16 v[84:87], v[236:239], v[36:39], v[84:87]\n\t" \
    "s_waitcnt lgkmcnt(5)\n\t" \
    "v_mfma_f32_16x16x32_bf16 v[72:75], a[200:203], v[40:43], v[72:75]\n\t" \
    "ds_read_b128 v[64:67], v2 offset:1024\n\t" \
    "s_waitcnt lgkmcnt(5)\n\t" \
    "v_mfma_f32_16x16x32_bf16 v[76:79], a[200:203], v[44:47], v[76:79]\n\t" \
    "ds_read_b128 v[68:71], v2 offset:19456\n\t" \
    "v_mfma_f32_16x16x32_bf16 v[80:83], v[240:243], v[40:43], v[80:83]\n\t" \
    "v_mfma_f32_16x16x32_bf16 v[84:87], v[240:243], v[44:47], v[84:87]\n\t" \
    "s_waitcnt lgkmcnt(5)\n\t" \
    "v_mfma_f32_16x16x32_bf16 v[72:75], a[204:207], v[48:51], v[72:75]\n\t" \
    "ds_read_b128 v[24:27], v22 offset:0\n\t" \
    "s_waitcnt lgkmcnt(5)\n\t" \
    "v_mfma_f32_16x16x32_bf16 v[76:79], a[204:207], v[52:55], v[76:79]\n\t" \
    "ds_read_b128 v[28:31], v22 offset:18432\n\t" \
    "v_mfma_f32_16x16x32_bf16 v[80:83], v[244:247], v[48:51], v[80:83]\n\t" \
    "v_mfma_f32_16x16x32_bf16 v[84:87], v[244:247], v[52:55], v[84:87]\n\t" \
    "s_waitcnt lgkmcnt(5)\n\t" \
    "v_mfma_f32_16x16x32_bf16 v[72:75], a[208:211], v[56:59], v[72:75]\n\t" \
    "ds_read_b128 v[32:35], v23 offset:0\n\t" \
    "s_waitcnt lgkmcnt(5)\n\t" \
    "v_mfma_f32_16x16x32_bf16 v[76:79], a[208:211], v[60:63], v[76:79]\n\t" \
    "ds_read_b128 v[36:39], v23 offset:18432\n\t" \
    "v_mfma_f32_16x16x32_bf16 v[80:83], v[248:251], v[56:59], v[80:83]\n\t" \
    "v_mfma_f32_16x16x32_bf16 v[84:87], v[248:251], v[60:63], v[84:87]\n\t" \
    "s_waitcnt lgkmcnt(5)\n\t" \
    "v_mfma_f32_16x16x32_bf16 v[72:75], a[212:215], v[64:67], v[72:75]\n\t" \
    "ds_read_b128 v[40:43], v22 offset:128\n\t" \
    "s_waitcnt lgkmcnt(5)\n\t" \
    "v_mfma_f32_16x16x32_bf16 v[76:79], a[212:215], v[68:71], v[76:79]\n\t" \
    "ds_read_b128 v[44:47], v22 offset:18560\n\t" \
    "v_mfma_f32_16x16x32_bf16 v[80:83], v[252:255], v[64:67], v[80:83]\n\t" \
    "v_mfma_f32_16x16x32_bf16 v[84:87], v[252:255], v[68:71], v[84:87]\n\t" \
    "v_mov_b32 v1, v22\n\t" \
    "v_mov_b32 v2, v23\n\t" \
    "v_cvt_pk_bf16_f32 v104, v72, v73\n\t" \
    "v_cvt_pk_bf16_f32 v105, v74, v75\n\t" \
    "s_nop 2\n\t" \
    "v_cvt_pk_bf16_f32 v106, v80, v81\n\t" \
    "v_cvt_pk_bf16_f32 v107, v82, v83\n\t" \
    "global_store_dwordx4 v8, v[104:107], s[40:41] offset:0 sc1\n\t" \
    "v_cvt_pk_bf16_f32 v108, v76, v77\n\t" \
    "v_cvt_pk_bf16_f32 v109, v78, v79\n\t" \
    "v_cvt_pk_bf16_f32 v110, v84, v85\n\t" \
    "v_cvt_pk_bf16_f32 v111, v86, v87\n\t" \
    "global_store_dwordx4 v8, v[108:111], s[40:41] offset:1024 sc1\n\t" \
    "s_cmp_ge_u32 s65, 1\n\t" \
    "s_cbranch_scc1 gx_nofast7_%=\n\t" \
    "s_waitcnt vmcnt(0)\n\t" \
    "s_barrier\n\t" \
    "s_cmp_lg_u32 s66, 0\n\t" \
    "s_cbranch_scc1 gx_SF10_%=\n\t" \
    "s_lshl_b32 s69, s67, 7\n\t" \
    "s_add_u32 s69, s69, 5771264\n\t" \
    "s_add_u32 s70, s65, 0\n\t" \
    "v_mov_b32 v156, s69\n\t" \
    "v_mov_b32 v157, s70\n\t" \
    "s_mov_b64 exec, 1\n\t" \
    "global_store_dword v156, v157, s[36:37] sc1\n\t" \
    "s_mov_b64 exec, -1\n\t" \
    "gx_SF10_%=:\n\t" \
    "gx_nofast7_%=:\n\t" \
    "s_add_u32 s65, s65, 1\n\t" \
    "s_cmp_lt_u32 s65, 10\n\t" \
    "s_cbranch_scc1 gx_l16_%=\n\t" \
    "s_waitcnt vmcnt(0) lgkmcnt(0)\n\t" \
    "s_barrier\n\t" \
    "s_cmp_lg_u32 s66, 0\n\t" \
    "s_cbranch_scc1 gx_SF11_%=\n\t" \
    "s_lshl_b32 s69, s67, 7\n\t" \
    "s_add_u32 s69, s69, 5771264\n\t" \
    "s_add_u32 s70, s65, -1\n\t" \
    "v_mov_b32 v156, s69\n\t" \
    "v_mov_b32 v157, s70\n\t" \
    "s_mov_b64 exec, 1\n\t" \
    "global_store_dword v156, v157, s[36:37] sc1\n\t" \
    "s_mov_b64 exec, -1\n\t" \
    "gx_SF11_%=:\n\t" \
    "s_add_u32 s46, s44, 0\n\t" \
    "s_addc_u32 s47, s45, 0\n\t" \
    "s_add_u32 m0, s82, 0\n\t" \
    "s_nop 0\n\t" \
    "global_load_lds_dwordx4 v200, s[46:47]\n\t" \
    "s_add_u32 s46, s44, 131072\n\t" \
    "s_addc_u32 s47, s45, 0\n\t" \
    "s_add_u32 m0, s82, 4096\n\t" \
    "s_nop 0\n\t" \
    "global_load_lds_dwordx4 v200, s[46:47]\n\t" \
    "s_add_u32 s46, s44, 262144\n\t" \
    "s_addc_u32 s47, s45, 0\n\t" \
    "s_add_u32 m0, s82, 8192\n\t" \
    "s_nop 0\n\t" \
    "global_load_lds_dwordx4 v200, s[46:47]\n\t" \
    "s_add_u32 s46, s44, 393216\n\t" \
    "s_addc_u32 s47, s45, 0\n\t" \
    "s_add_u32 m0, s82, 12288\n\t" \
    "s_nop 0\n\t" \
    "global_load_lds_dwordx4 v200, s[46:47]\n\t" \
    "s_add_u32 s46, s44, 524288\n\t" \
    "s_addc_u32 s47, s45, 0\n\t" \
    "s_add_u32 m0, s82, 16384\n\t" \
    "s_nop 0\n\t" \
    "global_load_lds_dwordx4 v200, s[46:47]\n\t" \
    "s_add_u32 s46, s44, 655360\n\t" \
    "s_addc_u32 s47, s45, 0\n\t" \
    "s_add_u32 m0, s82, 20480\n\t" \
    "s_nop 0\n\t" \
    "global_load_lds_dwordx4 v200, s[46:47]\n\t" \
    "s_add_u32 s46, s44, 786432\n\t" \
    "s_addc_u32 s47, s45, 0\n\t" \
    "s_add_u32 m0, s82, 24576\n\t" \
    "s_nop 0\n\t" \
    "global_load_lds_dwordx4 v200, s[46:47]\n\t" \
    "s_add_u32 s46, s44, 917504\n\t" \
    "s_addc_u32 s47, s45, 0\n\t" \
    "s_add_u32 m0, s82, 28672\n\t" \
    "s_nop 0\n\t" \
    "global_load_lds_dwordx4 v200, s[46:47]\n\t" \
    "s_add_u32 s46, s44, 1048576\n\t" \
    "s_addc_u32 s47, s45, 0\n\t" \
    "s_add_u32 m0, s82, 32768\n\t" \
    "s_nop 0\n\t" \
    "global_load_lds_dwordx4 v200, s[46:47]\n\t" \
    "s_add_u32 s46, s44, 1179648\n\t" \
    "s_addc_u32 s47, s45, 0\n\t" \
    "s_add_u32 m0, s82, 36864\n\t" \
    "s_nop 0\n\t" \
    "global_load_lds_dwordx4 v200, s[46:47]\n\t" \
    "s_add_u32 s46, s44, 1310720\n\t" \
    "s_addc_u32 s47, s45, 0\n\t" \
    "s_add_u32 m0, s82, 40960\n\t" \
    "s_nop 0\n\t" \
    "global_load_lds_dwordx4 v200, s[46:47]\n\t" \
    "s_add_u32 s46, s44, 1441792\n\t" \
    "s_addc_u32 s47, s45, 0\n\t" \
    "s_add_u32 m0, s82, 45056\n\t" \
    "s_nop 0\n\t" \
    "global_load_lds_dwordx4 v200, s[46:47]\n\t" \
    "s_add_u32 s46, s44, 1572864\n\t" \
    "s_addc_u32 s47, s45, 0\n\t" \
    "s_add_u32 m0, s82, 49152\n\t" \
    "s_nop 0\n\t" \
    "global_load_lds_dwordx4 v200, s[46:47]\n\t" \
    "s_add_u32 s46, s44, 1703936\n\t" \
    "s_addc_u32 s47, s45, 0\n\t" \
    "s_add_u32 m0, s82, 53248\n\t" \
    "s_nop 0\n\t" \
    "global_load_lds_dwordx4 v200, s[46:47]\n\t" \
    "s_add_u32 s46, s44, 1835008\n\t" \
    "s_addc_u32 s47, s45, 0\n\t" \
    "s_add_u32 m0, s82, 57344\n\t" \
    "s_nop 0\n\t" \
    "global_load_lds_dwordx4 v200, s[46:47]\n\t" \
    "s_add_u32 s46, s44, 1966080\n\t" \
    "s_addc_u32 s47, s45, 0\n\t" \
    "s_add_u32 m0, s82, 61440\n\t" \
    "s_nop 0\n\t" \
    "global_load_lds_dwordx4 v200, s[46:47]\n\t" \
    "s_add_u32 s46, s44, 2097152\n\t" \
    "s_addc_u32 s47, s45, 0\n\t" \
    "s_add_u32 m0, s82, 65536\n\t" \
    "s_nop 0\n\t" \
    "global_load_lds_dwordx4 v200, s[46:47]\n\t" \
    "s_add_u32 s46, s44, 2228224\n\t" \
    "s_addc_u32 s47, s45, 0\n\t" \
    "s_add_u32 m0, s82, 69632\n\t" \
    "s_nop 0\n\t" \
    "global_load_lds_dwordx4 v200, s[46:47]\n\t" \
    "s_add_u32 s46, s44, 2359296\n\t" \
    "s_addc_u32 s47, s45, 0\n\t" \
    "s_add_u32 m0, s82, 73728\n\t" \
    "s_nop 0\n\t" \
    "global_load_lds_dwordx4 v200, s[46:47]\n\t" \
    "s_add_u32 s46, s44, 2490368\n\t" \
    "s_addc_u32 s47, s45, 0\n\t" \
    "s_add_u32 m0, s82, 77824\n\t" \
    "s_nop 0\n\t" \
    "global_load_lds_dwordx4 v200, s[46:47]\n\t" \
    "s_add_u32 s46, s44, 2621440\n\t" \
    "s_addc_u32 s47, s45, 0\n\t" \
    "s_add_u32 m0, s82, 81920\n\t" \
    "s_nop 0\n\t" \
    "global_load_lds_dwordx4 v200, s[46:47]\n\t" \
    "s_add_u32 s46, s44, 2752512\n\t" \
    "s_addc_u32 s47, s45, 0\n\t" \
    "s_add_u32 m0, s82, 86016\n\t" \
    "s_nop 0\n\t" \
    "global_load_lds_dwordx4 v200, s[46:47]\n\t" \
    "s_add_u32 s46, s44, 2883584\n\t" \
    "s_addc_u32 s47, s45, 0\n\t" \
    "s_add_u32 m0, s82, 90112\n\t" \
    "s_nop 0\n\t" \
    "global_load_lds_dwordx4 v200, s[46:47]\n\t" \
    "s_add_u32 s46, s44, 3014656\n\t" \
    "s_addc_u32 s47, s45, 0\n\t" \
    "s_add_u32 m0, s82, 94208\n\t" \
    "s_nop 0\n\t" \
    "global_load_lds_dwordx4 v200, s[46:47]\n\t" \
    "s_add_u32 s46, s44, 3145728\n\t" \
    "s_addc_u32 s47, s45, 0\n\t" \
    "s_add_u32 m0, s82, 98304\n\t" \
    "s_nop 0\n\t" \
    "global_load_lds_dwordx4 v200, s[46:47]\n\t" \
    "s_add_u32 s46, s44, 3276800\n\t" \
    "s_addc_u32 s47, s45, 0\n\t" \
    "s_add_u32 m0, s82, 102400\n\t" \
    "s_nop 0\n\t" \
    "global_load_lds_dwordx4 v200, s[46:47]\n\t" \
    "s_add_u32 s46, s44, 3407872\n\t" \
    "s_addc_u32 s47, s45, 0\n\t" \
    "s_add_u32 m0, s82, 106496\n\t" \
    "s_nop 0\n\t" \
    "global_load_lds_dwordx4 v200, s[46:47]\n\t" \
    "s_add_u32 s46, s44, 3538944\n\t" \
    "s_addc_u32 s47, s45, 0\n\t" \
    "s_add_u32 m0, s82, 110592\n\t" \
    "s_nop 0\n\t" \
    "global_load_lds_dwordx4 v200, s[46:47]\n\t" \
    "s_add_u32 s46, s44, 3670016\n\t" \
    "s_addc_u32 s47, s45, 0\n\t" \
    "s_add_u32 m0, s82, 114688\n\t" \
    "s_nop 0\n\t" \
    "global_load_lds_dwordx4 v200, s[46:47]\n\t" \
    "s_add_u32 s46, s44, 3801088\n\t" \
    "s_addc_u32 s47, s45, 0\n\t" \
    "s_add_u32 m0, s82, 118784\n\t" \
    "s_nop 0\n\t" \
    "global_load_lds_dwordx4 v200, s[46:47]\n\t" \
    "s_add_u32 s46, s44, 3932160\n\t" \
    "s_addc_u32 s47, s45, 0\n\t" \
    "s_add_u32 m0, s82, 122880\n\t" \
    "s_nop 0\n\t" \
    "global_load_lds_dwordx4 v200, s[46:47]\n\t" \
    "s_add_u32 s46, s44, 4063232\n\t" \
    "s_addc_u32 s47, s45, 0\n\t" \
    "s_add_u32 m0, s82, 126976\n\t" \
    "s_nop 0\n\t" \
    "global_load_lds_dwordx4 v200, s[46:47]\n\t" \
    "s_add_u32 s46, s44, 4194304\n\t" \
    "s_addc_u32 s47, s45, 0\n\t" \
    "s_add_u32 m0, s82, 131072\n\t" \
    "s_nop 0\n\t" \
    "global_load_lds_dwordx4 v200, s[46:47]\n\t" \
    "s_add_u32 s46, s44, 4325376\n\t" \
    "s_addc_u32 s47, s45, 0\n\t" \
    "s_add_u32 m0, s82, 135168\n\t" \
    "s_nop 0\n\t" \
    "global_load_lds_dwordx4 v200, s[46:47]\n\t" \
    "s_add_u32 s46, s44, 4456448\n\t" \
    "s_addc_u32 s47, s45, 0\n\t" \
    "s_add_u32 m0, s82, 139264\n\t" \
    "s_nop 0\n\t" \
    "global_load_lds_dwordx4 v200, s[46:47]\n\t" \
    "s_add_u32 s46, s44, 4587520\n\t" \
    "s_addc_u32 s47, s45, 0\n\t" \
    "s_add_u32 m0, s82, 143360\n\t" \
    "s_nop 0\n\t" \
    "global_load_lds_dwordx4 v200, s[46:47]\n\t" \
    "global_load_dwordx4 v[120:123], v203, s[50:51]\n\t" \
    "global_load_dwordx4 v[124:127], v204, s[50:51]\n\t" \
    "global_load_dwordx4 v[128:131], v205, s[50:51]\n\t" \
    "global_load_dwordx4 v[132:135], v206, s[50:51]\n\t" \
    "global_load_dwordx4 v[136:139], v207, s[50:51]\n\t" \
    "global_load_dwordx4 v[140:143], v208, s[50:51]\n\t" \
    "global_load_dwordx4 v[144:147], v209, s[50:51]\n\t" \
    "global_load_dwordx4 v[148:151], v210, s[50:51]\n\t" \
    "global_load_dwordx4 v[152:155], v211, s[50:51]\n\t" \
    "s_add_u32 s52, s50, 368640\n\t" \
    "s_addc_u32 s53, s51, 0\n\t" \
    "global_load_dwordx4 v[72:75], v203, s[52:53]\n\t" \
    "global_load_dwordx4 v[76:79], v204, s[52:53]\n\t" \
    "global_load_dwordx4 v[80:83], v205, s[52:53]\n\t" \
    "global_load_dwordx4 v[84:87], v206, s[52:53]\n\t" \
    "global_load_dwordx4 v[88:91], v207, s[52:53]\n\t" \
    "global_load_dwordx4 v[92:95], v208, s[52:53]\n\t" \
    "global_load_dwordx4 v[96:99], v209, s[52:53]\n\t" \
    "global_load_dwordx4 v[100:103], v210, s[52:53]\n\t" \
    "global_load_dwordx4 v[104:107], v211, s[52:53]\n\t" \
    "s_waitcnt vmcnt(50) lgkmcnt(0)\n\t" \
    "s_barrier\n\t" \
    "ds_read_b64_tr_b16 a[0:1], v201 offset:0\n\t" \
    "ds_read_b64_tr_b16 a[2:3], v201 offset:1024\n\t" \
    "ds_read_b64_tr_b16 a[4:5], v201 offset:8192\n\t" \
    "ds_read_b64_tr_b16 a[6:7], v201 offset:9216\n\t" \
    "ds_read_b64_tr_b16 a[72:73], v202 offset:0\n\t" \
    "ds_read_b64_tr_b16 a[74:75], v202 offset:1024\n\t" \
    "ds_read_b64_tr_b16 a[76:77], v202 offset:8192\n\t" \
    "ds_read_b64_tr_b16 a[78:79], v202 offset:9216\n\t" \
    "s_waitcnt vmcnt(46) lgkmcnt(0)\n\t" \
    "s_barrier\n\t" \
    "ds_read_b64_tr_b16 a[8:9], v201 offset:16384\n\t" \
    "ds_read_b64_tr_b16 a[10:11], v201 offset:17408\n\t" \
    "ds_read_b64_tr_b16 a[12:13], v201 offset:24576\n\t" \
    "ds_read_b64_tr_b16 a[14:15], v201 offset:25600\n\t" \
    "ds_read_b64_tr_b16 a[80:81], v202 offset:16384\n\t" \
    "ds_read_b64_tr_b16 a[82:83], v202 offset:17408\n\t" \
    "ds_read_b64_tr_b16 a[84:85], v202 offset:24576\n\t" \
    "ds_read_b64_tr_b16 a[86:87], v202 offset:25600\n\t" \
    "s_waitcnt vmcnt(42) lgkmcnt(0)\n\t" \
    "s_barrier\n\t" \
    "ds_read_b64_tr_b16 a[16:17], v201 offset:32768\n\t" \
    "ds_read_b64_tr_b16 a[18:19], v201 offset:33792\n\t" \
    "ds_read_b64_tr_b16 a[20:21], v201 offset:40960\n\t" \
    "ds_read_b64_tr_b16 a[22:23], v201 offset:41984\n\t" \
    "ds_read_b64_tr_b16 a[88:89], v202 offset:32768\n\t" \
    "ds_read_b64_tr_b16 a[90:91], v202 offset:33792\n\t" \
    "ds_read_b64_tr_b16 a[92:93], v202 offset:40960\n\t" \
    "ds_read_b64_tr_b16 a[94:95], v202 offset:41984\n\t" \
    "s_waitcnt vmcnt(38) lgkmcnt(0)\n\t" \
    "s_barrier\n\t" \
    "ds_read_b64_tr_b16 a[24:25], v201 offset:49152\n\t" \
    "ds_read_b64_tr_b16 a[26:27], v201 offset:50176\n\t" \
    "ds_read_b64_tr_b16 a[28:29], v201 offset:57344\n\t" \
    "ds_read_b64_tr_b16 a[30:31], v201 offset:58368\n\t" \
    "ds_read_b64_tr_b16 a[96:97], v202 offset:49152\n\t" \
    "ds_read_b64_tr_b16 a[98:99], v202 offset:50176\n\t" \
    "ds_read_b64_tr_b16 a[100:101], v202 offset:57344\n\t" \
    "ds_read_b64_tr_b16 a[102:103], v202 offset:58368\n\t" \
    "s_waitcnt vmcnt(34) lgkmcnt(0)\n\t" \
    "s_barrier\n\t" \
    "v_add_u32 v201, 0x10000, v201\n\t" \
    "v_add_u32 v202, 0x10000, v202\n\t" \
    "ds_read_b64_tr_b16 a[32:33], v201 offset:0\n\t" \
    "ds_read_b64_tr_b16 a[34:35], v201 offset:1024\n\t" \
    "ds_read_b64_tr_b16 a[36:37], v201 offset:8192\n\t" \
    "ds_read_b64_tr_b16 a[38:39], v201 offset:9216\n\t" \
    "ds_read_b64_tr_b16 a[104:105], v202 offset:0\n\t" \
    "ds_read_b64_tr_b16 a[106:107], v202 offset:1024\n\t" \
    "ds_read_b64_tr_b16 a[108:109], v202 offset:8192\n\t" \
    "ds_read_b64_tr_b16 a[110:111], v202 offset:9216\n\t" \
    "s_waitcnt vmcnt(30) lgkmcnt(0)\n\t" \
    "s_barrier\n\t" \
    "ds_read_b64_tr_b16 a[40:41], v201 offset:16384\n\t" \
    "ds_read_b64_tr_b16 a[42:43], v201 offset:17408\n\t" \
    "ds_read_b64_tr_b16 a[44:45], v201 offset:24576\n\t" \
    "ds_read_b64_tr_b16 a[46:47], v201 offset:25600\n\t" \
    "ds_read_b64_tr_b16 a[112:113], v202 offset:16384\n\t" \
    "ds_read_b64_tr_b16 a[114:115], v202 offset:17408\n\t" \
    "ds_read_b64_tr_b16 a[116:117], v202 offset:24576\n\t" \
    "ds_read_b64_tr_b16 a[118:119], v202 offset:25600\n\t" \
    "s_waitcnt vmcnt(26) lgkmcnt(0)\n\t" \
    "s_barrier\n\t" \
    "ds_read_b64_tr_b16 a[48:49], v201 offset:32768\n\t" \
    "ds_read_b64_tr_b16 a[50:51], v201 offset:33792\n\t" \
    "ds_read_b64_tr_b16 a[52:53], v201 offset:40960\n\t" \
    "ds_read_b64_tr_b16 a[54:55], v201 offset:41984\n\t" \
    "ds_read_b64_tr_b16 a[120:121], v202 offset:32768\n\t" \
    "ds_read_b64_tr_b16 a[122:123], v202 offset:33792\n\t" \
    "ds_read_b64_tr_b16 a[124:125], v202 offset:40960\n\t" \
    "ds_read_b64_tr_b16 a[126:127], v202 offset:41984\n\t" \
    "s_waitcnt vmcnt(22) lgkmcnt(0)\n\t" \
    "s_barrier\n\t" \
    "ds_read_b64_tr_b16 a[56:57], v201 offset:49152\n\t" \
    "ds_read_b64_tr_b16 a[58:59], v201 offset:50176\n\t" \
    "ds_read_b64_tr_b16 a[60:61], v201 offset:57344\n\t" \
    "ds_read_b64_tr_b16 a[62:63], v201 offset:58368\n\t" \
    "ds_read_b64_tr_b16 a[128:129], v202 offset:49152\n\t" \
    "ds_read_b64_tr_b16 a[130:131], v202 offset:50176\n\t" \
    "ds_read_b64_tr_b16 a[132:133], v202 offset:57344\n\t" \
    "ds_read_b64_tr_b16 a[134:135], v202 offset:58368\n\t" \
    "s_waitcnt vmcnt(18) lgkmcnt(0)\n\t" \
    "s_barrier\n\t" \
    "v_add_u32 v201, 0x10000, v201\n\t" \
    "v_add_u32 v202, 0x10000, v202\n\t" \
    "ds_read_b64_tr_b16 a[64:65], v201 offset:0\n\t" \
    "ds_read_b64_tr_b16 a[66:67], v201 offset:1024\n\t" \
    "ds_read_b64_tr_b16 a[68:69], v201 offset:8192\n\t" \
    "ds_read_b64_tr_b16 a[70:71], v201 offset:9216\n\t" \
    "ds_read_b64_tr_b16 a[136:137], v202 offset:0\n\t" \
    "ds_read_b64_tr_b16 a[138:139], v202 offset:1024\n\t" \
    "ds_read_b64_tr_b16 a[140:141], v202 offset:8192\n\t" \
    "ds_read_b64_tr_b16 a[142:143], v202 offset:9216\n\t" \
    "v_add_u32 v201, 0xfffe0000, v201\n\t" \
    "v_add_u32 v202, 0xfffe0000, v202\n\t" \
    "s_waitcnt lgkmcnt(0)\n\t" \
    "s_barrier\n\t" \
    "s_waitcnt vmcnt(0)\n\t" \
    "ds_write_b128 v3, v[120:123] offset:0\n\t" \
    "ds_write_b128 v3, v[124:127] offset:1024\n\t" \
    "ds_write_b128 v3, v[128:131] offset:2048\n\t" \
    "ds_write_b128 v3, v[132:135] offset:3072\n\t" \
    "ds_write_b128 v3, v[136:139] offset:4096\n\t" \
    "ds_write_b128 v3, v[140:143] offset:5120\n\t" \
    "ds_write_b128 v3, v[144:147] offset:6144\n\t" \
    "ds_write_b128 v3, v[148:151] offset:7168\n\t" \
    "ds_write_b128 v3, v[152:155] offset:8192\n\t" \
    "ds_write_b128 v3, v[72:75] offset:36864\n\t" \
    "ds_write_b128 v3, v[76:79] offset:37888\n\t" \
    "ds_write_b128 v3, v[80:83] offset:38912\n\t" \
    "ds_write_b128 v3, v[84:87] offset:39936\n\t" \
    "ds_write_b128 v3, v[88:91] offset:40960\n\t" \
    "ds_write_b128 v3, v[92:95] offset:41984\n\t" \
    "ds_write_b128 v3, v[96:99] offset:43008\n\t" \
    "ds_write_b128 v3, v[100:103] offset:44032\n\t" \
    "ds_write_b128 v3, v[104:107] offset:45056\n\t" \
    "s_mov_b32 s65, 0\n\t" \
    "v_mov_b32 v1, v0\n\t" \
    "v_xor_b32 v2, 64, v0\n\t" \
    "s_mov_b32 s90, 0\n\t" \
    "s_add_u32 s91, s89, 0x12000\n\t" \
    "s_add_u32 s85, s91, s89\n\t" \
    "s_add_u32 s52, s50, 36864\n\t" \
    "s_addc_u32 s53, s51, 0\n\t" \
    "s_mul_i32 s69, s67, 8192\n\t" \
    "s_add_u32 s69, s69, 175112192\n\t" \
    "s_add_u32 s42, s36, s69\n\t" \
    "s_addc_u32 s43, s37, 0\n\t" \
    "s_mov_b64 s[40:41], s[42:43]\n\t" \
    "s_waitcnt lgkmcnt(0)\n\t" \
    "s_barrier\n\t" \
    "ds_read_b128 v[24:27], v1 offset:0\n\t" \
    "ds_read_b128 v[28:31], v1 offset:18432\n\t" \
    "ds_read_b128 v[32:35], v2 offset:0\n\t" \
    "ds_read_b128 v[36:39], v2 offset:18432\n\t" \
    "ds_read_b128 v[40:43], v1 offset:128\n\t" \
    "ds_read_b128 v[44:47], v1 offset:18560\n\t" \
    ".p2align 6\n\t" \
    "s_nop 0\n\t" \
    "s_nop 0\n\t" \
    "gx_l28_%=:\n\t" \
    "s_waitcnt lgkmcnt(5)\n\t" \
    "v_mfma_f32_16x16x32_bf16 v[88:91], a[0:3], v[24:27], 0\n\t" \
    "ds_read_b128 v[48:51], v2 offset:128\n\t" \
    "s_waitcnt lgkmcnt(5)\n\t" \
    "v_mfma_f32_16x16x32_bf16 v[92:95], a[0:3], v[28:31], 0\n\t" \
    "ds_read_b128 v[52:55], v2 offset:18560\n\t" \
    "v_mfma_f32_16x16x32_bf16 v[96:99], a[72:75], v[24:27], 0\n\t" \
    "s_add_u32 m0, s91, 0\n\t" \
    "v_mfma_f32_16x16x32_bf16 v[100:103], a[72:75], v[28:31], 0\n\t" \
    "global_load_lds_dwordx4 v203, s[52:53]\n\t" \
    "s_waitcnt lgkmcnt(5)\n\t" \
    "v_mfma_f32_16x16x32_bf16 v[88:91], a[4:7], v[32:35], v[88:91]\n\t" \
    "ds_read_b128 v[56:59], v1 offset:256\n\t" \
    "s_waitcnt lgkmcnt(5)\n\t" \
    "v_mfma_f32_16x16x32_bf16 v[92:95], a[4:7], v[36:39], v[92:95]\n\t" \
    "ds_read_b128 v[60:63], v1 offset:18688\n\t" \
    "v_mfma_f32_16x16x32_bf16 v[96:99], a[76:79], v[32:35], v[96:99]\n\t" \
    "s_add_u32 m0, s91, 1024\n\t" \
    "v_mfma_f32_16x16x32_bf16 v[100:103], a[76:79], v[36:39], v[100:103]\n\t" \
    "global_load_lds_dwordx4 v204, s[52:53]\n\t" \
    "s_waitcnt lgkmcnt(5)\n\t" \
    "v_mfma_f32_16x16x32_bf16 v[88:91], a[8:11], v[40:43], v[88:91]\n\t" \
    "ds_read_b128 v[64:67], v2 offset:256\n\t" \
    "s_waitcnt lgkmcnt(5)\n\t" \
    "v_mfma_f32_16x16x32_bf16 v[92:95], a[8:11], v[44:47], v[92:95]\n\t" \
    "ds_read_b128 v[68:71], v2 offset:18688\n\t" \
    "v_cvt_pk_bf16_f32 v104, v72, v73\n\t" \
    "v_mfma_f32_16x16x32_bf16 v[96:99], a[80:83], v[40:43], v[96:99]\n\t" \
    "s_add_u32 m0, s91, 2048\n\t" \
    "v_cvt_pk_bf16_f32 v105, v74, v75\n\t" \
    "v_mfma_f32_16x16x32_bf16 v[100:103], a[80:83], v[44:47], v[100:103]\n\t" \
    "global_load_lds_dwordx4 v205, s[52:53]\n\t" \
    "s_waitcnt lgkmcnt(5)\n\t" \
    "v_mfma_f32_16x16x32_bf16 v[88:91], a[12:15], v[48:51], v[88:91]\n\t" \
    "ds_read_b128 v[24:27], v1 offset:384\n\t" \
    "s_waitcnt lgkmcnt(5)\n\t" \
    "v_mfma_f32_16x16x32_bf16 v[92:95], a[12:15], v[52:55], v[92:95]\n\t" \
    "ds_read_b128 v[28:31], v1 offset:18816\n\t" \
    "v_cvt_pk_bf16_f32 v106, v80, v81\n\t" \
    "v_mfma_f32_16x16x32_bf16 v[96:99], a[84:87], v[48:51], v[96:99]\n\t" \
    "s_add_u32 m0, s91, 3072\n\t" \
    "v_cvt_pk_bf16_f32 v107, v82, v83\n\t" \
    "v_mfma_f32_16x16x32_bf16 v[100:103], a[84:87], v[52:55], v[100:103]\n\t" \
    "global_load_lds_dwordx4 v206, s[52:53]\n\t" \
    "s_waitcnt lgkmcnt(5)\n\t" \
    "v_mfma_f32_16x16x32_bf16 v[88:91], a[16:19], v[56:59], v[88:91]\n\t" \
    "ds_read_b128 v[32:35], v2 offset:384\n\t" \
    "s_waitcnt lgkmcnt(5)\n\t" \
    "v_mfma_f32_16x16x32_bf16 v[92:95], a[16:19], v[60:63], v[92:95]\n\t" \
    "ds_read_b128 v[36:39], v2 offset:18816\n\t" \
    "global_store_dwordx4 v8, v[104:107], s[40:41] offset:0 sc1\n\t" \
    "v_mfma_f32_16x16x32_bf16 v[96:99], a[88:91], v[56:59], v[96:99]\n\t" \
    "s_add_u32 m0, s91, 4096\n\t" \
    "v_cvt_pk_bf16_f32 v108, v76, v77\n\t" \
    "v_mfma_f32_16x16x32_bf16 v[100:103], a[88:91], v[60:63], v[100:103]\n\t" \
    "global_load_lds_dwordx4 v207, s[52:53]\n\t" \
    "s_waitcnt lgkmcnt(5)\n\t" \
    "v_mfma_f32_16x16x32_bf16 v[88:91], a[20:23], v[64:67], v[88:91]\n\t" \
    "ds_read_b128 v[40:43], v1 offset:512\n\t" \
    "s_waitcnt lgkmcnt(5)\n\t" \
    "v_mfma_f32_16x16x32_bf16 v[92:95], a[20:23], v[68:71], v[92:95]\n\t" \
    "ds_read_b128 v[44:47], v1 offset:18944\n\t" \
    "v_cvt_pk_bf16_f32 v109, v78, v79\n\t" \
    "v_mfma_f32_16x16x32_bf16 v[96:99], a[92:95], v[64:67], v[96:99]\n\t" \
    "s_add_u32 m0, s91, 5120\n\t" \
    "v_cvt_pk_bf16_f32 v110, v84, v85\n\t" \
    "v_mfma_f32_16x16x32_bf16 v[100:103], a[92:95], v[68:71], v[100:103]\n\t" \
    "global_load_lds_dwordx4 v208, s[52:53]\n\t" \
    "s_waitcnt lgkmcnt(5)\n\t" \
    "v_mfma_f32_16x16x32_bf16 v[88:91], a[24:27], v[24:27], v[88:91]\n\t" \
    "ds_read_b128 v[48:51], v2 offset:512\n\t" \
    "s_waitcnt lgkmcnt(5)\n\t" \
    "v_mfma_f32_16x16x32_bf16 v[92:95], a[24:27], v[28:31], v[92:95]\n\t" \
    "ds_read_b128 v[52:55], v2 offset:18944\n\t" \
    "v_cvt_pk_bf16_f32 v111, v86, v87\n\t" \
    "v_mfma_f32_16x16x32_bf16 v[96:99], a[96:99], v[24:27], v[96:99]\n\t" \
    "s_add_u32 m0, s91, 6144\n\t" \
    "global_store_dwordx4 v8, v[108:111], s[40:41] offset:1024 sc1\n\t" \
    "v_mfma_f32_16x16x32_bf16 v[100:103], a[96:99], v[28:31], v[100:103]\n\t" \
    "global_load_lds_dwordx4 v209, s[52:53]\n\t" \
    "s_waitcnt lgkmcnt(5)\n\t" \
    "v_mfma_f32_16x16x32_bf16 v[88:91], a[28:31], v[32:35], v[88:91]\n\t" \
    "ds_read_b128 v[56:59], v1 offset:640\n\t" \
    "s_waitcnt lgkmcnt(5)\n\t" \
    "v_mfma_f32_16x16x32_bf16 v[92:95], a[28:31], v[36:39], v[92:95]\n\t" \
    "ds_read_b128 v[60:63], v1 offset:19072\n\t" \
    "v_mfma_f32_16x16x32_bf16 v[96:99], a[100:103], v[32:35], v[96:99]\n\t" \
    "s_add_u32 m0, s91, 7168\n\t" \
    "v_mfma_f32_16x16x32_bf16 v[100:103], a[100:103], v[36:39], v[100:103]\n\t" \
    "global_load_lds_dwordx4 v210, s[52:53]\n\t" \
    "s_waitcnt lgkmcnt(5)\n\t" \
    "v_mfma_f32_16x16x32_bf16 v[88:91], a[32:35], v[40:43], v[88:91]\n\t" \
    "ds_read_b128 v[64:67], v2 offset:640\n\t" \
    "s_waitcnt lgkmcnt(5)\n\t" \
    "v_mfma_f32_16x16x32_bf16 v[92:95], a[32:35], v[44:47], v[92:95]\n\t" \
    "ds_read_b128 v[68:71], v2 offset:19072\n\t" \
    "v_mfma_f32_16x16x32_bf16 v[96:99], a[104:107], v[40:43], v[96:99]\n\t" \
    "s_add_u32 m0, s91, 8192\n\t" \
    "v_mfma_f32_16x16x32_bf16 v[100:103], a[104:107], v[44:47], v[100:103]\n\t" \
    "global_load_lds_dwordx4 v211, s[52:53]\n\t" \
    "s_waitcnt lgkmcnt(5)\n\t" \
    "v_mfma_f32_16x16x32_bf16 v[88:91], a[36:39], v[48:51], v[88:91]\n\t" \
    "ds_read_b128 v[24:27], v1 offset:768\n\t" \
    "s_waitcnt lgkmcnt(5)\n\t" \
    "v_mfma_f32_16x16x32_bf16 v[92:95], a[36:39], v[52:55], v[92:95]\n\t" \
    "ds_read_b128 v[28:31], v1 offset:19200\n\t" \
    "v_mfma_f32_16x16x32_bf16 v[96:99], a[108:111], v[48:51], v[96:99]\n\t" \
    "v_mfma_f32_16x16x32_bf16 v[100:103], a[108:111], v[52:55], v[100:103]\n\t" \
    "s_waitcnt lgkmcnt(5)\n\t" \
    "v_mfma_f32_16x16x32_bf16 v[88:91], a[40:43], v[56:59], v[88:91]\n\t" \
    "ds_read_b128 v[32:35], v2 offset:768\n\t" \
    "s_waitcnt lgkmcnt(5)\n\t" \
    "v_mfma_f32_16x16x32_bf16 v[92:95], a[40:43], v[60:63], v[92:95]\n\t" \
    "ds_read_b128 v[36:39], v2 offset:19200\n\t" \
    "v_mfma_f32_16x16x32_bf16 v[96:99], a[112:115], v[56:59], v[96:99]\n\t" \
    "s_waitcnt vmcnt(11)\n\t" \
    "s_barrier\n\t" \
    "v_mfma_f32_16x16x32_bf16 v[100:103], a[112:115], v[60:63], v[100:103]\n\t" \
    "s_cmp_lg_u32 s66, 0\n\t" \
    "s_cbranch_scc1 gx_SF12_%=\n\t" \
    "s_lshl_b32 s69, s67, 7\n\t" \
    "s_add_u32 s69, s69, 5849088\n\t" \
    "s_add_u32 s70, s65, -1\n\t" \
    "v_mov_b32 v156, s69\n\t" \
    "v_mov_b32 v157, s70\n\t" \
    "s_mov_b64 exec, 1\n\t" \
    "global_store_dword v156, v157, s[36:37] sc1\n\t" \
    "s_mov_b64 exec, -1\n\t" \
    "gx_SF12_%=:\n\t" \
    "s_waitcnt lgkmcnt(5)\n\t" \
    "v_mfma_f32_16x16x32_bf16 v[88:91], a[44:47], v[64:67], v[88:91]\n\t" \
    "ds_read_b128 v[40:43], v1 offset:896\n\t" \
    "s_waitcnt lgkmcnt(5)\n\t" \
    "v_mfma_f32_16x16x32_bf16 v[92:95], a[44:47], v[68:71], v[92:95]\n\t" \
    "ds_read_b128 v[44:47], v1 offset:19328\n\t" \
    "v_mfma_f32_16x16x32_bf16 v[96:99], a[116:119], v[64:67], v[96:99]\n\t" \
    "v_add_u32 v22, 0x9000, v1\n\t" \
    "v_add_u32 v23, 0x9000, v2\n\t" \
    "v_mfma_f32_16x16x32_bf16 v[100:103], a[116:119], v[68:71], v[100:103]\n\t" \
    "s_add_u32 s84, s65, 11\n\t" \
    "s_min_u32 s69, s84, 63\n\t" \
    "s_waitcnt lgkmcnt(5)\n\t" \
    "v_mfma_f32_16x16x32_bf16 v[88:91], a[48:51], v[24:27], v[88:91]\n\t" \
    "ds_read_b128 v[48:51], v2 offset:896\n\t" \
    "s_waitcnt lgkmcnt(5)\n\t" \
    "v_mfma_f32_16x16x32_bf16 v[92:95], a[48:51], v[28:31], v[92:95]\n\t" \
    "ds_read_b128 v[52:55], v2 offset:19328\n\t" \
    "v_mfma_f32_16x16x32_bf16 v[96:99], a[120:123], v[24:27], v[96:99]\n\t" \
    "s_mul_i32 s69, s69, 36864\n\t" \
    "s_add_u32 s48, s50, s69\n\t" \
    "v_mfma_f32_16x16x32_bf16 v[100:103], a[120:123], v[28:31], v[100:103]\n\t" \
    "s_addc_u32 s49, s51, 0\n\t" \
    "s_mul_i32 s70, s67, 7\n\t" \
    "s_waitcnt lgkmcnt(5)\n\t" \
    "v_mfma_f32_16x16x32_bf16 v[88:91], a[52:55], v[32:35], v[88:91]\n\t" \
    "ds_read_b128 v[56:59], v1 offset:1024\n\t" \
    "s_waitcnt lgkmcnt(5)\n\t" \
    "v_mfma_f32_16x16x32_bf16 v[92:95], a[52:55], v[36:39], v[92:95]\n\t" \
    "ds_read_b128 v[60:63], v1 offset:19456\n\t" \
    "v_mfma_f32_16x16x32_bf16 v[96:99], a[124:127], v[32:35], v[96:99]\n\t" \
    "s_add_u32 s69, s65, 0\n\t" \
    "s_xor_b32 s69, s69, s70\n\t" \
    "v_mfma_f32_16x16x32_bf16 v[100:103], a[124:127], v[36:39], v[100:103]\n\t" \
    "s_and_b32 s69, s69, 63\n\t" \
    "s_lshl_b32 s69, s69, 13\n\t" \
    "s_waitcnt lgkmcnt(5)\n\t" \
    "v_mfma_f32_16x16x32_bf16 v[88:91], a[56:59], v[40:43], v[88:91]\n\t" \
    "ds_read_b128 v[64:67], v2 offset:1024\n\t" \
    "s_waitcnt lgkmcnt(5)\n\t" \
    "v_mfma_f32_16x16x32_bf16 v[92:95], a[56:59], v[44:47], v[92:95]\n\t" \
    "ds_read_b128 v[68:71], v2 offset:19456\n\t" \
    "v_mfma_f32_16x16x32_bf16 v[96:99], a[128:131], v[40:43], v[96:99]\n\t" \
    "s_mul_i32 s70, s67, 524288\n\t" \
    "s_add_u32 s69, s69, s70\n\t" \
    "v_mfma_f32_16x16x32_bf16 v[100:103], a[128:131], v[44:47], v[100:103]\n\t" \
    "s_add_u32 s69, s69, 6291456\n\t" \
    "s_add_u32 s38, s36, s69\n\t" \
    "s_waitcnt lgkmcnt(5)\n\t" \
    "v_mfma_f32_16x16x32_bf16 v[88:91], a[60:63], v[48:51], v[88:91]\n\t" \
    "ds_read_b128 v[24:27], v22 offset:0\n\t" \
    "s_waitcnt lgkmcnt(5)\n\t" \
    "v_mfma_f32_16x16x32_bf16 v[92:95], a[60:63], v[52:55], v[92:95]\n\t" \
    "ds_read_b128 v[28:31], v22 offset:18432\n\t" \
    "v_mfma_f32_16x16x32_bf16 v[96:99], a[132:135], v[48:51], v[96:99]\n\t" \
    "s_addc_u32 s39, s37, 0\n\t" \
    "v_mfma_f32_16x16x32_bf16 v[100:103], a[132:135], v[52:55], v[100:103]\n\t" \
    "s_waitcnt lgkmcnt(5)\n\t" \
    "v_mfma_f32_16x16x32_bf16 v[88:91], a[64:67], v[56:59], v[88:91]\n\t" \
    "ds_read_b128 v[32:35], v23 offset:0\n\t" \
    "s_waitcnt lgkmcnt(5)\n\t" \
    "v_mfma_f32_16x16x32_bf16 v[92:95], a[64:67], v[60:63], v[92:95]\n\t" \
    "ds_read_b128 v[36:39], v23 offset:18432\n\t" \
    "v_mfma_f32_16x16x32_bf16 v[96:99], a[136:139], v[56:59], v[96:99]\n\t" \
    "v_mfma_f32_16x16x32_bf16 v[100:103], a[136:139], v[60:63], v[100:103]\n\t" \
    "s_waitcnt lgkmcnt(5)\n\t" \
    "v_mfma_f32_16x16x32_bf16 v[88:91], a[68:71], v[64:67], v[88:91]\n\t" \
    "ds_read_b128 v[40:43], v22 offset:128\n\t" \
    "s_waitcnt lgkmcnt(5)\n\t" \
    "v_mfma_f32_16x16x32_bf16 v[92:95], a[68:71], v[68:71], v[92:95]\n\t" \
    "ds_read_b128 v[44:47], v22 offset:18560\n\t" \
    "v_mfma_f32_16x16x32_bf16 v[96:99], a[140:143], v[64:67], v[96:99]\n\t" \
    "v_mfma_f32_16x16x32_bf16 v[100:103], a[140:143], v[68:71], v[100:103]\n\t" \
    "s_waitcnt lgkmcnt(5)\n\t" \
    "v_mfma_f32_16x16x32_bf16 v[72:75], a[144:147], v[24:27], 0\n\t" \
    "ds_read_b128 v[48:51], v23 offset:128\n\t" \
    "s_waitcnt lgkmcnt(5)\n\t" \
    "v_mfma_f32_16x16x32_bf16 v[76:79], a[144:147], v[28:31], 0\n\t" \
    "ds_read_b128 v[52:55], v23 offset:18560\n\t" \
    "v_mfma_f32_16x16x32_bf16 v[80:83], a[216:219], v[24:27], 0\n\t" \
    "s_add_u32 m0, s91, 36864\n\t" \
    "v_mfma_f32_16x16x32_bf16 v[84:87], a[216:219], v[28:31], 0\n\t" \
    "global_load_lds_dwordx4 v203, s[48:49]\n\t" \
    "s_waitcnt lgkmcnt(5)\n\t" \
    "v_mfma_f32_16x16x32_bf16 v[72:75], a[148:151], v[32:35], v[72:75]\n\t" \
    "ds_read_b128 v[56:59], v22 offset:256\n\t" \
    "s_waitcnt lgkmcnt(5)\n\t" \
    "v_mfma_f32_16x16x32_bf16 v[76:79], a[148:151], v[36:39], v[76:79]\n\t" \
    "ds_read_b128 v[60:63], v22 offset:18688\n\t" \
    "v_mfma_f32_16x16x32_bf16 v[80:83], a[220:223], v[32:35], v[80:83]\n\t" \
    "s_add_u32 m0, s91, 37888\n\t" \
    "v_mfma_f32_16x16x32_bf16 v[84:87], a[220:223], v[36:39], v[84:87]\n\t" \
    "global_load_lds_dwordx4 v204, s[48:49]\n\t" \
    "s_waitcnt lgkmcnt(5)\n\t" \
    "v_mfma_f32_16x16x32_bf16 v[72:75], a[152:155], v[40:43], v[72:75]\n\t" \
    "ds_read_b128 v[64:67], v23 offset:256\n\t" \
    "s_waitcnt lgkmcnt(5)\n\t" \
    "v_mfma_f32_16x16x32_bf16 v[76:79], a[152:155], v[44:47], v[76:79]\n\t" \
    "ds_read_b128 v[68:71], v23 offset:18688\n\t" \
    "v_cvt_pk_bf16_f32 v104, v88, v89\n\t" \
    "v_mfma_f32_16x16x32_bf16 v[80:83], a[224:227], v[40:43], v[80:83]\n\t" \
    "s_add_u32 m0, s91, 38912\n\t" \
    "v_cvt_pk_bf16_f32 v105, v90, v91\n\t" \
    "v_mfma_f32_16x16x32_bf16 v[84:87], a[224:227], v[44:47], v[84:87]\n\t" \
    "global_load_lds_dwordx4 v205, s[48:49]\n\t" \
    "s_waitcnt lgkmcnt(5)\n\t" \
    "v_mfma_f32_16x16x32_bf16 v[72:75], a[156:159], v[48:51], v[72:75]\n\t" \
    "ds_read_b128 v[24:27], v22 offset:384\n\t" \
    "s_waitcnt lgkmcnt(5)\n\t" \
    "v_mfma_f32_16x16x32_bf16 v[76:79], a[156:159], v[52:55], v[76:79]\n\t" \
    "ds_read_b128 v[28:31], v22 offset:18816\n\t" \
    "v_cvt_pk_bf16_f32 v106, v96, v97\n\t" \
    "v_mfma_f32_16x16x32_bf16 v[80:83], a[228:231], v[48:51], v[80:83]\n\t" \
    "s_add_u32 m0, s91, 39936\n\t" \
    "v_cvt_pk_bf16_f32 v107, v98, v99\n\t" \
    "v_mfma_f32_16x16x32_bf16 v[84:87], a[228:231], v[52:55], v[84:87]\n\t" \
    "global_load_lds_dwordx4 v206, s[48:49]\n\t" \
    "s_waitcnt lgkmcnt(5)\n\t" \
    "v_mfma_f32_16x16x32_bf16 v[72:75], a[160:163], v[56:59], v[72:75]\n\t" \
    "ds_read_b128 v[32:35], v23 offset:384\n\t" \
    "s_waitcnt lgkmcnt(5)\n\t" \
    "v_mfma_f32_16x16x32_bf16 v[76:79], a[160:163], v[60:63], v[76:79]\n\t" \
    "ds_read_b128 v[36:39], v23 offset:18816\n\t" \
    "global_store_dwordx4 v8, v[104:107], s[38:39] offset:0 sc1\n\t" \
    "v_mfma_f32_16x16x32_bf16 v[80:83], a[232:235], v[56:59], v[80:83]\n\t" \
    "s_add_u32 m0, s91, 40960\n\t" \
    "v_cvt_pk_bf16_f32 v108, v92, v93\n\t" \
    "v_mfma_f32_16x16x32_bf16 v[84:87], a[232:235], v[60:63], v[84:87]\n\t" \
    "global_load_lds_dwordx4 v207, s[48:49]\n\t" \
    "s_waitcnt lgkmcnt(5)\n\t" \
    "v_mfma_f32_16x16x32_bf16 v[72:75], a[164:167], v[64:67], v[72:75]\n\t" \
    "ds_read_b128 v[40:43], v22 offset:512\n\t" \
    "s_waitcnt lgkmcnt(5)\n\t" \
    "v_mfma_f32_16x16x32_bf16 v[76:79], a[164:167], v[68:71], v[76:79]\n\t" \
    "ds_read_b128 v[44:47], v22 offset:18944\n\t" \
    "v_cvt_pk_bf16_f32 v109, v94, v95\n\t" \
    "v_mfma_f32_16x16x32_bf16 v[80:83], a[236:239], v[64:67], v[80:83]\n\t" \
    "s_add_u32 m0, s91, 41984\n\t" \
    "v_cvt_pk_bf16_f32 v110, v100, v101\n\t" \
    "v_mfma_f32_16x16x32_bf16 v[84:87], a[236:239], v[68:71], v[84:87]\n\t" \
    "global_load_lds_dwordx4 v208, s[48:49]\n\t" \
    "s_waitcnt lgkmcnt(5)\n\t" \
    "v_mfma_f32_16x16x32_bf16 v[72:75], a[168:171], v[24:27], v[72:75]\n\t" \
    "ds_read_b128 v[48:51], v23 offset:512\n\t" \
    "s_waitcnt lgkmcnt(5)\n\t" \
    "v_mfma_f32_16x16x32_bf16 v[76:79], a[168:171], v[28:31], v[76:79]\n\t" \
    "ds_read_b128 v[52:55], v23 offset:18944\n\t" \
    "v_cvt_pk_bf16_f32 v111, v102, v103\n\t" \
    "v_mfma_f32_16x16x32_bf16 v[80:83], a[240:243], v[24:27], v[80:83]\n\t" \
    "s_add_u32 m0, s91, 43008\n\t" \
    "global_store_dwordx4 v8, v[108:111], s[38:39] offset:1024 sc1\n\t" \
    "v_mfma_f32_16x16x32_bf16 v[84:87], a[240:243], v[28:31], v[84:87]\n\t" \
    "global_load_lds_dwordx4 v209, s[48:49]\n\t" \
    "s_waitcnt lgkmcnt(5)\n\t" \
    "v_mfma_f32_16x16x32_bf16 v[72:75], a[172:175], v[32:35], v[72:75]\n\t" \
    "ds_read_b128 v[56:59], v22 offset:640\n\t" \
    "s_waitcnt lgkmcnt(5)\n\t" \
    "v_mfma_f32_16x16x32_bf16 v[76:79], a[172:175], v[36:39], v[76:79]\n\t" \
    "ds_read_b128 v[60:63], v22 offset:19072\n\t" \
    "v_mfma_f32_16x16x32_bf16 v[80:83], a[244:247], v[32:35], v[80:83]\n\t" \
    "s_add_u32 m0, s91, 44032\n\t" \
    "v_mfma_f32_16x16x32_bf16 v[84:87], a[244:247], v[36:39], v[84:87]\n\t" \
    "global_load_lds_dwordx4 v210, s[48:49]\n\t" \
    "s_waitcnt lgkmcnt(5)\n\t" \
    "v_mfma_f32_16x16x32_bf16 v[72:75], a[176:179], v[40:43], v[72:75]\n\t" \
    "ds_read_b128 v[64:67], v23 offset:640\n\t" \
    "s_waitcnt lgkmcnt(5)\n\t" \
    "v_mfma_f32_16x16x32_bf16 v[76:79], a[176:179], v[44:47], v[76:79]\n\t" \
    "ds_read_b128 v[68:71], v23 offset:19072\n\t" \
    "v_mfma_f32_16x16x32_bf16 v[80:83], a[248:251], v[40:43], v[80:83]\n\t" \
    "s_add_u32 m0, s91, 45056\n\t" \
    "v_mfma_f32_16x16x32_bf16 v[84:87], a[248:251], v[44:47], v[84:87]\n\t" \
    "global_load_lds_dwordx4 v211, s[48:49]\n\t" \
    "s_waitcnt lgkmcnt(5)\n\t" \
    "v_mfma_f32_16x16x32_bf16 v[72:75], a[180:183], v[48:51], v[72:75]\n\t" \
    "ds_read_b128 v[24:27], v22 offset:768\n\t" \
    "s_waitcnt lgkmcnt(5)\n\t" \
    "v_mfma_f32_16x16x32_bf16 v[76:79], a[180:183], v[52:55], v[76:79]\n\t" \
    "ds_read_b128 v[28:31], v22 offset:19200\n\t" \
    "v_mfma_f32_16x16x32_bf16 v[80:83], a[252:255], v[48:51], v[80:83]\n\t" \
    "v_mfma_f32_16x16x32_bf16 v[84:87], a[252:255], v[52:55], v[84:87]\n\t" \
    "s_waitcnt lgkmcnt(5)\n\t" \
    "v_mfma_f32_16x16x32_bf16 v[72:75], a[184:187], v[56:59], v[72:75]\n\t" \
    "ds_read_b128 v[32:35], v23 offset:768\n\t" \
    "s_waitcnt lgkmcnt(5)\n\t" \
    "v_mfma_f32_16x16x32_bf16 v[76:79], a[184:187], v[60:63], v[76:79]\n\t" \
    "ds_read_b128 v[36:39], v23 offset:19200\n\t" \
    "v_mfma_f32_16x16x32_bf16 v[80:83], v[224:227], v[56:59], v[80:83]\n\t" \
    "s_waitcnt vmcnt(11)\n\t" \
    "s_barrier\n\t" \
    "v_mfma_f32_16x16x32_bf16 v[84:87], v[224:227], v[60:63], v[84:87]\n\t" \
    "s_add_u32 s71, s65, 9\n\t" \
    "s_cmp_lg_u32 s66, 0\n\t" \
    "s_cbranch_scc1 gx_SF13_%=\n\t" \
    "s_lshl_b32 s69, s67, 7\n\t" \
    "s_add_u32 s69, s69, 5771264\n\t" \
    "s_add_u32 s70, s71, 0\n\t" \
    "v_mov_b32 v156, s69\n\t" \
    "v_mov_b32 v157, s70\n\t" \
    "s_mov_b64 exec, 1\n\t" \
    "global_store_dword v156, v157, s[36:37] sc1\n\t" \
    "s_mov_b64 exec, -1\n\t" \
    "gx_SF13_%=:\n\t" \
    "s_waitcnt lgkmcnt(5)\n\t" \
    "v_mfma_f32_16x16x32_bf16 v[72:75], a[188:191], v[64:67], v[72:75]\n\t" \
    "ds_read_b128 v[40:43], v22 offset:896\n\t" \
    "s_waitcnt lgkmcnt(5)\n\t" \
    "v_mfma_f32_16x16x32_bf16 v[76:79], a[188:191], v[68:71], v[76:79]\n\t" \
    "ds_read_b128 v[44:47], v22 offset:19328\n\t" \
    "v_mfma_f32_16x16x32_bf16 v[80:83], v[228:231], v[64:67], v[80:83]\n\t" \
    "s_xor_b32 s90, s90, 0x12000\n\t" \
    "v_add_u32 v1, s90, v0\n\t" \
    "v_mfma_f32_16x16x32_bf16 v[84:87], v[228:231], v[68:71], v[84:87]\n\t" \
    "v_xor_b32 v2, 64, v1\n\t" \
    "s_add_u32 s84, s65, 2\n\t" \
    "s_waitcnt lgkmcnt(5)\n\t" \
    "v_mfma_f32_16x16x32_bf16 v[72:75], a[192:195], v[24:27], v[72:75]\n\t" \
    "ds_read_b128 v[48:51], v23 offset:896\n\t" \
    "s_waitcnt lgkmcnt(5)\n\t" \
    "v_mfma_f32_16x16x32_bf16 v[76:79], a[192:195], v[28:31], v[76:79]\n\t" \
    "ds_read_b128 v[52:55], v23 offset:19328\n\t" \
    "v_mfma_f32_16x16x32_bf16 v[80:83], v[232:235], v[24:27], v[80:83]\n\t" \
    "s_min_u32 s69, s84, 63\n\t" \
    "s_mul_i32 s69, s69, 36864\n\t" \
    "v_mfma_f32_16x16x32_bf16 v[84:87], v[232:235], v[28:31], v[84:87]\n\t" \
    "s_add_u32 s52, s50, s69\n\t" \
    "s_addc_u32 s53, s51, 0\n\t" \
    "s_waitcnt lgkmcnt(5)\n\t" \
    "v_mfma_f32_16x16x32_bf16 v[72:75], a[196:199], v[32:35], v[72:75]\n\t" \
    "ds_read_b128 v[56:59], v22 offset:1024\n\t" \
    "s_waitcnt lgkmcnt(5)\n\t" \
    "v_mfma_f32_16x16x32_bf16 v[76:79], a[196:199], v[36:39], v[76:79]\n\t" \
    "ds_read_b128 v[60:63], v22 offset:19456\n\t" \
    "v_mfma_f32_16x16x32_bf16 v[80:83], v[236:239], v[32:35], v[80:83]\n\t" \
    "s_mul_i32 s70, s67, 7\n\t" \
    "s_add_u32 s69, s65, 10\n\t" \
    "v_mfma_f32_16x16x32_bf16 v[84:87], v[236:239], v[36:39], v[84:87]\n\t" \
    "s_xor_b32 s69, s69, s70\n\t" \
    "s_and_b32 s69, s69, 63\n\t" \
    "s_waitcnt lgkmcnt(5)\n\t" \
    "v_mfma_f32_16x16x32_bf16 v[72:75], a[200:203], v[40:43], v[72:75]\n\t" \
    "ds_read_b128 v[64:67], v23 offset:1024\n\t" \
    "s_waitcnt lgkmcnt(5)\n\t" \
    "v_mfma_f32_16x16x32_bf16 v[76:79], a[200:203], v[44:47], v[76:79]\n\t" \
    "ds_read_b128 v[68:71], v23 offset:19456\n\t" \
    "v_mfma_f32_16x16x32_bf16 v[80:83], v[240:243], v[40:43], v[80:83]\n\t" \
    "s_lshl_b32 s69, s69, 13\n\t" \
    "s_mul_i32 s70, s67, 524288\n\t" \
    "v_mfma_f32_16x16x32_bf16 v[84:87], v[240:243], v[44:47], v[84:87]\n\t" \
    "s_add_u32 s69, s69, s70\n\t" \
    "s_add_u32 s69, s69, 23068672\n\t" \
    "s_waitcnt lgkmcnt(5)\n\t" \
    "v_mfma_f32_16x16x32_bf16 v[72:75], a[204:207], v[48:51], v[72:75]\n\t" \
    "ds_read_b128 v[24:27], v1 offset:0\n\t" \
    "s_waitcnt lgkmcnt(5)\n\t" \
    "v_mfma_f32_16x16x32_bf16 v[76:79], a[204:207], v[52:55], v[76:79]\n\t" \
    "ds_read_b128 v[28:31], v1 offset:18432\n\t" \
    "v_mfma_f32_16x16x32_bf16 v[80:83], v[244:247], v[48:51], v[80:83]\n\t" \
    "s_add_u32 s40, s36, s69\n\t" \
    "s_addc_u32 s41, s37, 0\n\t" \
    "v_mfma_f32_16x16x32_bf16 v[84:87], v[244:247], v[52:55], v[84:87]\n\t" \
    "s_cmp_lt_u32 s65, 54\n\t" \
    "s_cselect_b64 s[40:41], s[40:41], s[42:43]\n\t" \
    "s_waitcnt lgkmcnt(5)\n\t" \
    "v_mfma_f32_16x16x32_bf16 v[72:75], a[208:211], v[56:59], v[72:75]\n\t" \
    "ds_read_b128 v[32:35], v2 offset:0\n\t" \
    "s_waitcnt lgkmcnt(5)\n\t" \
    "v_mfma_f32_16x16x32_bf16 v[76:79], a[208:211], v[60:63], v[76:79]\n\t" \
    "ds_read_b128 v[36:39], v2 offset:18432\n\t" \
    "v_mfma_f32_16x16x32_bf16 v[80:83], v[248:251], v[56:59], v[80:83]\n\t" \
    "s_sub_u32 s91, s85, s91\n\t" \
    "s_add_u32 s65, s65, 1\n\t" \
    "v_mfma_f32_16x16x32_bf16 v[84:87], v[248:251], v[60:63], v[84:87]\n\t" \
    "s_waitcnt lgkmcnt(5)\n\t" \
    "v_mfma_f32_16x16x32_bf16 v[72:75], a[212:215], v[64:67], v[72:75]\n\t" \
    "ds_read_b128 v[40:43], v1 offset:128\n\t" \
    "s_waitcnt lgkmcnt(5)\n\t" \
    "v_mfma_f32_16x16x32_bf16 v[76:79], a[212:215], v[68:71], v[76:79]\n\t" \
    "ds_read_b128 v[44:47], v1 offset:18560\n\t" \
    "v_mfma_f32_16x16x32_bf16 v[80:83], v[252:255], v[64:67], v[80:83]\n\t" \
    "v_mfma_f32_16x16x32_bf16 v[84:87], v[252:255], v[68:71], v[84:87]\n\t" \
    "s_cmp_lt_u32 s65, 64\n\t" \
    "s_cbranch_scc1 gx_l28_%=\n\t" \
    "s_waitcnt vmcnt(0) lgkmcnt(0)\n\t" \
    "s_barrier\n\t" \
    "s_cmp_lg_u32 s66, 0\n\t" \
    "s_cbranch_scc1 gx_SF14_%=\n\t" \
    "s_lshl_b32 s69, s67, 7\n\t" \
    "s_add_u32 s69, s69, 5849088\n\t" \
    "s_add_u32 s70, s65, -1\n\t" \
    "v_mov_b32 v156, s69\n\t" \
    "v_mov_b32 v157, s70\n\t" \
    "s_mov_b64 exec, 1\n\t" \
    "global_store_dword v156, v157, s[36:37] sc1\n\t" \
    "s_mov_b64 exec, -1\n\t" \
    "gx_SF14_%=:\n\t" \
    "gx_last_%=:\n\t" \
    ""
#define ASM_GV \
    "s_load_dwordx2 s[42:43], %3, 56\n\t" \
    "s_load_dwordx2 s[44:45], %3, 64\n\t" \
    "s_load_dwordx2 s[36:37], %3, 0x58\n\t" \
    "s_load_dwordx2 s[50:51], %3, 0x48\n\t" \
    "s_mov_b32 s66, %4\n\t" \
    "s_mov_b32 s67, %5\n\t" \
    "s_mov_b32 s68, 262144\n\t" \
    "s_mov_b32 s72, 0\n\t" \
    "s_mov_b32 s83, -1\n\t" \
    "s_mov_b32 s65, 0\n\t" \
    "s_lshl_b32 s82, s66, 10\n\t" \
    "s_waitcnt lgkmcnt(0)\n\t" \
    "s_lshr_b32 s65, s67, 5\n\t" \
    "s_cselect_b32 s88, 24, 64\n\t" \
    "s_cmp_lt_u32 s65, 24\n\t" \
    "s_addc_u32 s84, s65, 1\n\t" \
    "s_cmp_lt_u32 s84, 24\n\t" \
    "s_addc_u32 s85, s84, 1\n\t" \
    "s_mov_b32 s86, -1\n\t" \
    "s_mov_b32 s87, -1\n\t" \
    "s_add_u32 s38, s36, 48234496\n\t" \
    "s_addc_u32 s39, s37, 0\n\t" \
    "s_and_b32 s69, s67, 31\n\t" \
    "s_mul_i32 s69, s69, 147456\n\t" \
    "s_add_u32 s44, s44, s69\n\t" \
    "s_addc_u32 s45, s45, 0\n\t" \
    "s_mul_i32 s89, s66, 9216\n\t" \
    "s_mov_b32 s90, 0\n\t" \
    "v_mov_b32 v196, -1\n\t" \
    "v_mov_b32 v197, -1\n\t" \
    "v_mov_b32 v198, -1\n\t" \
    "v_mov_b32 v199, -1\n\t" \
    "s_add_u32 s46, s42, 2097152\n\t" \
    "s_addc_u32 s47, s43, 0\n\t" \
    "s_add_u32 m0, s82, 65536\n\t" \
    "s_nop 0\n\t" \
    "global_load_lds_dwordx4 v200, s[46:47]\n\t" \
    "s_add_u32 s46, s42, 2228224\n\t" \
    "s_addc_u32 s47, s43, 0\n\t" \
    "s_add_u32 m0, s82, 69632\n\t" \
    "s_nop 0\n\t" \
    "global_load_lds_dwordx4 v200, s[46:47]\n\t" \
    "s_add_u32 s46, s42, 2359296\n\t" \
    "s_addc_u32 s47, s43, 0\n\t" \
    "s_add_u32 m0, s82, 73728\n\t" \
    "s_nop 0\n\t" \
    "global_load_lds_dwordx4 v200, s[46:47]\n\t" \
    "s_add_u32 s46, s42, 2490368\n\t" \
    "s_addc_u32 s47, s43, 0\n\t" \
    "s_add_u32 m0, s82, 77824\n\t" \
    "s_nop 0\n\t" \
    "global_load_lds_dwordx4 v200, s[46:47]\n\t" \
    "s_add_u32 s46, s42, 2621440\n\t" \
    "s_addc_u32 s47, s43, 0\n\t" \
    "s_add_u32 m0, s82, 81920\n\t" \
    "s_nop 0\n\t" \
    "global_load_lds_dwordx4 v200, s[46:47]\n\t" \
    "s_add_u32 s46, s42, 2752512\n\t" \
    "s_addc_u32 s47, s43, 0\n\t" \
    "s_add_u32 m0, s82, 86016\n\t" \
    "s_nop 0\n\t" \
    "global_load_lds_dwordx4 v200, s[46:47]\n\t" \
    "s_add_u32 s46, s42, 2883584\n\t" \
    "s_addc_u32 s47, s43, 0\n\t" \
    "s_add_u32 m0, s82, 90112\n\t" \
    "s_nop 0\n\t" \
    "global_load_lds_dwordx4 v200, s[46:47]\n\t" \
    "s_add_u32 s46, s42, 3014656\n\t" \
    "s_addc_u32 s47, s43, 0\n\t" \
    "s_add_u32 m0, s82, 94208\n\t" \
    "s_nop 0\n\t" \
    "global_load_lds_dwordx4 v200, s[46:47]\n\t" \
    "s_add_u32 s46, s42, 3145728\n\t" \
    "s_addc_u32 s47, s43, 0\n\t" \
    "s_add_u32 m0, s82, 98304\n\t" \
    "s_nop 0\n\t" \
    "global_load_lds_dwordx4 v200, s[46:47]\n\t" \
    "s_add_u32 s46, s42, 3276800\n\t" \
    "s_addc_u32 s47, s43, 0\n\t" \
    "s_add_u32 m0, s82, 102400\n\t" \
    "s_nop 0\n\t" \
    "global_load_lds_dwordx4 v200, s[46:47]\n\t" \
    "s_add_u32 s46, s42, 3407872\n\t" \
    "s_addc_u32 s47, s43, 0\n\t" \
    "s_add_u32 m0, s82, 106496\n\t" \
    "s_nop 0\n\t" \
    "global_load_lds_dwordx4 v200, s[46:47]\n\t" \
    "s_add_u32 s46, s42, 3538944\n\t" \
    "s_addc_u32 s47, s43, 0\n\t" \
    "s_add_u32 m0, s82, 110592\n\t" \
    "s_nop 0\n\t" \
    "global_load_lds_dwordx4 v200, s[46:47]\n\t" \
    "s_add_u32 s46, s42, 3670016\n\t" \
    "s_addc_u32 s47, s43, 0\n\t" \
    "s_add_u32 m0, s82, 114688\n\t" \
    "s_nop 0\n\t" \
    "global_load_lds_dwordx4 v200, s[46:47]\n\t" \
    "s_add_u32 s46, s42, 3801088\n\t" \
    "s_addc_u32 s47, s43, 0\n\t" \
    "s_add_u32 m0, s82, 118784\n\t" \
    "s_nop 0\n\t" \
    "global_load_lds_dwordx4 v200, s[46:47]\n\t" \
    "s_add_u32 s46, s42, 3932160\n\t" \
    "s_addc_u32 s47, s43, 0\n\t" \
    "s_add_u32 m0, s82, 122880\n\t" \
    "s_nop 0\n\t" \
    "global_load_lds_dwordx4 v200, s[46:47]\n\t" \
    "s_add_u32 s46, s42, 4063232\n\t" \
    "s_addc_u32 s47, s43, 0\n\t" \
    "s_add_u32 m0, s82, 126976\n\t" \
    "s_nop 0\n\t" \
    "global_load_lds_dwordx4 v200, s[46:47]\n\t" \
    "s_add_u32 s46, s42, 4194304\n\t" \
    "s_addc_u32 s47, s43, 0\n\t" \
    "s_add_u32 m0, s82, 131072\n\t" \
    "s_nop 0\n\t" \
    "global_load_lds_dwordx4 v200, s[46:47]\n\t" \
    "s_add_u32 s46, s42, 4325376\n\t" \
    "s_addc_u32 s47, s43, 0\n\t" \
    "s_add_u32 m0, s82, 135168\n\t" \
    "s_nop 0\n\t" \
    "global_load_lds_dwordx4 v200, s[46:47]\n\t" \
    "s_add_u32 s46, s42, 4456448\n\t" \
    "s_addc_u32 s47, s43, 0\n\t" \
    "s_add_u32 m0, s82, 139264\n\t" \
    "s_nop 0\n\t" \
    "global_load_lds_dwordx4 v200, s[46:47]\n\t" \
    "s_add_u32 s46, s42, 4587520\n\t" \
    "s_addc_u32 s47, s43, 0\n\t" \
    "s_add_u32 m0, s82, 143360\n\t" \
    "s_nop 0\n\t" \
    "global_load_lds_dwordx4 v200, s[46:47]\n\t" \
    "s_min_u32 s69, s65, 63\n\t" \
    "s_mul_i32 s69, s69, 36864\n\t" \
    "s_add_u32 s52, s50, s69\n\t" \
    "s_addc_u32 s53, s51, 0\n\t" \
    "global_load_dwordx4 v[120:123], v203, s[52:53]\n\t" \
    "global_load_dwordx4 v[124:127], v204, s[52:53]\n\t" \
    "global_load_dwordx4 v[128:131], v205, s[52:53]\n\t" \
    "global_load_dwordx4 v[132:135], v206, s[52:53]\n\t" \
    "global_load_dwordx4 v[136:139], v207, s[52:53]\n\t" \
    "global_load_dwordx4 v[140:143], v208, s[52:53]\n\t" \
    "global_load_dwordx4 v[144:147], v209, s[52:53]\n\t" \
    "global_load_dwordx4 v[148:151], v210, s[52:53]\n\t" \
    "global_load_dwordx4 v[152:155], v211, s[52:53]\n\t" \
    "s_min_u32 s69, s84, 63\n\t" \
    "s_mul_i32 s69, s69, 36864\n\t" \
    "s_add_u32 s52, s50, s69\n\t" \
    "s_addc_u32 s53, s51, 0\n\t" \
    "global_load_dwordx4 v[72:75], v203, s[52:53]\n\t" \
    "global_load_dwordx4 v[76:79], v204, s[52:53]\n\t" \
    "global_load_dwordx4 v[80:83], v205, s[52:53]\n\t" \
    "global_load_dwordx4 v[84:87], v206, s[52:53]\n\t" \
    "global_load_dwordx4 v[88:91], v207, s[52:53]\n\t" \
    "global_load_dwordx4 v[92:95], v208, s[52:53]\n\t" \
    "global_load_dwordx4 v[96:99], v209, s[52:53]\n\t" \
    "global_load_dwordx4 v[100:103], v210, s[52:53]\n\t" \
    "global_load_dwordx4 v[104:107], v211, s[52:53]\n\t" \
    "s_waitcnt vmcnt(50) lgkmcnt(0)\n\t" \
    "s_barrier\n\t" \
    "ds_read_b64_tr_b16 a[0:1], v201 offset:0\n\t" \
    "ds_read_b64_tr_b16 a[2:3], v201 offset:1024\n\t" \
    "ds_read_b64_tr_b16 a[4:5], v201 offset:8192\n\t" \
    "ds_read_b64_tr_b16 a[6:7], v201 offset:9216\n\t" \
    "ds_read_b64_tr_b16 a[72:73], v202 offset:0\n\t" \
    "ds_read_b64_tr_b16 a[74:75], v202 offset:1024\n\t" \
    "ds_read_b64_tr_b16 a[76:77], v202 offset:8192\n\t" \
    "ds_read_b64_tr_b16 a[78:79], v202 offset:9216\n\t" \
    "s_waitcnt vmcnt(46) lgkmcnt(0)\n\t" \
    "s_barrier\n\t" \
    "ds_read_b64_tr_b16 a[8:9], v201 offset:16384\n\t" \
    "ds_read_b64_tr_b16 a[10:11], v201 offset:17408\n\t" \
    "ds_read_b64_tr_b16 a[12:13], v201 offset:24576\n\t" \
    "ds_read_b64_tr_b16 a[14:15], v201 offset:25600\n\t" \
    "ds_read_b64_tr_b16 a[80:81], v202 offset:16384\n\t" \
    "ds_read_b64_tr_b16 a[82:83], v202 offset:17408\n\t" \
    "ds_read_b64_tr_b16 a[84:85], v202 offset:24576\n\t" \
    "ds_read_b64_tr_b16 a[86:87], v202 offset:25600\n\t" \
    "s_waitcnt vmcnt(42) lgkmcnt(0)\n\t" \
    "s_barrier\n\t" \
    "ds_read_b64_tr_b16 a[16:17], v201 offset:32768\n\t" \
    "ds_read_b64_tr_b16 a[18:19], v201 offset:33792\n\t" \
    "ds_read_b64_tr_b16 a[20:21], v201 offset:40960\n\t" \
    "ds_read_b64_tr_b16 a[22:23], v201 offset:41984\n\t" \
    "ds_read_b64_tr_b16 a[88:89], v202 offset:32768\n\t" \
    "ds_read_b64_tr_b16 a[90:91], v202 offset:33792\n\t" \
    "ds_read_b64_tr_b16 a[92:93], v202 offset:40960\n\t" \
    "ds_read_b64_tr_b16 a[94:95], v202 offset:41984\n\t" \
    "s_waitcnt vmcnt(38) lgkmcnt(0)\n\t" \
    "s_barrier\n\t" \
    "ds_read_b64_tr_b16 a[24:25], v201 offset:49152\n\t" \
    "ds_read_b64_tr_b16 a[26:27], v201 offset:50176\n\t" \
    "ds_read_b64_tr_b16 a[28:29], v201 offset:57344\n\t" \
    "ds_read_b64_tr_b16 a[30:31], v201 offset:58368\n\t" \
    "ds_read_b64_tr_b16 a[96:97], v202 offset:49152\n\t" \
    "ds_read_b64_tr_b16 a[98:99], v202 offset:50176\n\t" \
    "ds_read_b64_tr_b16 a[100:101], v202 offset:57344\n\t" \
    "ds_read_b64_tr_b16 a[102:103], v202 offset:58368\n\t" \
    "s_add_u32 s46, s44, 0\n\t" \
    "s_addc_u32 s47, s45, 0\n\t" \
    "s_mul_i32 s69, s82, 9\n\t" \
    "s_add_u32 m0, s69, 0\n\t" \
    "s_nop 0\n\t" \
    "global_load_lds_dwordx4 v203, s[46:47]\n\t" \
    "s_add_u32 m0, s69, 1024\n\t" \
    "s_nop 0\n\t" \
    "global_load_lds_dwordx4 v204, s[46:47]\n\t" \
    "s_add_u32 m0, s69, 2048\n\t" \
    "s_nop 0\n\t" \
    "global_load_lds_dwordx4 v205, s[46:47]\n\t" \
    "s_add_u32 m0, s69, 3072\n\t" \
    "s_nop 0\n\t" \
    "global_load_lds_dwordx4 v206, s[46:47]\n\t" \
    "s_add_u32 m0, s69, 4096\n\t" \
    "s_nop 0\n\t" \
    "global_load_lds_dwordx4 v207, s[46:47]\n\t" \
    "s_add_u32 m0, s69, 5120\n\t" \
    "s_nop 0\n\t" \
    "global_load_lds_dwordx4 v208, s[46:47]\n\t" \
    "s_add_u32 m0, s69, 6144\n\t" \
    "s_nop 0\n\t" \
    "global_load_lds_dwordx4 v209, s[46:47]\n\t" \
    "s_add_u32 m0, s69, 7168\n\t" \
    "s_nop 0\n\t" \
    "global_load_lds_dwordx4 v210, s[46:47]\n\t" \
    "s_add_u32 m0, s69, 8192\n\t" \
    "s_nop 0\n\t" \
    "global_load_lds_dwordx4 v211, s[46:47]\n\t" \
    "s_waitcnt vmcnt(43) lgkmcnt(0)\n\t" \
    "s_barrier\n\t" \
    "v_add_u32 v201, 0x10000, v201\n\t" \
    "v_add_u32 v202, 0x10000, v202\n\t" \
    "ds_read_b64_tr_b16 a[32:33], v201 offset:0\n\t" \
    "ds_read_b64_tr_b16 a[34:35], v201 offset:1024\n\t" \
    "ds_read_b64_tr_b16 a[36:37], v201 offset:8192\n\t" \
    "ds_read_b64_tr_b16 a[38:39], v201 offset:9216\n\t" \
    "ds_read_b64_tr_b16 a[104:105], v202 offset:0\n\t" \
    "ds_read_b64_tr_b16 a[106:107], v202 offset:1024\n\t" \
    "ds_read_b64_tr_b16 a[108:109], v202 offset:8192\n\t" \
    "ds_read_b64_tr_b16 a[110:111], v202 offset:9216\n\t" \
    "s_waitcnt vmcnt(39) lgkmcnt(0)\n\t" \
    "s_barrier\n\t" \
    "ds_read_b64_tr_b16 a[40:41], v201 offset:16384\n\t" \
    "ds_read_b64_tr_b16 a[42:43], v201 offset:17408\n\t" \
    "ds_read_b64_tr_b16 a[44:45], v201 offset:24576\n\t" \
    "ds_read_b64_tr_b16 a[46:47], v201 offset:25600\n\t" \
    "ds_read_b64_tr_b16 a[112:113], v202 offset:16384\n\t" \
    "ds_read_b64_tr_b16 a[114:115], v202 offset:17408\n\t" \
    "ds_read_b64_tr_b16 a[116:117], v202 offset:24576\n\t" \
    "ds_read_b64_tr_b16 a[118:119], v202 offset:25600\n\t" \
    "s_add_u32 s46, s44, 36864\n\t" \
    "s_addc_u32 s47, s45, 0\n\t" \
    "s_mul_i32 s69, s82, 9\n\t" \
    "s_add_u32 m0, s69, 36864\n\t" \
    "s_nop 0\n\t" \
    "global_load_lds_dwordx4 v203, s[46:47]\n\t" \
    "s_add_u32 m0, s69, 37888\n\t" \
    "s_nop 0\n\t" \
    "global_load_lds_dwordx4 v204, s[46:47]\n\t" \
    "s_add_u32 m0, s69, 38912\n\t" \
    "s_nop 0\n\t" \
    "global_load_lds_dwordx4 v205, s[46:47]\n\t" \
    "s_add_u32 m0, s69, 39936\n\t" \
    "s_nop 0\n\t" \
    "global_load_lds_dwordx4 v206, s[46:47]\n\t" \
    "s_add_u32 m0, s69, 40960\n\t" \
    "s_nop 0\n\t" \
    "global_load_lds_dwordx4 v207, s[46:47]\n\t" \
    "s_add_u32 m0, s69, 41984\n\t" \
    "s_nop 0\n\t" \
    "global_load_lds_dwordx4 v208, s[46:47]\n\t" \
    "s_add_u32 m0, s69, 43008\n\t" \
    "s_nop 0\n\t" \
    "global_load_lds_dwordx4 v209, s[46:47]\n\t" \
    "s_add_u32 m0, s69, 44032\n\t" \
    "s_nop 0\n\t" \
    "global_load_lds_dwordx4 v210, s[46:47]\n\t" \
    "s_add_u32 m0, s69, 45056\n\t" \
    "s_nop 0\n\t" \
    "global_load_lds_dwordx4 v211, s[46:47]\n\t" \
    "s_waitcnt vmcnt(44) lgkmcnt(0)\n\t" \
    "s_barrier\n\t" \
    "ds_read_b64_tr_b16 a[48:49], v201 offset:32768\n\t" \
    "ds_read_b64_tr_b16 a[50:51], v201 offset:33792\n\t" \
    "ds_read_b64_tr_b16 a[52:53], v201 offset:40960\n\t" \
    "ds_read_b64_tr_b16 a[54:55], v201 offset:41984\n\t" \
    "ds_read_b64_tr_b16 a[120:121], v202 offset:32768\n\t" \
    "ds_read_b64_tr_b16 a[122:123], v202 offset:33792\n\t" \
    "ds_read_b64_tr_b16 a[124:125], v202 offset:40960\n\t" \
    "ds_read_b64_tr_b16 a[126:127], v202 offset:41984\n\t" \
    "s_waitcnt vmcnt(40) lgkmcnt(0)\n\t" \
    "s_barrier\n\t" \
    "ds_read_b64_tr_b16 a[56:57], v201 offset:49152\n\t" \
    "ds_read_b64_tr_b16 a[58:59], v201 offset:50176\n\t" \
    "ds_read_b64_tr_b16 a[60:61], v201 offset:57344\n\t" \
    "ds_read_b64_tr_b16 a[62:63], v201 offset:58368\n\t" \
    "ds_read_b64_tr_b16 a[128:129], v202 offset:49152\n\t" \
    "ds_read_b64_tr_b16 a[130:131], v202 offset:50176\n\t" \
    "ds_read_b64_tr_b16 a[132:133], v202 offset:57344\n\t" \
    "ds_read_b64_tr_b16 a[134:135], v202 offset:58368\n\t" \
    "s_add_u32 s46, s44, 73728\n\t" \
    "s_addc_u32 s47, s45, 0\n\t" \
    "s_mul_i32 s69, s82, 9\n\t" \
    "s_add_u32 m0, s69, 73728\n\t" \
    "s_nop 0\n\t" \
    "global_load_lds_dwordx4 v203, s[46:47]\n\t" \
    "s_add_u32 m0, s69, 74752\n\t" \
    "s_nop 0\n\t" \
    "global_load_lds_dwordx4 v204, s[46:47]\n\t" \
    "s_add_u32 m0, s69, 75776\n\t" \
    "s_nop 0\n\t" \
    "global_load_lds_dwordx4 v205, s[46:47]\n\t" \
    "s_add_u32 m0, s69, 76800\n\t" \
    "s_nop 0\n\t" \
    "global_load_lds_dwordx4 v206, s[46:47]\n\t" \
    "s_add_u32 m0, s69, 77824\n\t" \
    "s_nop 0\n\t" \
    "global_load_lds_dwordx4 v207, s[46:47]\n\t" \
    "s_add_u32 m0, s69, 78848\n\t" \
    "s_nop 0\n\t" \
    "global_load_lds_dwordx4 v208, s[46:47]\n\t" \
    "s_add_u32 m0, s69, 79872\n\t" \
    "s_nop 0\n\t" \
    "global_load_lds_dwordx4 v209, s[46:47]\n\t" \
    "s_add_u32 m0, s69, 80896\n\t" \
    "s_nop 0\n\t" \
    "global_load_lds_dwordx4 v210, s[46:47]\n\t" \
    "s_add_u32 m0, s69, 81920\n\t" \
    "s_nop 0\n\t" \
    "global_load_lds_dwordx4 v211, s[46:47]\n\t" \
    "v_mbcnt_lo_u32_b32 v164, -1, 0\n\t" \
    "v_mbcnt_hi_u32_b32 v164, -1, v164\n\t" \
    "v_lshlrev_b32 v164, 6, v164\n\t" \
    "v_add_u32 v164, 6225920, v164\n\t" \
    "v_mov_b32 v165, 6234112\n\t" \
    "global_load_dword v165, v165, s[36:37] sc1\n\t" \
    "global_load_dwordx2 v[166:167], v164, s[36:37] sc1\n\t" \
    "global_load_dword v168, v164, s[36:37] offset:2048 sc1\n\t" \
    "s_waitcnt vmcnt(48) lgkmcnt(0)\n\t" \
    "s_barrier\n\t" \
    "v_add_u32 v201, 0x10000, v201\n\t" \
    "v_add_u32 v202, 0x10000, v202\n\t" \
    "ds_read_b64_tr_b16 a[64:65], v201 offset:0\n\t" \
    "ds_read_b64_tr_b16 a[66:67], v201 offset:1024\n\t" \
    "ds_read_b64_tr_b16 a[68:69], v201 offset:8192\n\t" \
    "ds_read_b64_tr_b16 a[70:71], v201 offset:9216\n\t" \
    "ds_read_b64_tr_b16 a[136:137], v202 offset:0\n\t" \
    "ds_read_b64_tr_b16 a[138:139], v202 offset:1024\n\t" \
    "ds_read_b64_tr_b16 a[140:141], v202 offset:8192\n\t" \
    "ds_read_b64_tr_b16 a[142:143], v202 offset:9216\n\t" \
    "v_add_u32 v201, 0xfffe0000, v201\n\t" \
    "v_add_u32 v202, 0xfffe0000, v202\n\t" \
    "s_waitcnt lgkmcnt(0)\n\t" \
    "s_barrier\n\t" \
    "s_add_u32 s46, s44, 110592\n\t" \
    "s_addc_u32 s47, s45, 0\n\t" \
    "s_mul_i32 s69, s82, 9\n\t" \
    "s_add_u32 m0, s69, 110592\n\t" \
    "s_nop 0\n\t" \
    "global_load_lds_dwordx4 v203, s[46:47]\n\t" \
    "s_add_u32 m0, s69, 111616\n\t" \
    "s_nop 0\n\t" \
    "global_load_lds_dwordx4 v204, s[46:47]\n\t" \
    "s_add_u32 m0, s69, 112640\n\t" \
    "s_nop 0\n\t" \
    "global_load_lds_dwordx4 v205, s[46:47]\n\t" \
    "s_add_u32 m0, s69, 113664\n\t" \
    "s_nop 0\n\t" \
    "global_load_lds_dwordx4 v206, s[46:47]\n\t" \
    "s_add_u32 m0, s69, 114688\n\t" \
    "s_nop 0\n\t" \
    "global_load_lds_dwordx4 v207, s[46:47]\n\t" \
    "s_add_u32 m0, s69, 115712\n\t" \
    "s_nop 0\n\t" \
    "global_load_lds_dwordx4 v208, s[46:47]\n\t" \
    "s_add_u32 m0, s69, 116736\n\t" \
    "s_nop 0\n\t" \
    "global_load_lds_dwordx4 v209, s[46:47]\n\t" \
    "s_add_u32 m0, s69, 117760\n\t" \
    "s_nop 0\n\t" \
    "global_load_lds_dwordx4 v210, s[46:47]\n\t" \
    "s_add_u32 m0, s69, 118784\n\t" \
    "s_nop 0\n\t" \
    "global_load_lds_dwordx4 v211, s[46:47]\n\t" \
    "s_waitcnt vmcnt(30) lgkmcnt(0)\n\t" \
    "s_barrier\n\t" \
    "ds_read_b64_tr_b16 a[144:145], v212 offset:0\n\t" \
    "ds_read_b64_tr_b16 a[146:147], v212 offset:18432\n\t" \
    "ds_read_b64_tr_b16 a[160:161], v213 offset:0\n\t" \
    "ds_read_b64_tr_b16 a[162:163], v213 offset:18432\n\t" \
    "ds_read_b64_tr_b16 a[176:177], v214 offset:0\n\t" \
    "ds_read_b64_tr_b16 a[178:179], v214 offset:18432\n\t" \
    "s_waitcnt lgkmcnt(6)\n\t" \
    "ds_read_b64_tr_b16 a[192:193], v215 offset:0\n\t" \
    "ds_read_b64_tr_b16 a[194:195], v215 offset:18432\n\t" \
    "ds_read_b64_tr_b16 a[208:209], v212 offset:128\n\t" \
    "ds_read_b64_tr_b16 a[210:211], v212 offset:18560\n\t" \
    "ds_read_b64_tr_b16 a[224:225], v213 offset:128\n\t" \
    "ds_read_b64_tr_b16 a[226:227], v213 offset:18560\n\t" \
    "s_waitcnt lgkmcnt(6)\n\t" \
    "ds_read_b64_tr_b16 a[240:241], v214 offset:128\n\t" \
    "ds_read_b64_tr_b16 a[242:243], v214 offset:18560\n\t" \
    "ds_read_b64_tr_b16 v[224:225], v215 offset:128\n\t" \
    "ds_read_b64_tr_b16 v[226:227], v215 offset:18560\n\t" \
    "ds_read_b64_tr_b16 v[240:241], v212 offset:256\n\t" \
    "ds_read_b64_tr_b16 v[242:243], v212 offset:18688\n\t" \
    "s_waitcnt lgkmcnt(6)\n\t" \
    "v_add_u32 v212, 0x9000, v212\n\t" \
    "v_add_u32 v213, 0x9000, v213\n\t" \
    "v_add_u32 v214, 0x9000, v214\n\t" \
    "v_add_u32 v215, 0x9000, v215\n\t" \
    "s_waitcnt vmcnt(21) lgkmcnt(0)\n\t" \
    "s_barrier\n\t" \
    "ds_read_b64_tr_b16 a[148:149], v212 offset:0\n\t" \
    "ds_read_b64_tr_b16 a[150:151], v212 offset:18432\n\t" \
    "ds_read_b64_tr_b16 a[164:165], v213 offset:0\n\t" \
    "ds_read_b64_tr_b16 a[166:167], v213 offset:18432\n\t" \
    "ds_read_b64_tr_b16 a[180:181], v214 offset:0\n\t" \
    "ds_read_b64_tr_b16 a[182:183], v214 offset:18432\n\t" \
    "s_waitcnt lgkmcnt(6)\n\t" \
    "ds_read_b64_tr_b16 a[196:197], v215 offset:0\n\t" \
    "ds_read_b64_tr_b16 a[198:199], v215 offset:18432\n\t" \
    "ds_read_b64_tr_b16 a[212:213], v212 offset:128\n\t" \
    "ds_read_b64_tr_b16 a[214:215], v212 offset:18560\n\t" \
    "ds_read_b64_tr_b16 a[228:229], v213 offset:128\n\t" \
    "ds_read_b64_tr_b16 a[230:231], v213 offset:18560\n\t" \
    "s_waitcnt lgkmcnt(6)\n\t" \
    "ds_read_b64_tr_b16 a[244:245], v214 offset:128\n\t" \
    "ds_read_b64_tr_b16 a[246:247], v214 offset:18560\n\t" \
    "ds_read_b64_tr_b16 v[228:229], v215 offset:128\n\t" \
    "ds_read_b64_tr_b16 v[230:231], v215 offset:18560\n\t" \
    "ds_read_b64_tr_b16 v[244:245], v212 offset:256\n\t" \
    "ds_read_b64_tr_b16 v[246:247], v212 offset:18688\n\t" \
    "s_waitcnt lgkmcnt(6)\n\t" \
    "v_add_u32 v212, 0x9000, v212\n\t" \
    "v_add_u32 v213, 0x9000, v213\n\t" \
    "v_add_u32 v214, 0x9000, v214\n\t" \
    "v_add_u32 v215, 0x9000, v215\n\t" \
    "ds_write_b128 v3, v[120:123] offset:0\n\t" \
    "ds_write_b128 v3, v[124:127] offset:1024\n\t" \
    "ds_write_b128 v3, v[128:131] offset:2048\n\t" \
    "ds_write_b128 v3, v[132:135] offset:3072\n\t" \
    "ds_write_b128 v3, v[136:139] offset:4096\n\t" \
    "ds_write_b128 v3, v[140:143] offset:5120\n\t" \
    "ds_write_b128 v3, v[144:147] offset:6144\n\t" \
    "ds_write_b128 v3, v[148:151] offset:7168\n\t" \
    "ds_write_b128 v3, v[152:155] offset:8192\n\t" \
    "s_waitcnt vmcnt(12) lgkmcnt(0)\n\t" \
    "s_barrier\n\t" \
    "ds_read_b64_tr_b16 a[152:153], v212 offset:0\n\t" \
    "ds_read_b64_tr_b16 a[154:155], v212 offset:18432\n\t" \
    "ds_read_b64_tr_b16 a[168:169], v213 offset:0\n\t" \
    "ds_read_b64_tr_b16 a[170:171], v213 offset:18432\n\t" \
    "ds_read_b64_tr_b16 a[184:185], v214 offset:0\n\t" \
    "ds_read_b64_tr_b16 a[186:187], v214 offset:18432\n\t" \
    "s_waitcnt lgkmcnt(6)\n\t" \
    "ds_read_b64_tr_b16 a[200:201], v215 offset:0\n\t" \
    "ds_read_b64_tr_b16 a[202:203], v215 offset:18432\n\t" \
    "ds_read_b64_tr_b16 a[216:217], v212 offset:128\n\t" \
    "ds_read_b64_tr_b16 a[218:219], v212 offset:18560\n\t" \
    "ds_read_b64_tr_b16 a[232:233], v213 offset:128\n\t" \
    "ds_read_b64_tr_b16 a[234:235], v213 offset:18560\n\t" \
    "s_waitcnt lgkmcnt(6)\n\t" \
    "ds_read_b64_tr_b16 a[248:249], v214 offset:128\n\t" \
    "ds_read_b64_tr_b16 a[250:251], v214 offset:18560\n\t" \
    "ds_read_b64_tr_b16 v[232:233], v215 offset:128\n\t" \
    "ds_read_b64_tr_b16 v[234:235], v215 offset:18560\n\t" \
    "ds_read_b64_tr_b16 v[248:249], v212 offset:256\n\t" \
    "ds_read_b64_tr_b16 v[250:251], v212 offset:18688\n\t" \
    "s_waitcnt lgkmcnt(6)\n\t" \
    "v_add_u32 v212, 0x9000, v212\n\t" \
    "v_add_u32 v213, 0x9000, v213\n\t" \
    "v_add_u32 v214, 0x9000, v214\n\t" \
    "v_add_u32 v215, 0x9000, v215\n\t" \
    "ds_write_b128 v3, v[72:75] offset:36864\n\t" \
    "ds_write_b128 v3, v[76:79] offset:37888\n\t" \
    "ds_write_b128 v3, v[80:83] offset:38912\n\t" \
    "ds_write_b128 v3, v[84:87] offset:39936\n\t" \
    "ds_write_b128 v3, v[88:91] offset:40960\n\t" \
    "ds_write_b128 v3, v[92:95] offset:41984\n\t" \
    "ds_write_b128 v3, v[96:99] offset:43008\n\t" \
    "ds_write_b128 v3, v[100:103] offset:44032\n\t" \
    "ds_write_b128 v3, v[104:107] offset:45056\n\t" \
    "s_waitcnt vmcnt(9)\n\t" \
    "gv_C1_%=:\n\t" \
    "v_cmp_ne_u32 s[54:55], v166, v168\n\t" \
    "v_sub_u32 v166, v166, v165\n\t" \
    "v_cmp_ne_u32 vcc, 1, v166\n\t" \
    "s_nop 1\n\t" \
    "s_or_b64 vcc, vcc, s[54:55]\n\t" \
    "s_cbranch_scc0 gv_Cok2_%=\n\t" \
    "s_cmp_eq_u32 s68, 0\n\t" \
    "s_cbranch_scc1 gv_F4_%=\n\t" \
    "s_sub_u32 s68, s68, 1\n\t" \
    "s_sleep 2\n\t" \
    "s_branch gv_Cag3_%=\n\t" \
    "gv_F4_%=:\n\t" \
    "v_mov_b32 v168, 5767424\n\t" \
    "v_mov_b32 v169, 4\n\t" \
    "global_store_dword v168, v169, s[36:37] sc1\n\t" \
    "s_waitcnt vmcnt(0)\n\t" \
    "s_branch gv_Cok2_%=\n\t" \
    "gv_Cag3_%=:\n\t" \
    "global_load_dwordx2 v[166:167], v164, s[36:37] sc1\n\t" \
    "global_load_dword v168, v164, s[36:37] offset:2048 sc1\n\t" \
    "s_waitcnt vmcnt(0)\n\t" \
    "s_branch gv_C1_%=\n\t" \
    "gv_Cok2_%=:\n\t" \
    "s_cmp_lg_u32 s66, 0\n\t" \
    "s_cbranch_scc1 gv_Ca5_%=\n\t" \
    "v_mov_b32 v164, 5836800\n\t" \
    "s_mov_b64 exec, 1\n\t" \
    "global_atomic_add v164, v166, s[36:37] sc1\n\t" \
    "s_mov_b64 exec, -1\n\t" \
    "gv_Ca5_%=:\n\t" \
    "v_mov_b32_dpp v164, v167 quad_perm:[1,0,3,2] row_mask:0xf bank_mask:0xf\n\t" \
    "v_cmp_lt_u32 s[44:45], v164, v167\n\t" \
    "s_nop 1\n\t" \
    "s_waitcnt lgkmcnt(0)\n\t" \
    "s_barrier\n\t" \
    "ds_read_b128 v[24:27], v1 offset:0\n\t" \
    "ds_read_b128 v[28:31], v1 offset:18432\n\t" \
    "ds_read_b128 v[32:35], v2 offset:0\n\t" \
    "ds_read_b128 v[36:39], v2 offset:18432\n\t" \
    "ds_read_b128 v[40:43], v1 offset:128\n\t" \
    "ds_read_b128 v[44:47], v1 offset:18560\n\t" \
    ".p2align 6\n\t" \
    "s_nop 0\n\t" \
    "s_nop 0\n\t" \
    "gv_loop6_%=:\n\t" \
    "s_min_u32 s69, s85, 63\n\t" \
    "s_mul_i32 s69, s69, 36864\n\t" \
    "s_add_u32 s52, s50, s69\n\t" \
    "s_addc_u32 s53, s51, 0\n\t" \
    "s_add_u32 s90, s90, 36864\n\t" \
    "s_cmp_eq_u32 s90, 110592\n\t" \
    "s_cselect_b32 s90, 0, s90\n\t" \
    "s_add_u32 s91, s90, 36864\n\t" \
    "s_cmp_eq_u32 s91, 110592\n\t" \
    "s_cselect_b32 s91, 0, s91\n\t" \
    "s_add_u32 s91, s91, s89\n\t" \
    "v_add_u32 v22, s90, v0\n\t" \
    "v_xor_b32 v23, 64, v22\n\t" \
    "s_lshl_b32 s40, s65, 15\n\t" \
    "s_cselect_b32 s70, s65, 64\n\t" \
    "s_mul_i32 s69, s70, 171\n\t" \
    "s_lshr_b32 s69, s69, 9\n\t" \
    "s_mul_i32 s70, s70, 589824\n\t" \
    "s_mul_i32 s71, s69, 327680\n\t" \
    "s_add_u32 s70, s70, s71\n\t" \
    "s_lshl1_add_u32 s69, s69, 4\n\t" \
    "s_bitcmp1_b64 s[44:45], s69\n\t" \
    "s_cselect_b32 s69, 8192, 0\n\t" \
    "s_add_u32 s69, s69, s70\n\t" \
    "s_add_u32 s69, s69, 123731968\n\t" \
    "s_add_u32 s48, s36, s69\n\t" \
    "s_addc_u32 s49, s37, 0\n\t" \
    "s_waitcnt lgkmcnt(5)\n\t" \
    "v_mfma_f32_16x16x32_bf16 v[72:75], a[0:3], v[24:27], 0\n\t" \
    "ds_read_b128 v[48:51], v2 offset:128\n\t" \
    "s_waitcnt lgkmcnt(5)\n\t" \
    "v_mfma_f32_16x16x32_bf16 v[76:79], a[0:3], v[28:31], 0\n\t" \
    "ds_read_b128 v[52:55], v2 offset:18560\n\t" \
    "v_mfma_f32_16x16x32_bf16 v[80:83], a[72:75], v[24:27], 0\n\t" \
    "s_add_u32 m0, s91, 0\n\t" \
    "v_mfma_f32_16x16x32_bf16 v[84:87], a[72:75], v[28:31], 0\n\t" \
    "global_load_lds_dwordx4 v203, s[52:53]\n\t" \
    "s_waitcnt lgkmcnt(5)\n\t" \
    "v_mfma_f32_16x16x32_bf16 v[72:75], a[4:7], v[32:35], v[72:75]\n\t" \
    "ds_read_b128 v[56:59], v1 offset:256\n\t" \
    "s_waitcnt lgkmcnt(5)\n\t" \
    "v_mfma_f32_16x16x32_bf16 v[76:79], a[4:7], v[36:39], v[76:79]\n\t" \
    "ds_read_b128 v[60:63], v1 offset:18688\n\t" \
    "v_mfma_f32_16x16x32_bf16 v[80:83], a[76:79], v[32:35], v[80:83]\n\t" \
    "s_add_u32 m0, s91, 1024\n\t" \
    "v_mfma_f32_16x16x32_bf16 v[84:87], a[76:79], v[36:39], v[84:87]\n\t" \
    "global_load_lds_dwordx4 v204, s[52:53]\n\t" \
    "s_waitcnt lgkmcnt(5)\n\t" \
    "v_mfma_f32_16x16x32_bf16 v[72:75], a[8:11], v[40:43], v[72:75]\n\t" \
    "ds_read_b128 v[64:67], v2 offset:256\n\t" \
    "s_waitcnt lgkmcnt(5)\n\t" \
    "v_mfma_f32_16x16x32_bf16 v[76:79], a[8:11], v[44:47], v[76:79]\n\t" \
    "ds_read_b128 v[68:71], v2 offset:18688\n\t" \
    "v_mfma_f32_16x16x32_bf16 v[80:83], a[80:83], v[40:43], v[80:83]\n\t" \
    "s_add_u32 m0, s91, 2048\n\t" \
    "v_mfma_f32_16x16x32_bf16 v[84:87], a[80:83], v[44:47], v[84:87]\n\t" \
    "global_load_lds_dwordx4 v205, s[52:53]\n\t" \
    "s_waitcnt lgkmcnt(5)\n\t" \
    "v_mfma_f32_16x16x32_bf16 v[72:75], a[12:15], v[48:51], v[72:75]\n\t" \
    "ds_read_b128 v[24:27], v1 offset:384\n\t" \
    "s_waitcnt lgkmcnt(5)\n\t" \
    "v_mfma_f32_16x16x32_bf16 v[76:79], a[12:15], v[52:55], v[76:79]\n\t" \
    "ds_read_b128 v[28:31], v1 offset:18816\n\t" \
    "v_mfma_f32_16x16x32_bf16 v[80:83], a[84:87], v[48:51], v[80:83]\n\t" \
    "s_add_u32 m0, s91, 3072\n\t" \
    "v_mfma_f32_16x16x32_bf16 v[84:87], a[84:87], v[52:55], v[84:87]\n\t" \
    "global_load_lds_dwordx4 v206, s[52:53]\n\t" \
    "s_waitcnt lgkmcnt(5)\n\t" \
    "v_mfma_f32_16x16x32_bf16 v[72:75], a[16:19], v[56:59], v[72:75]\n\t" \
    "ds_read_b128 v[32:35], v2 offset:384\n\t" \
    "s_waitcnt lgkmcnt(5)\n\t" \
    "v_mfma_f32_16x16x32_bf16 v[76:79], a[16:19], v[60:63], v[76:79]\n\t" \
    "ds_read_b128 v[36:39], v2 offset:18816\n\t" \
    "v_mfma_f32_16x16x32_bf16 v[80:83], a[88:91], v[56:59], v[80:83]\n\t" \
    "s_add_u32 m0, s91, 4096\n\t" \
    "v_mfma_f32_16x16x32_bf16 v[84:87], a[88:91], v[60:63], v[84:87]\n\t" \
    "global_load_lds_dwordx4 v207, s[52:53]\n\t" \
    "s_waitcnt lgkmcnt(5)\n\t" \
    "v_mfma_f32_16x16x32_bf16 v[72:75], a[20:23], v[64:67], v[72:75]\n\t" \
    "ds_read_b128 v[40:43], v1 offset:512\n\t" \
    "s_waitcnt lgkmcnt(5)\n\t" \
    "v_mfma_f32_16x16x32_bf16 v[76:79], a[20:23], v[68:71], v[76:79]\n\t" \
    "ds_read_b128 v[44:47], v1 offset:18944\n\t" \
    "v_mfma_f32_16x16x32_bf16 v[80:83], a[92:95], v[64:67], v[80:83]\n\t" \
    "s_add_u32 m0, s91, 5120\n\t" \
    "v_mfma_f32_16x16x32_bf16 v[84:87], a[92:95], v[68:71], v[84:87]\n\t" \
    "global_load_lds_dwordx4 v208, s[52:53]\n\t" \
    "s_waitcnt lgkmcnt(5)\n\t" \
    "v_mfma_f32_16x16x32_bf16 v[72:75], a[24:27], v[24:27], v[72:75]\n\t" \
    "ds_read_b128 v[48:51], v2 offset:512\n\t" \
    "s_waitcnt lgkmcnt(5)\n\t" \
    "v_mfma_f32_16x16x32_bf16 v[76:79], a[24:27], v[28:31], v[76:79]\n\t" \
    "ds_read_b128 v[52:55], v2 offset:18944\n\t" \
    "v_mfma_f32_16x16x32_bf16 v[80:83], a[96:99], v[24:27], v[80:83]\n\t" \
    "s_add_u32 m0, s91, 6144\n\t" \
    "v_mfma_f32_16x16x32_bf16 v[84:87], a[96:99], v[28:31], v[84:87]\n\t" \
    "global_load_lds_dwordx4 v209, s[52:53]\n\t" \
    "s_waitcnt lgkmcnt(5)\n\t" \
    "v_mfma_f32_16x16x32_bf16 v[72:75], a[28:31], v[32:35], v[72:75]\n\t" \
    "ds_read_b128 v[56:59], v1 offset:640\n\t" \
    "s_waitcnt lgkmcnt(5)\n\t" \
    "v_mfma_f32_16x16x32_bf16 v[76:79], a[28:31], v[36:39], v[76:79]\n\t" \
    "ds_read_b128 v[60:63], v1 offset:19072\n\t" \
    "v_mfma_f32_16x16x32_bf16 v[80:83], a[100:103], v[32:35], v[80:83]\n\t" \
    "s_add_u32 m0, s91, 7168\n\t" \
    "v_mfma_f32_16x16x32_bf16 v[84:87], a[100:103], v[36:39], v[84:87]\n\t" \
    "global_load_lds_dwordx4 v210, s[52:53]\n\t" \
    "s_waitcnt lgkmcnt(5)\n\t" \
    "v_mfma_f32_16x16x32_bf16 v[72:75], a[32:35], v[40:43], v[72:75]\n\t" \
    "ds_read_b128 v[64:67], v2 offset:640\n\t" \
    "s_waitcnt lgkmcnt(5)\n\t" \
    "v_mfma_f32_16x16x32_bf16 v[76:79], a[32:35], v[44:47], v[76:79]\n\t" \
    "ds_read_b128 v[68:71], v2 offset:19072\n\t" \
    "v_mfma_f32_16x16x32_bf16 v[80:83], a[104:107], v[40:43], v[80:83]\n\t" \
    "s_add_u32 m0, s91, 8192\n\t" \
    "v_mfma_f32_16x16x32_bf16 v[84:87], a[104:107], v[44:47], v[84:87]\n\t" \
    "global_load_lds_dwordx4 v211, s[52:53]\n\t" \
    "s_waitcnt lgkmcnt(5)\n\t" \
    "v_mfma_f32_16x16x32_bf16 v[72:75], a[36:39], v[48:51], v[72:75]\n\t" \
    "ds_read_b128 v[24:27], v1 offset:768\n\t" \
    "s_waitcnt lgkmcnt(5)\n\t" \
    "v_mfma_f32_16x16x32_bf16 v[76:79], a[36:39], v[52:55], v[76:79]\n\t" \
    "ds_read_b128 v[28:31], v1 offset:19200\n\t" \
    "v_mfma_f32_16x16x32_bf16 v[80:83], a[108:111], v[48:51], v[80:83]\n\t" \
    "v_mfma_f32_16x16x32_bf16 v[84:87], a[108:111], v[52:55], v[84:87]\n\t" \
    "s_waitcnt lgkmcnt(5)\n\t" \
    "v_mfma_f32_16x16x32_bf16 v[72:75], a[40:43], v[56:59], v[72:75]\n\t" \
    "ds_read_b128 v[32:35], v2 offset:768\n\t" \
    "s_waitcnt lgkmcnt(5)\n\t" \
    "v_mfma_f32_16x16x32_bf16 v[76:79], a[40:43], v[60:63], v[76:79]\n\t" \
    "ds_read_b128 v[36:39], v2 offset:19200\n\t" \
    "v_mfma_f32_16x16x32_bf16 v[80:83], a[112:115], v[56:59], v[80:83]\n\t" \
    "v_mfma_f32_16x16x32_bf16 v[84:87], a[112:115], v[60:63], v[84:87]\n\t" \
    "global_store_dwordx4 v19, v[196:199], s[48:49] sc1\n\t" \
    "s_waitcnt lgkmcnt(5)\n\t" \
    "v_mfma_f32_16x16x32_bf16 v[72:75], a[44:47], v[64:67], v[72:75]\n\t" \
    "ds_read_b128 v[40:43], v1 offset:896\n\t" \
    "s_waitcnt lgkmcnt(5)\n\t" \
    "v_mfma_f32_16x16x32_bf16 v[76:79], a[44:47], v[68:71], v[76:79]\n\t" \
    "ds_read_b128 v[44:47], v1 offset:19328\n\t" \
    "v_mfma_f32_16x16x32_bf16 v[80:83], a[116:119], v[64:67], v[80:83]\n\t" \
    "v_mfma_f32_16x16x32_bf16 v[84:87], a[116:119], v[68:71], v[84:87]\n\t" \
    "s_waitcnt lgkmcnt(5)\n\t" \
    "v_mfma_f32_16x16x32_bf16 v[72:75], a[48:51], v[24:27], v[72:75]\n\t" \
    "ds_read_b128 v[48:51], v2 offset:896\n\t" \
    "s_waitcnt lgkmcnt(5)\n\t" \
    "v_mfma_f32_16x16x32_bf16 v[76:79], a[48:51], v[28:31], v[76:79]\n\t" \
    "ds_read_b128 v[52:55], v2 offset:19328\n\t" \
    "v_mfma_f32_16x16x32_bf16 v[80:83], a[120:123], v[24:27], v[80:83]\n\t" \
    "v_mfma_f32_16x16x32_bf16 v[84:87], a[120:123], v[28:31], v[84:87]\n\t" \
    "s_waitcnt lgkmcnt(5)\n\t" \
    "v_mfma_f32_16x16x32_bf16 v[72:75], a[52:55], v[32:35], v[72:75]\n\t" \
    "ds_read_b128 v[56:59], v1 offset:1024\n\t" \
    "s_waitcnt lgkmcnt(5)\n\t" \
    "v_mfma_f32_16x16x32_bf16 v[76:79], a[52:55], v[36:39], v[76:79]\n\t" \
    "ds_read_b128 v[60:63], v1 offset:19456\n\t" \
    "v_mfma_f32_16x16x32_bf16 v[80:83], a[124:127], v[32:35], v[80:83]\n\t" \
    "v_mfma_f32_16x16x32_bf16 v[84:87], a[124:127], v[36:39], v[84:87]\n\t" \
    "global_store_dwordx4 v20, v[196:199], s[48:49] sc1\n\t" \
    "s_waitcnt lgkmcnt(5)\n\t" \
    "v_mfma_f32_16x16x32_bf16 v[72:75], a[56:59], v[40:43], v[72:75]\n\t" \
    "ds_read_b128 v[64:67], v2 offset:1024\n\t" \
    "s_waitcnt lgkmcnt(5)\n\t" \
    "v_mfma_f32_16x16x32_bf16 v[76:79], a[56:59], v[44:47], v[76:79]\n\t" \
    "ds_read_b128 v[68:71], v2 offset:19456\n\t" \
    "v_mfma_f32_16x16x32_bf16 v[80:83], a[128:131], v[40:43], v[80:83]\n\t" \
    "v_mfma_f32_16x16x32_bf16 v[84:87], a[128:131], v[44:47], v[84:87]\n\t" \
    "s_waitcnt lgkmcnt(5)\n\t" \
    "v_mfma_f32_16x16x32_bf16 v[72:75], a[60:63], v[48:51], v[72:75]\n\t" \
    "s_waitcnt lgkmcnt(4)\n\t" \
    "v_mfma_f32_16x16x32_bf16 v[76:79], a[60:63], v[52:55], v[76:79]\n\t" \
    "v_mfma_f32_16x16x32_bf16 v[80:83], a[132:135], v[48:51], v[80:83]\n\t" \
    "v_mfma_f32_16x16x32_bf16 v[84:87], a[132:135], v[52:55], v[84:87]\n\t" \
    "s_waitcnt lgkmcnt(3)\n\t" \
    "v_mfma_f32_16x16x32_bf16 v[72:75], a[64:67], v[56:59], v[72:75]\n\t" \
    "s_waitcnt lgkmcnt(2)\n\t" \
    "v_mfma_f32_16x16x32_bf16 v[76:79], a[64:67], v[60:63], v[76:79]\n\t" \
    "v_mfma_f32_16x16x32_bf16 v[80:83], a[136:139], v[56:59], v[80:83]\n\t" \
    "v_mfma_f32_16x16x32_bf16 v[84:87], a[136:139], v[60:63], v[84:87]\n\t" \
    "global_store_dword v21, v196, s[48:49] sc1\n\t" \
    "s_waitcnt lgkmcnt(1)\n\t" \
    "v_mfma_f32_16x16x32_bf16 v[72:75], a[68:71], v[64:67], v[72:75]\n\t" \
    "s_waitcnt lgkmcnt(0)\n\t" \
    "v_mfma_f32_16x16x32_bf16 v[76:79], a[68:71], v[68:71], v[76:79]\n\t" \
    "v_mfma_f32_16x16x32_bf16 v[80:83], a[140:143], v[64:67], v[80:83]\n\t" \
    "v_mfma_f32_16x16x32_bf16 v[84:87], a[140:143], v[68:71], v[84:87]\n\t" \
    "v_mov_b32 v1, v22\n\t" \
    "v_mov_b32 v2, v23\n\t" \
    "s_nop 1\n\t" \
    "v_cvt_pk_bf16_f32 v104, v72, v73\n\t" \
    "v_cvt_pk_bf16_f32 v105, v74, v75\n\t" \
    "s_nop 0\n\t" \
    "v_cvt_pk_bf16_f32 v106, v80, v81\n\t" \
    "v_cvt_pk_bf16_f32 v107, v82, v83\n\t" \
    "ds_write_b128 v4, v[104:107] offset:0\n\t" \
    "v_cvt_pk_bf16_f32 v108, v76, v77\n\t" \
    "v_cvt_pk_bf16_f32 v109, v78, v79\n\t" \
    "v_cvt_pk_bf16_f32 v110, v84, v85\n\t" \
    "v_cvt_pk_bf16_f32 v111, v86, v87\n\t" \
    "ds_write_b128 v4, v[108:111] offset:1024\n\t" \
    "s_cmp_lt_i32 s86, 0\n\t" \
    "s_cbranch_scc1 gv_rest8_%=\n\t" \
    "gv_back9_%=:\n\t" \
    "s_waitcnt vmcnt(21) lgkmcnt(0)\n\t" \
    "s_barrier\n\t" \
    "ds_read_b128 v[164:167], v5 offset:0\n\t" \
    "ds_read_b128 v[168:171], v5 offset:1024\n\t" \
    "ds_read_b128 v[172:175], v5 offset:2048\n\t" \
    "ds_read_b128 v[176:179], v5 offset:3072\n\t" \
    "ds_read_b128 v[180:183], v5 offset:4096\n\t" \
    "ds_read_b128 v[184:187], v5 offset:5120\n\t" \
    "ds_read_b128 v[188:191], v5 offset:6144\n\t" \
    "ds_read_b128 v[192:195], v5 offset:7168\n\t" \
    "s_cmp_lt_i32 s87, 12\n\t" \
    "s_cbranch_scc1 gv_fl10_%=\n\t" \
    "s_cmp_lg_u32 s66, 0\n\t" \
    "s_cbranch_scc1 gv_SF11_%=\n\t" \
    "s_lshl_b32 s69, s67, 7\n\t" \
    "s_add_u32 s69, s69, 5783552\n\t" \
    "s_add_u32 s70, s87, 0\n\t" \
    "v_mov_b32 v156, s69\n\t" \
    "v_mov_b32 v157, s70\n\t" \
    "s_mov_b64 exec, 1\n\t" \
    "global_store_dword v156, v157, s[36:37] sc1\n\t" \
    "s_mov_b64 exec, -1\n\t" \
    "gv_SF11_%=:\n\t" \
    "gv_fl10_%=:\n\t" \
    "v_xor_b32 v4, 0x2000, v4\n\t" \
    "v_xor_b32 v5, 0x2000, v5\n\t" \
    "s_waitcnt lgkmcnt(7)\n\t" \
    "v_mfma_f32_16x16x32_bf16 v[72:75], v[164:167], a[144:147], 0\n\t" \
    "s_waitcnt lgkmcnt(6)\n\t" \
    "v_mfma_f32_16x16x32_bf16 v[76:79], v[168:171], a[144:147], 0\n\t" \
    "s_waitcnt lgkmcnt(5)\n\t" \
    "v_mfma_f32_16x16x32_bf16 v[72:75], v[172:175], a[148:151], v[72:75]\n\t" \
    "s_waitcnt lgkmcnt(4)\n\t" \
    "v_mfma_f32_16x16x32_bf16 v[76:79], v[176:179], a[148:151], v[76:79]\n\t" \
    "s_waitcnt lgkmcnt(3)\n\t" \
    "v_mfma_f32_16x16x32_bf16 v[72:75], v[180:183], a[152:155], v[72:75]\n\t" \
    "s_waitcnt lgkmcnt(2)\n\t" \
    "v_mfma_f32_16x16x32_bf16 v[76:79], v[184:187], a[152:155], v[76:79]\n\t" \
    "s_waitcnt lgkmcnt(1)\n\t" \
    "v_mfma_f32_16x16x32_bf16 v[72:75], v[188:191], a[156:159], v[72:75]\n\t" \
    "s_waitcnt lgkmcnt(0)\n\t" \
    "v_mfma_f32_16x16x32_bf16 v[76:79], v[192:195], a[156:159], v[76:79]\n\t" \
    "v_mfma_f32_16x16x32_bf16 v[80:83], v[164:167], a[160:163], 0\n\t" \
    "v_mfma_f32_16x16x32_bf16 v[84:87], v[168:171], a[160:163], 0\n\t" \
    "v_mfma_f32_16x16x32_bf16 v[80:83], v[172:175], a[164:167], v[80:83]\n\t" \
    "v_mfma_f32_16x16x32_bf16 v[84:87], v[176:179], a[164:167], v[84:87]\n\t" \
    "v_mfma_f32_16x16x32_bf16 v[80:83], v[180:183], a[168:171], v[80:83]\n\t" \
    "v_mfma_f32_16x16x32_bf16 v[84:87], v[184:187], a[168:171], v[84:87]\n\t" \
    "v_cvt_pk_bf16_f32 v104, v72, v73\n\t" \
    "v_mfma_f32_16x16x32_bf16 v[80:83], v[188:191], a[172:175], v[80:83]\n\t" \
    "v_cvt_pk_bf16_f32 v105, v74, v75\n\t" \
    "v_mfma_f32_16x16x32_bf16 v[84:87], v[192:195], a[172:175], v[84:87]\n\t" \
    "v_cvt_pk_bf16_f32 v106, v76, v77\n\t" \
    "v_mfma_f32_16x16x32_bf16 v[88:91], v[164:167], a[176:179], 0\n\t" \
    "v_cvt_pk_bf16_f32 v107, v78, v79\n\t" \
    "v_mfma_f32_16x16x32_bf16 v[92:95], v[168:171], a[176:179], 0\n\t" \
    "v_xor_b32 v18, s40, v8\n\t" \
    "v_mfma_f32_16x16x32_bf16 v[88:91], v[172:175], a[180:183], v[88:91]\n\t" \
    "global_store_dwordx4 v18, v[104:107], s[38:39] sc1\n\t" \
    "v_mfma_f32_16x16x32_bf16 v[92:95], v[176:179], a[180:183], v[92:95]\n\t" \
    "v_mfma_f32_16x16x32_bf16 v[88:91], v[180:183], a[184:187], v[88:91]\n\t" \
    "v_mfma_f32_16x16x32_bf16 v[92:95], v[184:187], a[184:187], v[92:95]\n\t" \
    "v_cvt_pk_bf16_f32 v108, v80, v81\n\t" \
    "v_mfma_f32_16x16x32_bf16 v[88:91], v[188:191], a[188:191], v[88:91]\n\t" \
    "v_cvt_pk_bf16_f32 v109, v82, v83\n\t" \
    "v_mfma_f32_16x16x32_bf16 v[92:95], v[192:195], a[188:191], v[92:95]\n\t" \
    "v_cvt_pk_bf16_f32 v110, v84, v85\n\t" \
    "v_mfma_f32_16x16x32_bf16 v[72:75], v[164:167], a[192:195], 0\n\t" \
    "v_cvt_pk_bf16_f32 v111, v86, v87\n\t" \
    "v_mfma_f32_16x16x32_bf16 v[76:79], v[168:171], a[192:195], 0\n\t" \
    "v_xor_b32 v18, s40, v9\n\t" \
    "v_mfma_f32_16x16x32_bf16 v[72:75], v[172:175], a[196:199], v[72:75]\n\t" \
    "global_store_dwordx4 v18, v[108:111], s[38:39] sc1\n\t" \
    "v_mfma_f32_16x16x32_bf16 v[76:79], v[176:179], a[196:199], v[76:79]\n\t" \
    "v_mfma_f32_16x16x32_bf16 v[72:75], v[180:183], a[200:203], v[72:75]\n\t" \
    "v_mfma_f32_16x16x32_bf16 v[76:79], v[184:187], a[200:203], v[76:79]\n\t" \
    "v_cvt_pk_bf16_f32 v104, v88, v89\n\t" \
    "v_mfma_f32_16x16x32_bf16 v[72:75], v[188:191], a[204:207], v[72:75]\n\t" \
    "v_cvt_pk_bf16_f32 v105, v90, v91\n\t" \
    "v_mfma_f32_16x16x32_bf16 v[76:79], v[192:195], a[204:207], v[76:79]\n\t" \
    "v_cvt_pk_bf16_f32 v106, v92, v93\n\t" \
    "v_mfma_f32_16x16x32_bf16 v[80:83], v[164:167], a[208:211], 0\n\t" \
    "v_cvt_pk_bf16_f32 v107, v94, v95\n\t" \
    "v_mfma_f32_16x16x32_bf16 v[84:87], v[168:171], a[208:211], 0\n\t" \
    "v_xor_b32 v18, s40, v10\n\t" \
    "v_mfma_f32_16x16x32_bf16 v[80:83], v[172:175], a[212:215], v[80:83]\n\t" \
    "global_store_dwordx4 v18, v[104:107], s[38:39] sc1\n\t" \
    "v_mfma_f32_16x16x32_bf16 v[84:87], v[176:179], a[212:215], v[84:87]\n\t" \
    "v_mfma_f32_16x16x32_bf16 v[80:83], v[180:183], a[216:219], v[80:83]\n\t" \
    "v_mfma_f32_16x16x32_bf16 v[84:87], v[184:187], a[216:219], v[84:87]\n\t" \
    "v_cvt_pk_bf16_f32 v108, v72, v73\n\t" \
    "v_mfma_f32_16x16x32_bf16 v[80:83], v[188:191], a[220:223], v[80:83]\n\t" \
    "v_cvt_pk_bf16_f32 v109, v74, v75\n\t" \
    "v_mfma_f32_16x16x32_bf16 v[84:87], v[192:195], a[220:223], v[84:87]\n\t" \
    "v_cvt_pk_bf16_f32 v110, v76, v77\n\t" \
    "v_mfma_f32_16x16x32_bf16 v[88:91], v[164:167], a[224:227], 0\n\t" \
    "v_cvt_pk_bf16_f32 v111, v78, v79\n\t" \
    "v_mfma_f32_16x16x32_bf16 v[92:95], v[168:171], a[224:227], 0\n\t" \
    "v_xor_b32 v18, s40, v11\n\t" \
    "v_mfma_f32_16x16x32_bf16 v[88:91], v[172:175], a[228:231], v[88:91]\n\t" \
    "global_store_dwordx4 v18, v[108:111], s[38:39] sc1\n\t" \
    "v_mfma_f32_16x16x32_bf16 v[92:95], v[176:179], a[228:231], v[92:95]\n\t" \
    "v_mfma_f32_16x16x32_bf16 v[88:91], v[180:183], a[232:235], v[88:91]\n\t" \
    "v_mfma_f32_16x16x32_bf16 v[92:95], v[184:187], a[232:235], v[92:95]\n\t" \
    "v_cvt_pk_bf16_f32 v104, v80, v81\n\t" \
    "v_mfma_f32_16x16x32_bf16 v[88:91], v[188:191], a[236:239], v[88:91]\n\t" \
    "v_cvt_pk_bf16_f32 v105, v82, v83\n\t" \
    "v_mfma_f32_16x16x32_bf16 v[92:95], v[192:195], a[236:239], v[92:95]\n\t" \
    "v_cvt_pk_bf16_f32 v106, v84, v85\n\t" \
    "v_mfma_f32_16x16x32_bf16 v[72:75], v[164:167], a[240:243], 0\n\t" \
    "v_cvt_pk_bf16_f32 v107, v86, v87\n\t" \
    "v_mfma_f32_16x16x32_bf16 v[76:79], v[168:171], a[240:243], 0\n\t" \
    "v_xor_b32 v18, s40, v12\n\t" \
    "v_mfma_f32_16x16x32_bf16 v[72:75], v[172:175], a[244:247], v[72:75]\n\t" \
    "global_store_dwordx4 v18, v[104:107], s[38:39] sc1\n\t" \
    "v_mfma_f32_16x16x32_bf16 v[76:79], v[176:179], a[244:247], v[76:79]\n\t" \
    "v_mfma_f32_16x16x32_bf16 v[72:75], v[180:183], a[248:251], v[72:75]\n\t" \
    "v_mfma_f32_16x16x32_bf16 v[76:79], v[184:187], a[248:251], v[76:79]\n\t" \
    "v_cvt_pk_bf16_f32 v108, v88, v89\n\t" \
    "v_mfma_f32_16x16x32_bf16 v[72:75], v[188:191], a[252:255], v[72:75]\n\t" \
    "v_cvt_pk_bf16_f32 v109, v90, v91\n\t" \
    "v_mfma_f32_16x16x32_bf16 v[76:79], v[192:195], a[252:255], v[76:79]\n\t" \
    "v_cvt_pk_bf16_f32 v110, v92, v93\n\t" \
    "v_mfma_f32_16x16x32_bf16 v[80:83], v[164:167], v[224:227], 0\n\t" \
    "v_cvt_pk_bf16_f32 v111, v94, v95\n\t" \
    "v_mfma_f32_16x16x32_bf16 v[84:87], v[168:171], v[224:227], 0\n\t" \
    "v_xor_b32 v18, s40, v13\n\t" \
    "v_mfma_f32_16x16x32_bf16 v[80:83], v[172:175], v[228:231], v[80:83]\n\t" \
    "global_store_dwordx4 v18, v[108:111], s[38:39] sc1\n\t" \
    "v_mfma_f32_16x16x32_bf16 v[84:87], v[176:179], v[228:231], v[84:87]\n\t" \
    "v_mfma_f32_16x16x32_bf16 v[80:83], v[180:183], v[232:235], v[80:83]\n\t" \
    "v_mfma_f32_16x16x32_bf16 v[84:87], v[184:187], v[232:235], v[84:87]\n\t" \
    "v_cvt_pk_bf16_f32 v104, v72, v73\n\t" \
    "v_mfma_f32_16x16x32_bf16 v[80:83], v[188:191], v[236:239], v[80:83]\n\t" \
    "v_cvt_pk_bf16_f32 v105, v74, v75\n\t" \
    "v_mfma_f32_16x16x32_bf16 v[84:87], v[192:195], v[236:239], v[84:87]\n\t" \
    "v_cvt_pk_bf16_f32 v106, v76, v77\n\t" \
    "v_mfma_f32_16x16x32_bf16 v[88:91], v[164:167], v[240:243], 0\n\t" \
    "v_cvt_pk_bf16_f32 v107, v78, v79\n\t" \
    "ds_read_b128 v[24:27], v22 offset:0\n\t" \
    "v_mfma_f32_16x16x32_bf16 v[92:95], v[168:171], v[240:243], 0\n\t" \
    "v_xor_b32 v18, s40, v14\n\t" \
    "ds_read_b128 v[28:31], v22 offset:18432\n\t" \
    "v_mfma_f32_16x16x32_bf16 v[88:91], v[172:175], v[244:247], v[88:91]\n\t" \
    "global_store_dwordx4 v18, v[104:107], s[38:39] sc1\n\t" \
    "ds_read_b128 v[32:35], v23 offset:0\n\t" \
    "v_mfma_f32_16x16x32_bf16 v[92:95], v[176:179], v[244:247], v[92:95]\n\t" \
    "ds_read_b128 v[36:39], v23 offset:18432\n\t" \
    "v_mfma_f32_16x16x32_bf16 v[88:91], v[180:183], v[248:251], v[88:91]\n\t" \
    "ds_read_b128 v[40:43], v22 offset:128\n\t" \
    "v_mfma_f32_16x16x32_bf16 v[92:95], v[184:187], v[248:251], v[92:95]\n\t" \
    "v_cvt_pk_bf16_f32 v108, v80, v81\n\t" \
    "ds_read_b128 v[44:47], v22 offset:18560\n\t" \
    "v_mfma_f32_16x16x32_bf16 v[88:91], v[188:191], v[252:255], v[88:91]\n\t" \
    "v_cvt_pk_bf16_f32 v109, v82, v83\n\t" \
    "v_mfma_f32_16x16x32_bf16 v[92:95], v[192:195], v[252:255], v[92:95]\n\t" \
    "v_cvt_pk_bf16_f32 v110, v84, v85\n\t" \
    "v_cvt_pk_bf16_f32 v111, v86, v87\n\t" \
    "v_xor_b32 v18, s40, v15\n\t" \
    "global_store_dwordx4 v18, v[108:111], s[38:39] sc1\n\t" \
    "s_nop 1\n\t" \
    "v_cvt_pk_bf16_f32 v104, v88, v89\n\t" \
    "v_cvt_pk_bf16_f32 v105, v90, v91\n\t" \
    "v_cvt_pk_bf16_f32 v106, v92, v93\n\t" \
    "v_cvt_pk_bf16_f32 v107, v94, v95\n\t" \
    "v_xor_b32 v18, s40, v16\n\t" \
    "global_store_dwordx4 v18, v[104:107], s[38:39] sc1\n\t" \
    "s_cmp_ge_u32 s65, 12\n\t" \
    "s_cbranch_scc1 gv_nofast7_%=\n\t" \
    "s_waitcnt vmcnt(0)\n\t" \
    "s_barrier\n\t" \
    "s_cmp_lg_u32 s66, 0\n\t" \
    "s_cbranch_scc1 gv_SF12_%=\n\t" \
    "s_lshl_b32 s69, s67, 7\n\t" \
    "s_add_u32 s69, s69, 5783552\n\t" \
    "s_add_u32 s70, s65, 0\n\t" \
    "v_mov_b32 v156, s69\n\t" \
    "v_mov_b32 v157, s70\n\t" \
    "s_mov_b64 exec, 1\n\t" \
    "global_store_dword v156, v157, s[36:37] sc1\n\t" \
    "s_mov_b64 exec, -1\n\t" \
    "gv_SF12_%=:\n\t" \
    "s_xor_b32 s69, s66, 1\n\t" \
    "s_lshr_b32 s70, s65, 1\n\t" \
    "s_or_b32 s69, s69, s70\n\t" \
    "s_cbranch_scc1 gv_nofast7_%=\n\t" \
    "v_mov_b32 v156, 5838848\n\t" \
    "v_mov_b32 v157, 1\n\t" \
    "s_mov_b64 exec, 1\n\t" \
    "global_atomic_add v158, v156, v157, s[36:37] sc0 sc1\n\t" \
    "s_mov_b64 exec, -1\n\t" \
    "v_mbcnt_lo_u32_b32 v156, -1, 0\n\t" \
    "v_and_b32 v157, 3, v156\n\t" \
    "v_lshlrev_b32 v157, 16, v157\n\t" \
    "v_bfe_u32 v159, v156, 2, 2\n\t" \
    "v_lshl_add_u32 v157, v159, 11, v157\n\t" \
    "v_bfe_u32 v159, v156, 4, 1\n\t" \
    "v_lshl_add_u32 v157, v159, 14, v157\n\t" \
    "s_bitcmp1_b32 s44, 2\n\t" \
    "s_cselect_b32 s63, 8192, 0\n\t" \
    "s_or_b32 s63, s63, 0x200000\n\t" \
    "v_add_u32 v157, s63, v157\n\t" \
    "s_waitcnt vmcnt(0)\n\t" \
    "v_readfirstlane_b32 s69, v158\n\t" \
    "s_nop 1\n\t" \
    "s_cmp_lg_u32 s69, 126\n\t" \
    "s_cbranch_scc1 gv_go13_%=\n\t" \
    "s_memrealtime s[56:57]\n\t" \
    "s_waitcnt lgkmcnt(0)\n\t" \
    "s_lshl_b32 s69, s56, 4\n\t" \
    "s_add_u32 s69, s69, 2400\n\t" \
    "v_mov_b32 v156, s69\n\t" \
    "global_store_dword v157, v156, s[36:37] sc1\n\t" \
    "gv_go13_%=:\n\t" \
    "gv_nofast7_%=:\n\t" \
    "s_mov_b32 s87, s86\n\t" \
    "s_mov_b32 s86, s65\n\t" \
    "s_mov_b32 s65, s84\n\t" \
    "s_mov_b32 s84, s85\n\t" \
    "s_cmp_lt_u32 s84, 24\n\t" \
    "s_addc_u32 s85, s84, 1\n\t" \
    "s_cmp_lt_u32 s65, s88\n\t" \
    "s_cbranch_scc1 gv_loop6_%=\n\t" \
    "s_waitcnt vmcnt(0) lgkmcnt(0)\n\t" \
    "s_barrier\n\t" \
    "s_mov_b32 s65, 99\n\t" \
    "s_cmp_lg_u32 s66, 0\n\t" \
    "s_cbranch_scc1 gv_SF14_%=\n\t" \
    "s_lshl_b32 s69, s67, 7\n\t" \
    "s_add_u32 s69, s69, 5783552\n\t" \
    "s_add_u32 s70, s65, 0\n\t" \
    "v_mov_b32 v156, s69\n\t" \
    "v_mov_b32 v157, s70\n\t" \
    "s_mov_b64 exec, 1\n\t" \
    "global_store_dword v156, v157, s[36:37] sc1\n\t" \
    "s_mov_b64 exec, -1\n\t" \
    "gv_SF14_%=:\n\t" \
    "s_branch gv_end15_%=\n\t" \
    "gv_rest8_%=:\n\t" \
    "s_waitcnt vmcnt(12) lgkmcnt(0)\n\t" \
    "s_barrier\n\t" \
    "ds_read_b64_tr_b16 a[156:157], v212 offset:0\n\t" \
    "ds_read_b64_tr_b16 a[158:159], v212 offset:18432\n\t" \
    "ds_read_b64_tr_b16 a[172:173], v213 offset:0\n\t" \
    "ds_read_b64_tr_b16 a[174:175], v213 offset:18432\n\t" \
    "ds_read_b64_tr_b16 a[188:189], v214 offset:0\n\t" \
    "ds_read_b64_tr_b16 a[190:191], v214 offset:18432\n\t" \
    "s_waitcnt lgkmcnt(6)\n\t" \
    "ds_read_b64_tr_b16 a[204:205], v215 offset:0\n\t" \
    "ds_read_b64_tr_b16 a[206:207], v215 offset:18432\n\t" \
    "ds_read_b64_tr_b16 a[220:221], v212 offset:128\n\t" \
    "ds_read_b64_tr_b16 a[222:223], v212 offset:18560\n\t" \
    "ds_read_b64_tr_b16 a[236:237], v213 offset:128\n\t" \
    "ds_read_b64_tr_b16 a[238:239], v213 offset:18560\n\t" \
    "s_waitcnt lgkmcnt(6)\n\t" \
    "ds_read_b64_tr_b16 a[252:253], v214 offset:128\n\t" \
    "ds_read_b64_tr_b16 a[254:255], v214 offset:18560\n\t" \
    "ds_read_b64_tr_b16 v[236:237], v215 offset:128\n\t" \
    "ds_read_b64_tr_b16 v[238:239], v215 offset:18560\n\t" \
    "ds_read_b64_tr_b16 v[252:253], v212 offset:256\n\t" \
    "ds_read_b64_tr_b16 v[254:255], v212 offset:18688\n\t" \
    "s_waitcnt lgkmcnt(6)\n\t" \
    "v_add_u32 v212, 0x9000, v212\n\t" \
    "v_add_u32 v213, 0x9000, v213\n\t" \
    "v_add_u32 v214, 0x9000, v214\n\t" \
    "v_add_u32 v215, 0x9000, v215\n\t" \
    "s_branch gv_back9_%=\n\t" \
    "gv_end15_%=:\n\t" \
    "gv_last_%=:\n\t" \
    ""
#define ASM_CV \
    "v_mov_b32 v0, %0\n\t" \
    "v_mov_b32 v2, %2\n\t" \
    "v_subrev_u32 v1, 768, %1\n\t" \
    "v_mov_b32 v4, 0\n\t" \
    "v_mov_b32 v5, 0\n\t" \
    "v_mov_b32 v6, 0\n\t" \
    "v_mov_b32 v7, 0\n\t" \
    "v_mov_b32 v8, 0\n\t" \
    "v_mov_b32 v9, 0\n\t" \
    "v_mov_b32 v10, 0\n\t" \
    "v_mov_b32 v11, 0\n\t" \
    "v_mov_b32 v12, 0\n\t" \
    "v_mov_b32 v13, 0\n\t" \
    "v_mov_b32 v14, 0\n\t" \
    "v_mov_b32 v15, 0\n\t" \
    "v_mov_b32 v16, 0\n\t" \
    "v_mov_b32 v17, 0\n\t" \
    "v_mov_b32 v18, 0\n\t" \
    "v_mov_b32 v19, 0\n\t" \
    "v_mov_b32 v20, 0\n\t" \
    "v_mov_b32 v21, 0\n\t" \
    "v_mov_b32 v22, 0\n\t" \
    "v_mov_b32 v23, 0\n\t" \
    "v_mov_b32 v24, 0\n\t" \
    "v_mov_b32 v25, 0\n\t" \
    "v_mov_b32 v26, 0\n\t" \
    "v_mov_b32 v27, 0\n\t" \
    "v_mov_b32 v28, 0\n\t" \
    "v_mov_b32 v29, 0\n\t" \
    "v_mov_b32 v30, 0\n\t" \
    "v_mov_b32 v31, 0\n\t" \
    "v_mov_b32 v32, 0\n\t" \
    "v_mov_b32 v33, 0\n\t" \
    "v_mov_b32 v34, 0\n\t" \
    "v_mov_b32 v35, 0\n\t" \
    "s_waitcnt lgkmcnt(0)\n\t" \
    "ds_read_b64 v[36:37], v0 offset:0\n\t" \
    "ds_read_b64 v[38:39], v0 offset:8\n\t" \
    "ds_read_b64 v[40:41], v0 offset:32\n\t" \
    "ds_read_b64 v[42:43], v0 offset:40\n\t" \
    "ds_read_b128 v[44:47], v1 offset:768\n\t" \
    "ds_read_b128 v[48:51], v1 offset:2304\n\t" \
    "ds_read_b128 v[52:55], v1 offset:3840\n\t" \
    "ds_read_b128 v[56:59], v1 offset:5376\n\t" \
    "ds_read_b128 v[60:63], v1 offset:8448\n\t" \
    "ds_read_b128 v[64:67], v1 offset:9984\n\t" \
    "ds_read_b128 v[68:71], v1 offset:11520\n\t" \
    "ds_read_b128 v[72:75], v1 offset:13056\n\t" \
    "s_mov_b32 s69, 8\n\t" \
    "cv_c1_%=:\n\t" \
    "s_waitcnt lgkmcnt(7)\n\t" \
    "v_mfma_f32_16x16x32_bf16 v[4:7], v[36:39], v[44:47], v[4:7]\n\t" \
    "ds_read_b64 v[76:77], v0 offset:64\n\t" \
    "ds_read_b64 v[78:79], v0 offset:72\n\t" \
    "v_mfma_f32_16x16x32_bf16 v[20:23], v[40:43], v[44:47], v[20:23]\n\t" \
    "ds_read_b64 v[80:81], v0 offset:96\n\t" \
    "ds_read_b64 v[82:83], v0 offset:104\n\t" \
    "s_waitcnt lgkmcnt(10)\n\t" \
    "v_mfma_f32_16x16x32_bf16 v[8:11], v[36:39], v[48:51], v[8:11]\n\t" \
    "ds_read_b128 v[84:87], v1 offset:672\n\t" \
    "ds_read_b128 v[88:91], v1 offset:2208\n\t" \
    "v_mfma_f32_16x16x32_bf16 v[24:27], v[40:43], v[48:51], v[24:27]\n\t" \
    "ds_read_b128 v[92:95], v1 offset:3744\n\t" \
    "ds_read_b128 v[96:99], v1 offset:5280\n\t" \
    "s_waitcnt lgkmcnt(13)\n\t" \
    "v_mfma_f32_16x16x32_bf16 v[12:15], v[36:39], v[52:55], v[12:15]\n\t" \
    "ds_read_b128 v[100:103], v1 offset:8352\n\t" \
    "ds_read_b128 v[104:107], v1 offset:9888\n\t" \
    "v_mfma_f32_16x16x32_bf16 v[28:31], v[40:43], v[52:55], v[28:31]\n\t" \
    "s_waitcnt lgkmcnt(14)\n\t" \
    "v_mfma_f32_16x16x32_bf16 v[16:19], v[36:39], v[56:59], v[16:19]\n\t" \
    "ds_read_b128 v[108:111], v1 offset:11424\n\t" \
    "v_mfma_f32_16x16x32_bf16 v[32:35], v[40:43], v[56:59], v[32:35]\n\t" \
    "s_waitcnt lgkmcnt(14)\n\t" \
    "v_mfma_f32_16x16x32_bf16 v[4:7], v[36:39], v[60:63], v[4:7]\n\t" \
    "ds_read_b128 v[112:115], v1 offset:12960\n\t" \
    "v_mfma_f32_16x16x32_bf16 v[20:23], v[40:43], v[60:63], v[20:23]\n\t" \
    "s_waitcnt lgkmcnt(14)\n\t" \
    "v_mfma_f32_16x16x32_bf16 v[8:11], v[36:39], v[64:67], v[8:11]\n\t" \
    "v_mfma_f32_16x16x32_bf16 v[24:27], v[40:43], v[64:67], v[24:27]\n\t" \
    "s_waitcnt lgkmcnt(13)\n\t" \
    "v_mfma_f32_16x16x32_bf16 v[12:15], v[36:39], v[68:71], v[12:15]\n\t" \
    "v_mfma_f32_16x16x32_bf16 v[28:31], v[40:43], v[68:71], v[28:31]\n\t" \
    "s_waitcnt lgkmcnt(12)\n\t" \
    "v_mfma_f32_16x16x32_bf16 v[16:19], v[36:39], v[72:75], v[16:19]\n\t" \
    "v_mfma_f32_16x16x32_bf16 v[32:35], v[40:43], v[72:75], v[32:35]\n\t" \
    "s_waitcnt lgkmcnt(7)\n\t" \
    "v_mfma_f32_16x16x32_bf16 v[4:7], v[76:79], v[84:87], v[4:7]\n\t" \
    "ds_read_b64 v[36:37], v0 offset:128\n\t" \
    "ds_read_b64 v[38:39], v0 offset:136\n\t" \
    "v_mfma_f32_16x16x32_bf16 v[20:23], v[80:83], v[84:87], v[20:23]\n\t" \
    "ds_read_b64 v[40:41], v0 offset:160\n\t" \
    "ds_read_b64 v[42:43], v0 offset:168\n\t" \
    "s_waitcnt lgkmcnt(10)\n\t" \
    "v_mfma_f32_16x16x32_bf16 v[8:11], v[76:79], v[88:91], v[8:11]\n\t" \
    "ds_read_b128 v[44:47], v1 offset:576\n\t" \
    "ds_read_b128 v[48:51], v1 offset:2112\n\t" \
    "v_mfma_f32_16x16x32_bf16 v[24:27], v[80:83], v[88:91], v[24:27]\n\t" \
    "ds_read_b128 v[52:55], v1 offset:3648\n\t" \
    "ds_read_b128 v[56:59], v1 offset:5184\n\t" \
    "s_waitcnt lgkmcnt(13)\n\t" \
    "v_mfma_f32_16x16x32_bf16 v[12:15], v[76:79], v[92:95], v[12:15]\n\t" \
    "ds_read_b128 v[60:63], v1 offset:8256\n\t" \
    "ds_read_b128 v[64:67], v1 offset:9792\n\t" \
    "v_mfma_f32_16x16x32_bf16 v[28:31], v[80:83], v[92:95], v[28:31]\n\t" \
    "s_waitcnt lgkmcnt(14)\n\t" \
    "v_mfma_f32_16x16x32_bf16 v[16:19], v[76:79], v[96:99], v[16:19]\n\t" \
    "ds_read_b128 v[68:71], v1 offset:11328\n\t" \
    "v_mfma_f32_16x16x32_bf16 v[32:35], v[80:83], v[96:99], v[32:35]\n\t" \
    "s_waitcnt lgkmcnt(14)\n\t" \
    "v_mfma_f32_16x16x32_bf16 v[4:7], v[76:79], v[100:103], v[4:7]\n\t" \
    "ds_read_b128 v[72:75], v1 offset:12864\n\t" \
    "v_mfma_f32_16x16x32_bf16 v[20:23], v[80:83], v[100:103], v[20:23]\n\t" \
    "s_waitcnt lgkmcnt(14)\n\t" \
    "v_mfma_f32_16x16x32_bf16 v[8:11], v[76:79], v[104:107], v[8:11]\n\t" \
    "v_mfma_f32_16x16x32_bf16 v[24:27], v[80:83], v[104:107], v[24:27]\n\t" \
    "s_waitcnt lgkmcnt(13)\n\t" \
    "v_mfma_f32_16x16x32_bf16 v[12:15], v[76:79], v[108:111], v[12:15]\n\t" \
    "v_mfma_f32_16x16x32_bf16 v[28:31], v[80:83], v[108:111], v[28:31]\n\t" \
    "s_waitcnt lgkmcnt(12)\n\t" \
    "v_mfma_f32_16x16x32_bf16 v[16:19], v[76:79], v[112:115], v[16:19]\n\t" \
    "v_mfma_f32_16x16x32_bf16 v[32:35], v[80:83], v[112:115], v[32:35]\n\t" \
    "v_add_u32 v0, 128, v0\n\t" \
    "v_subrev_u32 v1, 192, v1\n\t" \
    "s_sub_u32 s69, s69, 1\n\t" \
    "s_cmp_lg_u32 s69, 0\n\t" \
    "s_cbranch_scc1 cv_c1_%=\n\t" \
    "s_waitcnt lgkmcnt(0)\n\t" \
    "s_mov_b32 s69, 8\n\t" \
    "cv_c2_%=:\n\t" \
    "s_waitcnt lgkmcnt(5)\n\t" \
    "v_mfma_f32_16x16x32_bf16 v[8:11], v[36:39], v[48:51], v[8:11]\n\t" \
    "ds_read_b64 v[76:77], v0 offset:64\n\t" \
    "ds_read_b64 v[78:79], v0 offset:72\n\t" \
    "v_mfma_f32_16x16x32_bf16 v[24:27], v[40:43], v[48:51], v[24:27]\n\t" \
    "ds_read_b64 v[80:81], v0 offset:96\n\t" \
    "ds_read_b64 v[82:83], v0 offset:104\n\t" \
    "s_waitcnt lgkmcnt(8)\n\t" \
    "v_mfma_f32_16x16x32_bf16 v[12:15], v[36:39], v[52:55], v[12:15]\n\t" \
    "ds_read_b128 v[88:91], v1 offset:2208\n\t" \
    "ds_read_b128 v[92:95], v1 offset:3744\n\t" \
    "v_mfma_f32_16x16x32_bf16 v[28:31], v[40:43], v[52:55], v[28:31]\n\t" \
    "ds_read_b128 v[96:99], v1 offset:5280\n\t" \
    "ds_read_b128 v[104:107], v1 offset:9888\n\t" \
    "s_waitcnt lgkmcnt(11)\n\t" \
    "v_mfma_f32_16x16x32_bf16 v[16:19], v[36:39], v[56:59], v[16:19]\n\t" \
    "ds_read_b128 v[108:111], v1 offset:11424\n\t" \
    "ds_read_b128 v[112:115], v1 offset:12960\n\t" \
    "v_mfma_f32_16x16x32_bf16 v[32:35], v[40:43], v[56:59], v[32:35]\n\t" \
    "s_waitcnt lgkmcnt(12)\n\t" \
    "v_mfma_f32_16x16x32_bf16 v[8:11], v[36:39], v[64:67], v[8:11]\n\t" \
    "v_mfma_f32_16x16x32_bf16 v[24:27], v[40:43], v[64:67], v[24:27]\n\t" \
    "s_waitcnt lgkmcnt(11)\n\t" \
    "v_mfma_f32_16x16x32_bf16 v[12:15], v[36:39], v[68:71], v[12:15]\n\t" \
    "v_mfma_f32_16x16x32_bf16 v[28:31], v[40:43], v[68:71], v[28:31]\n\t" \
    "s_waitcnt lgkmcnt(10)\n\t" \
    "v_mfma_f32_16x16x32_bf16 v[16:19], v[36:39], v[72:75], v[16:19]\n\t" \
    "v_mfma_f32_16x16x32_bf16 v[32:35], v[40:43], v[72:75], v[32:35]\n\t" \
    "s_waitcnt lgkmcnt(5)\n\t" \
    "v_mfma_f32_16x16x32_bf16 v[8:11], v[76:79], v[88:91], v[8:11]\n\t" \
    "ds_read_b64 v[36:37], v0 offset:128\n\t" \
    "ds_read_b64 v[38:39], v0 offset:136\n\t" \
    "v_mfma_f32_16x16x32_bf16 v[24:27], v[80:83], v[88:91], v[24:27]\n\t" \
    "ds_read_b64 v[40:41], v0 offset:160\n\t" \
    "ds_read_b64 v[42:43], v0 offset:168\n\t" \
    "s_waitcnt lgkmcnt(8)\n\t" \
    "v_mfma_f32_16x16x32_bf16 v[12:15], v[76:79], v[92:95], v[12:15]\n\t" \
    "ds_read_b128 v[48:51], v1 offset:2112\n\t" \
    "ds_read_b128 v[52:55], v1 offset:3648\n\t" \
    "v_mfma_f32_16x16x32_bf16 v[28:31], v[80:83], v[92:95], v[28:31]\n\t" \
    "ds_read_b128 v[56:59], v1 offset:5184\n\t" \
    "ds_read_b128 v[64:67], v1 offset:9792\n\t" \
    "s_waitcnt lgkmcnt(11)\n\t" \
    "v_mfma_f32_16x16x32_bf16 v[16:19], v[76:79], v[96:99], v[16:19]\n\t" \
    "ds_read_b128 v[68:71], v1 offset:11328\n\t" \
    "ds_read_b128 v[72:75], v1 offset:12864\n\t" \
    "v_mfma_f32_16x16x32_bf16 v[32:35], v[80:83], v[96:99], v[32:35]\n\t" \
    "s_waitcnt lgkmcnt(12)\n\t" \
    "v_mfma_f32_16x16x32_bf16 v[8:11], v[76:79], v[104:107], v[8:11]\n\t" \
    "v_mfma_f32_16x16x32_bf16 v[24:27], v[80:83], v[104:107], v[24:27]\n\t" \
    "s_waitcnt lgkmcnt(11)\n\t" \
    "v_mfma_f32_16x16x32_bf16 v[12:15], v[76:79], v[108:111], v[12:15]\n\t" \
    "v_mfma_f32_16x16x32_bf16 v[28:31], v[80:83], v[108:111], v[28:31]\n\t" \
    "s_waitcnt lgkmcnt(10)\n\t" \
    "v_mfma_f32_16x16x32_bf16 v[16:19], v[76:79], v[112:115], v[16:19]\n\t" \
    "v_mfma_f32_16x16x32_bf16 v[32:35], v[80:83], v[112:115], v[32:35]\n\t" \
    "v_add_u32 v0, 128, v0\n\t" \
    "v_subrev_u32 v1, 192, v1\n\t" \
    "s_sub_u32 s69, s69, 1\n\t" \
    "s_cmp_lg_u32 s69, 0\n\t" \
    "s_cbranch_scc1 cv_c2_%=\n\t" \
    "s_waitcnt lgkmcnt(0)\n\t" \
    "s_mov_b32 s69, 8\n\t" \
    "cv_c3_%=:\n\t" \
    "s_waitcnt lgkmcnt(3)\n\t" \
    "v_mfma_f32_16x16x32_bf16 v[12:15], v[36:39], v[52:55], v[12:15]\n\t" \
    "ds_read_b64 v[76:77], v0 offset:64\n\t" \
    "ds_read_b64 v[78:79], v0 offset:72\n\t" \
    "v_mfma_f32_16x16x32_bf16 v[28:31], v[40:43], v[52:55], v[28:31]\n\t" \
    "ds_read_b64 v[80:81], v0 offset:96\n\t" \
    "ds_read_b64 v[82:83], v0 offset:104\n\t" \
    "s_waitcnt lgkmcnt(6)\n\t" \
    "v_mfma_f32_16x16x32_bf16 v[16:19], v[36:39], v[56:59], v[16:19]\n\t" \
    "ds_read_b128 v[92:95], v1 offset:3744\n\t" \
    "ds_read_b128 v[96:99], v1 offset:5280\n\t" \
    "v_mfma_f32_16x16x32_bf16 v[32:35], v[40:43], v[56:59], v[32:35]\n\t" \
    "ds_read_b128 v[108:111], v1 offset:11424\n\t" \
    "ds_read_b128 v[112:115], v1 offset:12960\n\t" \
    "s_waitcnt lgkmcnt(9)\n\t" \
    "v_mfma_f32_16x16x32_bf16 v[12:15], v[36:39], v[68:71], v[12:15]\n\t" \
    "v_mfma_f32_16x16x32_bf16 v[28:31], v[40:43], v[68:71], v[28:31]\n\t" \
    "s_waitcnt lgkmcnt(8)\n\t" \
    "v_mfma_f32_16x16x32_bf16 v[16:19], v[36:39], v[72:75], v[16:19]\n\t" \
    "v_mfma_f32_16x16x32_bf16 v[32:35], v[40:43], v[72:75], v[32:35]\n\t" \
    "s_waitcnt lgkmcnt(3)\n\t" \
    "v_mfma_f32_16x16x32_bf16 v[12:15], v[76:79], v[92:95], v[12:15]\n\t" \
    "ds_read_b64 v[36:37], v0 offset:128\n\t" \
    "ds_read_b64 v[38:39], v0 offset:136\n\t" \
    "v_mfma_f32_16x16x32_bf16 v[28:31], v[80:83], v[92:95], v[28:31]\n\t" \
    "ds_read_b64 v[40:41], v0 offset:160\n\t" \
    "ds_read_b64 v[42:43], v0 offset:168\n\t" \
    "s_waitcnt lgkmcnt(6)\n\t" \
    "v_mfma_f32_16x16x32_bf16 v[16:19], v[76:79], v[96:99], v[16:19]\n\t" \
    "ds_read_b128 v[52:55], v1 offset:3648\n\t" \
    "ds_read_b128 v[56:59], v1 offset:5184\n\t" \
    "v_mfma_f32_16x16x32_bf16 v[32:35], v[80:83], v[96:99], v[32:35]\n\t" \
    "ds_read_b128 v[68:71], v1 offset:11328\n\t" \
    "ds_read_b128 v[72:75], v1 offset:12864\n\t" \
    "s_waitcnt lgkmcnt(9)\n\t" \
    "v_mfma_f32_16x16x32_bf16 v[12:15], v[76:79], v[108:111], v[12:15]\n\t" \
    "v_mfma_f32_16x16x32_bf16 v[28:31], v[80:83], v[108:111], v[28:31]\n\t" \
    "s_waitcnt lgkmcnt(8)\n\t" \
    "v_mfma_f32_16x16x32_bf16 v[16:19], v[76:79], v[112:115], v[16:19]\n\t" \
    "v_mfma_f32_16x16x32_bf16 v[32:35], v[80:83], v[112:115], v[32:35]\n\t" \
    "v_add_u32 v0, 128, v0\n\t" \
    "v_subrev_u32 v1, 192, v1\n\t" \
    "s_sub_u32 s69, s69, 1\n\t" \
    "s_cmp_lg_u32 s69, 0\n\t" \
    "s_cbranch_scc1 cv_c3_%=\n\t" \
    "s_waitcnt lgkmcnt(0)\n\t" \
    "ds_read_b64 v[36:37], v0 offset:0\n\t" \
    "ds_read_b64 v[38:39], v0 offset:8\n\t" \
    "ds_read_b64 v[40:41], v0 offset:32\n\t" \
    "ds_read_b64 v[42:43], v0 offset:40\n\t" \
    "ds_read_b128 v[56:59], v1 offset:5376\n\t" \
    "ds_read_b128 v[72:75], v1 offset:13056\n\t" \
    "ds_read_b64 v[76:77], v0 offset:64\n\t" \
    "ds_read_b64 v[78:79], v0 offset:72\n\t" \
    "ds_read_b64 v[80:81], v0 offset:96\n\t" \
    "ds_read_b64 v[82:83], v0 offset:104\n\t" \
    "ds_read_b128 v[96:99], v1 offset:5280\n\t" \
    "ds_read_b128 v[112:115], v1 offset:12960\n\t" \
    "s_mov_b32 s69, 4\n\t" \
    "cv_c4_%=:\n\t" \
    "s_waitcnt lgkmcnt(7)\n\t" \
    "v_mfma_f32_16x16x32_bf16 v[16:19], v[36:39], v[56:59], v[16:19]\n\t" \
    "ds_read_b64 v[116:117], v0 offset:128\n\t" \
    "ds_read_b64 v[118:119], v0 offset:136\n\t" \
    "v_mfma_f32_16x16x32_bf16 v[32:35], v[40:43], v[56:59], v[32:35]\n\t" \
    "ds_read_b64 v[120:121], v0 offset:160\n\t" \
    "ds_read_b64 v[122:123], v0 offset:168\n\t" \
    "s_waitcnt lgkmcnt(10)\n\t" \
    "v_mfma_f32_16x16x32_bf16 v[16:19], v[36:39], v[72:75], v[16:19]\n\t" \
    "ds_read_b128 v[136:139], v1 offset:5184\n\t" \
    "ds_read_b128 v[152:155], v1 offset:12864\n\t" \
    "v_mfma_f32_16x16x32_bf16 v[32:35], v[40:43], v[72:75], v[32:35]\n\t" \
    "ds_read_b64 v[156:157], v0 offset:192\n\t" \
    "ds_read_b64 v[158:159], v0 offset:200\n\t" \
    "s_waitcnt lgkmcnt(9)\n\t" \
    "v_mfma_f32_16x16x32_bf16 v[16:19], v[76:79], v[96:99], v[16:19]\n\t" \
    "ds_read_b64 v[160:161], v0 offset:224\n\t" \
    "ds_read_b64 v[162:163], v0 offset:232\n\t" \
    "v_mfma_f32_16x16x32_bf16 v[32:35], v[80:83], v[96:99], v[32:35]\n\t" \
    "ds_read_b128 v[176:179], v1 offset:5088\n\t" \
    "ds_read_b128 v[192:195], v1 offset:12768\n\t" \
    "s_waitcnt lgkmcnt(12)\n\t" \
    "v_mfma_f32_16x16x32_bf16 v[16:19], v[76:79], v[112:115], v[16:19]\n\t" \
    "v_mfma_f32_16x16x32_bf16 v[32:35], v[80:83], v[112:115], v[32:35]\n\t" \
    "s_waitcnt lgkmcnt(7)\n\t" \
    "v_mfma_f32_16x16x32_bf16 v[16:19], v[116:119], v[136:139], v[16:19]\n\t" \
    "ds_read_b64 v[36:37], v0 offset:256\n\t" \
    "ds_read_b64 v[38:39], v0 offset:264\n\t" \
    "v_mfma_f32_16x16x32_bf16 v[32:35], v[120:123], v[136:139], v[32:35]\n\t" \
    "ds_read_b64 v[40:41], v0 offset:288\n\t" \
    "ds_read_b64 v[42:43], v0 offset:296\n\t" \
    "s_waitcnt lgkmcnt(10)\n\t" \
    "v_mfma_f32_16x16x32_bf16 v[16:19], v[116:119], v[152:155], v[16:19]\n\t" \
    "ds_read_b128 v[56:59], v1 offset:4992\n\t" \
    "ds_read_b128 v[72:75], v1 offset:12672\n\t" \
    "v_mfma_f32_16x16x32_bf16 v[32:35], v[120:123], v[152:155], v[32:35]\n\t" \
    "ds_read_b64 v[76:77], v0 offset:320\n\t" \
    "ds_read_b64 v[78:79], v0 offset:328\n\t" \
    "s_waitcnt lgkmcnt(9)\n\t" \
    "v_mfma_f32_16x16x32_bf16 v[16:19], v[156:159], v[176:179], v[16:19]\n\t" \
    "ds_read_b64 v[80:81], v0 offset:352\n\t" \
    "ds_read_b64 v[82:83], v0 offset:360\n\t" \
    "v_mfma_f32_16x16x32_bf16 v[32:35], v[160:163], v[176:179], v[32:35]\n\t" \
    "ds_read_b128 v[96:99], v1 offset:4896\n\t" \
    "ds_read_b128 v[112:115], v1 offset:12576\n\t" \
    "s_waitcnt lgkmcnt(12)\n\t" \
    "v_mfma_f32_16x16x32_bf16 v[16:19], v[156:159], v[192:195], v[16:19]\n\t" \
    "v_mfma_f32_16x16x32_bf16 v[32:35], v[160:163], v[192:195], v[32:35]\n\t" \
    "v_add_u32 v0, 256, v0\n\t" \
    "v_subrev_u32 v1, 384, v1\n\t" \
    "s_sub_u32 s69, s69, 1\n\t" \
    "s_cmp_lg_u32 s69, 0\n\t" \
    "s_cbranch_scc1 cv_c4_%=\n\t" \
    "s_waitcnt lgkmcnt(0)\n\t" \
    "s_nop 7\n\t" \
    "ds_write_b128 v2, v[4:7] offset:0\n\t" \
    "ds_write_b128 v2, v[8:11] offset:1024\n\t" \
    "ds_write_b128 v2, v[12:15] offset:2048\n\t" \
    "ds_write_b128 v2, v[16:19] offset:3072\n\t" \
    "ds_write_b128 v2, v[20:23] offset:4096\n\t" \
    "ds_write_b128 v2, v[24:27] offset:5120\n\t" \
    "ds_write_b128 v2, v[28:31] offset:6144\n\t" \
    "ds_write_b128 v2, v[32:35] offset:7168\n\t" \
    "s_waitcnt lgkmcnt(0)\n\t" \
    "cv_last_%=:\n\t" \
    ""
#define ASM_EA \
    "s_cmp_lt_u32 %1, 64\n\t" \
    "s_cselect_b32 s40, 0x30, 0x38\n\t" \
    "s_load_dwordx2 s[42:43], %0, s40\n\t" \
    "s_load_dword s48, %0, 0x40\n\t" \
    "s_getpc_b64 s[44:45]\n\t" \
    "ea_pc_%=:\n\t" \
    "s_bfe_u32 s40, %1, 0x20003\n\t" \
    "s_lshl_b32 s40, s40, 15\n\t" \
    "v_lshl_add_u32 v1, %2, 7, s40\n\t" \
    "v_min_u32 v1, code_end_marker-ea_pc_%=, v1\n\t" \
    "s_mov_b32 m0, 155648\n\t" \
    "v_readfirstlane_b32 s41, %2\n\t" \
    "global_load_lds_dword v1, s[44:45]\n\t" \
    "s_bitcmp1_b32 %1, 2\n\t" \
    "s_cselect_b32 s40, 24, 16\n\t" \
    "s_lshr_b32 s47, %1, 3\n\t" \
    "s_cmp_ge_u32 s47, s40\n\t" \
    "s_cbranch_scc1 ea_done1_%=\n\t" \
    "s_lshl_b32 m0, s41, 4\n\t" \
    "s_and_b32 s40, %1, 3\n\t" \
    "s_lshl_b32 s40, s40, 11\n\t" \
    "s_bfe_u32 s46, %1, 0x30003\n\t" \
    "s_lshl_b32 s46, s46, 8\n\t" \
    "s_add_u32 s40, s40, s46\n\t" \
    "s_and_b32 s41, s41, 0x80\n\t" \
    "s_xor_b32 s40, s40, s41\n\t" \
    "v_lshrrev_b32 v1, 3, %2\n\t" \
    "v_and_b32 v1, 6, v1\n\t" \
    "v_xor_b32 v1, v1, %2\n\t" \
    "v_and_b32 v1, 15, v1\n\t" \
    "v_lshlrev_b32 v1, 4, v1\n\t" \
    "v_xor_b32 v1, s40, v1\n\t" \
    "v_lshrrev_b32 v2, 4, %2\n\t" \
    "v_lshl_add_u32 v1, v2, 13, v1\n\t" \
    "s_mov_b32 s40, 16\n\t" \
    "s_waitcnt lgkmcnt(0)\n\t" \
    "ea_lp2_%=:\n\t" \
    "global_load_lds_dwordx4 v1, s[42:43]\n\t" \
    "v_add_u32 v1, 0x20000, v1\n\t" \
    "s_add_u32 m0, m0, 4096\n\t" \
    "s_sub_u32 s40, s40, 1\n\t" \
    "s_cmp_lg_u32 s40, 0\n\t" \
    "s_cbranch_scc1 ea_lp2_%=\n\t" \
    "ea_done1_%=:\n\t" \
    "s_waitcnt lgkmcnt(0)\n\t" \
    "ea_last_%=:\n\t" \
    ""
#define ASM_CLOB "v0", "v1", "v2", "v3", "v4", "v5", "v6", "v7", "v8", "v9", "v10", "v11", "v12", "v13", "v14", "v15", "v16", "v17", "v18", "v19", "v20", "v21", "v22", "v23", "v24", "v25", "v26", "v27", "v28", "v29", "v30", "v31", "v32", "v33", "v34", "v35", "v36", "v37", "v38", "v39", "v40", "v41", "v42", "v43", "v44", "v45", "v46", "v47", "v48", "v49", "v50", "v51", "v52", "v53", "v54", "v55", "v56", "v57", "v58", "v59", "v60", "v61", "v62", "v63", "v64", "v65", "v66", "v67", "v68", "v69", "v70", "v71", "v72", "v73", "v74", "v75", "v76", "v77", "v78", "v79", "v80", "v81", "v82", "v83", "v84", "v85", "v86", "v87", "v88", "v89", "v90", "v91", "v92", "v93", "v94", "v95", "v96", "v97", "v98", "v99", "v100", "v101", "v102", "v103", "v104", "v105", "v106", "v107", "v108", "v109", "v110", "v111", "v112", "v113", "v114", "v115", "v116", "v117", "v118", "v119", "v120", "v121", "v122", "v123", "v124", "v125", "v126", "v127", "v128", "v129", "v130", "v131", "v132", "v133", "v134", "v135", "v136", "v137", "v138", "v139", "v140", "v141", "v142", "v143", "v144", "v145", "v146", "v147", "v148", "v149", "v150", "v151", "v152", "v153", "v154", "v155", "v156", "v157", "v158", "v159", "v160", "v161", "v162", "v163", "v164", "v165", "v166", "v167", "v168", "v169", "v170", "v171", "v172", "v173", "v174", "v175", "v176", "v177", "v178", "v179", "v180", "v181", "v182", "v183", "v184", "v185", "v186", "v187", "v188", "v189", "v190", "v191", "v192", "v193", "v194", "v195", "v196", "v197", "v198", "v199", "v200", "v201", "v202", "v203", "v204", "v205", "v206", "v207", "v208", "v209", "v210", "v211", "v212", "v213", "v214", "v215", "v222", "v223", "v224", "v225", "v226", "v227", "v228", "v229", "v230", "v231", "v232", "v233", "v234", "v235", "v236", "v237", "v238", "v239", "v240", "v241", "v242", "v243", "v244", "v245", "v246", "v247", "v248", "v249", "v250", "v251", "v252", "v253", "v254", "v255", "a0", "a1", "a2", "a3", "a4", "a5", "a6", "a7", "a8", "a9", "a10", "a11", "a12", "a13", "a14", "a15", "a16", "a17", "a18", "a19", "a20", "a21", "a22", "a23", "a24", "a25", "a26", "a27", "a28", "a29", "a30", "a31", "a32", "a33", "a34", "a35", "a36", "a37", "a38", "a39", "a40", "a41", "a42", "a43", "a44", "a45", "a46", "a47", "a48", "a49", "a50", "a51", "a52", "a53", "a54", "a55", "a56", "a57", "a58", "a59", "a60", "a61", "a62", "a63", "a64", "a65", "a66", "a67", "a68", "a69", "a70", "a71", "a72", "a73", "a74", "a75", "a76", "a77", "a78", "a79", "a80", "a81", "a82", "a83", "a84", "a85", "a86", "a87", "a88", "a89", "a90", "a91", "a92", "a93", "a94", "a95", "a96", "a97", "a98", "a99", "a100", "a101", "a102", "a103", "a104", "a105", "a106", "a107", "a108", "a109", "a110", "a111", "a112", "a113", "a114", "a115", "a116", "a117", "a118", "a119", "a120", "a121", "a122", "a123", "a124", "a125", "a126", "a127", "a128", "a129", "a130", "a131", "a132", "a133", "a134", "a135", "a136", "a137", "a138", "a139", "a140", "a141", "a142", "a143", "a144", "a145", "a146", "a147", "a148", "a149", "a150", "a151", "a152", "a153", "a154", "a155", "a156", "a157", "a158", "a159", "a160", "a161", "a162", "a163", "a164", "a165", "a166", "a167", "a168", "a169", "a170", "a171", "a172", "a173", "a174", "a175", "a176", "a177", "a178", "a179", "a180", "a181", "a182", "a183", "a184", "a185", "a186", "a187", "a188", "a189", "a190", "a191", "a192", "a193", "a194", "a195", "a196", "a197", "a198", "a199", "a200", "a201", "a202", "a203", "a204", "a205", "a206", "a207", "a208", "a209", "a210", "a211", "a212", "a213", "a214", "a215", "a216", "a217", "a218", "a219", "a220", "a221", "a222", "a223", "a224", "a225", "a226", "a227", "a228", "a229", "a230", "a231", "a232", "a233", "a234", "a235", "a236", "a237", "a238", "a239", "a240", "a241", "a242", "a243", "a244", "a245", "a246", "a247", "a248", "a249", "a250", "a251", "a252", "a253", "a254", "a255", "s33", "s34", "s35", "s36", "s37", "s38", "s39", "s40", "s41", "s42", "s43", "s44", "s45", "s46", "s47", "s48", "s49", "s50", "s51", "s52", "s53", "s54", "s55", "s56", "s57", "s58", "s59", "s60", "s61", "s62", "s63", "s64", "s65", "s66", "s67", "s68", "s69", "s70", "s71", "s72", "s73", "s74", "s75", "s76", "s77", "s78", "s79", "s80", "s81", "s82", "s83", "s84", "s85", "s86", "s87", "s88", "s89", "s90", "s91", "s92", "s93", "s94", "s95", "s96", "s97", "s98", "s99", "vcc", "scc", "memory"
#define ASM_CLOBW "v32", "v33", "v34", "v35", "v36", "v37", "v38", "v39", "v40", "v41", "v42", "v43", "v44", "v45", "v46", "v47", "v48", "v49", "v50", "v51", "v52", "v53", "v54", "v55", "v56", "v57", "v58", "v59", "v60", "v61", "v62", "v63", "v64", "v65", "v66", "v67", "v68", "v69", "v70", "v71", "v72", "v73", "v74", "v75", "v76", "v77", "v78", "v79", "v80", "v81", "v82", "v83", "v84", "v85", "v86", "v87", "v88", "v89", "v90", "v91", "v92", "v93", "v94", "v95", "v96", "v97", "v98", "v99", "v100", "v101", "v102", "v103", "v104", "v105", "v106", "v107", "v108", "v109", "v110", "v111", "v112", "v113", "v114", "v115", "v116", "v117", "v118", "v119", "v120", "v121", "v122", "v123", "v124", "v125", "v126", "v127", "v128", "v129", "v130", "v131", "v132", "v133", "v134", "v135", "v136", "v137", "v138", "v139", "v140", "v141", "v142", "v143", "v144", "v145", "v146", "v147", "v148", "v149", "v150", "v151", "v152", "v153", "v154", "v155", "v156", "v157", "v158", "v159", "v160", "v161", "v162", "v163", "v164", "v165", "v166", "v167", "v168", "v169", "v170", "v171", "v172", "v173", "v174", "v175", "v176", "v177", "v178", "v179", "v180", "v181", "v182", "v183", "v184", "v185", "v186", "v187", "v188", "v189", "v190", "v191", "v192", "v193", "v194", "v195", "v196", "v197", "v198", "v199", "v222", "v223", "v224", "v225", "v226", "v227", "v228", "v229", "v230", "v231", "v232", "v233", "v234", "v235", "v236", "v237", "v238", "v239", "v240", "v241", "v242", "v243", "v244", "v245", "v246", "v247", "v248", "v249", "v250", "v251", "v252", "v253", "v254", "v255", "a0", "a1", "a2", "a3", "a4", "a5", "a6", "a7", "a8", "a9", "a10", "a11", "a12", "a13", "a14", "a15", "a16", "a17", "a18", "a19", "a20", "a21", "a22", "a23", "a24", "a25", "a26", "a27", "a28", "a29", "a30", "a31", "a32", "a33", "a34", "a35", "a36", "a37", "a38", "a39", "a40", "a41", "a42", "a43", "a44", "a45", "a46", "a47", "a48", "a49", "a50", "a51", "a52", "a53", "a54", "a55", "a56", "a57", "a58", "a59", "a60", "a61", "a62", "a63", "a64", "a65", "a66", "a67", "a68", "a69", "a70", "a71", "a72", "a73", "a74", "a75", "a76", "a77", "a78", "a79", "a80", "a81", "a82", "a83", "a84", "a85", "a86", "a87", "a88", "a89", "a90", "a91", "a92", "a93", "a94", "a95", "a96", "a97", "a98", "a99", "a100", "a101", "a102", "a103", "a104", "a105", "a106", "a107", "a108", "a109", "a110", "a111", "a112", "a113", "a114", "a115", "a116", "a117", "a118", "a119", "a120", "a121", "a122", "a123", "a124", "a125", "a126", "a127", "a128", "a129", "a130", "a131", "a132", "a133", "a134", "a135", "a136", "a137", "a138", "a139", "a140", "a141", "a142", "a143", "a144", "a145", "a146", "a147", "a148", "a149", "a150", "a151", "a152", "a153", "a154", "a155", "a156", "a157", "a158", "a159", "a160", "a161", "a162", "a163", "a164", "a165", "a166", "a167", "a168", "a169", "a170", "a171", "a172", "a173", "a174", "a175", "a176", "a177", "a178", "a179", "a180", "a181", "a182", "a183", "a184", "a185", "a186", "a187", "a188", "a189", "a190", "a191", "a192", "a193", "a194", "a195", "a196", "a197", "a198", "a199", "a200", "a201", "a202", "a203", "a204", "a205", "a206", "a207", "a208", "a209", "a210", "a211", "a212", "a213", "a214", "a215", "a216", "a217", "a218", "a219", "a220", "a221", "a222", "a223", "a224", "a225", "a226", "a227", "a228", "a229", "a230", "a231", "a232", "a233", "a234", "a235", "a236", "a237", "a238", "a239", "a240", "a241", "a242", "a243", "a244", "a245", "a246", "a247", "a248", "a249", "a250", "a251", "a252", "a253", "a254", "a255", "s33", "s34", "s35", "s36", "s37", "s38", "s39", "s40", "s41", "s42", "s43", "s44", "s45", "s46", "s47", "s48", "s49", "s50", "s51", "s52", "s53", "s54", "s55", "s56", "s57", "s58", "s59", "s60", "s61", "s62", "s63", "s64", "s65", "s66", "s67", "s68", "s69", "s70", "s71", "s72", "s73", "s74", "s75", "s76", "s77", "s78", "s79", "s80", "s81", "s82", "s83", "s84", "s85", "s86", "s87", "s88", "s89", "s90", "s91", "s92", "s93", "s94", "s95", "s96", "s97", "s98", "s99", "vcc", "scc", "memory"
#define CV_CLOB "v0", "v1", "v2", "v3", "v4", "v5", "v6", "v7", "v8", "v9", "v10", "v11", "v12", "v13", "v14", "v15", "v16", "v17", "v18", "v19", "v20", "v21", "v22", "v23", "v24", "v25", "v26", "v27", "v28", "v29", "v30", "v31", "v32", "v33", "v34", "v35", "v36", "v37", "v38", "v39", "v40", "v41", "v42", "v43", "v44", "v45", "v46", "v47", "v48", "v49", "v50", "v51", "v52", "v53", "v54", "v55", "v56", "v57", "v58", "v59", "v60", "v61", "v62", "v63", "v64", "v65", "v66", "v67", "v68", "v69", "v70", "v71", "v72", "v73", "v74", "v75", "v76", "v77", "v78", "v79", "v80", "v81", "v82", "v83", "v84", "v85", "v86", "v87", "v88", "v89", "v90", "v91", "v92", "v93", "v94", "v95", "v96", "v97", "v98", "v99", "v100", "v101", "v102", "v103", "v104", "v105", "v106", "v107", "v108", "v109", "v110", "v111", "v112", "v113", "v114", "v115", "v116", "v117", "v118", "v119", "v120", "v121", "v122", "v123", "v124", "v125", "v126", "v127", "v128", "v129", "v130", "v131", "v132", "v133", "v134", "v135", "v136", "v137", "v138", "v139", "v140", "v141", "v142", "v143", "v144", "v145", "v146", "v147", "v148", "v149", "v150", "v151", "v152", "v153", "v154", "v155", "v156", "v157", "v158", "v159", "v160", "v161", "v162", "v163", "v164", "v165", "v166", "v167", "v168", "v169", "v170", "v171", "v172", "v173", "v174", "v175", "v176", "v177", "v178", "v179", "v180", "v181", "v182", "v183", "v184", "v185", "v186", "v187", "v188", "v189", "v190", "v191", "v192", "v193", "v194", "v195", "s69", "scc", "memory"
static constexpr unsigned G_STEP = 0x10000;
static constexpr unsigned G_WS_CTL = 0x580000;
static constexpr unsigned G_WS_DONE = 0x5f0000;
static constexpr unsigned G_WS_EP = 0x5f2000;
static constexpr unsigned G_WS_STAMPS = 0xc000000;
static constexpr unsigned G_C_CAL = 0x0;
static constexpr unsigned G_C_ERR = 0x100;
static constexpr unsigned G_C_TASK = 0x200;
static constexpr unsigned G_C_GX = 0x1000;
static constexpr unsigned G_C_GV = 0x4000;
static constexpr unsigned G_C_KM = 0x8000;
static constexpr unsigned G_C_UM = 0x9000;
static constexpr unsigned G_C_PC = 0xc000;
static constexpr unsigned G_C_UT = 0xd000;
static constexpr unsigned G_C_YC = 0x10000;
static constexpr unsigned G_C_ARR = 0x11000;
static constexpr unsigned G_C_KG = 0x12000;
static constexpr unsigned G_C_GQ = 0x14000;
static constexpr unsigned G_C_GO = 0x11800;
static constexpr unsigned G_C_RW = 0xf000;
static constexpr unsigned G_WS_H = 0x0;
static constexpr unsigned G_HS8 = 0x80000;
static constexpr unsigned G_HS4 = 0x40000;
static constexpr unsigned G_UXS = 0x200000;
static constexpr unsigned G_UMS = 0x90000;
static constexpr unsigned G_PROBE = 0x1bd400;
static constexpr unsigned G_D_QX = 0x600000;
static constexpr unsigned G_D_KX = 0x1600000;
static constexpr unsigned G_D_KM = 0x2600000;
static constexpr unsigned G_D_UX = 0x2e00000;
static constexpr unsigned G_D_UM = 0x7600000;
static constexpr unsigned G_D_PX = 0xa200000;
static constexpr unsigned G_D_PM = 0xa600000;
static constexpr unsigned G_D_KT = 0xa700000;
static constexpr unsigned G_D_UT = 0xac00000;
static constexpr unsigned G_D_YB = 0xaf00000;
static constexpr unsigned G_D_END = 0xb200000;
static constexpr unsigned G_SKM = 0x7;
static constexpr unsigned G_S1 = 0x18;
static constexpr unsigned G_GVN = 0x2;
static constexpr unsigned G_STG = 0x9000;
static constexpr unsigned G_KROW = 0x30;
#define FLG(x_) (kDefault[5])
#define FLH(x_) (kDefault[7])
static constexpr int kDefault[8] = {PRM_DEFAULT};
#define MFMA32(a_, b_, c_) __builtin_amdgcn_mfma_f32_16x16x32_bf16(__builtin_bit_cast(bf16x8, a_), __builtin_bit_cast(bf16x8, b_), c_, 0, 0, 0)
#define MFMA16(a_, b_, c_) __builtin_amdgcn_mfma_f32_16x16x16bf16_1k(__builtin_bit_cast(s16x4, a_), __builtin_bit_cast(s16x4, b_), c_, 0, 0, 0)
#define WLD8(d_, voff_, base_, off_) asm volatile("s_nop 4\n\tglobal_load_dwordx2 %0, %1, %2 offset:%3 sc1" : "=v"(d_) : "v"(voff_), "s"(base_), "i"(off_) : "memory")
#define WLD16(d_, voff_, base_, off_) asm volatile("s_nop 4\n\tglobal_load_dwordx4 %0, %1, %2 offset:%3 sc1" : "=v"(d_) : "v"(voff_), "s"(base_), "i"(off_) : "memory")
#define WST16(voff_, d_, base_, off_) asm volatile("s_nop 4\n\tglobal_store_dwordx4 %0, %1, %2 offset:%3 sc1\n\ts_nop 1" :: "v"(voff_), "v"(d_), "s"(base_), "i"(off_) : "memory")
#define WLD16X4(d0, d1, d2, d3, voff_, base_) asm volatile("s_nop 4\n\tglobal_load_dwordx4 %0, %4, %5 sc1\n\tglobal_load_dwordx4 %1, %4, %5 offset:1024 sc1\n\t" \
    "global_load_dwordx4 %2, %4, %5 offset:2048 sc1\n\tglobal_load_dwordx4 %3, %4, %5 offset:3072 sc1" \
    : "=&v"(d0), "=&v"(d1), "=&v"(d2), "=&v"(d3) : "v"(voff_), "s"(base_) : "memory")

__device__ __forceinline__ u32x4 pack8(const f32x4& a, const f32x4& b) {
    return u32x4{pk2bf(a[0], a[1]), pk2bf(a[2], a[3]), pk2bf(b[0], b[1]), pk2bf(b[2], b[3])};
}
template <class T> __device__ __forceinline__ T* uni(T* q) {
    const u64 v = (u64)q;
    unsigned lo = __builtin_amdgcn_readfirstlane((unsigned)v), hi = __builtin_amdgcn_readfirstlane((unsigned)(v >> 32));
    asm volatile("s_nop 4" : "+s"(lo), "+s"(hi));
    return (T*)(((u64)hi << 32) | lo);
}
template <bool FAST = false>
__device__ __forceinline__ void wait_word(const MainP& p, const char* f, int want, unsigned code) {
    unsigned tries = 0;
    while (__any((int)__hip_atomic_load((const gu32*)f, RLX_AGENT) < want)) {
        if (++tries > kWaitCap) { __hip_atomic_store((gu32*)(p.ws + G_WS_CTL + G_C_ERR), code, RLX_AGENT); break; }
        if ((tries & 1023u) == 0u && __hip_atomic_load((const gu32*)(p.ws + G_WS_CTL + G_C_ERR), RLX_AGENT) != 0xffffffffu) break;
        if (FAST) __builtin_amdgcn_s_sleep(1); else __builtin_amdgcn_s_sleep(10);
    }
}

__device__ __forceinline__ int skew(int s, int id) { return (s ^ (G_SKM * id)) & 63; }
__device__ __forceinline__ unsigned um_base(int s, unsigned nm) { return G_D_UM + (s / 3) * 0x200000 + (s % 3) * G_UMS + ((nm >> (2 + s / 3)) & 1) * 8192; }
typedef GAS unsigned long long gu64;
#define IST(k_, slot_)
__device__ __forceinline__ void init_done(char* ws, int slot, unsigned val) {
    const unsigned e = __hip_atomic_load((const gu32*)(ws + G_WS_EP), RLX_AGENT);
    __hip_atomic_store((gu64*)(ws + G_WS_DONE + slot * 64), ((u64)val << 32) | (e + 1u), RLX_AGENT);
}
__device__ __forceinline__ void init_wait(char* ws, int w, int lane) {
    const unsigned e = __hip_atomic_load((const gu32*)(ws + G_WS_EP), RLX_AGENT);
    for (unsigned tries = 0; tries < kWaitCap; ++tries) {
        if (!__any(__hip_atomic_load((const gu32*)(ws + G_WS_DONE + lane * 64), RLX_AGENT) != e + 1u || __hip_atomic_load((const gu32*)(ws + G_WS_DONE + 2048 + lane * 64), RLX_AGENT) != e + 1u)) break;
        __builtin_amdgcn_s_sleep(8);
    }
    __syncthreads();
    if (w == 0 && lane == 0) __hip_atomic_fetch_add((gu32*)(ws + G_WS_CTL + G_C_ARR), 1u, RLX_AGENT);
}
__device__ __forceinline__ void fill_block(char* ws, int b, int tid, const u32x4& v0, const u32x4& v1) {
    const unsigned o = b * 8192 + tid * 16, o2 = o + 4096;
    asm volatile("global_store_dwordx4 %0, %1, %2 sc1\n\ts_nop 1" :: "v"(o), "v"(v0), "s"(ws) : "memory");
    if (b >= 512 || (b & 6) != 6 || tid < 64)
        asm volatile("global_store_dwordx4 %0, %1, %2 sc1\n\ts_nop 1" :: "v"(o2), "v"(v1), "s"(ws) : "memory");
}
__device__ __forceinline__ void init_fill(char* ws, const u16* mem0, int f, int tid) {
    const u32x4 sent = {0xffffffffu, 0xffffffffu, 0xffffffffu, 0xffffffffu};
    u32x4 v0 = sent, v1 = sent;
    if (f < 32 && (f & 4)) {
#pragma unroll
        for (int k = 0; k < 2; ++k) {
            const int q = tid + 256 * k + ((f & 2) ? 256 : 0), m = q & 15;
            if (!(f & 2) || (q >= 512 && q < 576)) {
                const unsigned* src = (const unsigned*)(mem0 + (2 * (m >> 2) + (m & 1)) * DM + 16 * (q >> 4) + (m & 2));
                const u32x4 o = {min(src[0], 0xfffffffeu), min(src[2], 0xfffffffeu), min(src[4], 0xfffffffeu), min(src[6], 0xfffffffeu)};
                if (k) v1 = o; else v0 = o;
            }
        }
    }
#pragma unroll 1
    for (int q = f; q < 180; q += 48) {
        if (q >= 136 && q < 152 && (tid & 127)) { v0 = u32x4{0u, 0u, 0u, 0u}; v1 = v0; }
        fill_block(ws, q < 64 ? q : q < 136 ? q - 64 + G_D_UM / 8192 : q < 168 ? 256 + ((q - 136) >> 2) * 8 + (q & 3) : q - 168 + G_WS_CTL / 8192, tid, v0, v1);
        v0 = sent; v1 = sent;
    }
    const unsigned e = __hip_atomic_load((const gu32*)(ws + G_WS_EP), RLX_AGENT);
    asm volatile("s_waitcnt vmcnt(0)" :: "v"(e) : "memory");
    __syncthreads();
    if (tid == 0) __hip_atomic_store((gu64*)(ws + G_WS_DONE + (48 + f) * 64), (u64)(e + 1u), RLX_AGENT);
    if (tid == 0) IST(0, 48 + f);
}
__device__ __forceinline__ void init_cal(char* ws, int id, int tid, int n) {
    if (tid < 64) {
        const int pg = id >> 1;
        char* a = ws + (pg < 2 ? pg * 0x200000u : G_D_UM + (pg - 2) * 0x200000u) + G_PROBE + (id & 1) * 8192;
        const u32x4 z = {0u, 0u, 0u, 0u};
        asm volatile("global_store_dwordx4 %0, %1, off sc1\n\tglobal_store_dwordx4 %0, %1, off offset:1024 sc1\n\ts_waitcnt vmcnt(0)" :: "v"(a + tid * 16), "v"(z) : "memory");
        if (tid == 0) {
            unsigned best = 0xffffu;
            const unsigned ts1 = (unsigned)__builtin_amdgcn_s_memrealtime() << 4;
#pragma unroll 1
            for (int it = 0; it < n; ++it) {
                const unsigned c0 = (unsigned)__builtin_amdgcn_s_memtime();
                const unsigned v = __hip_atomic_load((const gu32*)(a + it * 128), RLX_AGENT);
                asm volatile("s_waitcnt vmcnt(0)" :: "v"(v) : "memory");
                best = min(best, (unsigned)__builtin_amdgcn_s_memtime() - c0);
            }
            init_done(ws, id, best);
            IST(0, id);
        }
    }
    __syncthreads();
}
__device__ __forceinline__ unsigned near_mask(char* ws, int lane) {
    const gu32* q = (const gu32*)(ws + G_WS_DONE + (lane & 31) * 128 + 4);
    return (unsigned)__ballot(__hip_atomic_load(q + 16, RLX_AGENT) < __hip_atomic_load(q, RLX_AGENT));
}

#define TST(slot_, i_)
#define PIN8(a) asm volatile("" : "+v"(a[0]), "+v"(a[1]), "+v"(a[2]), "+v"(a[3]), "+v"(a[4]), "+v"(a[5]), "+v"(a[6]), "+v"(a[7]))
struct AOps { u32x4 q[4], k[8]; int fl; };
struct APtr { const char* q; const char* k; const char* m; unsigned voff, vkm, voffm, vfl; };
__device__ __forceinline__ void attn_batch(const MainP& p, AOps& o, const APtr& a) {
    const char* q2 = a.q + 4096; const char* k2 = a.k + 4096;
    WLD16(o.q[0], a.voff, a.q, 0); WLD16(o.q[1], a.voff, a.q, 2048); WLD16(o.q[2], a.voff, q2, 0); WLD16(o.q[3], a.voff, q2, 2048);
    WLD16X4(o.k[0], o.k[1], o.k[2], o.k[3], a.voff, a.k); WLD16X4(o.k[4], o.k[5], o.k[6], o.k[7], a.voff, k2);
    asm volatile("global_load_dword %0, %1, %2 sc1" : "=v"(o.fl) : "v"(a.vfl), "s"(p.ws) : "memory");
}
__device__ __forceinline__ void attn_pin(AOps& o) {
    asm volatile("" : "+v"(o.fl), "+v"(o.q[0]), "+v"(o.q[1]), "+v"(o.q[2]), "+v"(o.q[3])); PIN8(o.k);
}
static constexpr int NFINE = 6;
static constexpr int NG1 = 64 - NFINE;
__device__ __forceinline__ void score_round4(float* r, const f32x4& x) {
    asm volatile("v_cvt_pk_bf16_f32 %0, 0, %4\n\tv_cvt_pk_bf16_f32 %1, 0, %5\n\tv_cvt_pk_bf16_f32 %2, 0, %6\n\tv_cvt_pk_bf16_f32 %3, 0, %7\n\t"
                 "v_mul_f32 %0, 0x3db504f3, %0\n\tv_mul_f32 %1, 0x3db504f3, %1\n\tv_mul_f32 %2, 0x3db504f3, %2\n\tv_mul_f32 %3, 0x3db504f3, %3\n\t"
                 "v_cvt_pk_bf16_f32 %0, 0, %0\n\tv_cvt_pk_bf16_f32 %1, 0, %1\n\tv_cvt_pk_bf16_f32 %2, 0, %2\n\tv_cvt_pk_bf16_f32 %3, 0, %3"
                 : "=&v"(r[0]), "=&v"(r[1]), "=&v"(r[2]), "=&v"(r[3]) : "v"(x[0]), "v"(x[1]), "v"(x[2]), "v"(x[3]));
}
__device__ __forceinline__ float vmax3(float a, float b, float c) { float r; asm("v_max3_f32 %0, %1, %2, %3" : "=v"(r) : "v"(a), "v"(b), "v"(c)); return r; }
template <int N>
__device__ __forceinline__ void attn_soft(float (&q)[12], u32x2& Pm, u32x4& Px) {
    float m = vmax3(q[0], q[1], q[2]);
#pragma unroll
    for (int i = 3; i < N; i += 2) m = vmax3(m, q[i], q[i + 1 < N ? i + 1 : i]);
    { auto a = __builtin_amdgcn_permlane16_swap(__float_as_uint(m), __float_as_uint(m), false, false); m = vmax3(m, __uint_as_float(a[0]), __uint_as_float(a[1])); }
    { auto a = __builtin_amdgcn_permlane32_swap(__float_as_uint(m), __float_as_uint(m), false, false); m = vmax3(m, __uint_as_float(a[0]), __uint_as_float(a[1])); }
    const float ml = -m * 1.4426950408889634f;
#pragma unroll
    for (int i = 0; i < N; ++i) q[i] = __builtin_amdgcn_exp2f(fmaf(q[i], 1.4426950408889634f, ml));
    float sum = 0.5f * ((q[0] + q[1]) + (q[2] + q[3])) + ((q[4] + q[5]) + (q[6] + q[7]));
    if (N > 8) sum += (q[8] + q[9]) + (q[10] + q[11]);
    sum = rowsum4(sum);
    const float inv = __builtin_amdgcn_rcpf(sum);
    Pm = u32x2{pk2bf(q[0] * inv, q[1] * inv), pk2bf(q[2] * inv, q[3] * inv)};
    Px = u32x4{pk2bf(q[4] * inv, q[5] * inv), pk2bf(q[6] * inv, q[7] * inv), N > 8 ? pk2bf(q[8] * inv, q[9] * inv) : 0u, N > 8 ? pk2bf(q[10] * inv, q[11] * inv) : 0u};
}
#define NASPLIT 28
static constexpr int NA = NASPLIT;
__device__ __forceinline__ int attn_next(int s) { return s < NA ? s + 1 : s + 2; }
__device__ __forceinline__ unsigned attn_flag(int s, int hq) { return G_WS_CTL + G_C_PC + ((s > NA && (s & 1)) ? 1024 : 0) + hq * 128; }
struct BOps { u32x4 q[8], k[8]; };
#define WLD16X4A(d0, d1, d2, d3, voff_, base_) asm volatile("s_nop 4\n\tglobal_load_dwordx4 %0, %4, %5 sc1\n\tglobal_load_dwordx4 %1, %4, %5 offset:1024 sc1\n\t" \
    "global_load_dwordx4 %2, %4, %5 offset:2048 sc1\n\tglobal_load_dwordx4 %3, %4, %5 offset:3072 sc1" \
    : "=&a"(d0), "=&a"(d1), "=&a"(d2), "=&a"(d3) : "v"(voff_), "s"(base_) : "memory")
#define PIN8A(a) asm volatile("" : "+a"(a[0]), "+a"(a[1]), "+a"(a[2]), "+a"(a[3]), "+a"(a[4]), "+a"(a[5]), "+a"(a[6]), "+a"(a[7]))
#define MFMA_AV0(acc, a_, b_) asm volatile("v_mfma_f32_16x16x32_bf16 %0, %1, %2, 0" : "=&v"(acc) : "a"(a_), "v"(b_))
#define MFMA_AV(acc, a_, b_) asm volatile("v_mfma_f32_16x16x32_bf16 %0, %1, %2, %0" : "+v"(acc) : "a"(a_), "v"(b_))
__device__ __forceinline__ void attn_batch8(const MainP& p, BOps& o, const APtr& a) {
    const char* q2 = a.q + 4096; const char* k2 = a.k + 4096;
    WLD16X4(o.q[0], o.q[1], o.q[2], o.q[3], a.voff, a.q); WLD16X4(o.q[4], o.q[5], o.q[6], o.q[7], a.voff, q2);
    WLD16X4A(o.k[0], o.k[1], o.k[2], o.k[3], a.voff, a.k); WLD16X4A(o.k[4], o.k[5], o.k[6], o.k[7], a.voff, k2);
}
__device__ __forceinline__ void attn_step(const MainP& p, BOps& o, BOps& n1, BOps& n2, APtr& a, int& kg, int& kk, int s, int h, int w, int lane) {
    const int g = lane >> 4, c = lane & 15;
    const float scale = 0.08838834764831845f;
    TST(s * 64 + h * 2, 0);
    const int s2 = attn_next(attn_next(s));
    if (kg < s2 || kk < s) {
        VMC0();
        wait_word<true>(p, p.ws + a.vfl, (lane & 1) ? s : s2, 13u);
    }
    TST(s * 64 + h * 2, 1);
    a.q = p.ws + G_D_QX + (size_t)h * G_HS8 + skew(s2, h) * 8192; a.k = a.q + (G_D_KX - G_D_QX); a.m = p.ws + G_D_KM + (size_t)h * G_HS4 + skew(s, h) * 4096;
    __builtin_amdgcn_sched_barrier(0);
    u32x4 km[4];
    int fl;
    WLD16X4A(km[0], km[1], km[2], km[3], a.vkm, a.m);
    asm volatile("global_load_dword %0, %1, %2 sc1" : "=v"(fl) : "v"(a.vfl), "s"(p.ws) : "memory");
    attn_batch8(p, n2, a);
    f32x4 y[5];
#pragma unroll
    for (int wc = 0; wc < 4; ++wc)
#pragma unroll
        for (int i = 0; i < 3; ++i) { if (wc == 0) MFMA_AV0(y[i], o.k[i >> 1], o.q[(i + 1) >> 1]); else MFMA_AV(y[i], o.k[wc * 2 + (i >> 1)], o.q[wc * 2 + ((i + 1) >> 1)]); }
    asm volatile("s_nop 7" : "+v"(y[0]), "+v"(y[1]), "+v"(y[2]));
    float q0[12], q1[12];
    score_round4(q0 + 4, y[0]); score_round4(q1 + 4, y[1]); score_round4(q1 + 8, y[2]);
#pragma unroll
    for (int i = 0; i < 4; ++i) {
        const bool vis = 4 * g + i <= c;
        q0[4 + i] = vis ? q0[4 + i] : -INFINITY; q1[8 + i] = vis ? q1[8 + i] : -INFINITY;
    }
    __builtin_amdgcn_sched_barrier(0);
    asm volatile("s_waitcnt vmcnt(16)\n\ts_barrier" : "+a"(km[0]), "+a"(km[1]), "+a"(km[2]), "+a"(km[3]), "+v"(fl) :: "memory");
    PIN8(n1.q); PIN8A(n1.k);
    { const unsigned fo = attn_flag(s, h >> 2); const int fv = s - 1;
      asm volatile("s_cmp_lg_u32 %3, 0\n\ts_cbranch_scc1 1f\n\tglobal_store_dword %0, %1, %2 sc1\n1:" :: "v"(fo), "v"(fv), "s"(p.ws), "s"(w) : "scc", "memory"); }
#pragma unroll
    for (int wc = 0; wc < 4; ++wc)
#pragma unroll
        for (int i = 0; i < 2; ++i) { if (wc == 0) MFMA_AV0(y[3 + i], km[0], o.q[i]); else MFMA_AV(y[3 + i], km[wc], o.q[wc * 2 + i]); }
    asm volatile("s_nop 7" : "+v"(y[3]), "+v"(y[4]));
    kg = __builtin_amdgcn_readlane(fl, 0); kk = __builtin_amdgcn_readlane(fl, 1);
    score_round4(q0, y[3]); score_round4(q1, y[4]);
    u32x2 Pm0, Pm1; u32x4 Px0, Px1;
    attn_soft<8>(q0, Pm0, Px0); attn_soft<12>(q1, Pm1, Px1);
    char* pxb = p.ws + G_D_PX + (size_t)(s * 32 + h) * 2048;
    char* pmb = p.ws + G_D_PM + (size_t)s * 16384 + h * 256; char* pm2 = pmb + 8192;
    WST16(a.voff, Px0, pxb, 0); WST16(a.voff, Px1, pxb, 1024);
    asm volatile("global_store_dwordx2 %0, %1, %3 sc1\n\tglobal_store_dwordx2 %0, %2, %4 sc1" :: "v"(a.voffm), "v"(Pm0), "v"(Pm1), "s"(pmb), "s"(pm2) : "memory");
    { const unsigned fo = attn_flag(s, h >> 2);
      asm volatile("s_cmp_lt_u32 %4, 46\n\ts_cbranch_scc1 1f\n\ts_waitcnt vmcnt(0)\n\ts_barrier\n\ts_cmp_lg_u32 %3, 0\n\ts_cbranch_scc1 1f\n\tglobal_store_dword %0, %1, %2 sc1\n1:"
                   :: "v"(fo), "v"(s), "s"(p.ws), "s"(w), "s"(s) : "scc", "memory"); }
    TST(s * 64 + h * 2, 2);
}
__device__ __forceinline__ void attn_role(const MainP& p, int s, int h, int w, int lane) {
    APtr a;
    a.q = p.ws + G_D_QX + (size_t)h * G_HS8 + skew(s, h) * 8192; a.k = a.q + (G_D_KX - G_D_QX);
    a.voff = lane * 16; a.vkm = (lane & 55) * 16; a.voffm = (lane & 15) * 16 + ((lane >> 4) & 1) * 8;
    a.vfl = G_WS_CTL + ((lane & 1) ? G_C_KM : G_C_GQ) + h * 128;
    BOps o0, o1, o2;
    wait_word(p, p.ws + G_WS_CTL + G_C_GQ + h * 128, attn_next(s), 10u);
    attn_batch8(p, o0, a);
    a.q = p.ws + G_D_QX + (size_t)h * G_HS8 + skew(attn_next(s), h) * 8192; a.k = a.q + (G_D_KX - G_D_QX);
    attn_batch8(p, o1, a);
    VMC0();
    PIN8(o0.q); PIN8A(o0.k); PIN8(o1.q); PIN8A(o1.k);
    int kg = -1, kk = -1;
    static_assert(NA % 2 == 0 && NG1 + 3 < NSEG, "ranges");
#pragma unroll 1
    for (;;) {
        attn_step(p, o0, o1, o2, a, kg, kk, s, h, w, lane);
        if ((s = attn_next(s)) >= NG1) break;
        attn_step(p, o1, o2, o0, a, kg, kk, s, h, w, lane);
        if ((s = attn_next(s)) >= NG1) break;
        attn_step(p, o2, o0, o1, a, kg, kk, s, h, w, lane);
        if ((s = attn_next(s)) >= NG1) break;
    }
    VMC0();
}
__device__ __forceinline__ unsigned umax4(const u32x4& v) { return max(max(v[0], v[1]), max(v[2], v[3])); }
__device__ __forceinline__ bool give_up(const MainP& p, unsigned& tries, unsigned code) {
    if (++tries <= kWaitCap / 4) return false;
    __hip_atomic_store((gu32*)(p.ws + G_WS_CTL + G_C_ERR), code, RLX_AGENT);
    return true;
}
__device__ __forceinline__ void task_attn_tail(const MainP& p, int s, int h, int w, int lane) {
    const int g = lane >> 4, c = lane & 15, qt = w & 1;
    const float scale = 0.08838834764831845f;
    APtr a;
    a.q = p.ws + G_D_QX + (size_t)h * G_HS8 + skew(s, h) * 8192 + qt * 1024; a.k = p.ws + G_D_KX + (size_t)h * G_HS8 + skew(s, h) * 8192; a.m = p.ws + G_D_KM + (size_t)h * G_HS4 + skew(s, h) * 4096;
    a.voff = lane * 16; a.vkm = (lane & 55) * 16; a.voffm = c * 16 + (g & 1) * 8; a.vfl = G_WS_CTL;
    AOps o;
    wait_word(p, p.ws + G_WS_CTL + G_C_GQ + h * 128, s, 14u);
    attn_batch(p, o, a);
    VMC0();
    attn_pin(o);
    float q[12];
    u32x4 km[4];
#pragma unroll
    for (int kt = 1; kt >= -1; --kt) {
        if (kt < 0) {
            TST(s * 64 + h * 2 + qt, 0);
            wait_word(p, p.ws + G_WS_CTL + G_C_KM + h * 128, s - 2, 15u);
            if (FLG(p.prm[5]) & 2) wait_word<true>(p, p.ws + G_WS_CTL + G_C_KM + h * 128, s - 1, 15u);
            for (unsigned tries = 0;;) {
                WLD16X4(km[0], km[1], km[2], km[3], a.vkm, a.m);
                asm volatile("s_waitcnt vmcnt(0)" : "+v"(km[0]), "+v"(km[1]), "+v"(km[2]), "+v"(km[3]) :: "memory");
                if (!__any(max(max(umax4(km[0]), umax4(km[1])), max(umax4(km[2]), umax4(km[3]))) == 0xffffffffu) || give_up(p, tries, 16u)) break;
            }
            TST(s * 64 + h * 2 + qt, 1);
        }
        f32x4 y = {0.f, 0.f, 0.f, 0.f};
#pragma unroll
        for (int wc = 0; wc < 4; ++wc) y = MFMA32(kt < 0 ? km[wc] : o.k[wc * 2 + (kt < 0 ? 0 : kt)], o.q[wc], y);
#pragma unroll
        for (int i = 0; i < 4; ++i) q[4 + 4 * kt + i] = (16 * kt + 4 * g + i <= 16 * qt + c) ? bfr(bfr(y[i]) * scale) : -INFINITY;
    }
    u32x2 Pm; u32x4 Px;
    attn_soft<12>(q, Pm, Px);
    char* pxb = p.ws + G_D_PX + (size_t)(s * 32 + h) * 2048 + qt * 1024;
    char* pmb = p.ws + G_D_PM + (size_t)s * 16384 + h * 256 + qt * 8192;
    WST16(a.voff, Px, pxb, 0);
    asm volatile("global_store_dwordx2 %0, %1, %2 sc1" :: "v"(a.voffm), "v"(Pm), "s"(pmb) : "memory");
    TST(s * 64 + h * 2 + qt, 2);
}

#define RLD(d_, voff_, base_, off_) asm volatile("global_load_dwordx4 %0, %1, %2 offset:%3 sc1" : "=v"(d_) : "v"(voff_), "s"(base_), "i"(off_) : "memory")
#define RLDA(d_, voff_, base_, off_) asm volatile("global_load_dwordx4 %0, %1, %2 offset:%3 sc1" : "=a"(d_) : "v"(voff_), "s"(base_), "i"(off_) : "memory")
#define MFMA_ACC_A(acc, a_, b_) asm volatile("v_mfma_f32_16x16x32_bf16 %0, %1, %2, %0" : "+v"(acc) : "a"(a_), "v"(b_))
#define VMW(n_) asm volatile("s_waitcnt vmcnt(%0)" :: "i"(n_) : "memory")
template <int NT, int DEPTH, bool TAIL>
__device__ __forceinline__ void task_out(const MainP& p, char* smem, int s, int ct0, int w, int lane) {
    const int g = lane >> 4, c = lane & 15;
    const unsigned voff = lane * 16;
    f32x4 acc[NT][2];
#pragma unroll
    for (int t = 0; t < NT; ++t) { acc[t][0] = f32x4{0.f, 0.f, 0.f, 0.f}; acc[t][1] = acc[t][0]; asm volatile("" : "+v"(acc[t][0]), "+v"(acc[t][1])); }
    const char* ub[NT];
#pragma unroll
    for (int t = 0; t < NT; ++t) ub[t] = uni(p.ws + G_D_UX + (size_t)(ct0 + t) * G_UXS + skew(s, ct0 + t) * 32768 + w * 8192);
    const char* pb = uni(p.ws + G_D_PX + (size_t)(s * 32 + w * 8) * 2048);
    const char* um = uni(p.ws + um_base(s, p.prm[0]) + ct0 * 16384);
    unsigned vu[NT];
#pragma unroll
    for (int t = 0; t < NT; ++t) { vu[t] = t * 16384 + w * 2048 + voff; asm volatile("" : "+v"(vu[t])); }
    const char* pm = uni(p.ws + G_D_PM + (size_t)s * 16384 + w * 2048);
    constexpr int NR = DEPTH + 1 > 10 ? 10 : DEPTH + 1;
    u32x4 a[NR][NT], b[10][2];
    unsigned vb = voff, va = voff;
    TST(4096 + (s * 36 + ct0) * 4 + w, 0);
    const int f0 = (int)__hip_atomic_load((const gu32*)(p.ws + G_WS_CTL + (lane < 8 ? G_C_UM + (w * 8 + lane) * 128 : lane < 16 ? G_C_GV + ((s < (int)G_S1 ? s & 1 : 0) * 32 + w * 8 + lane - 8) * 128
                                                                                                                                : attn_flag(s, w * 2 + (lane & 1)) - G_WS_CTL)), RLX_AGENT);
    const bool early = !TAIL && !__any(f0 < s);
    if (!early) wait_word(p, p.ws + G_WS_CTL + (lane < 8 ? G_C_GV + ((s < (int)G_S1 ? s & 1 : 0) * 32 + w * 8 + lane) * 128 : G_C_UM + w * 8 * 128), lane < 8 ? s : s - 3, 21u);
#define ALOAD(q) do { if ((q) < 8) { _Pragma("unroll") for (int t = 0; t < NT; ++t) RLDA(a[(q) % NR][t], va, ub[t], 0);  \
                                     asm volatile("v_add_u32 %0, 0x400, %0" : "+v"(va)); }                                            \
                      else { _Pragma("unroll") for (int t = 0; t < NT; ++t) RLDA(a[(q) % NR][t], vu[t], um, ((q) & 1) * 1024); } } while (0)
    constexpr int D0 = DEPTH < 8 ? DEPTH : 8;
    asm volatile("s_nop 4");
#pragma unroll
    for (int q = 0; q < D0; ++q) ALOAD(q);
    asm volatile("s_cmp_lg_u32 %0, 0\n\ts_cbranch_scc1 1f\n\ts_waitcnt vmcnt(0)\n1:\n\ts_waitcnt vmcnt(30)" :: "s"(__builtin_amdgcn_readfirstlane((int)early)) : "scc", "memory");
    unsigned tries = 0;
    if (TAIL) {
        const char* sp = pm + (lane & 7) * 256 + ((lane >> 3) & 1) * 8192;
        const int dl = (FLG(p.prm[5]) >> 26) & 31;
        if (dl) {
            wait_word<true>(p, p.ws + G_WS_CTL + G_C_KM + ((ct0 >> 1) + w) * 128, s - 1, 25u);
            for (int i = 0; i < dl + (s < 63 ? 5 : 0); ++i) __builtin_amdgcn_s_sleep(2);
        } else
        while (__any(__hip_atomic_load((const gu32*)sp, RLX_AGENT) == 0xffffffffu) && !give_up(p, tries, 23u)) __builtin_amdgcn_s_sleep(1);
    } else if (!early) wait_word(p, p.ws + G_WS_CTL + (lane < 8 ? G_C_UM + (w * 8 + lane) * 128 : attn_flag(s, w * 2 + (lane & 1)) - G_WS_CTL), s, 22u);
    if (!early) TST(4096 + (s * 36 + ct0) * 4 + w, 1);
    for (;;) {
    asm volatile("s_nop 4");
#pragma unroll
    for (int q = 0; q < 8; ++q) { RLD(b[q][0], vb, pb, 0); RLD(b[q][1], vb, pb, 1024); asm volatile("v_add_u32 %0, 0x800, %0" : "+v"(vb)); }
#pragma unroll
    for (int q = 8; q < 10; ++q) { RLD(b[q][0], voff, pm, (q & 1) * 1024); RLD(b[q][1], voff, pm + 8192, (q & 1) * 1024); }
#pragma unroll
    for (int q = D0; q < NR; ++q) ALOAD(q);
#pragma unroll
    for (int q = 0; q < 10; ++q) {
        const int issuedA = (NR + q < 10 ? NR + q : 10) - D0;
        if (q < D0) VMW(2 * (9 - q) + issuedA * NT); else VMW((issuedA - (q - D0 + 1)) * NT);
#pragma unroll
        for (int t = 0; t < NT; ++t) { MFMA_ACC_A(acc[t][0], a[q % NR][t], b[q][0]); MFMA_ACC_A(acc[t][1], a[q % NR][t], b[q][1]); }
        if (NR + q < 10) ALOAD(NR + q);
    }
    asm volatile("s_nop 7" ::: "memory");
#pragma unroll
    for (int t = 0; t < NT; ++t) asm volatile("" : "+v"(acc[t][0]), "+v"(acc[t][1]));
    if (!TAIL) break;
    f32x4 chk = acc[0][0];
#pragma unroll
    for (int t = 0; t < NT; ++t) chk += acc[t][1] + acc[t][0];
    const float ck = (chk[0] + chk[1]) + (chk[2] + chk[3]);
    if (!__any(ck != ck)) break;
    unsigned mx = 0;
#pragma unroll
    for (int q = 0; q < 10; ++q) { asm volatile("" : "+v"(b[q][0]), "+v"(b[q][1])); mx = max(mx, max(umax4(b[q][0]), umax4(b[q][1]))); }
#pragma unroll
    for (int t = 0; t < NT; ++t) { asm volatile("" : "+a"(a[8 % NR][t]), "+a"(a[9 % NR][t])); mx = max(mx, max(umax4(a[8 % NR][t]), umax4(a[9 % NR][t]))); }
    if (!__any(mx == 0xffffffffu) || give_up(p, tries, 24u)) break;
    vb = voff;
#pragma unroll
    for (int t = 0; t < NT; ++t) { acc[t][0] = f32x4{0.f, 0.f, 0.f, 0.f}; acc[t][1] = acc[t][0]; asm volatile("" : "+v"(acc[t][0]), "+v"(acc[t][1])); }
    }
    TST(4096 + (s * 36 + ct0) * 4 + w, 2);
    f32x4* red = (f32x4*)smem;
#pragma unroll
    for (int t = 0; t < NT; ++t) { red[((w * NT + t) * 2 + 0) * 64 + lane] = acc[t][0]; red[((w * NT + t) * 2 + 1) * 64 + lane] = acc[t][1]; }
    __syncthreads();
    u16* orow = p.out + (size_t)(32 * s + 16 * (w & 1) + c) * OUTW + 16 * ct0 + 4 * g;
#pragma unroll
    for (int t = 0; t < NT; ++t)
        if ((t & 1) == (w >> 1)) {
            const int m = w & 1;
            const f32x4 v = (red[((0 * NT + t) * 2 + m) * 64 + lane] + red[((1 * NT + t) * 2 + m) * 64 + lane]) +
                            (red[((2 * NT + t) * 2 + m) * 64 + lane] + red[((3 * NT + t) * 2 + m) * 64 + lane]);
            *(u32x2*)(orow + 16 * t) = u32x2{pk2bf(v[0], v[1]), pk2bf(v[2], v[3])};
        }
}

__device__ __forceinline__ int index_of_l(int l) {
    const int pv = (int)bfr((float)l);
    if (pv <= 256) return pv;
    if (pv <= 512) return 256 + ((pv - 256) >> 1);
    if (pv <= 1024) return 384 + ((pv - 512) >> 2);
    return 512 + ((pv - 1024) >> 3);
}
static constexpr int KROW = G_KROW;
static constexpr int KARR = 80 * KROW;
static constexpr int UCS = 2208;
struct BCh {
    u16 uc[4][UCS];
    f32x4 cst[NST];
    unsigned Kd[648];
    u16 Kh[KARR];
    u16 Kl[KARR];
};
static_assert(4 * sizeof(BCh) <= LDS_TOTAL - 32, "LDS");
__device__ __forceinline__ unsigned prm(unsigned hi, unsigned lo, unsigned sel) { return __builtin_amdgcn_perm(hi, lo, sel); }
#define BST(i) asm volatile("" ::: "memory")
template <class F>
__device__ __forceinline__ void task_ut(const MainP& p, char* smem, int rb, int w, int lane, F more) {
    const int tid = w * 64 + lane, g = lane >> 4, c = lane & 15;
    { char* ws = p.ws; if (tid == 0) IST(4, rb); }
    const u32x4* src = (const u32x4*)(p.U + (size_t)rb * 64 * DM);
    u32x4 v[18];
#pragma unroll
    for (int i = 0; i < 18; ++i) v[i] = __builtin_nontemporal_load(src + tid + 256 * i);
#pragma unroll
    for (int i = 0; i < 18; ++i) { const int ch = tid + 256 * i, row = ch / 72; *(u32x4*)(smem + row * 1184 + (ch - 72 * row) * 16) = v[i]; }
    __syncthreads();
    { char* ws = p.ws; if (tid == 0) IST(3, rb); }
    const unsigned ad = (unsigned)(uintptr_t)smem + (16 * g + (c >> 2)) * 1184 + (w * 144 + 4 * (c & 3)) * 2;
    char* tw = smem + 77824 + w * 20736;
#pragma unroll
    for (int i = 0; i < 9; ++i) {
        u32x2 x0, x1, x2, x3;
        asm volatile("ds_read_b64_tr_b16 %0, %4 offset:%5\n\tds_read_b64_tr_b16 %1, %4 offset:%6\n\tds_read_b64_tr_b16 %2, %4 offset:%7\n\tds_read_b64_tr_b16 %3, %4 offset:%8\n\ts_waitcnt lgkmcnt(0)"
                     : "=&v"(x0), "=&v"(x1), "=&v"(x2), "=&v"(x3) : "v"(ad), "i"(i * 32), "i"(4 * 1184 + i * 32), "i"(8 * 1184 + i * 32), "i"(12 * 1184 + i * 32) : "memory");
        u32x4* o = (u32x4*)(tw + (16 * i + c) * 144 + g * 32);
        o[0] = u32x4{x0[0], x0[1], x1[0], x1[1]}; o[1] = u32x4{x2[0], x2[1], x3[0], x3[1]};
    }
    asm volatile("" ::: "memory");
    const unsigned vo = (w * 144 + (lane >> 3)) * (SL * 2) + rb * 128 + (lane & 7) * 16;
    const char* dst = p.ws + G_D_UT;
#pragma unroll
    for (int i = 0; i < 18; ++i) {
        const u32x4 a = *(const u32x4*)(tw + (8 * i + (lane >> 3)) * 144 + (lane & 7) * 16);
        const unsigned vi = vo + i * 8 * (SL * 2);
        WST16(vi, a, dst, 0);
    }
    VMC0();
    init_wait(p.ws, w, lane);
    if (tid == 0) __hip_atomic_store((gu32*)(p.ws + G_WS_CTL + G_C_UT + rb * 128), 1u, RLX_AGENT);
    { char* ws = p.ws; if (tid == 0) IST(2, rb); }
    more();
    VMC0();
    __syncthreads();
    if (tid == 0) __hip_atomic_store((gu32*)(p.ws + G_WS_CTL + G_C_UT + rb * 128), 2u, RLX_AGENT);
}
struct KPar { u16 ldt, pa[2][4]; };
__device__ __forceinline__ KPar kgen_par(const MainP& p, int d, int lane) {
    KPar k;
    k.ldt = p.logdt[d];
#pragma unroll
    for (int q = 0; q < 2; ++q) { const int i = d * NST + lane + 64 * q; k.pa[q][0] = p.logA[i]; k.pa[q][1] = p.Aim[i]; k.pa[q][2] = p.Cre[i]; k.pa[q][3] = p.Cim[i]; }
    return k;
}
__device__ __forceinline__ void task_s4d(const MainP& p, BCh& L, int d, int lane, bool first, int& nb) {
    const int g = lane >> 4, c = lane & 15;
    BST(0);
    const KPar kp = kgen_par(p, d, lane);
    const float dsk = bf2f(p.Dskip[d]);
    const auto& pa = kp.pa;
    const float dt = bfr(expf(bf2f(kp.ldt)));
#pragma unroll
    for (int q = 0; q < 2; ++q) {
        const float ar = -bfr(expf(bf2f(pa[q][0])));
        const float cr = bf2f(pa[q][2]) * dt, ci = bf2f(pa[q][3]) * dt;
        L.cst[lane + 64 * q] = f32x4{ar * dt * 1.4426950408889634f, bf2f(pa[q][1]) * dt * 0.15915494309189535f,
                                     __log2f(hypotf(cr, ci)), atan2f(ci, cr) * 0.15915494309189535f};
    }
    BST(1);
    {
        f32x4 acc[3];
#pragma unroll
        for (int v = 0; v < 3; ++v) acc[v] = f32x4{0.f, 0.f, 0.f, 0.f};
        auto base_of = [](int v, int r) { return v == 0 ? (r & 7) + 128 * (r >> 3) : v == 1 ? (r < 8 ? 256 + 2 * (r & 3) + 128 * (r >> 2) : 512 + 4 * (r & 1) + 128 * ((r - 8) >> 1))
                                                                                           : 1024 + 128 * min(r, 8); };
        const float b0 = (float)base_of(0, c), b1 = (float)base_of(1, c), b2 = (float)base_of(2, c), off = (float)(8 * c);
#define KG_POLAR(h_, l_, lg_, f_, a_, b_) do { const float m_ = __builtin_amdgcn_exp2f(fmaf(cc[0], b_, lg_)), p_ = __builtin_amdgcn_fractf(fmaf(a_, b_, f_)); \
            const float re_ = m_ * __builtin_amdgcn_cosf(p_), im_ = m_ * __builtin_amdgcn_sinf(p_);                                                            \
            h_ = pk2bf(re_, im_); l_ = pk2bf(re_ - lo2f(h_), im_ - hi2f(h_)); } while (0)
#pragma unroll 1
        for (int t = 0; t < NST / 16; ++t) {
            u32x4 fh, fl, ah[3], al[3];
#pragma unroll
            for (int j = 0; j < 4; ++j) {
                const f32x4 cc = L.cst[16 * t + 4 * g + j];
                KG_POLAR(fh[j], fl[j], 0.f, 0.f, -cc[1], off);
                KG_POLAR(ah[0][j], al[0][j], cc[2], cc[3], cc[1], b0); KG_POLAR(ah[1][j], al[1][j], cc[2], cc[3], cc[1], b1); KG_POLAR(ah[2][j], al[2][j], cc[2], cc[3], cc[1], b2);
            }
#pragma unroll
            for (int v = 0; v < 3; ++v) { acc[v] = MFMA32(al[v], fh, acc[v]); acc[v] = MFMA32(ah[v], fl, acc[v]); acc[v] = MFMA32(ah[v], fh, acc[v]); }
        }
        BST(2);
        auto split = [](float k) { const unsigned hi = pk2bf(k, 0.f) & 0xffffu; return hi | (pk2bf(0.f, k - __uint_as_float(hi << 16)) & 0xffff0000u); };
#pragma unroll
        for (int v = 0; v < 3; ++v)
#pragma unroll
            for (int i = 0; i < 4; ++i) {
                const int pos = base_of(v, 4 * g + i) + 8 * c;
                if (pos <= 2048) L.Kd[index_of_l(pos)] = split(2.0f * acc[v][i]);
            }
    }
    if (first) wait_word(p, p.ws + G_WS_CTL + G_C_UT + (lane & 31) * 128, 1, 30u);
    const char* ut = uni(p.ws + G_D_UT + (size_t)d * 4096);
    const unsigned voff = lane * 16;
    u32x4 uu[4];
    WLD16X4(uu[0], uu[1], uu[2], uu[3], voff, ut);
    const u32x4 z4 = {0u, 0u, 0u, 0u};
    *(u32x4*)&L.Kh[8 * lane] = z4; *(u32x4*)&L.Kl[8 * lane] = z4;
    *(u32x4*)&L.Kh[512 + 8 * (lane & 31)] = z4; *(u32x4*)&L.Kl[512 + 8 * (lane & 31)] = z4;
    *(u32x4*)&L.uc[0][64 + SL] = z4;
    *(u32x4*)&L.uc[(lane >> 3) & 3][8 * (lane & 7)] = z4;
    asm volatile("" ::: "memory");
#pragma unroll
    for (int q = 0; q < 4; ++q) {
        const int e = lane + 64 * q, at = (16 + (e >> 2)) * KROW + 24 - 8 * (e & 3);
        unsigned k[8];
#pragma unroll
        for (int j = 0; j < 8; ++j) k[j] = L.Kd[index_of_l(8 * e + 7 - j)];
        u32x4 kh, kl;
#pragma unroll
        for (int j = 0; j < 4; ++j) { kh[j] = prm(k[2 * j + 1], k[2 * j], 0x05040100u); kl[j] = prm(k[2 * j + 1], k[2 * j], 0x07060302u); }
        *(u32x4*)&L.Kh[at] = kh; *(u32x4*)&L.Kl[at] = kl;
    }
    __builtin_amdgcn_sched_barrier(0);
    asm volatile("s_waitcnt vmcnt(0)" : "+v"(uu[0]), "+v"(uu[1]), "+v"(uu[2]), "+v"(uu[3]) :: "memory");
    BST(3);
#pragma unroll
    for (int q = 0; q < 4; ++q) *(u32x4*)&L.uc[0][64 + 8 * (lane + 64 * q)] = uu[q];
    asm volatile("" ::: "memory");
#pragma unroll
    for (int q = 0; q < 4; ++q) {
        const int e = lane + 64 * q;
        const int ue = 56 + 8 * e;
        const u32x4 a = *(const u32x4*)&L.uc[0][ue];
        const u32x2 b = *(const u32x2*)&L.uc[0][ue + 8];
        const unsigned s0 = __builtin_amdgcn_alignbit(a[1], a[0], 16), s1 = __builtin_amdgcn_alignbit(a[2], a[1], 16), s2 = __builtin_amdgcn_alignbit(a[3], a[2], 16),
                       s3 = __builtin_amdgcn_alignbit(b[0], a[3], 16), s4 = __builtin_amdgcn_alignbit(b[1], b[0], 16);
        *(u32x4*)&L.uc[1][ue] = u32x4{s0, s1, s2, s3};
        *(u32x4*)&L.uc[2][ue] = u32x4{a[1], a[2], a[3], b[0]};
        *(u32x4*)&L.uc[3][ue] = u32x4{s1, s2, s3, s4};
    }
    if (lane == 0) {
        const u32x4 a = *(const u32x4*)&L.uc[0][56 + SL];
        const unsigned s0 = __builtin_amdgcn_alignbit(a[1], a[0], 16), s1 = __builtin_amdgcn_alignbit(a[2], a[1], 16), s2 = __builtin_amdgcn_alignbit(a[3], a[2], 16), s3 = a[3] >> 16;
        *(u32x4*)&L.uc[1][56 + SL] = u32x4{s0, s1, s2, s3};
        *(u32x4*)&L.uc[2][56 + SL] = u32x4{a[1], a[2], a[3], 0u};
        *(u32x4*)&L.uc[3][56 + SL] = u32x4{s1, s2, s3, 0u};
    }
    BST(4);
    {
        const int o = 33 + c + 8 * g;
        const unsigned ua = (unsigned)(uintptr_t)&L.uc[o & 3][o & ~3], ka = (unsigned)(uintptr_t)&L.Kh[(16 + c) * KROW + 8 * g];
        const f32x4* res = (const f32x4*)L.Kh + lane;
        asm volatile(ASM_CV :: "v"(ua), "v"(ka), "v"((unsigned)(uintptr_t)res) : CV_CLOB);
        BST(5);
#pragma unroll
        for (int par = 0; par < 2; ++par)
#pragma unroll
            for (int t = 0; t < 4; ++t) {
                const int n = 16 * (par + 2 * (16 * t + c)) + 4 * g;
                const u32x2 uu = *(const u32x2*)&L.uc[0][64 + n];
                const f32x4 y4 = res[(par * 4 + t) * 64];
                const float r0 = bfr(y4[0]) + bfr(lo2f(uu[0]) * dsk), r1 = bfr(y4[1]) + bfr(hi2f(uu[0]) * dsk);
                const float r2 = bfr(y4[2]) + bfr(lo2f(uu[1]) * dsk), r3 = bfr(y4[3]) + bfr(hi2f(uu[1]) * dsk);
                *(u32x2*)&L.uc[1][n] = u32x2{pk2bf(r0, r1), pk2bf(r2, r3)};
            }
    }
    BST(6);
    char* yb = uni(p.ws + G_D_YB + (size_t)d * 4096);
#pragma unroll
    for (int q = 0; q < 4; ++q) { const u32x4 o = *(const u32x4*)&L.uc[1][8 * (lane + 64 * q)]; WST16(voff, o, yb, q * 1024); }
    if (lane == 0) nb = (int)(__hip_atomic_fetch_add((gu32*)(p.ws + G_WS_CTL + G_C_TASK + 128), nb >= 0 ? 1u : 0u, RLX_AGENT) + 1u);
    VMC0();
    if (lane == 0) __hip_atomic_fetch_add((gu32*)(p.ws + G_WS_CTL + G_C_YC + (d >> 6) * 128), 1u, RLX_AGENT);
    BST(7);
}
__device__ __forceinline__ void s4d_wave(const MainP& p, char* smem, int w, int lane, int quota) {
    int nb = 0;
    if (lane == 0) nb = (int)(__hip_atomic_fetch_add((gu32*)(p.ws + G_WS_CTL + G_C_TASK + 128), 1u, RLX_AGENT) + 1u);
#pragma unroll 1
    for (int n = 0; n < quota; ++n) {
        const int d = __builtin_amdgcn_readfirstlane(nb);
        if (d >= DM) break;
        nb = n + 1 < quota ? 0 : -1;
        task_s4d(p, reinterpret_cast<BCh*>(smem)[w], d, lane, n == 0, nb);
    }
}
__device__ __forceinline__ void task_merge(const MainP& p, char* smem, int gr, int i, int w, int lane) {
    const int g = lane >> 4, c = lane & 15;
    wait_word(p, p.ws + G_WS_CTL + G_C_YC + gr * 128, 63, 31u);
    const char* sb = uni(p.ws + G_D_YB + (size_t)gr * 262144 + i * 1024 + w * 256);
    const unsigned vt = g * 4096 + c * 16;
    u32x4 z[16];
#pragma unroll
    for (int j = 0; j < 16; ++j) { const char* sj = sb + j * 16384; WLD16(z[j], vt, sj, 0); }
    asm volatile("s_waitcnt vmcnt(0)" : "+v"(z[0]), "+v"(z[1]), "+v"(z[2]), "+v"(z[3]), "+v"(z[4]), "+v"(z[5]), "+v"(z[6]), "+v"(z[7]),
                 "+v"(z[8]), "+v"(z[9]), "+v"(z[10]), "+v"(z[11]), "+v"(z[12]), "+v"(z[13]), "+v"(z[14]), "+v"(z[15]) :: "memory");
    char* tw = smem + w * 17408;
#pragma unroll
    for (int j = 0; j < 16; ++j) *(u32x4*)(tw + (4 * j + g) * 272 + c * 16) = z[j];
    const unsigned ad = (unsigned)(uintptr_t)tw + (8 * g + (c >> 2)) * 272 + (c & 3) * 8;
    u16* orow = p.out + (size_t)(512 * i + 128 * w + c) * OUTW + DM + 64 * gr + 8 * g;
#pragma unroll
    for (int k = 0; k < 8; ++k) {
        u32x2 x0, x1, x2, x3;
        asm volatile("ds_read_b64_tr_b16 %0, %4 offset:%5\n\tds_read_b64_tr_b16 %1, %4 offset:%6\n\tds_read_b64_tr_b16 %2, %4 offset:%7\n\tds_read_b64_tr_b16 %3, %4 offset:%8\n\ts_waitcnt lgkmcnt(0)"
                     : "=&v"(x0), "=&v"(x1), "=&v"(x2), "=&v"(x3) : "v"(ad), "i"(k * 32), "i"(4 * 272 + k * 32), "i"(32 * 272 + k * 32), "i"(36 * 272 + k * 32) : "memory");
        *(u32x4*)(orow + (size_t)16 * k * OUTW) = u32x4{x0[0], x0[1], x1[0], x1[1]};
        *(u32x4*)(orow + (size_t)16 * k * OUTW + 32) = u32x4{x2[0], x2[1], x3[0], x3[1]};
    }
}

static constexpr int Q1 = NG1 * 6 + 36, Q2 = Q1 + (NFINE - 2) * 25;
static constexpr int NTASK = Q2 + 68;
__host__ __device__ __forceinline__ void task_of(int u, int m0, int& sg, int& r) {
    if (u < 6 * m0) { sg = u / 6; r = u % 6; }
    else if (u < 6 * m0 + 108) { sg = m0 + (u - 6 * m0) / 9; r = (u - 6 * m0) % 9; }
    else if (u < Q1) { sg = (u - 36) / 6; r = (u - 36) % 6; }
    else if (u < Q2) { sg = NG1 + (u - Q1) / 25; r = (u - Q1) % 25; }
    else { sg = 62 + (u - Q2) / 34; r = (u - Q2) % 34; }
}
__device__ __forceinline__ void worker_body(const MainP& p, char* smem, const int w, const int lane) {
    const int tid = w * 64 + lane;
    int* slot = (int*)(smem + LDS_TOTAL - 16);
    int nst = 0, nxt = -1;
    gu32* qa = (gu32*)(p.ws + G_WS_CTL + G_C_TASK);
    const int m0 = (FLH(p.prm[7]) >> 4) & 63;
    for (;;) {
        if (tid == 0) {
            *slot = nxt >= 0 ? nxt : (int)(__hip_atomic_fetch_add(qa, 1u, RLX_AGENT) + 1u);
            nxt = -1;
        }
        __syncthreads();
        const int u = __builtin_amdgcn_readfirstlane(*slot);
        if (u >= NTASK) break;
        if (tid == 0 && u < Q1 - 140) nxt = (int)(__hip_atomic_fetch_add(qa, 1u, RLX_AGENT) + 1u);
        const bool last2 = u >= Q2;
        int sg, r;
        task_of(u, m0, sg, r);
        if (u < Q1) {
            if (r < 6) task_out<6, 6, false>(p, smem, sg, 6 * r, w, lane);
            else { const int m = 3 * (sg - m0) + r - 6; task_merge(p, smem, m >> 2, m & 3, w, lane); }
        }
        else if (r < 16) task_attn_tail(p, sg, 2 * r + (w >> 1), w, lane);
        else if (last2) task_out<2, 9, true>(p, smem, sg, 2 * (r - 16), w, lane);
        else task_out<4, 9, true>(p, smem, sg, 4 * (r - 16), w, lane);
        if (last2) break;
        __syncthreads();
    }
}

__host__ __device__ constexpr unsigned phys(unsigned x) { return (x >> 13) * 16384 + (x & 8191); }
__host__ __device__ constexpr unsigned aMB(int f, int b) { return phys(f < 8 ? 16384 + f * 1024 + b : 28672 + b); }
__host__ __device__ constexpr unsigned aPX(int pr, int b) { return phys(pr * 1024 + b); }
__host__ __device__ constexpr unsigned aPM(int u, int b) { return phys(24576 + u * 1024 + b); }
static constexpr int L_KI = 8192, L_QI = 16384, L_VI = 147456;

__device__ __forceinline__ int lane_id() { int l; asm volatile("v_mbcnt_lo_u32_b32 %0, -1, 0\n\tv_mbcnt_hi_u32_b32 %0, -1, %0" : "=v"(l)); return l; }
typedef const __attribute__((address_space(4))) MainP* KArg;

__global__ __launch_bounds__(256, 1) void main_kernel(MainP p_) {
    __shared__ __attribute__((aligned(16))) char smem[LDS_TOTAL];
    KArg ka = (KArg)__builtin_amdgcn_kernarg_segment_ptr();
    static_assert(offsetof(MainP, W) == 0x28, "argument layout");
    asm volatile(ASM_EA :: "s"(ka), "s"(blockIdx.x), "v"(threadIdx.x) : "v1", "v2", "s40", "s41", "s42", "s43", "s44", "s45", "s46", "s47", "s48", "s49", "scc", "memory");
    asm volatile("" : "+s"(ka));
    const int b = blockIdx.x, x = b & 7, ix = b >> 3;
    const int w = __builtin_amdgcn_readfirstlane(threadIdx.x >> 6);
    const bool isr = x < 4 && ix >= 16;
    constexpr int NRF = 8 + 8 * G_GVN;
    if (ix < (x < 4 ? 16 : NRF)) {
        const int h = (x & 3) * 8 + (ix & 7), id = h + (ix >> 4) * 32;
        const unsigned stb = 0;
#define UNR _Pragma("unroll")
#define CW(k, val) do { if ((k) < 16) ca[(k) & 15] = (unsigned)(val); else if ((k) < 32) cb[(k) & 15] = (unsigned)(val); else cl[((k) - 40) & 15] = (unsigned)(val); } while (0)
#define ROLEW(A_) asm volatile(A_ : "+{v[0:15]}"(ca), "+{v[16:31]}"(cb), "+{v[200:215]}"(cl) : "s"(ka), "s"(w), "s"(id), "s"(stb) : ASM_CLOBW)
#define CW_ALL const int lane = lane_id(), g = lane >> 4, c = lane & 15, q = c >> 2; u32x16 ca = {}, cb = {}, cl = {};                                      \
        CW(0, lane * 16); CW(40, (4 * w + g) * 8192 + h * 256 + (c ^ (2 * g + 8 * (w >> 1))) * 16);                                                          \
        UNR for (int k = 0; k < 2; ++k) CW(41 + k, (8 * g + q) * 256 + ((4 * w + 2 * k + ((c >> 1) & 1)) ^ (2 * q + 8 * (g & 1))) * 16 + (c & 1) * 8)
#define CW_SRC(b_, key_) UNR for (int k = 0; k < 9; ++k) { const int pp = 64 * k + lane, r = pp / 72, pc = pp - 72 * r; CW(43 + k, (b_) + (8 * w + r) * 1152 + (pc ^ (key_)) * 16); }
#define UNIT(u_, key_) (((u_) >> 2) * 128 + (((u_) & 3) ^ (key_)) * 32 + (c & 3) * 8)
#define CW_NEAR CW(1, x * 65536 + aMB(2 * w, lane * 16)); CW(2, x * 65536 + aMB(8, lane * 16)); CW(3, 2 * w * 1024 + lane * 16);                            \
        UNR for (int i = 0; i < 6; ++i) CW(4 + i, ((2 * w + 2 + i) & 7) * 1024 + lane * 16);                                                                 \
        CW(10, (c & 2) ? 0xEEEE : 0x4444);                                                                                                                   \
        CW(20, x * 65536 + 22528 + (lane & 31) * 64); CW(21, lane < 32 ? (lane & 3) * 65536 + phys((lane >> 2) * 2048) + h * 64 : 32768 + h * 128);          \
        CW(22, lane * 4); CW(23, x * 65536 + aMB(2 * w + 1, lane * 16))
#define CW_FAR CW(0, c * 1152 + (g ^ (c >> 1)) * 16); CW(1, ca[0]); CW(2, ca[0] ^ 64); CW(3, w * 9216 + lane * 16); CW_SRC(0, (r >> 1) + 4 * (w & 1))                                   \
        CW(4, L_VI + w * 2048 + lane * 16); CW(5, L_VI + lane * 16); CW(6, lane * 4)
        if (x >= 4 && ix >= 8) {
            CW_ALL; CW_FAR;
            UNR for (int t = 0; t < 9; ++t) CW(8 + t, (9 * w + t) * G_UXS + skew(0, 9 * w + t) * 32768 + h * 1024 + lane * 16);
            UNR for (int k = 0; k < 2; ++k) CW(19 + k, (9 * w + 4 * k + g) * 16384 + h * 256 + c * 16);
            CW(21, (9 * w + 8) * 16384 + h * 256 + lane * 4);
            UNR for (int k = 0; k < 4; ++k) CW(52 + k, (4 * g + q) * 1152 + ((9 * w + k) >> 2) * 128 + ((2 * ((9 * w + k) & 3) + ((c >> 1) & 1)) ^ (2 * g + (q >> 1))) * 16 + (c & 1) * 8);
            ROLEW(ASM_GV);
        } else if (x >= 4) {
            CW_ALL; CW_FAR;
            CW(8, w * 2048 + lane * 16);
            ROLEW(ASM_GX);
        } else if (ix < 8) {
            CW_ALL; CW_NEAR;
            CW(12, L_QI + (16 * w + (c & 13)) * 32 + 8 * g + 4 * ((c >> 1) & 1));
            CW(13, L_QI + (8 * g + q) * 32 + (c & 3) * 8);
            CW(14, G_WS_CTL + G_C_KM + h * 128); CW(15, lane * 16 + 4096);
            CW(16, L_KI + (32 * w + c) * 16 + 4 * g);
            CW(17, L_KI + (4 * g + q) * 16 + (c & 1) * 8);
            const int mm = 8 * (w & 1) + 4 * (g >> 1) + (g & 1);
            CW(18, (x ^ 3) * 65536 + aPX(h >> 1, (h & 1) * 512 + (w & 1) * 256 + g * 64 + (c >> 3) * 32 + ((c >> 1) & 1) * 16 + (2 * (q & 1) + (c & 1)) * 4));
            CW(19, ((c & 1) + 2 * (c >> 3)) * 65536 + aPM(h >> 3, ((h >> 1) & 3) * 256 + (mm + 2 * ((c >> 1) & 1)) * 16 + (h & 1) * 8 + (q & 1) * 4));
            CW(24, G_WS_CTL + G_C_GX + h * 128);
            ROLEW(ASM_QK);
        } else {
            CW_ALL; CW_NEAR; CW_SRC(h * 147456, r & 4);
            const int tq = 9 * w + 2 * (g & 1) + (g >> 1);
            CW(14, tq * 16384 + h * 256 + c * 16);
            CW(15, (tq + 4) * 16384 + h * 256 + c * 16);
            CW(16, (9 * w + 8) * 16384 + h * 256 + c * 16 + (g & 1) * 8);
            CW(19, (c & 8) ? 0xEEEE : 0x4444);
            CW(12, L_QI + w * 576 + c * 16 + 4 * g);
            CW(13, L_QI + (8 * g + q) * 16 + (c & 1) * 8 + ((c >> 1) & 1) * 576);
            CW(25, L_QI + (8 * g + q) * 16 + (c & 1) * 8 + (1 - ((c >> 1) & 1)) * 576);
            UNR for (int k = 0; k < 4; ++k) CW(52 + k, ((k >> 1) ^ (q >> 1)) * 36864 + (4 * g + (q & 1)) * 1152 + UNIT(9 * w + 2 * (k & 1) + (q >> 1), 2 * (g & 1)));
            CW(24, (q >> 1) * 36864 + (4 * g + (q & 1)) * 1152 + UNIT(9 * w + 8, 2 * (g & 1)));
            ROLEW(ASM_V);
        }
    } else {
    if (x >= 4 && !(FLG(ka->prm[5]) & 1)) {
        const char* src = (const char*)ka->W[3] + (size_t)((x & 3) * 8 + (ix & 7)) * 147456 + threadIdx.x * 128;
#pragma unroll
        for (int i = 0; i < 5; ++i)
            if (i < 4 || w < 2) asm volatile("s_mov_b32 m0, %1\n\ts_nop 0\n\tglobal_load_lds_dword %0, off" :: "v"(src + i * 32768), "s"(155648) : "memory");
    }
    if (isr && (ix < 25 || ix >= 29)) init_cal(ka->ws, x * 12 + (ix < 25 ? ix - 13 : ix - 29), threadIdx.x, (FLG(ka->prm[5]) >> 16) & 15);
    if (x >= 4 ? ix >= 24 : (ix >= 25 && ix < 29)) init_fill(ka->ws, ka->mem0, x >= 4 ? (ix - 24) * 4 + (x & 3) : 32 + (ix - 25) * 4 + x, threadIdx.x);
    if (isr) init_wait(ka->ws, w, threadIdx.x & 63);
    asm volatile("" : "+s"(ka));
    if (x < 4 && (ix < 25 || w == 0)) {
        const int lane = threadIdx.x & 63, g = lane >> 4, c = lane & 15;
        const unsigned lb = w * G_STG;
        unsigned* ct = (unsigned*)(smem + lb) + lane;
        asm volatile("" :: "v"(ct) : "memory");
#define CTW(k, val) ct[(k) * 64] = (unsigned)(val)
        const u64 ws = (u64)ka->ws, prm = (u64)ka;
        const u64 stb = 0;
        CTW(0, lane * 16);
        const int j = x * 9 + ix - 16;
        if (ix >= 25) {
            CTW(1, x * 65536 + 22528 + (lane & 31) * 64);
            for (int k = 0; k < 7; ++k) {
                const int q = min(ix - 25 + 7 * k, 43), id = q < 36 ? 9 * x + (q >> 2) : 8 * x + q - 36;
                CTW(4 + k, (q < 36 ? G_D_UX + id * G_UXS + skew(0, id) * 32768 + (q & 3) * 8192 : G_D_KX + id * G_HS8 + skew(0, id) * 8192) + lane * 128);
                CTW(11 + k, q < 36 ? 32768 : 8192);
            }
        } else {
            const int rot = (FLG(ka->prm[5]) & 16) ? 0 : ((ix - 16) * 16) / 9;
            if (w >= 2) {
                return;
            }
            CTW(1, lane * 16 + 4096);
            CTW(2, x * 65536 + aMB(j >> 2, (j & 3) * 256 + (4 * g + 2 * ((c >> 1) & 1) + (c & 1)) * 16 + (c >> 2) * 4));
            for (int k = 0; k < 5; ++k) { const int q = 9 * k + ix - 16; CTW(46 + k, x * 65536 + lane * 16 + (q < 29 ? phys(q * 1024) : 0x200000 + phys((q - 29) * 1024))); }
            CTW(4, (c & 2) ? 0xEEEE : 0x4444); CTW(5, (c & 1) ? 0x03020706u : 0x05040100u);
            CTW(6, G_WS_CTL + G_C_GV + ((w & (G_GVN - 1)) * 32 + (lane & 31)) * 128); CTW(7, x * 65536 + (lane & 31) * 64); CTW(8, lane * 4); CTW(9, 131072);
            for (int k = 0; k < 16; ++k) { const int i = (k + rot) & 15; CTW(10 + k, x * 65536 + aPX(i, (g >> 1) * 512 + (2 * (c >> 2) + (c & 1)) * 64 + (g & 1) * 32 + ((c >> 1) & 1) * 16)); CTW(26 + k, i * 2048 + lane * 16); }
            for (int k = 0; k < 4; ++k) CTW(42 + k, x * 65536 + aPM(k, lane * 16));
        }
        const int id = ix >= 25 ? ix - 25 : j;
#define ROLE(A_) asm volatile(A_ :: "s"(ws), "s"(ws), "s"(ws), "s"(w), "s"(id), "s"(ws), "s"(prm), "s"(lb), "s"(stb) : ASM_CLOB)
        if (ix >= 25) ROLE(ASM_H);
        else ROLE(ASM_R);
    }
    else if (x >= 4 && ix >= 24) {
        if (ix >= 25) for (int i = 0; i < ((FLG(ka->prm[5]) >> 5) & 7); ++i) __builtin_amdgcn_s_sleep(36);
        const char* xs = (const char*)ka->X + (size_t)((ix - 24) * 4 + (x & 3)) * 73728 + threadIdx.x * 128;
        const int f = (ix - 24) * 4 + (x & 3);
        const u16* ps = (w == 0 ? ka->logA : w == 1 ? ka->Aim : w == 2 ? ka->Cre : ka->Cim) + f * 2304 + min((int)(threadIdx.x & 63), 35) * 64;
        asm volatile("s_mov_b32 m0, %4\n\ts_nop 0\n\tglobal_load_lds_dword %0, off\n\tglobal_load_lds_dword %1, off\n\tglobal_load_lds_dword %2, off\n\tglobal_load_lds_dword %3, off"
                     :: "v"(xs), "v"(xs + 32768), "v"(xs + 65536 - w * 8192), "v"(ps), "s"(163072) : "memory");
        if (ix < 25) for (int i = 0; i < ((FLG(ka->prm[5]) >> 5) & 7); ++i) __builtin_amdgcn_s_sleep(36);
    }
    }
    asm volatile("" : "+s"(ka));
    MainP p;
    p.prm[5] = ka->prm[5]; p.prm[7] = ka->prm[7];
    p.U = ka->U; p.ws = ka->ws; p.logA = ka->logA; p.Aim = ka->Aim; p.Cre = ka->Cre; p.Cim = ka->Cim; p.logdt = ka->logdt; p.Dskip = ka->Dskip; p.out = ka->out;
    if (x >= 4 && ix >= 24) {
        const u32x4 sent = {0xffffffffu, 0xffffffffu, 0xffffffffu, 0xffffffffu};
        const int tid = w * 64 + lane_id(), h = (ix - 24) * 4 + (x & 3);
        task_ut(p, smem, h, w, lane_id(), [&] {
#pragma unroll 1
            for (int sg = NG1; sg < 64; ++sg) {
                const unsigned ok = G_D_KM + h * G_HS4 + skew(sg, h) * 4096 + tid * 16;
                const unsigned op = tid < 128 ? G_D_PX + (sg * 32 + h) * 2048 + tid * 16 : G_D_PM + sg * 16384 + ((tid >> 4) & 1) * 8192 + h * 256 + (tid & 15) * 16;
                asm volatile("global_store_dwordx4 %0, %2, %3 sc1\n\tglobal_store_dwordx4 %1, %2, %3 sc1\n\ts_nop 1" :: "v"(ok), "v"(op), "v"(sent), "s"(p.ws) : "memory");
            }
        });
    }
    {
        const int nq = x < 4 ? ((FLH(p.prm[7]) & 2) ? 0 : 1000) : ix < 8 ? (FLG(p.prm[5]) >> 8) & 3 : ix < 16 ? (FLG(p.prm[5]) >> 10) & 3 : ix < 22 ? (FLG(p.prm[5]) >> 12) & 3 : ix >= 24 && ix < 30 ? (FLG(p.prm[5]) >> 14) & 3 : 0;
        if (nq) {
            for (int i = 0; i < (int)(((ix & 7) * 4 + w) * ((FLG(p.prm[5]) >> 23) & 7)); ++i) __builtin_amdgcn_s_sleep(3);
            s4d_wave(p, smem, w, lane_id(), nq == 3 ? 1000 : nq);
        }
        if (x < 4) return;
        __syncthreads();
    }
    if ((ix & 22) == 22) { attn_role(p, ix >= 30 ? 0 : NA + 1, (x & 3) * 8 + (ix & 1) * 4 + w, w, lane_id());
        if (b == 255) {
            wait_word(p, p.ws + G_WS_CTL + G_C_ARR, 255, 50u);
            if (w == 0 && lane_id() == 0) __hip_atomic_fetch_add((gu32*)(p.ws + G_WS_EP), 1u, RLX_AGENT);
        }
        __syncthreads();
    }
    p.prm[0] = near_mask(p.ws, lane_id());
    worker_body(p, smem, w, lane_id());
}

extern "C" __global__ void code_end_marker() {}

static constexpr int g_prm[8] = {PRM_DEFAULT};

extern "C" void kernel_launch(void* const* d_in, const int* in_sizes, int n_in, void* d_out, int out_size,
                              void* d_ws, size_t ws_size, hipStream_t stream) {
    char* ws = (char*)(((uintptr_t)d_ws + 0x1FFFFF) & ~(uintptr_t)0x1FFFFF);
    MainP p;
    for (int i = 0; i < 8; ++i) p.prm[i] = g_prm[i];
    p.mem0 = (const u16*)d_in[1];
    for (int i = 0; i < 4; ++i) p.W[i] = (const u16*)d_in[2 + i];
    p.X = (const u16*)d_in[0]; p.U = (const u16*)d_in[7]; p.ws = ws;
    p.logA = (const u16*)d_in[8]; p.Aim = (const u16*)d_in[9]; p.Cre = (const u16*)d_in[10]; p.Cim = (const u16*)d_in[11];
    p.logdt = (const u16*)d_in[12]; p.Dskip = (const u16*)d_in[13]; p.out = (u16*)d_out;
    main_kernel<<<256, 256, 0, stream>>>(p);
}
```

```cpp
#include <hip/hip_runtime.h>
#include <hip/hip_bf16.h>
#include <stdint.h>
#include <stddef.h>

typedef __attribute__((ext_vector_type(8))) __bf16 bf16x8;
typedef __attribute__((ext_vector_type(4))) short s16x4;
typedef __attribute__((ext_vector_type(4))) float f32x4;
typedef __attribute__((ext_vector_type(4))) unsigned u32x4;
typedef __attribute__((ext_vector_type(2))) unsigned u32x2;
typedef __attribute__((ext_vector_type(2))) float f32x2;
typedef __attribute__((ext_vector_type(16))) unsigned u32x16;
typedef unsigned short u16;
typedef unsigned long long u64;

#define GAS __attribute__((address_space(1)))
typedef GAS unsigned gu32;
#define RLX_AGENT __ATOMIC_RELAXED, __HIP_MEMORY_SCOPE_AGENT

static constexpr int DM = 576, NH = 32, DH = 128, MM = 8, SEG = 32, NSEG = 64, SL = 2048;
static constexpr int HD = NH * DH;
static constexpr int OUTW = 2 * DM;
static constexpr int NST = 128;

__device__ __forceinline__ float bf2f(u16 h) { return __uint_as_float(((unsigned)h) << 16); }
typedef __attribute__((ext_vector_type(2))) __bf16 bf16x2_t;
__device__ __forceinline__ unsigned pk2bf(float a, float b) {
    bf16x2_t v; v[0] = (__bf16)a; v[1] = (__bf16)b;
    return __builtin_bit_cast(unsigned, v);
}
__device__ __forceinline__ float bfr(float f) { return __uint_as_float(pk2bf(f, f) << 16); }
__device__ __forceinline__ float lo2f(unsigned u) { return __uint_as_float(u << 16); }
__device__ __forceinline__ float hi2f(unsigned u) { return __uint_as_float(u & 0xffff0000u); }
__device__ __forceinline__ float rowmax4(float x) {
    auto a = __builtin_amdgcn_permlane16_swap(__float_as_uint(x), __float_as_uint(x), false, false);
    x = fmaxf(__uint_as_float(a[0]), __uint_as_float(a[1]));
    auto b = __builtin_amdgcn_permlane32_swap(__float_as_uint(x), __float_as_uint(x), false, false);
    return fmaxf(__uint_as_float(b[0]), __uint_as_float(b[1]));
}
__device__ __forceinline__ float rowsum4(float x) {
    auto a = __builtin_amdgcn_permlane16_swap(__float_as_uint(x), __float_as_uint(x), false, false);
    x = __uint_as_float(a[0]) + __uint_as_float(a[1]);
    auto b = __builtin_amdgcn_permlane32_swap(__float_as_uint(x), __float_as_uint(x), false, false);
    return __uint_as_float(b[0]) + __uint_as_float(b[1]);
}

struct MainP {
    int prm[8];
    const u16* mem0; const u16* W[4]; const u16* X; const u16* U;
    char* ws;
    const u16* logA; const u16* Aim; const u16* Cre; const u16* Cim; const u16* logdt; const u16* Dskip;
    u16* out;
};
static constexpr int LDS_TOTAL = 163840;
static constexpr unsigned kWaitCap = 1u << 18;
#define VMC0() asm volatile("s_waitcnt vmcnt(0)" ::: "memory")
#define PRM_DEFAULT 1060, 600, 120, 680, 752, 1653346507, 3145936, 705
#define ASM_QK \
    "s_load_dwordx2 s[42:43], %3, 40\n\t" \
    "s_load_dwordx2 s[44:45], %3, 48\n\t" \
    "s_load_dwordx2 s[36:37], %3, 0x58\n\t" \
    "s_load_dwordx2 s[50:51], %3, 0x48\n\t" \
    "s_load_dwordx8 s[84:91], %3, 0\n\t" \
    "s_mov_b32 s66, %4\n\t" \
    "s_mov_b32 s67, %5\n\t" \
    "s_mov_b32 s68, 262144\n\t" \
    "s_mov_b32 s72, 0\n\t" \
    "s_mov_b32 s83, -1\n\t" \
    "s_mov_b32 s65, 0\n\t" \
    "s_lshl_b32 s82, s66, 10\n\t" \
    "s_waitcnt lgkmcnt(0)\n\t" \
    "s_add_u32 s46, s42, 2097152\n\t" \
    "s_addc_u32 s47, s43, 0\n\t" \
    "s_add_u32 m0, s82, 65536\n\t" \
    "s_nop 0\n\t" \
    "global_load_lds_dwordx4 v200, s[46:47]\n\t" \
    "s_add_u32 s46, s42, 2228224\n\t" \
    "s_addc_u32 s47, s43, 0\n\t" \
    "s_add_u32 m0, s82, 69632\n\t" \
    "s_nop 0\n\t" \
    "global_load_lds_dwordx4 v200, s[46:47]\n\t" \
    "s_add_u32 s46, s42, 2359296\n\t" \
    "s_addc_u32 s47, s43, 0\n\t" \
    "s_add_u32 m0, s82, 73728\n\t" \
    "s_nop 0\n\t" \
    "global_load_lds_dwordx4 v200, s[46:47]\n\t" \
    "s_add_u32 s46, s42, 2490368\n\t" \
    "s_addc_u32 s47, s43, 0\n\t" \
    "s_add_u32 m0, s82, 77824\n\t" \
    "s_nop 0\n\t" \
    "global_load_lds_dwordx4 v200, s[46:47]\n\t" \
    "s_add_u32 s46, s42, 2621440\n\t" \
    "s_addc_u32 s47, s43, 0\n\t" \
    "s_add_u32 m0, s82, 81920\n\t" \
    "s_nop 0\n\t" \
    "global_load_lds_dwordx4 v200, s[46:47]\n\t" \
    "s_add_u32 s46, s42, 2752512\n\t" \
    "s_addc_u32 s47, s43, 0\n\t" \
    "s_add_u32 m0, s82, 86016\n\t" \
    "s_nop 0\n\t" \
    "global_load_lds_dwordx4 v200, s[46:47]\n\t" \
    "s_add_u32 s46, s42, 2883584\n\t" \
    "s_addc_u32 s47, s43, 0\n\t" \
    "s_add_u32 m0, s82, 90112\n\t" \
    "s_nop 0\n\t" \
    "global_load_lds_dwordx4 v200, s[46:47]\n\t" \
    "s_add_u32 s46, s42, 3014656\n\t" \
    "s_addc_u32 s47, s43, 0\n\t" \
    "s_add_u32 m0, s82, 94208\n\t" \
    "s_nop 0\n\t" \
    "global_load_lds_dwordx4 v200, s[46:47]\n\t" \
    "s_add_u32 s46, s42, 3145728\n\t" \
    "s_addc_u32 s47, s43, 0\n\t" \
    "s_add_u32 m0, s82, 98304\n\t" \
    "s_nop 0\n\t" \
    "global_load_lds_dwordx4 v200, s[46:47]\n\t" \
    "s_add_u32 s46, s42, 3276800\n\t" \
    "s_addc_u32 s47, s43, 0\n\t" \
    "s_add_u32 m0, s82, 102400\n\t" \
    "s_nop 0\n\t" \
    "global_load_lds_dwordx4 v200, s[46:47]\n\t" \
    "s_add_u32 s46, s42, 3407872\n\t" \
    "s_addc_u32 s47, s43, 0\n\t" \
    "s_add_u32 m0, s82, 106496\n\t" \
    "s_nop 0\n\t" \
    "global_load_lds_dwordx4 v200, s[46:47]\n\t" \
    "s_add_u32 s46, s42, 3538944\n\t" \
    "s_addc_u32 s47, s43, 0\n\t" \
    "s_add_u32 m0, s82, 110592\n\t" \
    "s_nop 0\n\t" \
    "global_load_lds_dwordx4 v200, s[46:47]\n\t" \
    "s_add_u32 s46, s42, 3670016\n\t" \
    "s_addc_u32 s47, s43, 0\n\t" \
    "s_add_u32 m0, s82, 114688\n\t" \
    "s_nop 0\n\t" \
    "global_load_lds_dwordx4 v200, s[46:47]\n\t" \
    "s_add_u32 s46, s42, 3801088\n\t" \
    "s_addc_u32 s47, s43, 0\n\t" \
    "s_add_u32 m0, s82, 118784\n\t" \
    "s_nop 0\n\t" \
    "global_load_lds_dwordx4 v200, s[46:47]\n\t" \
    "s_add_u32 s46, s42, 3932160\n\t" \
    "s_addc_u32 s47, s43, 0\n\t" \
    "s_add_u32 m0, s82, 122880\n\t" \
    "s_nop 0\n\t" \
    "global_load_lds_dwordx4 v200, s[46:47]\n\t" \
    "s_add_u32 s46, s42, 4063232\n\t" \
    "s_addc_u32 s47, s43, 0\n\t" \
    "s_add_u32 m0, s82, 126976\n\t" \
    "s_nop 0\n\t" \
    "global_load_lds_dwordx4 v200, s[46:47]\n\t" \
    "s_add_u32 s46, s42, 4194304\n\t" \
    "s_addc_u32 s47, s43, 0\n\t" \
    "s_add_u32 m0, s82, 131072\n\t" \
    "s_nop 0\n\t" \
    "global_load_lds_dwordx4 v200, s[46:47]\n\t" \
    "s_add_u32 s46, s42, 4325376\n\t" \
    "s_addc_u32 s47, s43, 0\n\t" \
    "s_add_u32 m0, s82, 135168\n\t" \
    "s_nop 0\n\t" \
    "global_load_lds_dwordx4 v200, s[46:47]\n\t" \
    "s_add_u32 s46, s42, 4456448\n\t" \
    "s_addc_u32 s47, s43, 0\n\t" \
    "s_add_u32 m0, s82, 139264\n\t" \
    "s_nop 0\n\t" \
    "global_load_lds_dwordx4 v200, s[46:47]\n\t" \
    "s_add_u32 s46, s42, 4587520\n\t" \
    "s_addc_u32 s47, s43, 0\n\t" \
    "s_add_u32 m0, s82, 143360\n\t" \
    "s_nop 0\n\t" \
    "global_load_lds_dwordx4 v200, s[46:47]\n\t" \
    "s_mov_b32 s77, 3019898880\n\t" \
    "s_mov_b32 s78, 3054198966\n\t" \
    "s_waitcnt lgkmcnt(0)\n\t" \
    "s_mov_b32 s70, 116\n\t" \
    "qk_E1_%=:\n\t" \
    "s_memtime s[56:57]\n\t" \
    "s_memrealtime s[2:3]\n\t" \
    "s_nop 0\n\t" \
    "s_memrealtime s[4:5]\n\t" \
    "s_nop 0\n\t" \
    "s_memrealtime s[6:7]\n\t" \
    "s_nop 0\n\t" \
    "s_memrealtime s[8:9]\n\t" \
    "s_nop 0\n\t" \
    "s_memrealtime s[10:11]\n\t" \
    "s_nop 0\n\t" \
    "s_memrealtime s[12:13]\n\t" \
    "s_nop 0\n\t" \
    "s_memrealtime s[14:15]\n\t" \
    "s_nop 0\n\t" \
    "s_memrealtime s[16:17]\n\t" \
    "s_nop 0\n\t" \
    "s_memrealtime s[18:19]\n\t" \
    "s_nop 0\n\t" \
    "s_memrealtime s[20:21]\n\t" \
    "s_nop 0\n\t" \
    "s_memrealtime s[22:23]\n\t" \
    "s_nop 0\n\t" \
    "s_memrealtime s[24:25]\n\t" \
    "s_nop 0\n\t" \
    "s_memrealtime s[26:27]\n\t" \
    "s_nop 0\n\t" \
    "s_memrealtime s[28:29]\n\t" \
    "s_memtime s[54:55]\n\t" \
    "s_add_u32 s70, s70, 8\n\t" \
    "s_waitcnt lgkmcnt(0)\n\t" \
    "s_sub_u32 s69, s54, s56\n\t" \
    "s_cmp_gt_u32 s69, s70\n\t" \
    "s_cbranch_scc1 qk_E1_%=\n\t" \
    "s_add_u32 s2, s2, s4\n\t" \
    "s_add_u32 s6, s6, s8\n\t" \
    "s_add_u32 s10, s10, s12\n\t" \
    "s_add_u32 s14, s14, s16\n\t" \
    "s_add_u32 s18, s18, s20\n\t" \
    "s_add_u32 s22, s22, s24\n\t" \
    "s_add_u32 s2, s2, s6\n\t" \
    "s_add_u32 s10, s10, s14\n\t" \
    "s_add_u32 s18, s18, s22\n\t" \
    "s_add_u32 s2, s2, s26\n\t" \
    "s_add_u32 s10, s10, s18\n\t" \
    "s_add_u32 s2, s2, s10\n\t" \
    "s_mul_i32 s70, s28, 13\n\t" \
    "s_sub_u32 s70, s70, s2\n\t" \
    "s_min_u32 s70, s70, 52\n\t" \
    "s_mul_i32 s69, s69, s70\n\t" \
    "s_mul_hi_u32 s69, s69, 76695844\n\t" \
    "s_mov_b32 s75, s73\n\t" \
    "s_mov_b32 s76, s74\n\t" \
    "s_add_u32 s73, s56, s69\n\t" \
    "s_lshl_b32 s74, s28, 4\n\t" \
    "s_waitcnt vmcnt(32) lgkmcnt(0)\n\t" \
    "s_barrier\n\t" \
    "ds_read_b64_tr_b16 a[0:1], v201 offset:0\n\t" \
    "ds_read_b64_tr_b16 a[2:3], v201 offset:1024\n\t" \
    "ds_read_b64_tr_b16 a[4:5], v201 offset:8192\n\t" \
    "ds_read_b64_tr_b16 a[6:7], v201 offset:9216\n\t" \
    "ds_read_b64_tr_b16 a[72:73], v202 offset:0\n\t" \
    "ds_read_b64_tr_b16 a[74:75], v202 offset:1024\n\t" \
    "ds_read_b64_tr_b16 a[76:77], v202 offset:8192\n\t" \
    "ds_read_b64_tr_b16 a[78:79], v202 offset:9216\n\t" \
    "s_waitcnt vmcnt(28) lgkmcnt(0)\n\t" \
    "s_barrier\n\t" \
    "ds_read_b64_tr_b16 a[8:9], v201 offset:16384\n\t" \
    "ds_read_b64_tr_b16 a[10:11], v201 offset:17408\n\t" \
    "ds_read_b64_tr_b16 a[12:13], v201 offset:24576\n\t" \
    "ds_read_b64_tr_b16 a[14:15], v201 offset:25600\n\t" \
    "ds_read_b64_tr_b16 a[80:81], v202 offset:16384\n\t" \
    "ds_read_b64_tr_b16 a[82:83], v202 offset:17408\n\t" \
    "ds_read_b64_tr_b16 a[84:85], v202 offset:24576\n\t" \
    "ds_read_b64_tr_b16 a[86:87], v202 offset:25600\n\t" \
    "s_add_u32 s46, s44, 0\n\t" \
    "s_addc_u32 s47, s45, 0\n\t" \
    "s_add_u32 m0, s82, 0\n\t" \
    "s_nop 0\n\t" \
    "global_load_lds_dwordx4 v200, s[46:47]\n\t" \
    "s_add_u32 s46, s44, 131072\n\t" \
    "s_addc_u32 s47, s45, 0\n\t" \
    "s_add_u32 m0, s82, 4096\n\t" \
    "s_nop 0\n\t" \
    "global_load_lds_dwordx4 v200, s[46:47]\n\t" \
    "s_add_u32 s46, s44, 262144\n\t" \
    "s_addc_u32 s47, s45, 0\n\t" \
    "s_add_u32 m0, s82, 8192\n\t" \
    "s_nop 0\n\t" \
    "global_load_lds_dwordx4 v200, s[46:47]\n\t" \
    "s_add_u32 s46, s44, 393216\n\t" \
    "s_addc_u32 s47, s45, 0\n\t" \
    "s_add_u32 m0, s82, 12288\n\t" \
    "s_nop 0\n\t" \
    "global_load_lds_dwordx4 v200, s[46:47]\n\t" \
    "s_waitcnt vmcnt(28) lgkmcnt(0)\n\t" \
    "s_barrier\n\t" \
    "ds_read_b64_tr_b16 a[16:17], v201 offset:32768\n\t" \
    "ds_read_b64_tr_b16 a[18:19], v201 offset:33792\n\t" \
    "ds_read_b64_tr_b16 a[20:21], v201 offset:40960\n\t" \
    "ds_read_b64_tr_b16 a[22:23], v201 offset:41984\n\t" \
    "ds_read_b64_tr_b16 a[88:89], v202 offset:32768\n\t" \
    "ds_read_b64_tr_b16 a[90:91], v202 offset:33792\n\t" \
    "ds_read_b64_tr_b16 a[92:93], v202 offset:40960\n\t" \
    "ds_read_b64_tr_b16 a[94:95], v202 offset:41984\n\t" \
    "s_add_u32 s46, s44, 524288\n\t" \
    "s_addc_u32 s47, s45, 0\n\t" \
    "s_add_u32 m0, s82, 16384\n\t" \
    "s_nop 0\n\t" \
    "global_load_lds_dwordx4 v200, s[46:47]\n\t" \
    "s_add_u32 s46, s44, 655360\n\t" \
    "s_addc_u32 s47, s45, 0\n\t" \
    "s_add_u32 m0, s82, 20480\n\t" \
    "s_nop 0\n\t" \
    "global_load_lds_dwordx4 v200, s[46:47]\n\t" \
    "s_add_u32 s46, s44, 786432\n\t" \
    "s_addc_u32 s47, s45, 0\n\t" \
    "s_add_u32 m0, s82, 24576\n\t" \
    "s_nop 0\n\t" \
    "global_load_lds_dwordx4 v200, s[46:47]\n\t" \
    "s_add_u32 s46, s44, 917504\n\t" \
    "s_addc_u32 s47, s45, 0\n\t" \
    "s_add_u32 m0, s82, 28672\n\t" \
    "s_nop 0\n\t" \
    "global_load_lds_dwordx4 v200, s[46:47]\n\t" \
    "s_waitcnt vmcnt(28) lgkmcnt(0)\n\t" \
    "s_barrier\n\t" \
    "ds_read_b64_tr_b16 a[24:25], v201 offset:49152\n\t" \
    "ds_read_b64_tr_b16 a[26:27], v201 offset:50176\n\t" \
    "ds_read_b64_tr_b16 a[28:29], v201 offset:57344\n\t" \
    "ds_read_b64_tr_b16 a[30:31], v201 offset:58368\n\t" \
    "ds_read_b64_tr_b16 a[96:97], v202 offset:49152\n\t" \
    "ds_read_b64_tr_b16 a[98:99], v202 offset:50176\n\t" \
    "ds_read_b64_tr_b16 a[100:101], v202 offset:57344\n\t" \
    "ds_read_b64_tr_b16 a[102:103], v202 offset:58368\n\t" \
    "s_add_u32 s46, s44, 1048576\n\t" \
    "s_addc_u32 s47, s45, 0\n\t" \
    "s_add_u32 m0, s82, 32768\n\t" \
    "s_nop 0\n\t" \
    "global_load_lds_dwordx4 v200, s[46:47]\n\t" \
    "s_add_u32 s46, s44, 1179648\n\t" \
    "s_addc_u32 s47, s45, 0\n\t" \
    "s_add_u32 m0, s82, 36864\n\t" \
    "s_nop 0\n\t" \
    "global_load_lds_dwordx4 v200, s[46:47]\n\t" \
    "s_add_u32 s46, s44, 1310720\n\t" \
    "s_addc_u32 s47, s45, 0\n\t" \
    "s_add_u32 m0, s82, 40960\n\t" \
    "s_nop 0\n\t" \
    "global_load_lds_dwordx4 v200, s[46:47]\n\t" \
    "s_add_u32 s46, s44, 1441792\n\t" \
    "s_addc_u32 s47, s45, 0\n\t" \
    "s_add_u32 m0, s82, 45056\n\t" \
    "s_nop 0\n\t" \
    "global_load_lds_dwordx4 v200, s[46:47]\n\t" \
    "s_waitcnt vmcnt(28) lgkmcnt(0)\n\t" \
    "s_barrier\n\t" \
    "v_add_u32 v201, 0x10000, v201\n\t" \
    "v_add_u32 v202, 0x10000, v202\n\t" \
    "ds_read_b64_tr_b16 a[32:33], v201 offset:0\n\t" \
    "ds_read_b64_tr_b16 a[34:35], v201 offset:1024\n\t" \
    "ds_read_b64_tr_b16 a[36:37], v201 offset:8192\n\t" \
    "ds_read_b64_tr_b16 a[38:39], v201 offset:9216\n\t" \
    "ds_read_b64_tr_b16 a[104:105], v202 offset:0\n\t" \
    "ds_read_b64_tr_b16 a[106:107], v202 offset:1024\n\t" \
    "ds_read_b64_tr_b16 a[108:109], v202 offset:8192\n\t" \
    "ds_read_b64_tr_b16 a[110:111], v202 offset:9216\n\t" \
    "s_add_u32 s46, s44, 1572864\n\t" \
    "s_addc_u32 s47, s45, 0\n\t" \
    "s_add_u32 m0, s82, 49152\n\t" \
    "s_nop 0\n\t" \
    "global_load_lds_dwordx4 v200, s[46:47]\n\t" \
    "s_add_u32 s46, s44, 1703936\n\t" \
    "s_addc_u32 s47, s45, 0\n\t" \
    "s_add_u32 m0, s82, 53248\n\t" \
    "s_nop 0\n\t" \
    "global_load_lds_dwordx4 v200, s[46:47]\n\t" \
    "s_add_u32 s46, s44, 1835008\n\t" \
    "s_addc_u32 s47, s45, 0\n\t" \
    "s_add_u32 m0, s82, 57344\n\t" \
    "s_nop 0\n\t" \
    "global_load_lds_dwordx4 v200, s[46:47]\n\t" \
    "s_add_u32 s46, s44, 1966080\n\t" \
    "s_addc_u32 s47, s45, 0\n\t" \
    "s_add_u32 m0, s82, 61440\n\t" \
    "s_nop 0\n\t" \
    "global_load_lds_dwordx4 v200, s[46:47]\n\t" \
    "s_waitcnt vmcnt(28) lgkmcnt(0)\n\t" \
    "s_barrier\n\t" \
    "ds_read_b64_tr_b16 a[40:41], v201 offset:16384\n\t" \
    "ds_read_b64_tr_b16 a[42:43], v201 offset:17408\n\t" \
    "ds_read_b64_tr_b16 a[44:45], v201 offset:24576\n\t" \
    "ds_read_b64_tr_b16 a[46:47], v201 offset:25600\n\t" \
    "ds_read_b64_tr_b16 a[112:113], v202 offset:16384\n\t" \
    "ds_read_b64_tr_b16 a[114:115], v202 offset:17408\n\t" \
    "ds_read_b64_tr_b16 a[116:117], v202 offset:24576\n\t" \
    "ds_read_b64_tr_b16 a[118:119], v202 offset:25600\n\t" \
    "s_add_u32 s46, s44, 2097152\n\t" \
    "s_addc_u32 s47, s45, 0\n\t" \
    "s_add_u32 m0, s82, 65536\n\t" \
    "s_nop 0\n\t" \
    "global_load_lds_dwordx4 v200, s[46:47]\n\t" \
    "s_add_u32 s46, s44, 2228224\n\t" \
    "s_addc_u32 s47, s45, 0\n\t" \
    "s_add_u32 m0, s82, 69632\n\t" \
    "s_nop 0\n\t" \
    "global_load_lds_dwordx4 v200, s[46:47]\n\t" \
    "s_add_u32 s46, s44, 2359296\n\t" \
    "s_addc_u32 s47, s45, 0\n\t" \
    "s_add_u32 m0, s82, 73728\n\t" \
    "s_nop 0\n\t" \
    "global_load_lds_dwordx4 v200, s[46:47]\n\t" \
    "s_add_u32 s46, s44, 2490368\n\t" \
    "s_addc_u32 s47, s45, 0\n\t" \
    "s_add_u32 m0, s82, 77824\n\t" \
    "s_nop 0\n\t" \
    "global_load_lds_dwordx4 v200, s[46:47]\n\t" \
    "s_waitcnt vmcnt(28) lgkmcnt(0)\n\t" \
    "s_barrier\n\t" \
    "ds_read_b64_tr_b16 a[48:49], v201 offset:32768\n\t" \
    "ds_read_b64_tr_b16 a[50:51], v201 offset:33792\n\t" \
    "ds_read_b64_tr_b16 a[52:53], v201 offset:40960\n\t" \
    "ds_read_b64_tr_b16 a[54:55], v201 offset:41984\n\t" \
    "ds_read_b64_tr_b16 a[120:121], v202 offset:32768\n\t" \
    "ds_read_b64_tr_b16 a[122:123], v202 offset:33792\n\t" \
    "ds_read_b64_tr_b16 a[124:125], v202 offset:40960\n\t" \
    "ds_read_b64_tr_b16 a[126:127], v202 offset:41984\n\t" \
    "s_add_u32 s46, s44, 2621440\n\t" \
    "s_addc_u32 s47, s45, 0\n\t" \
    "s_add_u32 m0, s82, 81920\n\t" \
    "s_nop 0\n\t" \
    "global_load_lds_dwordx4 v200, s[46:47]\n\t" \
    "s_add_u32 s46, s44, 2752512\n\t" \
    "s_addc_u32 s47, s45, 0\n\t" \
    "s_add_u32 m0, s82, 86016\n\t" \
    "s_nop 0\n\t" \
    "global_load_lds_dwordx4 v200, s[46:47]\n\t" \
    "s_add_u32 s46, s44, 2883584\n\t" \
    "s_addc_u32 s47, s45, 0\n\t" \
    "s_add_u32 m0, s82, 90112\n\t" \
    "s_nop 0\n\t" \
    "global_load_lds_dwordx4 v200, s[46:47]\n\t" \
    "s_add_u32 s46, s44, 3014656\n\t" \
    "s_addc_u32 s47, s45, 0\n\t" \
    "s_add_u32 m0, s82, 94208\n\t" \
    "s_nop 0\n\t" \
    "global_load_lds_dwordx4 v200, s[46:47]\n\t" \
    "s_waitcnt vmcnt(28) lgkmcnt(0)\n\t" \
    "s_barrier\n\t" \
    "ds_read_b64_tr_b16 a[56:57], v201 offset:49152\n\t" \
    "ds_read_b64_tr_b16 a[58:59], v201 offset:50176\n\t" \
    "ds_read_b64_tr_b16 a[60:61], v201 offset:57344\n\t" \
    "ds_read_b64_tr_b16 a[62:63], v201 offset:58368\n\t" \
    "ds_read_b64_tr_b16 a[128:129], v202 offset:49152\n\t" \
    "ds_read_b64_tr_b16 a[130:131], v202 offset:50176\n\t" \
    "ds_read_b64_tr_b16 a[132:133], v202 offset:57344\n\t" \
    "ds_read_b64_tr_b16 a[134:135], v202 offset:58368\n\t" \
    "s_add_u32 s46, s44, 3145728\n\t" \
    "s_addc_u32 s47, s45, 0\n\t" \
    "s_add_u32 m0, s82, 98304\n\t" \
    "s_nop 0\n\t" \
    "global_load_lds_dwordx4 v200, s[46:47]\n\t" \
    "s_add_u32 s46, s44, 3276800\n\t" \
    "s_addc_u32 s47, s45, 0\n\t" \
    "s_add_u32 m0, s82, 102400\n\t" \
    "s_nop 0\n\t" \
    "global_load_lds_dwordx4 v200, s[46:47]\n\t" \
    "s_add_u32 s46, s44, 3407872\n\t" \
    "s_addc_u32 s47, s45, 0\n\t" \
    "s_add_u32 m0, s82, 106496\n\t" \
    "s_nop 0\n\t" \
    "global_load_lds_dwordx4 v200, s[46:47]\n\t" \
    "s_add_u32 s46, s44, 3538944\n\t" \
    "s_addc_u32 s47, s45, 0\n\t" \
    "s_add_u32 m0, s82, 110592\n\t" \
    "s_nop 0\n\t" \
    "global_load_lds_dwordx4 v200, s[46:47]\n\t" \
    "v_mbcnt_lo_u32_b32 v160, -1, 0\n\t" \
    "v_mbcnt_hi_u32_b32 v160, -1, v160\n\t" \
    "v_lshlrev_b32 v160, 6, v160\n\t" \
    "v_add_u32 v160, 6225920, v160\n\t" \
    "v_mov_b32 v161, 6234112\n\t" \
    "global_load_dword v161, v161, s[36:37] sc1\n\t" \
    "global_load_dwordx2 v[162:163], v160, s[36:37] sc1\n\t" \
    "global_load_dword v164, v160, s[36:37] offset:2048 sc1\n\t" \
    "s_waitcnt vmcnt(31) lgkmcnt(0)\n\t" \
    "s_barrier\n\t" \
    "v_add_u32 v201, 0x10000, v201\n\t" \
    "v_add_u32 v202, 0x10000, v202\n\t" \
    "ds_read_b64_tr_b16 a[64:65], v201 offset:0\n\t" \
    "ds_read_b64_tr_b16 a[66:67], v201 offset:1024\n\t" \
    "ds_read_b64_tr_b16 a[68:69], v201 offset:8192\n\t" \
    "ds_read_b64_tr_b16 a[70:71], v201 offset:9216\n\t" \
    "ds_read_b64_tr_b16 a[136:137], v202 offset:0\n\t" \
    "ds_read_b64_tr_b16 a[138:139], v202 offset:1024\n\t" \
    "ds_read_b64_tr_b16 a[140:141], v202 offset:8192\n\t" \
    "ds_read_b64_tr_b16 a[142:143], v202 offset:9216\n\t" \
    "v_add_u32 v201, 0xfffe0000, v201\n\t" \
    "v_add_u32 v202, 0xfffe0000, v202\n\t" \
    "s_add_u32 s46, s44, 3670016\n\t" \
    "s_addc_u32 s47, s45, 0\n\t" \
    "s_add_u32 m0, s82, 114688\n\t" \
    "s_nop 0\n\t" \
    "global_load_lds_dwordx4 v200, s[46:47]\n\t" \
    "s_add_u32 s46, s44, 3801088\n\t" \
    "s_addc_u32 s47, s45, 0\n\t" \
    "s_add_u32 m0, s82, 118784\n\t" \
    "s_nop 0\n\t" \
    "global_load_lds_dwordx4 v200, s[46:47]\n\t" \
    "s_add_u32 s46, s44, 3932160\n\t" \
    "s_addc_u32 s47, s45, 0\n\t" \
    "s_add_u32 m0, s82, 122880\n\t" \
    "s_nop 0\n\t" \
    "global_load_lds_dwordx4 v200, s[46:47]\n\t" \
    "s_add_u32 s46, s44, 4063232\n\t" \
    "s_addc_u32 s47, s45, 0\n\t" \
    "s_add_u32 m0, s82, 126976\n\t" \
    "s_nop 0\n\t" \
    "global_load_lds_dwordx4 v200, s[46:47]\n\t" \
    "s_waitcnt lgkmcnt(0)\n\t" \
    "s_barrier\n\t" \
    "s_add_u32 s46, s44, 4194304\n\t" \
    "s_addc_u32 s47, s45, 0\n\t" \
    "s_add_u32 m0, s82, 131072\n\t" \
    "s_nop 0\n\t" \
    "global_load_lds_dwordx4 v200, s[46:47]\n\t" \
    "s_add_u32 s46, s44, 4325376\n\t" \
    "s_addc_u32 s47, s45, 0\n\t" \
    "s_add_u32 m0, s82, 135168\n\t" \
    "s_nop 0\n\t" \
    "global_load_lds_dwordx4 v200, s[46:47]\n\t" \
    "s_add_u32 s46, s44, 4456448\n\t" \
    "s_addc_u32 s47, s45, 0\n\t" \
    "s_add_u32 m0, s82, 139264\n\t" \
    "s_nop 0\n\t" \
    "global_load_lds_dwordx4 v200, s[46:47]\n\t" \
    "s_add_u32 s46, s44, 4587520\n\t" \
    "s_addc_u32 s47, s45, 0\n\t" \
    "s_add_u32 m0, s82, 143360\n\t" \
    "s_nop 0\n\t" \
    "global_load_lds_dwordx4 v200, s[46:47]\n\t" \
    "s_waitcnt vmcnt(35) lgkmcnt(0)\n\t" \
    "s_barrier\n\t" \
    "ds_read_b64_tr_b16 a[144:145], v201 offset:0\n\t" \
    "ds_read_b64_tr_b16 a[146:147], v201 offset:1024\n\t" \
    "ds_read_b64_tr_b16 a[148:149], v201 offset:8192\n\t" \
    "ds_read_b64_tr_b16 a[150:151], v201 offset:9216\n\t" \
    "ds_read_b64_tr_b16 a[216:217], v202 offset:0\n\t" \
    "ds_read_b64_tr_b16 a[218:219], v202 offset:1024\n\t" \
    "ds_read_b64_tr_b16 a[220:221], v202 offset:8192\n\t" \
    "ds_read_b64_tr_b16 a[222:223], v202 offset:9216\n\t" \
    "s_waitcnt vmcnt(31) lgkmcnt(0)\n\t" \
    "s_barrier\n\t" \
    "ds_read_b64_tr_b16 a[152:153], v201 offset:16384\n\t" \
    "ds_read_b64_tr_b16 a[154:155], v201 offset:17408\n\t" \
    "ds_read_b64_tr_b16 a[156:157], v201 offset:24576\n\t" \
    "ds_read_b64_tr_b16 a[158:159], v201 offset:25600\n\t" \
    "ds_read_b64_tr_b16 a[224:225], v202 offset:16384\n\t" \
    "ds_read_b64_tr_b16 a[226:227], v202 offset:17408\n\t" \
    "ds_read_b64_tr_b16 a[228:229], v202 offset:24576\n\t" \
    "ds_read_b64_tr_b16 a[230:231], v202 offset:25600\n\t" \
    "s_waitcnt vmcnt(27) lgkmcnt(0)\n\t" \
    "s_barrier\n\t" \
    "ds_read_b64_tr_b16 a[160:161], v201 offset:32768\n\t" \
    "ds_read_b64_tr_b16 a[162:163], v201 offset:33792\n\t" \
    "ds_read_b64_tr_b16 a[164:165], v201 offset:40960\n\t" \
    "ds_read_b64_tr_b16 a[166:167], v201 offset:41984\n\t" \
    "ds_read_b64_tr_b16 a[232:233], v202 offset:32768\n\t" \
    "ds_read_b64_tr_b16 a[234:235], v202 offset:33792\n\t" \
    "ds_read_b64_tr_b16 a[236:237], v202 offset:40960\n\t" \
    "ds_read_b64_tr_b16 a[238:239], v202 offset:41984\n\t" \
    "s_waitcnt vmcnt(23) lgkmcnt(0)\n\t" \
    "s_barrier\n\t" \
    "ds_read_b64_tr_b16 a[168:169], v201 offset:49152\n\t" \
    "ds_read_b64_tr_b16 a[170:171], v201 offset:50176\n\t" \
    "ds_read_b64_tr_b16 a[172:173], v201 offset:57344\n\t" \
    "ds_read_b64_tr_b16 a[174:175], v201 offset:58368\n\t" \
    "ds_read_b64_tr_b16 a[240:241], v202 offset:49152\n\t" \
    "ds_read_b64_tr_b16 a[242:243], v202 offset:50176\n\t" \
    "ds_read_b64_tr_b16 a[244:245], v202 offset:57344\n\t" \
    "ds_read_b64_tr_b16 a[246:247], v202 offset:58368\n\t" \
    "s_waitcnt vmcnt(19) lgkmcnt(0)\n\t" \
    "s_barrier\n\t" \
    "v_add_u32 v201, 0x10000, v201\n\t" \
    "v_add_u32 v202, 0x10000, v202\n\t" \
    "ds_read_b64_tr_b16 a[176:177], v201 offset:0\n\t" \
    "ds_read_b64_tr_b16 a[178:179], v201 offset:1024\n\t" \
    "ds_read_b64_tr_b16 a[180:181], v201 offset:8192\n\t" \
    "ds_read_b64_tr_b16 a[182:183], v201 offset:9216\n\t" \
    "ds_read_b64_tr_b16 a[248:249], v202 offset:0\n\t" \
    "ds_read_b64_tr_b16 a[250:251], v202 offset:1024\n\t" \
    "ds_read_b64_tr_b16 a[252:253], v202 offset:8192\n\t" \
    "ds_read_b64_tr_b16 a[254:255], v202 offset:9216\n\t" \
    "s_waitcnt vmcnt(15) lgkmcnt(0)\n\t" \
    "s_barrier\n\t" \
    "ds_read_b64_tr_b16 a[184:185], v201 offset:16384\n\t" \
    "ds_read_b64_tr_b16 a[186:187], v201 offset:17408\n\t" \
    "ds_read_b64_tr_b16 a[188:189], v201 offset:24576\n\t" \
    "ds_read_b64_tr_b16 a[190:191], v201 offset:25600\n\t" \
    "ds_read_b64_tr_b16 v[224:225], v202 offset:16384\n\t" \
    "ds_read_b64_tr_b16 v[226:227], v202 offset:17408\n\t" \
    "ds_read_b64_tr_b16 v[228:229], v202 offset:24576\n\t" \
    "ds_read_b64_tr_b16 v[230:231], v202 offset:25600\n\t" \
    "s_waitcnt vmcnt(11) lgkmcnt(0)\n\t" \
    "s_barrier\n\t" \
    "ds_read_b64_tr_b16 a[192:193], v201 offset:32768\n\t" \
    "ds_read_b64_tr_b16 a[194:195], v201 offset:33792\n\t" \
    "ds_read_b64_tr_b16 a[196:197], v201 offset:40960\n\t" \
    "ds_read_b64_tr_b16 a[198:199], v201 offset:41984\n\t" \
    "ds_read_b64_tr_b16 v[232:233], v202 offset:32768\n\t" \
    "ds_read_b64_tr_b16 v[234:235], v202 offset:33792\n\t" \
    "ds_read_b64_tr_b16 v[236:237], v202 offset:40960\n\t" \
    "ds_read_b64_tr_b16 v[238:239], v202 offset:41984\n\t" \
    "s_waitcnt vmcnt(8)\n\t" \
    "qk_C2_%=:\n\t" \
    "v_cmp_ne_u32 s[54:55], v162, v164\n\t" \
    "v_sub_u32 v162, v162, v161\n\t" \
    "v_cmp_ne_u32 vcc, 1, v162\n\t" \
    "s_nop 1\n\t" \
    "s_or_b64 vcc, vcc, s[54:55]\n\t" \
    "s_cbranch_scc0 qk_Cok3_%=\n\t" \
    "s_cmp_eq_u32 s68, 0\n\t" \
    "s_cbranch_scc1 qk_F5_%=\n\t" \
    "s_sub_u32 s68, s68, 1\n\t" \
    "s_sleep 6\n\t" \
    "s_branch qk_Cag4_%=\n\t" \
    "qk_F5_%=:\n\t" \
    "v_mov_b32 v164, 5767424\n\t" \
    "v_mov_b32 v165, 4\n\t" \
    "global_store_dword v164, v165, s[36:37] sc1\n\t" \
    "s_waitcnt vmcnt(0)\n\t" \
    "s_branch qk_Cok3_%=\n\t" \
    "qk_Cag4_%=:\n\t" \
    "global_load_dwordx2 v[162:163], v160, s[36:37] sc1\n\t" \
    "global_load_dword v164, v160, s[36:37] offset:2048 sc1\n\t" \
    "s_waitcnt vmcnt(0)\n\t" \
    "s_branch qk_C2_%=\n\t" \
    "qk_Cok3_%=:\n\t" \
    "s_cmp_lg_u32 s66, 0\n\t" \
    "s_cbranch_scc1 qk_Ca6_%=\n\t" \
    "v_mov_b32 v160, 5836800\n\t" \
    "s_mov_b64 exec, 1\n\t" \
    "global_atomic_add v160, v162, s[36:37] sc1\n\t" \
    "s_mov_b64 exec, -1\n\t" \
    "qk_Ca6_%=:\n\t" \
    "v_mov_b32_dpp v160, v163 quad_perm:[1,0,3,2] row_mask:0xf bank_mask:0xf\n\t" \
    "v_cmp_lt_u32 s[44:45], v160, v163\n\t" \
    "s_nop 1\n\t" \
    "s_bitcmp1_b32 s44, 0\n\t" \
    "s_cselect_b32 s62, 8192, 0\n\t" \
    "s_bitcmp1_b32 s44, 2\n\t" \
    "s_cselect_b32 s63, 8192, 0\n\t" \
    "s_or_b32 s63, s63, 0x200000\n\t" \
    "v_min_u32 v160, v160, v163\n\t" \
    "v_cmp_lt_u32 vcc, 660, v160\n\t" \
    "s_nop 1\n\t" \
    "s_cmp_lg_u64 vcc, 0\n\t" \
    "s_cselect_b32 s69, 1, 0\n\t" \
    "s_mul_i32 s70, s69, 160\n\t" \
    "s_add_u32 s86, s86, s70\n\t" \
    "s_mul_i32 s70, s69, 536\n\t" \
    "s_add_u32 s87, s87, s70\n\t" \
    "s_mul_i32 s70, s69, 256\n\t" \
    "s_add_u32 s88, s88, s70\n\t" \
    "global_load_dword v25, v24, s[36:37] sc1\n\t" \
    "s_waitcnt vmcnt(5) lgkmcnt(0)\n\t" \
    "s_barrier\n\t" \
    "ds_read_b64_tr_b16 a[200:201], v201 offset:49152\n\t" \
    "ds_read_b64_tr_b16 a[202:203], v201 offset:50176\n\t" \
    "ds_read_b64_tr_b16 a[204:205], v201 offset:57344\n\t" \
    "ds_read_b64_tr_b16 a[206:207], v201 offset:58368\n\t" \
    "ds_read_b64_tr_b16 v[240:241], v202 offset:49152\n\t" \
    "ds_read_b64_tr_b16 v[242:243], v202 offset:50176\n\t" \
    "ds_read_b64_tr_b16 v[244:245], v202 offset:57344\n\t" \
    "ds_read_b64_tr_b16 v[246:247], v202 offset:58368\n\t" \
    "s_waitcnt vmcnt(1) lgkmcnt(0)\n\t" \
    "s_barrier\n\t" \
    "v_add_u32 v201, 0x10000, v201\n\t" \
    "v_add_u32 v202, 0x10000, v202\n\t" \
    "ds_read_b64_tr_b16 a[208:209], v201 offset:0\n\t" \
    "ds_read_b64_tr_b16 a[210:211], v201 offset:1024\n\t" \
    "ds_read_b64_tr_b16 a[212:213], v201 offset:8192\n\t" \
    "ds_read_b64_tr_b16 a[214:215], v201 offset:9216\n\t" \
    "ds_read_b64_tr_b16 v[248:249], v202 offset:0\n\t" \
    "ds_read_b64_tr_b16 v[250:251], v202 offset:1024\n\t" \
    "ds_read_b64_tr_b16 v[252:253], v202 offset:8192\n\t" \
    "ds_read_b64_tr_b16 v[254:255], v202 offset:9216\n\t" \
    "v_add_u32 v201, 0xfffe0000, v201\n\t" \
    "v_add_u32 v202, 0xfffe0000, v202\n\t" \
    "s_waitcnt lgkmcnt(0)\n\t" \
    "s_barrier\n\t" \
    "s_mul_i32 s70, s67, 7\n\t" \
    "s_add_u32 s69, s65, 0\n\t" \
    "s_xor_b32 s69, s69, s70\n\t" \
    "s_and_b32 s69, s69, 63\n\t" \
    "s_lshl_b32 s69, s69, 13\n\t" \
    "s_mul_i32 s70, s67, 524288\n\t" \
    "s_add_u32 s69, s69, s70\n\t" \
    "s_add_u32 s69, s69, 23068672\n\t" \
    "s_add_u32 s38, s36, s69\n\t" \
    "s_addc_u32 s39, s37, 0\n\t" \
    "v_mov_b32 v26, 0x3db504f3\n\t" \
    "v_mov_b32 v28, 0x3fb8aa3b\n\t" \
    "v_mov_b32 v27, 0x3db504f3\n\t" \
    "v_mov_b32 v29, 0x3fb8aa3b\n\t" \
    "v_xor_b32 v180, 65536, v18\n\t" \
    "v_xor_b32 v181, 131072, v18\n\t" \
    "v_xor_b32 v182, 196608, v18\n\t" \
    "s_mov_b32 s48, 0xaaaaaaaa\n\t" \
    "s_mov_b32 s49, 0xaaaaaaaa\n\t" \
    "s_mov_b32 s58, 0x00ff00ff\n\t" \
    "s_mov_b32 s59, 0x00ff00ff\n\t" \
    "v_mov_b32 v31, -1\n\t" \
    "s_mov_b32 s91, -1\n\t" \
    "ds_write_b32 v12, v31 offset:0\n\t" \
    "ds_write_b32 v12, v31 offset:64\n\t" \
    "s_waitcnt vmcnt(0)\n\t" \
    "s_cmp_gt_u32 s66, 1\n\t" \
    "s_cbranch_scc1 qk_nk7_%=\n\t" \
    "v_readfirstlane_b32 s83, v25\n\t" \
    "s_nop 1\n\t" \
    "qk_WF8_%=:\n\t" \
    "s_cmp_ge_i32 s83, s65\n\t" \
    "s_cbranch_scc1 qk_WFok9_%=\n\t" \
    "global_load_dword v160, v24, s[36:37] sc1\n\t" \
    "s_waitcnt vmcnt(0)\n\t" \
    "v_readfirstlane_b32 s83, v160\n\t" \
    "s_nop 1\n\t" \
    "s_cmp_ge_i32 s83, s65\n\t" \
    "s_cbranch_scc1 qk_WFok9_%=\n\t" \
    "s_cmp_eq_u32 s68, 0\n\t" \
    "s_cbranch_scc1 qk_F10_%=\n\t" \
    "s_sub_u32 s68, s68, 1\n\t" \
    "s_sleep 2\n\t" \
    "s_branch qk_WF8_%=\n\t" \
    "qk_F10_%=:\n\t" \
    "v_mov_b32 v160, 5767424\n\t" \
    "v_mov_b32 v161, 8\n\t" \
    "global_store_dword v160, v161, s[36:37] sc1\n\t" \
    "s_waitcnt vmcnt(0)\n\t" \
    "s_branch qk_WFok9_%=\n\t" \
    "qk_WFok9_%=:\n\t" \
    "global_load_dwordx4 v[100:103], v0, s[38:39] offset:0 sc1\n\t" \
    "global_load_dwordx4 v[104:107], v0, s[38:39] offset:2048 sc1\n\t" \
    "global_load_dwordx4 v[108:111], v15, s[38:39] offset:0 sc1\n\t" \
    "global_load_dwordx4 v[112:115], v15, s[38:39] offset:2048 sc1\n\t" \
    "global_load_dwordx4 v[116:119], v0, s[38:39] offset:1024 sc1\n\t" \
    "global_load_dwordx4 v[120:123], v0, s[38:39] offset:3072 sc1\n\t" \
    "global_load_dwordx4 v[124:127], v15, s[38:39] offset:1024 sc1\n\t" \
    "global_load_dwordx4 v[128:131], v15, s[38:39] offset:3072 sc1\n\t" \
    "qk_nk7_%=:\n\t" \
    "v_mov_b32 v33, v21\n\t" \
    "v_readlane_b32 s69, v21, 63\n\t" \
    "s_nop 1\n\t" \
    "v_mov_b32 v21, s69\n\t" \
    "s_mov_b32 s79, s74\n\t" \
    "s_mov_b32 s80, s74\n\t" \
    "s_cmp_eq_u32 s66, 1\n\t" \
    "s_cbranch_scc1 qk_loop12_%=\n\t" \
    "s_cmp_eq_u32 s66, 2\n\t" \
    "s_cbranch_scc1 qk_loop13_%=\n\t" \
    "s_cmp_eq_u32 s66, 3\n\t" \
    "s_cbranch_scc1 qk_loop14_%=\n\t" \
    "qk_loop11_%=:\n\t" \
    "v_mov_b32 v72, 0\n\t" \
    "v_mov_b32 v73, 0\n\t" \
    "v_mov_b32 v74, 0\n\t" \
    "v_mov_b32 v75, 0\n\t" \
    "v_mov_b32 v76, 0\n\t" \
    "v_mov_b32 v77, 0\n\t" \
    "v_mov_b32 v78, 0\n\t" \
    "v_mov_b32 v79, 0\n\t" \
    "v_mov_b32 v80, 0\n\t" \
    "v_mov_b32 v81, 0\n\t" \
    "v_mov_b32 v82, 0\n\t" \
    "v_mov_b32 v83, 0\n\t" \
    "v_mov_b32 v84, 0\n\t" \
    "v_mov_b32 v85, 0\n\t" \
    "v_mov_b32 v86, 0\n\t" \
    "v_mov_b32 v87, 0\n\t" \
    "v_mov_b32 v88, 0\n\t" \
    "v_mov_b32 v89, 0\n\t" \
    "v_mov_b32 v90, 0\n\t" \
    "v_mov_b32 v91, 0\n\t" \
    "v_mov_b32 v92, 0\n\t" \
    "v_mov_b32 v93, 0\n\t" \
    "v_mov_b32 v94, 0\n\t" \
    "v_mov_b32 v95, 0\n\t" \
    "v_mov_b32 v96, 0\n\t" \
    "v_mov_b32 v97, 0\n\t" \
    "v_mov_b32 v98, 0\n\t" \
    "v_mov_b32 v99, 0\n\t" \
    "s_add_u32 s69, s65, 0\n\t" \
    "s_and_b32 s69, s69, 7\n\t" \
    "s_lshl_b32 s69, s69, 18\n\t" \
    "s_add_u32 s69, s69, s62\n\t" \
    "s_add_u32 s46, s36, s69\n\t" \
    "s_addc_u32 s47, s37, 0\n\t" \
    "s_sub_u32 s70, s79, s74\n\t" \
    "s_max_i32 s70, s70, 0\n\t" \
    "s_min_i32 s70, s70, 16000\n\t" \
    "s_lshl_b32 s70, s70, 1\n\t" \
    "s_mul_hi_u32 s70, s70, s77\n\t" \
    "s_add_u32 s70, s70, s73\n\t" \
    ".p2align 6\n\t" \
    "s_nop 0\n\t" \
    "s_nop 0\n\t" \
    "s_nop 0\n\t" \
    "s_nop 0\n\t" \
    "s_nop 0\n\t" \
    "s_nop 0\n\t" \
    "qk_go16_%=:\n\t" \
    "s_getpc_b64 s[30:31]\n\t" \
    "qk_Wp18_%=:\n\t" \
    "s_add_u32 s30, s30, qk_We19_%=-qk_Wp18_%=\n\t" \
    "s_addc_u32 s31, s31, 0\n\t" \
    "s_sub_u32 s70, s70, 96\n\t" \
    "qk_W17_%=:\n\t" \
    "s_memtime s[56:57]\n\t" \
    "s_waitcnt lgkmcnt(0)\n\t" \
    "s_sub_u32 s69, s70, s56\n\t" \
    "s_cmp_gt_i32 s69, 80\n\t" \
    "s_cbranch_scc1 qk_W17_%=\n\t" \
    "s_max_i32 s69, s69, 0\n\t" \
    "s_and_b32 s69, s69, -4\n\t" \
    "s_sub_u32 s30, s30, s69\n\t" \
    "s_subb_u32 s31, s31, 0\n\t" \
    "s_setpc_b64 s[30:31]\n\t" \
    "s_nop 0\n\t" \
    "s_nop 0\n\t" \
    "s_nop 0\n\t" \
    "s_nop 0\n\t" \
    "s_nop 0\n\t" \
    "s_nop 0\n\t" \
    "s_nop 0\n\t" \
    "s_nop 0\n\t" \
    "s_nop 0\n\t" \
    "s_nop 0\n\t" \
    "s_nop 0\n\t" \
    "s_nop 0\n\t" \
    "s_nop 0\n\t" \
    "s_nop 0\n\t" \
    "s_nop 0\n\t" \
    "s_nop 0\n\t" \
    "s_nop 0\n\t" \
    "s_nop 0\n\t" \
    "s_nop 0\n\t" \
    "s_nop 0\n\t" \
    "qk_We19_%=:\n\t" \
    "qk_G20_%=:\n\t" \
    "global_load_dwordx4 v[36:39], v1, s[46:47] sc1\n\t" \
    "global_load_dword v161, v2, s[46:47] sc1\n\t" \
    "global_load_dwordx4 v[40:43], v23, s[46:47] sc1\n\t" \
    "s_waitcnt vmcnt(2)\n\t" \
    "ds_write_b128 v3, v[36:39]\n\t" \
    "v_smfmac_f32_16x16x64_bf16 v[72:75], v[36:39], a[0:7], v10\n\t" \
    "s_waitcnt vmcnt(1)\n\t" \
    "ds_write_b32 v22, v161 offset:19456\n\t" \
    "v_smfmac_f32_16x16x64_bf16 v[76:79], v[36:39], a[72:79], v10\n\t" \
    "s_waitcnt vmcnt(0)\n\t" \
    "ds_write_b128 v3, v[40:43] offset:1024\n\t" \
    "v_max3_u32 v160, v36, v40, v161\n\t" \
    "v_cmp_eq_u32 vcc, -1, v160\n\t" \
    "v_smfmac_f32_16x16x64_bf16 v[80:83], v[36:39], a[144:151], v10\n\t" \
    "v_smfmac_f32_16x16x64_bf16 v[84:87], v[36:39], a[216:223], v10\n\t" \
    "v_smfmac_f32_16x16x64_bf16 v[72:75], v[40:43], a[8:15], v10\n\t" \
    "s_cbranch_vccnz qk_Gbad21_%=\n\t" \
    "qk_Gc22_%=:\n\t" \
    "s_waitcnt lgkmcnt(0)\n\t" \
    "s_barrier\n\t" \
    "ds_read_b128 v[48:51], v4\n\t" \
    "ds_read_b128 v[52:55], v5\n\t" \
    "v_smfmac_f32_16x16x64_bf16 v[76:79], v[40:43], a[80:87], v10\n\t" \
    "ds_read_b128 v[56:59], v6\n\t" \
    "ds_read_b128 v[60:63], v7\n\t" \
    "v_smfmac_f32_16x16x64_bf16 v[80:83], v[40:43], a[152:159], v10\n\t" \
    "ds_read_b128 v[64:67], v8\n\t" \
    "v_smfmac_f32_16x16x64_bf16 v[84:87], v[40:43], a[224:231], v10\n\t" \
    "ds_read_b128 v[68:71], v9\n\t" \
    "s_waitcnt lgkmcnt(5)\n\t" \
    "v_smfmac_f32_16x16x64_bf16 v[72:75], v[48:51], a[16:23], v10\n\t" \
    "ds_read_b128 v[44:47], v0 offset:19456\n\t" \
    "s_waitcnt lgkmcnt(5)\n\t" \
    "v_smfmac_f32_16x16x64_bf16 v[72:75], v[52:55], a[24:31], v10\n\t" \
    "s_waitcnt lgkmcnt(4)\n\t" \
    "v_smfmac_f32_16x16x64_bf16 v[72:75], v[56:59], a[32:39], v10\n\t" \
    "s_waitcnt lgkmcnt(3)\n\t" \
    "v_smfmac_f32_16x16x64_bf16 v[72:75], v[60:63], a[40:47], v10\n\t" \
    "s_waitcnt lgkmcnt(2)\n\t" \
    "v_smfmac_f32_16x16x64_bf16 v[72:75], v[64:67], a[48:55], v10\n\t" \
    "s_waitcnt lgkmcnt(1)\n\t" \
    "v_smfmac_f32_16x16x64_bf16 v[72:75], v[68:71], a[56:63], v10\n\t" \
    "s_waitcnt lgkmcnt(0)\n\t" \
    "v_smfmac_f32_16x16x64_bf16 v[72:75], v[44:47], a[64:71], v10\n\t" \
    "s_nop 2\n\t" \
    "v_smfmac_f32_16x16x64_bf16 v[76:79], v[48:51], a[88:95], v10\n\t" \
    "s_nop 2\n\t" \
    "v_smfmac_f32_16x16x64_bf16 v[76:79], v[52:55], a[96:103], v10\n\t" \
    "v_add_f32 v72, v72, v74\n\t" \
    "v_smfmac_f32_16x16x64_bf16 v[76:79], v[56:59], a[104:111], v10\n\t" \
    "v_add_f32 v73, v73, v75\n\t" \
    "v_smfmac_f32_16x16x64_bf16 v[76:79], v[60:63], a[112:119], v10\n\t" \
    "v_cvt_pk_bf16_f32 v156, v72, v73\n\t" \
    "v_smfmac_f32_16x16x64_bf16 v[76:79], v[64:67], a[120:127], v10\n\t" \
    "ds_write_b32 v12, v156 offset:0\n\t" \
    "v_smfmac_f32_16x16x64_bf16 v[76:79], v[68:71], a[128:135], v10\n\t" \
    "v_smfmac_f32_16x16x64_bf16 v[76:79], v[44:47], a[136:143], v10\n\t" \
    "s_nop 2\n\t" \
    "v_smfmac_f32_16x16x64_bf16 v[80:83], v[48:51], a[160:167], v10\n\t" \
    "s_nop 2\n\t" \
    "v_smfmac_f32_16x16x64_bf16 v[80:83], v[52:55], a[168:175], v10\n\t" \
    "v_add_f32 v76, v76, v78\n\t" \
    "v_smfmac_f32_16x16x64_bf16 v[80:83], v[56:59], a[176:183], v10\n\t" \
    "v_add_f32 v77, v77, v79\n\t" \
    "v_smfmac_f32_16x16x64_bf16 v[80:83], v[60:63], a[184:191], v10\n\t" \
    "v_cvt_pk_bf16_f32 v157, v76, v77\n\t" \
    "v_smfmac_f32_16x16x64_bf16 v[80:83], v[64:67], a[192:199], v10\n\t" \
    "ds_write_b32 v12, v157 offset:64\n\t" \
    "v_smfmac_f32_16x16x64_bf16 v[80:83], v[68:71], a[200:207], v10\n\t" \
    "v_smfmac_f32_16x16x64_bf16 v[80:83], v[44:47], a[208:215], v10\n\t" \
    "s_nop 2\n\t" \
    "v_smfmac_f32_16x16x64_bf16 v[84:87], v[48:51], a[232:239], v10\n\t" \
    "s_nop 2\n\t" \
    "v_smfmac_f32_16x16x64_bf16 v[84:87], v[52:55], a[240:247], v10\n\t" \
    "v_add_f32 v80, v80, v82\n\t" \
    "v_smfmac_f32_16x16x64_bf16 v[84:87], v[56:59], a[248:255], v10\n\t" \
    "v_add_f32 v81, v81, v83\n\t" \
    "v_smfmac_f32_16x16x64_bf16 v[84:87], v[60:63], v[224:231], v10\n\t" \
    "v_cvt_pk_bf16_f32 v158, v80, v81\n\t" \
    "v_smfmac_f32_16x16x64_bf16 v[84:87], v[64:67], v[232:239], v10\n\t" \
    "ds_write_b32 v16, v158 offset:0\n\t" \
    "v_smfmac_f32_16x16x64_bf16 v[84:87], v[68:71], v[240:247], v10\n\t" \
    "ds_read_b64_tr_b16 v[132:133], v13 offset:0\n\t" \
    "ds_read_b64_tr_b16 v[134:135], v13 offset:128\n\t" \
    "v_smfmac_f32_16x16x64_bf16 v[84:87], v[44:47], v[248:255], v10\n\t" \
    "ds_read_b64_tr_b16 v[136:137], v13 offset:1024\n\t" \
    "ds_read_b64_tr_b16 v[138:139], v13 offset:1152\n\t" \
    "s_nop 4\n\t" \
    "v_cmp_u_f32 vcc, v72, v73\n\t" \
    "v_pk_add_f32 v[84:85], v[84:85], v[86:87]\n\t" \
    "v_cvt_pk_bf16_f32 v159, v84, v85\n\t" \
    "ds_write_b32 v16, v159 offset:256\n\t" \
    "s_cbranch_vccnz qk_Gslow23_%=\n\t" \
    "qk_Gback24_%=:\n\t" \
    "s_waitcnt lgkmcnt(3)\n\t" \
    "v_smfmac_f32_16x16x64_bf16 v[88:91], v[132:135], v[100:107], v10\n\t" \
    "s_waitcnt lgkmcnt(1)\n\t" \
    "v_smfmac_f32_16x16x64_bf16 v[88:91], v[136:139], v[108:115], v10\n\t" \
    "s_waitcnt lgkmcnt(0)\n\t" \
    "s_barrier\n\t" \
    "ds_read_b64_tr_b16 v[140:141], v17 offset:0\n\t" \
    "v_smfmac_f32_16x16x64_bf16 v[92:95], v[132:135], v[116:123], v10\n\t" \
    "ds_read_b64_tr_b16 v[142:143], v17 offset:256\n\t" \
    "ds_read_b64_tr_b16 v[144:145], v17 offset:512\n\t" \
    "ds_read_b64_tr_b16 v[146:147], v17 offset:768\n\t" \
    "ds_read_b64_tr_b16 v[148:149], v17 offset:1024\n\t" \
    "v_smfmac_f32_16x16x64_bf16 v[92:95], v[136:139], v[124:131], v10\n\t" \
    "ds_read_b64_tr_b16 v[150:151], v17 offset:1280\n\t" \
    "ds_read_b64_tr_b16 v[152:153], v17 offset:1536\n\t" \
    "ds_read_b64_tr_b16 v[154:155], v17 offset:1792\n\t" \
    "v_smfmac_f32_16x16x64_bf16 v[184:187], v[132:135], v[100:107], v10\n\t" \
    "qk_qb26_%=:\n\t" \
    "s_waitcnt lgkmcnt(4)\n\t" \
    "v_smfmac_f32_16x16x64_bf16 v[96:99], v[132:135], v[140:147], v10\n\t" \
    "s_waitcnt lgkmcnt(0)\n\t" \
    "v_smfmac_f32_16x16x64_bf16 v[96:99], v[136:139], v[148:155], v10\n\t" \
    "v_add_f32 v168, v88, v90\n\t" \
    "v_add_f32 v169, v92, v94\n\t" \
    "v_cmp_u_f32 vcc, v168, v169\n\t" \
    "v_cvt_pk_bf16_f32 v168, 0, v168\n\t" \
    "v_cvt_pk_bf16_f32 v169, 0, v169\n\t" \
    "v_mul_f32 v168, v168, v26\n\t" \
    "v_mul_f32 v169, v169, v26\n\t" \
    "v_cvt_pk_bf16_f32 v168, 0, v168\n\t" \
    "v_add_f32 v172, v96, v98\n\t" \
    "v_cvt_pk_bf16_f32 v169, 0, v169\n\t" \
    "v_max_f32 v178, v168, v169\n\t" \
    "v_cvt_pk_bf16_f32 v172, 0, v172\n\t" \
    "v_mul_f32 v172, v172, v26\n\t" \
    "v_max_f32_dpp v178, v178, v178 row_mirror row_mask:0xf bank_mask:0xf\n\t" \
    "v_cvt_pk_bf16_f32 v172, 0, v172\n\t" \
    "v_max_f32 v173, v178, v172\n\t" \
    "v_mul_f32 v168, v168, v28\n\t" \
    "v_mul_f32 v169, v169, v28\n\t" \
    "v_max_f32_dpp v173, v173, v173 quad_perm:[1,0,3,2] row_mask:0xf bank_mask:0xf\n\t" \
    "v_mul_f32 v172, v172, v28\n\t" \
    "s_cbranch_vccnz qk_qs25_%=\n\t" \
    "qk_qb227_%=:\n\t" \
    "v_max_f32_dpp v173, v173, v173 quad_perm:[2,3,0,1] row_mask:0xf bank_mask:0xf\n\t" \
    "s_nop 1\n\t" \
    "v_max_f32_dpp v173, v173, v173 row_half_mirror row_mask:0xf bank_mask:0xf\n\t" \
    "v_fma_f32 v172, -v173, v28, v172\n\t" \
    "v_fma_f32 v168, -v173, v28, v168\n\t" \
    "v_fma_f32 v169, -v173, v28, v169\n\t" \
    "v_exp_f32 v172, v172\n\t" \
    "v_exp_f32 v168, v168\n\t" \
    "v_exp_f32 v169, v169\n\t" \
    "v_fma_f32 v173, v172, 0.5, v168\n\t" \
    "v_add_f32 v173, v173, v169\n\t" \
    "v_mov_b32_dpp v174, v172 quad_perm:[0,0,2,2] row_mask:0xf bank_mask:0xf\n\t" \
    "v_mov_b32_dpp v175, v172 quad_perm:[1,1,3,3] row_mask:0xf bank_mask:0xf\n\t" \
    "v_add_f32_dpp v173, v173, v173 quad_perm:[1,0,3,2] row_mask:0xf bank_mask:0xf\n\t" \
    "v_mov_b32_dpp v176, v168 quad_perm:[1,0,3,2] row_mask:0xf bank_mask:0xf\n\t" \
    "v_mov_b32_dpp v177, v169 quad_perm:[1,0,3,2] row_mask:0xf bank_mask:0xf\n\t" \
    "v_add_f32_dpp v173, v173, v173 quad_perm:[2,3,0,1] row_mask:0xf bank_mask:0xf\n\t" \
    "v_cndmask_b32 v168, v168, v177, s[48:49]\n\t" \
    "v_cndmask_b32 v169, v176, v169, s[48:49]\n\t" \
    "v_add_f32_dpp v173, v173, v173 row_half_mirror row_mask:0xf bank_mask:0xf\n\t" \
    "s_nop 1\n\t" \
    "v_add_f32_dpp v173, v173, v173 row_mirror row_mask:0xf bank_mask:0xf\n\t" \
    "v_rcp_f32 v173, v173\n\t" \
    "s_nop 0\n\t" \
    "v_pk_mul_f32 v[168:169], v[168:169], v[172:173] op_sel:[0,1] op_sel_hi:[1,1]\n\t" \
    "v_cvt_pk_bf16_f32 v176, v168, v169\n\t" \
    "global_store_dword v18, v176, s[46:47] sc1\n\t" \
    "global_store_dword v180, v176, s[46:47] sc1\n\t" \
    "global_store_dword v181, v176, s[46:47] sc1\n\t" \
    "global_store_dword v182, v176, s[46:47] sc1\n\t" \
    "v_pk_mul_f32 v[174:175], v[174:175], v[172:173] op_sel:[0,1] op_sel_hi:[1,1]\n\t" \
    "v_cvt_pk_bf16_f32 v177, v174, v175\n\t" \
    "global_store_dword v19, v177, s[46:47] sc1\n\t" \
    "s_cmp_eq_u32 s65, 63\n\t" \
    "s_cbranch_scc1 qk_last15_%=\n\t" \
    "v_readfirstlane_b32 s69, v25\n\t" \
    "s_add_u32 s65, s65, 1\n\t" \
    "s_max_i32 s83, s83, s69\n\t" \
    "s_cmp_gt_u32 s65, 6\n\t" \
    "s_cselect_b32 s83, 99, s83\n\t" \
    "v_mov_b32 v72, 0\n\t" \
    "v_mov_b32 v73, 0\n\t" \
    "v_mov_b32 v74, 0\n\t" \
    "v_mov_b32 v75, 0\n\t" \
    "v_mov_b32 v76, 0\n\t" \
    "v_mov_b32 v77, 0\n\t" \
    "v_mov_b32 v78, 0\n\t" \
    "v_mov_b32 v79, 0\n\t" \
    "v_mov_b32 v80, 0\n\t" \
    "v_mov_b32 v81, 0\n\t" \
    "v_mov_b32 v82, 0\n\t" \
    "v_mov_b32 v83, 0\n\t" \
    "v_mov_b32 v84, 0\n\t" \
    "v_mov_b32 v85, 0\n\t" \
    "v_mov_b32 v86, 0\n\t" \
    "v_mov_b32 v87, 0\n\t" \
    "v_mov_b32 v88, 0\n\t" \
    "v_mov_b32 v89, 0\n\t" \
    "v_mov_b32 v90, 0\n\t" \
    "v_mov_b32 v91, 0\n\t" \
    "v_mov_b32 v92, 0\n\t" \
    "v_mov_b32 v93, 0\n\t" \
    "v_mov_b32 v94, 0\n\t" \
    "v_mov_b32 v95, 0\n\t" \
    "v_mov_b32 v96, 0\n\t" \
    "v_mov_b32 v97, 0\n\t" \
    "v_mov_b32 v98, 0\n\t" \
    "v_mov_b32 v99, 0\n\t" \
    "s_add_u32 s69, s65, 0\n\t" \
    "s_and_b32 s69, s69, 7\n\t" \
    "s_lshl_b32 s69, s69, 18\n\t" \
    "s_add_u32 s69, s69, s62\n\t" \
    "s_add_u32 s46, s36, s69\n\t" \
    "s_addc_u32 s47, s37, 0\n\t" \
    "s_mul_i32 s70, s67, 7\n\t" \
    "s_add_u32 s69, s65, 0\n\t" \
    "s_xor_b32 s69, s69, s70\n\t" \
    "s_and_b32 s69, s69, 63\n\t" \
    "s_lshl_b32 s69, s69, 13\n\t" \
    "s_mul_i32 s70, s67, 524288\n\t" \
    "s_add_u32 s69, s69, s70\n\t" \
    "s_add_u32 s69, s69, 23068672\n\t" \
    "s_add_u32 s38, s36, s69\n\t" \
    "s_addc_u32 s39, s37, 0\n\t" \
    "qk_WF28_%=:\n\t" \
    "s_cmp_ge_i32 s83, s65\n\t" \
    "s_cbranch_scc1 qk_WFok29_%=\n\t" \
    "global_load_dword v160, v24, s[36:37] sc1\n\t" \
    "s_waitcnt vmcnt(0)\n\t" \
    "v_readfirstlane_b32 s83, v160\n\t" \
    "s_nop 1\n\t" \
    "s_cmp_ge_i32 s83, s65\n\t" \
    "s_cbranch_scc1 qk_WFok29_%=\n\t" \
    "s_cmp_eq_u32 s68, 0\n\t" \
    "s_cbranch_scc1 qk_F30_%=\n\t" \
    "s_sub_u32 s68, s68, 1\n\t" \
    "s_sleep 2\n\t" \
    "s_branch qk_WF28_%=\n\t" \
    "qk_F30_%=:\n\t" \
    "v_mov_b32 v160, 5767424\n\t" \
    "v_mov_b32 v161, 9\n\t" \
    "global_store_dword v160, v161, s[36:37] sc1\n\t" \
    "s_waitcnt vmcnt(0)\n\t" \
    "s_branch qk_WFok29_%=\n\t" \
    "qk_WFok29_%=:\n\t" \
    "global_load_dwordx4 v[100:103], v0, s[38:39] offset:0 sc1\n\t" \
    "global_load_dwordx4 v[104:107], v0, s[38:39] offset:2048 sc1\n\t" \
    "global_load_dwordx4 v[108:111], v15, s[38:39] offset:0 sc1\n\t" \
    "global_load_dwordx4 v[112:115], v15, s[38:39] offset:2048 sc1\n\t" \
    "global_load_dwordx4 v[116:119], v0, s[38:39] offset:1024 sc1\n\t" \
    "global_load_dwordx4 v[120:123], v0, s[38:39] offset:3072 sc1\n\t" \
    "global_load_dwordx4 v[124:127], v15, s[38:39] offset:1024 sc1\n\t" \
    "global_load_dwordx4 v[128:131], v15, s[38:39] offset:3072 sc1\n\t" \
    "s_cmp_gt_i32 s83, s65\n\t" \
    "s_cbranch_scc1 qk_nf31_%=\n\t" \
    "global_load_dword v25, v24, s[36:37] sc1\n\t" \
    "qk_nf31_%=:\n\t" \
    "s_barrier\n\t" \
    "ds_read_b32 v168, v11 offset:20480\n\t" \
    "s_waitcnt lgkmcnt(0)\n\t" \
    "v_readfirstlane_b32 s70, v168\n\t" \
    "ds_write_b32 v12, v31 offset:0\n\t" \
    "ds_write_b32 v12, v31 offset:64\n\t" \
    "s_branch qk_go16_%=\n\t" \
    "qk_Gbad21_%=:\n\t" \
    "s_waitcnt lgkmcnt(0)\n\t" \
    "s_nop 7\n\t" \
    "v_mov_b32 v72, 0\n\t" \
    "v_mov_b32 v73, 0\n\t" \
    "v_mov_b32 v74, 0\n\t" \
    "v_mov_b32 v75, 0\n\t" \
    "v_mov_b32 v76, 0\n\t" \
    "v_mov_b32 v77, 0\n\t" \
    "v_mov_b32 v78, 0\n\t" \
    "v_mov_b32 v79, 0\n\t" \
    "v_mov_b32 v80, 0\n\t" \
    "v_mov_b32 v81, 0\n\t" \
    "v_mov_b32 v82, 0\n\t" \
    "v_mov_b32 v83, 0\n\t" \
    "v_mov_b32 v84, 0\n\t" \
    "v_mov_b32 v85, 0\n\t" \
    "v_mov_b32 v86, 0\n\t" \
    "v_mov_b32 v87, 0\n\t" \
    "s_cmp_eq_u32 s68, 0\n\t" \
    "s_cbranch_scc1 qk_F32_%=\n\t" \
    "s_sub_u32 s68, s68, 1\n\t" \
    "s_branch qk_G20_%=\n\t" \
    "qk_F32_%=:\n\t" \
    "v_mov_b32 v160, 5767424\n\t" \
    "v_mov_b32 v161, 1\n\t" \
    "global_store_dword v160, v161, s[36:37] sc1\n\t" \
    "s_waitcnt vmcnt(0)\n\t" \
    "s_branch qk_Gc22_%=\n\t" \
    "qk_Gslow23_%=:\n\t" \
    "v_max_u32 v160, v36, v37\n\t" \
    "v_max3_u32 v160, v160, v38, v39\n\t" \
    "v_max3_u32 v160, v160, v40, v41\n\t" \
    "v_max3_u32 v160, v160, v42, v43\n\t" \
    "v_max3_u32 v160, v160, v44, v45\n\t" \
    "v_max3_u32 v160, v160, v46, v47\n\t" \
    "v_max3_u32 v160, v160, v48, v49\n\t" \
    "v_max3_u32 v160, v160, v50, v51\n\t" \
    "v_max3_u32 v160, v160, v52, v53\n\t" \
    "v_max3_u32 v160, v160, v54, v55\n\t" \
    "v_max3_u32 v160, v160, v56, v57\n\t" \
    "v_max3_u32 v160, v160, v58, v59\n\t" \
    "v_max3_u32 v160, v160, v60, v61\n\t" \
    "v_max3_u32 v160, v160, v62, v63\n\t" \
    "v_max3_u32 v160, v160, v64, v65\n\t" \
    "v_max3_u32 v160, v160, v66, v67\n\t" \
    "v_max3_u32 v160, v160, v68, v69\n\t" \
    "v_max3_u32 v160, v160, v70, v71\n\t" \
    "v_cmp_eq_u32 vcc, -1, v160\n\t" \
    "s_cbranch_vccz qk_Gback24_%=\n\t" \
    "s_waitcnt lgkmcnt(0)\n\t" \
    "v_sub_u32 v160, v1, v3\n\t" \
    "s_nop 0\n\t" \
    "v_readfirstlane_b32 s69, v160\n\t" \
    "v_add_u32 v163, 20480, v0\n\t" \
    "s_nop 7\n\t" \
    "s_add_u32 s30, s46, s69\n\t" \
    "s_addc_u32 s31, s47, 0\n\t" \
    "qk_Gag33_%=:\n\t" \
    "global_load_dwordx4 v[36:39], v1, s[46:47] sc1\n\t" \
    "global_load_dwordx4 v[40:43], v23, s[46:47] sc1\n\t" \
    "global_load_dwordx4 v[44:47], v163, s[30:31] sc1\n\t" \
    "global_load_dwordx4 v[48:51], v4, s[30:31] sc1\n\t" \
    "global_load_dwordx4 v[52:55], v5, s[30:31] sc1\n\t" \
    "global_load_dwordx4 v[56:59], v6, s[30:31] sc1\n\t" \
    "global_load_dwordx4 v[60:63], v7, s[30:31] sc1\n\t" \
    "global_load_dwordx4 v[64:67], v8, s[30:31] sc1\n\t" \
    "global_load_dwordx4 v[68:71], v9, s[30:31] sc1\n\t" \
    "s_waitcnt vmcnt(0)\n\t" \
    "v_max_u32 v160, v36, v37\n\t" \
    "v_max3_u32 v160, v160, v38, v39\n\t" \
    "v_max3_u32 v160, v160, v40, v41\n\t" \
    "v_max3_u32 v160, v160, v42, v43\n\t" \
    "v_max3_u32 v160, v160, v44, v45\n\t" \
    "v_max3_u32 v160, v160, v46, v47\n\t" \
    "v_max3_u32 v160, v160, v48, v49\n\t" \
    "v_max3_u32 v160, v160, v50, v51\n\t" \
    "v_max3_u32 v160, v160, v52, v53\n\t" \
    "v_max3_u32 v160, v160, v54, v55\n\t" \
    "v_max3_u32 v160, v160, v56, v57\n\t" \
    "v_max3_u32 v160, v160, v58, v59\n\t" \
    "v_max3_u32 v160, v160, v60, v61\n\t" \
    "v_max3_u32 v160, v160, v62, v63\n\t" \
    "v_max3_u32 v160, v160, v64, v65\n\t" \
    "v_max3_u32 v160, v160, v66, v67\n\t" \
    "v_max3_u32 v160, v160, v68, v69\n\t" \
    "v_max3_u32 v160, v160, v70, v71\n\t" \
    "v_cmp_eq_u32 vcc, -1, v160\n\t" \
    "s_cbranch_vccz qk_Gredo34_%=\n\t" \
    "s_cmp_eq_u32 s68, 0\n\t" \
    "s_cbranch_scc1 qk_F35_%=\n\t" \
    "s_sub_u32 s68, s68, 1\n\t" \
    "s_sleep 1\n\t" \
    "s_branch qk_Gag33_%=\n\t" \
    "qk_F35_%=:\n\t" \
    "v_mov_b32 v160, 5767424\n\t" \
    "v_mov_b32 v161, 2\n\t" \
    "global_store_dword v160, v161, s[36:37] sc1\n\t" \
    "s_waitcnt vmcnt(0)\n\t" \
    "s_branch qk_Gredo34_%=\n\t" \
    "qk_Gredo34_%=:\n\t" \
    "v_mov_b32 v72, 0\n\t" \
    "v_mov_b32 v73, 0\n\t" \
    "v_mov_b32 v74, 0\n\t" \
    "v_mov_b32 v75, 0\n\t" \
    "v_mov_b32 v76, 0\n\t" \
    "v_mov_b32 v77, 0\n\t" \
    "v_mov_b32 v78, 0\n\t" \
    "v_mov_b32 v79, 0\n\t" \
    "v_mov_b32 v80, 0\n\t" \
    "v_mov_b32 v81, 0\n\t" \
    "v_mov_b32 v82, 0\n\t" \
    "v_mov_b32 v83, 0\n\t" \
    "v_mov_b32 v84, 0\n\t" \
    "v_mov_b32 v85, 0\n\t" \
    "v_mov_b32 v86, 0\n\t" \
    "v_mov_b32 v87, 0\n\t" \
    "v_smfmac_f32_16x16x64_bf16 v[72:75], v[36:39], a[0:7], v10\n\t" \
    "v_smfmac_f32_16x16x64_bf16 v[72:75], v[40:43], a[8:15], v10\n\t" \
    "v_smfmac_f32_16x16x64_bf16 v[72:75], v[44:47], a[64:71], v10\n\t" \
    "v_smfmac_f32_16x16x64_bf16 v[72:75], v[48:51], a[16:23], v10\n\t" \
    "v_smfmac_f32_16x16x64_bf16 v[72:75], v[52:55], a[24:31], v10\n\t" \
    "v_smfmac_f32_16x16x64_bf16 v[72:75], v[56:59], a[32:39], v10\n\t" \
    "v_smfmac_f32_16x16x64_bf16 v[72:75], v[60:63], a[40:47], v10\n\t" \
    "v_smfmac_f32_16x16x64_bf16 v[72:75], v[64:67], a[48:55], v10\n\t" \
    "v_smfmac_f32_16x16x64_bf16 v[72:75], v[68:71], a[56:63], v10\n\t" \
    "v_smfmac_f32_16x16x64_bf16 v[76:79], v[36:39], a[72:79], v10\n\t" \
    "v_smfmac_f32_16x16x64_bf16 v[76:79], v[40:43], a[80:87], v10\n\t" \
    "v_smfmac_f32_16x16x64_bf16 v[76:79], v[44:47], a[136:143], v10\n\t" \
    "v_smfmac_f32_16x16x64_bf16 v[76:79], v[48:51], a[88:95], v10\n\t" \
    "v_smfmac_f32_16x16x64_bf16 v[76:79], v[52:55], a[96:103], v10\n\t" \
    "v_smfmac_f32_16x16x64_bf16 v[76:79], v[56:59], a[104:111], v10\n\t" \
    "v_smfmac_f32_16x16x64_bf16 v[76:79], v[60:63], a[112:119], v10\n\t" \
    "v_smfmac_f32_16x16x64_bf16 v[76:79], v[64:67], a[120:127], v10\n\t" \
    "v_smfmac_f32_16x16x64_bf16 v[76:79], v[68:71], a[128:135], v10\n\t" \
    "v_smfmac_f32_16x16x64_bf16 v[80:83], v[36:39], a[144:151], v10\n\t" \
    "v_smfmac_f32_16x16x64_bf16 v[80:83], v[40:43], a[152:159], v10\n\t" \
    "v_smfmac_f32_16x16x64_bf16 v[80:83], v[44:47], a[208:215], v10\n\t" \
    "v_smfmac_f32_16x16x64_bf16 v[80:83], v[48:51], a[160:167], v10\n\t" \
    "v_smfmac_f32_16x16x64_bf16 v[80:83], v[52:55], a[168:175], v10\n\t" \
    "v_smfmac_f32_16x16x64_bf16 v[80:83], v[56:59], a[176:183], v10\n\t" \
    "v_smfmac_f32_16x16x64_bf16 v[80:83], v[60:63], a[184:191], v10\n\t" \
    "v_smfmac_f32_16x16x64_bf16 v[80:83], v[64:67], a[192:199], v10\n\t" \
    "v_smfmac_f32_16x16x64_bf16 v[80:83], v[68:71], a[200:207], v10\n\t" \
    "v_smfmac_f32_16x16x64_bf16 v[84:87], v[36:39], a[216:223], v10\n\t" \
    "v_smfmac_f32_16x16x64_bf16 v[84:87], v[40:43], a[224:231], v10\n\t" \
    "v_smfmac_f32_16x16x64_bf16 v[84:87], v[44:47], v[248:255], v10\n\t" \
    "v_smfmac_f32_16x16x64_bf16 v[84:87], v[48:51], a[232:239], v10\n\t" \
    "v_smfmac_f32_16x16x64_bf16 v[84:87], v[52:55], a[240:247], v10\n\t" \
    "v_smfmac_f32_16x16x64_bf16 v[84:87], v[56:59], a[248:255], v10\n\t" \
    "v_smfmac_f32_16x16x64_bf16 v[84:87], v[60:63], v[224:231], v10\n\t" \
    "v_smfmac_f32_16x16x64_bf16 v[84:87], v[64:67], v[232:239], v10\n\t" \
    "v_smfmac_f32_16x16x64_bf16 v[84:87], v[68:71], v[240:247], v10\n\t" \
    "v_add_f32 v72, v72, v74\n\t" \
    "v_add_f32 v73, v73, v75\n\t" \
    "v_cvt_pk_bf16_f32 v156, v72, v73\n\t" \
    "ds_write_b32 v12, v156 offset:0\n\t" \
    "v_add_f32 v76, v76, v78\n\t" \
    "v_add_f32 v77, v77, v79\n\t" \
    "v_cvt_pk_bf16_f32 v157, v76, v77\n\t" \
    "ds_write_b32 v12, v157 offset:64\n\t" \
    "v_add_f32 v80, v80, v82\n\t" \
    "v_add_f32 v81, v81, v83\n\t" \
    "v_cvt_pk_bf16_f32 v158, v80, v81\n\t" \
    "ds_write_b32 v16, v158 offset:0\n\t" \
    "v_pk_add_f32 v[84:85], v[84:85], v[86:87]\n\t" \
    "v_cvt_pk_bf16_f32 v159, v84, v85\n\t" \
    "ds_write_b32 v16, v159 offset:256\n\t" \
    "s_branch qk_Gback24_%=\n\t" \
    "qk_qs25_%=:\n\t" \
    "s_cmp_eq_u32 s91, s65\n\t" \
    "s_cbranch_scc1 qk_qb227_%=\n\t" \
    "s_mov_b32 s91, s65\n\t" \
    "s_nop 7\n\t" \
    "ds_read_b64_tr_b16 v[132:133], v13 offset:0\n\t" \
    "ds_read_b64_tr_b16 v[134:135], v13 offset:128\n\t" \
    "ds_read_b64_tr_b16 v[136:137], v13 offset:1024\n\t" \
    "ds_read_b64_tr_b16 v[138:139], v13 offset:1152\n\t" \
    "v_mov_b32 v88, 0\n\t" \
    "v_mov_b32 v89, 0\n\t" \
    "v_mov_b32 v90, 0\n\t" \
    "v_mov_b32 v91, 0\n\t" \
    "v_mov_b32 v92, 0\n\t" \
    "v_mov_b32 v93, 0\n\t" \
    "v_mov_b32 v94, 0\n\t" \
    "v_mov_b32 v95, 0\n\t" \
    "v_mov_b32 v96, 0\n\t" \
    "v_mov_b32 v97, 0\n\t" \
    "v_mov_b32 v98, 0\n\t" \
    "v_mov_b32 v99, 0\n\t" \
    "s_waitcnt lgkmcnt(0)\n\t" \
    "v_smfmac_f32_16x16x64_bf16 v[88:91], v[132:135], v[100:107], v10\n\t" \
    "v_smfmac_f32_16x16x64_bf16 v[92:95], v[132:135], v[116:123], v10\n\t" \
    "v_smfmac_f32_16x16x64_bf16 v[88:91], v[136:139], v[108:115], v10\n\t" \
    "v_smfmac_f32_16x16x64_bf16 v[92:95], v[136:139], v[124:131], v10\n\t" \
    "s_nop 7\n\t" \
    "s_branch qk_qb26_%=\n\t" \
    "qk_last15_%=:\n\t" \
    "s_endpgm\n\t" \
    "qk_loop12_%=:\n\t" \
    "v_mov_b32 v72, 0\n\t" \
    "v_mov_b32 v73, 0\n\t" \
    "v_mov_b32 v74, 0\n\t" \
    "v_mov_b32 v75, 0\n\t" \
    "v_mov_b32 v76, 0\n\t" \
    "v_mov_b32 v77, 0\n\t" \
    "v_mov_b32 v78, 0\n\t" \
    "v_mov_b32 v79, 0\n\t" \
    "v_mov_b32 v80, 0\n\t" \
    "v_mov_b32 v81, 0\n\t" \
    "v_mov_b32 v82, 0\n\t" \
    "v_mov_b32 v83, 0\n\t" \
    "v_mov_b32 v84, 0\n\t" \
    "v_mov_b32 v85, 0\n\t" \
    "v_mov_b32 v86, 0\n\t" \
    "v_mov_b32 v87, 0\n\t" \
    "v_mov_b32 v88, 0\n\t" \
    "v_mov_b32 v89, 0\n\t" \
    "v_mov_b32 v90, 0\n\t" \
    "v_mov_b32 v91, 0\n\t" \
    "v_mov_b32 v92, 0\n\t" \
    "v_mov_b32 v93, 0\n\t" \
    "v_mov_b32 v94, 0\n\t" \
    "v_mov_b32 v95, 0\n\t" \
    "v_mov_b32 v96, 0\n\t" \
    "v_mov_b32 v97, 0\n\t" \
    "v_mov_b32 v98, 0\n\t" \
    "v_mov_b32 v99, 0\n\t" \
    "s_add_u32 s69, s65, 0\n\t" \
    "s_and_b32 s69, s69, 7\n\t" \
    "s_lshl_b32 s69, s69, 18\n\t" \
    "s_add_u32 s69, s69, s62\n\t" \
    "s_add_u32 s46, s36, s69\n\t" \
    "s_addc_u32 s47, s37, 0\n\t" \
    "s_sub_u32 s70, s79, s74\n\t" \
    "s_max_i32 s70, s70, 0\n\t" \
    "s_min_i32 s70, s70, 16000\n\t" \
    "s_lshl_b32 s70, s70, 1\n\t" \
    "s_mul_hi_u32 s70, s70, s77\n\t" \
    "s_add_u32 s70, s70, s73\n\t" \
    ".p2align 6\n\t" \
    "s_nop 0\n\t" \
    "s_nop 0\n\t" \
    "s_nop 0\n\t" \
    "s_nop 0\n\t" \
    "s_nop 0\n\t" \
    "s_nop 0\n\t" \
    "qk_go37_%=:\n\t" \
    "s_getpc_b64 s[30:31]\n\t" \
    "qk_Wp39_%=:\n\t" \
    "s_add_u32 s30, s30, qk_We40_%=-qk_Wp39_%=\n\t" \
    "s_addc_u32 s31, s31, 0\n\t" \
    "s_sub_u32 s70, s70, 96\n\t" \
    "qk_W38_%=:\n\t" \
    "s_memtime s[56:57]\n\t" \
    "s_waitcnt lgkmcnt(0)\n\t" \
    "s_sub_u32 s69, s70, s56\n\t" \
    "s_cmp_gt_i32 s69, 80\n\t" \
    "s_cbranch_scc1 qk_W38_%=\n\t" \
    "s_max_i32 s69, s69, 0\n\t" \
    "s_and_b32 s69, s69, -4\n\t" \
    "s_sub_u32 s30, s30, s69\n\t" \
    "s_subb_u32 s31, s31, 0\n\t" \
    "s_setpc_b64 s[30:31]\n\t" \
    "s_nop 0\n\t" \
    "s_nop 0\n\t" \
    "s_nop 0\n\t" \
    "s_nop 0\n\t" \
    "s_nop 0\n\t" \
    "s_nop 0\n\t" \
    "s_nop 0\n\t" \
    "s_nop 0\n\t" \
    "s_nop 0\n\t" \
    "s_nop 0\n\t" \
    "s_nop 0\n\t" \
    "s_nop 0\n\t" \
    "s_nop 0\n\t" \
    "s_nop 0\n\t" \
    "s_nop 0\n\t" \
    "s_nop 0\n\t" \
    "s_nop 0\n\t" \
    "s_nop 0\n\t" \
    "s_nop 0\n\t" \
    "s_nop 0\n\t" \
    "qk_We40_%=:\n\t" \
    "qk_G41_%=:\n\t" \
    "global_load_dwordx4 v[36:39], v1, s[46:47] sc1\n\t" \
    "global_load_dword v161, v2, s[46:47] sc1\n\t" \
    "global_load_dwordx4 v[40:43], v23, s[46:47] sc1\n\t" \
    "s_waitcnt vmcnt(2)\n\t" \
    "ds_write_b128 v3, v[36:39]\n\t" \
    "v_smfmac_f32_16x16x64_bf16 v[72:75], v[36:39], a[16:23], v10\n\t" \
    "s_waitcnt vmcnt(1)\n\t" \
    "ds_write_b32 v22, v161 offset:19712\n\t" \
    "v_smfmac_f32_16x16x64_bf16 v[76:79], v[36:39], a[88:95], v10\n\t" \
    "s_waitcnt vmcnt(0)\n\t" \
    "ds_write_b128 v3, v[40:43] offset:1024\n\t" \
    "v_max3_u32 v160, v36, v40, v161\n\t" \
    "v_cmp_eq_u32 vcc, -1, v160\n\t" \
    "v_smfmac_f32_16x16x64_bf16 v[80:83], v[36:39], a[160:167], v10\n\t" \
    "v_smfmac_f32_16x16x64_bf16 v[84:87], v[36:39], a[232:239], v10\n\t" \
    "v_smfmac_f32_16x16x64_bf16 v[72:75], v[40:43], a[24:31], v10\n\t" \
    "s_cbranch_vccnz qk_Gbad42_%=\n\t" \
    "qk_Gc43_%=:\n\t" \
    "s_waitcnt lgkmcnt(0)\n\t" \
    "s_barrier\n\t" \
    "ds_read_b128 v[48:51], v4\n\t" \
    "ds_read_b128 v[52:55], v5\n\t" \
    "v_smfmac_f32_16x16x64_bf16 v[76:79], v[40:43], a[96:103], v10\n\t" \
    "ds_read_b128 v[56:59], v6\n\t" \
    "ds_read_b128 v[60:63], v7\n\t" \
    "v_smfmac_f32_16x16x64_bf16 v[80:83], v[40:43], a[168:175], v10\n\t" \
    "ds_read_b128 v[64:67], v8\n\t" \
    "v_smfmac_f32_16x16x64_bf16 v[84:87], v[40:43], a[240:247], v10\n\t" \
    "ds_read_b128 v[68:71], v9\n\t" \
    "s_waitcnt lgkmcnt(5)\n\t" \
    "v_smfmac_f32_16x16x64_bf16 v[72:75], v[48:51], a[32:39], v10\n\t" \
    "ds_read_b128 v[44:47], v0 offset:19456\n\t" \
    "s_waitcnt lgkmcnt(5)\n\t" \
    "v_smfmac_f32_16x16x64_bf16 v[72:75], v[52:55], a[40:47], v10\n\t" \
    "s_waitcnt lgkmcnt(4)\n\t" \
    "v_smfmac_f32_16x16x64_bf16 v[72:75], v[56:59], a[48:55], v10\n\t" \
    "s_waitcnt lgkmcnt(3)\n\t" \
    "v_smfmac_f32_16x16x64_bf16 v[72:75], v[60:63], a[56:63], v10\n\t" \
    "s_waitcnt lgkmcnt(2)\n\t" \
    "v_smfmac_f32_16x16x64_bf16 v[72:75], v[64:67], a[0:7], v10\n\t" \
    "s_waitcnt lgkmcnt(1)\n\t" \
    "v_smfmac_f32_16x16x64_bf16 v[72:75], v[68:71], a[8:15], v10\n\t" \
    "s_waitcnt lgkmcnt(0)\n\t" \
    "v_smfmac_f32_16x16x64_bf16 v[72:75], v[44:47], a[64:71], v10\n\t" \
    "s_nop 2\n\t" \
    "v_smfmac_f32_16x16x64_bf16 v[76:79], v[48:51], a[104:111], v10\n\t" \
    "s_nop 2\n\t" \
    "v_smfmac_f32_16x16x64_bf16 v[76:79], v[52:55], a[112:119], v10\n\t" \
    "v_add_f32 v72, v72, v74\n\t" \
    "v_smfmac_f32_16x16x64_bf16 v[76:79], v[56:59], a[120:127], v10\n\t" \
    "v_add_f32 v73, v73, v75\n\t" \
    "v_smfmac_f32_16x16x64_bf16 v[76:79], v[60:63], a[128:135], v10\n\t" \
    "v_cvt_pk_bf16_f32 v156, v72, v73\n\t" \
    "v_smfmac_f32_16x16x64_bf16 v[76:79], v[64:67], a[72:79], v10\n\t" \
    "ds_write_b32 v12, v156 offset:0\n\t" \
    "v_smfmac_f32_16x16x64_bf16 v[76:79], v[68:71], a[80:87], v10\n\t" \
    "v_smfmac_f32_16x16x64_bf16 v[76:79], v[44:47], a[136:143], v10\n\t" \
    "s_nop 2\n\t" \
    "v_smfmac_f32_16x16x64_bf16 v[80:83], v[48:51], a[176:183], v10\n\t" \
    "s_nop 2\n\t" \
    "v_smfmac_f32_16x16x64_bf16 v[80:83], v[52:55], a[184:191], v10\n\t" \
    "v_add_f32 v76, v76, v78\n\t" \
    "v_smfmac_f32_16x16x64_bf16 v[80:83], v[56:59], a[192:199], v10\n\t" \
    "v_add_f32 v77, v77, v79\n\t" \
    "v_smfmac_f32_16x16x64_bf16 v[80:83], v[60:63], a[200:207], v10\n\t" \
    "v_cvt_pk_bf16_f32 v157, v76, v77\n\t" \
    "v_smfmac_f32_16x16x64_bf16 v[80:83], v[64:67], a[144:151], v10\n\t" \
    "ds_write_b32 v12, v157 offset:64\n\t" \
    "v_smfmac_f32_16x16x64_bf16 v[80:83], v[68:71], a[152:159], v10\n\t" \
    "v_smfmac_f32_16x16x64_bf16 v[80:83], v[44:47], a[208:215], v10\n\t" \
    "s_nop 2\n\t" \
    "v_smfmac_f32_16x16x64_bf16 v[84:87], v[48:51], a[248:255], v10\n\t" \
    "s_nop 2\n\t" \
    "v_smfmac_f32_16x16x64_bf16 v[84:87], v[52:55], v[224:231], v10\n\t" \
    "v_add_f32 v80, v80, v82\n\t" \
    "v_smfmac_f32_16x16x64_bf16 v[84:87], v[56:59], v[232:239], v10\n\t" \
    "v_add_f32 v81, v81, v83\n\t" \
    "v_smfmac_f32_16x16x64_bf16 v[84:87], v[60:63], v[240:247], v10\n\t" \
    "v_cvt_pk_bf16_f32 v158, v80, v81\n\t" \
    "v_smfmac_f32_16x16x64_bf16 v[84:87], v[64:67], a[216:223], v10\n\t" \
    "ds_write_b32 v16, v158 offset:0\n\t" \
    "v_smfmac_f32_16x16x64_bf16 v[84:87], v[68:71], a[224:231], v10\n\t" \
    "ds_read_b64_tr_b16 v[132:133], v13 offset:0\n\t" \
    "ds_read_b64_tr_b16 v[134:135], v13 offset:128\n\t" \
    "v_smfmac_f32_16x16x64_bf16 v[84:87], v[44:47], v[248:255], v10\n\t" \
    "ds_read_b64_tr_b16 v[136:137], v13 offset:1024\n\t" \
    "ds_read_b64_tr_b16 v[138:139], v13 offset:1152\n\t" \
    "s_nop 4\n\t" \
    "v_cmp_u_f32 vcc, v72, v73\n\t" \
    "v_pk_add_f32 v[84:85], v[84:85], v[86:87]\n\t" \
    "v_cvt_pk_bf16_f32 v159, v84, v85\n\t" \
    "ds_write_b32 v16, v159 offset:256\n\t" \
    "s_cbranch_vccnz qk_Gslow44_%=\n\t" \
    "qk_Gback45_%=:\n\t" \
    "s_waitcnt lgkmcnt(3)\n\t" \
    "v_smfmac_f32_16x16x64_bf16 v[88:91], v[132:135], v[100:107], v10\n\t" \
    "s_waitcnt lgkmcnt(1)\n\t" \
    "v_smfmac_f32_16x16x64_bf16 v[88:91], v[136:139], v[108:115], v10\n\t" \
    "s_waitcnt lgkmcnt(0)\n\t" \
    "s_barrier\n\t" \
    "ds_read_b64_tr_b16 v[140:141], v17 offset:0\n\t" \
    "v_smfmac_f32_16x16x64_bf16 v[92:95], v[132:135], v[116:123], v10\n\t" \
    "ds_read_b64_tr_b16 v[142:143], v17 offset:256\n\t" \
    "ds_read_b64_tr_b16 v[144:145], v17 offset:512\n\t" \
    "ds_read_b64_tr_b16 v[146:147], v17 offset:768\n\t" \
    "ds_read_b64_tr_b16 v[148:149], v17 offset:1024\n\t" \
    "v_smfmac_f32_16x16x64_bf16 v[92:95], v[136:139], v[124:131], v10\n\t" \
    "ds_read_b64_tr_b16 v[150:151], v17 offset:1280\n\t" \
    "ds_read_b64_tr_b16 v[152:153], v17 offset:1536\n\t" \
    "ds_read_b64_tr_b16 v[154:155], v17 offset:1792\n\t" \
    "v_smfmac_f32_16x16x64_bf16 v[184:187], v[132:135], v[100:107], v10\n\t" \
    "qk_qb47_%=:\n\t" \
    "s_waitcnt lgkmcnt(4)\n\t" \
    "v_smfmac_f32_16x16x64_bf16 v[96:99], v[132:135], v[140:147], v10\n\t" \
    "s_waitcnt lgkmcnt(0)\n\t" \
    "v_smfmac_f32_16x16x64_bf16 v[96:99], v[136:139], v[148:155], v10\n\t" \
    "v_add_f32 v168, v89, v91\n\t" \
    "v_add_f32 v169, v93, v95\n\t" \
    "v_cmp_u_f32 vcc, v168, v169\n\t" \
    "v_cvt_pk_bf16_f32 v168, 0, v168\n\t" \
    "v_cvt_pk_bf16_f32 v169, 0, v169\n\t" \
    "v_mul_f32 v168, v168, v26\n\t" \
    "v_mul_f32 v169, v169, v26\n\t" \
    "v_cvt_pk_bf16_f32 v168, 0, v168\n\t" \
    "v_add_f32 v172, v97, v99\n\t" \
    "v_cvt_pk_bf16_f32 v169, 0, v169\n\t" \
    "v_max_f32 v178, v168, v169\n\t" \
    "v_cvt_pk_bf16_f32 v172, 0, v172\n\t" \
    "v_mul_f32 v172, v172, v26\n\t" \
    "v_max_f32_dpp v178, v178, v178 row_mirror row_mask:0xf bank_mask:0xf\n\t" \
    "v_cvt_pk_bf16_f32 v172, 0, v172\n\t" \
    "v_max_f32 v173, v178, v172\n\t" \
    "v_mul_f32 v168, v168, v28\n\t" \
    "v_mul_f32 v169, v169, v28\n\t" \
    "v_max_f32_dpp v173, v173, v173 quad_perm:[1,0,3,2] row_mask:0xf bank_mask:0xf\n\t" \
    "v_mul_f32 v172, v172, v28\n\t" \
    "s_cbranch_vccnz qk_qs46_%=\n\t" \
    "qk_qb248_%=:\n\t" \
    "v_max_f32_dpp v173, v173, v173 quad_perm:[2,3,0,1] row_mask:0xf bank_mask:0xf\n\t" \
    "s_nop 1\n\t" \
    "v_max_f32_dpp v173, v173, v173 row_half_mirror row_mask:0xf bank_mask:0xf\n\t" \
    "v_fma_f32 v172, -v173, v28, v172\n\t" \
    "v_fma_f32 v168, -v173, v28, v168\n\t" \
    "v_fma_f32 v169, -v173, v28, v169\n\t" \
    "v_exp_f32 v172, v172\n\t" \
    "v_exp_f32 v168, v168\n\t" \
    "v_exp_f32 v169, v169\n\t" \
    "v_fma_f32 v173, v172, 0.5, v168\n\t" \
    "v_add_f32 v173, v173, v169\n\t" \
    "v_mov_b32_dpp v174, v172 quad_perm:[0,0,2,2] row_mask:0xf bank_mask:0xf\n\t" \
    "v_mov_b32_dpp v175, v172 quad_perm:[1,1,3,3] row_mask:0xf bank_mask:0xf\n\t" \
    "v_add_f32_dpp v173, v173, v173 quad_perm:[1,0,3,2] row_mask:0xf bank_mask:0xf\n\t" \
    "v_mov_b32_dpp v176, v168 quad_perm:[1,0,3,2] row_mask:0xf bank_mask:0xf\n\t" \
    "v_mov_b32_dpp v177, v169 quad_perm:[1,0,3,2] row_mask:0xf bank_mask:0xf\n\t" \
    "v_add_f32_dpp v173, v173, v173 quad_perm:[2,3,0,1] row_mask:0xf bank_mask:0xf\n\t" \
    "v_cndmask_b32 v168, v168, v177, s[48:49]\n\t" \
    "v_cndmask_b32 v169, v176, v169, s[48:49]\n\t" \
    "v_add_f32_dpp v173, v173, v173 row_half_mirror row_mask:0xf bank_mask:0xf\n\t" \
    "s_nop 1\n\t" \
    "v_add_f32_dpp v173, v173, v173 row_mirror row_mask:0xf bank_mask:0xf\n\t" \
    "v_rcp_f32 v173, v173\n\t" \
    "s_nop 0\n\t" \
    "v_pk_mul_f32 v[168:169], v[168:169], v[172:173] op_sel:[0,1] op_sel_hi:[1,1]\n\t" \
    "v_cvt_pk_bf16_f32 v176, v168, v169\n\t" \
    "global_store_dword v18, v176, s[46:47] sc1\n\t" \
    "global_store_dword v180, v176, s[46:47] sc1\n\t" \
    "global_store_dword v181, v176, s[46:47] sc1\n\t" \
    "global_store_dword v182, v176, s[46:47] sc1\n\t" \
    "v_pk_mul_f32 v[174:175], v[174:175], v[172:173] op_sel:[0,1] op_sel_hi:[1,1]\n\t" \
    "v_cvt_pk_bf16_f32 v177, v174, v175\n\t" \
    "global_store_dword v19, v177, s[46:47] sc1\n\t" \
    "s_cmp_eq_u32 s65, 63\n\t" \
    "s_cbranch_scc1 qk_last36_%=\n\t" \
    "v_readfirstlane_b32 s69, v25\n\t" \
    "s_add_u32 s65, s65, 1\n\t" \
    "s_max_i32 s83, s83, s69\n\t" \
    "s_cmp_gt_u32 s65, 6\n\t" \
    "s_cselect_b32 s83, 99, s83\n\t" \
    "v_mov_b32 v72, 0\n\t" \
    "v_mov_b32 v73, 0\n\t" \
    "v_mov_b32 v74, 0\n\t" \
    "v_mov_b32 v75, 0\n\t" \
    "v_mov_b32 v76, 0\n\t" \
    "v_mov_b32 v77, 0\n\t" \
    "v_mov_b32 v78, 0\n\t" \
    "v_mov_b32 v79, 0\n\t" \
    "v_mov_b32 v80, 0\n\t" \
    "v_mov_b32 v81, 0\n\t" \
    "v_mov_b32 v82, 0\n\t" \
    "v_mov_b32 v83, 0\n\t" \
    "v_mov_b32 v84, 0\n\t" \
    "v_mov_b32 v85, 0\n\t" \
    "v_mov_b32 v86, 0\n\t" \
    "v_mov_b32 v87, 0\n\t" \
    "v_mov_b32 v88, 0\n\t" \
    "v_mov_b32 v89, 0\n\t" \
    "v_mov_b32 v90, 0\n\t" \
    "v_mov_b32 v91, 0\n\t" \
    "v_mov_b32 v92, 0\n\t" \
    "v_mov_b32 v93, 0\n\t" \
    "v_mov_b32 v94, 0\n\t" \
    "v_mov_b32 v95, 0\n\t" \
    "v_mov_b32 v96, 0\n\t" \
    "v_mov_b32 v97, 0\n\t" \
    "v_mov_b32 v98, 0\n\t" \
    "v_mov_b32 v99, 0\n\t" \
    "s_add_u32 s69, s65, 0\n\t" \
    "s_and_b32 s69, s69, 7\n\t" \
    "s_lshl_b32 s69, s69, 18\n\t" \
    "s_add_u32 s69, s69, s62\n\t" \
    "s_add_u32 s46, s36, s69\n\t" \
    "s_addc_u32 s47, s37, 0\n\t" \
    "s_mul_i32 s70, s67, 7\n\t" \
    "s_add_u32 s69, s65, 0\n\t" \
    "s_xor_b32 s69, s69, s70\n\t" \
    "s_and_b32 s69, s69, 63\n\t" \
    "s_lshl_b32 s69, s69, 13\n\t" \
    "s_mul_i32 s70, s67, 524288\n\t" \
    "s_add_u32 s69, s69, s70\n\t" \
    "s_add_u32 s69, s69, 23068672\n\t" \
    "s_add_u32 s38, s36, s69\n\t" \
    "s_addc_u32 s39, s37, 0\n\t" \
    "qk_WF49_%=:\n\t" \
    "s_cmp_ge_i32 s83, s65\n\t" \
    "s_cbranch_scc1 qk_WFok50_%=\n\t" \
    "global_load_dword v160, v24, s[36:37] sc1\n\t" \
    "s_waitcnt vmcnt(0)\n\t" \
    "v_readfirstlane_b32 s83, v160\n\t" \
    "s_nop 1\n\t" \
    "s_cmp_ge_i32 s83, s65\n\t" \
    "s_cbranch_scc1 qk_WFok50_%=\n\t" \
    "s_cmp_eq_u32 s68, 0\n\t" \
    "s_cbranch_scc1 qk_F51_%=\n\t" \
    "s_sub_u32 s68, s68, 1\n\t" \
    "s_sleep 2\n\t" \
    "s_branch qk_WF49_%=\n\t" \
    "qk_F51_%=:\n\t" \
    "v_mov_b32 v160, 5767424\n\t" \
    "v_mov_b32 v161, 9\n\t" \
    "global_store_dword v160, v161, s[36:37] sc1\n\t" \
    "s_waitcnt vmcnt(0)\n\t" \
    "s_branch qk_WFok50_%=\n\t" \
    "qk_WFok50_%=:\n\t" \
    "global_load_dwordx4 v[100:103], v0, s[38:39] offset:0 sc1\n\t" \
    "global_load_dwordx4 v[104:107], v0, s[38:39] offset:2048 sc1\n\t" \
    "global_load_dwordx4 v[108:111], v15, s[38:39] offset:0 sc1\n\t" \
    "global_load_dwordx4 v[112:115], v15, s[38:39] offset:2048 sc1\n\t" \
    "global_load_dwordx4 v[116:119], v0, s[38:39] offset:1024 sc1\n\t" \
    "global_load_dwordx4 v[120:123], v0, s[38:39] offset:3072 sc1\n\t" \
    "global_load_dwordx4 v[124:127], v15, s[38:39] offset:1024 sc1\n\t" \
    "global_load_dwordx4 v[128:131], v15, s[38:39] offset:3072 sc1\n\t" \
    "s_cmp_gt_i32 s83, s65\n\t" \
    "s_cbranch_scc1 qk_nf52_%=\n\t" \
    "global_load_dword v25, v24, s[36:37] sc1\n\t" \
    "qk_nf52_%=:\n\t" \
    "s_barrier\n\t" \
    "ds_read_b32 v168, v11 offset:20480\n\t" \
    "s_waitcnt lgkmcnt(0)\n\t" \
    "v_readfirstlane_b32 s70, v168\n\t" \
    "ds_write_b32 v12, v31 offset:0\n\t" \
    "ds_write_b32 v12, v31 offset:64\n\t" \
    "s_branch qk_go37_%=\n\t" \
    "qk_Gbad42_%=:\n\t" \
    "s_waitcnt lgkmcnt(0)\n\t" \
    "s_nop 7\n\t" \
    "v_mov_b32 v72, 0\n\t" \
    "v_mov_b32 v73, 0\n\t" \
    "v_mov_b32 v74, 0\n\t" \
    "v_mov_b32 v75, 0\n\t" \
    "v_mov_b32 v76, 0\n\t" \
    "v_mov_b32 v77, 0\n\t" \
    "v_mov_b32 v78, 0\n\t" \
    "v_mov_b32 v79, 0\n\t" \
    "v_mov_b32 v80, 0\n\t" \
    "v_mov_b32 v81, 0\n\t" \
    "v_mov_b32 v82, 0\n\t" \
    "v_mov_b32 v83, 0\n\t" \
    "v_mov_b32 v84, 0\n\t" \
    "v_mov_b32 v85, 0\n\t" \
    "v_mov_b32 v86, 0\n\t" \
    "v_mov_b32 v87, 0\n\t" \
    "s_cmp_eq_u32 s68, 0\n\t" \
    "s_cbranch_scc1 qk_F53_%=\n\t" \
    "s_sub_u32 s68, s68, 1\n\t" \
    "s_branch qk_G41_%=\n\t" \
    "qk_F53_%=:\n\t" \
    "v_mov_b32 v160, 5767424\n\t" \
    "v_mov_b32 v161, 1\n\t" \
    "global_store_dword v160, v161, s[36:37] sc1\n\t" \
    "s_waitcnt vmcnt(0)\n\t" \
    "s_branch qk_Gc43_%=\n\t" \
    "qk_Gslow44_%=:\n\t" \
    "v_max_u32 v160, v36, v37\n\t" \
    "v_max3_u32 v160, v160, v38, v39\n\t" \
    "v_max3_u32 v160, v160, v40, v41\n\t" \
    "v_max3_u32 v160, v160, v42, v43\n\t" \
    "v_max3_u32 v160, v160, v44, v45\n\t" \
    "v_max3_u32 v160, v160, v46, v47\n\t" \
    "v_max3_u32 v160, v160, v48, v49\n\t" \
    "v_max3_u32 v160, v160, v50, v51\n\t" \
    "v_max3_u32 v160, v160, v52, v53\n\t" \
    "v_max3_u32 v160, v160, v54, v55\n\t" \
    "v_max3_u32 v160, v160, v56, v57\n\t" \
    "v_max3_u32 v160, v160, v58, v59\n\t" \
    "v_max3_u32 v160, v160, v60, v61\n\t" \
    "v_max3_u32 v160, v160, v62, v63\n\t" \
    "v_max3_u32 v160, v160, v64, v65\n\t" \
    "v_max3_u32 v160, v160, v66, v67\n\t" \
    "v_max3_u32 v160, v160, v68, v69\n\t" \
    "v_max3_u32 v160, v160, v70, v71\n\t" \
    "v_cmp_eq_u32 vcc, -1, v160\n\t" \
    "s_cbranch_vccz qk_Gback45_%=\n\t" \
    "s_waitcnt lgkmcnt(0)\n\t" \
    "v_sub_u32 v160, v1, v3\n\t" \
    "s_nop 0\n\t" \
    "v_readfirstlane_b32 s69, v160\n\t" \
    "v_add_u32 v163, 20480, v0\n\t" \
    "s_nop 7\n\t" \
    "s_add_u32 s30, s46, s69\n\t" \
    "s_addc_u32 s31, s47, 0\n\t" \
    "qk_Gag54_%=:\n\t" \
    "global_load_dwordx4 v[36:39], v1, s[46:47] sc1\n\t" \
    "global_load_dwordx4 v[40:43], v23, s[46:47] sc1\n\t" \
    "global_load_dwordx4 v[44:47], v163, s[30:31] sc1\n\t" \
    "global_load_dwordx4 v[48:51], v4, s[30:31] sc1\n\t" \
    "global_load_dwordx4 v[52:55], v5, s[30:31] sc1\n\t" \
    "global_load_dwordx4 v[56:59], v6, s[30:31] sc1\n\t" \
    "global_load_dwordx4 v[60:63], v7, s[30:31] sc1\n\t" \
    "global_load_dwordx4 v[64:67], v8, s[30:31] sc1\n\t" \
    "global_load_dwordx4 v[68:71], v9, s[30:31] sc1\n\t" \
    "s_waitcnt vmcnt(0)\n\t" \
    "v_max_u32 v160, v36, v37\n\t" \
    "v_max3_u32 v160, v160, v38, v39\n\t" \
    "v_max3_u32 v160, v160, v40, v41\n\t" \
    "v_max3_u32 v160, v160, v42, v43\n\t" \
    "v_max3_u32 v160, v160, v44, v45\n\t" \
    "v_max3_u32 v160, v160, v46, v47\n\t" \
    "v_max3_u32 v160, v160, v48, v49\n\t" \
    "v_max3_u32 v160, v160, v50, v51\n\t" \
    "v_max3_u32 v160, v160, v52, v53\n\t" \
    "v_max3_u32 v160, v160, v54, v55\n\t" \
    "v_max3_u32 v160, v160, v56, v57\n\t" \
    "v_max3_u32 v160, v160, v58, v59\n\t" \
    "v_max3_u32 v160, v160, v60, v61\n\t" \
    "v_max3_u32 v160, v160, v62, v63\n\t" \
    "v_max3_u32 v160, v160, v64, v65\n\t" \
    "v_max3_u32 v160, v160, v66, v67\n\t" \
    "v_max3_u32 v160, v160, v68, v69\n\t" \
    "v_max3_u32 v160, v160, v70, v71\n\t" \
    "v_cmp_eq_u32 vcc, -1, v160\n\t" \
    "s_cbranch_vccz qk_Gredo55_%=\n\t" \
    "s_cmp_eq_u32 s68, 0\n\t" \
    "s_cbranch_scc1 qk_F56_%=\n\t" \
    "s_sub_u32 s68, s68, 1\n\t" \
    "s_sleep 1\n\t" \
    "s_branch qk_Gag54_%=\n\t" \
    "qk_F56_%=:\n\t" \
    "v_mov_b32 v160, 5767424\n\t" \
    "v_mov_b32 v161, 2\n\t" \
    "global_store_dword v160, v161, s[36:37] sc1\n\t" \
    "s_waitcnt vmcnt(0)\n\t" \
    "s_branch qk_Gredo55_%=\n\t" \
    "qk_Gredo55_%=:\n\t" \
    "v_mov_b32 v72, 0\n\t" \
    "v_mov_b32 v73, 0\n\t" \
    "v_mov_b32 v74, 0\n\t" \
    "v_mov_b32 v75, 0\n\t" \
    "v_mov_b32 v76, 0\n\t" \
    "v_mov_b32 v77, 0\n\t" \
    "v_mov_b32 v78, 0\n\t" \
    "v_mov_b32 v79, 0\n\t" \
    "v_mov_b32 v80, 0\n\t" \
    "v_mov_b32 v81, 0\n\t" \
    "v_mov_b32 v82, 0\n\t" \
    "v_mov_b32 v83, 0\n\t" \
    "v_mov_b32 v84, 0\n\t" \
    "v_mov_b32 v85, 0\n\t" \
    "v_mov_b32 v86, 0\n\t" \
    "v_mov_b32 v87, 0\n\t" \
    "v_smfmac_f32_16x16x64_bf16 v[72:75], v[36:39], a[16:23], v10\n\t" \
    "v_smfmac_f32_16x16x64_bf16 v[72:75], v[40:43], a[24:31], v10\n\t" \
    "v_smfmac_f32_16x16x64_bf16 v[72:75], v[44:47], a[64:71], v10\n\t" \
    "v_smfmac_f32_16x16x64_bf16 v[72:75], v[48:51], a[32:39], v10\n\t" \
    "v_smfmac_f32_16x16x64_bf16 v[72:75], v[52:55], a[40:47], v10\n\t" \
    "v_smfmac_f32_16x16x64_bf16 v[72:75], v[56:59], a[48:55], v10\n\t" \
    "v_smfmac_f32_16x16x64_bf16 v[72:75], v[60:63], a[56:63], v10\n\t" \
    "v_smfmac_f32_16x16x64_bf16 v[72:75], v[64:67], a[0:7], v10\n\t" \
    "v_smfmac_f32_16x16x64_bf16 v[72:75], v[68:71], a[8:15], v10\n\t" \
    "v_smfmac_f32_16x16x64_bf16 v[76:79], v[36:39], a[88:95], v10\n\t" \
    "v_smfmac_f32_16x16x64_bf16 v[76:79], v[40:43], a[96:103], v10\n\t" \
    "v_smfmac_f32_16x16x64_bf16 v[76:79], v[44:47], a[136:143], v10\n\t" \
    "v_smfmac_f32_16x16x64_bf16 v[76:79], v[48:51], a[104:111], v10\n\t" \
    "v_smfmac_f32_16x16x64_bf16 v[76:79], v[52:55], a[112:119], v10\n\t" \
    "v_smfmac_f32_16x16x64_bf16 v[76:79], v[56:59], a[120:127], v10\n\t" \
    "v_smfmac_f32_16x16x64_bf16 v[76:79], v[60:63], a[128:135], v10\n\t" \
    "v_smfmac_f32_16x16x64_bf16 v[76:79], v[64:67], a[72:79], v10\n\t" \
    "v_smfmac_f32_16x16x64_bf16 v[76:79], v[68:71], a[80:87], v10\n\t" \
    "v_smfmac_f32_16x16x64_bf16 v[80:83], v[36:39], a[160:167], v10\n\t" \
    "v_smfmac_f32_16x16x64_bf16 v[80:83], v[40:43], a[168:175], v10\n\t" \
    "v_smfmac_f32_16x16x64_bf16 v[80:83], v[44:47], a[208:215], v10\n\t" \
    "v_smfmac_f32_16x16x64_bf16 v[80:83], v[48:51], a[176:183], v10\n\t" \
    "v_smfmac_f32_16x16x64_bf16 v[80:83], v[52:55], a[184:191], v10\n\t" \
    "v_smfmac_f32_16x16x64_bf16 v[80:83], v[56:59], a[192:199], v10\n\t" \
    "v_smfmac_f32_16x16x64_bf16 v[80:83], v[60:63], a[200:207], v10\n\t" \
    "v_smfmac_f32_16x16x64_bf16 v[80:83], v[64:67], a[144:151], v10\n\t" \
    "v_smfmac_f32_16x16x64_bf16 v[80:83], v[68:71], a[152:159], v10\n\t" \
    "v_smfmac_f32_16x16x64_bf16 v[84:87], v[36:39], a[232:239], v10\n\t" \
    "v_smfmac_f32_16x16x64_bf16 v[84:87], v[40:43], a[240:247], v10\n\t" \
    "v_smfmac_f32_16x16x64_bf16 v[84:87], v[44:47], v[248:255], v10\n\t" \
    "v_smfmac_f32_16x16x64_bf16 v[84:87], v[48:51], a[248:255], v10\n\t" \
    "v_smfmac_f32_16x16x64_bf16 v[84:87], v[52:55], v[224:231], v10\n\t" \
    "v_smfmac_f32_16x16x64_bf16 v[84:87], v[56:59], v[232:239], v10\n\t" \
    "v_smfmac_f32_16x16x64_bf16 v[84:87], v[60:63], v[240:247], v10\n\t" \
    "v_smfmac_f32_16x16x64_bf16 v[84:87], v[64:67], a[216:223], v10\n\t" \
    "v_smfmac_f32_16x16x64_bf16 v[84:87], v[68:71], a[224:231], v10\n\t" \
    "v_add_f32 v72, v72, v74\n\t" \
    "v_add_f32 v73, v73, v75\n\t" \
    "v_cvt_pk_bf16_f32 v156, v72, v73\n\t" \
    "ds_write_b32 v12, v156 offset:0\n\t" \
    "v_add_f32 v76, v76, v78\n\t" \
    "v_add_f32 v77, v77, v79\n\t" \
    "v_cvt_pk_bf16_f32 v157, v76, v77\n\t" \
    "ds_write_b32 v12, v157 offset:64\n\t" \
    "v_add_f32 v80, v80, v82\n\t" \
    "v_add_f32 v81, v81, v83\n\t" \
    "v_cvt_pk_bf16_f32 v158, v80, v81\n\t" \
    "ds_write_b32 v16, v158 offset:0\n\t" \
    "v_pk_add_f32 v[84:85], v[84:85], v[86:87]\n\t" \
    "v_cvt_pk_bf16_f32 v159, v84, v85\n\t" \
    "ds_write_b32 v16, v159 offset:256\n\t" \
    "s_branch qk_Gback45_%=\n\t" \
    "qk_qs46_%=:\n\t" \
    "s_cmp_eq_u32 s91, s65\n\t" \
    "s_cbranch_scc1 qk_qb248_%=\n\t" \
    "s_mov_b32 s91, s65\n\t" \
    "s_nop 7\n\t" \
    "ds_read_b64_tr_b16 v[132:133], v13 offset:0\n\t" \
    "ds_read_b64_tr_b16 v[134:135], v13 offset:128\n\t" \
    "ds_read_b64_tr_b16 v[136:137], v13 offset:1024\n\t" \
    "ds_read_b64_tr_b16 v[138:139], v13 offset:1152\n\t" \
    "v_mov_b32 v88, 0\n\t" \
    "v_mov_b32 v89, 0\n\t" \
    "v_mov_b32 v90, 0\n\t" \
    "v_mov_b32 v91, 0\n\t" \
    "v_mov_b32 v92, 0\n\t" \
    "v_mov_b32 v93, 0\n\t" \
    "v_mov_b32 v94, 0\n\t" \
    "v_mov_b32 v95, 0\n\t" \
    "v_mov_b32 v96, 0\n\t" \
    "v_mov_b32 v97, 0\n\t" \
    "v_mov_b32 v98, 0\n\t" \
    "v_mov_b32 v99, 0\n\t" \
    "s_waitcnt lgkmcnt(0)\n\t" \
    "v_smfmac_f32_16x16x64_bf16 v[88:91], v[132:135], v[100:107], v10\n\t" \
    "v_smfmac_f32_16x16x64_bf16 v[92:95], v[132:135], v[116:123], v10\n\t" \
    "v_smfmac_f32_16x16x64_bf16 v[88:91], v[136:139], v[108:115], v10\n\t" \
    "v_smfmac_f32_16x16x64_bf16 v[92:95], v[136:139], v[124:131], v10\n\t" \
    "s_nop 7\n\t" \
    "s_branch qk_qb47_%=\n\t" \
    "qk_last36_%=:\n\t" \
    "s_endpgm\n\t" \
    "qk_loop13_%=:\n\t" \
    "v_mov_b32 v72, 0\n\t" \
    "v_mov_b32 v73, 0\n\t" \
    "v_mov_b32 v74, 0\n\t" \
    "v_mov_b32 v75, 0\n\t" \
    "v_mov_b32 v76, 0\n\t" \
    "v_mov_b32 v77, 0\n\t" \
    "v_mov_b32 v78, 0\n\t" \
    "v_mov_b32 v79, 0\n\t" \
    "v_mov_b32 v80, 0\n\t" \
    "v_mov_b32 v81, 0\n\t" \
    "v_mov_b32 v82, 0\n\t" \
    "v_mov_b32 v83, 0\n\t" \
    "v_mov_b32 v84, 0\n\t" \
    "v_mov_b32 v85, 0\n\t" \
    "v_mov_b32 v86, 0\n\t" \
    "v_mov_b32 v87, 0\n\t" \
    "s_add_u32 s69, s65, 0\n\t" \
    "s_and_b32 s69, s69, 7\n\t" \
    "s_lshl_b32 s69, s69, 18\n\t" \
    "s_add_u32 s69, s69, s62\n\t" \
    "s_add_u32 s46, s36, s69\n\t" \
    "s_addc_u32 s47, s37, 0\n\t" \
    "s_mul_i32 s70, s67, 7\n\t" \
    "s_add_u32 s69, s65, 0\n\t" \
    "s_xor_b32 s69, s69, s70\n\t" \
    "s_and_b32 s69, s69, 63\n\t" \
    "s_lshl_b32 s69, s69, 12\n\t" \
    "s_mul_i32 s70, s67, 262144\n\t" \
    "s_add_u32 s69, s69, s70\n\t" \
    "s_add_u32 s69, s69, 39845888\n\t" \
    "s_add_u32 s40, s36, s69\n\t" \
    "s_addc_u32 s41, s37, 0\n\t" \
    "s_sub_u32 s91, s65, 1\n\t" \
    "v_mov_b32 v25, s91\n\t" \
    "s_sub_u32 s70, s79, s74\n\t" \
    "s_max_i32 s70, s70, 0\n\t" \
    "s_min_i32 s70, s70, 16000\n\t" \
    "s_lshl_b32 s70, s70, 1\n\t" \
    "s_mul_hi_u32 s70, s70, s77\n\t" \
    "s_add_u32 s70, s70, s73\n\t" \
    ".p2align 6\n\t" \
    "s_nop 0\n\t" \
    "s_nop 0\n\t" \
    "s_nop 0\n\t" \
    "s_nop 0\n\t" \
    "s_nop 0\n\t" \
    "s_nop 0\n\t" \
    "qk_go58_%=:\n\t" \
    "s_getpc_b64 s[30:31]\n\t" \
    "qk_Wp62_%=:\n\t" \
    "s_add_u32 s30, s30, qk_We63_%=-qk_Wp62_%=\n\t" \
    "s_addc_u32 s31, s31, 0\n\t" \
    "s_sub_u32 s70, s70, 96\n\t" \
    "qk_W61_%=:\n\t" \
    "s_memtime s[56:57]\n\t" \
    "s_waitcnt lgkmcnt(0)\n\t" \
    "s_sub_u32 s69, s70, s56\n\t" \
    "s_cmp_gt_i32 s69, 80\n\t" \
    "s_cbranch_scc1 qk_W61_%=\n\t" \
    "s_max_i32 s69, s69, 0\n\t" \
    "s_and_b32 s69, s69, -4\n\t" \
    "s_sub_u32 s30, s30, s69\n\t" \
    "s_subb_u32 s31, s31, 0\n\t" \
    "s_setpc_b64 s[30:31]\n\t" \
    "s_nop 0\n\t" \
    "s_nop 0\n\t" \
    "s_nop 0\n\t" \
    "s_nop 0\n\t" \
    "s_nop 0\n\t" \
    "s_nop 0\n\t" \
    "s_nop 0\n\t" \
    "s_nop 0\n\t" \
    "s_nop 0\n\t" \
    "s_nop 0\n\t" \
    "s_nop 0\n\t" \
    "s_nop 0\n\t" \
    "s_nop 0\n\t" \
    "s_nop 0\n\t" \
    "s_nop 0\n\t" \
    "s_nop 0\n\t" \
    "s_nop 0\n\t" \
    "s_nop 0\n\t" \
    "s_nop 0\n\t" \
    "s_nop 0\n\t" \
    "qk_We63_%=:\n\t" \
    "qk_G64_%=:\n\t" \
    "global_load_dwordx4 v[36:39], v1, s[46:47] sc1\n\t" \
    "global_load_dword v161, v2, s[46:47] sc1\n\t" \
    "global_load_dwordx4 v[40:43], v23, s[46:47] sc1\n\t" \
    "s_waitcnt vmcnt(2)\n\t" \
    "ds_write_b128 v3, v[36:39]\n\t" \
    "v_smfmac_f32_16x16x64_bf16 v[72:75], v[36:39], a[32:39], v10\n\t" \
    "s_waitcnt vmcnt(1)\n\t" \
    "ds_write_b32 v22, v161 offset:19968\n\t" \
    "v_smfmac_f32_16x16x64_bf16 v[76:79], v[36:39], a[104:111], v10\n\t" \
    "s_waitcnt vmcnt(0)\n\t" \
    "ds_write_b128 v3, v[40:43] offset:1024\n\t" \
    "v_max3_u32 v160, v36, v40, v161\n\t" \
    "v_cmp_eq_u32 vcc, -1, v160\n\t" \
    "v_smfmac_f32_16x16x64_bf16 v[80:83], v[36:39], a[176:183], v10\n\t" \
    "v_smfmac_f32_16x16x64_bf16 v[84:87], v[36:39], a[248:255], v10\n\t" \
    "v_smfmac_f32_16x16x64_bf16 v[72:75], v[40:43], a[40:47], v10\n\t" \
    "s_cbranch_vccnz qk_Gbad65_%=\n\t" \
    "qk_Gc66_%=:\n\t" \
    "s_waitcnt lgkmcnt(0)\n\t" \
    "s_barrier\n\t" \
    "ds_read_b128 v[48:51], v4\n\t" \
    "ds_read_b128 v[52:55], v5\n\t" \
    "v_smfmac_f32_16x16x64_bf16 v[76:79], v[40:43], a[112:119], v10\n\t" \
    "ds_read_b128 v[56:59], v6\n\t" \
    "ds_read_b128 v[60:63], v7\n\t" \
    "v_smfmac_f32_16x16x64_bf16 v[80:83], v[40:43], a[184:191], v10\n\t" \
    "ds_read_b128 v[64:67], v8\n\t" \
    "v_smfmac_f32_16x16x64_bf16 v[84:87], v[40:43], v[224:231], v10\n\t" \
    "ds_read_b128 v[68:71], v9\n\t" \
    "s_waitcnt lgkmcnt(5)\n\t" \
    "v_smfmac_f32_16x16x64_bf16 v[72:75], v[48:51], a[48:55], v10\n\t" \
    "ds_read_b128 v[44:47], v0 offset:19456\n\t" \
    "s_waitcnt lgkmcnt(5)\n\t" \
    "v_smfmac_f32_16x16x64_bf16 v[72:75], v[52:55], a[56:63], v10\n\t" \
    "s_waitcnt lgkmcnt(4)\n\t" \
    "v_smfmac_f32_16x16x64_bf16 v[72:75], v[56:59], a[0:7], v10\n\t" \
    "s_waitcnt lgkmcnt(3)\n\t" \
    "v_smfmac_f32_16x16x64_bf16 v[72:75], v[60:63], a[8:15], v10\n\t" \
    "s_waitcnt lgkmcnt(2)\n\t" \
    "v_smfmac_f32_16x16x64_bf16 v[72:75], v[64:67], a[16:23], v10\n\t" \
    "s_waitcnt lgkmcnt(1)\n\t" \
    "v_smfmac_f32_16x16x64_bf16 v[72:75], v[68:71], a[24:31], v10\n\t" \
    "s_waitcnt lgkmcnt(0)\n\t" \
    "v_smfmac_f32_16x16x64_bf16 v[72:75], v[44:47], a[64:71], v10\n\t" \
    "s_nop 2\n\t" \
    "v_smfmac_f32_16x16x64_bf16 v[76:79], v[48:51], a[120:127], v10\n\t" \
    "s_cmp_ge_u32 s91, 57\n\t" \
    "s_cbranch_scc1 qk_kf59_%=\n\t" \
    "qk_kb60_%=:\n\t" \
    "v_smfmac_f32_16x16x64_bf16 v[76:79], v[52:55], a[128:135], v10\n\t" \
    "s_nop 0\n\t" \
    "v_add_f32 v72, v72, v74\n\t" \
    "v_smfmac_f32_16x16x64_bf16 v[76:79], v[56:59], a[72:79], v10\n\t" \
    "v_add_f32 v73, v73, v75\n\t" \
    "v_smfmac_f32_16x16x64_bf16 v[76:79], v[60:63], a[80:87], v10\n\t" \
    "v_cvt_pk_bf16_f32 v156, v72, v73\n\t" \
    "v_smfmac_f32_16x16x64_bf16 v[76:79], v[64:67], a[88:95], v10\n\t" \
    "ds_write_b32 v12, v156 offset:0\n\t" \
    "v_smfmac_f32_16x16x64_bf16 v[76:79], v[68:71], a[96:103], v10\n\t" \
    "v_smfmac_f32_16x16x64_bf16 v[76:79], v[44:47], a[136:143], v10\n\t" \
    "s_nop 2\n\t" \
    "v_smfmac_f32_16x16x64_bf16 v[80:83], v[48:51], a[192:199], v10\n\t" \
    "s_nop 2\n\t" \
    "v_smfmac_f32_16x16x64_bf16 v[80:83], v[52:55], a[200:207], v10\n\t" \
    "v_add_f32 v76, v76, v78\n\t" \
    "v_smfmac_f32_16x16x64_bf16 v[80:83], v[56:59], a[144:151], v10\n\t" \
    "v_add_f32 v77, v77, v79\n\t" \
    "v_smfmac_f32_16x16x64_bf16 v[80:83], v[60:63], a[152:159], v10\n\t" \
    "v_cvt_pk_bf16_f32 v157, v76, v77\n\t" \
    "v_smfmac_f32_16x16x64_bf16 v[80:83], v[64:67], a[160:167], v10\n\t" \
    "ds_write_b32 v12, v157 offset:64\n\t" \
    "v_smfmac_f32_16x16x64_bf16 v[80:83], v[68:71], a[168:175], v10\n\t" \
    "v_smfmac_f32_16x16x64_bf16 v[80:83], v[44:47], a[208:215], v10\n\t" \
    "s_nop 2\n\t" \
    "v_smfmac_f32_16x16x64_bf16 v[84:87], v[48:51], v[232:239], v10\n\t" \
    "s_nop 2\n\t" \
    "v_smfmac_f32_16x16x64_bf16 v[84:87], v[52:55], v[240:247], v10\n\t" \
    "v_add_f32 v80, v80, v82\n\t" \
    "v_smfmac_f32_16x16x64_bf16 v[84:87], v[56:59], a[216:223], v10\n\t" \
    "v_add_f32 v81, v81, v83\n\t" \
    "v_smfmac_f32_16x16x64_bf16 v[84:87], v[60:63], a[224:231], v10\n\t" \
    "v_cvt_pk_bf16_f32 v158, v80, v81\n\t" \
    "v_smfmac_f32_16x16x64_bf16 v[84:87], v[64:67], a[232:239], v10\n\t" \
    "ds_write_b32 v16, v158 offset:0\n\t" \
    "v_smfmac_f32_16x16x64_bf16 v[84:87], v[68:71], a[240:247], v10\n\t" \
    "v_smfmac_f32_16x16x64_bf16 v[84:87], v[44:47], v[248:255], v10\n\t" \
    "s_nop 6\n\t" \
    "v_cmp_u_f32 vcc, v72, v73\n\t" \
    "v_pk_add_f32 v[84:85], v[84:85], v[86:87]\n\t" \
    "v_cvt_pk_bf16_f32 v159, v84, v85\n\t" \
    "ds_write_b32 v16, v159 offset:256\n\t" \
    "s_cbranch_vccnz qk_Gslow67_%=\n\t" \
    "qk_Gback68_%=:\n\t" \
    "s_waitcnt lgkmcnt(0)\n\t" \
    "s_barrier\n\t" \
    "s_sleep 1\n\t" \
    "ds_read_b64_tr_b16 v[140:141], v17 offset:0\n\t" \
    "ds_read_b64_tr_b16 v[142:143], v17 offset:256\n\t" \
    "ds_read_b64_tr_b16 v[144:145], v17 offset:512\n\t" \
    "ds_read_b64_tr_b16 v[146:147], v17 offset:768\n\t" \
    "ds_read_b64_tr_b16 v[148:149], v17 offset:1024\n\t" \
    "ds_read_b64_tr_b16 v[150:151], v17 offset:1280\n\t" \
    "ds_read_b64_tr_b16 v[152:153], v17 offset:1536\n\t" \
    "ds_read_b64_tr_b16 v[154:155], v17 offset:1792\n\t" \
    "s_waitcnt lgkmcnt(0)\n\t" \
    "s_cmp_eq_u32 s65, 63\n\t" \
    "s_cbranch_scc1 qk_last57_%=\n\t" \
    "s_cmp_ge_u32 s65, 58\n\t" \
    "s_cbranch_scc1 qk_nsl69_%=\n\t" \
    "s_sleep 6\n\t" \
    "global_store_dword v14, v25, s[36:37] sc1\n\t" \
    "qk_nsl69_%=:\n\t" \
    "s_mov_b64 exec, s[58:59]\n\t" \
    "global_store_dwordx4 v0, v[140:143], s[40:41] offset:0 sc1\n\t" \
    "global_store_dwordx4 v0, v[144:147], s[40:41] offset:1024 sc1\n\t" \
    "global_store_dwordx4 v0, v[148:151], s[40:41] offset:2048 sc1\n\t" \
    "global_store_dwordx4 v0, v[152:155], s[40:41] offset:3072 sc1\n\t" \
    "s_mov_b64 exec, -1\n\t" \
    "s_add_u32 s65, s65, 1\n\t" \
    "v_mov_b32 v72, 0\n\t" \
    "v_mov_b32 v73, 0\n\t" \
    "v_mov_b32 v74, 0\n\t" \
    "v_mov_b32 v75, 0\n\t" \
    "v_mov_b32 v76, 0\n\t" \
    "v_mov_b32 v77, 0\n\t" \
    "v_mov_b32 v78, 0\n\t" \
    "v_mov_b32 v79, 0\n\t" \
    "v_mov_b32 v80, 0\n\t" \
    "v_mov_b32 v81, 0\n\t" \
    "v_mov_b32 v82, 0\n\t" \
    "v_mov_b32 v83, 0\n\t" \
    "v_mov_b32 v84, 0\n\t" \
    "v_mov_b32 v85, 0\n\t" \
    "v_mov_b32 v86, 0\n\t" \
    "v_mov_b32 v87, 0\n\t" \
    "s_add_u32 s69, s65, 0\n\t" \
    "s_and_b32 s69, s69, 7\n\t" \
    "s_lshl_b32 s69, s69, 18\n\t" \
    "s_add_u32 s69, s69, s62\n\t" \
    "s_add_u32 s46, s36, s69\n\t" \
    "s_addc_u32 s47, s37, 0\n\t" \
    "s_mul_i32 s70, s67, 7\n\t" \
    "s_add_u32 s69, s65, 0\n\t" \
    "s_xor_b32 s69, s69, s70\n\t" \
    "s_and_b32 s69, s69, 63\n\t" \
    "s_lshl_b32 s69, s69, 12\n\t" \
    "s_mul_i32 s70, s67, 262144\n\t" \
    "s_add_u32 s69, s69, s70\n\t" \
    "s_add_u32 s69, s69, 39845888\n\t" \
    "s_add_u32 s40, s36, s69\n\t" \
    "s_addc_u32 s41, s37, 0\n\t" \
    "s_sub_u32 s91, s65, 1\n\t" \
    "v_mov_b32 v25, s91\n\t" \
    "s_barrier\n\t" \
    "ds_read_b32 v168, v11 offset:20480\n\t" \
    "s_waitcnt lgkmcnt(0)\n\t" \
    "v_readfirstlane_b32 s70, v168\n\t" \
    "ds_write_b32 v12, v31 offset:0\n\t" \
    "ds_write_b32 v12, v31 offset:64\n\t" \
    "s_branch qk_go58_%=\n\t" \
    "qk_Gbad65_%=:\n\t" \
    "s_waitcnt lgkmcnt(0)\n\t" \
    "s_nop 7\n\t" \
    "v_mov_b32 v72, 0\n\t" \
    "v_mov_b32 v73, 0\n\t" \
    "v_mov_b32 v74, 0\n\t" \
    "v_mov_b32 v75, 0\n\t" \
    "v_mov_b32 v76, 0\n\t" \
    "v_mov_b32 v77, 0\n\t" \
    "v_mov_b32 v78, 0\n\t" \
    "v_mov_b32 v79, 0\n\t" \
    "v_mov_b32 v80, 0\n\t" \
    "v_mov_b32 v81, 0\n\t" \
    "v_mov_b32 v82, 0\n\t" \
    "v_mov_b32 v83, 0\n\t" \
    "v_mov_b32 v84, 0\n\t" \
    "v_mov_b32 v85, 0\n\t" \
    "v_mov_b32 v86, 0\n\t" \
    "v_mov_b32 v87, 0\n\t" \
    "s_cmp_eq_u32 s68, 0\n\t" \
    "s_cbranch_scc1 qk_F70_%=\n\t" \
    "s_sub_u32 s68, s68, 1\n\t" \
    "s_branch qk_G64_%=\n\t" \
    "qk_F70_%=:\n\t" \
    "v_mov_b32 v160, 5767424\n\t" \
    "v_mov_b32 v161, 1\n\t" \
    "global_store_dword v160, v161, s[36:37] sc1\n\t" \
    "s_waitcnt vmcnt(0)\n\t" \
    "s_branch qk_Gc66_%=\n\t" \
    "qk_Gslow67_%=:\n\t" \
    "v_max_u32 v160, v36, v37\n\t" \
    "v_max3_u32 v160, v160, v38, v39\n\t" \
    "v_max3_u32 v160, v160, v40, v41\n\t" \
    "v_max3_u32 v160, v160, v42, v43\n\t" \
    "v_max3_u32 v160, v160, v44, v45\n\t" \
    "v_max3_u32 v160, v160, v46, v47\n\t" \
    "v_max3_u32 v160, v160, v48, v49\n\t" \
    "v_max3_u32 v160, v160, v50, v51\n\t" \
    "v_max3_u32 v160, v160, v52, v53\n\t" \
    "v_max3_u32 v160, v160, v54, v55\n\t" \
    "v_max3_u32 v160, v160, v56, v57\n\t" \
    "v_max3_u32 v160, v160, v58, v59\n\t" \
    "v_max3_u32 v160, v160, v60, v61\n\t" \
    "v_max3_u32 v160, v160, v62, v63\n\t" \
    "v_max3_u32 v160, v160, v64, v65\n\t" \
    "v_max3_u32 v160, v160, v66, v67\n\t" \
    "v_max3_u32 v160, v160, v68, v69\n\t" \
    "v_max3_u32 v160, v160, v70, v71\n\t" \
    "v_cmp_eq_u32 vcc, -1, v160\n\t" \
    "s_cbranch_vccz qk_Gback68_%=\n\t" \
    "s_waitcnt lgkmcnt(0)\n\t" \
    "v_sub_u32 v160, v1, v3\n\t" \
    "s_nop 0\n\t" \
    "v_readfirstlane_b32 s69, v160\n\t" \
    "v_add_u32 v163, 20480, v0\n\t" \
    "s_nop 7\n\t" \
    "s_add_u32 s30, s46, s69\n\t" \
    "s_addc_u32 s31, s47, 0\n\t" \
    "qk_Gag71_%=:\n\t" \
    "global_load_dwordx4 v[36:39], v1, s[46:47] sc1\n\t" \
    "global_load_dwordx4 v[40:43], v23, s[46:47] sc1\n\t" \
    "global_load_dwordx4 v[44:47], v163, s[30:31] sc1\n\t" \
    "global_load_dwordx4 v[48:51], v4, s[30:31] sc1\n\t" \
    "global_load_dwordx4 v[52:55], v5, s[30:31] sc1\n\t" \
    "global_load_dwordx4 v[56:59], v6, s[30:31] sc1\n\t" \
    "global_load_dwordx4 v[60:63], v7, s[30:31] sc1\n\t" \
    "global_load_dwordx4 v[64:67], v8, s[30:31] sc1\n\t" \
    "global_load_dwordx4 v[68:71], v9, s[30:31] sc1\n\t" \
    "s_waitcnt vmcnt(0)\n\t" \
    "v_max_u32 v160, v36, v37\n\t" \
    "v_max3_u32 v160, v160, v38, v39\n\t" \
    "v_max3_u32 v160, v160, v40, v41\n\t" \
    "v_max3_u32 v160, v160, v42, v43\n\t" \
    "v_max3_u32 v160, v160, v44, v45\n\t" \
    "v_max3_u32 v160, v160, v46, v47\n\t" \
    "v_max3_u32 v160, v160, v48, v49\n\t" \
    "v_max3_u32 v160, v160, v50, v51\n\t" \
    "v_max3_u32 v160, v160, v52, v53\n\t" \
    "v_max3_u32 v160, v160, v54, v55\n\t" \
    "v_max3_u32 v160, v160, v56, v57\n\t" \
    "v_max3_u32 v160, v160, v58, v59\n\t" \
    "v_max3_u32 v160, v160, v60, v61\n\t" \
    "v_max3_u32 v160, v160, v62, v63\n\t" \
    "v_max3_u32 v160, v160, v64, v65\n\t" \
    "v_max3_u32 v160, v160, v66, v67\n\t" \
    "v_max3_u32 v160, v160, v68, v69\n\t" \
    "v_max3_u32 v160, v160, v70, v71\n\t" \
    "v_cmp_eq_u32 vcc, -1, v160\n\t" \
    "s_cbranch_vccz qk_Gredo72_%=\n\t" \
    "s_cmp_eq_u32 s68, 0\n\t" \
    "s_cbranch_scc1 qk_F73_%=\n\t" \
    "s_sub_u32 s68, s68, 1\n\t" \
    "s_sleep 1\n\t" \
    "s_branch qk_Gag71_%=\n\t" \
    "qk_F73_%=:\n\t" \
    "v_mov_b32 v160, 5767424\n\t" \
    "v_mov_b32 v161, 2\n\t" \
    "global_store_dword v160, v161, s[36:37] sc1\n\t" \
    "s_waitcnt vmcnt(0)\n\t" \
    "s_branch qk_Gredo72_%=\n\t" \
    "qk_Gredo72_%=:\n\t" \
    "v_mov_b32 v72, 0\n\t" \
    "v_mov_b32 v73, 0\n\t" \
    "v_mov_b32 v74, 0\n\t" \
    "v_mov_b32 v75, 0\n\t" \
    "v_mov_b32 v76, 0\n\t" \
    "v_mov_b32 v77, 0\n\t" \
    "v_mov_b32 v78, 0\n\t" \
    "v_mov_b32 v79, 0\n\t" \
    "v_mov_b32 v80, 0\n\t" \
    "v_mov_b32 v81, 0\n\t" \
    "v_mov_b32 v82, 0\n\t" \
    "v_mov_b32 v83, 0\n\t" \
    "v_mov_b32 v84, 0\n\t" \
    "v_mov_b32 v85, 0\n\t" \
    "v_mov_b32 v86, 0\n\t" \
    "v_mov_b32 v87, 0\n\t" \
    "v_smfmac_f32_16x16x64_bf16 v[72:75], v[36:39], a[32:39], v10\n\t" \
    "v_smfmac_f32_16x16x64_bf16 v[72:75], v[40:43], a[40:47], v10\n\t" \
    "v_smfmac_f32_16x16x64_bf16 v[72:75], v[44:47], a[64:71], v10\n\t" \
    "v_smfmac_f32_16x16x64_bf16 v[72:75], v[48:51], a[48:55], v10\n\t" \
    "v_smfmac_f32_16x16x64_bf16 v[72:75], v[52:55], a[56:63], v10\n\t" \
    "v_smfmac_f32_16x16x64_bf16 v[72:75], v[56:59], a[0:7], v10\n\t" \
    "v_smfmac_f32_16x16x64_bf16 v[72:75], v[60:63], a[8:15], v10\n\t" \
    "v_smfmac_f32_16x16x64_bf16 v[72:75], v[64:67], a[16:23], v10\n\t" \
    "v_smfmac_f32_16x16x64_bf16 v[72:75], v[68:71], a[24:31], v10\n\t" \
    "v_smfmac_f32_16x16x64_bf16 v[76:79], v[36:39], a[104:111], v10\n\t" \
    "v_smfmac_f32_16x16x64_bf16 v[76:79], v[40:43], a[112:119], v10\n\t" \
    "v_smfmac_f32_16x16x64_bf16 v[76:79], v[44:47], a[136:143], v10\n\t" \
    "v_smfmac_f32_16x16x64_bf16 v[76:79], v[48:51], a[120:127], v10\n\t" \
    "v_smfmac_f32_16x16x64_bf16 v[76:79], v[52:55], a[128:135], v10\n\t" \
    "v_smfmac_f32_16x16x64_bf16 v[76:79], v[56:59], a[72:79], v10\n\t" \
    "v_smfmac_f32_16x16x64_bf16 v[76:79], v[60:63], a[80:87], v10\n\t" \
    "v_smfmac_f32_16x16x64_bf16 v[76:79], v[64:67], a[88:95], v10\n\t" \
    "v_smfmac_f32_16x16x64_bf16 v[76:79], v[68:71], a[96:103], v10\n\t" \
    "v_smfmac_f32_16x16x64_bf16 v[80:83], v[36:39], a[176:183], v10\n\t" \
    "v_smfmac_f32_16x16x64_bf16 v[80:83], v[40:43], a[184:191], v10\n\t" \
    "v_smfmac_f32_16x16x64_bf16 v[80:83], v[44:47], a[208:215], v10\n\t" \
    "v_smfmac_f32_16x16x64_bf16 v[80:83], v[48:51], a[192:199], v10\n\t" \
    "v_smfmac_f32_16x16x64_bf16 v[80:83], v[52:55], a[200:207], v10\n\t" \
    "v_smfmac_f32_16x16x64_bf16 v[80:83], v[56:59], a[144:151], v10\n\t" \
    "v_smfmac_f32_16x16x64_bf16 v[80:83], v[60:63], a[152:159], v10\n\t" \
    "v_smfmac_f32_16x16x64_bf16 v[80:83], v[64:67], a[160:167], v10\n\t" \
    "v_smfmac_f32_16x16x64_bf16 v[80:83], v[68:71], a[168:175], v10\n\t" \
    "v_smfmac_f32_16x16x64_bf16 v[84:87], v[36:39], a[248:255], v10\n\t" \
    "v_smfmac_f32_16x16x64_bf16 v[84:87], v[40:43], v[224:231], v10\n\t" \
    "v_smfmac_f32_16x16x64_bf16 v[84:87], v[44:47], v[248:255], v10\n\t" \
    "v_smfmac_f32_16x16x64_bf16 v[84:87], v[48:51], v[232:239], v10\n\t" \
    "v_smfmac_f32_16x16x64_bf16 v[84:87], v[52:55], v[240:247], v10\n\t" \
    "v_smfmac_f32_16x16x64_bf16 v[84:87], v[56:59], a[216:223], v10\n\t" \
    "v_smfmac_f32_16x16x64_bf16 v[84:87], v[60:63], a[224:231], v10\n\t" \
    "v_smfmac_f32_16x16x64_bf16 v[84:87], v[64:67], a[232:239], v10\n\t" \
    "v_smfmac_f32_16x16x64_bf16 v[84:87], v[68:71], a[240:247], v10\n\t" \
    "v_add_f32 v72, v72, v74\n\t" \
    "v_add_f32 v73, v73, v75\n\t" \
    "v_cvt_pk_bf16_f32 v156, v72, v73\n\t" \
    "ds_write_b32 v12, v156 offset:0\n\t" \
    "v_add_f32 v76, v76, v78\n\t" \
    "v_add_f32 v77, v77, v79\n\t" \
    "v_cvt_pk_bf16_f32 v157, v76, v77\n\t" \
    "ds_write_b32 v12, v157 offset:64\n\t" \
    "v_add_f32 v80, v80, v82\n\t" \
    "v_add_f32 v81, v81, v83\n\t" \
    "v_cvt_pk_bf16_f32 v158, v80, v81\n\t" \
    "ds_write_b32 v16, v158 offset:0\n\t" \
    "v_pk_add_f32 v[84:85], v[84:85], v[86:87]\n\t" \
    "v_cvt_pk_bf16_f32 v159, v84, v85\n\t" \
    "ds_write_b32 v16, v159 offset:256\n\t" \
    "s_branch qk_Gback68_%=\n\t" \
    "qk_kf59_%=:\n\t" \
    "global_store_dword v14, v25, s[36:37] sc1\n\t" \
    "s_branch qk_kb60_%=\n\t" \
    "qk_last57_%=:\n\t" \
    "s_mov_b64 exec, s[58:59]\n\t" \
    "global_store_dwordx4 v0, v[140:143], s[40:41] offset:0 sc1\n\t" \
    "global_store_dwordx4 v0, v[144:147], s[40:41] offset:1024 sc1\n\t" \
    "global_store_dwordx4 v0, v[148:151], s[40:41] offset:2048 sc1\n\t" \
    "global_store_dwordx4 v0, v[152:155], s[40:41] offset:3072 sc1\n\t" \
    "s_mov_b64 exec, -1\n\t" \
    "s_endpgm\n\t" \
    "qk_loop14_%=:\n\t" \
    "s_mov_b32 s38, 0x22226a00\n\t" \
    "s_mov_b32 s39, 0x2b\n\t" \
    "v_mov_b32 v72, 0\n\t" \
    "v_mov_b32 v73, 0\n\t" \
    "v_mov_b32 v74, 0\n\t" \
    "v_mov_b32 v75, 0\n\t" \
    "v_mov_b32 v76, 0\n\t" \
    "v_mov_b32 v77, 0\n\t" \
    "v_mov_b32 v78, 0\n\t" \
    "v_mov_b32 v79, 0\n\t" \
    "v_mov_b32 v80, 0\n\t" \
    "v_mov_b32 v81, 0\n\t" \
    "v_mov_b32 v82, 0\n\t" \
    "v_mov_b32 v83, 0\n\t" \
    "v_mov_b32 v84, 0\n\t" \
    "v_mov_b32 v85, 0\n\t" \
    "v_mov_b32 v86, 0\n\t" \
    "v_mov_b32 v87, 0\n\t" \
    "s_add_u32 s69, s65, 0\n\t" \
    "s_and_b32 s69, s69, 7\n\t" \
    "s_lshl_b32 s69, s69, 18\n\t" \
    "s_add_u32 s69, s69, s62\n\t" \
    "s_add_u32 s46, s36, s69\n\t" \
    "s_addc_u32 s47, s37, 0\n\t" \
    "s_lshr_b64 s[38:39], s[38:39], 4\n\t" \
    "s_and_b32 s97, s38, 15\n\t" \
    "s_lshl_b32 s97, s97, 6\n\t" \
    "s_and_b32 s69, s65, 7\n\t" \
    "s_lshl_b32 s69, s69, 18\n\t" \
    "s_add_u32 s69, s69, s63\n\t" \
    "s_add_u32 s52, s36, s69\n\t" \
    "s_addc_u32 s53, s37, 0\n\t" \
    "s_sub_u32 s70, s79, s74\n\t" \
    "s_max_i32 s70, s70, 0\n\t" \
    "s_min_i32 s70, s70, 16000\n\t" \
    "s_lshl_b32 s70, s70, 1\n\t" \
    "s_mul_hi_u32 s70, s70, s77\n\t" \
    "s_add_u32 s70, s70, s73\n\t" \
    ".p2align 6\n\t" \
    "s_nop 0\n\t" \
    "s_nop 0\n\t" \
    "s_nop 0\n\t" \
    "s_nop 0\n\t" \
    "s_nop 0\n\t" \
    "s_nop 0\n\t" \
    "qk_go75_%=:\n\t" \
    "s_getpc_b64 s[30:31]\n\t" \
    "qk_Wp77_%=:\n\t" \
    "s_add_u32 s30, s30, qk_We78_%=-qk_Wp77_%=\n\t" \
    "s_addc_u32 s31, s31, 0\n\t" \
    "s_sub_u32 s70, s70, 96\n\t" \
    "qk_W76_%=:\n\t" \
    "s_memtime s[56:57]\n\t" \
    "s_waitcnt lgkmcnt(0)\n\t" \
    "s_sub_u32 s69, s70, s56\n\t" \
    "s_cmp_gt_i32 s69, 80\n\t" \
    "s_cbranch_scc1 qk_W76_%=\n\t" \
    "s_max_i32 s69, s69, 0\n\t" \
    "s_and_b32 s69, s69, -4\n\t" \
    "s_sub_u32 s30, s30, s69\n\t" \
    "s_subb_u32 s31, s31, 0\n\t" \
    "s_setpc_b64 s[30:31]\n\t" \
    "s_nop 0\n\t" \
    "s_nop 0\n\t" \
    "s_nop 0\n\t" \
    "s_nop 0\n\t" \
    "s_nop 0\n\t" \
    "s_nop 0\n\t" \
    "s_nop 0\n\t" \
    "s_nop 0\n\t" \
    "s_nop 0\n\t" \
    "s_nop 0\n\t" \
    "s_nop 0\n\t" \
    "s_nop 0\n\t" \
    "s_nop 0\n\t" \
    "s_nop 0\n\t" \
    "s_nop 0\n\t" \
    "s_nop 0\n\t" \
    "s_nop 0\n\t" \
    "s_nop 0\n\t" \
    "s_nop 0\n\t" \
    "s_nop 0\n\t" \
    "qk_We78_%=:\n\t" \
    "qk_G79_%=:\n\t" \
    "global_load_dwordx4 v[36:39], v1, s[46:47] sc1\n\t" \
    "global_load_dword v161, v2, s[46:47] sc1\n\t" \
    "global_load_dwordx4 v[40:43], v23, s[46:47] sc1\n\t" \
    "s_mul_hi_u32 s69, s84, s78\n\t" \
    "s_add_u32 s33, s74, s69\n\t" \
    "s_add_u32 s33, s33, s97\n\t" \
    "s_lshr_b32 s69, s90, 16\n\t" \
    "s_add_u32 s34, s33, s69\n\t" \
    "s_and_b32 s69, s90, 0xffff\n\t" \
    "s_sub_u32 s35, s80, s74\n\t" \
    "s_sub_u32 s35, s35, s69\n\t" \
    "s_add_u32 s99, s35, 240\n\t" \
    "s_add_u32 s98, s33, 400\n\t" \
    "s_sub_u32 s96, s79, s74\n\t" \
    "s_add_u32 s96, s96, 448\n\t" \
    "s_waitcnt vmcnt(2)\n\t" \
    "ds_write_b128 v3, v[36:39]\n\t" \
    "v_smfmac_f32_16x16x64_bf16 v[72:75], v[36:39], a[48:55], v10\n\t" \
    "s_waitcnt vmcnt(1)\n\t" \
    "ds_write_b32 v22, v161 offset:20224\n\t" \
    "v_smfmac_f32_16x16x64_bf16 v[76:79], v[36:39], a[120:127], v10\n\t" \
    "s_waitcnt vmcnt(0)\n\t" \
    "ds_write_b128 v3, v[40:43] offset:1024\n\t" \
    "v_max3_u32 v160, v36, v40, v161\n\t" \
    "v_cmp_eq_u32 vcc, -1, v160\n\t" \
    "v_smfmac_f32_16x16x64_bf16 v[80:83], v[36:39], a[192:199], v10\n\t" \
    "v_smfmac_f32_16x16x64_bf16 v[84:87], v[36:39], v[232:239], v10\n\t" \
    "v_smfmac_f32_16x16x64_bf16 v[72:75], v[40:43], a[56:63], v10\n\t" \
    "s_cbranch_vccnz qk_Gbad80_%=\n\t" \
    "qk_Gc81_%=:\n\t" \
    "s_waitcnt lgkmcnt(0)\n\t" \
    "s_barrier\n\t" \
    "s_memtime s[56:57]\n\t" \
    "ds_read_b128 v[48:51], v4\n\t" \
    "ds_read_b128 v[52:55], v5\n\t" \
    "v_smfmac_f32_16x16x64_bf16 v[76:79], v[40:43], a[128:135], v10\n\t" \
    "ds_read_b128 v[56:59], v6\n\t" \
    "ds_read_b128 v[60:63], v7\n\t" \
    "v_smfmac_f32_16x16x64_bf16 v[80:83], v[40:43], a[200:207], v10\n\t" \
    "ds_read_b128 v[64:67], v8\n\t" \
    "ds_read_b128 v[68:71], v9\n\t" \
    "v_smfmac_f32_16x16x64_bf16 v[84:87], v[40:43], v[240:247], v10\n\t" \
    "ds_read_b128 v[44:47], v0 offset:19456\n\t" \
    "s_waitcnt lgkmcnt(6)\n\t" \
    "v_smfmac_f32_16x16x64_bf16 v[72:75], v[48:51], a[0:7], v10\n\t" \
    "s_sub_u32 s69, s56, s73\n\t" \
    "s_mul_hi_u32 s69, s69, s78\n\t" \
    "s_max_i32 s69, s69, s96\n\t" \
    "s_waitcnt lgkmcnt(5)\n\t" \
    "v_smfmac_f32_16x16x64_bf16 v[72:75], v[52:55], a[8:15], v10\n\t" \
    "s_cmp_gt_i32 s69, s35\n\t" \
    "s_cselect_b32 s70, s34, s33\n\t" \
    "s_cmp_gt_i32 s69, s99\n\t" \
    "s_waitcnt lgkmcnt(4)\n\t" \
    "v_smfmac_f32_16x16x64_bf16 v[72:75], v[56:59], a[16:23], v10\n\t" \
    "s_cselect_b32 s70, s98, s70\n\t" \
    "s_add_u32 s70, s69, s70\n\t" \
    "v_and_b32_e64 v162, -2, s70\n\t" \
    "s_waitcnt lgkmcnt(3)\n\t" \
    "v_smfmac_f32_16x16x64_bf16 v[72:75], v[60:63], a[24:31], v10\n\t" \
    "global_store_dword v21, v162, s[52:53] sc1\n\t" \
    "s_waitcnt lgkmcnt(2)\n\t" \
    "v_smfmac_f32_16x16x64_bf16 v[72:75], v[64:67], a[32:39], v10\n\t" \
    "s_waitcnt lgkmcnt(1)\n\t" \
    "v_smfmac_f32_16x16x64_bf16 v[72:75], v[68:71], a[40:47], v10\n\t" \
    "s_waitcnt lgkmcnt(0)\n\t" \
    "v_smfmac_f32_16x16x64_bf16 v[72:75], v[44:47], a[64:71], v10\n\t" \
    "s_nop 2\n\t" \
    "v_smfmac_f32_16x16x64_bf16 v[76:79], v[48:51], a[72:79], v10\n\t" \
    "s_nop 2\n\t" \
    "v_smfmac_f32_16x16x64_bf16 v[76:79], v[52:55], a[80:87], v10\n\t" \
    "v_add_f32 v72, v72, v74\n\t" \
    "v_smfmac_f32_16x16x64_bf16 v[76:79], v[56:59], a[88:95], v10\n\t" \
    "v_add_f32 v73, v73, v75\n\t" \
    "v_smfmac_f32_16x16x64_bf16 v[76:79], v[60:63], a[96:103], v10\n\t" \
    "v_cvt_pk_bf16_f32 v156, v72, v73\n\t" \
    "v_smfmac_f32_16x16x64_bf16 v[76:79], v[64:67], a[104:111], v10\n\t" \
    "ds_write_b32 v12, v156 offset:0\n\t" \
    "v_smfmac_f32_16x16x64_bf16 v[76:79], v[68:71], a[112:119], v10\n\t" \
    "v_smfmac_f32_16x16x64_bf16 v[76:79], v[44:47], a[136:143], v10\n\t" \
    "s_nop 2\n\t" \
    "v_smfmac_f32_16x16x64_bf16 v[80:83], v[48:51], a[144:151], v10\n\t" \
    "s_nop 2\n\t" \
    "v_smfmac_f32_16x16x64_bf16 v[80:83], v[52:55], a[152:159], v10\n\t" \
    "v_add_f32 v76, v76, v78\n\t" \
    "v_smfmac_f32_16x16x64_bf16 v[80:83], v[56:59], a[160:167], v10\n\t" \
    "v_add_f32 v77, v77, v79\n\t" \
    "v_smfmac_f32_16x16x64_bf16 v[80:83], v[60:63], a[168:175], v10\n\t" \
    "v_cvt_pk_bf16_f32 v157, v76, v77\n\t" \
    "v_smfmac_f32_16x16x64_bf16 v[80:83], v[64:67], a[176:183], v10\n\t" \
    "ds_write_b32 v12, v157 offset:64\n\t" \
    "v_smfmac_f32_16x16x64_bf16 v[80:83], v[68:71], a[184:191], v10\n\t" \
    "v_smfmac_f32_16x16x64_bf16 v[80:83], v[44:47], a[208:215], v10\n\t" \
    "s_nop 2\n\t" \
    "v_smfmac_f32_16x16x64_bf16 v[84:87], v[48:51], a[216:223], v10\n\t" \
    "s_nop 2\n\t" \
    "v_smfmac_f32_16x16x64_bf16 v[84:87], v[52:55], a[224:231], v10\n\t" \
    "v_add_f32 v80, v80, v82\n\t" \
    "v_smfmac_f32_16x16x64_bf16 v[84:87], v[56:59], a[232:239], v10\n\t" \
    "v_add_f32 v81, v81, v83\n\t" \
    "v_smfmac_f32_16x16x64_bf16 v[84:87], v[60:63], a[240:247], v10\n\t" \
    "v_cvt_pk_bf16_f32 v158, v80, v81\n\t" \
    "v_smfmac_f32_16x16x64_bf16 v[84:87], v[64:67], a[248:255], v10\n\t" \
    "ds_write_b32 v16, v158 offset:0\n\t" \
    "v_smfmac_f32_16x16x64_bf16 v[84:87], v[68:71], v[224:231], v10\n\t" \
    "v_smfmac_f32_16x16x64_bf16 v[84:87], v[44:47], v[248:255], v10\n\t" \
    "s_nop 6\n\t" \
    "v_cmp_u_f32 vcc, v72, v73\n\t" \
    "v_pk_add_f32 v[84:85], v[84:85], v[86:87]\n\t" \
    "v_cvt_pk_bf16_f32 v159, v84, v85\n\t" \
    "ds_write_b32 v16, v159 offset:256\n\t" \
    "s_cbranch_vccnz qk_Gslow82_%=\n\t" \
    "qk_Gback83_%=:\n\t" \
    "s_waitcnt lgkmcnt(0)\n\t" \
    "s_barrier\n\t" \
    "s_cmp_eq_u32 s65, 63\n\t" \
    "s_cbranch_scc1 qk_last74_%=\n\t" \
    "s_cmp_lg_u32 s65, 0\n\t" \
    "s_cbranch_scc1 qk_ng84_%=\n\t" \
    "v_mov_b32 v168, 5838848\n\t" \
    "v_mov_b32 v169, 1\n\t" \
    "s_mov_b64 exec, 1\n\t" \
    "global_atomic_add v170, v168, v169, s[36:37] sc0 sc1\n\t" \
    "s_mov_b64 exec, -1\n\t" \
    "v_mbcnt_lo_u32_b32 v168, -1, 0\n\t" \
    "v_and_b32 v169, 3, v168\n\t" \
    "v_lshlrev_b32 v169, 16, v169\n\t" \
    "v_bfe_u32 v171, v168, 2, 2\n\t" \
    "v_lshl_add_u32 v169, v171, 11, v169\n\t" \
    "v_bfe_u32 v171, v168, 4, 1\n\t" \
    "v_lshl_add_u32 v169, v171, 14, v169\n\t" \
    "v_add_u32 v169, s63, v169\n\t" \
    "s_waitcnt vmcnt(0)\n\t" \
    "v_readfirstlane_b32 s69, v170\n\t" \
    "s_nop 1\n\t" \
    "s_cmp_lg_u32 s69, 126\n\t" \
    "s_cbranch_scc1 qk_go85_%=\n\t" \
    "s_memrealtime s[56:57]\n\t" \
    "s_waitcnt lgkmcnt(0)\n\t" \
    "s_lshl_b32 s69, s56, 4\n\t" \
    "s_add_u32 s69, s69, 2480\n\t" \
    "s_add_u32 s70, s56, 24\n\t" \
    "s_lshr_b32 s70, s70, 4\n\t" \
    "v_lshrrev_b32 v171, 2, v168\n\t" \
    "v_add_u32 v171, s70, v171\n\t" \
    "v_and_b32 v171, 63, v171\n\t" \
    "v_lshlrev_b32 v171, 6, v171\n\t" \
    "v_and_b32 v170, 3, v168\n\t" \
    "v_lshl_add_u32 v171, v170, 12, v171\n\t" \
    "v_add_u32 v171, 5865472, v171\n\t" \
    "v_mov_b32 v168, s69\n\t" \
    "s_mov_b32 exec_hi, 0\n\t" \
    "global_store_dword v171, v168, s[36:37] sc1\n\t" \
    "global_store_dword v169, v168, s[36:37] sc1\n\t" \
    "s_mov_b32 exec_hi, -1\n\t" \
    "qk_go85_%=:\n\t" \
    "v_mov_b32 v21, v33\n\t" \
    "qk_ng84_%=:\n\t" \
    "s_sub_u32 s70, s80, s74\n\t" \
    "s_max_i32 s70, s70, 0\n\t" \
    "s_min_i32 s70, s70, 16000\n\t" \
    "s_lshl_b32 s70, s70, 1\n\t" \
    "s_mul_hi_u32 s70, s70, s77\n\t" \
    "s_add_u32 s70, s70, s73\n\t" \
    "qk_W86_%=:\n\t" \
    "s_memtime s[56:57]\n\t" \
    "s_waitcnt lgkmcnt(0)\n\t" \
    "s_sub_u32 s69, s70, s56\n\t" \
    "s_cmp_gt_i32 s69, 24\n\t" \
    "s_cbranch_scc1 qk_W86_%=\n\t" \
    "global_load_dword v160, v20, s[52:53] sc1\n\t" \
    "s_waitcnt lgkmcnt(0)\n\t" \
    "s_mov_b32 s70, 116\n\t" \
    "qk_E90_%=:\n\t" \
    "s_memtime s[56:57]\n\t" \
    "s_memrealtime s[2:3]\n\t" \
    "s_nop 0\n\t" \
    "s_memrealtime s[4:5]\n\t" \
    "s_nop 0\n\t" \
    "s_memrealtime s[6:7]\n\t" \
    "s_nop 0\n\t" \
    "s_memrealtime s[8:9]\n\t" \
    "s_nop 0\n\t" \
    "s_memrealtime s[10:11]\n\t" \
    "s_nop 0\n\t" \
    "s_memrealtime s[12:13]\n\t" \
    "s_nop 0\n\t" \
    "s_memrealtime s[14:15]\n\t" \
    "s_nop 0\n\t" \
    "s_memrealtime s[16:17]\n\t" \
    "s_nop 0\n\t" \
    "s_memrealtime s[18:19]\n\t" \
    "s_nop 0\n\t" \
    "s_memrealtime s[20:21]\n\t" \
    "s_nop 0\n\t" \
    "s_memrealtime s[22:23]\n\t" \
    "s_nop 0\n\t" \
    "s_memrealtime s[24:25]\n\t" \
    "s_nop 0\n\t" \
    "s_memrealtime s[26:27]\n\t" \
    "s_nop 0\n\t" \
    "s_memrealtime s[28:29]\n\t" \
    "s_memtime s[54:55]\n\t" \
    "s_add_u32 s70, s70, 8\n\t" \
    "s_waitcnt lgkmcnt(0)\n\t" \
    "s_sub_u32 s69, s54, s56\n\t" \
    "s_cmp_gt_u32 s69, s70\n\t" \
    "s_cbranch_scc1 qk_E90_%=\n\t" \
    "s_add_u32 s2, s2, s4\n\t" \
    "s_add_u32 s6, s6, s8\n\t" \
    "s_add_u32 s10, s10, s12\n\t" \
    "s_add_u32 s14, s14, s16\n\t" \
    "s_add_u32 s18, s18, s20\n\t" \
    "s_add_u32 s22, s22, s24\n\t" \
    "s_add_u32 s2, s2, s6\n\t" \
    "s_add_u32 s10, s10, s14\n\t" \
    "s_add_u32 s18, s18, s22\n\t" \
    "s_add_u32 s2, s2, s26\n\t" \
    "s_add_u32 s10, s10, s18\n\t" \
    "s_add_u32 s2, s2, s10\n\t" \
    "s_mul_i32 s70, s28, 13\n\t" \
    "s_sub_u32 s70, s70, s2\n\t" \
    "s_min_u32 s70, s70, 52\n\t" \
    "s_mul_i32 s69, s69, s70\n\t" \
    "s_mul_hi_u32 s69, s69, 76695844\n\t" \
    "s_mov_b32 s75, s73\n\t" \
    "s_mov_b32 s76, s74\n\t" \
    "s_add_u32 s73, s56, s69\n\t" \
    "s_lshl_b32 s74, s28, 4\n\t" \
    "s_sub_u32 s69, s73, s75\n\t" \
    "s_sub_u32 s70, s74, s76\n\t" \
    "s_sub_u32 s71, s70, 1600\n\t" \
    "s_cmp_lt_u32 s71, 400000\n\t" \
    "s_cbranch_scc0 qk_K91_%=\n\t" \
    "v_cvt_f32_u32 v168, s69\n\t" \
    "v_cvt_f32_u32 v169, s70\n\t" \
    "v_rcp_f32 v170, v168\n\t" \
    "v_rcp_f32 v171, v169\n\t" \
    "v_mul_f32 v170, v170, v169\n\t" \
    "v_mul_f32 v171, v171, v168\n\t" \
    "v_mul_f32 v170, 0x4f800000, v170\n\t" \
    "v_mul_f32 v171, 0x4f000000, v171\n\t" \
    "v_cvt_u32_f32 v170, v170\n\t" \
    "v_cvt_u32_f32 v171, v171\n\t" \
    "v_readfirstlane_b32 s69, v170\n\t" \
    "v_readfirstlane_b32 s70, v171\n\t" \
    "s_nop 1\n\t" \
    "s_min_u32 s71, s65, 1\n\t" \
    "s_sub_u32 s69, s69, s78\n\t" \
    "s_ashr_i32 s69, s69, s71\n\t" \
    "s_add_u32 s78, s78, s69\n\t" \
    "s_sub_u32 s70, s70, s77\n\t" \
    "s_ashr_i32 s70, s70, s71\n\t" \
    "s_add_u32 s77, s77, s70\n\t" \
    "qk_K91_%=:\n\t" \
    "s_add_u32 s65, s65, 1\n\t" \
    "v_mov_b32 v72, 0\n\t" \
    "v_mov_b32 v73, 0\n\t" \
    "v_mov_b32 v74, 0\n\t" \
    "v_mov_b32 v75, 0\n\t" \
    "v_mov_b32 v76, 0\n\t" \
    "v_mov_b32 v77, 0\n\t" \
    "v_mov_b32 v78, 0\n\t" \
    "v_mov_b32 v79, 0\n\t" \
    "v_mov_b32 v80, 0\n\t" \
    "v_mov_b32 v81, 0\n\t" \
    "v_mov_b32 v82, 0\n\t" \
    "v_mov_b32 v83, 0\n\t" \
    "v_mov_b32 v84, 0\n\t" \
    "v_mov_b32 v85, 0\n\t" \
    "v_mov_b32 v86, 0\n\t" \
    "v_mov_b32 v87, 0\n\t" \
    "s_add_u32 s69, s65, 0\n\t" \
    "s_and_b32 s69, s69, 7\n\t" \
    "s_lshl_b32 s69, s69, 18\n\t" \
    "s_add_u32 s69, s69, s62\n\t" \
    "s_add_u32 s46, s36, s69\n\t" \
    "s_addc_u32 s47, s37, 0\n\t" \
    "s_lshr_b64 s[38:39], s[38:39], 4\n\t" \
    "s_and_b32 s97, s38, 15\n\t" \
    "s_lshl_b32 s97, s97, 6\n\t" \
    "s_waitcnt vmcnt(0)\n\t" \
    "v_cmp_eq_u32 vcc, -1, v160\n\t" \
    "s_cbranch_vccz qk_Hok88_%=\n\t" \
    "qk_H87_%=:\n\t" \
    "global_load_dword v160, v20, s[52:53] sc1\n\t" \
    "s_waitcnt vmcnt(0)\n\t" \
    "v_cmp_eq_u32 vcc, -1, v160\n\t" \
    "s_cbranch_vccz qk_Hc89_%=\n\t" \
    "s_cmp_eq_u32 s68, 0\n\t" \
    "s_cbranch_scc1 qk_F92_%=\n\t" \
    "s_sub_u32 s68, s68, 1\n\t" \
    "s_sleep 2\n\t" \
    "s_branch qk_H87_%=\n\t" \
    "qk_F92_%=:\n\t" \
    "v_mov_b32 v161, 5767424\n\t" \
    "v_mov_b32 v162, 5\n\t" \
    "global_store_dword v161, v162, s[36:37] sc1\n\t" \
    "s_waitcnt vmcnt(0)\n\t" \
    "s_branch qk_Hc89_%=\n\t" \
    "qk_Hc89_%=:\n\t" \
    "s_memtime s[56:57]\n\t" \
    "s_waitcnt lgkmcnt(0)\n\t" \
    "s_sub_u32 s69, s56, s73\n\t" \
    "s_cmp_lt_u32 s69, 3000\n\t" \
    "s_cbranch_scc1 qk_St93_%=\n\t" \
    "s_waitcnt lgkmcnt(0)\n\t" \
    "s_mov_b32 s70, 116\n\t" \
    "qk_E94_%=:\n\t" \
    "s_memtime s[56:57]\n\t" \
    "s_memrealtime s[2:3]\n\t" \
    "s_nop 0\n\t" \
    "s_memrealtime s[4:5]\n\t" \
    "s_nop 0\n\t" \
    "s_memrealtime s[6:7]\n\t" \
    "s_nop 0\n\t" \
    "s_memrealtime s[8:9]\n\t" \
    "s_nop 0\n\t" \
    "s_memrealtime s[10:11]\n\t" \
    "s_nop 0\n\t" \
    "s_memrealtime s[12:13]\n\t" \
    "s_nop 0\n\t" \
    "s_memrealtime s[14:15]\n\t" \
    "s_nop 0\n\t" \
    "s_memrealtime s[16:17]\n\t" \
    "s_nop 0\n\t" \
    "s_memrealtime s[18:19]\n\t" \
    "s_nop 0\n\t" \
    "s_memrealtime s[20:21]\n\t" \
    "s_nop 0\n\t" \
    "s_memrealtime s[22:23]\n\t" \
    "s_nop 0\n\t" \
    "s_memrealtime s[24:25]\n\t" \
    "s_nop 0\n\t" \
    "s_memrealtime s[26:27]\n\t" \
    "s_nop 0\n\t" \
    "s_memrealtime s[28:29]\n\t" \
    "s_memtime s[54:55]\n\t" \
    "s_add_u32 s70, s70, 8\n\t" \
    "s_waitcnt lgkmcnt(0)\n\t" \
    "s_sub_u32 s69, s54, s56\n\t" \
    "s_cmp_gt_u32 s69, s70\n\t" \
    "s_cbranch_scc1 qk_E94_%=\n\t" \
    "s_add_u32 s2, s2, s4\n\t" \
    "s_add_u32 s6, s6, s8\n\t" \
    "s_add_u32 s10, s10, s12\n\t" \
    "s_add_u32 s14, s14, s16\n\t" \
    "s_add_u32 s18, s18, s20\n\t" \
    "s_add_u32 s22, s22, s24\n\t" \
    "s_add_u32 s2, s2, s6\n\t" \
    "s_add_u32 s10, s10, s14\n\t" \
    "s_add_u32 s18, s18, s22\n\t" \
    "s_add_u32 s2, s2, s26\n\t" \
    "s_add_u32 s10, s10, s18\n\t" \
    "s_add_u32 s2, s2, s10\n\t" \
    "s_mul_i32 s70, s28, 13\n\t" \
    "s_sub_u32 s70, s70, s2\n\t" \
    "s_min_u32 s70, s70, 52\n\t" \
    "s_mul_i32 s69, s69, s70\n\t" \
    "s_mul_hi_u32 s69, s69, 76695844\n\t" \
    "s_mov_b32 s75, s73\n\t" \
    "s_mov_b32 s76, s74\n\t" \
    "s_add_u32 s73, s56, s69\n\t" \
    "s_lshl_b32 s74, s28, 4\n\t" \
    "qk_St93_%=:\n\t" \
    "qk_Hok88_%=:\n\t" \
    "v_max_u32_dpp v160, v160, v160 row_ror:1 row_mask:0xf bank_mask:0xf\n\t" \
    "s_nop 1\n\t" \
    "v_max_u32_dpp v160, v160, v160 row_ror:2 row_mask:0xf bank_mask:0xf\n\t" \
    "s_nop 1\n\t" \
    "v_max_u32_dpp v160, v160, v160 row_ror:4 row_mask:0xf bank_mask:0xf\n\t" \
    "s_nop 1\n\t" \
    "v_max_u32_dpp v160, v160, v160 row_ror:8 row_mask:0xf bank_mask:0xf\n\t" \
    "s_nop 0\n\t" \
    "v_readlane_b32 s69, v160, 0\n\t" \
    "v_readlane_b32 s71, v160, 16\n\t" \
    "s_nop 1\n\t" \
    "s_max_u32 s81, s69, s71\n\t" \
    "s_mul_hi_u32 s69, s85, s78\n\t" \
    "s_add_u32 s79, s81, s69\n\t" \
    "s_add_u32 s79, s79, s87\n\t" \
    "s_cmp_ge_u32 s65, 53\n\t" \
    "s_cselect_b32 s69, 32, 0\n\t" \
    "s_sub_u32 s79, s79, s69\n\t" \
    "s_cmp_lt_u32 s65, 10\n\t" \
    "s_cselect_b32 s69, 64, 0\n\t" \
    "s_add_u32 s79, s79, s69\n\t" \
    "s_add_u32 s80, s79, s88\n\t" \
    "s_sub_u32 s70, s79, s74\n\t" \
    "s_max_i32 s70, s70, 0\n\t" \
    "s_min_i32 s70, s70, 16000\n\t" \
    "s_lshl_b32 s70, s70, 1\n\t" \
    "s_mul_hi_u32 s70, s70, s77\n\t" \
    "s_add_u32 s70, s70, s73\n\t" \
    "v_mov_b32 v168, s70\n\t" \
    "ds_write_b32 v11, v168 offset:20480\n\t" \
    "s_and_b32 s69, s65, 7\n\t" \
    "s_lshl_b32 s69, s69, 18\n\t" \
    "s_add_u32 s69, s69, s63\n\t" \
    "s_add_u32 s52, s36, s69\n\t" \
    "s_addc_u32 s53, s37, 0\n\t" \
    "s_waitcnt lgkmcnt(0)\n\t" \
    "s_barrier\n\t" \
    "ds_write_b32 v12, v31 offset:0\n\t" \
    "ds_write_b32 v12, v31 offset:64\n\t" \
    "s_branch qk_go75_%=\n\t" \
    "qk_Gbad80_%=:\n\t" \
    "s_waitcnt lgkmcnt(0)\n\t" \
    "s_nop 7\n\t" \
    "v_mov_b32 v72, 0\n\t" \
    "v_mov_b32 v73, 0\n\t" \
    "v_mov_b32 v74, 0\n\t" \
    "v_mov_b32 v75, 0\n\t" \
    "v_mov_b32 v76, 0\n\t" \
    "v_mov_b32 v77, 0\n\t" \
    "v_mov_b32 v78, 0\n\t" \
    "v_mov_b32 v79, 0\n\t" \
    "v_mov_b32 v80, 0\n\t" \
    "v_mov_b32 v81, 0\n\t" \
    "v_mov_b32 v82, 0\n\t" \
    "v_mov_b32 v83, 0\n\t" \
    "v_mov_b32 v84, 0\n\t" \
    "v_mov_b32 v85, 0\n\t" \
    "v_mov_b32 v86, 0\n\t" \
    "v_mov_b32 v87, 0\n\t" \
    "s_cmp_eq_u32 s68, 0\n\t" \
    "s_cbranch_scc1 qk_F95_%=\n\t" \
    "s_sub_u32 s68, s68, 1\n\t" \
    "s_branch qk_G79_%=\n\t" \
    "qk_F95_%=:\n\t" \
    "v_mov_b32 v160, 5767424\n\t" \
    "v_mov_b32 v161, 1\n\t" \
    "global_store_dword v160, v161, s[36:37] sc1\n\t" \
    "s_waitcnt vmcnt(0)\n\t" \
    "s_branch qk_Gc81_%=\n\t" \
    "qk_Gslow82_%=:\n\t" \
    "v_max_u32 v160, v36, v37\n\t" \
    "v_max3_u32 v160, v160, v38, v39\n\t" \
    "v_max3_u32 v160, v160, v40, v41\n\t" \
    "v_max3_u32 v160, v160, v42, v43\n\t" \
    "v_max3_u32 v160, v160, v44, v45\n\t" \
    "v_max3_u32 v160, v160, v46, v47\n\t" \
    "v_max3_u32 v160, v160, v48, v49\n\t" \
    "v_max3_u32 v160, v160, v50, v51\n\t" \
    "v_max3_u32 v160, v160, v52, v53\n\t" \
    "v_max3_u32 v160, v160, v54, v55\n\t" \
    "v_max3_u32 v160, v160, v56, v57\n\t" \
    "v_max3_u32 v160, v160, v58, v59\n\t" \
    "v_max3_u32 v160, v160, v60, v61\n\t" \
    "v_max3_u32 v160, v160, v62, v63\n\t" \
    "v_max3_u32 v160, v160, v64, v65\n\t" \
    "v_max3_u32 v160, v160, v66, v67\n\t" \
    "v_max3_u32 v160, v160, v68, v69\n\t" \
    "v_max3_u32 v160, v160, v70, v71\n\t" \
    "v_cmp_eq_u32 vcc, -1, v160\n\t" \
    "s_cbranch_vccz qk_Gback83_%=\n\t" \
    "s_waitcnt lgkmcnt(0)\n\t" \
    "v_sub_u32 v160, v1, v3\n\t" \
    "s_nop 0\n\t" \
    "v_readfirstlane_b32 s69, v160\n\t" \
    "v_add_u32 v163, 20480, v0\n\t" \
    "s_nop 7\n\t" \
    "s_add_u32 s30, s46, s69\n\t" \
    "s_addc_u32 s31, s47, 0\n\t" \
    "qk_Gag96_%=:\n\t" \
    "global_load_dwordx4 v[36:39], v1, s[46:47] sc1\n\t" \
    "global_load_dwordx4 v[40:43], v23, s[46:47] sc1\n\t" \
    "global_load_dwordx4 v[44:47], v163, s[30:31] sc1\n\t" \
    "global_load_dwordx4 v[48:51], v4, s[30:31] sc1\n\t" \
    "global_load_dwordx4 v[52:55], v5, s[30:31] sc1\n\t" \
    "global_load_dwordx4 v[56:59], v6, s[30:31] sc1\n\t" \
    "global_load_dwordx4 v[60:63], v7, s[30:31] sc1\n\t" \
    "global_load_dwordx4 v[64:67], v8, s[30:31] sc1\n\t" \
    "global_load_dwordx4 v[68:71], v9, s[30:31] sc1\n\t" \
    "s_waitcnt vmcnt(0)\n\t" \
    "v_max_u32 v160, v36, v37\n\t" \
    "v_max3_u32 v160, v160, v38, v39\n\t" \
    "v_max3_u32 v160, v160, v40, v41\n\t" \
    "v_max3_u32 v160, v160, v42, v43\n\t" \
    "v_max3_u32 v160, v160, v44, v45\n\t" \
    "v_max3_u32 v160, v160, v46, v47\n\t" \
    "v_max3_u32 v160, v160, v48, v49\n\t" \
    "v_max3_u32 v160, v160, v50, v51\n\t" \
    "v_max3_u32 v160, v160, v52, v53\n\t" \
    "v_max3_u32 v160, v160, v54, v55\n\t" \
    "v_max3_u32 v160, v160, v56, v57\n\t" \
    "v_max3_u32 v160, v160, v58, v59\n\t" \
    "v_max3_u32 v160, v160, v60, v61\n\t" \
    "v_max3_u32 v160, v160, v62, v63\n\t" \
    "v_max3_u32 v160, v160, v64, v65\n\t" \
    "v_max3_u32 v160, v160, v66, v67\n\t" \
    "v_max3_u32 v160, v160, v68, v69\n\t" \
    "v_max3_u32 v160, v160, v70, v71\n\t" \
    "v_cmp_eq_u32 vcc, -1, v160\n\t" \
    "s_cbranch_vccz qk_Gredo97_%=\n\t" \
    "s_cmp_eq_u32 s68, 0\n\t" \
    "s_cbranch_scc1 qk_F98_%=\n\t" \
    "s_sub_u32 s68, s68, 1\n\t" \
    "s_sleep 1\n\t" \
    "s_branch qk_Gag96_%=\n\t" \
    "qk_F98_%=:\n\t" \
    "v_mov_b32 v160, 5767424\n\t" \
    "v_mov_b32 v161, 2\n\t" \
    "global_store_dword v160, v161, s[36:37] sc1\n\t" \
    "s_waitcnt vmcnt(0)\n\t" \
    "s_branch qk_Gredo97_%=\n\t" \
    "qk_Gredo97_%=:\n\t" \
    "v_mov_b32 v72, 0\n\t" \
    "v_mov_b32 v73, 0\n\t" \
    "v_mov_b32 v74, 0\n\t" \
    "v_mov_b32 v75, 0\n\t" \
    "v_mov_b32 v76, 0\n\t" \
    "v_mov_b32 v77, 0\n\t" \
    "v_mov_b32 v78, 0\n\t" \
    "v_mov_b32 v79, 0\n\t" \
    "v_mov_b32 v80, 0\n\t" \
    "v_mov_b32 v81, 0\n\t" \
    "v_mov_b32 v82, 0\n\t" \
    "v_mov_b32 v83, 0\n\t" \
    "v_mov_b32 v84, 0\n\t" \
    "v_mov_b32 v85, 0\n\t" \
    "v_mov_b32 v86, 0\n\t" \
    "v_mov_b32 v87, 0\n\t" \
    "v_smfmac_f32_16x16x64_bf16 v[72:75], v[36:39], a[48:55], v10\n\t" \
    "v_smfmac_f32_16x16x64_bf16 v[72:75], v[40:43], a[56:63], v10\n\t" \
    "v_smfmac_f32_16x16x64_bf16 v[72:75], v[44:47], a[64:71], v10\n\t" \
    "v_smfmac_f32_16x16x64_bf16 v[72:75], v[48:51], a[0:7], v10\n\t" \
    "v_smfmac_f32_16x16x64_bf16 v[72:75], v[52:55], a[8:15], v10\n\t" \
    "v_smfmac_f32_16x16x64_bf16 v[72:75], v[56:59], a[16:23], v10\n\t" \
    "v_smfmac_f32_16x16x64_bf16 v[72:75], v[60:63], a[24:31], v10\n\t" \
    "v_smfmac_f32_16x16x64_bf16 v[72:75], v[64:67], a[32:39], v10\n\t" \
    "v_smfmac_f32_16x16x64_bf16 v[72:75], v[68:71], a[40:47], v10\n\t" \
    "v_smfmac_f32_16x16x64_bf16 v[76:79], v[36:39], a[120:127], v10\n\t" \
    "v_smfmac_f32_16x16x64_bf16 v[76:79], v[40:43], a[128:135], v10\n\t" \
    "v_smfmac_f32_16x16x64_bf16 v[76:79], v[44:47], a[136:143], v10\n\t" \
    "v_smfmac_f32_16x16x64_bf16 v[76:79], v[48:51], a[72:79], v10\n\t" \
    "v_smfmac_f32_16x16x64_bf16 v[76:79], v[52:55], a[80:87], v10\n\t" \
    "v_smfmac_f32_16x16x64_bf16 v[76:79], v[56:59], a[88:95], v10\n\t" \
    "v_smfmac_f32_16x16x64_bf16 v[76:79], v[60:63], a[96:103], v10\n\t" \
    "v_smfmac_f32_16x16x64_bf16 v[76:79], v[64:67], a[104:111], v10\n\t" \
    "v_smfmac_f32_16x16x64_bf16 v[76:79], v[68:71], a[112:119], v10\n\t" \
    "v_smfmac_f32_16x16x64_bf16 v[80:83], v[36:39], a[192:199], v10\n\t" \
    "v_smfmac_f32_16x16x64_bf16 v[80:83], v[40:43], a[200:207], v10\n\t" \
    "v_smfmac_f32_16x16x64_bf16 v[80:83], v[44:47], a[208:215], v10\n\t" \
    "v_smfmac_f32_16x16x64_bf16 v[80:83], v[48:51], a[144:151], v10\n\t" \
    "v_smfmac_f32_16x16x64_bf16 v[80:83], v[52:55], a[152:159], v10\n\t" \
    "v_smfmac_f32_16x16x64_bf16 v[80:83], v[56:59], a[160:167], v10\n\t" \
    "v_smfmac_f32_16x16x64_bf16 v[80:83], v[60:63], a[168:175], v10\n\t" \
    "v_smfmac_f32_16x16x64_bf16 v[80:83], v[64:67], a[176:183], v10\n\t" \
    "v_smfmac_f32_16x16x64_bf16 v[80:83], v[68:71], a[184:191], v10\n\t" \
    "v_smfmac_f32_16x16x64_bf16 v[84:87], v[36:39], v[232:239], v10\n\t" \
    "v_smfmac_f32_16x16x64_bf16 v[84:87], v[40:43], v[240:247], v10\n\t" \
    "v_smfmac_f32_16x16x64_bf16 v[84:87], v[44:47], v[248:255], v10\n\t" \
    "v_smfmac_f32_16x16x64_bf16 v[84:87], v[48:51], a[216:223], v10\n\t" \
    "v_smfmac_f32_16x16x64_bf16 v[84:87], v[52:55], a[224:231], v10\n\t" \
    "v_smfmac_f32_16x16x64_bf16 v[84:87], v[56:59], a[232:239], v10\n\t" \
    "v_smfmac_f32_16x16x64_bf16 v[84:87], v[60:63], a[240:247], v10\n\t" \
    "v_smfmac_f32_16x16x64_bf16 v[84:87], v[64:67], a[248:255], v10\n\t" \
    "v_smfmac_f32_16x16x64_bf16 v[84:87], v[68:71], v[224:231], v10\n\t" \
    "v_add_f32 v72, v72, v74\n\t" \
    "v_add_f32 v73, v73, v75\n\t" \
    "v_cvt_pk_bf16_f32 v156, v72, v73\n\t" \
    "ds_write_b32 v12, v156 offset:0\n\t" \
    "v_add_f32 v76, v76, v78\n\t" \
    "v_add_f32 v77, v77, v79\n\t" \
    "v_cvt_pk_bf16_f32 v157, v76, v77\n\t" \
    "ds_write_b32 v12, v157 offset:64\n\t" \
    "v_add_f32 v80, v80, v82\n\t" \
    "v_add_f32 v81, v81, v83\n\t" \
    "v_cvt_pk_bf16_f32 v158, v80, v81\n\t" \
    "ds_write_b32 v16, v158 offset:0\n\t" \
    "v_pk_add_f32 v[84:85], v[84:85], v[86:87]\n\t" \
    "v_cvt_pk_bf16_f32 v159, v84, v85\n\t" \
    "ds_write_b32 v16, v159 offset:256\n\t" \
    "s_branch qk_Gback83_%=\n\t" \
    "qk_last74_%=:\n\t" \
    "s_endpgm\n\t" \
    "qk_last_%=:\n\t" \
    ""
#define ASM_V \
    "s_load_dwordx2 s[42:43], %3, 56\n\t" \
    "s_load_dwordx2 s[44:45], %3, 64\n\t" \
    "s_load_dwordx2 s[36:37], %3, 0x58\n\t" \
    "s_load_dwordx2 s[50:51], %3, 0x48\n\t" \
    "s_load_dwordx8 s[84:91], %3, 0\n\t" \
    "s_mov_b32 s66, %4\n\t" \
    "s_mov_b32 s67, %5\n\t" \
    "s_mov_b32 s68, 262144\n\t" \
    "s_mov_b32 s72, 0\n\t" \
    "s_mov_b32 s83, -1\n\t" \
    "s_mov_b32 s65, 0\n\t" \
    "s_lshl_b32 s82, s66, 10\n\t" \
    "s_waitcnt lgkmcnt(0)\n\t" \
    "s_add_u32 s46, s42, 2097152\n\t" \
    "s_addc_u32 s47, s43, 0\n\t" \
    "s_add_u32 m0, s82, 65536\n\t" \
    "s_nop 0\n\t" \
    "global_load_lds_dwordx4 v200, s[46:47]\n\t" \
    "s_add_u32 s46, s42, 2228224\n\t" \
    "s_addc_u32 s47, s43, 0\n\t" \
    "s_add_u32 m0, s82, 69632\n\t" \
    "s_nop 0\n\t" \
    "global_load_lds_dwordx4 v200, s[46:47]\n\t" \
    "s_add_u32 s46, s42, 2359296\n\t" \
    "s_addc_u32 s47, s43, 0\n\t" \
    "s_add_u32 m0, s82, 73728\n\t" \
    "s_nop 0\n\t" \
    "global_load_lds_dwordx4 v200, s[46:47]\n\t" \
    "s_add_u32 s46, s42, 2490368\n\t" \
    "s_addc_u32 s47, s43, 0\n\t" \
    "s_add_u32 m0, s82, 77824\n\t" \
    "s_nop 0\n\t" \
    "global_load_lds_dwordx4 v200, s[46:47]\n\t" \
    "s_add_u32 s46, s42, 2621440\n\t" \
    "s_addc_u32 s47, s43, 0\n\t" \
    "s_add_u32 m0, s82, 81920\n\t" \
    "s_nop 0\n\t" \
    "global_load_lds_dwordx4 v200, s[46:47]\n\t" \
    "s_add_u32 s46, s42, 2752512\n\t" \
    "s_addc_u32 s47, s43, 0\n\t" \
    "s_add_u32 m0, s82, 86016\n\t" \
    "s_nop 0\n\t" \
    "global_load_lds_dwordx4 v200, s[46:47]\n\t" \
    "s_add_u32 s46, s42, 2883584\n\t" \
    "s_addc_u32 s47, s43, 0\n\t" \
    "s_add_u32 m0, s82, 90112\n\t" \
    "s_nop 0\n\t" \
    "global_load_lds_dwordx4 v200, s[46:47]\n\t" \
    "s_add_u32 s46, s42, 3014656\n\t" \
    "s_addc_u32 s47, s43, 0\n\t" \
    "s_add_u32 m0, s82, 94208\n\t" \
    "s_nop 0\n\t" \
    "global_load_lds_dwordx4 v200, s[46:47]\n\t" \
    "s_add_u32 s46, s42, 3145728\n\t" \
    "s_addc_u32 s47, s43, 0\n\t" \
    "s_add_u32 m0, s82, 98304\n\t" \
    "s_nop 0\n\t" \
    "global_load_lds_dwordx4 v200, s[46:47]\n\t" \
    "s_add_u32 s46, s42, 3276800\n\t" \
    "s_addc_u32 s47, s43, 0\n\t" \
    "s_add_u32 m0, s82, 102400\n\t" \
    "s_nop 0\n\t" \
    "global_load_lds_dwordx4 v200, s[46:47]\n\t" \
    "s_add_u32 s46, s42, 3407872\n\t" \
    "s_addc_u32 s47, s43, 0\n\t" \
    "s_add_u32 m0, s82, 106496\n\t" \
    "s_nop 0\n\t" \
    "global_load_lds_dwordx4 v200, s[46:47]\n\t" \
    "s_add_u32 s46, s42, 3538944\n\t" \
    "s_addc_u32 s47, s43, 0\n\t" \
    "s_add_u32 m0, s82, 110592\n\t" \
    "s_nop 0\n\t" \
    "global_load_lds_dwordx4 v200, s[46:47]\n\t" \
    "s_add_u32 s46, s42, 3670016\n\t" \
    "s_addc_u32 s47, s43, 0\n\t" \
    "s_add_u32 m0, s82, 114688\n\t" \
    "s_nop 0\n\t" \
    "global_load_lds_dwordx4 v200, s[46:47]\n\t" \
    "s_add_u32 s46, s42, 3801088\n\t" \
    "s_addc_u32 s47, s43, 0\n\t" \
    "s_add_u32 m0, s82, 118784\n\t" \
    "s_nop 0\n\t" \
    "global_load_lds_dwordx4 v200, s[46:47]\n\t" \
    "s_add_u32 s46, s42, 3932160\n\t" \
    "s_addc_u32 s47, s43, 0\n\t" \
    "s_add_u32 m0, s82, 122880\n\t" \
    "s_nop 0\n\t" \
    "global_load_lds_dwordx4 v200, s[46:47]\n\t" \
    "s_add_u32 s46, s42, 4063232\n\t" \
    "s_addc_u32 s47, s43, 0\n\t" \
    "s_add_u32 m0, s82, 126976\n\t" \
    "s_nop 0\n\t" \
    "global_load_lds_dwordx4 v200, s[46:47]\n\t" \
    "s_add_u32 s46, s42, 4194304\n\t" \
    "s_addc_u32 s47, s43, 0\n\t" \
    "s_add_u32 m0, s82, 131072\n\t" \
    "s_nop 0\n\t" \
    "global_load_lds_dwordx4 v200, s[46:47]\n\t" \
    "s_add_u32 s46, s42, 4325376\n\t" \
    "s_addc_u32 s47, s43, 0\n\t" \
    "s_add_u32 m0, s82, 135168\n\t" \
    "s_nop 0\n\t" \
    "global_load_lds_dwordx4 v200, s[46:47]\n\t" \
    "s_add_u32 s46, s42, 4456448\n\t" \
    "s_addc_u32 s47, s43, 0\n\t" \
    "s_add_u32 m0, s82, 139264\n\t" \
    "s_nop 0\n\t" \
    "global_load_lds_dwordx4 v200, s[46:47]\n\t" \
    "s_add_u32 s46, s42, 4587520\n\t" \
    "s_addc_u32 s47, s43, 0\n\t" \
    "s_add_u32 m0, s82, 143360\n\t" \
    "s_nop 0\n\t" \
    "global_load_lds_dwordx4 v200, s[46:47]\n\t" \
    "s_mov_b32 s77, 3019898880\n\t" \
    "s_mov_b32 s78, 3054198966\n\t" \
    "s_waitcnt lgkmcnt(0)\n\t" \
    "s_mov_b32 s70, 116\n\t" \
    "vv_E1_%=:\n\t" \
    "s_memtime s[56:57]\n\t" \
    "s_memrealtime s[2:3]\n\t" \
    "s_nop 0\n\t" \
    "s_memrealtime s[4:5]\n\t" \
    "s_nop 0\n\t" \
    "s_memrealtime s[6:7]\n\t" \
    "s_nop 0\n\t" \
    "s_memrealtime s[8:9]\n\t" \
    "s_nop 0\n\t" \
    "s_memrealtime s[10:11]\n\t" \
    "s_nop 0\n\t" \
    "s_memrealtime s[12:13]\n\t" \
    "s_nop 0\n\t" \
    "s_memrealtime s[14:15]\n\t" \
    "s_nop 0\n\t" \
    "s_memrealtime s[16:17]\n\t" \
    "s_nop 0\n\t" \
    "s_memrealtime s[18:19]\n\t" \
    "s_nop 0\n\t" \
    "s_memrealtime s[20:21]\n\t" \
    "s_nop 0\n\t" \
    "s_memrealtime s[22:23]\n\t" \
    "s_nop 0\n\t" \
    "s_memrealtime s[24:25]\n\t" \
    "s_nop 0\n\t" \
    "s_memrealtime s[26:27]\n\t" \
    "s_nop 0\n\t" \
    "s_memrealtime s[28:29]\n\t" \
    "s_memtime s[54:55]\n\t" \
    "s_add_u32 s70, s70, 8\n\t" \
    "s_waitcnt lgkmcnt(0)\n\t" \
    "s_sub_u32 s69, s54, s56\n\t" \
    "s_cmp_gt_u32 s69, s70\n\t" \
    "s_cbranch_scc1 vv_E1_%=\n\t" \
    "s_add_u32 s2, s2, s4\n\t" \
    "s_add_u32 s6, s6, s8\n\t" \
    "s_add_u32 s10, s10, s12\n\t" \
    "s_add_u32 s14, s14, s16\n\t" \
    "s_add_u32 s18, s18, s20\n\t" \
    "s_add_u32 s22, s22, s24\n\t" \
    "s_add_u32 s2, s2, s6\n\t" \
    "s_add_u32 s10, s10, s14\n\t" \
    "s_add_u32 s18, s18, s22\n\t" \
    "s_add_u32 s2, s2, s26\n\t" \
    "s_add_u32 s10, s10, s18\n\t" \
    "s_add_u32 s2, s2, s10\n\t" \
    "s_mul_i32 s70, s28, 13\n\t" \
    "s_sub_u32 s70, s70, s2\n\t" \
    "s_min_u32 s70, s70, 52\n\t" \
    "s_mul_i32 s69, s69, s70\n\t" \
    "s_mul_hi_u32 s69, s69, 76695844\n\t" \
    "s_mov_b32 s75, s73\n\t" \
    "s_mov_b32 s76, s74\n\t" \
    "s_add_u32 s73, s56, s69\n\t" \
    "s_lshl_b32 s74, s28, 4\n\t" \
    "s_waitcnt vmcnt(32) lgkmcnt(0)\n\t" \
    "s_barrier\n\t" \
    "ds_read_b64_tr_b16 a[0:1], v201 offset:0\n\t" \
    "ds_read_b64_tr_b16 a[2:3], v201 offset:1024\n\t" \
    "ds_read_b64_tr_b16 a[4:5], v201 offset:8192\n\t" \
    "ds_read_b64_tr_b16 a[6:7], v201 offset:9216\n\t" \
    "ds_read_b64_tr_b16 a[72:73], v202 offset:0\n\t" \
    "ds_read_b64_tr_b16 a[74:75], v202 offset:1024\n\t" \
    "ds_read_b64_tr_b16 a[76:77], v202 offset:8192\n\t" \
    "ds_read_b64_tr_b16 a[78:79], v202 offset:9216\n\t" \
    "s_waitcnt vmcnt(28) lgkmcnt(0)\n\t" \
    "s_barrier\n\t" \
    "ds_read_b64_tr_b16 a[8:9], v201 offset:16384\n\t" \
    "ds_read_b64_tr_b16 a[10:11], v201 offset:17408\n\t" \
    "ds_read_b64_tr_b16 a[12:13], v201 offset:24576\n\t" \
    "ds_read_b64_tr_b16 a[14:15], v201 offset:25600\n\t" \
    "ds_read_b64_tr_b16 a[80:81], v202 offset:16384\n\t" \
    "ds_read_b64_tr_b16 a[82:83], v202 offset:17408\n\t" \
    "ds_read_b64_tr_b16 a[84:85], v202 offset:24576\n\t" \
    "ds_read_b64_tr_b16 a[86:87], v202 offset:25600\n\t" \
    "s_waitcnt vmcnt(24) lgkmcnt(0)\n\t" \
    "s_barrier\n\t" \
    "ds_read_b64_tr_b16 a[16:17], v201 offset:32768\n\t" \
    "ds_read_b64_tr_b16 a[18:19], v201 offset:33792\n\t" \
    "ds_read_b64_tr_b16 a[20:21], v201 offset:40960\n\t" \
    "ds_read_b64_tr_b16 a[22:23], v201 offset:41984\n\t" \
    "ds_read_b64_tr_b16 a[88:89], v202 offset:32768\n\t" \
    "ds_read_b64_tr_b16 a[90:91], v202 offset:33792\n\t" \
    "ds_read_b64_tr_b16 a[92:93], v202 offset:40960\n\t" \
    "ds_read_b64_tr_b16 a[94:95], v202 offset:41984\n\t" \
    "s_waitcnt vmcnt(20) lgkmcnt(0)\n\t" \
    "s_barrier\n\t" \
    "ds_read_b64_tr_b16 a[24:25], v201 offset:49152\n\t" \
    "ds_read_b64_tr_b16 a[26:27], v201 offset:50176\n\t" \
    "ds_read_b64_tr_b16 a[28:29], v201 offset:57344\n\t" \
    "ds_read_b64_tr_b16 a[30:31], v201 offset:58368\n\t" \
    "ds_read_b64_tr_b16 a[96:97], v202 offset:49152\n\t" \
    "ds_read_b64_tr_b16 a[98:99], v202 offset:50176\n\t" \
    "ds_read_b64_tr_b16 a[100:101], v202 offset:57344\n\t" \
    "ds_read_b64_tr_b16 a[102:103], v202 offset:58368\n\t" \
    "s_add_u32 s46, s44, 0\n\t" \
    "s_addc_u32 s47, s45, 0\n\t" \
    "s_mul_i32 s69, s82, 9\n\t" \
    "s_add_u32 m0, s69, 0\n\t" \
    "s_nop 0\n\t" \
    "global_load_lds_dwordx4 v203, s[46:47]\n\t" \
    "s_add_u32 m0, s69, 1024\n\t" \
    "s_nop 0\n\t" \
    "global_load_lds_dwordx4 v204, s[46:47]\n\t" \
    "s_add_u32 m0, s69, 2048\n\t" \
    "s_nop 0\n\t" \
    "global_load_lds_dwordx4 v205, s[46:47]\n\t" \
    "s_add_u32 m0, s69, 3072\n\t" \
    "s_nop 0\n\t" \
    "global_load_lds_dwordx4 v206, s[46:47]\n\t" \
    "s_add_u32 m0, s69, 4096\n\t" \
    "s_nop 0\n\t" \
    "global_load_lds_dwordx4 v207, s[46:47]\n\t" \
    "s_add_u32 m0, s69, 5120\n\t" \
    "s_nop 0\n\t" \
    "global_load_lds_dwordx4 v208, s[46:47]\n\t" \
    "s_add_u32 m0, s69, 6144\n\t" \
    "s_nop 0\n\t" \
    "global_load_lds_dwordx4 v209, s[46:47]\n\t" \
    "s_add_u32 m0, s69, 7168\n\t" \
    "s_nop 0\n\t" \
    "global_load_lds_dwordx4 v210, s[46:47]\n\t" \
    "s_add_u32 m0, s69, 8192\n\t" \
    "s_nop 0\n\t" \
    "global_load_lds_dwordx4 v211, s[46:47]\n\t" \
    "s_waitcnt vmcnt(25) lgkmcnt(0)\n\t" \
    "s_barrier\n\t" \
    "v_add_u32 v201, 0x10000, v201\n\t" \
    "v_add_u32 v202, 0x10000, v202\n\t" \
    "ds_read_b64_tr_b16 a[32:33], v201 offset:0\n\t" \
    "ds_read_b64_tr_b16 a[34:35], v201 offset:1024\n\t" \
    "ds_read_b64_tr_b16 a[36:37], v201 offset:8192\n\t" \
    "ds_read_b64_tr_b16 a[38:39], v201 offset:9216\n\t" \
    "ds_read_b64_tr_b16 a[104:105], v202 offset:0\n\t" \
    "ds_read_b64_tr_b16 a[106:107], v202 offset:1024\n\t" \
    "ds_read_b64_tr_b16 a[108:109], v202 offset:8192\n\t" \
    "ds_read_b64_tr_b16 a[110:111], v202 offset:9216\n\t" \
    "s_waitcnt vmcnt(21) lgkmcnt(0)\n\t" \
    "s_barrier\n\t" \
    "ds_read_b64_tr_b16 a[40:41], v201 offset:16384\n\t" \
    "ds_read_b64_tr_b16 a[42:43], v201 offset:17408\n\t" \
    "ds_read_b64_tr_b16 a[44:45], v201 offset:24576\n\t" \
    "ds_read_b64_tr_b16 a[46:47], v201 offset:25600\n\t" \
    "ds_read_b64_tr_b16 a[112:113], v202 offset:16384\n\t" \
    "ds_read_b64_tr_b16 a[114:115], v202 offset:17408\n\t" \
    "ds_read_b64_tr_b16 a[116:117], v202 offset:24576\n\t" \
    "ds_read_b64_tr_b16 a[118:119], v202 offset:25600\n\t" \
    "s_add_u32 s46, s44, 36864\n\t" \
    "s_addc_u32 s47, s45, 0\n\t" \
    "s_mul_i32 s69, s82, 9\n\t" \
    "s_add_u32 m0, s69, 36864\n\t" \
    "s_nop 0\n\t" \
    "global_load_lds_dwordx4 v203, s[46:47]\n\t" \
    "s_add_u32 m0, s69, 37888\n\t" \
    "s_nop 0\n\t" \
    "global_load_lds_dwordx4 v204, s[46:47]\n\t" \
    "s_add_u32 m0, s69, 38912\n\t" \
    "s_nop 0\n\t" \
    "global_load_lds_dwordx4 v205, s[46:47]\n\t" \
    "s_add_u32 m0, s69, 39936\n\t" \
    "s_nop 0\n\t" \
    "global_load_lds_dwordx4 v206, s[46:47]\n\t" \
    "s_add_u32 m0, s69, 40960\n\t" \
    "s_nop 0\n\t" \
    "global_load_lds_dwordx4 v207, s[46:47]\n\t" \
    "s_add_u32 m0, s69, 41984\n\t" \
    "s_nop 0\n\t" \
    "global_load_lds_dwordx4 v208, s[46:47]\n\t" \
    "s_add_u32 m0, s69, 43008\n\t" \
    "s_nop 0\n\t" \
    "global_load_lds_dwordx4 v209, s[46:47]\n\t" \
    "s_add_u32 m0, s69, 44032\n\t" \
    "s_nop 0\n\t" \
    "global_load_lds_dwordx4 v210, s[46:47]\n\t" \
    "s_add_u32 m0, s69, 45056\n\t" \
    "s_nop 0\n\t" \
    "global_load_lds_dwordx4 v211, s[46:47]\n\t" \
    "s_waitcnt vmcnt(26) lgkmcnt(0)\n\t" \
    "s_barrier\n\t" \
    "ds_read_b64_tr_b16 a[48:49], v201 offset:32768\n\t" \
    "ds_read_b64_tr_b16 a[50:51], v201 offset:33792\n\t" \
    "ds_read_b64_tr_b16 a[52:53], v201 offset:40960\n\t" \
    "ds_read_b64_tr_b16 a[54:55], v201 offset:41984\n\t" \
    "ds_read_b64_tr_b16 a[120:121], v202 offset:32768\n\t" \
    "ds_read_b64_tr_b16 a[122:123], v202 offset:33792\n\t" \
    "ds_read_b64_tr_b16 a[124:125], v202 offset:40960\n\t" \
    "ds_read_b64_tr_b16 a[126:127], v202 offset:41984\n\t" \
    "s_waitcnt vmcnt(22) lgkmcnt(0)\n\t" \
    "s_barrier\n\t" \
    "ds_read_b64_tr_b16 a[56:57], v201 offset:49152\n\t" \
    "ds_read_b64_tr_b16 a[58:59], v201 offset:50176\n\t" \
    "ds_read_b64_tr_b16 a[60:61], v201 offset:57344\n\t" \
    "ds_read_b64_tr_b16 a[62:63], v201 offset:58368\n\t" \
    "ds_read_b64_tr_b16 a[128:129], v202 offset:49152\n\t" \
    "ds_read_b64_tr_b16 a[130:131], v202 offset:50176\n\t" \
    "ds_read_b64_tr_b16 a[132:133], v202 offset:57344\n\t" \
    "ds_read_b64_tr_b16 a[134:135], v202 offset:58368\n\t" \
    "s_add_u32 s46, s44, 73728\n\t" \
    "s_addc_u32 s47, s45, 0\n\t" \
    "s_mul_i32 s69, s82, 9\n\t" \
    "s_add_u32 m0, s69, 73728\n\t" \
    "s_nop 0\n\t" \
    "global_load_lds_dwordx4 v203, s[46:47]\n\t" \
    "s_add_u32 m0, s69, 74752\n\t" \
    "s_nop 0\n\t" \
    "global_load_lds_dwordx4 v204, s[46:47]\n\t" \
    "s_add_u32 m0, s69, 75776\n\t" \
    "s_nop 0\n\t" \
    "global_load_lds_dwordx4 v205, s[46:47]\n\t" \
    "s_add_u32 m0, s69, 76800\n\t" \
    "s_nop 0\n\t" \
    "global_load_lds_dwordx4 v206, s[46:47]\n\t" \
    "s_add_u32 m0, s69, 77824\n\t" \
    "s_nop 0\n\t" \
    "global_load_lds_dwordx4 v207, s[46:47]\n\t" \
    "s_add_u32 m0, s69, 78848\n\t" \
    "s_nop 0\n\t" \
    "global_load_lds_dwordx4 v208, s[46:47]\n\t" \
    "s_add_u32 m0, s69, 79872\n\t" \
    "s_nop 0\n\t" \
    "global_load_lds_dwordx4 v209, s[46:47]\n\t" \
    "s_add_u32 m0, s69, 80896\n\t" \
    "s_nop 0\n\t" \
    "global_load_lds_dwordx4 v210, s[46:47]\n\t" \
    "s_add_u32 m0, s69, 81920\n\t" \
    "s_nop 0\n\t" \
    "global_load_lds_dwordx4 v211, s[46:47]\n\t" \
    "v_mbcnt_lo_u32_b32 v140, -1, 0\n\t" \
    "v_mbcnt_hi_u32_b32 v140, -1, v140\n\t" \
    "v_lshlrev_b32 v140, 6, v140\n\t" \
    "v_add_u32 v140, 6225920, v140\n\t" \
    "v_mov_b32 v141, 6234112\n\t" \
    "global_load_dword v141, v141, s[36:37] sc1\n\t" \
    "global_load_dwordx2 v[142:143], v140, s[36:37] sc1\n\t" \
    "global_load_dword v144, v140, s[36:37] offset:2048 sc1\n\t" \
    "s_waitcnt vmcnt(30) lgkmcnt(0)\n\t" \
    "s_barrier\n\t" \
    "v_add_u32 v201, 0x10000, v201\n\t" \
    "v_add_u32 v202, 0x10000, v202\n\t" \
    "ds_read_b64_tr_b16 a[64:65], v201 offset:0\n\t" \
    "ds_read_b64_tr_b16 a[66:67], v201 offset:1024\n\t" \
    "ds_read_b64_tr_b16 a[68:69], v201 offset:8192\n\t" \
    "ds_read_b64_tr_b16 a[70:71], v201 offset:9216\n\t" \
    "ds_read_b64_tr_b16 a[136:137], v202 offset:0\n\t" \
    "ds_read_b64_tr_b16 a[138:139], v202 offset:1024\n\t" \
    "ds_read_b64_tr_b16 a[140:141], v202 offset:8192\n\t" \
    "ds_read_b64_tr_b16 a[142:143], v202 offset:9216\n\t" \
    "v_add_u32 v201, 0xfffe0000, v201\n\t" \
    "v_add_u32 v202, 0xfffe0000, v202\n\t" \
    "s_waitcnt lgkmcnt(0)\n\t" \
    "s_barrier\n\t" \
    "s_add_u32 s46, s44, 110592\n\t" \
    "s_addc_u32 s47, s45, 0\n\t" \
    "s_mul_i32 s69, s82, 9\n\t" \
    "s_add_u32 m0, s69, 110592\n\t" \
    "s_nop 0\n\t" \
    "global_load_lds_dwordx4 v203, s[46:47]\n\t" \
    "s_add_u32 m0, s69, 111616\n\t" \
    "s_nop 0\n\t" \
    "global_load_lds_dwordx4 v204, s[46:47]\n\t" \
    "s_add_u32 m0, s69, 112640\n\t" \
    "s_nop 0\n\t" \
    "global_load_lds_dwordx4 v205, s[46:47]\n\t" \
    "s_add_u32 m0, s69, 113664\n\t" \
    "s_nop 0\n\t" \
    "global_load_lds_dwordx4 v206, s[46:47]\n\t" \
    "s_add_u32 m0, s69, 114688\n\t" \
    "s_nop 0\n\t" \
    "global_load_lds_dwordx4 v207, s[46:47]\n\t" \
    "s_add_u32 m0, s69, 115712\n\t" \
    "s_nop 0\n\t" \
    "global_load_lds_dwordx4 v208, s[46:47]\n\t" \
    "s_add_u32 m0, s69, 116736\n\t" \
    "s_nop 0\n\t" \
    "global_load_lds_dwordx4 v209, s[46:47]\n\t" \
    "s_add_u32 m0, s69, 117760\n\t" \
    "s_nop 0\n\t" \
    "global_load_lds_dwordx4 v210, s[46:47]\n\t" \
    "s_add_u32 m0, s69, 118784\n\t" \
    "s_nop 0\n\t" \
    "global_load_lds_dwordx4 v211, s[46:47]\n\t" \
    "s_waitcnt vmcnt(21) lgkmcnt(0)\n\t" \
    "s_barrier\n\t" \
    "ds_read_b64_tr_b16 a[144:145], v212 offset:0\n\t" \
    "ds_read_b64_tr_b16 a[146:147], v212 offset:2304\n\t" \
    "ds_read_b64_tr_b16 a[148:149], v212 offset:18432\n\t" \
    "ds_read_b64_tr_b16 a[150:151], v212 offset:20736\n\t" \
    "s_waitcnt lgkmcnt(6)\n\t" \
    "ds_read_b64_tr_b16 a[176:177], v213 offset:0\n\t" \
    "ds_read_b64_tr_b16 a[178:179], v213 offset:2304\n\t" \
    "ds_read_b64_tr_b16 a[180:181], v213 offset:18432\n\t" \
    "ds_read_b64_tr_b16 a[182:183], v213 offset:20736\n\t" \
    "s_waitcnt lgkmcnt(6)\n\t" \
    "ds_read_b64_tr_b16 a[208:209], v212 offset:128\n\t" \
    "ds_read_b64_tr_b16 a[210:211], v212 offset:2432\n\t" \
    "ds_read_b64_tr_b16 a[212:213], v212 offset:18560\n\t" \
    "ds_read_b64_tr_b16 a[214:215], v212 offset:20864\n\t" \
    "s_waitcnt lgkmcnt(6)\n\t" \
    "ds_read_b64_tr_b16 a[240:241], v213 offset:128\n\t" \
    "ds_read_b64_tr_b16 a[242:243], v213 offset:2432\n\t" \
    "ds_read_b64_tr_b16 a[244:245], v213 offset:18560\n\t" \
    "ds_read_b64_tr_b16 a[246:247], v213 offset:20864\n\t" \
    "s_waitcnt lgkmcnt(6)\n\t" \
    "ds_read_b64_tr_b16 a[152:153], v214 offset:0\n\t" \
    "ds_read_b64_tr_b16 a[154:155], v214 offset:2304\n\t" \
    "ds_read_b64_tr_b16 a[156:157], v214 offset:18432\n\t" \
    "ds_read_b64_tr_b16 a[158:159], v214 offset:20736\n\t" \
    "s_waitcnt lgkmcnt(6)\n\t" \
    "ds_read_b64_tr_b16 a[184:185], v215 offset:0\n\t" \
    "ds_read_b64_tr_b16 a[186:187], v215 offset:2304\n\t" \
    "ds_read_b64_tr_b16 a[188:189], v215 offset:18432\n\t" \
    "ds_read_b64_tr_b16 a[190:191], v215 offset:20736\n\t" \
    "s_waitcnt lgkmcnt(6)\n\t" \
    "ds_read_b64_tr_b16 a[216:217], v214 offset:128\n\t" \
    "ds_read_b64_tr_b16 a[218:219], v214 offset:2432\n\t" \
    "ds_read_b64_tr_b16 a[220:221], v214 offset:18560\n\t" \
    "ds_read_b64_tr_b16 a[222:223], v214 offset:20864\n\t" \
    "s_waitcnt lgkmcnt(6)\n\t" \
    "ds_read_b64_tr_b16 a[248:249], v215 offset:128\n\t" \
    "ds_read_b64_tr_b16 a[250:251], v215 offset:2432\n\t" \
    "ds_read_b64_tr_b16 a[252:253], v215 offset:18560\n\t" \
    "ds_read_b64_tr_b16 a[254:255], v215 offset:20864\n\t" \
    "s_waitcnt lgkmcnt(6)\n\t" \
    "ds_read_b64_tr_b16 v[240:241], v24 offset:0\n\t" \
    "ds_read_b64_tr_b16 v[242:243], v24 offset:2304\n\t" \
    "ds_read_b64_tr_b16 v[244:245], v24 offset:18432\n\t" \
    "ds_read_b64_tr_b16 v[246:247], v24 offset:20736\n\t" \
    "v_add_u32 v212, 0x12000, v212\n\t" \
    "v_add_u32 v213, 0x12000, v213\n\t" \
    "v_add_u32 v214, 0x12000, v214\n\t" \
    "v_add_u32 v215, 0x12000, v215\n\t" \
    "v_add_u32 v24, 0x12000, v24\n\t" \
    "s_waitcnt vmcnt(9)\n\t" \
    "vv_C2_%=:\n\t" \
    "v_cmp_ne_u32 s[54:55], v142, v144\n\t" \
    "v_sub_u32 v142, v142, v141\n\t" \
    "v_cmp_ne_u32 vcc, 1, v142\n\t" \
    "s_nop 1\n\t" \
    "s_or_b64 vcc, vcc, s[54:55]\n\t" \
    "s_cbranch_scc0 vv_Cok3_%=\n\t" \
    "s_cmp_eq_u32 s68, 0\n\t" \
    "s_cbranch_scc1 vv_F5_%=\n\t" \
    "s_sub_u32 s68, s68, 1\n\t" \
    "s_sleep 6\n\t" \
    "s_branch vv_Cag4_%=\n\t" \
    "vv_F5_%=:\n\t" \
    "v_mov_b32 v144, 5767424\n\t" \
    "v_mov_b32 v145, 4\n\t" \
    "global_store_dword v144, v145, s[36:37] sc1\n\t" \
    "s_waitcnt vmcnt(0)\n\t" \
    "s_branch vv_Cok3_%=\n\t" \
    "vv_Cag4_%=:\n\t" \
    "global_load_dwordx2 v[142:143], v140, s[36:37] sc1\n\t" \
    "global_load_dword v144, v140, s[36:37] offset:2048 sc1\n\t" \
    "s_waitcnt vmcnt(0)\n\t" \
    "s_branch vv_C2_%=\n\t" \
    "vv_Cok3_%=:\n\t" \
    "s_cmp_lg_u32 s66, 0\n\t" \
    "s_cbranch_scc1 vv_Ca6_%=\n\t" \
    "v_mov_b32 v140, 5836800\n\t" \
    "s_mov_b64 exec, 1\n\t" \
    "global_atomic_add v140, v142, s[36:37] sc1\n\t" \
    "s_mov_b64 exec, -1\n\t" \
    "vv_Ca6_%=:\n\t" \
    "v_mov_b32_dpp v140, v143 quad_perm:[1,0,3,2] row_mask:0xf bank_mask:0xf\n\t" \
    "v_cmp_lt_u32 s[44:45], v140, v143\n\t" \
    "s_nop 1\n\t" \
    "s_bitcmp1_b32 s44, 0\n\t" \
    "s_cselect_b32 s62, 8192, 0\n\t" \
    "s_bitcmp1_b32 s44, 2\n\t" \
    "s_cselect_b32 s63, 8192, 0\n\t" \
    "s_or_b32 s63, s63, 0x200000\n\t" \
    "v_min_u32 v140, v140, v143\n\t" \
    "v_cmp_lt_u32 vcc, 660, v140\n\t" \
    "s_nop 1\n\t" \
    "s_cmp_lg_u64 vcc, 0\n\t" \
    "s_cselect_b32 s69, 1, 0\n\t" \
    "s_mul_i32 s70, s69, 160\n\t" \
    "s_add_u32 s86, s86, s70\n\t" \
    "s_mul_i32 s70, s69, 536\n\t" \
    "s_add_u32 s87, s87, s70\n\t" \
    "s_mul_i32 s70, s69, 256\n\t" \
    "s_add_u32 s88, s88, s70\n\t" \
    "s_waitcnt vmcnt(0) lgkmcnt(0)\n\t" \
    "s_barrier\n\t" \
    "ds_read_b64_tr_b16 a[160:161], v212 offset:0\n\t" \
    "ds_read_b64_tr_b16 a[162:163], v212 offset:2304\n\t" \
    "ds_read_b64_tr_b16 a[164:165], v212 offset:18432\n\t" \
    "ds_read_b64_tr_b16 a[166:167], v212 offset:20736\n\t" \
    "s_waitcnt lgkmcnt(6)\n\t" \
    "ds_read_b64_tr_b16 a[192:193], v213 offset:0\n\t" \
    "ds_read_b64_tr_b16 a[194:195], v213 offset:2304\n\t" \
    "ds_read_b64_tr_b16 a[196:197], v213 offset:18432\n\t" \
    "ds_read_b64_tr_b16 a[198:199], v213 offset:20736\n\t" \
    "s_waitcnt lgkmcnt(6)\n\t" \
    "ds_read_b64_tr_b16 a[224:225], v212 offset:128\n\t" \
    "ds_read_b64_tr_b16 a[226:227], v212 offset:2432\n\t" \
    "ds_read_b64_tr_b16 a[228:229], v212 offset:18560\n\t" \
    "ds_read_b64_tr_b16 a[230:231], v212 offset:20864\n\t" \
    "s_waitcnt lgkmcnt(6)\n\t" \
    "ds_read_b64_tr_b16 v[224:225], v213 offset:128\n\t" \
    "ds_read_b64_tr_b16 v[226:227], v213 offset:2432\n\t" \
    "ds_read_b64_tr_b16 v[228:229], v213 offset:18560\n\t" \
    "ds_read_b64_tr_b16 v[230:231], v213 offset:20864\n\t" \
    "s_waitcnt lgkmcnt(6)\n\t" \
    "ds_read_b64_tr_b16 a[168:169], v214 offset:0\n\t" \
    "ds_read_b64_tr_b16 a[170:171], v214 offset:2304\n\t" \
    "ds_read_b64_tr_b16 a[172:173], v214 offset:18432\n\t" \
    "ds_read_b64_tr_b16 a[174:175], v214 offset:20736\n\t" \
    "s_waitcnt lgkmcnt(6)\n\t" \
    "ds_read_b64_tr_b16 a[200:201], v215 offset:0\n\t" \
    "ds_read_b64_tr_b16 a[202:203], v215 offset:2304\n\t" \
    "ds_read_b64_tr_b16 a[204:205], v215 offset:18432\n\t" \
    "ds_read_b64_tr_b16 a[206:207], v215 offset:20736\n\t" \
    "s_waitcnt lgkmcnt(6)\n\t" \
    "ds_read_b64_tr_b16 a[232:233], v214 offset:128\n\t" \
    "ds_read_b64_tr_b16 a[234:235], v214 offset:2432\n\t" \
    "ds_read_b64_tr_b16 a[236:237], v214 offset:18560\n\t" \
    "ds_read_b64_tr_b16 a[238:239], v214 offset:20864\n\t" \
    "s_waitcnt lgkmcnt(6)\n\t" \
    "ds_read_b64_tr_b16 v[232:233], v215 offset:128\n\t" \
    "ds_read_b64_tr_b16 v[234:235], v215 offset:2432\n\t" \
    "ds_read_b64_tr_b16 v[236:237], v215 offset:18560\n\t" \
    "ds_read_b64_tr_b16 v[238:239], v215 offset:20864\n\t" \
    "s_waitcnt lgkmcnt(6)\n\t" \
    "ds_read_b64_tr_b16 v[248:249], v24 offset:0\n\t" \
    "ds_read_b64_tr_b16 v[250:251], v24 offset:2304\n\t" \
    "ds_read_b64_tr_b16 v[252:253], v24 offset:18432\n\t" \
    "ds_read_b64_tr_b16 v[254:255], v24 offset:20736\n\t" \
    "v_add_u32 v212, 0x12000, v212\n\t" \
    "v_add_u32 v213, 0x12000, v213\n\t" \
    "v_add_u32 v214, 0x12000, v214\n\t" \
    "v_add_u32 v215, 0x12000, v215\n\t" \
    "v_add_u32 v24, 0x12000, v24\n\t" \
    "s_waitcnt lgkmcnt(0)\n\t" \
    "s_barrier\n\t" \
    "s_mov_b32 s79, s74\n\t" \
    "s_sub_u32 s80, s74, 32000\n\t" \
    "v_mov_b32 v72, 0\n\t" \
    "v_mov_b32 v73, 0\n\t" \
    "v_mov_b32 v74, 0\n\t" \
    "v_mov_b32 v75, 0\n\t" \
    "v_mov_b32 v76, 0\n\t" \
    "v_mov_b32 v77, 0\n\t" \
    "v_mov_b32 v78, 0\n\t" \
    "v_mov_b32 v79, 0\n\t" \
    "v_mov_b32 v108, 0\n\t" \
    "v_mov_b32 v109, 0\n\t" \
    "v_mov_b32 v110, 0\n\t" \
    "v_mov_b32 v111, 0\n\t" \
    "v_mov_b32 v112, 0\n\t" \
    "v_mov_b32 v113, 0\n\t" \
    "v_mov_b32 v114, 0\n\t" \
    "v_mov_b32 v115, 0\n\t" \
    "v_mov_b32 v116, 0\n\t" \
    "v_mov_b32 v117, 0\n\t" \
    "v_mov_b32 v118, 0\n\t" \
    "v_mov_b32 v119, 0\n\t" \
    "v_mov_b32 v120, 0\n\t" \
    "v_mov_b32 v121, 0\n\t" \
    "v_mov_b32 v122, 0\n\t" \
    "v_mov_b32 v123, 0\n\t" \
    "v_mov_b32 v124, 0\n\t" \
    "v_mov_b32 v125, 0\n\t" \
    "v_mov_b32 v126, 0\n\t" \
    "v_mov_b32 v127, 0\n\t" \
    "s_add_u32 s69, s65, 0\n\t" \
    "s_and_b32 s69, s69, 7\n\t" \
    "s_lshl_b32 s69, s69, 18\n\t" \
    "s_add_u32 s69, s69, s62\n\t" \
    "s_add_u32 s46, s36, s69\n\t" \
    "s_addc_u32 s47, s37, 0\n\t" \
    "s_add_u32 s70, s65, 0\n\t" \
    "s_mul_i32 s69, s70, 171\n\t" \
    "s_lshr_b32 s69, s69, 9\n\t" \
    "s_mul_i32 s70, s70, 589824\n\t" \
    "s_mul_i32 s71, s69, 327680\n\t" \
    "s_add_u32 s70, s70, s71\n\t" \
    "s_lshl1_add_u32 s69, s69, 4\n\t" \
    "s_bitcmp1_b64 s[44:45], s69\n\t" \
    "s_cselect_b32 s69, 8192, 0\n\t" \
    "s_add_u32 s69, s69, s70\n\t" \
    "s_add_u32 s69, s69, 123731968\n\t" \
    "s_add_u32 s38, s36, s69\n\t" \
    "s_addc_u32 s39, s37, 0\n\t" \
    "s_and_b32 s69, s69, 0xffe00000\n\t" \
    "s_add_u32 s69, s69, 1823744\n\t" \
    "v_mov_b32 v17, s69\n\t" \
    "global_load_dword v17, v17, s[36:37]\n\t" \
    "s_and_b32 s69, s65, 7\n\t" \
    "s_lshl_b32 s69, s69, 18\n\t" \
    "s_add_u32 s69, s69, s63\n\t" \
    "s_add_u32 s52, s36, s69\n\t" \
    "s_addc_u32 s53, s37, 0\n\t" \
    "v_mov_b32 v100, 0\n\t" \
    "v_mov_b32 v101, 0\n\t" \
    "v_mov_b32 v102, 0\n\t" \
    "v_mov_b32 v103, 0\n\t" \
    "s_sub_u32 s70, s79, s74\n\t" \
    "s_max_i32 s70, s70, 0\n\t" \
    "s_min_i32 s70, s70, 16000\n\t" \
    "s_lshl_b32 s70, s70, 1\n\t" \
    "s_mul_hi_u32 s70, s70, s77\n\t" \
    "s_add_u32 s70, s70, s73\n\t" \
    "s_cmp_eq_u32 s66, 1\n\t" \
    "s_cbranch_scc1 vv_loop10_%=\n\t" \
    "s_cmp_eq_u32 s66, 2\n\t" \
    "s_cbranch_scc1 vv_loop11_%=\n\t" \
    "s_cmp_eq_u32 s66, 3\n\t" \
    "s_cbranch_scc1 vv_loop12_%=\n\t" \
    ".p2align 6\n\t" \
    "s_nop 0\n\t" \
    "s_nop 0\n\t" \
    "s_nop 0\n\t" \
    "s_nop 0\n\t" \
    "s_nop 0\n\t" \
    "s_nop 0\n\t" \
    "vv_loop9_%=:\n\t" \
    "s_getpc_b64 s[30:31]\n\t" \
    "vv_Wp15_%=:\n\t" \
    "s_add_u32 s30, s30, vv_We16_%=-vv_Wp15_%=\n\t" \
    "s_addc_u32 s31, s31, 0\n\t" \
    "s_sub_u32 s70, s70, 96\n\t" \
    "vv_W14_%=:\n\t" \
    "s_memtime s[56:57]\n\t" \
    "s_waitcnt lgkmcnt(0)\n\t" \
    "s_sub_u32 s69, s70, s56\n\t" \
    "s_cmp_gt_i32 s69, 80\n\t" \
    "s_cbranch_scc1 vv_W14_%=\n\t" \
    "s_max_i32 s69, s69, 0\n\t" \
    "s_and_b32 s69, s69, -4\n\t" \
    "s_sub_u32 s30, s30, s69\n\t" \
    "s_subb_u32 s31, s31, 0\n\t" \
    "s_setpc_b64 s[30:31]\n\t" \
    "s_nop 0\n\t" \
    "s_nop 0\n\t" \
    "s_nop 0\n\t" \
    "s_nop 0\n\t" \
    "s_nop 0\n\t" \
    "s_nop 0\n\t" \
    "s_nop 0\n\t" \
    "s_nop 0\n\t" \
    "s_nop 0\n\t" \
    "s_nop 0\n\t" \
    "s_nop 0\n\t" \
    "s_nop 0\n\t" \
    "s_nop 0\n\t" \
    "s_nop 0\n\t" \
    "s_nop 0\n\t" \
    "s_nop 0\n\t" \
    "s_nop 0\n\t" \
    "s_nop 0\n\t" \
    "s_nop 0\n\t" \
    "s_nop 0\n\t" \
    "vv_We16_%=:\n\t" \
    "vv_G17_%=:\n\t" \
    "global_load_dwordx4 v[36:39], v1, s[46:47] sc1\n\t" \
    "global_load_dword v141, v2, s[46:47] sc1\n\t" \
    "global_load_dwordx4 v[40:43], v23, s[46:47] sc1\n\t" \
    "s_waitcnt vmcnt(2)\n\t" \
    "ds_write_b128 v3, v[36:39]\n\t" \
    "v_smfmac_f32_16x16x64_bf16 v[72:75], v[36:39], a[0:7], v10\n\t" \
    "s_waitcnt vmcnt(1)\n\t" \
    "ds_write_b32 v22, v141 offset:19456\n\t" \
    "s_waitcnt vmcnt(0)\n\t" \
    "ds_write_b128 v3, v[40:43] offset:1024\n\t" \
    "v_max3_u32 v140, v36, v40, v141\n\t" \
    "v_cmp_eq_u32 vcc, -1, v140\n\t" \
    "s_cbranch_vccnz vv_Gbad18_%=\n\t" \
    "vv_Gc19_%=:\n\t" \
    "s_waitcnt lgkmcnt(0)\n\t" \
    "s_barrier\n\t" \
    "ds_read_b128 v[48:51], v4\n\t" \
    "ds_read_b128 v[52:55], v5\n\t" \
    "v_smfmac_f32_16x16x64_bf16 v[76:79], v[36:39], a[72:79], v10\n\t" \
    "ds_read_b128 v[56:59], v6\n\t" \
    "ds_read_b128 v[60:63], v7\n\t" \
    "v_smfmac_f32_16x16x64_bf16 v[72:75], v[40:43], a[8:15], v10\n\t" \
    "ds_read_b128 v[64:67], v8\n\t" \
    "v_smfmac_f32_16x16x64_bf16 v[76:79], v[40:43], a[80:87], v10\n\t" \
    "ds_read_b128 v[68:71], v9\n\t" \
    "s_waitcnt lgkmcnt(5)\n\t" \
    "v_smfmac_f32_16x16x64_bf16 v[72:75], v[48:51], a[16:23], v10\n\t" \
    "ds_read_b128 v[44:47], v0 offset:19456\n\t" \
    "s_waitcnt lgkmcnt(5)\n\t" \
    "v_smfmac_f32_16x16x64_bf16 v[72:75], v[52:55], a[24:31], v10\n\t" \
    "s_waitcnt lgkmcnt(4)\n\t" \
    "v_smfmac_f32_16x16x64_bf16 v[72:75], v[56:59], a[32:39], v10\n\t" \
    "s_waitcnt lgkmcnt(3)\n\t" \
    "v_smfmac_f32_16x16x64_bf16 v[72:75], v[60:63], a[40:47], v10\n\t" \
    "s_waitcnt lgkmcnt(2)\n\t" \
    "v_smfmac_f32_16x16x64_bf16 v[72:75], v[64:67], a[48:55], v10\n\t" \
    "s_waitcnt lgkmcnt(1)\n\t" \
    "v_smfmac_f32_16x16x64_bf16 v[72:75], v[68:71], a[56:63], v10\n\t" \
    "s_waitcnt lgkmcnt(0)\n\t" \
    "v_smfmac_f32_16x16x64_bf16 v[72:75], v[44:47], a[64:71], v10\n\t" \
    "s_nop 2\n\t" \
    "v_smfmac_f32_16x16x64_bf16 v[76:79], v[48:51], a[88:95], v10\n\t" \
    "s_nop 2\n\t" \
    "v_smfmac_f32_16x16x64_bf16 v[76:79], v[52:55], a[96:103], v10\n\t" \
    "v_add_f32 v72, v72, v74\n\t" \
    "v_smfmac_f32_16x16x64_bf16 v[76:79], v[56:59], a[104:111], v10\n\t" \
    "v_add_f32 v73, v73, v75\n\t" \
    "v_smfmac_f32_16x16x64_bf16 v[76:79], v[60:63], a[112:119], v10\n\t" \
    "v_cvt_pk_bf16_f32 v80, v72, v73\n\t" \
    "v_smfmac_f32_16x16x64_bf16 v[76:79], v[64:67], a[120:127], v10\n\t" \
    "ds_write_b32 v12, v80 offset:0\n\t" \
    "v_smfmac_f32_16x16x64_bf16 v[76:79], v[68:71], a[128:135], v10\n\t" \
    "v_smfmac_f32_16x16x64_bf16 v[76:79], v[44:47], a[136:143], v10\n\t" \
    "s_nop 6\n\t" \
    "v_cmp_u_f32 vcc, v72, v73\n\t" \
    "v_pk_add_f32 v[76:77], v[76:77], v[78:79]\n\t" \
    "v_cvt_pk_bf16_f32 v81, v76, v77\n\t" \
    "ds_write_b32 v12, v81 offset:256\n\t" \
    "s_cbranch_vccnz vv_Gslow20_%=\n\t" \
    "vv_Gback21_%=:\n\t" \
    "s_waitcnt lgkmcnt(0)\n\t" \
    "s_barrier\n\t" \
    "ds_read_b64_tr_b16 v[84:85], v13 offset:0\n\t" \
    "v_smfmac_f32_16x16x64_bf16 v[100:103], v[36:39], a[0:7], v10\n\t" \
    "ds_read_b64_tr_b16 v[86:87], v13 offset:64\n\t" \
    "ds_read_b64_tr_b16 v[92:93], v13 offset:1152\n\t" \
    "v_smfmac_f32_16x16x64_bf16 v[100:103], v[36:39], a[0:7], v10\n\t" \
    "ds_read_b64_tr_b16 v[94:95], v13 offset:1216\n\t" \
    "ds_read_b64_tr_b16 v[88:89], v25 offset:0\n\t" \
    "v_smfmac_f32_16x16x64_bf16 v[100:103], v[36:39], a[0:7], v10\n\t" \
    "ds_read_b64_tr_b16 v[90:91], v25 offset:64\n\t" \
    "s_waitcnt lgkmcnt(4)\n\t" \
    "v_smfmac_f32_16x16x64_bf16 v[124:127], v[84:87], v[240:247], v19\n\t" \
    "ds_read_b64_tr_b16 v[96:97], v25 offset:1152\n\t" \
    "ds_read_b64_tr_b16 v[98:99], v25 offset:1216\n\t" \
    "s_waitcnt lgkmcnt(4)\n\t" \
    "v_smfmac_f32_16x16x64_bf16 v[124:127], v[92:95], v[248:255], v19\n\t" \
    "s_nop 2\n\t" \
    "v_smfmac_f32_16x16x64_bf16 v[108:111], v[84:87], a[144:151], v19\n\t" \
    "s_nop 2\n\t" \
    "v_smfmac_f32_16x16x64_bf16 v[108:111], v[92:95], a[160:167], v19\n\t" \
    "s_nop 1\n\t" \
    "v_permlane32_swap_b32 v124, v126\n\t" \
    "v_smfmac_f32_16x16x64_bf16 v[112:115], v[84:87], a[176:183], v19\n\t" \
    "s_nop 1\n\t" \
    "v_permlane32_swap_b32 v125, v127\n\t" \
    "v_smfmac_f32_16x16x64_bf16 v[112:115], v[92:95], a[192:199], v19\n\t" \
    "s_nop 0\n\t" \
    "v_add_f32 v124, v124, v126\n\t" \
    "s_waitcnt lgkmcnt(2)\n\t" \
    "v_smfmac_f32_16x16x64_bf16 v[108:111], v[88:91], a[152:159], v19\n\t" \
    "s_nop 1\n\t" \
    "v_add_f32 v125, v125, v127\n\t" \
    "v_smfmac_f32_16x16x64_bf16 v[112:115], v[88:91], a[184:191], v19\n\t" \
    "s_nop 0\n\t" \
    "v_cvt_pk_bf16_f32 v136, v124, v125\n\t" \
    "s_waitcnt lgkmcnt(0)\n\t" \
    "v_smfmac_f32_16x16x64_bf16 v[108:111], v[96:99], a[168:175], v19\n\t" \
    "s_nop 1\n\t" \
    "v_cvt_pk_bf16_f32 v137, v124, v125\n\t" \
    "v_smfmac_f32_16x16x64_bf16 v[112:115], v[96:99], a[200:207], v19\n\t" \
    "s_nop 1\n\t" \
    "v_permlane32_swap_b32 v136, v137\n\t" \
    "v_smfmac_f32_16x16x64_bf16 v[116:119], v[84:87], a[208:215], v19\n\t" \
    "s_mov_b32 exec_hi, 0\n\t" \
    "global_store_dwordx2 v16, v[136:137], s[38:39] sc1\n\t" \
    "s_mov_b32 exec_hi, -1\n\t" \
    "v_cvt_pk_bf16_f32 v128, v108, v109\n\t" \
    "v_smfmac_f32_16x16x64_bf16 v[116:119], v[88:91], a[216:223], v19\n\t" \
    "s_nop 1\n\t" \
    "v_cvt_pk_bf16_f32 v129, v110, v111\n\t" \
    "v_smfmac_f32_16x16x64_bf16 v[116:119], v[92:95], a[224:231], v19\n\t" \
    "s_nop 1\n\t" \
    "v_cvt_pk_bf16_f32 v130, v112, v113\n\t" \
    "v_smfmac_f32_16x16x64_bf16 v[116:119], v[96:99], a[232:239], v19\n\t" \
    "s_nop 1\n\t" \
    "v_cvt_pk_bf16_f32 v131, v114, v115\n\t" \
    "v_smfmac_f32_16x16x64_bf16 v[120:123], v[84:87], a[240:247], v19\n\t" \
    "s_nop 1\n\t" \
    "v_permlane16_swap_b32 v128, v130\n\t" \
    "v_smfmac_f32_16x16x64_bf16 v[120:123], v[88:91], a[248:255], v19\n\t" \
    "s_nop 1\n\t" \
    "v_permlane16_swap_b32 v129, v131\n\t" \
    "v_smfmac_f32_16x16x64_bf16 v[120:123], v[92:95], v[224:231], v19\n\t" \
    "s_nop 0\n\t" \
    "global_store_dwordx4 v14, v[128:131], s[38:39] sc1\n\t" \
    "v_cvt_pk_bf16_f32 v132, v116, v117\n\t" \
    "v_smfmac_f32_16x16x64_bf16 v[120:123], v[96:99], v[232:239], v19\n\t" \
    "s_nop 1\n\t" \
    "v_cvt_pk_bf16_f32 v133, v118, v119\n\t" \
    "s_nop 4\n\t" \
    "v_cvt_pk_bf16_f32 v134, v120, v121\n\t" \
    "v_cvt_pk_bf16_f32 v135, v122, v123\n\t" \
    "s_nop 0\n\t" \
    "v_permlane16_swap_b32 v132, v134\n\t" \
    "v_permlane16_swap_b32 v133, v135\n\t" \
    "global_store_dwordx4 v15, v[132:135], s[38:39] sc1\n\t" \
    "s_cmp_eq_u32 s65, 63\n\t" \
    "s_cbranch_scc1 vv_end13_%=\n\t" \
    "s_cmp_lg_u32 s66, 0\n\t" \
    "s_cbranch_scc1 vv_SF22_%=\n\t" \
    "s_lshl_b32 s69, s67, 7\n\t" \
    "s_add_u32 s69, s69, 5804032\n\t" \
    "s_add_u32 s70, s65, -1\n\t" \
    "v_mov_b32 v142, s69\n\t" \
    "v_mov_b32 v143, s70\n\t" \
    "s_mov_b64 exec, 1\n\t" \
    "global_store_dword v142, v143, s[36:37] sc1\n\t" \
    "s_mov_b64 exec, -1\n\t" \
    "vv_SF22_%=:\n\t" \
    "s_add_u32 s65, s65, 1\n\t" \
    "v_mov_b32 v72, 0\n\t" \
    "v_mov_b32 v73, 0\n\t" \
    "v_mov_b32 v74, 0\n\t" \
    "v_mov_b32 v75, 0\n\t" \
    "v_mov_b32 v76, 0\n\t" \
    "v_mov_b32 v77, 0\n\t" \
    "v_mov_b32 v78, 0\n\t" \
    "v_mov_b32 v79, 0\n\t" \
    "v_mov_b32 v108, 0\n\t" \
    "v_mov_b32 v109, 0\n\t" \
    "v_mov_b32 v110, 0\n\t" \
    "v_mov_b32 v111, 0\n\t" \
    "v_mov_b32 v112, 0\n\t" \
    "v_mov_b32 v113, 0\n\t" \
    "v_mov_b32 v114, 0\n\t" \
    "v_mov_b32 v115, 0\n\t" \
    "v_mov_b32 v116, 0\n\t" \
    "v_mov_b32 v117, 0\n\t" \
    "v_mov_b32 v118, 0\n\t" \
    "v_mov_b32 v119, 0\n\t" \
    "v_mov_b32 v120, 0\n\t" \
    "v_mov_b32 v121, 0\n\t" \
    "v_mov_b32 v122, 0\n\t" \
    "v_mov_b32 v123, 0\n\t" \
    "v_mov_b32 v124, 0\n\t" \
    "v_mov_b32 v125, 0\n\t" \
    "v_mov_b32 v126, 0\n\t" \
    "v_mov_b32 v127, 0\n\t" \
    "s_add_u32 s69, s65, 0\n\t" \
    "s_and_b32 s69, s69, 7\n\t" \
    "s_lshl_b32 s69, s69, 18\n\t" \
    "s_add_u32 s69, s69, s62\n\t" \
    "s_add_u32 s46, s36, s69\n\t" \
    "s_addc_u32 s47, s37, 0\n\t" \
    "s_add_u32 s70, s65, 0\n\t" \
    "s_mul_i32 s69, s70, 171\n\t" \
    "s_lshr_b32 s69, s69, 9\n\t" \
    "s_mul_i32 s70, s70, 589824\n\t" \
    "s_mul_i32 s71, s69, 327680\n\t" \
    "s_add_u32 s70, s70, s71\n\t" \
    "s_lshl1_add_u32 s69, s69, 4\n\t" \
    "s_bitcmp1_b64 s[44:45], s69\n\t" \
    "s_cselect_b32 s69, 8192, 0\n\t" \
    "s_add_u32 s69, s69, s70\n\t" \
    "s_add_u32 s69, s69, 123731968\n\t" \
    "s_add_u32 s38, s36, s69\n\t" \
    "s_addc_u32 s39, s37, 0\n\t" \
    "s_and_b32 s69, s69, 0xffe00000\n\t" \
    "s_add_u32 s69, s69, 1823744\n\t" \
    "v_mov_b32 v17, s69\n\t" \
    "global_load_dword v17, v17, s[36:37]\n\t" \
    "s_barrier\n\t" \
    "ds_read_b32 v160, v11 offset:20480\n\t" \
    "s_waitcnt lgkmcnt(0)\n\t" \
    "v_readfirstlane_b32 s70, v160\n\t" \
    "s_branch vv_loop9_%=\n\t" \
    "vv_Gbad18_%=:\n\t" \
    "s_waitcnt lgkmcnt(0)\n\t" \
    "s_nop 7\n\t" \
    "v_mov_b32 v72, 0\n\t" \
    "v_mov_b32 v73, 0\n\t" \
    "v_mov_b32 v74, 0\n\t" \
    "v_mov_b32 v75, 0\n\t" \
    "v_mov_b32 v76, 0\n\t" \
    "v_mov_b32 v77, 0\n\t" \
    "v_mov_b32 v78, 0\n\t" \
    "v_mov_b32 v79, 0\n\t" \
    "s_cmp_eq_u32 s68, 0\n\t" \
    "s_cbranch_scc1 vv_F23_%=\n\t" \
    "s_sub_u32 s68, s68, 1\n\t" \
    "s_branch vv_G17_%=\n\t" \
    "vv_F23_%=:\n\t" \
    "v_mov_b32 v140, 5767424\n\t" \
    "v_mov_b32 v141, 1\n\t" \
    "global_store_dword v140, v141, s[36:37] sc1\n\t" \
    "s_waitcnt vmcnt(0)\n\t" \
    "s_branch vv_Gc19_%=\n\t" \
    "vv_Gslow20_%=:\n\t" \
    "v_max_u32 v140, v36, v37\n\t" \
    "v_max3_u32 v140, v140, v38, v39\n\t" \
    "v_max3_u32 v140, v140, v40, v41\n\t" \
    "v_max3_u32 v140, v140, v42, v43\n\t" \
    "v_max3_u32 v140, v140, v44, v45\n\t" \
    "v_max3_u32 v140, v140, v46, v47\n\t" \
    "v_max3_u32 v140, v140, v48, v49\n\t" \
    "v_max3_u32 v140, v140, v50, v51\n\t" \
    "v_max3_u32 v140, v140, v52, v53\n\t" \
    "v_max3_u32 v140, v140, v54, v55\n\t" \
    "v_max3_u32 v140, v140, v56, v57\n\t" \
    "v_max3_u32 v140, v140, v58, v59\n\t" \
    "v_max3_u32 v140, v140, v60, v61\n\t" \
    "v_max3_u32 v140, v140, v62, v63\n\t" \
    "v_max3_u32 v140, v140, v64, v65\n\t" \
    "v_max3_u32 v140, v140, v66, v67\n\t" \
    "v_max3_u32 v140, v140, v68, v69\n\t" \
    "v_max3_u32 v140, v140, v70, v71\n\t" \
    "v_cmp_eq_u32 vcc, -1, v140\n\t" \
    "s_cbranch_vccz vv_Gback21_%=\n\t" \
    "s_waitcnt lgkmcnt(0)\n\t" \
    "v_sub_u32 v140, v1, v3\n\t" \
    "s_nop 0\n\t" \
    "v_readfirstlane_b32 s69, v140\n\t" \
    "v_add_u32 v143, 20480, v0\n\t" \
    "s_nop 7\n\t" \
    "s_add_u32 s30, s46, s69\n\t" \
    "s_addc_u32 s31, s47, 0\n\t" \
    "vv_Gag24_%=:\n\t" \
    "global_load_dwordx4 v[36:39], v1, s[46:47] sc1\n\t" \
    "global_load_dwordx4 v[40:43], v23, s[46:47] sc1\n\t" \
    "global_load_dwordx4 v[44:47], v143, s[30:31] sc1\n\t" \
    "global_load_dwordx4 v[48:51], v4, s[30:31] sc1\n\t" \
    "global_load_dwordx4 v[52:55], v5, s[30:31] sc1\n\t" \
    "global_load_dwordx4 v[56:59], v6, s[30:31] sc1\n\t" \
    "global_load_dwordx4 v[60:63], v7, s[30:31] sc1\n\t" \
    "global_load_dwordx4 v[64:67], v8, s[30:31] sc1\n\t" \
    "global_load_dwordx4 v[68:71], v9, s[30:31] sc1\n\t" \
    "s_waitcnt vmcnt(0)\n\t" \
    "v_max_u32 v140, v36, v37\n\t" \
    "v_max3_u32 v140, v140, v38, v39\n\t" \
    "v_max3_u32 v140, v140, v40, v41\n\t" \
    "v_max3_u32 v140, v140, v42, v43\n\t" \
    "v_max3_u32 v140, v140, v44, v45\n\t" \
    "v_max3_u32 v140, v140, v46, v47\n\t" \
    "v_max3_u32 v140, v140, v48, v49\n\t" \
    "v_max3_u32 v140, v140, v50, v51\n\t" \
    "v_max3_u32 v140, v140, v52, v53\n\t" \
    "v_max3_u32 v140, v140, v54, v55\n\t" \
    "v_max3_u32 v140, v140, v56, v57\n\t" \
    "v_max3_u32 v140, v140, v58, v59\n\t" \
    "v_max3_u32 v140, v140, v60, v61\n\t" \
    "v_max3_u32 v140, v140, v62, v63\n\t" \
    "v_max3_u32 v140, v140, v64, v65\n\t" \
    "v_max3_u32 v140, v140, v66, v67\n\t" \
    "v_max3_u32 v140, v140, v68, v69\n\t" \
    "v_max3_u32 v140, v140, v70, v71\n\t" \
    "v_cmp_eq_u32 vcc, -1, v140\n\t" \
    "s_cbranch_vccz vv_Gredo25_%=\n\t" \
    "s_cmp_eq_u32 s68, 0\n\t" \
    "s_cbranch_scc1 vv_F26_%=\n\t" \
    "s_sub_u32 s68, s68, 1\n\t" \
    "s_sleep 1\n\t" \
    "s_branch vv_Gag24_%=\n\t" \
    "vv_F26_%=:\n\t" \
    "v_mov_b32 v140, 5767424\n\t" \
    "v_mov_b32 v141, 2\n\t" \
    "global_store_dword v140, v141, s[36:37] sc1\n\t" \
    "s_waitcnt vmcnt(0)\n\t" \
    "s_branch vv_Gredo25_%=\n\t" \
    "vv_Gredo25_%=:\n\t" \
    "v_mov_b32 v72, 0\n\t" \
    "v_mov_b32 v73, 0\n\t" \
    "v_mov_b32 v74, 0\n\t" \
    "v_mov_b32 v75, 0\n\t" \
    "v_mov_b32 v76, 0\n\t" \
    "v_mov_b32 v77, 0\n\t" \
    "v_mov_b32 v78, 0\n\t" \
    "v_mov_b32 v79, 0\n\t" \
    "v_smfmac_f32_16x16x64_bf16 v[72:75], v[36:39], a[0:7], v10\n\t" \
    "v_smfmac_f32_16x16x64_bf16 v[72:75], v[40:43], a[8:15], v10\n\t" \
    "v_smfmac_f32_16x16x64_bf16 v[72:75], v[44:47], a[64:71], v10\n\t" \
    "v_smfmac_f32_16x16x64_bf16 v[72:75], v[48:51], a[16:23], v10\n\t" \
    "v_smfmac_f32_16x16x64_bf16 v[72:75], v[52:55], a[24:31], v10\n\t" \
    "v_smfmac_f32_16x16x64_bf16 v[72:75], v[56:59], a[32:39], v10\n\t" \
    "v_smfmac_f32_16x16x64_bf16 v[72:75], v[60:63], a[40:47], v10\n\t" \
    "v_smfmac_f32_16x16x64_bf16 v[72:75], v[64:67], a[48:55], v10\n\t" \
    "v_smfmac_f32_16x16x64_bf16 v[72:75], v[68:71], a[56:63], v10\n\t" \
    "v_smfmac_f32_16x16x64_bf16 v[76:79], v[36:39], a[72:79], v10\n\t" \
    "v_smfmac_f32_16x16x64_bf16 v[76:79], v[40:43], a[80:87], v10\n\t" \
    "v_smfmac_f32_16x16x64_bf16 v[76:79], v[44:47], a[136:143], v10\n\t" \
    "v_smfmac_f32_16x16x64_bf16 v[76:79], v[48:51], a[88:95], v10\n\t" \
    "v_smfmac_f32_16x16x64_bf16 v[76:79], v[52:55], a[96:103], v10\n\t" \
    "v_smfmac_f32_16x16x64_bf16 v[76:79], v[56:59], a[104:111], v10\n\t" \
    "v_smfmac_f32_16x16x64_bf16 v[76:79], v[60:63], a[112:119], v10\n\t" \
    "v_smfmac_f32_16x16x64_bf16 v[76:79], v[64:67], a[120:127], v10\n\t" \
    "v_smfmac_f32_16x16x64_bf16 v[76:79], v[68:71], a[128:135], v10\n\t" \
    "v_add_f32 v72, v72, v74\n\t" \
    "v_add_f32 v73, v73, v75\n\t" \
    "v_cvt_pk_bf16_f32 v80, v72, v73\n\t" \
    "ds_write_b32 v12, v80 offset:0\n\t" \
    "s_nop 3\n\t" \
    "v_pk_add_f32 v[76:77], v[76:77], v[78:79]\n\t" \
    "v_cvt_pk_bf16_f32 v81, v76, v77\n\t" \
    "ds_write_b32 v12, v81 offset:256\n\t" \
    "s_branch vv_Gback21_%=\n\t" \
    "vv_end13_%=:\n\t" \
    "s_endpgm\n\t" \
    ".p2align 6\n\t" \
    "s_nop 0\n\t" \
    "s_nop 0\n\t" \
    "s_nop 0\n\t" \
    "s_nop 0\n\t" \
    "s_nop 0\n\t" \
    "s_nop 0\n\t" \
    "vv_loop10_%=:\n\t" \
    "s_getpc_b64 s[30:31]\n\t" \
    "vv_Wp29_%=:\n\t" \
    "s_add_u32 s30, s30, vv_We30_%=-vv_Wp29_%=\n\t" \
    "s_addc_u32 s31, s31, 0\n\t" \
    "s_sub_u32 s70, s70, 96\n\t" \
    "vv_W28_%=:\n\t" \
    "s_memtime s[56:57]\n\t" \
    "s_waitcnt lgkmcnt(0)\n\t" \
    "s_sub_u32 s69, s70, s56\n\t" \
    "s_cmp_gt_i32 s69, 80\n\t" \
    "s_cbranch_scc1 vv_W28_%=\n\t" \
    "s_max_i32 s69, s69, 0\n\t" \
    "s_and_b32 s69, s69, -4\n\t" \
    "s_sub_u32 s30, s30, s69\n\t" \
    "s_subb_u32 s31, s31, 0\n\t" \
    "s_setpc_b64 s[30:31]\n\t" \
    "s_nop 0\n\t" \
    "s_nop 0\n\t" \
    "s_nop 0\n\t" \
    "s_nop 0\n\t" \
    "s_nop 0\n\t" \
    "s_nop 0\n\t" \
    "s_nop 0\n\t" \
    "s_nop 0\n\t" \
    "s_nop 0\n\t" \
    "s_nop 0\n\t" \
    "s_nop 0\n\t" \
    "s_nop 0\n\t" \
    "s_nop 0\n\t" \
    "s_nop 0\n\t" \
    "s_nop 0\n\t" \
    "s_nop 0\n\t" \
    "s_nop 0\n\t" \
    "s_nop 0\n\t" \
    "s_nop 0\n\t" \
    "s_nop 0\n\t" \
    "vv_We30_%=:\n\t" \
    "vv_G31_%=:\n\t" \
    "global_load_dwordx4 v[36:39], v1, s[46:47] sc1\n\t" \
    "global_load_dword v141, v2, s[46:47] sc1\n\t" \
    "global_load_dwordx4 v[40:43], v23, s[46:47] sc1\n\t" \
    "s_waitcnt vmcnt(2)\n\t" \
    "ds_write_b128 v3, v[36:39]\n\t" \
    "v_smfmac_f32_16x16x64_bf16 v[72:75], v[36:39], a[16:23], v10\n\t" \
    "s_waitcnt vmcnt(1)\n\t" \
    "ds_write_b32 v22, v141 offset:19712\n\t" \
    "s_waitcnt vmcnt(0)\n\t" \
    "ds_write_b128 v3, v[40:43] offset:1024\n\t" \
    "v_max3_u32 v140, v36, v40, v141\n\t" \
    "v_cmp_eq_u32 vcc, -1, v140\n\t" \
    "s_cbranch_vccnz vv_Gbad32_%=\n\t" \
    "vv_Gc33_%=:\n\t" \
    "s_waitcnt lgkmcnt(0)\n\t" \
    "s_barrier\n\t" \
    "ds_read_b128 v[48:51], v4\n\t" \
    "ds_read_b128 v[52:55], v5\n\t" \
    "v_smfmac_f32_16x16x64_bf16 v[76:79], v[36:39], a[88:95], v10\n\t" \
    "ds_read_b128 v[56:59], v6\n\t" \
    "ds_read_b128 v[60:63], v7\n\t" \
    "v_smfmac_f32_16x16x64_bf16 v[72:75], v[40:43], a[24:31], v10\n\t" \
    "ds_read_b128 v[64:67], v8\n\t" \
    "v_smfmac_f32_16x16x64_bf16 v[76:79], v[40:43], a[96:103], v10\n\t" \
    "ds_read_b128 v[68:71], v9\n\t" \
    "s_waitcnt lgkmcnt(5)\n\t" \
    "v_smfmac_f32_16x16x64_bf16 v[72:75], v[48:51], a[32:39], v10\n\t" \
    "ds_read_b128 v[44:47], v0 offset:19456\n\t" \
    "s_waitcnt lgkmcnt(5)\n\t" \
    "v_smfmac_f32_16x16x64_bf16 v[72:75], v[52:55], a[40:47], v10\n\t" \
    "s_waitcnt lgkmcnt(4)\n\t" \
    "v_smfmac_f32_16x16x64_bf16 v[72:75], v[56:59], a[48:55], v10\n\t" \
    "s_waitcnt lgkmcnt(3)\n\t" \
    "v_smfmac_f32_16x16x64_bf16 v[72:75], v[60:63], a[56:63], v10\n\t" \
    "s_waitcnt lgkmcnt(2)\n\t" \
    "v_smfmac_f32_16x16x64_bf16 v[72:75], v[64:67], a[0:7], v10\n\t" \
    "s_waitcnt lgkmcnt(1)\n\t" \
    "v_smfmac_f32_16x16x64_bf16 v[72:75], v[68:71], a[8:15], v10\n\t" \
    "s_waitcnt lgkmcnt(0)\n\t" \
    "v_smfmac_f32_16x16x64_bf16 v[72:75], v[44:47], a[64:71], v10\n\t" \
    "s_nop 2\n\t" \
    "v_smfmac_f32_16x16x64_bf16 v[76:79], v[48:51], a[104:111], v10\n\t" \
    "s_nop 2\n\t" \
    "v_smfmac_f32_16x16x64_bf16 v[76:79], v[52:55], a[112:119], v10\n\t" \
    "v_add_f32 v72, v72, v74\n\t" \
    "v_smfmac_f32_16x16x64_bf16 v[76:79], v[56:59], a[120:127], v10\n\t" \
    "v_add_f32 v73, v73, v75\n\t" \
    "v_smfmac_f32_16x16x64_bf16 v[76:79], v[60:63], a[128:135], v10\n\t" \
    "v_cvt_pk_bf16_f32 v80, v72, v73\n\t" \
    "v_smfmac_f32_16x16x64_bf16 v[76:79], v[64:67], a[72:79], v10\n\t" \
    "ds_write_b32 v12, v80 offset:0\n\t" \
    "v_smfmac_f32_16x16x64_bf16 v[76:79], v[68:71], a[80:87], v10\n\t" \
    "v_smfmac_f32_16x16x64_bf16 v[76:79], v[44:47], a[136:143], v10\n\t" \
    "s_nop 6\n\t" \
    "v_cmp_u_f32 vcc, v72, v73\n\t" \
    "v_pk_add_f32 v[76:77], v[76:77], v[78:79]\n\t" \
    "v_cvt_pk_bf16_f32 v81, v76, v77\n\t" \
    "ds_write_b32 v12, v81 offset:256\n\t" \
    "s_cbranch_vccnz vv_Gslow34_%=\n\t" \
    "vv_Gback35_%=:\n\t" \
    "s_waitcnt lgkmcnt(0)\n\t" \
    "s_barrier\n\t" \
    "ds_read_b64_tr_b16 v[84:85], v13 offset:0\n\t" \
    "v_smfmac_f32_16x16x64_bf16 v[100:103], v[36:39], a[0:7], v10\n\t" \
    "ds_read_b64_tr_b16 v[86:87], v13 offset:64\n\t" \
    "ds_read_b64_tr_b16 v[92:93], v13 offset:1152\n\t" \
    "v_smfmac_f32_16x16x64_bf16 v[100:103], v[36:39], a[0:7], v10\n\t" \
    "ds_read_b64_tr_b16 v[94:95], v13 offset:1216\n\t" \
    "ds_read_b64_tr_b16 v[88:89], v25 offset:0\n\t" \
    "v_smfmac_f32_16x16x64_bf16 v[100:103], v[36:39], a[0:7], v10\n\t" \
    "ds_read_b64_tr_b16 v[90:91], v25 offset:64\n\t" \
    "s_waitcnt lgkmcnt(4)\n\t" \
    "v_smfmac_f32_16x16x64_bf16 v[124:127], v[84:87], v[240:247], v19\n\t" \
    "ds_read_b64_tr_b16 v[96:97], v25 offset:1152\n\t" \
    "ds_read_b64_tr_b16 v[98:99], v25 offset:1216\n\t" \
    "s_waitcnt lgkmcnt(4)\n\t" \
    "v_smfmac_f32_16x16x64_bf16 v[124:127], v[92:95], v[248:255], v19\n\t" \
    "s_nop 2\n\t" \
    "v_smfmac_f32_16x16x64_bf16 v[108:111], v[84:87], a[144:151], v19\n\t" \
    "s_nop 2\n\t" \
    "v_smfmac_f32_16x16x64_bf16 v[108:111], v[92:95], a[160:167], v19\n\t" \
    "s_nop 1\n\t" \
    "v_permlane32_swap_b32 v124, v126\n\t" \
    "v_smfmac_f32_16x16x64_bf16 v[112:115], v[84:87], a[176:183], v19\n\t" \
    "s_nop 1\n\t" \
    "v_permlane32_swap_b32 v125, v127\n\t" \
    "v_smfmac_f32_16x16x64_bf16 v[112:115], v[92:95], a[192:199], v19\n\t" \
    "s_nop 0\n\t" \
    "v_add_f32 v124, v124, v126\n\t" \
    "s_waitcnt lgkmcnt(2)\n\t" \
    "v_smfmac_f32_16x16x64_bf16 v[108:111], v[88:91], a[152:159], v19\n\t" \
    "s_nop 1\n\t" \
    "v_add_f32 v125, v125, v127\n\t" \
    "v_smfmac_f32_16x16x64_bf16 v[112:115], v[88:91], a[184:191], v19\n\t" \
    "s_nop 0\n\t" \
    "v_cvt_pk_bf16_f32 v136, v124, v125\n\t" \
    "s_waitcnt lgkmcnt(0)\n\t" \
    "v_smfmac_f32_16x16x64_bf16 v[108:111], v[96:99], a[168:175], v19\n\t" \
    "s_nop 1\n\t" \
    "v_cvt_pk_bf16_f32 v137, v124, v125\n\t" \
    "v_smfmac_f32_16x16x64_bf16 v[112:115], v[96:99], a[200:207], v19\n\t" \
    "s_nop 1\n\t" \
    "v_permlane32_swap_b32 v136, v137\n\t" \
    "v_smfmac_f32_16x16x64_bf16 v[116:119], v[84:87], a[208:215], v19\n\t" \
    "s_mov_b32 exec_hi, 0\n\t" \
    "global_store_dwordx2 v16, v[136:137], s[38:39] sc1\n\t" \
    "s_mov_b32 exec_hi, -1\n\t" \
    "v_cvt_pk_bf16_f32 v128, v108, v109\n\t" \
    "v_smfmac_f32_16x16x64_bf16 v[116:119], v[88:91], a[216:223], v19\n\t" \
    "s_nop 1\n\t" \
    "v_cvt_pk_bf16_f32 v129, v110, v111\n\t" \
    "v_smfmac_f32_16x16x64_bf16 v[116:119], v[92:95], a[224:231], v19\n\t" \
    "s_nop 1\n\t" \
    "v_cvt_pk_bf16_f32 v130, v112, v113\n\t" \
    "v_smfmac_f32_16x16x64_bf16 v[116:119], v[96:99], a[232:239], v19\n\t" \
    "s_nop 1\n\t" \
    "v_cvt_pk_bf16_f32 v131, v114, v115\n\t" \
    "v_smfmac_f32_16x16x64_bf16 v[120:123], v[84:87], a[240:247], v19\n\t" \
    "s_nop 1\n\t" \
    "v_permlane16_swap_b32 v128, v130\n\t" \
    "v_smfmac_f32_16x16x64_bf16 v[120:123], v[88:91], a[248:255], v19\n\t" \
    "s_nop 1\n\t" \
    "v_permlane16_swap_b32 v129, v131\n\t" \
    "v_smfmac_f32_16x16x64_bf16 v[120:123], v[92:95], v[224:231], v19\n\t" \
    "s_nop 0\n\t" \
    "global_store_dwordx4 v14, v[128:131], s[38:39] sc1\n\t" \
    "v_cvt_pk_bf16_f32 v132, v116, v117\n\t" \
    "v_smfmac_f32_16x16x64_bf16 v[120:123], v[96:99], v[232:239], v19\n\t" \
    "s_nop 1\n\t" \
    "v_cvt_pk_bf16_f32 v133, v118, v119\n\t" \
    "s_nop 4\n\t" \
    "v_cvt_pk_bf16_f32 v134, v120, v121\n\t" \
    "v_cvt_pk_bf16_f32 v135, v122, v123\n\t" \
    "s_nop 0\n\t" \
    "v_permlane16_swap_b32 v132, v134\n\t" \
    "v_permlane16_swap_b32 v133, v135\n\t" \
    "global_store_dwordx4 v15, v[132:135], s[38:39] sc1\n\t" \
    "s_cmp_eq_u32 s65, 63\n\t" \
    "s_cbranch_scc1 vv_end27_%=\n\t" \
    "s_add_u32 s65, s65, 1\n\t" \
    "v_mov_b32 v72, 0\n\t" \
    "v_mov_b32 v73, 0\n\t" \
    "v_mov_b32 v74, 0\n\t" \
    "v_mov_b32 v75, 0\n\t" \
    "v_mov_b32 v76, 0\n\t" \
    "v_mov_b32 v77, 0\n\t" \
    "v_mov_b32 v78, 0\n\t" \
    "v_mov_b32 v79, 0\n\t" \
    "v_mov_b32 v108, 0\n\t" \
    "v_mov_b32 v109, 0\n\t" \
    "v_mov_b32 v110, 0\n\t" \
    "v_mov_b32 v111, 0\n\t" \
    "v_mov_b32 v112, 0\n\t" \
    "v_mov_b32 v113, 0\n\t" \
    "v_mov_b32 v114, 0\n\t" \
    "v_mov_b32 v115, 0\n\t" \
    "v_mov_b32 v116, 0\n\t" \
    "v_mov_b32 v117, 0\n\t" \
    "v_mov_b32 v118, 0\n\t" \
    "v_mov_b32 v119, 0\n\t" \
    "v_mov_b32 v120, 0\n\t" \
    "v_mov_b32 v121, 0\n\t" \
    "v_mov_b32 v122, 0\n\t" \
    "v_mov_b32 v123, 0\n\t" \
    "v_mov_b32 v124, 0\n\t" \
    "v_mov_b32 v125, 0\n\t" \
    "v_mov_b32 v126, 0\n\t" \
    "v_mov_b32 v127, 0\n\t" \
    "s_add_u32 s69, s65, 0\n\t" \
    "s_and_b32 s69, s69, 7\n\t" \
    "s_lshl_b32 s69, s69, 18\n\t" \
    "s_add_u32 s69, s69, s62\n\t" \
    "s_add_u32 s46, s36, s69\n\t" \
    "s_addc_u32 s47, s37, 0\n\t" \
    "s_add_u32 s70, s65, 0\n\t" \
    "s_mul_i32 s69, s70, 171\n\t" \
    "s_lshr_b32 s69, s69, 9\n\t" \
    "s_mul_i32 s70, s70, 589824\n\t" \
    "s_mul_i32 s71, s69, 327680\n\t" \
    "s_add_u32 s70, s70, s71\n\t" \
    "s_lshl1_add_u32 s69, s69, 4\n\t" \
    "s_bitcmp1_b64 s[44:45], s69\n\t" \
    "s_cselect_b32 s69, 8192, 0\n\t" \
    "s_add_u32 s69, s69, s70\n\t" \
    "s_add_u32 s69, s69, 123731968\n\t" \
    "s_add_u32 s38, s36, s69\n\t" \
    "s_addc_u32 s39, s37, 0\n\t" \
    "s_and_b32 s69, s69, 0xffe00000\n\t" \
    "s_add_u32 s69, s69, 1823744\n\t" \
    "v_mov_b32 v17, s69\n\t" \
    "global_load_dword v17, v17, s[36:37]\n\t" \
    "s_barrier\n\t" \
    "ds_read_b32 v160, v11 offset:20480\n\t" \
    "s_waitcnt lgkmcnt(0)\n\t" \
    "v_readfirstlane_b32 s70, v160\n\t" \
    "s_branch vv_loop10_%=\n\t" \
    "vv_Gbad32_%=:\n\t" \
    "s_waitcnt lgkmcnt(0)\n\t" \
    "s_nop 7\n\t" \
    "v_mov_b32 v72, 0\n\t" \
    "v_mov_b32 v73, 0\n\t" \
    "v_mov_b32 v74, 0\n\t" \
    "v_mov_b32 v75, 0\n\t" \
    "v_mov_b32 v76, 0\n\t" \
    "v_mov_b32 v77, 0\n\t" \
    "v_mov_b32 v78, 0\n\t" \
    "v_mov_b32 v79, 0\n\t" \
    "s_cmp_eq_u32 s68, 0\n\t" \
    "s_cbranch_scc1 vv_F36_%=\n\t" \
    "s_sub_u32 s68, s68, 1\n\t" \
    "s_branch vv_G31_%=\n\t" \
    "vv_F36_%=:\n\t" \
    "v_mov_b32 v140, 5767424\n\t" \
    "v_mov_b32 v141, 1\n\t" \
    "global_store_dword v140, v141, s[36:37] sc1\n\t" \
    "s_waitcnt vmcnt(0)\n\t" \
    "s_branch vv_Gc33_%=\n\t" \
    "vv_Gslow34_%=:\n\t" \
    "v_max_u32 v140, v36, v37\n\t" \
    "v_max3_u32 v140, v140, v38, v39\n\t" \
    "v_max3_u32 v140, v140, v40, v41\n\t" \
    "v_max3_u32 v140, v140, v42, v43\n\t" \
    "v_max3_u32 v140, v140, v44, v45\n\t" \
    "v_max3_u32 v140, v140, v46, v47\n\t" \
    "v_max3_u32 v140, v140, v48, v49\n\t" \
    "v_max3_u32 v140, v140, v50, v51\n\t" \
    "v_max3_u32 v140, v140, v52, v53\n\t" \
    "v_max3_u32 v140, v140, v54, v55\n\t" \
    "v_max3_u32 v140, v140, v56, v57\n\t" \
    "v_max3_u32 v140, v140, v58, v59\n\t" \
    "v_max3_u32 v140, v140, v60, v61\n\t" \
    "v_max3_u32 v140, v140, v62, v63\n\t" \
    "v_max3_u32 v140, v140, v64, v65\n\t" \
    "v_max3_u32 v140, v140, v66, v67\n\t" \
    "v_max3_u32 v140, v140, v68, v69\n\t" \
    "v_max3_u32 v140, v140, v70, v71\n\t" \
    "v_cmp_eq_u32 vcc, -1, v140\n\t" \
    "s_cbranch_vccz vv_Gback35_%=\n\t" \
    "s_waitcnt lgkmcnt(0)\n\t" \
    "v_sub_u32 v140, v1, v3\n\t" \
    "s_nop 0\n\t" \
    "v_readfirstlane_b32 s69, v140\n\t" \
    "v_add_u32 v143, 20480, v0\n\t" \
    "s_nop 7\n\t" \
    "s_add_u32 s30, s46, s69\n\t" \
    "s_addc_u32 s31, s47, 0\n\t" \
    "vv_Gag37_%=:\n\t" \
    "global_load_dwordx4 v[36:39], v1, s[46:47] sc1\n\t" \
    "global_load_dwordx4 v[40:43], v23, s[46:47] sc1\n\t" \
    "global_load_dwordx4 v[44:47], v143, s[30:31] sc1\n\t" \
    "global_load_dwordx4 v[48:51], v4, s[30:31] sc1\n\t" \
    "global_load_dwordx4 v[52:55], v5, s[30:31] sc1\n\t" \
    "global_load_dwordx4 v[56:59], v6, s[30:31] sc1\n\t" \
    "global_load_dwordx4 v[60:63], v7, s[30:31] sc1\n\t" \
    "global_load_dwordx4 v[64:67], v8, s[30:31] sc1\n\t" \
    "global_load_dwordx4 v[68:71], v9, s[30:31] sc1\n\t" \
    "s_waitcnt vmcnt(0)\n\t" \
    "v_max_u32 v140, v36, v37\n\t" \
    "v_max3_u32 v140, v140, v38, v39\n\t" \
    "v_max3_u32 v140, v140, v40, v41\n\t" \
    "v_max3_u32 v140, v140, v42, v43\n\t" \
    "v_max3_u32 v140, v140, v44, v45\n\t" \
    "v_max3_u32 v140, v140, v46, v47\n\t" \
    "v_max3_u32 v140, v140, v48, v49\n\t" \
    "v_max3_u32 v140, v140, v50, v51\n\t" \
    "v_max3_u32 v140, v140, v52, v53\n\t" \
    "v_max3_u32 v140, v140, v54, v55\n\t" \
    "v_max3_u32 v140, v140, v56, v57\n\t" \
    "v_max3_u32 v140, v140, v58, v59\n\t" \
    "v_max3_u32 v140, v140, v60, v61\n\t" \
    "v_max3_u32 v140, v140, v62, v63\n\t" \
    "v_max3_u32 v140, v140, v64, v65\n\t" \
    "v_max3_u32 v140, v140, v66, v67\n\t" \
    "v_max3_u32 v140, v140, v68, v69\n\t" \
    "v_max3_u32 v140, v140, v70, v71\n\t" \
    "v_cmp_eq_u32 vcc, -1, v140\n\t" \
    "s_cbranch_vccz vv_Gredo38_%=\n\t" \
    "s_cmp_eq_u32 s68, 0\n\t" \
    "s_cbranch_scc1 vv_F39_%=\n\t" \
    "s_sub_u32 s68, s68, 1\n\t" \
    "s_sleep 1\n\t" \
    "s_branch vv_Gag37_%=\n\t" \
    "vv_F39_%=:\n\t" \
    "v_mov_b32 v140, 5767424\n\t" \
    "v_mov_b32 v141, 2\n\t" \
    "global_store_dword v140, v141, s[36:37] sc1\n\t" \
    "s_waitcnt vmcnt(0)\n\t" \
    "s_branch vv_Gredo38_%=\n\t" \
    "vv_Gredo38_%=:\n\t" \
    "v_mov_b32 v72, 0\n\t" \
    "v_mov_b32 v73, 0\n\t" \
    "v_mov_b32 v74, 0\n\t" \
    "v_mov_b32 v75, 0\n\t" \
    "v_mov_b32 v76, 0\n\t" \
    "v_mov_b32 v77, 0\n\t" \
    "v_mov_b32 v78, 0\n\t" \
    "v_mov_b32 v79, 0\n\t" \
    "v_smfmac_f32_16x16x64_bf16 v[72:75], v[36:39], a[16:23], v10\n\t" \
    "v_smfmac_f32_16x16x64_bf16 v[72:75], v[40:43], a[24:31], v10\n\t" \
    "v_smfmac_f32_16x16x64_bf16 v[72:75], v[44:47], a[64:71], v10\n\t" \
    "v_smfmac_f32_16x16x64_bf16 v[72:75], v[48:51], a[32:39], v10\n\t" \
    "v_smfmac_f32_16x16x64_bf16 v[72:75], v[52:55], a[40:47], v10\n\t" \
    "v_smfmac_f32_16x16x64_bf16 v[72:75], v[56:59], a[48:55], v10\n\t" \
    "v_smfmac_f32_16x16x64_bf16 v[72:75], v[60:63], a[56:63], v10\n\t" \
    "v_smfmac_f32_16x16x64_bf16 v[72:75], v[64:67], a[0:7], v10\n\t" \
    "v_smfmac_f32_16x16x64_bf16 v[72:75], v[68:71], a[8:15], v10\n\t" \
    "v_smfmac_f32_16x16x64_bf16 v[76:79], v[36:39], a[88:95], v10\n\t" \
    "v_smfmac_f32_16x16x64_bf16 v[76:79], v[40:43], a[96:103], v10\n\t" \
    "v_smfmac_f32_16x16x64_bf16 v[76:79], v[44:47], a[136:143], v10\n\t" \
    "v_smfmac_f32_16x16x64_bf16 v[76:79], v[48:51], a[104:111], v10\n\t" \
    "v_smfmac_f32_16x16x64_bf16 v[76:79], v[52:55], a[112:119], v10\n\t" \
    "v_smfmac_f32_16x16x64_bf16 v[76:79], v[56:59], a[120:127], v10\n\t" \
    "v_smfmac_f32_16x16x64_bf16 v[76:79], v[60:63], a[128:135], v10\n\t" \
    "v_smfmac_f32_16x16x64_bf16 v[76:79], v[64:67], a[72:79], v10\n\t" \
    "v_smfmac_f32_16x16x64_bf16 v[76:79], v[68:71], a[80:87], v10\n\t" \
    "v_add_f32 v72, v72, v74\n\t" \
    "v_add_f32 v73, v73, v75\n\t" \
    "v_cvt_pk_bf16_f32 v80, v72, v73\n\t" \
    "ds_write_b32 v12, v80 offset:0\n\t" \
    "s_nop 3\n\t" \
    "v_pk_add_f32 v[76:77], v[76:77], v[78:79]\n\t" \
    "v_cvt_pk_bf16_f32 v81, v76, v77\n\t" \
    "ds_write_b32 v12, v81 offset:256\n\t" \
    "s_branch vv_Gback35_%=\n\t" \
    "vv_end27_%=:\n\t" \
    "s_endpgm\n\t" \
    ".p2align 6\n\t" \
    "s_nop 0\n\t" \
    "s_nop 0\n\t" \
    "s_nop 0\n\t" \
    "s_nop 0\n\t" \
    "s_nop 0\n\t" \
    "s_nop 0\n\t" \
    "vv_loop11_%=:\n\t" \
    "s_getpc_b64 s[30:31]\n\t" \
    "vv_Wp42_%=:\n\t" \
    "s_add_u32 s30, s30, vv_We43_%=-vv_Wp42_%=\n\t" \
    "s_addc_u32 s31, s31, 0\n\t" \
    "s_sub_u32 s70, s70, 96\n\t" \
    "vv_W41_%=:\n\t" \
    "s_memtime s[56:57]\n\t" \
    "s_waitcnt lgkmcnt(0)\n\t" \
    "s_sub_u32 s69, s70, s56\n\t" \
    "s_cmp_gt_i32 s69, 80\n\t" \
    "s_cbranch_scc1 vv_W41_%=\n\t" \
    "s_max_i32 s69, s69, 0\n\t" \
    "s_and_b32 s69, s69, -4\n\t" \
    "s_sub_u32 s30, s30, s69\n\t" \
    "s_subb_u32 s31, s31, 0\n\t" \
    "s_setpc_b64 s[30:31]\n\t" \
    "s_nop 0\n\t" \
    "s_nop 0\n\t" \
    "s_nop 0\n\t" \
    "s_nop 0\n\t" \
    "s_nop 0\n\t" \
    "s_nop 0\n\t" \
    "s_nop 0\n\t" \
    "s_nop 0\n\t" \
    "s_nop 0\n\t" \
    "s_nop 0\n\t" \
    "s_nop 0\n\t" \
    "s_nop 0\n\t" \
    "s_nop 0\n\t" \
    "s_nop 0\n\t" \
    "s_nop 0\n\t" \
    "s_nop 0\n\t" \
    "s_nop 0\n\t" \
    "s_nop 0\n\t" \
    "s_nop 0\n\t" \
    "s_nop 0\n\t" \
    "vv_We43_%=:\n\t" \
    "vv_G44_%=:\n\t" \
    "global_load_dwordx4 v[36:39], v1, s[46:47] sc1\n\t" \
    "global_load_dword v141, v2, s[46:47] sc1\n\t" \
    "global_load_dwordx4 v[40:43], v23, s[46:47] sc1\n\t" \
    "s_waitcnt vmcnt(2)\n\t" \
    "ds_write_b128 v3, v[36:39]\n\t" \
    "v_smfmac_f32_16x16x64_bf16 v[72:75], v[36:39], a[32:39], v10\n\t" \
    "s_waitcnt vmcnt(1)\n\t" \
    "ds_write_b32 v22, v141 offset:19968\n\t" \
    "s_waitcnt vmcnt(0)\n\t" \
    "ds_write_b128 v3, v[40:43] offset:1024\n\t" \
    "v_max3_u32 v140, v36, v40, v141\n\t" \
    "v_cmp_eq_u32 vcc, -1, v140\n\t" \
    "s_cbranch_vccnz vv_Gbad45_%=\n\t" \
    "vv_Gc46_%=:\n\t" \
    "s_waitcnt lgkmcnt(0)\n\t" \
    "s_barrier\n\t" \
    "ds_read_b128 v[48:51], v4\n\t" \
    "ds_read_b128 v[52:55], v5\n\t" \
    "v_smfmac_f32_16x16x64_bf16 v[76:79], v[36:39], a[104:111], v10\n\t" \
    "ds_read_b128 v[56:59], v6\n\t" \
    "ds_read_b128 v[60:63], v7\n\t" \
    "v_smfmac_f32_16x16x64_bf16 v[72:75], v[40:43], a[40:47], v10\n\t" \
    "ds_read_b128 v[64:67], v8\n\t" \
    "v_smfmac_f32_16x16x64_bf16 v[76:79], v[40:43], a[112:119], v10\n\t" \
    "ds_read_b128 v[68:71], v9\n\t" \
    "s_waitcnt lgkmcnt(5)\n\t" \
    "v_smfmac_f32_16x16x64_bf16 v[72:75], v[48:51], a[48:55], v10\n\t" \
    "ds_read_b128 v[44:47], v0 offset:19456\n\t" \
    "s_waitcnt lgkmcnt(5)\n\t" \
    "v_smfmac_f32_16x16x64_bf16 v[72:75], v[52:55], a[56:63], v10\n\t" \
    "s_waitcnt lgkmcnt(4)\n\t" \
    "v_smfmac_f32_16x16x64_bf16 v[72:75], v[56:59], a[0:7], v10\n\t" \
    "s_waitcnt lgkmcnt(3)\n\t" \
    "v_smfmac_f32_16x16x64_bf16 v[72:75], v[60:63], a[8:15], v10\n\t" \
    "s_waitcnt lgkmcnt(2)\n\t" \
    "v_smfmac_f32_16x16x64_bf16 v[72:75], v[64:67], a[16:23], v10\n\t" \
    "s_waitcnt lgkmcnt(1)\n\t" \
    "v_smfmac_f32_16x16x64_bf16 v[72:75], v[68:71], a[24:31], v10\n\t" \
    "s_waitcnt lgkmcnt(0)\n\t" \
    "v_smfmac_f32_16x16x64_bf16 v[72:75], v[44:47], a[64:71], v10\n\t" \
    "s_nop 2\n\t" \
    "v_smfmac_f32_16x16x64_bf16 v[76:79], v[48:51], a[120:127], v10\n\t" \
    "s_nop 2\n\t" \
    "v_smfmac_f32_16x16x64_bf16 v[76:79], v[52:55], a[128:135], v10\n\t" \
    "v_add_f32 v72, v72, v74\n\t" \
    "v_smfmac_f32_16x16x64_bf16 v[76:79], v[56:59], a[72:79], v10\n\t" \
    "v_add_f32 v73, v73, v75\n\t" \
    "v_smfmac_f32_16x16x64_bf16 v[76:79], v[60:63], a[80:87], v10\n\t" \
    "v_cvt_pk_bf16_f32 v80, v72, v73\n\t" \
    "v_smfmac_f32_16x16x64_bf16 v[76:79], v[64:67], a[88:95], v10\n\t" \
    "ds_write_b32 v12, v80 offset:0\n\t" \
    "v_smfmac_f32_16x16x64_bf16 v[76:79], v[68:71], a[96:103], v10\n\t" \
    "v_smfmac_f32_16x16x64_bf16 v[76:79], v[44:47], a[136:143], v10\n\t" \
    "s_nop 6\n\t" \
    "v_cmp_u_f32 vcc, v72, v73\n\t" \
    "v_pk_add_f32 v[76:77], v[76:77], v[78:79]\n\t" \
    "v_cvt_pk_bf16_f32 v81, v76, v77\n\t" \
    "ds_write_b32 v12, v81 offset:256\n\t" \
    "s_cbranch_vccnz vv_Gslow47_%=\n\t" \
    "vv_Gback48_%=:\n\t" \
    "s_waitcnt lgkmcnt(0)\n\t" \
    "s_barrier\n\t" \
    "ds_read_b64_tr_b16 v[84:85], v13 offset:0\n\t" \
    "v_smfmac_f32_16x16x64_bf16 v[100:103], v[36:39], a[0:7], v10\n\t" \
    "ds_read_b64_tr_b16 v[86:87], v13 offset:64\n\t" \
    "ds_read_b64_tr_b16 v[92:93], v13 offset:1152\n\t" \
    "v_smfmac_f32_16x16x64_bf16 v[100:103], v[36:39], a[0:7], v10\n\t" \
    "ds_read_b64_tr_b16 v[94:95], v13 offset:1216\n\t" \
    "ds_read_b64_tr_b16 v[88:89], v25 offset:0\n\t" \
    "v_smfmac_f32_16x16x64_bf16 v[100:103], v[36:39], a[0:7], v10\n\t" \
    "ds_read_b64_tr_b16 v[90:91], v25 offset:64\n\t" \
    "s_waitcnt lgkmcnt(4)\n\t" \
    "v_smfmac_f32_16x16x64_bf16 v[124:127], v[84:87], v[240:247], v19\n\t" \
    "ds_read_b64_tr_b16 v[96:97], v25 offset:1152\n\t" \
    "ds_read_b64_tr_b16 v[98:99], v25 offset:1216\n\t" \
    "s_waitcnt lgkmcnt(4)\n\t" \
    "v_smfmac_f32_16x16x64_bf16 v[124:127], v[92:95], v[248:255], v19\n\t" \
    "s_nop 2\n\t" \
    "v_smfmac_f32_16x16x64_bf16 v[108:111], v[84:87], a[144:151], v19\n\t" \
    "s_nop 2\n\t" \
    "v_smfmac_f32_16x16x64_bf16 v[108:111], v[92:95], a[160:167], v19\n\t" \
    "s_nop 1\n\t" \
    "v_permlane32_swap_b32 v124, v126\n\t" \
    "v_smfmac_f32_16x16x64_bf16 v[112:115], v[84:87], a[176:183], v19\n\t" \
    "s_nop 1\n\t" \
    "v_permlane32_swap_b32 v125, v127\n\t" \
    "v_smfmac_f32_16x16x64_bf16 v[112:115], v[92:95], a[192:199], v19\n\t" \
    "s_nop 0\n\t" \
    "v_add_f32 v124, v124, v126\n\t" \
    "s_waitcnt lgkmcnt(2)\n\t" \
    "v_smfmac_f32_16x16x64_bf16 v[108:111], v[88:91], a[152:159], v19\n\t" \
    "s_nop 1\n\t" \
    "v_add_f32 v125, v125, v127\n\t" \
    "v_smfmac_f32_16x16x64_bf16 v[112:115], v[88:91], a[184:191], v19\n\t" \
    "s_nop 0\n\t" \
    "v_cvt_pk_bf16_f32 v136, v124, v125\n\t" \
    "s_waitcnt lgkmcnt(0)\n\t" \
    "v_smfmac_f32_16x16x64_bf16 v[108:111], v[96:99], a[168:175], v19\n\t" \
    "s_nop 1\n\t" \
    "v_cvt_pk_bf16_f32 v137, v124, v125\n\t" \
    "v_smfmac_f32_16x16x64_bf16 v[112:115], v[96:99], a[200:207], v19\n\t" \
    "s_nop 1\n\t" \
    "v_permlane32_swap_b32 v136, v137\n\t" \
    "v_smfmac_f32_16x16x64_bf16 v[116:119], v[84:87], a[208:215], v19\n\t" \
    "s_mov_b32 exec_hi, 0\n\t" \
    "global_store_dwordx2 v16, v[136:137], s[38:39] sc1\n\t" \
    "s_mov_b32 exec_hi, -1\n\t" \
    "v_cvt_pk_bf16_f32 v128, v108, v109\n\t" \
    "v_smfmac_f32_16x16x64_bf16 v[116:119], v[88:91], a[216:223], v19\n\t" \
    "s_nop 1\n\t" \
    "v_cvt_pk_bf16_f32 v129, v110, v111\n\t" \
    "v_smfmac_f32_16x16x64_bf16 v[116:119], v[92:95], a[224:231], v19\n\t" \
    "s_nop 1\n\t" \
    "v_cvt_pk_bf16_f32 v130, v112, v113\n\t" \
    "v_smfmac_f32_16x16x64_bf16 v[116:119], v[96:99], a[232:239], v19\n\t" \
    "s_nop 1\n\t" \
    "v_cvt_pk_bf16_f32 v131, v114, v115\n\t" \
    "v_smfmac_f32_16x16x64_bf16 v[120:123], v[84:87], a[240:247], v19\n\t" \
    "s_nop 1\n\t" \
    "v_permlane16_swap_b32 v128, v130\n\t" \
    "v_smfmac_f32_16x16x64_bf16 v[120:123], v[88:91], a[248:255], v19\n\t" \
    "s_nop 1\n\t" \
    "v_permlane16_swap_b32 v129, v131\n\t" \
    "v_smfmac_f32_16x16x64_bf16 v[120:123], v[92:95], v[224:231], v19\n\t" \
    "s_nop 0\n\t" \
    "global_store_dwordx4 v14, v[128:131], s[38:39] sc1\n\t" \
    "v_cvt_pk_bf16_f32 v132, v116, v117\n\t" \
    "v_smfmac_f32_16x16x64_bf16 v[120:123], v[96:99], v[232:239], v19\n\t" \
    "s_nop 1\n\t" \
    "v_cvt_pk_bf16_f32 v133, v118, v119\n\t" \
    "s_nop 4\n\t" \
    "v_cvt_pk_bf16_f32 v134, v120, v121\n\t" \
    "v_cvt_pk_bf16_f32 v135, v122, v123\n\t" \
    "s_nop 0\n\t" \
    "v_permlane16_swap_b32 v132, v134\n\t" \
    "v_permlane16_swap_b32 v133, v135\n\t" \
    "global_store_dwordx4 v15, v[132:135], s[38:39] sc1\n\t" \
    "s_cmp_eq_u32 s65, 63\n\t" \
    "s_cbranch_scc1 vv_end40_%=\n\t" \
    "s_add_u32 s65, s65, 1\n\t" \
    "v_mov_b32 v72, 0\n\t" \
    "v_mov_b32 v73, 0\n\t" \
    "v_mov_b32 v74, 0\n\t" \
    "v_mov_b32 v75, 0\n\t" \
    "v_mov_b32 v76, 0\n\t" \
    "v_mov_b32 v77, 0\n\t" \
    "v_mov_b32 v78, 0\n\t" \
    "v_mov_b32 v79, 0\n\t" \
    "v_mov_b32 v108, 0\n\t" \
    "v_mov_b32 v109, 0\n\t" \
    "v_mov_b32 v110, 0\n\t" \
    "v_mov_b32 v111, 0\n\t" \
    "v_mov_b32 v112, 0\n\t" \
    "v_mov_b32 v113, 0\n\t" \
    "v_mov_b32 v114, 0\n\t" \
    "v_mov_b32 v115, 0\n\t" \
    "v_mov_b32 v116, 0\n\t" \
    "v_mov_b32 v117, 0\n\t" \
    "v_mov_b32 v118, 0\n\t" \
    "v_mov_b32 v119, 0\n\t" \
    "v_mov_b32 v120, 0\n\t" \
    "v_mov_b32 v121, 0\n\t" \
    "v_mov_b32 v122, 0\n\t" \
    "v_mov_b32 v123, 0\n\t" \
    "v_mov_b32 v124, 0\n\t" \
    "v_mov_b32 v125, 0\n\t" \
    "v_mov_b32 v126, 0\n\t" \
    "v_mov_b32 v127, 0\n\t" \
    "s_add_u32 s69, s65, 0\n\t" \
    "s_and_b32 s69, s69, 7\n\t" \
    "s_lshl_b32 s69, s69, 18\n\t" \
    "s_add_u32 s69, s69, s62\n\t" \
    "s_add_u32 s46, s36, s69\n\t" \
    "s_addc_u32 s47, s37, 0\n\t" \
    "s_add_u32 s70, s65, 0\n\t" \
    "s_mul_i32 s69, s70, 171\n\t" \
    "s_lshr_b32 s69, s69, 9\n\t" \
    "s_mul_i32 s70, s70, 589824\n\t" \
    "s_mul_i32 s71, s69, 327680\n\t" \
    "s_add_u32 s70, s70, s71\n\t" \
    "s_lshl1_add_u32 s69, s69, 4\n\t" \
    "s_bitcmp1_b64 s[44:45], s69\n\t" \
    "s_cselect_b32 s69, 8192, 0\n\t" \
    "s_add_u32 s69, s69, s70\n\t" \
    "s_add_u32 s69, s69, 123731968\n\t" \
    "s_add_u32 s38, s36, s69\n\t" \
    "s_addc_u32 s39, s37, 0\n\t" \
    "s_and_b32 s69, s69, 0xffe00000\n\t" \
    "s_add_u32 s69, s69, 1823744\n\t" \
    "v_mov_b32 v17, s69\n\t" \
    "global_load_dword v17, v17, s[36:37]\n\t" \
    "s_barrier\n\t" \
    "ds_read_b32 v160, v11 offset:20480\n\t" \
    "s_waitcnt lgkmcnt(0)\n\t" \
    "v_readfirstlane_b32 s70, v160\n\t" \
    "s_branch vv_loop11_%=\n\t" \
    "vv_Gbad45_%=:\n\t" \
    "s_waitcnt lgkmcnt(0)\n\t" \
    "s_nop 7\n\t" \
    "v_mov_b32 v72, 0\n\t" \
    "v_mov_b32 v73, 0\n\t" \
    "v_mov_b32 v74, 0\n\t" \
    "v_mov_b32 v75, 0\n\t" \
    "v_mov_b32 v76, 0\n\t" \
    "v_mov_b32 v77, 0\n\t" \
    "v_mov_b32 v78, 0\n\t" \
    "v_mov_b32 v79, 0\n\t" \
    "s_cmp_eq_u32 s68, 0\n\t" \
    "s_cbranch_scc1 vv_F49_%=\n\t" \
    "s_sub_u32 s68, s68, 1\n\t" \
    "s_branch vv_G44_%=\n\t" \
    "vv_F49_%=:\n\t" \
    "v_mov_b32 v140, 5767424\n\t" \
    "v_mov_b32 v141, 1\n\t" \
    "global_store_dword v140, v141, s[36:37] sc1\n\t" \
    "s_waitcnt vmcnt(0)\n\t" \
    "s_branch vv_Gc46_%=\n\t" \
    "vv_Gslow47_%=:\n\t" \
    "v_max_u32 v140, v36, v37\n\t" \
    "v_max3_u32 v140, v140, v38, v39\n\t" \
    "v_max3_u32 v140, v140, v40, v41\n\t" \
    "v_max3_u32 v140, v140, v42, v43\n\t" \
    "v_max3_u32 v140, v140, v44, v45\n\t" \
    "v_max3_u32 v140, v140, v46, v47\n\t" \
    "v_max3_u32 v140, v140, v48, v49\n\t" \
    "v_max3_u32 v140, v140, v50, v51\n\t" \
    "v_max3_u32 v140, v140, v52, v53\n\t" \
    "v_max3_u32 v140, v140, v54, v55\n\t" \
    "v_max3_u32 v140, v140, v56, v57\n\t" \
    "v_max3_u32 v140, v140, v58, v59\n\t" \
    "v_max3_u32 v140, v140, v60, v61\n\t" \
    "v_max3_u32 v140, v140, v62, v63\n\t" \
    "v_max3_u32 v140, v140, v64, v65\n\t" \
    "v_max3_u32 v140, v140, v66, v67\n\t" \
    "v_max3_u32 v140, v140, v68, v69\n\t" \
    "v_max3_u32 v140, v140, v70, v71\n\t" \
    "v_cmp_eq_u32 vcc, -1, v140\n\t" \
    "s_cbranch_vccz vv_Gback48_%=\n\t" \
    "s_waitcnt lgkmcnt(0)\n\t" \
    "v_sub_u32 v140, v1, v3\n\t" \
    "s_nop 0\n\t" \
    "v_readfirstlane_b32 s69, v140\n\t" \
    "v_add_u32 v143, 20480, v0\n\t" \
    "s_nop 7\n\t" \
    "s_add_u32 s30, s46, s69\n\t" \
    "s_addc_u32 s31, s47, 0\n\t" \
    "vv_Gag50_%=:\n\t" \
    "global_load_dwordx4 v[36:39], v1, s[46:47] sc1\n\t" \
    "global_load_dwordx4 v[40:43], v23, s[46:47] sc1\n\t" \
    "global_load_dwordx4 v[44:47], v143, s[30:31] sc1\n\t" \
    "global_load_dwordx4 v[48:51], v4, s[30:31] sc1\n\t" \
    "global_load_dwordx4 v[52:55], v5, s[30:31] sc1\n\t" \
    "global_load_dwordx4 v[56:59], v6, s[30:31] sc1\n\t" \
    "global_load_dwordx4 v[60:63], v7, s[30:31] sc1\n\t" \
    "global_load_dwordx4 v[64:67], v8, s[30:31] sc1\n\t" \
    "global_load_dwordx4 v[68:71], v9, s[30:31] sc1\n\t" \
    "s_waitcnt vmcnt(0)\n\t" \
    "v_max_u32 v140, v36, v37\n\t" \
    "v_max3_u32 v140, v140, v38, v39\n\t" \
    "v_max3_u32 v140, v140, v40, v41\n\t" \
    "v_max3_u32 v140, v140, v42, v43\n\t" \
    "v_max3_u32 v140, v140, v44, v45\n\t" \
    "v_max3_u32 v140, v140, v46, v47\n\t" \
    "v_max3_u32 v140, v140, v48, v49\n\t" \
    "v_max3_u32 v140, v140, v50, v51\n\t" \
    "v_max3_u32 v140, v140, v52, v53\n\t" \
    "v_max3_u32 v140, v140, v54, v55\n\t" \
    "v_max3_u32 v140, v140, v56, v57\n\t" \
    "v_max3_u32 v140, v140, v58, v59\n\t" \
    "v_max3_u32 v140, v140, v60, v61\n\t" \
    "v_max3_u32 v140, v140, v62, v63\n\t" \
    "v_max3_u32 v140, v140, v64, v65\n\t" \
    "v_max3_u32 v140, v140, v66, v67\n\t" \
    "v_max3_u32 v140, v140, v68, v69\n\t" \
    "v_max3_u32 v140, v140, v70, v71\n\t" \
    "v_cmp_eq_u32 vcc, -1, v140\n\t" \
    "s_cbranch_vccz vv_Gredo51_%=\n\t" \
    "s_cmp_eq_u32 s68, 0\n\t" \
    "s_cbranch_scc1 vv_F52_%=\n\t" \
    "s_sub_u32 s68, s68, 1\n\t" \
    "s_sleep 1\n\t" \
    "s_branch vv_Gag50_%=\n\t" \
    "vv_F52_%=:\n\t" \
    "v_mov_b32 v140, 5767424\n\t" \
    "v_mov_b32 v141, 2\n\t" \
    "global_store_dword v140, v141, s[36:37] sc1\n\t" \
    "s_waitcnt vmcnt(0)\n\t" \
    "s_branch vv_Gredo51_%=\n\t" \
    "vv_Gredo51_%=:\n\t" \
    "v_mov_b32 v72, 0\n\t" \
    "v_mov_b32 v73, 0\n\t" \
    "v_mov_b32 v74, 0\n\t" \
    "v_mov_b32 v75, 0\n\t" \
    "v_mov_b32 v76, 0\n\t" \
    "v_mov_b32 v77, 0\n\t" \
    "v_mov_b32 v78, 0\n\t" \
    "v_mov_b32 v79, 0\n\t" \
    "v_smfmac_f32_16x16x64_bf16 v[72:75], v[36:39], a[32:39], v10\n\t" \
    "v_smfmac_f32_16x16x64_bf16 v[72:75], v[40:43], a[40:47], v10\n\t" \
    "v_smfmac_f32_16x16x64_bf16 v[72:75], v[44:47], a[64:71], v10\n\t" \
    "v_smfmac_f32_16x16x64_bf16 v[72:75], v[48:51], a[48:55], v10\n\t" \
    "v_smfmac_f32_16x16x64_bf16 v[72:75], v[52:55], a[56:63], v10\n\t" \
    "v_smfmac_f32_16x16x64_bf16 v[72:75], v[56:59], a[0:7], v10\n\t" \
    "v_smfmac_f32_16x16x64_bf16 v[72:75], v[60:63], a[8:15], v10\n\t" \
    "v_smfmac_f32_16x16x64_bf16 v[72:75], v[64:67], a[16:23], v10\n\t" \
    "v_smfmac_f32_16x16x64_bf16 v[72:75], v[68:71], a[24:31], v10\n\t" \
    "v_smfmac_f32_16x16x64_bf16 v[76:79], v[36:39], a[104:111], v10\n\t" \
    "v_smfmac_f32_16x16x64_bf16 v[76:79], v[40:43], a[112:119], v10\n\t" \
    "v_smfmac_f32_16x16x64_bf16 v[76:79], v[44:47], a[136:143], v10\n\t" \
    "v_smfmac_f32_16x16x64_bf16 v[76:79], v[48:51], a[120:127], v10\n\t" \
    "v_smfmac_f32_16x16x64_bf16 v[76:79], v[52:55], a[128:135], v10\n\t" \
    "v_smfmac_f32_16x16x64_bf16 v[76:79], v[56:59], a[72:79], v10\n\t" \
    "v_smfmac_f32_16x16x64_bf16 v[76:79], v[60:63], a[80:87], v10\n\t" \
    "v_smfmac_f32_16x16x64_bf16 v[76:79], v[64:67], a[88:95], v10\n\t" \
    "v_smfmac_f32_16x16x64_bf16 v[76:79], v[68:71], a[96:103], v10\n\t" \
    "v_add_f32 v72, v72, v74\n\t" \
    "v_add_f32 v73, v73, v75\n\t" \
    "v_cvt_pk_bf16_f32 v80, v72, v73\n\t" \
    "ds_write_b32 v12, v80 offset:0\n\t" \
    "s_nop 3\n\t" \
    "v_pk_add_f32 v[76:77], v[76:77], v[78:79]\n\t" \
    "v_cvt_pk_bf16_f32 v81, v76, v77\n\t" \
    "ds_write_b32 v12, v81 offset:256\n\t" \
    "s_branch vv_Gback48_%=\n\t" \
    "vv_end40_%=:\n\t" \
    "s_endpgm\n\t" \
    ".p2align 6\n\t" \
    "s_nop 0\n\t" \
    "s_nop 0\n\t" \
    "s_nop 0\n\t" \
    "s_nop 0\n\t" \
    "s_nop 0\n\t" \
    "s_nop 0\n\t" \
    "vv_loop12_%=:\n\t" \
    "s_getpc_b64 s[30:31]\n\t" \
    "vv_Wp55_%=:\n\t" \
    "s_add_u32 s30, s30, vv_We56_%=-vv_Wp55_%=\n\t" \
    "s_addc_u32 s31, s31, 0\n\t" \
    "s_sub_u32 s70, s70, 96\n\t" \
    "vv_W54_%=:\n\t" \
    "s_memtime s[56:57]\n\t" \
    "s_waitcnt lgkmcnt(0)\n\t" \
    "s_sub_u32 s69, s70, s56\n\t" \
    "s_cmp_gt_i32 s69, 80\n\t" \
    "s_cbranch_scc1 vv_W54_%=\n\t" \
    "s_max_i32 s69, s69, 0\n\t" \
    "s_and_b32 s69, s69, -4\n\t" \
    "s_sub_u32 s30, s30, s69\n\t" \
    "s_subb_u32 s31, s31, 0\n\t" \
    "s_setpc_b64 s[30:31]\n\t" \
    "s_nop 0\n\t" \
    "s_nop 0\n\t" \
    "s_nop 0\n\t" \
    "s_nop 0\n\t" \
    "s_nop 0\n\t" \
    "s_nop 0\n\t" \
    "s_nop 0\n\t" \
    "s_nop 0\n\t" \
    "s_nop 0\n\t" \
    "s_nop 0\n\t" \
    "s_nop 0\n\t" \
    "s_nop 0\n\t" \
    "s_nop 0\n\t" \
    "s_nop 0\n\t" \
    "s_nop 0\n\t" \
    "s_nop 0\n\t" \
    "s_nop 0\n\t" \
    "s_nop 0\n\t" \
    "s_nop 0\n\t" \
    "s_nop 0\n\t" \
    "vv_We56_%=:\n\t" \
    "vv_G57_%=:\n\t" \
    "global_load_dwordx4 v[36:39], v1, s[46:47] sc1\n\t" \
    "global_load_dword v141, v2, s[46:47] sc1\n\t" \
    "global_load_dwordx4 v[40:43], v23, s[46:47] sc1\n\t" \
    "s_waitcnt vmcnt(2)\n\t" \
    "ds_write_b128 v3, v[36:39]\n\t" \
    "v_smfmac_f32_16x16x64_bf16 v[72:75], v[36:39], a[48:55], v10\n\t" \
    "s_waitcnt vmcnt(1)\n\t" \
    "ds_write_b32 v22, v141 offset:20224\n\t" \
    "s_waitcnt vmcnt(0)\n\t" \
    "ds_write_b128 v3, v[40:43] offset:1024\n\t" \
    "v_max3_u32 v140, v36, v40, v141\n\t" \
    "v_cmp_eq_u32 vcc, -1, v140\n\t" \
    "s_cbranch_vccnz vv_Gbad58_%=\n\t" \
    "vv_Gc59_%=:\n\t" \
    "s_waitcnt lgkmcnt(0)\n\t" \
    "s_barrier\n\t" \
    "ds_read_b128 v[48:51], v4\n\t" \
    "ds_read_b128 v[52:55], v5\n\t" \
    "v_smfmac_f32_16x16x64_bf16 v[76:79], v[36:39], a[120:127], v10\n\t" \
    "ds_read_b128 v[56:59], v6\n\t" \
    "ds_read_b128 v[60:63], v7\n\t" \
    "v_smfmac_f32_16x16x64_bf16 v[72:75], v[40:43], a[56:63], v10\n\t" \
    "ds_read_b128 v[64:67], v8\n\t" \
    "v_smfmac_f32_16x16x64_bf16 v[76:79], v[40:43], a[128:135], v10\n\t" \
    "ds_read_b128 v[68:71], v9\n\t" \
    "s_waitcnt lgkmcnt(5)\n\t" \
    "v_smfmac_f32_16x16x64_bf16 v[72:75], v[48:51], a[0:7], v10\n\t" \
    "ds_read_b128 v[44:47], v0 offset:19456\n\t" \
    "s_waitcnt lgkmcnt(5)\n\t" \
    "v_smfmac_f32_16x16x64_bf16 v[72:75], v[52:55], a[8:15], v10\n\t" \
    "s_waitcnt lgkmcnt(4)\n\t" \
    "v_smfmac_f32_16x16x64_bf16 v[72:75], v[56:59], a[16:23], v10\n\t" \
    "s_waitcnt lgkmcnt(3)\n\t" \
    "v_smfmac_f32_16x16x64_bf16 v[72:75], v[60:63], a[24:31], v10\n\t" \
    "s_waitcnt lgkmcnt(2)\n\t" \
    "v_smfmac_f32_16x16x64_bf16 v[72:75], v[64:67], a[32:39], v10\n\t" \
    "s_waitcnt lgkmcnt(1)\n\t" \
    "v_smfmac_f32_16x16x64_bf16 v[72:75], v[68:71], a[40:47], v10\n\t" \
    "s_waitcnt lgkmcnt(0)\n\t" \
    "v_smfmac_f32_16x16x64_bf16 v[72:75], v[44:47], a[64:71], v10\n\t" \
    "s_nop 2\n\t" \
    "v_smfmac_f32_16x16x64_bf16 v[76:79], v[48:51], a[72:79], v10\n\t" \
    "s_nop 2\n\t" \
    "v_smfmac_f32_16x16x64_bf16 v[76:79], v[52:55], a[80:87], v10\n\t" \
    "v_add_f32 v72, v72, v74\n\t" \
    "v_smfmac_f32_16x16x64_bf16 v[76:79], v[56:59], a[88:95], v10\n\t" \
    "v_add_f32 v73, v73, v75\n\t" \
    "v_smfmac_f32_16x16x64_bf16 v[76:79], v[60:63], a[96:103], v10\n\t" \
    "v_cvt_pk_bf16_f32 v80, v72, v73\n\t" \
    "v_smfmac_f32_16x16x64_bf16 v[76:79], v[64:67], a[104:111], v10\n\t" \
    "ds_write_b32 v12, v80 offset:0\n\t" \
    "v_smfmac_f32_16x16x64_bf16 v[76:79], v[68:71], a[112:119], v10\n\t" \
    "v_smfmac_f32_16x16x64_bf16 v[76:79], v[44:47], a[136:143], v10\n\t" \
    "s_nop 6\n\t" \
    "v_cmp_u_f32 vcc, v72, v73\n\t" \
    "v_pk_add_f32 v[76:77], v[76:77], v[78:79]\n\t" \
    "v_cvt_pk_bf16_f32 v81, v76, v77\n\t" \
    "ds_write_b32 v12, v81 offset:256\n\t" \
    "s_cbranch_vccnz vv_Gslow60_%=\n\t" \
    "vv_Gback61_%=:\n\t" \
    "s_waitcnt lgkmcnt(0)\n\t" \
    "s_barrier\n\t" \
    "ds_read_b64_tr_b16 v[84:85], v13 offset:0\n\t" \
    "v_smfmac_f32_16x16x64_bf16 v[100:103], v[36:39], a[0:7], v10\n\t" \
    "ds_read_b64_tr_b16 v[86:87], v13 offset:64\n\t" \
    "ds_read_b64_tr_b16 v[92:93], v13 offset:1152\n\t" \
    "v_smfmac_f32_16x16x64_bf16 v[100:103], v[36:39], a[0:7], v10\n\t" \
    "ds_read_b64_tr_b16 v[94:95], v13 offset:1216\n\t" \
    "ds_read_b64_tr_b16 v[88:89], v25 offset:0\n\t" \
    "v_smfmac_f32_16x16x64_bf16 v[100:103], v[36:39], a[0:7], v10\n\t" \
    "ds_read_b64_tr_b16 v[90:91], v25 offset:64\n\t" \
    "s_waitcnt lgkmcnt(4)\n\t" \
    "v_smfmac_f32_16x16x64_bf16 v[124:127], v[84:87], v[240:247], v19\n\t" \
    "ds_read_b64_tr_b16 v[96:97], v25 offset:1152\n\t" \
    "ds_read_b64_tr_b16 v[98:99], v25 offset:1216\n\t" \
    "s_waitcnt lgkmcnt(4)\n\t" \
    "v_smfmac_f32_16x16x64_bf16 v[124:127], v[92:95], v[248:255], v19\n\t" \
    "s_nop 2\n\t" \
    "v_smfmac_f32_16x16x64_bf16 v[108:111], v[84:87], a[144:151], v19\n\t" \
    "s_nop 2\n\t" \
    "v_smfmac_f32_16x16x64_bf16 v[108:111], v[92:95], a[160:167], v19\n\t" \
    "s_nop 1\n\t" \
    "v_permlane32_swap_b32 v124, v126\n\t" \
    "v_smfmac_f32_16x16x64_bf16 v[112:115], v[84:87], a[176:183], v19\n\t" \
    "s_nop 1\n\t" \
    "v_permlane32_swap_b32 v125, v127\n\t" \
    "v_smfmac_f32_16x16x64_bf16 v[112:115], v[92:95], a[192:199], v19\n\t" \
    "s_nop 0\n\t" \
    "v_add_f32 v124, v124, v126\n\t" \
    "s_waitcnt lgkmcnt(2)\n\t" \
    "v_smfmac_f32_16x16x64_bf16 v[108:111], v[88:91], a[152:159], v19\n\t" \
    "s_nop 1\n\t" \
    "v_add_f32 v125, v125, v127\n\t" \
    "v_smfmac_f32_16x16x64_bf16 v[112:115], v[88:91], a[184:191], v19\n\t" \
    "s_nop 0\n\t" \
    "v_cvt_pk_bf16_f32 v136, v124, v125\n\t" \
    "s_waitcnt lgkmcnt(0)\n\t" \
    "v_smfmac_f32_16x16x64_bf16 v[108:111], v[96:99], a[168:175], v19\n\t" \
    "s_nop 1\n\t" \
    "v_cvt_pk_bf16_f32 v137, v124, v125\n\t" \
    "v_smfmac_f32_16x16x64_bf16 v[112:115], v[96:99], a[200:207], v19\n\t" \
    "s_nop 1\n\t" \
    "v_permlane32_swap_b32 v136, v137\n\t" \
    "v_smfmac_f32_16x16x64_bf16 v[116:119], v[84:87], a[208:215], v19\n\t" \
    "s_mov_b32 exec_hi, 0\n\t" \
    "global_store_dwordx2 v16, v[136:137], s[38:39] sc1\n\t" \
    "s_mov_b32 exec_hi, -1\n\t" \
    "v_cvt_pk_bf16_f32 v128, v108, v109\n\t" \
    "v_smfmac_f32_16x16x64_bf16 v[116:119], v[88:91], a[216:223], v19\n\t" \
    "s_nop 1\n\t" \
    "v_cvt_pk_bf16_f32 v129, v110, v111\n\t" \
    "v_smfmac_f32_16x16x64_bf16 v[116:119], v[92:95], a[224:231], v19\n\t" \
    "s_nop 1\n\t" \
    "v_cvt_pk_bf16_f32 v130, v112, v113\n\t" \
    "v_smfmac_f32_16x16x64_bf16 v[116:119], v[96:99], a[232:239], v19\n\t" \
    "s_nop 1\n\t" \
    "v_cvt_pk_bf16_f32 v131, v114, v115\n\t" \
    "v_smfmac_f32_16x16x64_bf16 v[120:123], v[84:87], a[240:247], v19\n\t" \
    "s_nop 1\n\t" \
    "v_permlane16_swap_b32 v128, v130\n\t" \
    "v_smfmac_f32_16x16x64_bf16 v[120:123], v[88:91], a[248:255], v19\n\t" \
    "s_nop 1\n\t" \
    "v_permlane16_swap_b32 v129, v131\n\t" \
    "v_smfmac_f32_16x16x64_bf16 v[120:123], v[92:95], v[224:231], v19\n\t" \
    "s_nop 0\n\t" \
    "global_store_dwordx4 v14, v[128:131], s[38:39] sc1\n\t" \
    "v_cvt_pk_bf16_f32 v132, v116, v117\n\t" \
    "v_smfmac_f32_16x16x64_bf16 v[120:123], v[96:99], v[232:239], v19\n\t" \
    "s_nop 1\n\t" \
    "v_cvt_pk_bf16_f32 v133, v118, v119\n\t" \
    "s_nop 4\n\t" \
    "v_cvt_pk_bf16_f32 v134, v120, v121\n\t" \
    "v_cvt_pk_bf16_f32 v135, v122, v123\n\t" \
    "s_nop 0\n\t" \
    "v_permlane16_swap_b32 v132, v134\n\t" \
    "v_permlane16_swap_b32 v133, v135\n\t" \
    "global_store_dwordx4 v15, v[132:135], s[38:39] sc1\n\t" \
    "global_load_dword v140, v20, s[52:53] sc1\n\t" \
    "s_cmp_eq_u32 s65, 63\n\t" \
    "s_cbranch_scc1 vv_end53_%=\n\t" \
    "s_cmp_lg_u32 s65, 0\n\t" \
    "s_cbranch_scc1 vv_ng62_%=\n\t" \
    "v_mov_b32 v160, 5838848\n\t" \
    "v_mov_b32 v161, 1\n\t" \
    "s_mov_b64 exec, 1\n\t" \
    "global_atomic_add v162, v160, v161, s[36:37] sc0 sc1\n\t" \
    "s_mov_b64 exec, -1\n\t" \
    "v_mbcnt_lo_u32_b32 v160, -1, 0\n\t" \
    "v_and_b32 v161, 3, v160\n\t" \
    "v_lshlrev_b32 v161, 16, v161\n\t" \
    "v_bfe_u32 v163, v160, 2, 2\n\t" \
    "v_lshl_add_u32 v161, v163, 11, v161\n\t" \
    "v_bfe_u32 v163, v160, 4, 1\n\t" \
    "v_lshl_add_u32 v161, v163, 14, v161\n\t" \
    "v_add_u32 v161, s63, v161\n\t" \
    "s_waitcnt vmcnt(0)\n\t" \
    "v_readfirstlane_b32 s69, v162\n\t" \
    "s_nop 1\n\t" \
    "s_cmp_lg_u32 s69, 126\n\t" \
    "s_cbranch_scc1 vv_go63_%=\n\t" \
    "s_memrealtime s[56:57]\n\t" \
    "s_waitcnt lgkmcnt(0)\n\t" \
    "s_lshl_b32 s69, s56, 4\n\t" \
    "s_add_u32 s69, s69, 2480\n\t" \
    "s_add_u32 s70, s56, 24\n\t" \
    "s_lshr_b32 s70, s70, 4\n\t" \
    "v_lshrrev_b32 v163, 2, v160\n\t" \
    "v_add_u32 v163, s70, v163\n\t" \
    "v_and_b32 v163, 63, v163\n\t" \
    "v_lshlrev_b32 v163, 6, v163\n\t" \
    "v_and_b32 v162, 3, v160\n\t" \
    "v_lshl_add_u32 v163, v162, 12, v163\n\t" \
    "v_add_u32 v163, 5865472, v163\n\t" \
    "v_mov_b32 v160, s69\n\t" \
    "s_mov_b32 exec_hi, 0\n\t" \
    "global_store_dword v163, v160, s[36:37] sc1\n\t" \
    "global_store_dword v161, v160, s[36:37] sc1\n\t" \
    "s_mov_b32 exec_hi, -1\n\t" \
    "vv_go63_%=:\n\t" \
    "vv_ng62_%=:\n\t" \
    "s_waitcnt lgkmcnt(0)\n\t" \
    "s_mov_b32 s70, 116\n\t" \
    "vv_E67_%=:\n\t" \
    "s_memtime s[56:57]\n\t" \
    "s_memrealtime s[2:3]\n\t" \
    "s_nop 0\n\t" \
    "s_memrealtime s[4:5]\n\t" \
    "s_nop 0\n\t" \
    "s_memrealtime s[6:7]\n\t" \
    "s_nop 0\n\t" \
    "s_memrealtime s[8:9]\n\t" \
    "s_nop 0\n\t" \
    "s_memrealtime s[10:11]\n\t" \
    "s_nop 0\n\t" \
    "s_memrealtime s[12:13]\n\t" \
    "s_nop 0\n\t" \
    "s_memrealtime s[14:15]\n\t" \
    "s_nop 0\n\t" \
    "s_memrealtime s[16:17]\n\t" \
    "s_nop 0\n\t" \
    "s_memrealtime s[18:19]\n\t" \
    "s_nop 0\n\t" \
    "s_memrealtime s[20:21]\n\t" \
    "s_nop 0\n\t" \
    "s_memrealtime s[22:23]\n\t" \
    "s_nop 0\n\t" \
    "s_memrealtime s[24:25]\n\t" \
    "s_nop 0\n\t" \
    "s_memrealtime s[26:27]\n\t" \
    "s_nop 0\n\t" \
    "s_memrealtime s[28:29]\n\t" \
    "s_memtime s[54:55]\n\t" \
    "s_add_u32 s70, s70, 8\n\t" \
    "s_waitcnt lgkmcnt(0)\n\t" \
    "s_sub_u32 s69, s54, s56\n\t" \
    "s_cmp_gt_u32 s69, s70\n\t" \
    "s_cbranch_scc1 vv_E67_%=\n\t" \
    "s_add_u32 s2, s2, s4\n\t" \
    "s_add_u32 s6, s6, s8\n\t" \
    "s_add_u32 s10, s10, s12\n\t" \
    "s_add_u32 s14, s14, s16\n\t" \
    "s_add_u32 s18, s18, s20\n\t" \
    "s_add_u32 s22, s22, s24\n\t" \
    "s_add_u32 s2, s2, s6\n\t" \
    "s_add_u32 s10, s10, s14\n\t" \
    "s_add_u32 s18, s18, s22\n\t" \
    "s_add_u32 s2, s2, s26\n\t" \
    "s_add_u32 s10, s10, s18\n\t" \
    "s_add_u32 s2, s2, s10\n\t" \
    "s_mul_i32 s70, s28, 13\n\t" \
    "s_sub_u32 s70, s70, s2\n\t" \
    "s_min_u32 s70, s70, 52\n\t" \
    "s_mul_i32 s69, s69, s70\n\t" \
    "s_mul_hi_u32 s69, s69, 76695844\n\t" \
    "s_mov_b32 s75, s73\n\t" \
    "s_mov_b32 s76, s74\n\t" \
    "s_add_u32 s73, s56, s69\n\t" \
    "s_lshl_b32 s74, s28, 4\n\t" \
    "s_sub_u32 s69, s73, s75\n\t" \
    "s_sub_u32 s70, s74, s76\n\t" \
    "s_sub_u32 s71, s70, 1600\n\t" \
    "s_cmp_lt_u32 s71, 400000\n\t" \
    "s_cbranch_scc0 vv_K68_%=\n\t" \
    "v_cvt_f32_u32 v160, s69\n\t" \
    "v_cvt_f32_u32 v161, s70\n\t" \
    "v_rcp_f32 v162, v160\n\t" \
    "v_rcp_f32 v163, v161\n\t" \
    "v_mul_f32 v162, v162, v161\n\t" \
    "v_mul_f32 v163, v163, v160\n\t" \
    "v_mul_f32 v162, 0x4f800000, v162\n\t" \
    "v_mul_f32 v163, 0x4f000000, v163\n\t" \
    "v_cvt_u32_f32 v162, v162\n\t" \
    "v_cvt_u32_f32 v163, v163\n\t" \
    "v_readfirstlane_b32 s69, v162\n\t" \
    "v_readfirstlane_b32 s70, v163\n\t" \
    "s_nop 1\n\t" \
    "s_min_u32 s71, s65, 1\n\t" \
    "s_sub_u32 s69, s69, s78\n\t" \
    "s_ashr_i32 s69, s69, s71\n\t" \
    "s_add_u32 s78, s78, s69\n\t" \
    "s_sub_u32 s70, s70, s77\n\t" \
    "s_ashr_i32 s70, s70, s71\n\t" \
    "s_add_u32 s77, s77, s70\n\t" \
    "vv_K68_%=:\n\t" \
    "s_add_u32 s65, s65, 1\n\t" \
    "v_mov_b32 v72, 0\n\t" \
    "v_mov_b32 v73, 0\n\t" \
    "v_mov_b32 v74, 0\n\t" \
    "v_mov_b32 v75, 0\n\t" \
    "v_mov_b32 v76, 0\n\t" \
    "v_mov_b32 v77, 0\n\t" \
    "v_mov_b32 v78, 0\n\t" \
    "v_mov_b32 v79, 0\n\t" \
    "v_mov_b32 v108, 0\n\t" \
    "v_mov_b32 v109, 0\n\t" \
    "v_mov_b32 v110, 0\n\t" \
    "v_mov_b32 v111, 0\n\t" \
    "v_mov_b32 v112, 0\n\t" \
    "v_mov_b32 v113, 0\n\t" \
    "v_mov_b32 v114, 0\n\t" \
    "v_mov_b32 v115, 0\n\t" \
    "v_mov_b32 v116, 0\n\t" \
    "v_mov_b32 v117, 0\n\t" \
    "v_mov_b32 v118, 0\n\t" \
    "v_mov_b32 v119, 0\n\t" \
    "v_mov_b32 v120, 0\n\t" \
    "v_mov_b32 v121, 0\n\t" \
    "v_mov_b32 v122, 0\n\t" \
    "v_mov_b32 v123, 0\n\t" \
    "v_mov_b32 v124, 0\n\t" \
    "v_mov_b32 v125, 0\n\t" \
    "v_mov_b32 v126, 0\n\t" \
    "v_mov_b32 v127, 0\n\t" \
    "s_add_u32 s69, s65, 0\n\t" \
    "s_and_b32 s69, s69, 7\n\t" \
    "s_lshl_b32 s69, s69, 18\n\t" \
    "s_add_u32 s69, s69, s62\n\t" \
    "s_add_u32 s46, s36, s69\n\t" \
    "s_addc_u32 s47, s37, 0\n\t" \
    "s_add_u32 s70, s65, 0\n\t" \
    "s_mul_i32 s69, s70, 171\n\t" \
    "s_lshr_b32 s69, s69, 9\n\t" \
    "s_mul_i32 s70, s70, 589824\n\t" \
    "s_mul_i32 s71, s69, 327680\n\t" \
    "s_add_u32 s70, s70, s71\n\t" \
    "s_lshl1_add_u32 s69, s69, 4\n\t" \
    "s_bitcmp1_b64 s[44:45], s69\n\t" \
    "s_cselect_b32 s69, 8192, 0\n\t" \
    "s_add_u32 s69, s69, s70\n\t" \
    "s_add_u32 s69, s69, 123731968\n\t" \
    "s_add_u32 s38, s36, s69\n\t" \
    "s_addc_u32 s39, s37, 0\n\t" \
    "s_and_b32 s69, s69, 0xffe00000\n\t" \
    "s_add_u32 s69, s69, 1823744\n\t" \
    "v_mov_b32 v17, s69\n\t" \
    "global_load_dword v17, v17, s[36:37]\n\t" \
    "s_waitcnt vmcnt(0)\n\t" \
    "v_cmp_eq_u32 vcc, -1, v140\n\t" \
    "s_cbranch_vccz vv_Hok65_%=\n\t" \
    "vv_H64_%=:\n\t" \
    "global_load_dword v140, v20, s[52:53] sc1\n\t" \
    "s_waitcnt vmcnt(0)\n\t" \
    "v_cmp_eq_u32 vcc, -1, v140\n\t" \
    "s_cbranch_vccz vv_Hc66_%=\n\t" \
    "s_cmp_eq_u32 s68, 0\n\t" \
    "s_cbranch_scc1 vv_F69_%=\n\t" \
    "s_sub_u32 s68, s68, 1\n\t" \
    "s_sleep 2\n\t" \
    "s_branch vv_H64_%=\n\t" \
    "vv_F69_%=:\n\t" \
    "v_mov_b32 v141, 5767424\n\t" \
    "v_mov_b32 v142, 6\n\t" \
    "global_store_dword v141, v142, s[36:37] sc1\n\t" \
    "s_waitcnt vmcnt(0)\n\t" \
    "s_branch vv_Hc66_%=\n\t" \
    "vv_Hc66_%=:\n\t" \
    "s_memtime s[56:57]\n\t" \
    "s_waitcnt lgkmcnt(0)\n\t" \
    "s_sub_u32 s69, s56, s73\n\t" \
    "s_cmp_lt_u32 s69, 3000\n\t" \
    "s_cbranch_scc1 vv_St70_%=\n\t" \
    "s_waitcnt lgkmcnt(0)\n\t" \
    "s_mov_b32 s70, 116\n\t" \
    "vv_E71_%=:\n\t" \
    "s_memtime s[56:57]\n\t" \
    "s_memrealtime s[2:3]\n\t" \
    "s_nop 0\n\t" \
    "s_memrealtime s[4:5]\n\t" \
    "s_nop 0\n\t" \
    "s_memrealtime s[6:7]\n\t" \
    "s_nop 0\n\t" \
    "s_memrealtime s[8:9]\n\t" \
    "s_nop 0\n\t" \
    "s_memrealtime s[10:11]\n\t" \
    "s_nop 0\n\t" \
    "s_memrealtime s[12:13]\n\t" \
    "s_nop 0\n\t" \
    "s_memrealtime s[14:15]\n\t" \
    "s_nop 0\n\t" \
    "s_memrealtime s[16:17]\n\t" \
    "s_nop 0\n\t" \
    "s_memrealtime s[18:19]\n\t" \
    "s_nop 0\n\t" \
    "s_memrealtime s[20:21]\n\t" \
    "s_nop 0\n\t" \
    "s_memrealtime s[22:23]\n\t" \
    "s_nop 0\n\t" \
    "s_memrealtime s[24:25]\n\t" \
    "s_nop 0\n\t" \
    "s_memrealtime s[26:27]\n\t" \
    "s_nop 0\n\t" \
    "s_memrealtime s[28:29]\n\t" \
    "s_memtime s[54:55]\n\t" \
    "s_add_u32 s70, s70, 8\n\t" \
    "s_waitcnt lgkmcnt(0)\n\t" \
    "s_sub_u32 s69, s54, s56\n\t" \
    "s_cmp_gt_u32 s69, s70\n\t" \
    "s_cbranch_scc1 vv_E71_%=\n\t" \
    "s_add_u32 s2, s2, s4\n\t" \
    "s_add_u32 s6, s6, s8\n\t" \
    "s_add_u32 s10, s10, s12\n\t" \
    "s_add_u32 s14, s14, s16\n\t" \
    "s_add_u32 s18, s18, s20\n\t" \
    "s_add_u32 s22, s22, s24\n\t" \
    "s_add_u32 s2, s2, s6\n\t" \
    "s_add_u32 s10, s10, s14\n\t" \
    "s_add_u32 s18, s18, s22\n\t" \
    "s_add_u32 s2, s2, s26\n\t" \
    "s_add_u32 s10, s10, s18\n\t" \
    "s_add_u32 s2, s2, s10\n\t" \
    "s_mul_i32 s70, s28, 13\n\t" \
    "s_sub_u32 s70, s70, s2\n\t" \
    "s_min_u32 s70, s70, 52\n\t" \
    "s_mul_i32 s69, s69, s70\n\t" \
    "s_mul_hi_u32 s69, s69, 76695844\n\t" \
    "s_mov_b32 s75, s73\n\t" \
    "s_mov_b32 s76, s74\n\t" \
    "s_add_u32 s73, s56, s69\n\t" \
    "s_lshl_b32 s74, s28, 4\n\t" \
    "vv_St70_%=:\n\t" \
    "vv_Hok65_%=:\n\t" \
    "v_max_u32_dpp v140, v140, v140 row_ror:1 row_mask:0xf bank_mask:0xf\n\t" \
    "s_nop 1\n\t" \
    "v_max_u32_dpp v140, v140, v140 row_ror:2 row_mask:0xf bank_mask:0xf\n\t" \
    "s_nop 1\n\t" \
    "v_max_u32_dpp v140, v140, v140 row_ror:4 row_mask:0xf bank_mask:0xf\n\t" \
    "s_nop 1\n\t" \
    "v_max_u32_dpp v140, v140, v140 row_ror:8 row_mask:0xf bank_mask:0xf\n\t" \
    "s_nop 0\n\t" \
    "v_readlane_b32 s69, v140, 0\n\t" \
    "v_readlane_b32 s71, v140, 16\n\t" \
    "s_nop 1\n\t" \
    "s_max_u32 s81, s69, s71\n\t" \
    "s_mul_hi_u32 s69, s85, s78\n\t" \
    "s_add_u32 s79, s81, s69\n\t" \
    "s_add_u32 s79, s79, s87\n\t" \
    "s_cmp_ge_u32 s65, 53\n\t" \
    "s_cselect_b32 s69, 32, 0\n\t" \
    "s_sub_u32 s79, s79, s69\n\t" \
    "s_cmp_lt_u32 s65, 10\n\t" \
    "s_cselect_b32 s69, 64, 0\n\t" \
    "s_add_u32 s79, s79, s69\n\t" \
    "s_add_u32 s80, s79, s88\n\t" \
    "s_sub_u32 s70, s79, s74\n\t" \
    "s_max_i32 s70, s70, 0\n\t" \
    "s_min_i32 s70, s70, 16000\n\t" \
    "s_lshl_b32 s70, s70, 1\n\t" \
    "s_mul_hi_u32 s70, s70, s77\n\t" \
    "s_add_u32 s70, s70, s73\n\t" \
    "v_mov_b32 v160, s70\n\t" \
    "ds_write_b32 v11, v160 offset:20480\n\t" \
    "s_and_b32 s69, s65, 7\n\t" \
    "s_lshl_b32 s69, s69, 18\n\t" \
    "s_add_u32 s69, s69, s63\n\t" \
    "s_add_u32 s52, s36, s69\n\t" \
    "s_addc_u32 s53, s37, 0\n\t" \
    "s_waitcnt lgkmcnt(0)\n\t" \
    "s_barrier\n\t" \
    "s_branch vv_loop12_%=\n\t" \
    "vv_Gbad58_%=:\n\t" \
    "s_waitcnt lgkmcnt(0)\n\t" \
    "s_nop 7\n\t" \
    "v_mov_b32 v72, 0\n\t" \
    "v_mov_b32 v73, 0\n\t" \
    "v_mov_b32 v74, 0\n\t" \
    "v_mov_b32 v75, 0\n\t" \
    "v_mov_b32 v76, 0\n\t" \
    "v_mov_b32 v77, 0\n\t" \
    "v_mov_b32 v78, 0\n\t" \
    "v_mov_b32 v79, 0\n\t" \
    "s_cmp_eq_u32 s68, 0\n\t" \
    "s_cbranch_scc1 vv_F72_%=\n\t" \
    "s_sub_u32 s68, s68, 1\n\t" \
    "s_branch vv_G57_%=\n\t" \
    "vv_F72_%=:\n\t" \
    "v_mov_b32 v140, 5767424\n\t" \
    "v_mov_b32 v141, 1\n\t" \
    "global_store_dword v140, v141, s[36:37] sc1\n\t" \
    "s_waitcnt vmcnt(0)\n\t" \
    "s_branch vv_Gc59_%=\n\t" \
    "vv_Gslow60_%=:\n\t" \
    "v_max_u32 v140, v36, v37\n\t" \
    "v_max3_u32 v140, v140, v38, v39\n\t" \
    "v_max3_u32 v140, v140, v40, v41\n\t" \
    "v_max3_u32 v140, v140, v42, v43\n\t" \
    "v_max3_u32 v140, v140, v44, v45\n\t" \
    "v_max3_u32 v140, v140, v46, v47\n\t" \
    "v_max3_u32 v140, v140, v48, v49\n\t" \
    "v_max3_u32 v140, v140, v50, v51\n\t" \
    "v_max3_u32 v140, v140, v52, v53\n\t" \
    "v_max3_u32 v140, v140, v54, v55\n\t" \
    "v_max3_u32 v140, v140, v56, v57\n\t" \
    "v_max3_u32 v140, v140, v58, v59\n\t" \
    "v_max3_u32 v140, v140, v60, v61\n\t" \
    "v_max3_u32 v140, v140, v62, v63\n\t" \
    "v_max3_u32 v140, v140, v64, v65\n\t" \
    "v_max3_u32 v140, v140, v66, v67\n\t" \
    "v_max3_u32 v140, v140, v68, v69\n\t" \
    "v_max3_u32 v140, v140, v70, v71\n\t" \
    "v_cmp_eq_u32 vcc, -1, v140\n\t" \
    "s_cbranch_vccz vv_Gback61_%=\n\t" \
    "s_waitcnt lgkmcnt(0)\n\t" \
    "v_sub_u32 v140, v1, v3\n\t" \
    "s_nop 0\n\t" \
    "v_readfirstlane_b32 s69, v140\n\t" \
    "v_add_u32 v143, 20480, v0\n\t" \
    "s_nop 7\n\t" \
    "s_add_u32 s30, s46, s69\n\t" \
    "s_addc_u32 s31, s47, 0\n\t" \
    "vv_Gag73_%=:\n\t" \
    "global_load_dwordx4 v[36:39], v1, s[46:47] sc1\n\t" \
    "global_load_dwordx4 v[40:43], v23, s[46:47] sc1\n\t" \
    "global_load_dwordx4 v[44:47], v143, s[30:31] sc1\n\t" \
    "global_load_dwordx4 v[48:51], v4, s[30:31] sc1\n\t" \
    "global_load_dwordx4 v[52:55], v5, s[30:31] sc1\n\t" \
    "global_load_dwordx4 v[56:59], v6, s[30:31] sc1\n\t" \
    "global_load_dwordx4 v[60:63], v7, s[30:31] sc1\n\t" \
    "global_load_dwordx4 v[64:67], v8, s[30:31] sc1\n\t" \
    "global_load_dwordx4 v[68:71], v9, s[30:31] sc1\n\t" \
    "s_waitcnt vmcnt(0)\n\t" \
    "v_max_u32 v140, v36, v37\n\t" \
    "v_max3_u32 v140, v140, v38, v39\n\t" \
    "v_max3_u32 v140, v140, v40, v41\n\t" \
    "v_max3_u32 v140, v140, v42, v43\n\t" \
    "v_max3_u32 v140, v140, v44, v45\n\t" \
    "v_max3_u32 v140, v140, v46, v47\n\t" \
    "v_max3_u32 v140, v140, v48, v49\n\t" \
    "v_max3_u32 v140, v140, v50, v51\n\t" \
    "v_max3_u32 v140, v140, v52, v53\n\t" \
    "v_max3_u32 v140, v140, v54, v55\n\t" \
    "v_max3_u32 v140, v140, v56, v57\n\t" \
    "v_max3_u32 v140, v140, v58, v59\n\t" \
    "v_max3_u32 v140, v140, v60, v61\n\t" \
    "v_max3_u32 v140, v140, v62, v63\n\t" \
    "v_max3_u32 v140, v140, v64, v65\n\t" \
    "v_max3_u32 v140, v140, v66, v67\n\t" \
    "v_max3_u32 v140, v140, v68, v69\n\t" \
    "v_max3_u32 v140, v140, v70, v71\n\t" \
    "v_cmp_eq_u32 vcc, -1, v140\n\t" \
    "s_cbranch_vccz vv_Gredo74_%=\n\t" \
    "s_cmp_eq_u32 s68, 0\n\t" \
    "s_cbranch_scc1 vv_F75_%=\n\t" \
    "s_sub_u32 s68, s68, 1\n\t" \
    "s_sleep 1\n\t" \
    "s_branch vv_Gag73_%=\n\t" \
    "vv_F75_%=:\n\t" \
    "v_mov_b32 v140, 5767424\n\t" \
    "v_mov_b32 v141, 2\n\t" \
    "global_store_dword v140, v141, s[36:37] sc1\n\t" \
    "s_waitcnt vmcnt(0)\n\t" \
    "s_branch vv_Gredo74_%=\n\t" \
    "vv_Gredo74_%=:\n\t" \
    "v_mov_b32 v72, 0\n\t" \
    "v_mov_b32 v73, 0\n\t" \
    "v_mov_b32 v74, 0\n\t" \
    "v_mov_b32 v75, 0\n\t" \
    "v_mov_b32 v76, 0\n\t" \
    "v_mov_b32 v77, 0\n\t" \
    "v_mov_b32 v78, 0\n\t" \
    "v_mov_b32 v79, 0\n\t" \
    "v_smfmac_f32_16x16x64_bf16 v[72:75], v[36:39], a[48:55], v10\n\t" \
    "v_smfmac_f32_16x16x64_bf16 v[72:75], v[40:43], a[56:63], v10\n\t" \
    "v_smfmac_f32_16x16x64_bf16 v[72:75], v[44:47], a[64:71], v10\n\t" \
    "v_smfmac_f32_16x16x64_bf16 v[72:75], v[48:51], a[0:7], v10\n\t" \
    "v_smfmac_f32_16x16x64_bf16 v[72:75], v[52:55], a[8:15], v10\n\t" \
    "v_smfmac_f32_16x16x64_bf16 v[72:75], v[56:59], a[16:23], v10\n\t" \
    "v_smfmac_f32_16x16x64_bf16 v[72:75], v[60:63], a[24:31], v10\n\t" \
    "v_smfmac_f32_16x16x64_bf16 v[72:75], v[64:67], a[32:39], v10\n\t" \
    "v_smfmac_f32_16x16x64_bf16 v[72:75], v[68:71], a[40:47], v10\n\t" \
    "v_smfmac_f32_16x16x64_bf16 v[76:79], v[36:39], a[120:127], v10\n\t" \
    "v_smfmac_f32_16x16x64_bf16 v[76:79], v[40:43], a[128:135], v10\n\t" \
    "v_smfmac_f32_16x16x64_bf16 v[76:79], v[44:47], a[136:143], v10\n\t" \
    "v_smfmac_f32_16x16x64_bf16 v[76:79], v[48:51], a[72:79], v10\n\t" \
    "v_smfmac_f32_16x16x64_bf16 v[76:79], v[52:55], a[80:87], v10\n\t" \
    "v_smfmac_f32_16x16x64_bf16 v[76:79], v[56:59], a[88:95], v10\n\t" \
    "v_smfmac_f32_16x16x64_bf16 v[76:79], v[60:63], a[96:103], v10\n\t" \
    "v_smfmac_f32_16x16x64_bf16 v[76:79], v[64:67], a[104:111], v10\n\t" \
    "v_smfmac_f32_16x16x64_bf16 v[76:79], v[68:71], a[112:119], v10\n\t" \
    "v_add_f32 v72, v72, v74\n\t" \
    "v_add_f32 v73, v73, v75\n\t" \
    "v_cvt_pk_bf16_f32 v80, v72, v73\n\t" \
    "ds_write_b32 v12, v80 offset:0\n\t" \
    "s_nop 3\n\t" \
    "v_pk_add_f32 v[76:77], v[76:77], v[78:79]\n\t" \
    "v_cvt_pk_bf16_f32 v81, v76, v77\n\t" \
    "ds_write_b32 v12, v81 offset:256\n\t" \
    "s_branch vv_Gback61_%=\n\t" \
    "vv_end53_%=:\n\t" \
    "s_endpgm\n\t" \
    "vv_last_%=:\n\t" \
    ""
#define ASM_R \
    "s_mov_b64 s[36:37], %0\n\t" \
    "s_mov_b64 s[42:43], %1\n\t" \
    "s_mov_b64 s[44:45], %2\n\t" \
    "s_mov_b32 s66, %3\n\t" \
    "s_mov_b32 s67, %4\n\t" \
    "s_mov_b64 s[50:51], %5\n\t" \
    "s_mov_b32 s82, %7\n\t" \
    "s_load_dwordx8 s[84:91], %6, 0\n\t" \
    "v_mbcnt_lo_u32_b32 v223, -1, 0\n\t" \
    "v_mbcnt_hi_u32_b32 v223, -1, v223\n\t" \
    "s_getpc_b64 s[46:47]\n\t" \
    "rr_pc_%=:\n\t" \
    "s_lshl_b32 s69, s66, 13\n\t" \
    "v_lshl_add_u32 v222, v223, 7, s69\n\t" \
    "v_min_u32 v222, rr_last_%=-rr_pc_%=-4, v222\n\t" \
    "global_load_dword v222, v222, s[46:47]\n\t" \
    "v_lshlrev_b32 v223, 2, v223\n\t" \
    "v_add_u32 v223, s82, v223\n\t" \
    "ds_read_b32 v0, v223 offset:0\n\t" \
    "ds_read_b32 v1, v223 offset:256\n\t" \
    "ds_read_b32 v2, v223 offset:512\n\t" \
    "ds_read_b32 v3, v223 offset:768\n\t" \
    "ds_read_b32 v4, v223 offset:1024\n\t" \
    "ds_read_b32 v5, v223 offset:1280\n\t" \
    "ds_read_b32 v6, v223 offset:1536\n\t" \
    "ds_read_b32 v7, v223 offset:1792\n\t" \
    "ds_read_b32 v8, v223 offset:2048\n\t" \
    "ds_read_b32 v9, v223 offset:2304\n\t" \
    "ds_read_b32 v10, v223 offset:2560\n\t" \
    "ds_read_b32 v11, v223 offset:2816\n\t" \
    "s_waitcnt lgkmcnt(0)\n\t" \
    "ds_read_b32 v12, v223 offset:3072\n\t" \
    "ds_read_b32 v13, v223 offset:3328\n\t" \
    "ds_read_b32 v14, v223 offset:3584\n\t" \
    "ds_read_b32 v15, v223 offset:3840\n\t" \
    "ds_read_b32 v16, v223 offset:4096\n\t" \
    "ds_read_b32 v17, v223 offset:4352\n\t" \
    "ds_read_b32 v18, v223 offset:4608\n\t" \
    "ds_read_b32 v19, v223 offset:4864\n\t" \
    "ds_read_b32 v20, v223 offset:5120\n\t" \
    "ds_read_b32 v21, v223 offset:5376\n\t" \
    "ds_read_b32 v22, v223 offset:5632\n\t" \
    "ds_read_b32 v23, v223 offset:5888\n\t" \
    "s_waitcnt lgkmcnt(0)\n\t" \
    "ds_read_b32 v24, v223 offset:6144\n\t" \
    "ds_read_b32 v25, v223 offset:6400\n\t" \
    "ds_read_b32 v26, v223 offset:6656\n\t" \
    "ds_read_b32 v27, v223 offset:6912\n\t" \
    "ds_read_b32 v28, v223 offset:7168\n\t" \
    "ds_read_b32 v29, v223 offset:7424\n\t" \
    "ds_read_b32 v30, v223 offset:7680\n\t" \
    "ds_read_b32 v31, v223 offset:7936\n\t" \
    "ds_read_b32 v32, v223 offset:8192\n\t" \
    "ds_read_b32 v33, v223 offset:8448\n\t" \
    "ds_read_b32 v34, v223 offset:8704\n\t" \
    "ds_read_b32 v35, v223 offset:8960\n\t" \
    "s_waitcnt lgkmcnt(0)\n\t" \
    "ds_read_b32 v36, v223 offset:9216\n\t" \
    "ds_read_b32 v37, v223 offset:9472\n\t" \
    "ds_read_b32 v38, v223 offset:9728\n\t" \
    "ds_read_b32 v39, v223 offset:9984\n\t" \
    "ds_read_b32 v40, v223 offset:10240\n\t" \
    "ds_read_b32 v41, v223 offset:10496\n\t" \
    "ds_read_b32 v42, v223 offset:10752\n\t" \
    "ds_read_b32 v43, v223 offset:11008\n\t" \
    "ds_read_b32 v44, v223 offset:11264\n\t" \
    "ds_read_b32 v45, v223 offset:11520\n\t" \
    "ds_read_b32 v184, v223 offset:11776\n\t" \
    "ds_read_b32 v185, v223 offset:12032\n\t" \
    "s_waitcnt lgkmcnt(0)\n\t" \
    "ds_read_b32 v186, v223 offset:12288\n\t" \
    "ds_read_b32 v187, v223 offset:12544\n\t" \
    "ds_read_b32 v188, v223 offset:12800\n\t" \
    "s_waitcnt lgkmcnt(0)\n\t" \
    "s_mov_b32 s68, 262144\n\t" \
    "s_mov_b32 s72, 0\n\t" \
    "s_mov_b32 s83, -1\n\t" \
    "s_mov_b32 s65, 0\n\t" \
    "v_xor_b32 v180, 65536, v2\n\t" \
    "v_xor_b32 v181, 131072, v2\n\t" \
    "v_xor_b32 v182, 196608, v2\n\t" \
    "v_add_u32 v189, 4096, v7\n\t" \
    "v_add_u32 v190, 16384, v7\n\t" \
    "v_add_u32 v191, 20480, v7\n\t" \
    "s_mul_i32 s69, s67, 57\n\t" \
    "s_lshr_b32 s69, s69, 9\n\t" \
    "s_mul_i32 s69, s69, 9\n\t" \
    "s_sub_u32 s69, s67, s69\n\t" \
    "s_cmp_lt_u32 s69, 4\n\t" \
    "s_cselect_b32 s70, 21, 16\n\t" \
    "s_cmp_eq_u32 s69, 8\n\t" \
    "s_cselect_b32 s70, 26, s70\n\t" \
    "s_lshr_b32 s97, s91, s70\n\t" \
    "s_and_b32 s97, s97, 31\n\t" \
    "s_min_u32 s97, s97, 0\n\t" \
    "s_lshl_b32 s97, s97, 4\n\t" \
    "s_mov_b32 s77, 3019898880\n\t" \
    "s_mov_b32 s78, 3054198966\n\t" \
    "s_waitcnt lgkmcnt(0)\n\t" \
    "s_mov_b32 s70, 116\n\t" \
    "rr_E1_%=:\n\t" \
    "s_memtime s[56:57]\n\t" \
    "s_memrealtime s[2:3]\n\t" \
    "s_nop 0\n\t" \
    "s_memrealtime s[4:5]\n\t" \
    "s_nop 0\n\t" \
    "s_memrealtime s[6:7]\n\t" \
    "s_nop 0\n\t" \
    "s_memrealtime s[8:9]\n\t" \
    "s_nop 0\n\t" \
    "s_memrealtime s[10:11]\n\t" \
    "s_nop 0\n\t" \
    "s_memrealtime s[12:13]\n\t" \
    "s_nop 0\n\t" \
    "s_memrealtime s[14:15]\n\t" \
    "s_nop 0\n\t" \
    "s_memrealtime s[16:17]\n\t" \
    "s_nop 0\n\t" \
    "s_memrealtime s[18:19]\n\t" \
    "s_nop 0\n\t" \
    "s_memrealtime s[20:21]\n\t" \
    "s_nop 0\n\t" \
    "s_memrealtime s[22:23]\n\t" \
    "s_nop 0\n\t" \
    "s_memrealtime s[24:25]\n\t" \
    "s_nop 0\n\t" \
    "s_memrealtime s[26:27]\n\t" \
    "s_nop 0\n\t" \
    "s_memrealtime s[28:29]\n\t" \
    "s_memtime s[54:55]\n\t" \
    "s_add_u32 s70, s70, 8\n\t" \
    "s_waitcnt lgkmcnt(0)\n\t" \
    "s_sub_u32 s69, s54, s56\n\t" \
    "s_cmp_gt_u32 s69, s70\n\t" \
    "s_cbranch_scc1 rr_E1_%=\n\t" \
    "s_add_u32 s2, s2, s4\n\t" \
    "s_add_u32 s6, s6, s8\n\t" \
    "s_add_u32 s10, s10, s12\n\t" \
    "s_add_u32 s14, s14, s16\n\t" \
    "s_add_u32 s18, s18, s20\n\t" \
    "s_add_u32 s22, s22, s24\n\t" \
    "s_add_u32 s2, s2, s6\n\t" \
    "s_add_u32 s10, s10, s14\n\t" \
    "s_add_u32 s18, s18, s22\n\t" \
    "s_add_u32 s2, s2, s26\n\t" \
    "s_add_u32 s10, s10, s18\n\t" \
    "s_add_u32 s2, s2, s10\n\t" \
    "s_mul_i32 s70, s28, 13\n\t" \
    "s_sub_u32 s70, s70, s2\n\t" \
    "s_min_u32 s70, s70, 52\n\t" \
    "s_mul_i32 s69, s69, s70\n\t" \
    "s_mul_hi_u32 s69, s69, 76695844\n\t" \
    "s_mov_b32 s75, s73\n\t" \
    "s_mov_b32 s76, s74\n\t" \
    "s_add_u32 s73, s56, s69\n\t" \
    "s_lshl_b32 s74, s28, 4\n\t" \
    "v_mov_b32 v172, 0xd800\n\t" \
    "v_mov_b32 v173, 0x20d800\n\t" \
    "global_load_dword v172, v172, s[36:37]\n\t" \
    "global_load_dword v173, v173, s[36:37]\n\t" \
    "s_mov_b32 s62, 0\n\t" \
    "s_mov_b32 s63, 0x200000\n\t" \
    "s_mov_b64 s[44:45], 0\n\t" \
    "v_mov_b32 v170, 0\n\t" \
    "s_mov_b32 s65, s66\n\t" \
    "s_lshl_b32 s58, s67, 14\n\t" \
    "s_sub_u32 s80, s74, 32000\n\t" \
    "s_mov_b32 s64, 1\n\t" \
    "s_mov_b32 s65, 5\n\t" \
    "s_mov_b32 s83, 99\n\t" \
    "s_add_u32 s70, s65, 59\n\t" \
    "s_mul_i32 s69, s70, 171\n\t" \
    "s_lshr_b32 s69, s69, 9\n\t" \
    "s_mul_i32 s70, s70, 589824\n\t" \
    "s_mul_i32 s71, s69, 327680\n\t" \
    "s_add_u32 s70, s70, s71\n\t" \
    "s_lshl1_add_u32 s69, s69, 4\n\t" \
    "s_bitcmp1_b64 s[44:45], s69\n\t" \
    "s_cselect_b32 s69, 8192, 0\n\t" \
    "s_add_u32 s69, s69, s70\n\t" \
    "s_add_u32 s69, s69, 123731968\n\t" \
    "s_add_u32 s69, s69, s58\n\t" \
    "s_add_u32 s40, s36, s69\n\t" \
    "s_addc_u32 s41, s37, 0\n\t" \
    "v_mov_b32 v192, 0\n\t" \
    "s_mov_b64 exec, 1\n\t" \
    "s_branch rr_act3_%=\n\t" \
    "rr_g214_%=:\n\t" \
    "s_mov_b64 exec, -1\n\t" \
    "s_waitcnt vmcnt(0)\n\t" \
    "v_mbcnt_lo_u32_b32 v164, -1, 0\n\t" \
    "v_mbcnt_hi_u32_b32 v164, -1, v164\n\t" \
    "v_lshlrev_b32 v164, 6, v164\n\t" \
    "v_add_u32 v164, 6225920, v164\n\t" \
    "v_mov_b32 v165, 6234112\n\t" \
    "global_load_dword v165, v165, s[36:37] sc1\n\t" \
    "global_load_dwordx2 v[166:167], v164, s[36:37] sc1\n\t" \
    "global_load_dword v168, v164, s[36:37] offset:2048 sc1\n\t" \
    "s_waitcnt vmcnt(0)\n\t" \
    "rr_C15_%=:\n\t" \
    "v_cmp_ne_u32 s[54:55], v166, v168\n\t" \
    "v_sub_u32 v166, v166, v165\n\t" \
    "v_cmp_ne_u32 vcc, 1, v166\n\t" \
    "s_nop 1\n\t" \
    "s_or_b64 vcc, vcc, s[54:55]\n\t" \
    "s_cbranch_scc0 rr_Cok16_%=\n\t" \
    "s_cmp_eq_u32 s68, 0\n\t" \
    "s_cbranch_scc1 rr_F18_%=\n\t" \
    "s_sub_u32 s68, s68, 1\n\t" \
    "s_sleep 6\n\t" \
    "s_branch rr_Cag17_%=\n\t" \
    "rr_F18_%=:\n\t" \
    "v_mov_b32 v168, 5767424\n\t" \
    "v_mov_b32 v169, 4\n\t" \
    "global_store_dword v168, v169, s[36:37] sc1\n\t" \
    "s_waitcnt vmcnt(0)\n\t" \
    "s_branch rr_Cok16_%=\n\t" \
    "rr_Cag17_%=:\n\t" \
    "global_load_dwordx2 v[166:167], v164, s[36:37] sc1\n\t" \
    "global_load_dword v168, v164, s[36:37] offset:2048 sc1\n\t" \
    "s_waitcnt vmcnt(0)\n\t" \
    "s_branch rr_C15_%=\n\t" \
    "rr_Cok16_%=:\n\t" \
    "s_cmp_lg_u32 s66, 0\n\t" \
    "s_cbranch_scc1 rr_Ca19_%=\n\t" \
    "v_mov_b32 v164, 5836800\n\t" \
    "s_mov_b64 exec, 1\n\t" \
    "global_atomic_add v164, v166, s[36:37] sc1\n\t" \
    "s_mov_b64 exec, -1\n\t" \
    "rr_Ca19_%=:\n\t" \
    "v_mov_b32_dpp v164, v167 quad_perm:[1,0,3,2] row_mask:0xf bank_mask:0xf\n\t" \
    "v_cmp_lt_u32 s[44:45], v164, v167\n\t" \
    "s_nop 1\n\t" \
    "s_bitcmp1_b32 s44, 0\n\t" \
    "s_cselect_b32 s62, 8192, 0\n\t" \
    "s_bitcmp1_b32 s44, 2\n\t" \
    "s_cselect_b32 s63, 8192, 0\n\t" \
    "s_or_b32 s63, s63, 0x200000\n\t" \
    "v_min_u32 v164, v164, v167\n\t" \
    "v_cmp_lt_u32 vcc, 660, v164\n\t" \
    "s_nop 1\n\t" \
    "s_cmp_lg_u64 vcc, 0\n\t" \
    "s_cselect_b32 s69, 1, 0\n\t" \
    "s_mul_i32 s70, s69, 160\n\t" \
    "s_add_u32 s86, s86, s70\n\t" \
    "s_mul_i32 s70, s69, 536\n\t" \
    "s_add_u32 s87, s87, s70\n\t" \
    "s_mul_i32 s70, s69, 256\n\t" \
    "s_add_u32 s88, s88, s70\n\t" \
    "v_mov_b32 v172, -1\n\t" \
    "v_mov_b32 v173, -1\n\t" \
    "v_mov_b32 v174, -1\n\t" \
    "v_mov_b32 v175, -1\n\t" \
    "v_mov_b32 v176, 32000\n\t" \
    "s_sub_u32 s69, s63, s62\n\t" \
    "s_sub_u32 s69, s69, 0x200000\n\t" \
    "v_bfe_u32 v164, v187, 21, 1\n\t" \
    "v_mad_i32_i24 v187, v164, s69, v187\n\t" \
    "v_bfe_u32 v164, v188, 21, 1\n\t" \
    "v_mad_i32_i24 v188, v164, s69, v188\n\t" \
    "s_mov_b32 s64, 0\n\t" \
    "s_mov_b32 s65, s66\n\t" \
    "s_mov_b32 s83, 1\n\t" \
    "s_mov_b32 s77, 3154116608\n\t" \
    "rr_pf9_%=:\n\t" \
    "s_cmp_gt_u32 s65, 7\n\t" \
    "s_cselect_b32 s83, 99, s83\n\t" \
    "rr_WF20_%=:\n\t" \
    "s_cmp_ge_i32 s83, s65\n\t" \
    "s_cbranch_scc1 rr_WFok21_%=\n\t" \
    "global_load_dword v164, v6, s[36:37] sc1\n\t" \
    "s_waitcnt vmcnt(0)\n\t" \
    "v_min_i32_dpp v164, v164, v164 row_ror:1 row_mask:0xf bank_mask:0xf\n\t" \
    "s_nop 1\n\t" \
    "v_min_i32_dpp v164, v164, v164 row_ror:2 row_mask:0xf bank_mask:0xf\n\t" \
    "s_nop 1\n\t" \
    "v_min_i32_dpp v164, v164, v164 row_ror:4 row_mask:0xf bank_mask:0xf\n\t" \
    "s_nop 1\n\t" \
    "v_min_i32_dpp v164, v164, v164 row_ror:8 row_mask:0xf bank_mask:0xf\n\t" \
    "s_nop 0\n\t" \
    "v_readlane_b32 s69, v164, 0\n\t" \
    "v_readlane_b32 s70, v164, 16\n\t" \
    "s_nop 1\n\t" \
    "s_min_i32 s83, s69, s70\n\t" \
    "s_cmp_ge_i32 s83, s65\n\t" \
    "s_cbranch_scc1 rr_WFok21_%=\n\t" \
    "s_cmp_eq_u32 s68, 0\n\t" \
    "s_cbranch_scc1 rr_F22_%=\n\t" \
    "s_sub_u32 s68, s68, 1\n\t" \
    "s_sleep 2\n\t" \
    "s_branch rr_WF20_%=\n\t" \
    "rr_F22_%=:\n\t" \
    "v_mov_b32 v164, 5767424\n\t" \
    "v_mov_b32 v165, 10\n\t" \
    "global_store_dword v164, v165, s[36:37] sc1\n\t" \
    "s_waitcnt vmcnt(0)\n\t" \
    "s_branch rr_WFok21_%=\n\t" \
    "rr_WFok21_%=:\n\t" \
    "s_mul_i32 s70, s67, 7\n\t" \
    "s_add_u32 s69, s65, 0\n\t" \
    "s_xor_b32 s69, s69, s70\n\t" \
    "s_and_b32 s69, s69, 63\n\t" \
    "s_lshl_b32 s69, s69, 15\n\t" \
    "s_mul_i32 s70, s67, 2097152\n\t" \
    "s_add_u32 s69, s69, s70\n\t" \
    "s_add_u32 s69, s69, 48234496\n\t" \
    "s_add_u32 s38, s36, s69\n\t" \
    "s_addc_u32 s39, s37, 0\n\t" \
    "s_add_u32 s70, s65, 0\n\t" \
    "s_mul_i32 s69, s70, 171\n\t" \
    "s_lshr_b32 s69, s69, 9\n\t" \
    "s_mul_i32 s70, s70, 589824\n\t" \
    "s_mul_i32 s71, s69, 327680\n\t" \
    "s_add_u32 s70, s70, s71\n\t" \
    "s_lshl1_add_u32 s69, s69, 4\n\t" \
    "s_bitcmp1_b64 s[44:45], s69\n\t" \
    "s_cselect_b32 s69, 8192, 0\n\t" \
    "s_add_u32 s69, s69, s70\n\t" \
    "s_add_u32 s69, s69, 123731968\n\t" \
    "s_add_u32 s69, s69, s58\n\t" \
    "s_add_u32 s40, s36, s69\n\t" \
    "s_addc_u32 s41, s37, 0\n\t" \
    "s_and_b32 s69, s69, 0xffe00000\n\t" \
    "s_add_u32 s69, s69, 1823744\n\t" \
    "v_mov_b32 v168, s69\n\t" \
    "global_load_dword v168, v168, s[36:37]\n\t" \
    "v_mov_b32 v164, s83\n\t" \
    "s_add_u32 s69, s65, 2\n\t" \
    "s_cmp_ge_i32 s83, s69\n\t" \
    "s_cbranch_scc1 rr_nq23_%=\n\t" \
    "global_load_dword v164, v6, s[36:37] sc1\n\t" \
    "rr_nq23_%=:\n\t" \
    "s_cmp_gt_u32 s65, 61\n\t" \
    "s_cbranch_scc1 rr_idle2_%=\n\t" \
    "s_add_u32 s69, s65, 2\n\t" \
    "s_and_b32 s69, s69, 7\n\t" \
    "s_lshl_b32 s69, s69, 18\n\t" \
    "s_add_u32 s69, s69, s62\n\t" \
    "s_add_u32 s46, s36, s69\n\t" \
    "s_addc_u32 s47, s37, 0\n\t" \
    "global_store_dwordx4 v184, v[172:175], s[46:47] sc1\n\t" \
    "global_store_dwordx4 v185, v[172:175], s[46:47] sc1\n\t" \
    "global_store_dwordx4 v186, v[172:175], s[46:47] sc1\n\t" \
    "global_store_dwordx4 v187, v[172:175], s[46:47] sc1\n\t" \
    "global_store_dwordx4 v188, v[172:175], s[46:47] sc1\n\t" \
    "rr_idle2_%=:\n\t" \
    "v_mov_b32 v200, 0\n\t" \
    "s_cmp_lg_u32 s67, 35\n\t" \
    "s_cbranch_scc1 rr_cke24_%=\n\t" \
    "s_cmp_lt_u32 s65, 6\n\t" \
    "s_cbranch_scc1 rr_cke24_%=\n\t" \
    "s_lshl_b32 s69, s65, 7\n\t" \
    "s_add_u32 s69, s69, 5853184\n\t" \
    "v_mov_b32 v201, s69\n\t" \
    "global_load_dword v200, v201, s[36:37] sc1\n\t" \
    "rr_cke24_%=:\n\t" \
    "s_cmp_gt_u32 s65, 62\n\t" \
    "s_cbranch_scc1 rr_n225_%=\n\t" \
    "s_cmp_lt_u32 s65, 2\n\t" \
    "s_cbranch_scc1 rr_nw226_%=\n\t" \
    "s_mul_hi_u32 s69, s85, s78\n\t" \
    "s_add_u32 s79, s81, s69\n\t" \
    "s_add_u32 s79, s79, s87\n\t" \
    "s_cmp_ge_u32 s65, 54\n\t" \
    "s_cselect_b32 s69, 32, 0\n\t" \
    "s_sub_u32 s79, s79, s69\n\t" \
    "s_cmp_lt_u32 s65, 11\n\t" \
    "s_cselect_b32 s69, 64, 0\n\t" \
    "s_add_u32 s79, s79, s69\n\t" \
    "s_add_u32 s80, s79, s88\n\t" \
    "s_add_u32 s79, s79, 240\n\t" \
    "s_cmp_gt_u32 s65, 8\n\t" \
    "s_cselect_b32 s69, 0, 176\n\t" \
    "s_sub_u32 s79, s79, s69\n\t" \
    "s_sub_u32 s70, s79, s74\n\t" \
    "s_max_i32 s70, s70, 0\n\t" \
    "s_min_i32 s70, s70, 16000\n\t" \
    "s_lshl_b32 s70, s70, 1\n\t" \
    "s_mul_hi_u32 s70, s70, s77\n\t" \
    "s_add_u32 s70, s70, s73\n\t" \
    "rr_W27_%=:\n\t" \
    "s_memtime s[56:57]\n\t" \
    "s_waitcnt lgkmcnt(0)\n\t" \
    "s_sub_u32 s69, s70, s56\n\t" \
    "s_cmp_gt_i32 s69, 24\n\t" \
    "s_cbranch_scc1 rr_W27_%=\n\t" \
    "s_branch rr_g113_%=\n\t" \
    "rr_nw226_%=:\n\t" \
    "s_mov_b32 s96, 0xaaaaaaaa\n\t" \
    "v_mov_b32 v166, -1\n\t" \
    "v_readfirstlane_b32 s71, v7\n\t" \
    "s_nop 1\n\t" \
    "s_lshr_b32 s71, s71, 4\n\t" \
    "s_add_u32 s71, s71, 5865472\n\t" \
    "rr_g012_%=:\n\t" \
    "s_memrealtime s[54:55]\n\t" \
    "s_waitcnt lgkmcnt(0)\n\t" \
    "s_bfe_u32 s69, s54, 0x60004\n\t" \
    "s_lshl_b32 s69, s69, 6\n\t" \
    "s_add_u32 s69, s69, s71\n\t" \
    "v_mov_b32 v165, s69\n\t" \
    "global_load_dword v169, v165, s[36:37] sc1\n\t" \
    "s_and_b32 s69, s68, 63\n\t" \
    "s_cbranch_scc1 rr_gf28_%=\n\t" \
    "global_load_dword v166, v6, s[36:37] sc1\n\t" \
    "rr_gf28_%=:\n\t" \
    "s_waitcnt vmcnt(0)\n\t" \
    "v_cmp_ne_u32 s[30:31], -1, v169\n\t" \
    "v_cmp_le_i32 vcc, s65, v166\n\t" \
    "s_nop 1\n\t" \
    "s_lshr_b32 s69, vcc_lo, 1\n\t" \
    "s_and_b32 s69, s69, vcc_lo\n\t" \
    "s_or_b32 s69, s69, s30\n\t" \
    "s_andn2_b32 s69, s69, s96\n\t" \
    "s_cbranch_scc0 rr_gs29_%=\n\t" \
    "s_or_b32 s96, s96, s69\n\t" \
    "s_cmp_eq_u32 s66, 0\n\t" \
    "s_cbranch_scc1 rr_w030_%=\n\t" \
    "s_sleep 8\n\t" \
    "rr_w030_%=:\n\t" \
    "s_bitcmp0_b32 s69, 0\n\t" \
    "s_cbranch_scc1 rr_gk31_%=\n\t" \
    "global_load_dwordx4 a[0:3], v26, s[38:39] offset:0 sc1\n\t" \
    "global_load_dwordx4 a[4:7], v26, s[38:39] offset:1024 sc1\n\t" \
    "rr_gk31_%=:\n\t" \
    "s_bitcmp0_b32 s69, 2\n\t" \
    "s_cbranch_scc1 rr_gk32_%=\n\t" \
    "global_load_dwordx4 a[8:11], v27, s[38:39] offset:0 sc1\n\t" \
    "global_load_dwordx4 a[12:15], v27, s[38:39] offset:1024 sc1\n\t" \
    "rr_gk32_%=:\n\t" \
    "s_bitcmp0_b32 s69, 4\n\t" \
    "s_cbranch_scc1 rr_gk33_%=\n\t" \
    "global_load_dwordx4 a[16:19], v28, s[38:39] offset:0 sc1\n\t" \
    "global_load_dwordx4 a[20:23], v28, s[38:39] offset:1024 sc1\n\t" \
    "rr_gk33_%=:\n\t" \
    "s_bitcmp0_b32 s69, 6\n\t" \
    "s_cbranch_scc1 rr_gk34_%=\n\t" \
    "global_load_dwordx4 a[24:27], v29, s[38:39] offset:0 sc1\n\t" \
    "global_load_dwordx4 a[28:31], v29, s[38:39] offset:1024 sc1\n\t" \
    "rr_gk34_%=:\n\t" \
    "s_bitcmp0_b32 s69, 8\n\t" \
    "s_cbranch_scc1 rr_gk35_%=\n\t" \
    "global_load_dwordx4 a[32:35], v30, s[38:39] offset:0 sc1\n\t" \
    "global_load_dwordx4 a[36:39], v30, s[38:39] offset:1024 sc1\n\t" \
    "rr_gk35_%=:\n\t" \
    "s_bitcmp0_b32 s69, 10\n\t" \
    "s_cbranch_scc1 rr_gk36_%=\n\t" \
    "global_load_dwordx4 a[40:43], v31, s[38:39] offset:0 sc1\n\t" \
    "global_load_dwordx4 a[44:47], v31, s[38:39] offset:1024 sc1\n\t" \
    "rr_gk36_%=:\n\t" \
    "s_bitcmp0_b32 s69, 12\n\t" \
    "s_cbranch_scc1 rr_gk37_%=\n\t" \
    "global_load_dwordx4 a[48:51], v32, s[38:39] offset:0 sc1\n\t" \
    "global_load_dwordx4 a[52:55], v32, s[38:39] offset:1024 sc1\n\t" \
    "rr_gk37_%=:\n\t" \
    "s_bitcmp0_b32 s69, 14\n\t" \
    "s_cbranch_scc1 rr_gk38_%=\n\t" \
    "global_load_dwordx4 a[56:59], v33, s[38:39] offset:0 sc1\n\t" \
    "global_load_dwordx4 a[60:63], v33, s[38:39] offset:1024 sc1\n\t" \
    "rr_gk38_%=:\n\t" \
    "s_bitcmp0_b32 s69, 16\n\t" \
    "s_cbranch_scc1 rr_gk39_%=\n\t" \
    "global_load_dwordx4 a[64:67], v34, s[38:39] offset:0 sc1\n\t" \
    "global_load_dwordx4 a[68:71], v34, s[38:39] offset:1024 sc1\n\t" \
    "rr_gk39_%=:\n\t" \
    "s_bitcmp0_b32 s69, 18\n\t" \
    "s_cbranch_scc1 rr_gk40_%=\n\t" \
    "global_load_dwordx4 a[72:75], v35, s[38:39] offset:0 sc1\n\t" \
    "global_load_dwordx4 a[76:79], v35, s[38:39] offset:1024 sc1\n\t" \
    "rr_gk40_%=:\n\t" \
    "s_bitcmp0_b32 s69, 20\n\t" \
    "s_cbranch_scc1 rr_gk41_%=\n\t" \
    "global_load_dwordx4 a[80:83], v36, s[38:39] offset:0 sc1\n\t" \
    "global_load_dwordx4 a[84:87], v36, s[38:39] offset:1024 sc1\n\t" \
    "rr_gk41_%=:\n\t" \
    "s_bitcmp0_b32 s69, 22\n\t" \
    "s_cbranch_scc1 rr_gk42_%=\n\t" \
    "global_load_dwordx4 a[88:91], v37, s[38:39] offset:0 sc1\n\t" \
    "global_load_dwordx4 a[92:95], v37, s[38:39] offset:1024 sc1\n\t" \
    "rr_gk42_%=:\n\t" \
    "s_bitcmp0_b32 s69, 24\n\t" \
    "s_cbranch_scc1 rr_gk43_%=\n\t" \
    "global_load_dwordx4 a[96:99], v38, s[38:39] offset:0 sc1\n\t" \
    "global_load_dwordx4 a[100:103], v38, s[38:39] offset:1024 sc1\n\t" \
    "rr_gk43_%=:\n\t" \
    "s_bitcmp0_b32 s69, 26\n\t" \
    "s_cbranch_scc1 rr_gk44_%=\n\t" \
    "global_load_dwordx4 a[104:107], v39, s[38:39] offset:0 sc1\n\t" \
    "global_load_dwordx4 a[108:111], v39, s[38:39] offset:1024 sc1\n\t" \
    "rr_gk44_%=:\n\t" \
    "s_bitcmp0_b32 s69, 28\n\t" \
    "s_cbranch_scc1 rr_gk45_%=\n\t" \
    "global_load_dwordx4 a[112:115], v40, s[38:39] offset:0 sc1\n\t" \
    "global_load_dwordx4 a[116:119], v40, s[38:39] offset:1024 sc1\n\t" \
    "rr_gk45_%=:\n\t" \
    "s_bitcmp0_b32 s69, 30\n\t" \
    "s_cbranch_scc1 rr_gk46_%=\n\t" \
    "global_load_dwordx4 a[120:123], v41, s[38:39] offset:0 sc1\n\t" \
    "global_load_dwordx4 a[124:127], v41, s[38:39] offset:1024 sc1\n\t" \
    "rr_gk46_%=:\n\t" \
    "s_cmp_eq_u32 s96, -1\n\t" \
    "s_cbranch_scc1 rr_n225_%=\n\t" \
    "rr_gs29_%=:\n\t" \
    "s_cmp_eq_u32 s68, 0\n\t" \
    "s_cbranch_scc1 rr_F47_%=\n\t" \
    "s_sub_u32 s68, s68, 1\n\t" \
    "s_sleep 1\n\t" \
    "s_branch rr_g012_%=\n\t" \
    "rr_F47_%=:\n\t" \
    "v_mov_b32 v167, 5767424\n\t" \
    "v_mov_b32 v168, 16\n\t" \
    "global_store_dword v167, v168, s[36:37] sc1\n\t" \
    "s_waitcnt vmcnt(0)\n\t" \
    "s_branch rr_n225_%=\n\t" \
    "rr_g113_%=:\n\t" \
    "global_load_dwordx4 a[0:3], v26, s[38:39] offset:0 sc1\n\t" \
    "global_load_dwordx4 a[4:7], v26, s[38:39] offset:1024 sc1\n\t" \
    "global_load_dwordx4 a[8:11], v27, s[38:39] offset:0 sc1\n\t" \
    "global_load_dwordx4 a[12:15], v27, s[38:39] offset:1024 sc1\n\t" \
    "global_load_dwordx4 a[16:19], v28, s[38:39] offset:0 sc1\n\t" \
    "global_load_dwordx4 a[20:23], v28, s[38:39] offset:1024 sc1\n\t" \
    "global_load_dwordx4 a[24:27], v29, s[38:39] offset:0 sc1\n\t" \
    "global_load_dwordx4 a[28:31], v29, s[38:39] offset:1024 sc1\n\t" \
    "global_load_dwordx4 a[32:35], v30, s[38:39] offset:0 sc1\n\t" \
    "global_load_dwordx4 a[36:39], v30, s[38:39] offset:1024 sc1\n\t" \
    "global_load_dwordx4 a[40:43], v31, s[38:39] offset:0 sc1\n\t" \
    "global_load_dwordx4 a[44:47], v31, s[38:39] offset:1024 sc1\n\t" \
    "global_load_dwordx4 a[48:51], v32, s[38:39] offset:0 sc1\n\t" \
    "global_load_dwordx4 a[52:55], v32, s[38:39] offset:1024 sc1\n\t" \
    "global_load_dwordx4 a[56:59], v33, s[38:39] offset:0 sc1\n\t" \
    "global_load_dwordx4 a[60:63], v33, s[38:39] offset:1024 sc1\n\t" \
    "global_load_dwordx4 a[64:67], v34, s[38:39] offset:0 sc1\n\t" \
    "global_load_dwordx4 a[68:71], v34, s[38:39] offset:1024 sc1\n\t" \
    "global_load_dwordx4 a[72:75], v35, s[38:39] offset:0 sc1\n\t" \
    "global_load_dwordx4 a[76:79], v35, s[38:39] offset:1024 sc1\n\t" \
    "global_load_dwordx4 a[80:83], v36, s[38:39] offset:0 sc1\n\t" \
    "global_load_dwordx4 a[84:87], v36, s[38:39] offset:1024 sc1\n\t" \
    "global_load_dwordx4 a[88:91], v37, s[38:39] offset:0 sc1\n\t" \
    "global_load_dwordx4 a[92:95], v37, s[38:39] offset:1024 sc1\n\t" \
    "global_load_dwordx4 a[96:99], v38, s[38:39] offset:0 sc1\n\t" \
    "global_load_dwordx4 a[100:103], v38, s[38:39] offset:1024 sc1\n\t" \
    "global_load_dwordx4 a[104:107], v39, s[38:39] offset:0 sc1\n\t" \
    "global_load_dwordx4 a[108:111], v39, s[38:39] offset:1024 sc1\n\t" \
    "global_load_dwordx4 a[112:115], v40, s[38:39] offset:0 sc1\n\t" \
    "global_load_dwordx4 a[116:119], v40, s[38:39] offset:1024 sc1\n\t" \
    "global_load_dwordx4 a[120:123], v41, s[38:39] offset:0 sc1\n\t" \
    "global_load_dwordx4 a[124:127], v41, s[38:39] offset:1024 sc1\n\t" \
    "rr_n225_%=:\n\t" \
    "s_cmp_eq_u32 s65, 0\n\t" \
    "s_cbranch_scc1 rr_act3_%=\n\t" \
    "s_waitcnt vmcnt(0)\n\t" \
    "rr_ckl49_%=:\n\t" \
    "v_cmp_eq_u32 vcc, -1, v200\n\t" \
    "s_cbranch_vccz rr_ck48_%=\n\t" \
    "global_load_dword v200, v201, s[36:37] sc1\n\t" \
    "s_waitcnt vmcnt(0)\n\t" \
    "s_cmp_eq_u32 s68, 0\n\t" \
    "s_cbranch_scc1 rr_F50_%=\n\t" \
    "s_sub_u32 s68, s68, 1\n\t" \
    "s_sleep 1\n\t" \
    "s_branch rr_ckl49_%=\n\t" \
    "rr_F50_%=:\n\t" \
    "v_mov_b32 v166, 5767424\n\t" \
    "v_mov_b32 v167, 15\n\t" \
    "global_store_dword v166, v167, s[36:37] sc1\n\t" \
    "s_waitcnt vmcnt(0)\n\t" \
    "s_branch rr_ck48_%=\n\t" \
    "rr_ck48_%=:\n\t" \
    "v_min_i32_dpp v164, v164, v164 row_ror:1 row_mask:0xf bank_mask:0xf\n\t" \
    "s_nop 1\n\t" \
    "v_min_i32_dpp v164, v164, v164 row_ror:2 row_mask:0xf bank_mask:0xf\n\t" \
    "s_nop 1\n\t" \
    "v_min_i32_dpp v164, v164, v164 row_ror:4 row_mask:0xf bank_mask:0xf\n\t" \
    "s_nop 1\n\t" \
    "v_min_i32_dpp v164, v164, v164 row_ror:8 row_mask:0xf bank_mask:0xf\n\t" \
    "s_nop 0\n\t" \
    "v_readlane_b32 s69, v164, 0\n\t" \
    "v_readlane_b32 s70, v164, 16\n\t" \
    "s_nop 1\n\t" \
    "s_min_i32 s69, s69, s70\n\t" \
    "s_max_i32 s83, s83, s69\n\t" \
    "s_cmp_eq_u32 s65, 0\n\t" \
    "s_cbranch_scc1 rr_act3_%=\n\t" \
    "s_barrier\n\t" \
    "s_cmp_gt_u32 s65, 62\n\t" \
    "s_cbranch_scc1 rr_end4_%=\n\t" \
    "ds_read_b32 v164, v9\n\t" \
    "s_waitcnt lgkmcnt(0)\n\t" \
    "v_readfirstlane_b32 s81, v164\n\t" \
    "s_nop 1\n\t" \
    "s_cmp_lg_u32 s65, 1\n\t" \
    "s_cbranch_scc1 rr_n151_%=\n\t" \
    "s_waitcnt lgkmcnt(0)\n\t" \
    "s_mov_b32 s70, 116\n\t" \
    "rr_E52_%=:\n\t" \
    "s_memtime s[56:57]\n\t" \
    "s_memrealtime s[2:3]\n\t" \
    "s_nop 0\n\t" \
    "s_memrealtime s[4:5]\n\t" \
    "s_nop 0\n\t" \
    "s_memrealtime s[6:7]\n\t" \
    "s_nop 0\n\t" \
    "s_memrealtime s[8:9]\n\t" \
    "s_nop 0\n\t" \
    "s_memrealtime s[10:11]\n\t" \
    "s_nop 0\n\t" \
    "s_memrealtime s[12:13]\n\t" \
    "s_nop 0\n\t" \
    "s_memrealtime s[14:15]\n\t" \
    "s_nop 0\n\t" \
    "s_memrealtime s[16:17]\n\t" \
    "s_nop 0\n\t" \
    "s_memrealtime s[18:19]\n\t" \
    "s_nop 0\n\t" \
    "s_memrealtime s[20:21]\n\t" \
    "s_nop 0\n\t" \
    "s_memrealtime s[22:23]\n\t" \
    "s_nop 0\n\t" \
    "s_memrealtime s[24:25]\n\t" \
    "s_nop 0\n\t" \
    "s_memrealtime s[26:27]\n\t" \
    "s_nop 0\n\t" \
    "s_memrealtime s[28:29]\n\t" \
    "s_memtime s[54:55]\n\t" \
    "s_add_u32 s70, s70, 8\n\t" \
    "s_waitcnt lgkmcnt(0)\n\t" \
    "s_sub_u32 s69, s54, s56\n\t" \
    "s_cmp_gt_u32 s69, s70\n\t" \
    "s_cbranch_scc1 rr_E52_%=\n\t" \
    "s_add_u32 s2, s2, s4\n\t" \
    "s_add_u32 s6, s6, s8\n\t" \
    "s_add_u32 s10, s10, s12\n\t" \
    "s_add_u32 s14, s14, s16\n\t" \
    "s_add_u32 s18, s18, s20\n\t" \
    "s_add_u32 s22, s22, s24\n\t" \
    "s_add_u32 s2, s2, s6\n\t" \
    "s_add_u32 s10, s10, s14\n\t" \
    "s_add_u32 s18, s18, s22\n\t" \
    "s_add_u32 s2, s2, s26\n\t" \
    "s_add_u32 s10, s10, s18\n\t" \
    "s_add_u32 s2, s2, s10\n\t" \
    "s_mul_i32 s70, s28, 13\n\t" \
    "s_sub_u32 s70, s70, s2\n\t" \
    "s_min_u32 s70, s70, 52\n\t" \
    "s_mul_i32 s69, s69, s70\n\t" \
    "s_mul_hi_u32 s69, s69, 76695844\n\t" \
    "s_mov_b32 s75, s73\n\t" \
    "s_mov_b32 s76, s74\n\t" \
    "s_add_u32 s73, s56, s69\n\t" \
    "s_lshl_b32 s74, s28, 4\n\t" \
    "rr_n151_%=:\n\t" \
    "s_mul_hi_u32 s69, s85, s78\n\t" \
    "s_add_u32 s79, s81, s69\n\t" \
    "s_add_u32 s79, s79, s87\n\t" \
    "s_cmp_ge_u32 s65, 53\n\t" \
    "s_cselect_b32 s69, 32, 0\n\t" \
    "s_sub_u32 s79, s79, s69\n\t" \
    "s_cmp_lt_u32 s65, 10\n\t" \
    "s_cselect_b32 s69, 64, 0\n\t" \
    "s_add_u32 s79, s79, s69\n\t" \
    "s_add_u32 s80, s79, s88\n\t" \
    "s_sub_u32 s70, s79, s74\n\t" \
    "s_max_i32 s70, s70, 0\n\t" \
    "s_min_i32 s70, s70, 16000\n\t" \
    "s_lshl_b32 s70, s70, 1\n\t" \
    "s_mul_hi_u32 s70, s70, s77\n\t" \
    "s_add_u32 s70, s70, s73\n\t" \
    "rr_W53_%=:\n\t" \
    "s_memtime s[56:57]\n\t" \
    "s_waitcnt lgkmcnt(0)\n\t" \
    "s_sub_u32 s69, s70, s56\n\t" \
    "s_cmp_gt_i32 s69, 24\n\t" \
    "s_cbranch_scc1 rr_W53_%=\n\t" \
    "rr_act3_%=:\n\t" \
    "s_add_u32 s69, s65, 0\n\t" \
    "s_and_b32 s69, s69, 7\n\t" \
    "s_lshl_b32 s69, s69, 18\n\t" \
    "s_add_u32 s69, s69, s62\n\t" \
    "s_add_u32 s46, s36, s69\n\t" \
    "s_addc_u32 s47, s37, 0\n\t" \
    "s_add_u32 s69, s65, 1\n\t" \
    "s_and_b32 s69, s69, 7\n\t" \
    "s_lshl_b32 s69, s69, 18\n\t" \
    "s_add_u32 s69, s69, s62\n\t" \
    "s_add_u32 s48, s36, s69\n\t" \
    "s_addc_u32 s49, s37, 0\n\t" \
    "s_and_b32 s69, s65, 7\n\t" \
    "s_lshl_b32 s69, s69, 18\n\t" \
    "s_add_u32 s69, s69, s63\n\t" \
    "s_add_u32 s52, s36, s69\n\t" \
    "s_addc_u32 s53, s37, 0\n\t" \
    "s_cmp_eq_u32 s65, 0\n\t" \
    "s_cbranch_scc1 rr_nf54_%=\n\t" \
    "s_add_u32 s69, s65, 2\n\t" \
    "s_cmp_ge_i32 s83, s69\n\t" \
    "s_cbranch_scc1 rr_nf54_%=\n\t" \
    "global_load_dword v164, v6, s[36:37] sc1\n\t" \
    "s_waitcnt vmcnt(0)\n\t" \
    "v_min_i32_dpp v164, v164, v164 row_ror:1 row_mask:0xf bank_mask:0xf\n\t" \
    "s_nop 1\n\t" \
    "v_min_i32_dpp v164, v164, v164 row_ror:2 row_mask:0xf bank_mask:0xf\n\t" \
    "s_nop 1\n\t" \
    "v_min_i32_dpp v164, v164, v164 row_ror:4 row_mask:0xf bank_mask:0xf\n\t" \
    "s_nop 1\n\t" \
    "v_min_i32_dpp v164, v164, v164 row_ror:8 row_mask:0xf bank_mask:0xf\n\t" \
    "s_nop 0\n\t" \
    "v_readlane_b32 s69, v164, 0\n\t" \
    "v_readlane_b32 s70, v164, 16\n\t" \
    "s_nop 1\n\t" \
    "s_min_i32 s83, s69, s70\n\t" \
    "rr_nf54_%=:\n\t" \
    "s_waitcnt lgkmcnt(0)\n\t" \
    "s_mov_b32 s70, 116\n\t" \
    "rr_E55_%=:\n\t" \
    "s_memtime s[56:57]\n\t" \
    "s_memrealtime s[2:3]\n\t" \
    "s_nop 0\n\t" \
    "s_memrealtime s[4:5]\n\t" \
    "s_nop 0\n\t" \
    "s_memrealtime s[6:7]\n\t" \
    "s_nop 0\n\t" \
    "s_memrealtime s[8:9]\n\t" \
    "s_nop 0\n\t" \
    "s_memrealtime s[10:11]\n\t" \
    "s_nop 0\n\t" \
    "s_memrealtime s[12:13]\n\t" \
    "s_nop 0\n\t" \
    "s_memrealtime s[14:15]\n\t" \
    "s_nop 0\n\t" \
    "s_memrealtime s[16:17]\n\t" \
    "s_nop 0\n\t" \
    "s_memrealtime s[18:19]\n\t" \
    "s_nop 0\n\t" \
    "s_memrealtime s[20:21]\n\t" \
    "s_nop 0\n\t" \
    "s_memrealtime s[22:23]\n\t" \
    "s_nop 0\n\t" \
    "s_memrealtime s[24:25]\n\t" \
    "s_nop 0\n\t" \
    "s_memrealtime s[26:27]\n\t" \
    "s_nop 0\n\t" \
    "s_memrealtime s[28:29]\n\t" \
    "s_memtime s[54:55]\n\t" \
    "s_add_u32 s70, s70, 8\n\t" \
    "s_waitcnt lgkmcnt(0)\n\t" \
    "s_sub_u32 s69, s54, s56\n\t" \
    "s_cmp_gt_u32 s69, s70\n\t" \
    "s_cbranch_scc1 rr_E55_%=\n\t" \
    "s_add_u32 s2, s2, s4\n\t" \
    "s_add_u32 s6, s6, s8\n\t" \
    "s_add_u32 s10, s10, s12\n\t" \
    "s_add_u32 s14, s14, s16\n\t" \
    "s_add_u32 s18, s18, s20\n\t" \
    "s_add_u32 s22, s22, s24\n\t" \
    "s_add_u32 s2, s2, s6\n\t" \
    "s_add_u32 s10, s10, s14\n\t" \
    "s_add_u32 s18, s18, s22\n\t" \
    "s_add_u32 s2, s2, s26\n\t" \
    "s_add_u32 s10, s10, s18\n\t" \
    "s_add_u32 s2, s2, s10\n\t" \
    "s_mul_i32 s70, s28, 13\n\t" \
    "s_sub_u32 s70, s70, s2\n\t" \
    "s_min_u32 s70, s70, 52\n\t" \
    "s_mul_i32 s69, s69, s70\n\t" \
    "s_mul_hi_u32 s69, s69, 76695844\n\t" \
    "s_mov_b32 s75, s73\n\t" \
    "s_mov_b32 s76, s74\n\t" \
    "s_add_u32 s73, s56, s69\n\t" \
    "s_lshl_b32 s74, s28, 4\n\t" \
    "s_sub_u32 s69, s73, s75\n\t" \
    "s_sub_u32 s70, s74, s76\n\t" \
    "s_sub_u32 s71, s70, 1600\n\t" \
    "s_cmp_lt_u32 s71, 400000\n\t" \
    "s_cbranch_scc0 rr_K56_%=\n\t" \
    "v_cvt_f32_u32 v164, s69\n\t" \
    "v_cvt_f32_u32 v165, s70\n\t" \
    "v_rcp_f32 v166, v164\n\t" \
    "v_rcp_f32 v167, v165\n\t" \
    "v_mul_f32 v166, v166, v165\n\t" \
    "v_mul_f32 v167, v167, v164\n\t" \
    "v_mul_f32 v166, 0x4f800000, v166\n\t" \
    "v_mul_f32 v167, 0x4f000000, v167\n\t" \
    "v_cvt_u32_f32 v166, v166\n\t" \
    "v_cvt_u32_f32 v167, v167\n\t" \
    "v_readfirstlane_b32 s69, v166\n\t" \
    "v_readfirstlane_b32 s70, v167\n\t" \
    "s_nop 1\n\t" \
    "s_lshr_b32 s71, s65, 1\n\t" \
    "s_min_u32 s71, s71, 1\n\t" \
    "s_sub_u32 s69, s69, s78\n\t" \
    "s_ashr_i32 s69, s69, s71\n\t" \
    "s_add_u32 s78, s78, s69\n\t" \
    "s_sub_u32 s70, s70, s77\n\t" \
    "s_ashr_i32 s70, s70, s71\n\t" \
    "s_add_u32 s77, s77, s70\n\t" \
    "rr_K56_%=:\n\t" \
    "s_mov_b32 s91, s64\n\t" \
    "s_cmp_lg_u32 s64, 0\n\t" \
    "s_cselect_b32 s77, 0, s77\n\t" \
    "v_mov_b32 v160, 0\n\t" \
    "v_mov_b32 v161, 0\n\t" \
    "v_mov_b32 v162, 0\n\t" \
    "v_mov_b32 v163, 0\n\t" \
    ".p2align 6\n\t" \
    "s_nop 0\n\t" \
    "s_nop 0\n\t" \
    "s_sub_u32 s70, s80, s74\n\t" \
    "s_max_i32 s70, s70, 0\n\t" \
    "s_min_i32 s70, s70, 16000\n\t" \
    "s_lshl_b32 s70, s70, 1\n\t" \
    "s_mul_hi_u32 s70, s70, s77\n\t" \
    "s_add_u32 s70, s70, s73\n\t" \
    "s_getpc_b64 s[30:31]\n\t" \
    "rr_Wp59_%=:\n\t" \
    "s_add_u32 s30, s30, rr_We60_%=-rr_Wp59_%=\n\t" \
    "s_addc_u32 s31, s31, 0\n\t" \
    "s_sub_u32 s70, s70, 96\n\t" \
    "rr_W58_%=:\n\t" \
    "s_memtime s[56:57]\n\t" \
    "s_waitcnt lgkmcnt(0)\n\t" \
    "s_sub_u32 s69, s70, s56\n\t" \
    "s_cmp_gt_i32 s69, 80\n\t" \
    "s_cbranch_scc1 rr_W58_%=\n\t" \
    "s_max_i32 s69, s69, 0\n\t" \
    "s_and_b32 s69, s69, -4\n\t" \
    "s_sub_u32 s30, s30, s69\n\t" \
    "s_subb_u32 s31, s31, 0\n\t" \
    "s_setpc_b64 s[30:31]\n\t" \
    "s_nop 0\n\t" \
    "s_nop 0\n\t" \
    "s_nop 0\n\t" \
    "s_nop 0\n\t" \
    "s_nop 0\n\t" \
    "s_nop 0\n\t" \
    "s_nop 0\n\t" \
    "s_nop 0\n\t" \
    "s_nop 0\n\t" \
    "s_nop 0\n\t" \
    "s_nop 0\n\t" \
    "s_nop 0\n\t" \
    "s_nop 0\n\t" \
    "s_nop 0\n\t" \
    "s_nop 0\n\t" \
    "s_nop 0\n\t" \
    "s_nop 0\n\t" \
    "s_nop 0\n\t" \
    "s_nop 0\n\t" \
    "s_nop 0\n\t" \
    "rr_We60_%=:\n\t" \
    "global_load_dword v192, v7, s[52:53] offset:0 sc1\n\t" \
    "s_sub_u32 s71, s86, s74\n\t" \
    "s_lshl_b32 s71, s71, 2\n\t" \
    "s_lshr_b32 s33, s77, 1\n\t" \
    "s_sub_u32 s34, s73, 176\n\t" \
    "s_mov_b32 s96, 0\n\t" \
    "s_getpc_b64 s[30:31]\n\t" \
    "rr_rpc64_%=:\n\t" \
    "s_add_u32 s50, s30, rr_pgo67_%=-rr_rpc64_%=\n\t" \
    "s_addc_u32 s51, s31, 0\n\t" \
    "s_add_u32 s30, s30, rr_again5_%=-rr_rpc64_%=\n\t" \
    "s_addc_u32 s31, s31, 0\n\t" \
    "s_nop 7\n\t" \
    "s_nop 7\n\t" \
    "s_nop 7\n\t" \
    "s_nop 7\n\t" \
    "s_nop 3\n\t" \
    "global_load_dword v193, v7, s[52:53] offset:2048 sc1\n\t" \
    "s_nop 7\n\t" \
    "s_nop 7\n\t" \
    "s_nop 7\n\t" \
    "s_nop 7\n\t" \
    "s_nop 7\n\t" \
    "s_nop 7\n\t" \
    "global_load_dword v194, v189, s[52:53] offset:0 sc1\n\t" \
    "s_waitcnt vmcnt(2)\n\t" \
    "rr_hok61_%=:\n\t" \
    "s_memtime s[54:55]\n\t" \
    "v_max_u32_dpp v192, v192, v192 row_ror:1 row_mask:0xf bank_mask:0xf\n\t" \
    "s_nop 1\n\t" \
    "v_max_u32_dpp v192, v192, v192 row_ror:2 row_mask:0xf bank_mask:0xf\n\t" \
    "s_nop 1\n\t" \
    "v_max_u32_dpp v192, v192, v192 row_ror:4 row_mask:0xf bank_mask:0xf\n\t" \
    "s_nop 1\n\t" \
    "v_max_u32_dpp v192, v192, v192 row_ror:8 row_mask:0xf bank_mask:0xf\n\t" \
    "s_nop 0\n\t" \
    "v_readlane_b32 s69, v192, 0\n\t" \
    "v_readlane_b32 s70, v192, 16\n\t" \
    "s_waitcnt lgkmcnt(0)\n\t" \
    "s_sub_u32 s56, s34, s54\n\t" \
    "s_max_u32 s81, s69, s70\n\t" \
    "s_cmp_eq_u32 s81, -1\n\t" \
    "s_cbranch_scc1 rr_hlate62_%=\n\t" \
    "s_lshl2_add_u32 s70, s81, s71\n\t" \
    "s_mul_hi_i32 s70, s70, s33\n\t" \
    "s_add_u32 s69, s70, s56\n\t" \
    "s_max_i32 s69, s69, 0\n\t" \
    "s_cmp_gt_i32 s69, 80\n\t" \
    "s_cbranch_scc1 rr_rpoll65_%=\n\t" \
    "rr_rback66_%=:\n\t" \
    "s_and_b32 s69, s69, -4\n\t" \
    "s_sub_u32 s30, s30, s69\n\t" \
    "s_subb_u32 s31, s31, 0\n\t" \
    "s_setpc_b64 s[30:31]\n\t" \
    "rr_rpoll65_%=:\n\t" \
    "s_min_i32 s70, s70, 24000\n\t" \
    "s_add_u32 s70, s70, s73\n\t" \
    "s_sub_u32 s70, s70, 124\n\t" \
    "s_sub_u32 s70, s70, s96\n\t" \
    "rr_wl63_%=:\n\t" \
    "s_memtime s[56:57]\n\t" \
    "s_waitcnt lgkmcnt(0)\n\t" \
    "s_sub_u32 s69, s70, s56\n\t" \
    "s_cmp_gt_i32 s69, 80\n\t" \
    "s_cbranch_scc1 rr_wl63_%=\n\t" \
    "s_max_i32 s69, s69, 0\n\t" \
    "s_branch rr_rback66_%=\n\t" \
    "s_nop 0\n\t" \
    "s_nop 0\n\t" \
    "s_nop 0\n\t" \
    "s_nop 0\n\t" \
    "s_nop 0\n\t" \
    "s_nop 0\n\t" \
    "s_nop 0\n\t" \
    "s_nop 0\n\t" \
    "s_nop 0\n\t" \
    "s_nop 0\n\t" \
    "s_nop 0\n\t" \
    "s_nop 0\n\t" \
    "s_nop 0\n\t" \
    "s_nop 0\n\t" \
    "s_nop 0\n\t" \
    "s_nop 0\n\t" \
    "s_nop 0\n\t" \
    "s_nop 0\n\t" \
    "s_nop 0\n\t" \
    "s_nop 0\n\t" \
    "rr_again5_%=:\n\t" \
    "rr_pgo67_%=:\n\t" \
    "global_load_dwordx4 v[48:51], v10, s[46:47] sc1\n\t" \
    "global_load_dwordx4 v[52:55], v11, s[46:47] sc1\n\t" \
    "global_load_dwordx4 v[56:59], v12, s[46:47] sc1\n\t" \
    "global_load_dwordx4 v[60:63], v13, s[46:47] sc1\n\t" \
    "global_load_dwordx4 v[64:67], v14, s[46:47] sc1\n\t" \
    "global_load_dwordx4 v[68:71], v15, s[46:47] sc1\n\t" \
    "global_load_dwordx4 v[72:75], v16, s[46:47] sc1\n\t" \
    "global_load_dwordx4 v[76:79], v17, s[46:47] sc1\n\t" \
    "global_load_dwordx4 v[80:83], v18, s[46:47] sc1\n\t" \
    "global_load_dwordx4 v[84:87], v19, s[46:47] sc1\n\t" \
    "global_load_dwordx4 v[88:91], v20, s[46:47] sc1\n\t" \
    "global_load_dwordx4 v[92:95], v21, s[46:47] sc1\n\t" \
    "global_load_dwordx4 v[96:99], v22, s[46:47] sc1\n\t" \
    "global_load_dwordx4 v[100:103], v23, s[46:47] sc1\n\t" \
    "global_load_dwordx4 v[104:107], v24, s[46:47] sc1\n\t" \
    "global_load_dwordx4 v[108:111], v25, s[46:47] sc1\n\t" \
    "global_load_dwordx4 v[112:115], v42, s[46:47] sc1\n\t" \
    "global_load_dwordx4 v[116:119], v43, s[46:47] sc1\n\t" \
    "global_load_dwordx4 v[120:123], v44, s[46:47] sc1\n\t" \
    "global_load_dwordx4 v[124:127], v45, s[46:47] sc1\n\t" \
    "global_load_dwordx4 v[128:131], v0, s[40:41] offset:0 sc1\n\t" \
    "global_load_dwordx4 v[132:135], v0, s[40:41] offset:1024 sc1\n\t" \
    "global_load_dwordx4 v[136:139], v0, s[40:41] offset:2048 sc1\n\t" \
    "global_load_dwordx4 v[140:143], v0, s[40:41] offset:3072 sc1\n\t" \
    "global_load_dwordx4 v[144:147], v1, s[40:41] offset:0 sc1\n\t" \
    "global_load_dwordx4 v[148:151], v1, s[40:41] offset:1024 sc1\n\t" \
    "global_load_dwordx4 v[152:155], v1, s[40:41] offset:2048 sc1\n\t" \
    "global_load_dwordx4 v[156:159], v1, s[40:41] offset:3072 sc1\n\t" \
    "s_cmp_lg_u32 s91, 0\n\t" \
    "s_cbranch_scc1 rr_nob8_%=\n\t" \
    "v_mov_b32 v164, s81\n\t" \
    "ds_write_b32 v9, v164\n\t" \
    "s_waitcnt lgkmcnt(0)\n\t" \
    "s_barrier\n\t" \
    "rr_nob8_%=:\n\t" \
    "s_waitcnt vmcnt(27)\n\t" \
    "v_smfmac_f32_16x16x64_bf16 v[160:163], v[48:51], a[0:7], v4\n\t" \
    "s_waitcnt vmcnt(26)\n\t" \
    "v_smfmac_f32_16x16x64_bf16 v[160:163], v[52:55], a[8:15], v4\n\t" \
    "s_waitcnt vmcnt(25)\n\t" \
    "v_smfmac_f32_16x16x64_bf16 v[160:163], v[56:59], a[16:23], v4\n\t" \
    "s_waitcnt vmcnt(24)\n\t" \
    "v_smfmac_f32_16x16x64_bf16 v[160:163], v[60:63], a[24:31], v4\n\t" \
    "s_waitcnt vmcnt(23)\n\t" \
    "v_smfmac_f32_16x16x64_bf16 v[160:163], v[64:67], a[32:39], v4\n\t" \
    "s_waitcnt vmcnt(22)\n\t" \
    "v_smfmac_f32_16x16x64_bf16 v[160:163], v[68:71], a[40:47], v4\n\t" \
    "s_waitcnt vmcnt(21)\n\t" \
    "v_smfmac_f32_16x16x64_bf16 v[160:163], v[72:75], a[48:55], v4\n\t" \
    "s_waitcnt vmcnt(20)\n\t" \
    "v_smfmac_f32_16x16x64_bf16 v[160:163], v[76:79], a[56:63], v4\n\t" \
    "s_waitcnt vmcnt(19)\n\t" \
    "v_smfmac_f32_16x16x64_bf16 v[160:163], v[80:83], a[64:71], v4\n\t" \
    "s_waitcnt vmcnt(18)\n\t" \
    "v_smfmac_f32_16x16x64_bf16 v[160:163], v[84:87], a[72:79], v4\n\t" \
    "s_waitcnt vmcnt(17)\n\t" \
    "v_smfmac_f32_16x16x64_bf16 v[160:163], v[88:91], a[80:87], v4\n\t" \
    "s_waitcnt vmcnt(16)\n\t" \
    "v_smfmac_f32_16x16x64_bf16 v[160:163], v[92:95], a[88:95], v4\n\t" \
    "s_waitcnt vmcnt(15)\n\t" \
    "v_smfmac_f32_16x16x64_bf16 v[160:163], v[96:99], a[96:103], v4\n\t" \
    "s_waitcnt vmcnt(14)\n\t" \
    "v_smfmac_f32_16x16x64_bf16 v[160:163], v[100:103], a[104:111], v4\n\t" \
    "s_waitcnt vmcnt(13)\n\t" \
    "v_smfmac_f32_16x16x64_bf16 v[160:163], v[104:107], a[112:119], v4\n\t" \
    "s_waitcnt vmcnt(12)\n\t" \
    "v_smfmac_f32_16x16x64_bf16 v[160:163], v[108:111], a[120:127], v4\n\t" \
    "s_waitcnt vmcnt(6)\n\t" \
    "v_smfmac_f32_16x16x64_bf16 v[160:163], v[112:115], v[128:135], v4\n\t" \
    "s_waitcnt vmcnt(4)\n\t" \
    "v_smfmac_f32_16x16x64_bf16 v[160:163], v[116:119], v[136:143], v4\n\t" \
    "s_waitcnt vmcnt(2)\n\t" \
    "v_smfmac_f32_16x16x64_bf16 v[160:163], v[120:123], v[144:151], v4\n\t" \
    "s_waitcnt vmcnt(0)\n\t" \
    "v_smfmac_f32_16x16x64_bf16 v[160:163], v[124:127], v[152:159], v4\n\t" \
    "s_nop 7\n\t" \
    "v_pk_add_f32 v[160:161], v[160:161], v[162:163]\n\t" \
    "v_cvt_pk_bf16_f32 v164, v160, v161\n\t" \
    "v_cmp_u_f32 vcc, v160, v161\n\t" \
    "s_cbranch_vccnz rr_bad6_%=\n\t" \
    "rr_cont7_%=:\n\t" \
    "v_mov_b32_dpp v165, v164 quad_perm:[1,0,3,2] row_mask:0xf bank_mask:0xf\n\t" \
    "v_perm_b32 v164, v165, v164, v5\n\t" \
    "global_store_dword v180, v164, s[48:49] sc1\n\t" \
    "global_store_dword v181, v164, s[48:49] sc1\n\t" \
    "global_store_dword v182, v164, s[48:49] sc1\n\t" \
    "global_store_dword v2, v164, s[48:49] sc1\n\t" \
    "s_cmp_lg_u32 s64, 0\n\t" \
    "s_cbranch_scc1 rr_g214_%=\n\t" \
    "s_cmp_eq_u32 s65, 62\n\t" \
    "s_cbranch_scc1 rr_end4_%=\n\t" \
    "s_add_u32 s65, s65, 2\n\t" \
    "s_branch rr_pf9_%=\n\t" \
    "rr_bad6_%=:\n\t" \
    "s_cmp_lg_u32 s64, 0\n\t" \
    "s_cbranch_scc1 rr_cont7_%=\n\t" \
    "s_waitcnt lgkmcnt(0)\n\t" \
    "v_max_u32 v165, v48, v49\n\t" \
    "v_max3_u32 v165, v165, v50, v51\n\t" \
    "v_max3_u32 v165, v165, v52, v53\n\t" \
    "v_max3_u32 v165, v165, v54, v55\n\t" \
    "v_max3_u32 v165, v165, v56, v57\n\t" \
    "v_max3_u32 v165, v165, v58, v59\n\t" \
    "v_max3_u32 v165, v165, v60, v61\n\t" \
    "v_max3_u32 v165, v165, v62, v63\n\t" \
    "v_max3_u32 v165, v165, v64, v65\n\t" \
    "v_max3_u32 v165, v165, v66, v67\n\t" \
    "v_max3_u32 v165, v165, v68, v69\n\t" \
    "v_max3_u32 v165, v165, v70, v71\n\t" \
    "v_max3_u32 v165, v165, v72, v73\n\t" \
    "v_max3_u32 v165, v165, v74, v75\n\t" \
    "v_max3_u32 v165, v165, v76, v77\n\t" \
    "v_max3_u32 v165, v165, v78, v79\n\t" \
    "v_max3_u32 v165, v165, v80, v81\n\t" \
    "v_max3_u32 v165, v165, v82, v83\n\t" \
    "v_max3_u32 v165, v165, v84, v85\n\t" \
    "v_max3_u32 v165, v165, v86, v87\n\t" \
    "v_max3_u32 v165, v165, v88, v89\n\t" \
    "v_max3_u32 v165, v165, v90, v91\n\t" \
    "v_max3_u32 v165, v165, v92, v93\n\t" \
    "v_max3_u32 v165, v165, v94, v95\n\t" \
    "v_max3_u32 v165, v165, v96, v97\n\t" \
    "v_max3_u32 v165, v165, v98, v99\n\t" \
    "v_max3_u32 v165, v165, v100, v101\n\t" \
    "v_max3_u32 v165, v165, v102, v103\n\t" \
    "v_max3_u32 v165, v165, v104, v105\n\t" \
    "v_max3_u32 v165, v165, v106, v107\n\t" \
    "v_max3_u32 v165, v165, v108, v109\n\t" \
    "v_max3_u32 v165, v165, v110, v111\n\t" \
    "v_max3_u32 v165, v165, v112, v113\n\t" \
    "v_max3_u32 v165, v165, v114, v115\n\t" \
    "v_max3_u32 v165, v165, v116, v117\n\t" \
    "v_max3_u32 v165, v165, v118, v119\n\t" \
    "v_max3_u32 v165, v165, v120, v121\n\t" \
    "v_max3_u32 v165, v165, v122, v123\n\t" \
    "v_max3_u32 v165, v165, v124, v125\n\t" \
    "v_max3_u32 v165, v165, v126, v127\n\t" \
    "v_max_u32 v166, v128, v129\n\t" \
    "v_max3_u32 v166, v166, v130, v131\n\t" \
    "v_max3_u32 v166, v166, v132, v133\n\t" \
    "v_max3_u32 v166, v166, v134, v135\n\t" \
    "v_max3_u32 v166, v166, v136, v137\n\t" \
    "v_max3_u32 v166, v166, v138, v139\n\t" \
    "v_max3_u32 v166, v166, v140, v141\n\t" \
    "v_max3_u32 v166, v166, v142, v143\n\t" \
    "v_max3_u32 v166, v166, v144, v145\n\t" \
    "v_max3_u32 v166, v166, v146, v147\n\t" \
    "v_max3_u32 v166, v166, v148, v149\n\t" \
    "v_max3_u32 v166, v166, v150, v151\n\t" \
    "v_max3_u32 v166, v166, v152, v153\n\t" \
    "v_max3_u32 v166, v166, v154, v155\n\t" \
    "v_max3_u32 v166, v166, v156, v157\n\t" \
    "v_max3_u32 v166, v166, v158, v159\n\t" \
    "v_max_u32 v165, v165, v166\n\t" \
    "v_cmp_eq_u32 vcc, -1, v165\n\t" \
    "s_cbranch_vccz rr_cont7_%=\n\t" \
    "s_mov_b32 s91, 1\n\t" \
    "s_mov_b32 s96, 0\n\t" \
    "v_mov_b32 v160, 0\n\t" \
    "v_mov_b32 v161, 0\n\t" \
    "v_mov_b32 v162, 0\n\t" \
    "v_mov_b32 v163, 0\n\t" \
    "s_cmp_eq_u32 s68, 0\n\t" \
    "s_cbranch_scc1 rr_F68_%=\n\t" \
    "s_sub_u32 s68, s68, 1\n\t" \
    "s_branch rr_again5_%=\n\t" \
    "rr_F68_%=:\n\t" \
    "v_mov_b32 v166, 5767424\n\t" \
    "v_mov_b32 v167, 3\n\t" \
    "global_store_dword v166, v167, s[36:37] sc1\n\t" \
    "s_waitcnt vmcnt(0)\n\t" \
    "s_branch rr_cont7_%=\n\t" \
    "rr_hlate62_%=:\n\t" \
    "s_cmp_lg_u32 s64, 0\n\t" \
    "s_cbranch_scc1 rr_hpd70_%=\n\t" \
    "global_load_dword v195, v189, s[52:53] offset:2048 sc1\n\t" \
    "s_waitcnt vmcnt(2)\n\t" \
    "v_mov_b32 v192, v193\n\t" \
    "v_cmp_eq_u32 vcc, -1, v193\n\t" \
    "s_cbranch_vccz rr_hok61_%=\n\t" \
    "global_load_dword v196, v190, s[52:53] offset:0 sc1\n\t" \
    "s_waitcnt vmcnt(2)\n\t" \
    "v_mov_b32 v192, v194\n\t" \
    "v_cmp_eq_u32 vcc, -1, v194\n\t" \
    "s_cbranch_vccz rr_hok61_%=\n\t" \
    "global_load_dword v197, v190, s[52:53] offset:2048 sc1\n\t" \
    "s_waitcnt vmcnt(2)\n\t" \
    "v_mov_b32 v192, v195\n\t" \
    "v_cmp_eq_u32 vcc, -1, v195\n\t" \
    "s_cbranch_vccz rr_hok61_%=\n\t" \
    "s_waitcnt vmcnt(1)\n\t" \
    "v_mov_b32 v192, v196\n\t" \
    "v_cmp_eq_u32 vcc, -1, v196\n\t" \
    "s_cbranch_vccz rr_hok61_%=\n\t" \
    "s_waitcnt vmcnt(0)\n\t" \
    "v_mov_b32 v192, v197\n\t" \
    "v_cmp_eq_u32 vcc, -1, v197\n\t" \
    "s_cbranch_vccz rr_hok61_%=\n\t" \
    "rr_hp69_%=:\n\t" \
    "global_load_dword v192, v191, s[52:53] offset:0 sc1\n\t" \
    "s_waitcnt vmcnt(0)\n\t" \
    "v_cmp_eq_u32 vcc, -1, v192\n\t" \
    "s_cbranch_vccz rr_hpd70_%=\n\t" \
    "s_cmp_eq_u32 s68, 0\n\t" \
    "s_cbranch_scc1 rr_F71_%=\n\t" \
    "s_sub_u32 s68, s68, 1\n\t" \
    "s_sleep 1\n\t" \
    "s_branch rr_hp69_%=\n\t" \
    "rr_F71_%=:\n\t" \
    "v_mov_b32 v166, 5767424\n\t" \
    "v_mov_b32 v167, 7\n\t" \
    "global_store_dword v166, v167, s[36:37] sc1\n\t" \
    "s_waitcnt vmcnt(0)\n\t" \
    "s_branch rr_hpd70_%=\n\t" \
    "rr_hpd70_%=:\n\t" \
    "v_and_b32 v192, -2, v192\n\t" \
    "s_waitcnt lgkmcnt(0)\n\t" \
    "s_mov_b32 s70, 116\n\t" \
    "rr_E72_%=:\n\t" \
    "s_memtime s[56:57]\n\t" \
    "s_memrealtime s[2:3]\n\t" \
    "s_nop 0\n\t" \
    "s_memrealtime s[4:5]\n\t" \
    "s_nop 0\n\t" \
    "s_memrealtime s[6:7]\n\t" \
    "s_nop 0\n\t" \
    "s_memrealtime s[8:9]\n\t" \
    "s_nop 0\n\t" \
    "s_memrealtime s[10:11]\n\t" \
    "s_nop 0\n\t" \
    "s_memrealtime s[12:13]\n\t" \
    "s_nop 0\n\t" \
    "s_memrealtime s[14:15]\n\t" \
    "s_nop 0\n\t" \
    "s_memrealtime s[16:17]\n\t" \
    "s_nop 0\n\t" \
    "s_memrealtime s[18:19]\n\t" \
    "s_nop 0\n\t" \
    "s_memrealtime s[20:21]\n\t" \
    "s_nop 0\n\t" \
    "s_memrealtime s[22:23]\n\t" \
    "s_nop 0\n\t" \
    "s_memrealtime s[24:25]\n\t" \
    "s_nop 0\n\t" \
    "s_memrealtime s[26:27]\n\t" \
    "s_nop 0\n\t" \
    "s_memrealtime s[28:29]\n\t" \
    "s_memtime s[54:55]\n\t" \
    "s_add_u32 s70, s70, 8\n\t" \
    "s_waitcnt lgkmcnt(0)\n\t" \
    "s_sub_u32 s69, s54, s56\n\t" \
    "s_cmp_gt_u32 s69, s70\n\t" \
    "s_cbranch_scc1 rr_E72_%=\n\t" \
    "s_add_u32 s2, s2, s4\n\t" \
    "s_add_u32 s6, s6, s8\n\t" \
    "s_add_u32 s10, s10, s12\n\t" \
    "s_add_u32 s14, s14, s16\n\t" \
    "s_add_u32 s18, s18, s20\n\t" \
    "s_add_u32 s22, s22, s24\n\t" \
    "s_add_u32 s2, s2, s6\n\t" \
    "s_add_u32 s10, s10, s14\n\t" \
    "s_add_u32 s18, s18, s22\n\t" \
    "s_add_u32 s2, s2, s26\n\t" \
    "s_add_u32 s10, s10, s18\n\t" \
    "s_add_u32 s2, s2, s10\n\t" \
    "s_mul_i32 s70, s28, 13\n\t" \
    "s_sub_u32 s70, s70, s2\n\t" \
    "s_min_u32 s70, s70, 52\n\t" \
    "s_mul_i32 s69, s69, s70\n\t" \
    "s_mul_hi_u32 s69, s69, 76695844\n\t" \
    "s_mov_b32 s75, s73\n\t" \
    "s_mov_b32 s76, s74\n\t" \
    "s_add_u32 s73, s56, s69\n\t" \
    "s_lshl_b32 s74, s28, 4\n\t" \
    "s_sub_u32 s71, s86, s74\n\t" \
    "s_lshl_b32 s71, s71, 2\n\t" \
    "s_lshr_b32 s33, s77, 1\n\t" \
    "s_sub_u32 s34, s73, 176\n\t" \
    "s_branch rr_hok61_%=\n\t" \
    "rr_end4_%=:\n\t" \
    "s_endpgm\n\t" \
    "rr_last_%=:\n\t" \
    ""
#define ASM_H \
    "s_mov_b64 s[36:37], %0\n\t" \
    "s_mov_b64 s[42:43], %1\n\t" \
    "s_mov_b64 s[44:45], %2\n\t" \
    "s_mov_b32 s66, %3\n\t" \
    "s_mov_b32 s67, %4\n\t" \
    "s_mov_b64 s[50:51], %5\n\t" \
    "s_mov_b32 s82, %7\n\t" \
    "s_load_dwordx8 s[84:91], %6, 0\n\t" \
    "v_mbcnt_lo_u32_b32 v223, -1, 0\n\t" \
    "v_mbcnt_hi_u32_b32 v223, -1, v223\n\t" \
    "s_getpc_b64 s[46:47]\n\t" \
    "hh_pc_%=:\n\t" \
    "s_lshl_b32 s69, s66, 13\n\t" \
    "v_lshl_add_u32 v222, v223, 7, s69\n\t" \
    "v_min_u32 v222, hh_last_%=-hh_pc_%=-4, v222\n\t" \
    "global_load_dword v222, v222, s[46:47]\n\t" \
    "v_lshlrev_b32 v223, 2, v223\n\t" \
    "v_add_u32 v223, s82, v223\n\t" \
    "ds_read_b32 v0, v223 offset:0\n\t" \
    "ds_read_b32 v1, v223 offset:256\n\t" \
    "ds_read_b32 v2, v223 offset:512\n\t" \
    "ds_read_b32 v3, v223 offset:768\n\t" \
    "ds_read_b32 v4, v223 offset:1024\n\t" \
    "ds_read_b32 v5, v223 offset:1280\n\t" \
    "ds_read_b32 v6, v223 offset:1536\n\t" \
    "ds_read_b32 v7, v223 offset:1792\n\t" \
    "ds_read_b32 v8, v223 offset:2048\n\t" \
    "ds_read_b32 v9, v223 offset:2304\n\t" \
    "ds_read_b32 v10, v223 offset:2560\n\t" \
    "ds_read_b32 v11, v223 offset:2816\n\t" \
    "s_waitcnt lgkmcnt(0)\n\t" \
    "ds_read_b32 v12, v223 offset:3072\n\t" \
    "ds_read_b32 v13, v223 offset:3328\n\t" \
    "ds_read_b32 v14, v223 offset:3584\n\t" \
    "ds_read_b32 v15, v223 offset:3840\n\t" \
    "ds_read_b32 v16, v223 offset:4096\n\t" \
    "ds_read_b32 v17, v223 offset:4352\n\t" \
    "s_waitcnt lgkmcnt(0)\n\t" \
    "s_mov_b32 s68, 262144\n\t" \
    "s_mov_b32 s72, 0\n\t" \
    "s_mov_b32 s83, -1\n\t" \
    "s_mov_b32 s65, 0\n\t" \
    "s_mov_b32 s77, 3019898880\n\t" \
    "s_mov_b32 s78, 3054198966\n\t" \
    "s_waitcnt lgkmcnt(0)\n\t" \
    "s_mov_b32 s70, 116\n\t" \
    "hh_E1_%=:\n\t" \
    "s_memtime s[56:57]\n\t" \
    "s_memrealtime s[2:3]\n\t" \
    "s_nop 0\n\t" \
    "s_memrealtime s[4:5]\n\t" \
    "s_nop 0\n\t" \
    "s_memrealtime s[6:7]\n\t" \
    "s_nop 0\n\t" \
    "s_memrealtime s[8:9]\n\t" \
    "s_nop 0\n\t" \
    "s_memrealtime s[10:11]\n\t" \
    "s_nop 0\n\t" \
    "s_memrealtime s[12:13]\n\t" \
    "s_nop 0\n\t" \
    "s_memrealtime s[14:15]\n\t" \
    "s_nop 0\n\t" \
    "s_memrealtime s[16:17]\n\t" \
    "s_nop 0\n\t" \
    "s_memrealtime s[18:19]\n\t" \
    "s_nop 0\n\t" \
    "s_memrealtime s[20:21]\n\t" \
    "s_nop 0\n\t" \
    "s_memrealtime s[22:23]\n\t" \
    "s_nop 0\n\t" \
    "s_memrealtime s[24:25]\n\t" \
    "s_nop 0\n\t" \
    "s_memrealtime s[26:27]\n\t" \
    "s_nop 0\n\t" \
    "s_memrealtime s[28:29]\n\t" \
    "s_memtime s[54:55]\n\t" \
    "s_add_u32 s70, s70, 8\n\t" \
    "s_waitcnt lgkmcnt(0)\n\t" \
    "s_sub_u32 s69, s54, s56\n\t" \
    "s_cmp_gt_u32 s69, s70\n\t" \
    "s_cbranch_scc1 hh_E1_%=\n\t" \
    "s_add_u32 s2, s2, s4\n\t" \
    "s_add_u32 s6, s6, s8\n\t" \
    "s_add_u32 s10, s10, s12\n\t" \
    "s_add_u32 s14, s14, s16\n\t" \
    "s_add_u32 s18, s18, s20\n\t" \
    "s_add_u32 s22, s22, s24\n\t" \
    "s_add_u32 s2, s2, s6\n\t" \
    "s_add_u32 s10, s10, s14\n\t" \
    "s_add_u32 s18, s18, s22\n\t" \
    "s_add_u32 s2, s2, s26\n\t" \
    "s_add_u32 s10, s10, s18\n\t" \
    "s_add_u32 s2, s2, s10\n\t" \
    "s_mul_i32 s70, s28, 13\n\t" \
    "s_sub_u32 s70, s70, s2\n\t" \
    "s_min_u32 s70, s70, 52\n\t" \
    "s_mul_i32 s69, s69, s70\n\t" \
    "s_mul_hi_u32 s69, s69, 76695844\n\t" \
    "s_mov_b32 s75, s73\n\t" \
    "s_mov_b32 s76, s74\n\t" \
    "s_add_u32 s73, s56, s69\n\t" \
    "s_lshl_b32 s74, s28, 4\n\t" \
    "v_mbcnt_lo_u32_b32 v30, -1, 0\n\t" \
    "v_mbcnt_hi_u32_b32 v30, -1, v30\n\t" \
    "v_lshlrev_b32 v30, 6, v30\n\t" \
    "v_add_u32 v30, 6225920, v30\n\t" \
    "v_mov_b32 v31, 6234112\n\t" \
    "global_load_dword v31, v31, s[36:37] sc1\n\t" \
    "global_load_dwordx2 v[32:33], v30, s[36:37] sc1\n\t" \
    "global_load_dword v34, v30, s[36:37] offset:2048 sc1\n\t" \
    "s_waitcnt vmcnt(0)\n\t" \
    "hh_C2_%=:\n\t" \
    "v_cmp_ne_u32 s[54:55], v32, v34\n\t" \
    "v_sub_u32 v32, v32, v31\n\t" \
    "v_cmp_ne_u32 vcc, 1, v32\n\t" \
    "s_nop 1\n\t" \
    "s_or_b64 vcc, vcc, s[54:55]\n\t" \
    "s_cbranch_scc0 hh_Cok3_%=\n\t" \
    "s_cmp_eq_u32 s68, 0\n\t" \
    "s_cbranch_scc1 hh_F5_%=\n\t" \
    "s_sub_u32 s68, s68, 1\n\t" \
    "s_sleep 6\n\t" \
    "s_branch hh_Cag4_%=\n\t" \
    "hh_F5_%=:\n\t" \
    "v_mov_b32 v34, 5767424\n\t" \
    "v_mov_b32 v35, 4\n\t" \
    "global_store_dword v34, v35, s[36:37] sc1\n\t" \
    "s_waitcnt vmcnt(0)\n\t" \
    "s_branch hh_Cok3_%=\n\t" \
    "hh_Cag4_%=:\n\t" \
    "global_load_dwordx2 v[32:33], v30, s[36:37] sc1\n\t" \
    "global_load_dword v34, v30, s[36:37] offset:2048 sc1\n\t" \
    "s_waitcnt vmcnt(0)\n\t" \
    "s_branch hh_C2_%=\n\t" \
    "hh_Cok3_%=:\n\t" \
    "v_mov_b32_dpp v30, v33 quad_perm:[1,0,3,2] row_mask:0xf bank_mask:0xf\n\t" \
    "v_cmp_lt_u32 s[44:45], v30, v33\n\t" \
    "s_nop 1\n\t" \
    "s_bitcmp1_b32 s44, 0\n\t" \
    "s_cselect_b32 s62, 8192, 0\n\t" \
    "s_bitcmp1_b32 s44, 2\n\t" \
    "s_cselect_b32 s63, 8192, 0\n\t" \
    "s_or_b32 s63, s63, 0x200000\n\t" \
    "v_min_u32 v30, v30, v33\n\t" \
    "v_cmp_lt_u32 vcc, 660, v30\n\t" \
    "s_nop 1\n\t" \
    "s_cmp_lg_u64 vcc, 0\n\t" \
    "s_cselect_b32 s69, 1, 0\n\t" \
    "s_mul_i32 s70, s69, 160\n\t" \
    "s_add_u32 s86, s86, s70\n\t" \
    "s_mul_i32 s70, s69, 536\n\t" \
    "s_add_u32 s87, s87, s70\n\t" \
    "s_mul_i32 s70, s69, 256\n\t" \
    "s_add_u32 s88, s88, s70\n\t" \
    "s_waitcnt lgkmcnt(0)\n\t" \
    "s_mov_b32 s70, 116\n\t" \
    "hh_E6_%=:\n\t" \
    "s_memtime s[56:57]\n\t" \
    "s_memrealtime s[2:3]\n\t" \
    "s_nop 0\n\t" \
    "s_memrealtime s[4:5]\n\t" \
    "s_nop 0\n\t" \
    "s_memrealtime s[6:7]\n\t" \
    "s_nop 0\n\t" \
    "s_memrealtime s[8:9]\n\t" \
    "s_nop 0\n\t" \
    "s_memrealtime s[10:11]\n\t" \
    "s_nop 0\n\t" \
    "s_memrealtime s[12:13]\n\t" \
    "s_nop 0\n\t" \
    "s_memrealtime s[14:15]\n\t" \
    "s_nop 0\n\t" \
    "s_memrealtime s[16:17]\n\t" \
    "s_nop 0\n\t" \
    "s_memrealtime s[18:19]\n\t" \
    "s_nop 0\n\t" \
    "s_memrealtime s[20:21]\n\t" \
    "s_nop 0\n\t" \
    "s_memrealtime s[22:23]\n\t" \
    "s_nop 0\n\t" \
    "s_memrealtime s[24:25]\n\t" \
    "s_nop 0\n\t" \
    "s_memrealtime s[26:27]\n\t" \
    "s_nop 0\n\t" \
    "s_memrealtime s[28:29]\n\t" \
    "s_memtime s[54:55]\n\t" \
    "s_add_u32 s70, s70, 8\n\t" \
    "s_waitcnt lgkmcnt(0)\n\t" \
    "s_sub_u32 s69, s54, s56\n\t" \
    "s_cmp_gt_u32 s69, s70\n\t" \
    "s_cbranch_scc1 hh_E6_%=\n\t" \
    "s_add_u32 s2, s2, s4\n\t" \
    "s_add_u32 s6, s6, s8\n\t" \
    "s_add_u32 s10, s10, s12\n\t" \
    "s_add_u32 s14, s14, s16\n\t" \
    "s_add_u32 s18, s18, s20\n\t" \
    "s_add_u32 s22, s22, s24\n\t" \
    "s_add_u32 s2, s2, s6\n\t" \
    "s_add_u32 s10, s10, s14\n\t" \
    "s_add_u32 s18, s18, s22\n\t" \
    "s_add_u32 s2, s2, s26\n\t" \
    "s_add_u32 s10, s10, s18\n\t" \
    "s_add_u32 s2, s2, s10\n\t" \
    "s_mul_i32 s70, s28, 13\n\t" \
    "s_sub_u32 s70, s70, s2\n\t" \
    "s_min_u32 s70, s70, 52\n\t" \
    "s_mul_i32 s69, s69, s70\n\t" \
    "s_mul_hi_u32 s69, s69, 76695844\n\t" \
    "s_mov_b32 s75, s73\n\t" \
    "s_mov_b32 s76, s74\n\t" \
    "s_add_u32 s73, s56, s69\n\t" \
    "s_lshl_b32 s74, s28, 4\n\t" \
    "s_sub_u32 s69, s73, s75\n\t" \
    "s_sub_u32 s70, s74, s76\n\t" \
    "s_sub_u32 s71, s70, 1600\n\t" \
    "s_cmp_lt_u32 s71, 400000\n\t" \
    "s_cbranch_scc0 hh_K7_%=\n\t" \
    "v_cvt_f32_u32 v30, s69\n\t" \
    "v_cvt_f32_u32 v31, s70\n\t" \
    "v_rcp_f32 v32, v30\n\t" \
    "v_rcp_f32 v33, v31\n\t" \
    "v_mul_f32 v32, v32, v31\n\t" \
    "v_mul_f32 v33, v33, v30\n\t" \
    "v_mul_f32 v32, 0x4f800000, v32\n\t" \
    "v_mul_f32 v33, 0x4f000000, v33\n\t" \
    "v_cvt_u32_f32 v32, v32\n\t" \
    "v_cvt_u32_f32 v33, v33\n\t" \
    "v_readfirstlane_b32 s69, v32\n\t" \
    "v_readfirstlane_b32 s70, v33\n\t" \
    "s_nop 1\n\t" \
    "s_mov_b32 s78, s69\n\t" \
    "s_mov_b32 s77, s70\n\t" \
    "hh_K7_%=:\n\t" \
    "s_sub_u32 s80, s74, 32000\n\t" \
    "v_mov_b32 v38, -1\n\t" \
    "v_mov_b32 v39, -1\n\t" \
    "s_mov_b32 m0, 155648\n\t" \
    "s_mul_i32 s96, s67, 16\n\t" \
    "s_add_u32 s96, s96, 64\n\t" \
    "hh_loop8_%=:\n\t" \
    "s_and_b32 s69, s65, 7\n\t" \
    "s_lshl_b32 s69, s69, 18\n\t" \
    "s_add_u32 s69, s69, s63\n\t" \
    "s_add_u32 s52, s36, s69\n\t" \
    "s_addc_u32 s53, s37, 0\n\t" \
    "s_add_u32 s71, s80, 608\n\t" \
    "s_sub_u32 s70, s71, s74\n\t" \
    "s_max_i32 s70, s70, 0\n\t" \
    "s_min_i32 s70, s70, 16000\n\t" \
    "s_lshl_b32 s70, s70, 1\n\t" \
    "s_mul_hi_u32 s70, s70, s77\n\t" \
    "s_add_u32 s70, s70, s73\n\t" \
    "hh_W11_%=:\n\t" \
    "s_memtime s[56:57]\n\t" \
    "s_waitcnt lgkmcnt(0)\n\t" \
    "s_sub_u32 s69, s70, s56\n\t" \
    "s_cmp_gt_i32 s69, 24\n\t" \
    "s_cbranch_scc1 hh_W11_%=\n\t" \
    "global_load_dword v30, v1, s[52:53] sc1\n\t" \
    "s_waitcnt lgkmcnt(0)\n\t" \
    "s_mov_b32 s70, 116\n\t" \
    "hh_E15_%=:\n\t" \
    "s_memtime s[56:57]\n\t" \
    "s_memrealtime s[2:3]\n\t" \
    "s_nop 0\n\t" \
    "s_memrealtime s[4:5]\n\t" \
    "s_nop 0\n\t" \
    "s_memrealtime s[6:7]\n\t" \
    "s_nop 0\n\t" \
    "s_memrealtime s[8:9]\n\t" \
    "s_nop 0\n\t" \
    "s_memrealtime s[10:11]\n\t" \
    "s_nop 0\n\t" \
    "s_memrealtime s[12:13]\n\t" \
    "s_nop 0\n\t" \
    "s_memrealtime s[14:15]\n\t" \
    "s_nop 0\n\t" \
    "s_memrealtime s[16:17]\n\t" \
    "s_nop 0\n\t" \
    "s_memrealtime s[18:19]\n\t" \
    "s_nop 0\n\t" \
    "s_memrealtime s[20:21]\n\t" \
    "s_nop 0\n\t" \
    "s_memrealtime s[22:23]\n\t" \
    "s_nop 0\n\t" \
    "s_memrealtime s[24:25]\n\t" \
    "s_nop 0\n\t" \
    "s_memrealtime s[26:27]\n\t" \
    "s_nop 0\n\t" \
    "s_memrealtime s[28:29]\n\t" \
    "s_memtime s[54:55]\n\t" \
    "s_add_u32 s70, s70, 8\n\t" \
    "s_waitcnt lgkmcnt(0)\n\t" \
    "s_sub_u32 s69, s54, s56\n\t" \
    "s_cmp_gt_u32 s69, s70\n\t" \
    "s_cbranch_scc1 hh_E15_%=\n\t" \
    "s_add_u32 s2, s2, s4\n\t" \
    "s_add_u32 s6, s6, s8\n\t" \
    "s_add_u32 s10, s10, s12\n\t" \
    "s_add_u32 s14, s14, s16\n\t" \
    "s_add_u32 s18, s18, s20\n\t" \
    "s_add_u32 s22, s22, s24\n\t" \
    "s_add_u32 s2, s2, s6\n\t" \
    "s_add_u32 s10, s10, s14\n\t" \
    "s_add_u32 s18, s18, s22\n\t" \
    "s_add_u32 s2, s2, s26\n\t" \
    "s_add_u32 s10, s10, s18\n\t" \
    "s_add_u32 s2, s2, s10\n\t" \
    "s_mul_i32 s70, s28, 13\n\t" \
    "s_sub_u32 s70, s70, s2\n\t" \
    "s_min_u32 s70, s70, 52\n\t" \
    "s_mul_i32 s69, s69, s70\n\t" \
    "s_mul_hi_u32 s69, s69, 76695844\n\t" \
    "s_mov_b32 s75, s73\n\t" \
    "s_mov_b32 s76, s74\n\t" \
    "s_add_u32 s73, s56, s69\n\t" \
    "s_lshl_b32 s74, s28, 4\n\t" \
    "s_sub_u32 s69, s73, s75\n\t" \
    "s_sub_u32 s70, s74, s76\n\t" \
    "s_sub_u32 s71, s70, 1600\n\t" \
    "s_cmp_lt_u32 s71, 400000\n\t" \
    "s_cbranch_scc0 hh_K16_%=\n\t" \
    "v_cvt_f32_u32 v34, s69\n\t" \
    "v_cvt_f32_u32 v35, s70\n\t" \
    "v_rcp_f32 v36, v34\n\t" \
    "v_rcp_f32 v37, v35\n\t" \
    "v_mul_f32 v36, v36, v35\n\t" \
    "v_mul_f32 v37, v37, v34\n\t" \
    "v_mul_f32 v36, 0x4f800000, v36\n\t" \
    "v_mul_f32 v37, 0x4f000000, v37\n\t" \
    "v_cvt_u32_f32 v36, v36\n\t" \
    "v_cvt_u32_f32 v37, v37\n\t" \
    "v_readfirstlane_b32 s69, v36\n\t" \
    "v_readfirstlane_b32 s70, v37\n\t" \
    "s_nop 1\n\t" \
    "s_sub_u32 s69, s69, s78\n\t" \
    "s_ashr_i32 s69, s69, 1\n\t" \
    "s_add_u32 s78, s78, s69\n\t" \
    "s_sub_u32 s70, s70, s77\n\t" \
    "s_ashr_i32 s70, s70, 1\n\t" \
    "s_add_u32 s77, s77, s70\n\t" \
    "hh_K16_%=:\n\t" \
    "s_waitcnt vmcnt(0)\n\t" \
    "v_cmp_eq_u32 vcc, -1, v30\n\t" \
    "s_cbranch_vccz hh_Hok13_%=\n\t" \
    "hh_H12_%=:\n\t" \
    "global_load_dword v30, v1, s[52:53] sc1\n\t" \
    "s_waitcnt vmcnt(0)\n\t" \
    "v_cmp_eq_u32 vcc, -1, v30\n\t" \
    "s_cbranch_vccz hh_Hc14_%=\n\t" \
    "s_cmp_eq_u32 s68, 0\n\t" \
    "s_cbranch_scc1 hh_F17_%=\n\t" \
    "s_sub_u32 s68, s68, 1\n\t" \
    "s_sleep 2\n\t" \
    "s_branch hh_H12_%=\n\t" \
    "hh_F17_%=:\n\t" \
    "v_mov_b32 v31, 5767424\n\t" \
    "v_mov_b32 v32, 14\n\t" \
    "global_store_dword v31, v32, s[36:37] sc1\n\t" \
    "s_waitcnt vmcnt(0)\n\t" \
    "s_branch hh_Hc14_%=\n\t" \
    "hh_Hc14_%=:\n\t" \
    "s_waitcnt lgkmcnt(0)\n\t" \
    "s_mov_b32 s70, 116\n\t" \
    "hh_E18_%=:\n\t" \
    "s_memtime s[56:57]\n\t" \
    "s_memrealtime s[2:3]\n\t" \
    "s_nop 0\n\t" \
    "s_memrealtime s[4:5]\n\t" \
    "s_nop 0\n\t" \
    "s_memrealtime s[6:7]\n\t" \
    "s_nop 0\n\t" \
    "s_memrealtime s[8:9]\n\t" \
    "s_nop 0\n\t" \
    "s_memrealtime s[10:11]\n\t" \
    "s_nop 0\n\t" \
    "s_memrealtime s[12:13]\n\t" \
    "s_nop 0\n\t" \
    "s_memrealtime s[14:15]\n\t" \
    "s_nop 0\n\t" \
    "s_memrealtime s[16:17]\n\t" \
    "s_nop 0\n\t" \
    "s_memrealtime s[18:19]\n\t" \
    "s_nop 0\n\t" \
    "s_memrealtime s[20:21]\n\t" \
    "s_nop 0\n\t" \
    "s_memrealtime s[22:23]\n\t" \
    "s_nop 0\n\t" \
    "s_memrealtime s[24:25]\n\t" \
    "s_nop 0\n\t" \
    "s_memrealtime s[26:27]\n\t" \
    "s_nop 0\n\t" \
    "s_memrealtime s[28:29]\n\t" \
    "s_memtime s[54:55]\n\t" \
    "s_add_u32 s70, s70, 8\n\t" \
    "s_waitcnt lgkmcnt(0)\n\t" \
    "s_sub_u32 s69, s54, s56\n\t" \
    "s_cmp_gt_u32 s69, s70\n\t" \
    "s_cbranch_scc1 hh_E18_%=\n\t" \
    "s_add_u32 s2, s2, s4\n\t" \
    "s_add_u32 s6, s6, s8\n\t" \
    "s_add_u32 s10, s10, s12\n\t" \
    "s_add_u32 s14, s14, s16\n\t" \
    "s_add_u32 s18, s18, s20\n\t" \
    "s_add_u32 s22, s22, s24\n\t" \
    "s_add_u32 s2, s2, s6\n\t" \
    "s_add_u32 s10, s10, s14\n\t" \
    "s_add_u32 s18, s18, s22\n\t" \
    "s_add_u32 s2, s2, s26\n\t" \
    "s_add_u32 s10, s10, s18\n\t" \
    "s_add_u32 s2, s2, s10\n\t" \
    "s_mul_i32 s70, s28, 13\n\t" \
    "s_sub_u32 s70, s70, s2\n\t" \
    "s_min_u32 s70, s70, 52\n\t" \
    "s_mul_i32 s69, s69, s70\n\t" \
    "s_mul_hi_u32 s69, s69, 76695844\n\t" \
    "s_mov_b32 s75, s73\n\t" \
    "s_mov_b32 s76, s74\n\t" \
    "s_add_u32 s73, s56, s69\n\t" \
    "s_lshl_b32 s74, s28, 4\n\t" \
    "s_sub_u32 s69, s73, s75\n\t" \
    "s_sub_u32 s70, s74, s76\n\t" \
    "s_sub_u32 s71, s70, 1600\n\t" \
    "s_cmp_lt_u32 s71, 400000\n\t" \
    "s_cbranch_scc0 hh_K19_%=\n\t" \
    "v_cvt_f32_u32 v34, s69\n\t" \
    "v_cvt_f32_u32 v35, s70\n\t" \
    "v_rcp_f32 v36, v34\n\t" \
    "v_rcp_f32 v37, v35\n\t" \
    "v_mul_f32 v36, v36, v35\n\t" \
    "v_mul_f32 v37, v37, v34\n\t" \
    "v_mul_f32 v36, 0x4f800000, v36\n\t" \
    "v_mul_f32 v37, 0x4f000000, v37\n\t" \
    "v_cvt_u32_f32 v36, v36\n\t" \
    "v_cvt_u32_f32 v37, v37\n\t" \
    "v_readfirstlane_b32 s69, v36\n\t" \
    "v_readfirstlane_b32 s70, v37\n\t" \
    "s_nop 1\n\t" \
    "s_sub_u32 s69, s69, s78\n\t" \
    "s_ashr_i32 s69, s69, 1\n\t" \
    "s_add_u32 s78, s78, s69\n\t" \
    "s_sub_u32 s70, s70, s77\n\t" \
    "s_ashr_i32 s70, s70, 1\n\t" \
    "s_add_u32 s77, s77, s70\n\t" \
    "hh_K19_%=:\n\t" \
    "hh_Hok13_%=:\n\t" \
    "v_max_u32_dpp v30, v30, v30 row_ror:1 row_mask:0xf bank_mask:0xf\n\t" \
    "s_nop 1\n\t" \
    "v_max_u32_dpp v30, v30, v30 row_ror:2 row_mask:0xf bank_mask:0xf\n\t" \
    "s_nop 1\n\t" \
    "v_max_u32_dpp v30, v30, v30 row_ror:4 row_mask:0xf bank_mask:0xf\n\t" \
    "s_nop 1\n\t" \
    "v_max_u32_dpp v30, v30, v30 row_ror:8 row_mask:0xf bank_mask:0xf\n\t" \
    "s_nop 0\n\t" \
    "v_readlane_b32 s69, v30, 0\n\t" \
    "v_readlane_b32 s71, v30, 16\n\t" \
    "s_nop 1\n\t" \
    "s_max_u32 s81, s69, s71\n\t" \
    "s_mul_hi_u32 s69, s85, s78\n\t" \
    "s_add_u32 s79, s81, s69\n\t" \
    "s_add_u32 s79, s79, s87\n\t" \
    "s_cmp_ge_u32 s65, 52\n\t" \
    "s_cselect_b32 s69, 32, 0\n\t" \
    "s_sub_u32 s79, s79, s69\n\t" \
    "s_cmp_lt_u32 s65, 9\n\t" \
    "s_cselect_b32 s69, 64, 0\n\t" \
    "s_add_u32 s79, s79, s69\n\t" \
    "s_add_u32 s80, s79, s88\n\t" \
    "s_add_u32 s65, s65, 1\n\t" \
    "s_cmp_gt_u32 s65, 62\n\t" \
    "s_cbranch_scc1 hh_end10_%=\n\t" \
    "s_cmp_le_u32 s65, 6\n\t" \
    "s_cbranch_scc1 hh_loop8_%=\n\t" \
    "v_lshrrev_b32 v20, 15, v11\n\t" \
    "v_add3_u32 v20, v20, s65, 1\n\t" \
    "v_min_u32 v20, 63, v20\n\t" \
    "v_mul_u32_u24 v20, v20, v11\n\t" \
    "v_xor_b32 v20, v20, v4\n\t" \
    "v_lshrrev_b32 v21, 15, v12\n\t" \
    "v_add3_u32 v21, v21, s65, 1\n\t" \
    "v_min_u32 v21, 63, v21\n\t" \
    "v_mul_u32_u24 v21, v21, v12\n\t" \
    "v_xor_b32 v21, v21, v5\n\t" \
    "v_lshrrev_b32 v22, 15, v13\n\t" \
    "v_add3_u32 v22, v22, s65, 1\n\t" \
    "v_min_u32 v22, 63, v22\n\t" \
    "v_mul_u32_u24 v22, v22, v13\n\t" \
    "v_xor_b32 v22, v22, v6\n\t" \
    "v_lshrrev_b32 v23, 15, v14\n\t" \
    "v_add3_u32 v23, v23, s65, 1\n\t" \
    "v_min_u32 v23, 63, v23\n\t" \
    "v_mul_u32_u24 v23, v23, v14\n\t" \
    "v_xor_b32 v23, v23, v7\n\t" \
    "v_lshrrev_b32 v24, 15, v15\n\t" \
    "v_add3_u32 v24, v24, s65, 1\n\t" \
    "v_min_u32 v24, 63, v24\n\t" \
    "v_mul_u32_u24 v24, v24, v15\n\t" \
    "v_xor_b32 v24, v24, v8\n\t" \
    "v_lshrrev_b32 v25, 15, v16\n\t" \
    "v_add3_u32 v25, v25, s65, 1\n\t" \
    "v_min_u32 v25, 63, v25\n\t" \
    "v_mul_u32_u24 v25, v25, v16\n\t" \
    "v_xor_b32 v25, v25, v9\n\t" \
    "v_lshrrev_b32 v26, 15, v17\n\t" \
    "v_add3_u32 v26, v26, s65, 1\n\t" \
    "v_min_u32 v26, 63, v26\n\t" \
    "v_mul_u32_u24 v26, v26, v17\n\t" \
    "v_xor_b32 v26, v26, v10\n\t" \
    "s_add_u32 s71, s79, s96\n\t" \
    "s_sub_u32 s70, s71, s74\n\t" \
    "s_max_i32 s70, s70, 0\n\t" \
    "s_min_i32 s70, s70, 16000\n\t" \
    "s_lshl_b32 s70, s70, 1\n\t" \
    "s_mul_hi_u32 s70, s70, s77\n\t" \
    "s_add_u32 s70, s70, s73\n\t" \
    "s_getpc_b64 s[30:31]\n\t" \
    "hh_Wp22_%=:\n\t" \
    "s_add_u32 s30, s30, hh_We23_%=-hh_Wp22_%=\n\t" \
    "s_addc_u32 s31, s31, 0\n\t" \
    "s_sub_u32 s70, s70, 96\n\t" \
    "hh_W21_%=:\n\t" \
    "s_memtime s[56:57]\n\t" \
    "s_waitcnt lgkmcnt(0)\n\t" \
    "s_sub_u32 s69, s70, s56\n\t" \
    "s_cmp_gt_i32 s69, 80\n\t" \
    "s_cbranch_scc1 hh_W21_%=\n\t" \
    "s_max_i32 s69, s69, 0\n\t" \
    "s_and_b32 s69, s69, -4\n\t" \
    "s_sub_u32 s30, s30, s69\n\t" \
    "s_subb_u32 s31, s31, 0\n\t" \
    "s_setpc_b64 s[30:31]\n\t" \
    "s_nop 0\n\t" \
    "s_nop 0\n\t" \
    "s_nop 0\n\t" \
    "s_nop 0\n\t" \
    "s_nop 0\n\t" \
    "s_nop 0\n\t" \
    "s_nop 0\n\t" \
    "s_nop 0\n\t" \
    "s_nop 0\n\t" \
    "s_nop 0\n\t" \
    "s_nop 0\n\t" \
    "s_nop 0\n\t" \
    "s_nop 0\n\t" \
    "s_nop 0\n\t" \
    "s_nop 0\n\t" \
    "s_nop 0\n\t" \
    "s_nop 0\n\t" \
    "s_nop 0\n\t" \
    "s_nop 0\n\t" \
    "s_nop 0\n\t" \
    "hh_We23_%=:\n\t" \
    "global_load_lds_dword v26, s[36:37] sc1\n\t" \
    "s_sleep 2\n\t" \
    "s_nop 5\n\t" \
    "global_load_lds_dword v25, s[36:37] sc1\n\t" \
    "s_sleep 2\n\t" \
    "s_nop 5\n\t" \
    "global_load_lds_dword v24, s[36:37] sc1\n\t" \
    "s_sleep 2\n\t" \
    "s_nop 5\n\t" \
    "global_load_lds_dword v23, s[36:37] sc1\n\t" \
    "s_sleep 2\n\t" \
    "s_nop 5\n\t" \
    "global_load_lds_dword v22, s[36:37] sc1\n\t" \
    "s_sleep 2\n\t" \
    "s_nop 5\n\t" \
    "global_load_lds_dword v21, s[36:37] sc1\n\t" \
    "s_sleep 2\n\t" \
    "s_nop 5\n\t" \
    "global_load_lds_dword v20, s[36:37] sc1\n\t" \
    "s_branch hh_loop8_%=\n\t" \
    "hh_end10_%=:\n\t" \
    "s_waitcnt vmcnt(0)\n\t" \
    "s_endpgm\n\t" \
    "hh_last_%=:\n\t" \
    ""
#define ASM_GX \
    "s_load_dwordx2 s[42:43], %3, 48\n\t" \
    "s_load_dwordx2 s[44:45], %3, 40\n\t" \
    "s_load_dwordx2 s[36:37], %3, 0x58\n\t" \
    "s_load_dwordx2 s[50:51], %3, 0x48\n\t" \
    "s_mov_b32 s66, %4\n\t" \
    "s_mov_b32 s67, %5\n\t" \
    "s_mov_b32 s68, 262144\n\t" \
    "s_mov_b32 s72, 0\n\t" \
    "s_mov_b32 s83, -1\n\t" \
    "s_mov_b32 s65, 0\n\t" \
    "s_lshl_b32 s82, s66, 10\n\t" \
    "s_waitcnt lgkmcnt(0)\n\t" \
    "s_mul_i32 s89, s66, 9216\n\t" \
    "s_mov_b32 s90, 0\n\t" \
    "s_add_u32 s46, s42, 2097152\n\t" \
    "s_addc_u32 s47, s43, 0\n\t" \
    "s_add_u32 m0, s82, 65536\n\t" \
    "s_nop 0\n\t" \
    "global_load_lds_dwordx4 v200, s[46:47]\n\t" \
    "s_add_u32 s46, s42, 2228224\n\t" \
    "s_addc_u32 s47, s43, 0\n\t" \
    "s_add_u32 m0, s82, 69632\n\t" \
    "s_nop 0\n\t" \
    "global_load_lds_dwordx4 v200, s[46:47]\n\t" \
    "s_add_u32 s46, s42, 2359296\n\t" \
    "s_addc_u32 s47, s43, 0\n\t" \
    "s_add_u32 m0, s82, 73728\n\t" \
    "s_nop 0\n\t" \
    "global_load_lds_dwordx4 v200, s[46:47]\n\t" \
    "s_add_u32 s46, s42, 2490368\n\t" \
    "s_addc_u32 s47, s43, 0\n\t" \
    "s_add_u32 m0, s82, 77824\n\t" \
    "s_nop 0\n\t" \
    "global_load_lds_dwordx4 v200, s[46:47]\n\t" \
    "s_add_u32 s46, s42, 2621440\n\t" \
    "s_addc_u32 s47, s43, 0\n\t" \
    "s_add_u32 m0, s82, 81920\n\t" \
    "s_nop 0\n\t" \
    "global_load_lds_dwordx4 v200, s[46:47]\n\t" \
    "s_add_u32 s46, s42, 2752512\n\t" \
    "s_addc_u32 s47, s43, 0\n\t" \
    "s_add_u32 m0, s82, 86016\n\t" \
    "s_nop 0\n\t" \
    "global_load_lds_dwordx4 v200, s[46:47]\n\t" \
    "s_add_u32 s46, s42, 2883584\n\t" \
    "s_addc_u32 s47, s43, 0\n\t" \
    "s_add_u32 m0, s82, 90112\n\t" \
    "s_nop 0\n\t" \
    "global_load_lds_dwordx4 v200, s[46:47]\n\t" \
    "s_add_u32 s46, s42, 3014656\n\t" \
    "s_addc_u32 s47, s43, 0\n\t" \
    "s_add_u32 m0, s82, 94208\n\t" \
    "s_nop 0\n\t" \
    "global_load_lds_dwordx4 v200, s[46:47]\n\t" \
    "s_add_u32 s46, s42, 3145728\n\t" \
    "s_addc_u32 s47, s43, 0\n\t" \
    "s_add_u32 m0, s82, 98304\n\t" \
    "s_nop 0\n\t" \
    "global_load_lds_dwordx4 v200, s[46:47]\n\t" \
    "s_add_u32 s46, s42, 3276800\n\t" \
    "s_addc_u32 s47, s43, 0\n\t" \
    "s_add_u32 m0, s82, 102400\n\t" \
    "s_nop 0\n\t" \
    "global_load_lds_dwordx4 v200, s[46:47]\n\t" \
    "s_add_u32 s46, s42, 3407872\n\t" \
    "s_addc_u32 s47, s43, 0\n\t" \
    "s_add_u32 m0, s82, 106496\n\t" \
    "s_nop 0\n\t" \
    "global_load_lds_dwordx4 v200, s[46:47]\n\t" \
    "s_add_u32 s46, s42, 3538944\n\t" \
    "s_addc_u32 s47, s43, 0\n\t" \
    "s_add_u32 m0, s82, 110592\n\t" \
    "s_nop 0\n\t" \
    "global_load_lds_dwordx4 v200, s[46:47]\n\t" \
    "s_add_u32 s46, s42, 3670016\n\t" \
    "s_addc_u32 s47, s43, 0\n\t" \
    "s_add_u32 m0, s82, 114688\n\t" \
    "s_nop 0\n\t" \
    "global_load_lds_dwordx4 v200, s[46:47]\n\t" \
    "s_add_u32 s46, s42, 3801088\n\t" \
    "s_addc_u32 s47, s43, 0\n\t" \
    "s_add_u32 m0, s82, 118784\n\t" \
    "s_nop 0\n\t" \
    "global_load_lds_dwordx4 v200, s[46:47]\n\t" \
    "s_add_u32 s46, s42, 3932160\n\t" \
    "s_addc_u32 s47, s43, 0\n\t" \
    "s_add_u32 m0, s82, 122880\n\t" \
    "s_nop 0\n\t" \
    "global_load_lds_dwordx4 v200, s[46:47]\n\t" \
    "s_add_u32 s46, s42, 4063232\n\t" \
    "s_addc_u32 s47, s43, 0\n\t" \
    "s_add_u32 m0, s82, 126976\n\t" \
    "s_nop 0\n\t" \
    "global_load_lds_dwordx4 v200, s[46:47]\n\t" \
    "s_add_u32 s46, s42, 4194304\n\t" \
    "s_addc_u32 s47, s43, 0\n\t" \
    "s_add_u32 m0, s82, 131072\n\t" \
    "s_nop 0\n\t" \
    "global_load_lds_dwordx4 v200, s[46:47]\n\t" \
    "s_add_u32 s46, s42, 4325376\n\t" \
    "s_addc_u32 s47, s43, 0\n\t" \
    "s_add_u32 m0, s82, 135168\n\t" \
    "s_nop 0\n\t" \
    "global_load_lds_dwordx4 v200, s[46:47]\n\t" \
    "s_add_u32 s46, s42, 4456448\n\t" \
    "s_addc_u32 s47, s43, 0\n\t" \
    "s_add_u32 m0, s82, 139264\n\t" \
    "s_nop 0\n\t" \
    "global_load_lds_dwordx4 v200, s[46:47]\n\t" \
    "s_add_u32 s46, s42, 4587520\n\t" \
    "s_addc_u32 s47, s43, 0\n\t" \
    "s_add_u32 m0, s82, 143360\n\t" \
    "s_nop 0\n\t" \
    "global_load_lds_dwordx4 v200, s[46:47]\n\t" \
    "global_load_dwordx4 v[120:123], v203, s[50:51]\n\t" \
    "global_load_dwordx4 v[124:127], v204, s[50:51]\n\t" \
    "global_load_dwordx4 v[128:131], v205, s[50:51]\n\t" \
    "global_load_dwordx4 v[132:135], v206, s[50:51]\n\t" \
    "global_load_dwordx4 v[136:139], v207, s[50:51]\n\t" \
    "global_load_dwordx4 v[140:143], v208, s[50:51]\n\t" \
    "global_load_dwordx4 v[144:147], v209, s[50:51]\n\t" \
    "global_load_dwordx4 v[148:151], v210, s[50:51]\n\t" \
    "global_load_dwordx4 v[152:155], v211, s[50:51]\n\t" \
    "s_add_u32 s52, s50, 36864\n\t" \
    "s_addc_u32 s53, s51, 0\n\t" \
    "global_load_dwordx4 v[72:75], v203, s[52:53]\n\t" \
    "global_load_dwordx4 v[76:79], v204, s[52:53]\n\t" \
    "global_load_dwordx4 v[80:83], v205, s[52:53]\n\t" \
    "global_load_dwordx4 v[84:87], v206, s[52:53]\n\t" \
    "global_load_dwordx4 v[88:91], v207, s[52:53]\n\t" \
    "global_load_dwordx4 v[92:95], v208, s[52:53]\n\t" \
    "global_load_dwordx4 v[96:99], v209, s[52:53]\n\t" \
    "global_load_dwordx4 v[100:103], v210, s[52:53]\n\t" \
    "global_load_dwordx4 v[104:107], v211, s[52:53]\n\t" \
    "s_waitcnt vmcnt(50) lgkmcnt(0)\n\t" \
    "s_barrier\n\t" \
    "ds_read_b64_tr_b16 a[144:145], v201 offset:0\n\t" \
    "ds_read_b64_tr_b16 a[146:147], v201 offset:1024\n\t" \
    "ds_read_b64_tr_b16 a[148:149], v201 offset:8192\n\t" \
    "ds_read_b64_tr_b16 a[150:151], v201 offset:9216\n\t" \
    "ds_read_b64_tr_b16 a[216:217], v202 offset:0\n\t" \
    "ds_read_b64_tr_b16 a[218:219], v202 offset:1024\n\t" \
    "ds_read_b64_tr_b16 a[220:221], v202 offset:8192\n\t" \
    "ds_read_b64_tr_b16 a[222:223], v202 offset:9216\n\t" \
    "s_waitcnt vmcnt(46) lgkmcnt(0)\n\t" \
    "s_barrier\n\t" \
    "ds_read_b64_tr_b16 a[152:153], v201 offset:16384\n\t" \
    "ds_read_b64_tr_b16 a[154:155], v201 offset:17408\n\t" \
    "ds_read_b64_tr_b16 a[156:157], v201 offset:24576\n\t" \
    "ds_read_b64_tr_b16 a[158:159], v201 offset:25600\n\t" \
    "ds_read_b64_tr_b16 a[224:225], v202 offset:16384\n\t" \
    "ds_read_b64_tr_b16 a[226:227], v202 offset:17408\n\t" \
    "ds_read_b64_tr_b16 a[228:229], v202 offset:24576\n\t" \
    "ds_read_b64_tr_b16 a[230:231], v202 offset:25600\n\t" \
    "s_waitcnt vmcnt(42) lgkmcnt(0)\n\t" \
    "s_barrier\n\t" \
    "ds_read_b64_tr_b16 a[160:161], v201 offset:32768\n\t" \
    "ds_read_b64_tr_b16 a[162:163], v201 offset:33792\n\t" \
    "ds_read_b64_tr_b16 a[164:165], v201 offset:40960\n\t" \
    "ds_read_b64_tr_b16 a[166:167], v201 offset:41984\n\t" \
    "ds_read_b64_tr_b16 a[232:233], v202 offset:32768\n\t" \
    "ds_read_b64_tr_b16 a[234:235], v202 offset:33792\n\t" \
    "ds_read_b64_tr_b16 a[236:237], v202 offset:40960\n\t" \
    "ds_read_b64_tr_b16 a[238:239], v202 offset:41984\n\t" \
    "s_waitcnt vmcnt(38) lgkmcnt(0)\n\t" \
    "s_barrier\n\t" \
    "ds_read_b64_tr_b16 a[168:169], v201 offset:49152\n\t" \
    "ds_read_b64_tr_b16 a[170:171], v201 offset:50176\n\t" \
    "ds_read_b64_tr_b16 a[172:173], v201 offset:57344\n\t" \
    "ds_read_b64_tr_b16 a[174:175], v201 offset:58368\n\t" \
    "ds_read_b64_tr_b16 a[240:241], v202 offset:49152\n\t" \
    "ds_read_b64_tr_b16 a[242:243], v202 offset:50176\n\t" \
    "ds_read_b64_tr_b16 a[244:245], v202 offset:57344\n\t" \
    "ds_read_b64_tr_b16 a[246:247], v202 offset:58368\n\t" \
    "v_mbcnt_lo_u32_b32 v164, -1, 0\n\t" \
    "v_mbcnt_hi_u32_b32 v164, -1, v164\n\t" \
    "v_lshlrev_b32 v164, 6, v164\n\t" \
    "v_add_u32 v164, 6225920, v164\n\t" \
    "v_mov_b32 v165, 6234112\n\t" \
    "global_load_dword v165, v165, s[36:37] sc1\n\t" \
    "global_load_dwordx2 v[166:167], v164, s[36:37] sc1\n\t" \
    "global_load_dword v168, v164, s[36:37] offset:2048 sc1\n\t" \
    "s_waitcnt vmcnt(37) lgkmcnt(0)\n\t" \
    "s_barrier\n\t" \
    "v_add_u32 v201, 0x10000, v201\n\t" \
    "v_add_u32 v202, 0x10000, v202\n\t" \
    "ds_read_b64_tr_b16 a[176:177], v201 offset:0\n\t" \
    "ds_read_b64_tr_b16 a[178:179], v201 offset:1024\n\t" \
    "ds_read_b64_tr_b16 a[180:181], v201 offset:8192\n\t" \
    "ds_read_b64_tr_b16 a[182:183], v201 offset:9216\n\t" \
    "ds_read_b64_tr_b16 a[248:249], v202 offset:0\n\t" \
    "ds_read_b64_tr_b16 a[250:251], v202 offset:1024\n\t" \
    "ds_read_b64_tr_b16 a[252:253], v202 offset:8192\n\t" \
    "ds_read_b64_tr_b16 a[254:255], v202 offset:9216\n\t" \
    "s_waitcnt vmcnt(33) lgkmcnt(0)\n\t" \
    "s_barrier\n\t" \
    "ds_read_b64_tr_b16 a[184:185], v201 offset:16384\n\t" \
    "ds_read_b64_tr_b16 a[186:187], v201 offset:17408\n\t" \
    "ds_read_b64_tr_b16 a[188:189], v201 offset:24576\n\t" \
    "ds_read_b64_tr_b16 a[190:191], v201 offset:25600\n\t" \
    "ds_read_b64_tr_b16 v[224:225], v202 offset:16384\n\t" \
    "ds_read_b64_tr_b16 v[226:227], v202 offset:17408\n\t" \
    "ds_read_b64_tr_b16 v[228:229], v202 offset:24576\n\t" \
    "ds_read_b64_tr_b16 v[230:231], v202 offset:25600\n\t" \
    "s_waitcnt vmcnt(29) lgkmcnt(0)\n\t" \
    "s_barrier\n\t" \
    "ds_read_b64_tr_b16 a[192:193], v201 offset:32768\n\t" \
    "ds_read_b64_tr_b16 a[194:195], v201 offset:33792\n\t" \
    "ds_read_b64_tr_b16 a[196:197], v201 offset:40960\n\t" \
    "ds_read_b64_tr_b16 a[198:199], v201 offset:41984\n\t" \
    "ds_read_b64_tr_b16 v[232:233], v202 offset:32768\n\t" \
    "ds_read_b64_tr_b16 v[234:235], v202 offset:33792\n\t" \
    "ds_read_b64_tr_b16 v[236:237], v202 offset:40960\n\t" \
    "ds_read_b64_tr_b16 v[238:239], v202 offset:41984\n\t" \
    "s_waitcnt vmcnt(25) lgkmcnt(0)\n\t" \
    "s_barrier\n\t" \
    "ds_read_b64_tr_b16 a[200:201], v201 offset:49152\n\t" \
    "ds_read_b64_tr_b16 a[202:203], v201 offset:50176\n\t" \
    "ds_read_b64_tr_b16 a[204:205], v201 offset:57344\n\t" \
    "ds_read_b64_tr_b16 a[206:207], v201 offset:58368\n\t" \
    "ds_read_b64_tr_b16 v[240:241], v202 offset:49152\n\t" \
    "ds_read_b64_tr_b16 v[242:243], v202 offset:50176\n\t" \
    "ds_read_b64_tr_b16 v[244:245], v202 offset:57344\n\t" \
    "ds_read_b64_tr_b16 v[246:247], v202 offset:58368\n\t" \
    "s_waitcnt vmcnt(21) lgkmcnt(0)\n\t" \
    "s_barrier\n\t" \
    "v_add_u32 v201, 0x10000, v201\n\t" \
    "v_add_u32 v202, 0x10000, v202\n\t" \
    "ds_read_b64_tr_b16 a[208:209], v201 offset:0\n\t" \
    "ds_read_b64_tr_b16 a[210:211], v201 offset:1024\n\t" \
    "ds_read_b64_tr_b16 a[212:213], v201 offset:8192\n\t" \
    "ds_read_b64_tr_b16 a[214:215], v201 offset:9216\n\t" \
    "ds_read_b64_tr_b16 v[248:249], v202 offset:0\n\t" \
    "ds_read_b64_tr_b16 v[250:251], v202 offset:1024\n\t" \
    "ds_read_b64_tr_b16 v[252:253], v202 offset:8192\n\t" \
    "ds_read_b64_tr_b16 v[254:255], v202 offset:9216\n\t" \
    "v_add_u32 v201, 0xfffe0000, v201\n\t" \
    "v_add_u32 v202, 0xfffe0000, v202\n\t" \
    "s_waitcnt lgkmcnt(0)\n\t" \
    "s_barrier\n\t" \
    "s_waitcnt vmcnt(0)\n\t" \
    "ds_write_b128 v3, v[120:123] offset:0\n\t" \
    "ds_write_b128 v3, v[124:127] offset:1024\n\t" \
    "ds_write_b128 v3, v[128:131] offset:2048\n\t" \
    "ds_write_b128 v3, v[132:135] offset:3072\n\t" \
    "ds_write_b128 v3, v[136:139] offset:4096\n\t" \
    "ds_write_b128 v3, v[140:143] offset:5120\n\t" \
    "ds_write_b128 v3, v[144:147] offset:6144\n\t" \
    "ds_write_b128 v3, v[148:151] offset:7168\n\t" \
    "ds_write_b128 v3, v[152:155] offset:8192\n\t" \
    "ds_write_b128 v3, v[72:75] offset:36864\n\t" \
    "ds_write_b128 v3, v[76:79] offset:37888\n\t" \
    "ds_write_b128 v3, v[80:83] offset:38912\n\t" \
    "ds_write_b128 v3, v[84:87] offset:39936\n\t" \
    "ds_write_b128 v3, v[88:91] offset:40960\n\t" \
    "ds_write_b128 v3, v[92:95] offset:41984\n\t" \
    "ds_write_b128 v3, v[96:99] offset:43008\n\t" \
    "ds_write_b128 v3, v[100:103] offset:44032\n\t" \
    "ds_write_b128 v3, v[104:107] offset:45056\n\t" \
    "gx_C1_%=:\n\t" \
    "v_cmp_ne_u32 s[54:55], v166, v168\n\t" \
    "v_sub_u32 v166, v166, v165\n\t" \
    "v_cmp_ne_u32 vcc, 1, v166\n\t" \
    "s_nop 1\n\t" \
    "s_or_b64 vcc, vcc, s[54:55]\n\t" \
    "s_cbranch_scc0 gx_Cok2_%=\n\t" \
    "s_cmp_eq_u32 s68, 0\n\t" \
    "s_cbranch_scc1 gx_F4_%=\n\t" \
    "s_sub_u32 s68, s68, 1\n\t" \
    "s_sleep 6\n\t" \
    "s_branch gx_Cag3_%=\n\t" \
    "gx_F4_%=:\n\t" \
    "v_mov_b32 v168, 5767424\n\t" \
    "v_mov_b32 v169, 4\n\t" \
    "global_store_dword v168, v169, s[36:37] sc1\n\t" \
    "s_waitcnt vmcnt(0)\n\t" \
    "s_branch gx_Cok2_%=\n\t" \
    "gx_Cag3_%=:\n\t" \
    "global_load_dwordx2 v[166:167], v164, s[36:37] sc1\n\t" \
    "global_load_dword v168, v164, s[36:37] offset:2048 sc1\n\t" \
    "s_waitcnt vmcnt(0)\n\t" \
    "s_branch gx_C1_%=\n\t" \
    "gx_Cok2_%=:\n\t" \
    "s_cmp_lg_u32 s66, 0\n\t" \
    "s_cbranch_scc1 gx_Ca5_%=\n\t" \
    "v_mov_b32 v164, 5836800\n\t" \
    "s_mov_b64 exec, 1\n\t" \
    "global_atomic_add v164, v166, s[36:37] sc1\n\t" \
    "s_mov_b64 exec, -1\n\t" \
    "gx_Ca5_%=:\n\t" \
    "s_waitcnt lgkmcnt(0)\n\t" \
    "s_barrier\n\t" \
    "ds_read_b128 v[24:27], v1 offset:0\n\t" \
    "ds_read_b128 v[28:31], v1 offset:18432\n\t" \
    "ds_read_b128 v[32:35], v2 offset:0\n\t" \
    "ds_read_b128 v[36:39], v2 offset:18432\n\t" \
    "ds_read_b128 v[40:43], v1 offset:128\n\t" \
    "ds_read_b128 v[44:47], v1 offset:18560\n\t" \
    "gx_l16_%=:\n\t" \
    "s_add_u32 s84, s65, 2\n\t" \
    "s_min_u32 s69, s84, 63\n\t" \
    "s_mul_i32 s69, s69, 36864\n\t" \
    "s_add_u32 s52, s50, s69\n\t" \
    "s_addc_u32 s53, s51, 0\n\t" \
    "s_add_u32 s90, s90, 36864\n\t" \
    "s_cmp_eq_u32 s90, 147456\n\t" \
    "s_cselect_b32 s90, 0, s90\n\t" \
    "s_add_u32 s91, s90, 36864\n\t" \
    "s_cmp_eq_u32 s91, 147456\n\t" \
    "s_cselect_b32 s91, 0, s91\n\t" \
    "s_add_u32 s91, s91, s89\n\t" \
    "v_add_u32 v22, s90, v0\n\t" \
    "v_xor_b32 v23, 64, v22\n\t" \
    "s_mul_i32 s70, s67, 7\n\t" \
    "s_add_u32 s69, s65, 0\n\t" \
    "s_xor_b32 s69, s69, s70\n\t" \
    "s_and_b32 s69, s69, 63\n\t" \
    "s_lshl_b32 s69, s69, 13\n\t" \
    "s_mul_i32 s70, s67, 524288\n\t" \
    "s_add_u32 s69, s69, s70\n\t" \
    "s_add_u32 s69, s69, 23068672\n\t" \
    "s_add_u32 s40, s36, s69\n\t" \
    "s_addc_u32 s41, s37, 0\n\t" \
    "s_waitcnt lgkmcnt(5)\n\t" \
    "v_mfma_f32_16x16x32_bf16 v[72:75], a[144:147], v[24:27], 0\n\t" \
    "ds_read_b128 v[48:51], v2 offset:128\n\t" \
    "s_waitcnt lgkmcnt(5)\n\t" \
    "v_mfma_f32_16x16x32_bf16 v[76:79], a[144:147], v[28:31], 0\n\t" \
    "ds_read_b128 v[52:55], v2 offset:18560\n\t" \
    "v_mfma_f32_16x16x32_bf16 v[80:83], a[216:219], v[24:27], 0\n\t" \
    "s_add_u32 m0, s91, 0\n\t" \
    "v_mfma_f32_16x16x32_bf16 v[84:87], a[216:219], v[28:31], 0\n\t" \
    "global_load_lds_dwordx4 v203, s[52:53]\n\t" \
    "s_waitcnt lgkmcnt(5)\n\t" \
    "v_mfma_f32_16x16x32_bf16 v[72:75], a[148:151], v[32:35], v[72:75]\n\t" \
    "ds_read_b128 v[56:59], v1 offset:256\n\t" \
    "s_waitcnt lgkmcnt(5)\n\t" \
    "v_mfma_f32_16x16x32_bf16 v[76:79], a[148:151], v[36:39], v[76:79]\n\t" \
    "ds_read_b128 v[60:63], v1 offset:18688\n\t" \
    "v_mfma_f32_16x16x32_bf16 v[80:83], a[220:223], v[32:35], v[80:83]\n\t" \
    "s_add_u32 m0, s91, 1024\n\t" \
    "v_mfma_f32_16x16x32_bf16 v[84:87], a[220:223], v[36:39], v[84:87]\n\t" \
    "global_load_lds_dwordx4 v204, s[52:53]\n\t" \
    "s_waitcnt lgkmcnt(5)\n\t" \
    "v_mfma_f32_16x16x32_bf16 v[72:75], a[152:155], v[40:43], v[72:75]\n\t" \
    "ds_read_b128 v[64:67], v2 offset:256\n\t" \
    "s_waitcnt lgkmcnt(5)\n\t" \
    "v_mfma_f32_16x16x32_bf16 v[76:79], a[152:155], v[44:47], v[76:79]\n\t" \
    "ds_read_b128 v[68:71], v2 offset:18688\n\t" \
    "v_mfma_f32_16x16x32_bf16 v[80:83], a[224:227], v[40:43], v[80:83]\n\t" \
    "s_add_u32 m0, s91, 2048\n\t" \
    "v_mfma_f32_16x16x32_bf16 v[84:87], a[224:227], v[44:47], v[84:87]\n\t" \
    "global_load_lds_dwordx4 v205, s[52:53]\n\t" \
    "s_waitcnt lgkmcnt(5)\n\t" \
    "v_mfma_f32_16x16x32_bf16 v[72:75], a[156:159], v[48:51], v[72:75]\n\t" \
    "ds_read_b128 v[24:27], v1 offset:384\n\t" \
    "s_waitcnt lgkmcnt(5)\n\t" \
    "v_mfma_f32_16x16x32_bf16 v[76:79], a[156:159], v[52:55], v[76:79]\n\t" \
    "ds_read_b128 v[28:31], v1 offset:18816\n\t" \
    "v_mfma_f32_16x16x32_bf16 v[80:83], a[228:231], v[48:51], v[80:83]\n\t" \
    "s_add_u32 m0, s91, 3072\n\t" \
    "v_mfma_f32_16x16x32_bf16 v[84:87], a[228:231], v[52:55], v[84:87]\n\t" \
    "global_load_lds_dwordx4 v206, s[52:53]\n\t" \
    "s_waitcnt lgkmcnt(5)\n\t" \
    "v_mfma_f32_16x16x32_bf16 v[72:75], a[160:163], v[56:59], v[72:75]\n\t" \
    "ds_read_b128 v[32:35], v2 offset:384\n\t" \
    "s_waitcnt lgkmcnt(5)\n\t" \
    "v_mfma_f32_16x16x32_bf16 v[76:79], a[160:163], v[60:63], v[76:79]\n\t" \
    "ds_read_b128 v[36:39], v2 offset:18816\n\t" \
    "v_mfma_f32_16x16x32_bf16 v[80:83], a[232:235], v[56:59], v[80:83]\n\t" \
    "s_add_u32 m0, s91, 4096\n\t" \
    "v_mfma_f32_16x16x32_bf16 v[84:87], a[232:235], v[60:63], v[84:87]\n\t" \
    "global_load_lds_dwordx4 v207, s[52:53]\n\t" \
    "s_waitcnt lgkmcnt(5)\n\t" \
    "v_mfma_f32_16x16x32_bf16 v[72:75], a[164:167], v[64:67], v[72:75]\n\t" \
    "ds_read_b128 v[40:43], v1 offset:512\n\t" \
    "s_waitcnt lgkmcnt(5)\n\t" \
    "v_mfma_f32_16x16x32_bf16 v[76:79], a[164:167], v[68:71], v[76:79]\n\t" \
    "ds_read_b128 v[44:47], v1 offset:18944\n\t" \
    "v_mfma_f32_16x16x32_bf16 v[80:83], a[236:239], v[64:67], v[80:83]\n\t" \
    "s_add_u32 m0, s91, 5120\n\t" \
    "v_mfma_f32_16x16x32_bf16 v[84:87], a[236:239], v[68:71], v[84:87]\n\t" \
    "global_load_lds_dwordx4 v208, s[52:53]\n\t" \
    "s_waitcnt lgkmcnt(5)\n\t" \
    "v_mfma_f32_16x16x32_bf16 v[72:75], a[168:171], v[24:27], v[72:75]\n\t" \
    "ds_read_b128 v[48:51], v2 offset:512\n\t" \
    "s_waitcnt lgkmcnt(5)\n\t" \
    "v_mfma_f32_16x16x32_bf16 v[76:79], a[168:171], v[28:31], v[76:79]\n\t" \
    "ds_read_b128 v[52:55], v2 offset:18944\n\t" \
    "v_mfma_f32_16x16x32_bf16 v[80:83], a[240:243], v[24:27], v[80:83]\n\t" \
    "s_add_u32 m0, s91, 6144\n\t" \
    "v_mfma_f32_16x16x32_bf16 v[84:87], a[240:243], v[28:31], v[84:87]\n\t" \
    "global_load_lds_dwordx4 v209, s[52:53]\n\t" \
    "s_waitcnt lgkmcnt(5)\n\t" \
    "v_mfma_f32_16x16x32_bf16 v[72:75], a[172:175], v[32:35], v[72:75]\n\t" \
    "ds_read_b128 v[56:59], v1 offset:640\n\t" \
    "s_waitcnt lgkmcnt(5)\n\t" \
    "v_mfma_f32_16x16x32_bf16 v[76:79], a[172:175], v[36:39], v[76:79]\n\t" \
    "ds_read_b128 v[60:63], v1 offset:19072\n\t" \
    "v_mfma_f32_16x16x32_bf16 v[80:83], a[244:247], v[32:35], v[80:83]\n\t" \
    "s_add_u32 m0, s91, 7168\n\t" \
    "v_mfma_f32_16x16x32_bf16 v[84:87], a[244:247], v[36:39], v[84:87]\n\t" \
    "global_load_lds_dwordx4 v210, s[52:53]\n\t" \
    "s_waitcnt lgkmcnt(5)\n\t" \
    "v_mfma_f32_16x16x32_bf16 v[72:75], a[176:179], v[40:43], v[72:75]\n\t" \
    "ds_read_b128 v[64:67], v2 offset:640\n\t" \
    "s_waitcnt lgkmcnt(5)\n\t" \
    "v_mfma_f32_16x16x32_bf16 v[76:79], a[176:179], v[44:47], v[76:79]\n\t" \
    "ds_read_b128 v[68:71], v2 offset:19072\n\t" \
    "v_mfma_f32_16x16x32_bf16 v[80:83], a[248:251], v[40:43], v[80:83]\n\t" \
    "s_add_u32 m0, s91, 8192\n\t" \
    "v_mfma_f32_16x16x32_bf16 v[84:87], a[248:251], v[44:47], v[84:87]\n\t" \
    "global_load_lds_dwordx4 v211, s[52:53]\n\t" \
    "s_waitcnt lgkmcnt(5)\n\t" \
    "v_mfma_f32_16x16x32_bf16 v[72:75], a[180:183], v[48:51], v[72:75]\n\t" \
    "ds_read_b128 v[24:27], v1 offset:768\n\t" \
    "s_waitcnt lgkmcnt(5)\n\t" \
    "v_mfma_f32_16x16x32_bf16 v[76:79], a[180:183], v[52:55], v[76:79]\n\t" \
    "ds_read_b128 v[28:31], v1 offset:19200\n\t" \
    "v_mfma_f32_16x16x32_bf16 v[80:83], a[252:255], v[48:51], v[80:83]\n\t" \
    "v_mfma_f32_16x16x32_bf16 v[84:87], a[252:255], v[52:55], v[84:87]\n\t" \
    "s_waitcnt lgkmcnt(5)\n\t" \
    "v_mfma_f32_16x16x32_bf16 v[72:75], a[184:187], v[56:59], v[72:75]\n\t" \
    "ds_read_b128 v[32:35], v2 offset:768\n\t" \
    "s_waitcnt lgkmcnt(5)\n\t" \
    "v_mfma_f32_16x16x32_bf16 v[76:79], a[184:187], v[60:63], v[76:79]\n\t" \
    "ds_read_b128 v[36:39], v2 offset:19200\n\t" \
    "v_mfma_f32_16x16x32_bf16 v[80:83], v[224:227], v[56:59], v[80:83]\n\t" \
    "s_waitcnt vmcnt(9)\n\t" \
    "s_barrier\n\t" \
    "v_mfma_f32_16x16x32_bf16 v[84:87], v[224:227], v[60:63], v[84:87]\n\t" \
    "s_cmp_lg_u32 s66, 0\n\t" \
    "s_cbranch_scc1 gx_SF9_%=\n\t" \
    "s_lshl_b32 s69, s67, 7\n\t" \
    "s_add_u32 s69, s69, 5771264\n\t" \
    "s_add_u32 s70, s65, -1\n\t" \
    "v_mov_b32 v156, s69\n\t" \
    "v_mov_b32 v157, s70\n\t" \
    "s_mov_b64 exec, 1\n\t" \
    "global_store_dword v156, v157, s[36:37] sc1\n\t" \
    "s_mov_b64 exec, -1\n\t" \
    "gx_SF9_%=:\n\t" \
    "s_waitcnt lgkmcnt(5)\n\t" \
    "v_mfma_f32_16x16x32_bf16 v[72:75], a[188:191], v[64:67], v[72:75]\n\t" \
    "ds_read_b128 v[40:43], v1 offset:896\n\t" \
    "s_waitcnt lgkmcnt(5)\n\t" \
    "v_mfma_f32_16x16x32_bf16 v[76:79], a[188:191], v[68:71], v[76:79]\n\t" \
    "ds_read_b128 v[44:47], v1 offset:19328\n\t" \
    "v_mfma_f32_16x16x32_bf16 v[80:83], v[228:231], v[64:67], v[80:83]\n\t" \
    "v_mfma_f32_16x16x32_bf16 v[84:87], v[228:231], v[68:71], v[84:87]\n\t" \
    "s_waitcnt lgkmcnt(5)\n\t" \
    "v_mfma_f32_16x16x32_bf16 v[72:75], a[192:195], v[24:27], v[72:75]\n\t" \
    "ds_read_b128 v[48:51], v2 offset:896\n\t" \
    "s_waitcnt lgkmcnt(5)\n\t" \
    "v_mfma_f32_16x16x32_bf16 v[76:79], a[192:195], v[28:31], v[76:79]\n\t" \
    "ds_read_b128 v[52:55], v2 offset:19328\n\t" \
    "v_mfma_f32_16x16x32_bf16 v[80:83], v[232:235], v[24:27], v[80:83]\n\t" \
    "v_mfma_f32_16x16x32_bf16 v[84:87], v[232:235], v[28:31], v[84:87]\n\t" \
    "s_waitcnt lgkmcnt(5)\n\t" \
    "v_mfma_f32_16x16x32_bf16 v[72:75], a[196:199], v[32:35], v[72:75]\n\t" \
    "ds_read_b128 v[56:59], v1 offset:1024\n\t" \
    "s_waitcnt lgkmcnt(5)\n\t" \
    "v_mfma_f32_16x16x32_bf16 v[76:79], a[196:199], v[36:39], v[76:79]\n\t" \
    "ds_read_b128 v[60:63], v1 offset:19456\n\t" \
    "v_mfma_f32_16x16x32_bf16 v[80:83], v[236:239], v[32:35], v[80:83]\n\t" \
    "v_mfma_f32_16x16x32_bf16 v[84:87], v[236:239], v[36:39], v[84:87]\n\t" \
    "s_waitcnt lgkmcnt(5)\n\t" \
    "v_mfma_f32_16x16x32_bf16 v[72:75], a[200:203], v[40:43], v[72:75]\n\t" \
    "ds_read_b128 v[64:67], v2 offset:1024\n\t" \
    "s_waitcnt lgkmcnt(5)\n\t" \
    "v_mfma_f32_16x16x32_bf16 v[76:79], a[200:203], v[44:47], v[76:79]\n\t" \
    "ds_read_b128 v[68:71], v2 offset:19456\n\t" \
    "v_mfma_f32_16x16x32_bf16 v[80:83], v[240:243], v[40:43], v[80:83]\n\t" \
    "v_mfma_f32_16x16x32_bf16 v[84:87], v[240:243], v[44:47], v[84:87]\n\t" \
    "s_waitcnt lgkmcnt(5)\n\t" \
    "v_mfma_f32_16x16x32_bf16 v[72:75], a[204:207], v[48:51], v[72:75]\n\t" \
    "ds_read_b128 v[24:27], v22 offset:0\n\t" \
    "s_waitcnt lgkmcnt(5)\n\t" \
    "v_mfma_f32_16x16x32_bf16 v[76:79], a[204:207], v[52:55], v[76:79]\n\t" \
    "ds_read_b128 v[28:31], v22 offset:18432\n\t" \
    "v_mfma_f32_16x16x32_bf16 v[80:83], v[244:247], v[48:51], v[80:83]\n\t" \
    "v_mfma_f32_16x16x32_bf16 v[84:87], v[244:247], v[52:55], v[84:87]\n\t" \
    "s_waitcnt lgkmcnt(5)\n\t" \
    "v_mfma_f32_16x16x32_bf16 v[72:75], a[208:211], v[56:59], v[72:75]\n\t" \
    "ds_read_b128 v[32:35], v23 offset:0\n\t" \
    "s_waitcnt lgkmcnt(5)\n\t" \
    "v_mfma_f32_16x16x32_bf16 v[76:79], a[208:211], v[60:63], v[76:79]\n\t" \
    "ds_read_b128 v[36:39], v23 offset:18432\n\t" \
    "v_mfma_f32_16x16x32_bf16 v[80:83], v[248:251], v[56:59], v[80:83]\n\t" \
    "v_mfma_f32_16x16x32_bf16 v[84:87], v[248:251], v[60:63], v[84:87]\n\t" \
    "s_waitcnt lgkmcnt(5)\n\t" \
    "v_mfma_f32_16x16x32_bf16 v[72:75], a[212:215], v[64:67], v[72:75]\n\t" \
    "ds_read_b128 v[40:43], v22 offset:128\n\t" \
    "s_waitcnt lgkmcnt(5)\n\t" \
    "v_mfma_f32_16x16x32_bf16 v[76:79], a[212:215], v[68:71], v[76:79]\n\t" \
    "ds_read_b128 v[44:47], v22 offset:18560\n\t" \
    "v_mfma_f32_16x16x32_bf16 v[80:83], v[252:255], v[64:67], v[80:83]\n\t" \
    "v_mfma_f32_16x16x32_bf16 v[84:87], v[252:255], v[68:71], v[84:87]\n\t" \
    "v_mov_b32 v1, v22\n\t" \
    "v_mov_b32 v2, v23\n\t" \
    "v_cvt_pk_bf16_f32 v104, v72, v73\n\t" \
    "v_cvt_pk_bf16_f32 v105, v74, v75\n\t" \
    "s_nop 2\n\t" \
    "v_cvt_pk_bf16_f32 v106, v80, v81\n\t" \
    "v_cvt_pk_bf16_f32 v107, v82, v83\n\t" \
    "global_store_dwordx4 v8, v[104:107], s[40:41] offset:0 sc1\n\t" \
    "v_cvt_pk_bf16_f32 v108, v76, v77\n\t" \
    "v_cvt_pk_bf16_f32 v109, v78, v79\n\t" \
    "v_cvt_pk_bf16_f32 v110, v84, v85\n\t" \
    "v_cvt_pk_bf16_f32 v111, v86, v87\n\t" \
    "global_store_dwordx4 v8, v[108:111], s[40:41] offset:1024 sc1\n\t" \
    "s_cmp_ge_u32 s65, 1\n\t" \
    "s_cbranch_scc1 gx_nofast7_%=\n\t" \
    "s_waitcnt vmcnt(0)\n\t" \
    "s_barrier\n\t" \
    "s_cmp_lg_u32 s66, 0\n\t" \
    "s_cbranch_scc1 gx_SF10_%=\n\t" \
    "s_lshl_b32 s69, s67, 7\n\t" \
    "s_add_u32 s69, s69, 5771264\n\t" \
    "s_add_u32 s70, s65, 0\n\t" \
    "v_mov_b32 v156, s69\n\t" \
    "v_mov_b32 v157, s70\n\t" \
    "s_mov_b64 exec, 1\n\t" \
    "global_store_dword v156, v157, s[36:37] sc1\n\t" \
    "s_mov_b64 exec, -1\n\t" \
    "gx_SF10_%=:\n\t" \
    "gx_nofast7_%=:\n\t" \
    "s_sub_u32 s69, s65, 4\n\t" \
    "s_cmp_gt_u32 s69, 4\n\t" \
    "s_cbranch_scc1 gx_nt11_%=\n\t" \
    "s_lshl_b32 s69, s69, 7\n\t" \
    "s_lshl_b32 s70, s66, 5\n\t" \
    "s_add_u32 s69, s69, s70\n\t" \
    "v_lshrrev_b32 v156, 3, v6\n\t" \
    "v_add_u32 v156, s69, v156\n\t" \
    "v_min_u32 v156, 575, v156\n\t" \
    "v_and_b32 v157, 4, v6\n\t" \
    "v_lshlrev_b32 v157, 5, v157\n\t" \
    "v_lshl_add_u32 v156, v156, 13, v157\n\t" \
    "s_lshl_b32 s69, s67, 8\n\t" \
    "v_add_u32 v156, s69, v156\n\t" \
    "s_mov_b32 m0, 155648\n\t" \
    "s_nop 0\n\t" \
    "global_load_lds_dword v156, s[44:45]\n\t" \
    "gx_nt11_%=:\n\t" \
    "s_add_u32 s65, s65, 1\n\t" \
    "s_cmp_lt_u32 s65, 9\n\t" \
    "s_cbranch_scc1 gx_l16_%=\n\t" \
    "s_waitcnt vmcnt(0) lgkmcnt(0)\n\t" \
    "s_barrier\n\t" \
    "s_cmp_lg_u32 s66, 0\n\t" \
    "s_cbranch_scc1 gx_SF12_%=\n\t" \
    "s_lshl_b32 s69, s67, 7\n\t" \
    "s_add_u32 s69, s69, 5771264\n\t" \
    "s_add_u32 s70, s65, -1\n\t" \
    "v_mov_b32 v156, s69\n\t" \
    "v_mov_b32 v157, s70\n\t" \
    "s_mov_b64 exec, 1\n\t" \
    "global_store_dword v156, v157, s[36:37] sc1\n\t" \
    "s_mov_b64 exec, -1\n\t" \
    "gx_SF12_%=:\n\t" \
    "s_add_u32 s46, s44, 0\n\t" \
    "s_addc_u32 s47, s45, 0\n\t" \
    "s_add_u32 m0, s82, 0\n\t" \
    "s_nop 0\n\t" \
    "global_load_lds_dwordx4 v200, s[46:47]\n\t" \
    "s_add_u32 s46, s44, 131072\n\t" \
    "s_addc_u32 s47, s45, 0\n\t" \
    "s_add_u32 m0, s82, 4096\n\t" \
    "s_nop 0\n\t" \
    "global_load_lds_dwordx4 v200, s[46:47]\n\t" \
    "s_add_u32 s46, s44, 262144\n\t" \
    "s_addc_u32 s47, s45, 0\n\t" \
    "s_add_u32 m0, s82, 8192\n\t" \
    "s_nop 0\n\t" \
    "global_load_lds_dwordx4 v200, s[46:47]\n\t" \
    "s_add_u32 s46, s44, 393216\n\t" \
    "s_addc_u32 s47, s45, 0\n\t" \
    "s_add_u32 m0, s82, 12288\n\t" \
    "s_nop 0\n\t" \
    "global_load_lds_dwordx4 v200, s[46:47]\n\t" \
    "s_add_u32 s46, s44, 524288\n\t" \
    "s_addc_u32 s47, s45, 0\n\t" \
    "s_add_u32 m0, s82, 16384\n\t" \
    "s_nop 0\n\t" \
    "global_load_lds_dwordx4 v200, s[46:47]\n\t" \
    "s_add_u32 s46, s44, 655360\n\t" \
    "s_addc_u32 s47, s45, 0\n\t" \
    "s_add_u32 m0, s82, 20480\n\t" \
    "s_nop 0\n\t" \
    "global_load_lds_dwordx4 v200, s[46:47]\n\t" \
    "s_add_u32 s46, s44, 786432\n\t" \
    "s_addc_u32 s47, s45, 0\n\t" \
    "s_add_u32 m0, s82, 24576\n\t" \
    "s_nop 0\n\t" \
    "global_load_lds_dwordx4 v200, s[46:47]\n\t" \
    "s_add_u32 s46, s44, 917504\n\t" \
    "s_addc_u32 s47, s45, 0\n\t" \
    "s_add_u32 m0, s82, 28672\n\t" \
    "s_nop 0\n\t" \
    "global_load_lds_dwordx4 v200, s[46:47]\n\t" \
    "s_add_u32 s46, s44, 1048576\n\t" \
    "s_addc_u32 s47, s45, 0\n\t" \
    "s_add_u32 m0, s82, 32768\n\t" \
    "s_nop 0\n\t" \
    "global_load_lds_dwordx4 v200, s[46:47]\n\t" \
    "s_add_u32 s46, s44, 1179648\n\t" \
    "s_addc_u32 s47, s45, 0\n\t" \
    "s_add_u32 m0, s82, 36864\n\t" \
    "s_nop 0\n\t" \
    "global_load_lds_dwordx4 v200, s[46:47]\n\t" \
    "s_add_u32 s46, s44, 1310720\n\t" \
    "s_addc_u32 s47, s45, 0\n\t" \
    "s_add_u32 m0, s82, 40960\n\t" \
    "s_nop 0\n\t" \
    "global_load_lds_dwordx4 v200, s[46:47]\n\t" \
    "s_add_u32 s46, s44, 1441792\n\t" \
    "s_addc_u32 s47, s45, 0\n\t" \
    "s_add_u32 m0, s82, 45056\n\t" \
    "s_nop 0\n\t" \
    "global_load_lds_dwordx4 v200, s[46:47]\n\t" \
    "s_add_u32 s46, s44, 1572864\n\t" \
    "s_addc_u32 s47, s45, 0\n\t" \
    "s_add_u32 m0, s82, 49152\n\t" \
    "s_nop 0\n\t" \
    "global_load_lds_dwordx4 v200, s[46:47]\n\t" \
    "s_add_u32 s46, s44, 1703936\n\t" \
    "s_addc_u32 s47, s45, 0\n\t" \
    "s_add_u32 m0, s82, 53248\n\t" \
    "s_nop 0\n\t" \
    "global_load_lds_dwordx4 v200, s[46:47]\n\t" \
    "s_add_u32 s46, s44, 1835008\n\t" \
    "s_addc_u32 s47, s45, 0\n\t" \
    "s_add_u32 m0, s82, 57344\n\t" \
    "s_nop 0\n\t" \
    "global_load_lds_dwordx4 v200, s[46:47]\n\t" \
    "s_add_u32 s46, s44, 1966080\n\t" \
    "s_addc_u32 s47, s45, 0\n\t" \
    "s_add_u32 m0, s82, 61440\n\t" \
    "s_nop 0\n\t" \
    "global_load_lds_dwordx4 v200, s[46:47]\n\t" \
    "s_add_u32 s46, s44, 2097152\n\t" \
    "s_addc_u32 s47, s45, 0\n\t" \
    "s_add_u32 m0, s82, 65536\n\t" \
    "s_nop 0\n\t" \
    "global_load_lds_dwordx4 v200, s[46:47]\n\t" \
    "s_add_u32 s46, s44, 2228224\n\t" \
    "s_addc_u32 s47, s45, 0\n\t" \
    "s_add_u32 m0, s82, 69632\n\t" \
    "s_nop 0\n\t" \
    "global_load_lds_dwordx4 v200, s[46:47]\n\t" \
    "s_add_u32 s46, s44, 2359296\n\t" \
    "s_addc_u32 s47, s45, 0\n\t" \
    "s_add_u32 m0, s82, 73728\n\t" \
    "s_nop 0\n\t" \
    "global_load_lds_dwordx4 v200, s[46:47]\n\t" \
    "s_add_u32 s46, s44, 2490368\n\t" \
    "s_addc_u32 s47, s45, 0\n\t" \
    "s_add_u32 m0, s82, 77824\n\t" \
    "s_nop 0\n\t" \
    "global_load_lds_dwordx4 v200, s[46:47]\n\t" \
    "s_add_u32 s46, s44, 2621440\n\t" \
    "s_addc_u32 s47, s45, 0\n\t" \
    "s_add_u32 m0, s82, 81920\n\t" \
    "s_nop 0\n\t" \
    "global_load_lds_dwordx4 v200, s[46:47]\n\t" \
    "s_add_u32 s46, s44, 2752512\n\t" \
    "s_addc_u32 s47, s45, 0\n\t" \
    "s_add_u32 m0, s82, 86016\n\t" \
    "s_nop 0\n\t" \
    "global_load_lds_dwordx4 v200, s[46:47]\n\t" \
    "s_add_u32 s46, s44, 2883584\n\t" \
    "s_addc_u32 s47, s45, 0\n\t" \
    "s_add_u32 m0, s82, 90112\n\t" \
    "s_nop 0\n\t" \
    "global_load_lds_dwordx4 v200, s[46:47]\n\t" \
    "s_add_u32 s46, s44, 3014656\n\t" \
    "s_addc_u32 s47, s45, 0\n\t" \
    "s_add_u32 m0, s82, 94208\n\t" \
    "s_nop 0\n\t" \
    "global_load_lds_dwordx4 v200, s[46:47]\n\t" \
    "s_add_u32 s46, s44, 3145728\n\t" \
    "s_addc_u32 s47, s45, 0\n\t" \
    "s_add_u32 m0, s82, 98304\n\t" \
    "s_nop 0\n\t" \
    "global_load_lds_dwordx4 v200, s[46:47]\n\t" \
    "s_add_u32 s46, s44, 3276800\n\t" \
    "s_addc_u32 s47, s45, 0\n\t" \
    "s_add_u32 m0, s82, 102400\n\t" \
    "s_nop 0\n\t" \
    "global_load_lds_dwordx4 v200, s[46:47]\n\t" \
    "s_add_u32 s46, s44, 3407872\n\t" \
    "s_addc_u32 s47, s45, 0\n\t" \
    "s_add_u32 m0, s82, 106496\n\t" \
    "s_nop 0\n\t" \
    "global_load_lds_dwordx4 v200, s[46:47]\n\t" \
    "s_add_u32 s46, s44, 3538944\n\t" \
    "s_addc_u32 s47, s45, 0\n\t" \
    "s_add_u32 m0, s82, 110592\n\t" \
    "s_nop 0\n\t" \
    "global_load_lds_dwordx4 v200, s[46:47]\n\t" \
    "s_add_u32 s46, s44, 3670016\n\t" \
    "s_addc_u32 s47, s45, 0\n\t" \
    "s_add_u32 m0, s82, 114688\n\t" \
    "s_nop 0\n\t" \
    "global_load_lds_dwordx4 v200, s[46:47]\n\t" \
    "s_add_u32 s46, s44, 3801088\n\t" \
    "s_addc_u32 s47, s45, 0\n\t" \
    "s_add_u32 m0, s82, 118784\n\t" \
    "s_nop 0\n\t" \
    "global_load_lds_dwordx4 v200, s[46:47]\n\t" \
    "s_add_u32 s46, s44, 3932160\n\t" \
    "s_addc_u32 s47, s45, 0\n\t" \
    "s_add_u32 m0, s82, 122880\n\t" \
    "s_nop 0\n\t" \
    "global_load_lds_dwordx4 v200, s[46:47]\n\t" \
    "s_add_u32 s46, s44, 4063232\n\t" \
    "s_addc_u32 s47, s45, 0\n\t" \
    "s_add_u32 m0, s82, 126976\n\t" \
    "s_nop 0\n\t" \
    "global_load_lds_dwordx4 v200, s[46:47]\n\t" \
    "s_add_u32 s46, s44, 4194304\n\t" \
    "s_addc_u32 s47, s45, 0\n\t" \
    "s_add_u32 m0, s82, 131072\n\t" \
    "s_nop 0\n\t" \
    "global_load_lds_dwordx4 v200, s[46:47]\n\t" \
    "s_add_u32 s46, s44, 4325376\n\t" \
    "s_addc_u32 s47, s45, 0\n\t" \
    "s_add_u32 m0, s82, 135168\n\t" \
    "s_nop 0\n\t" \
    "global_load_lds_dwordx4 v200, s[46:47]\n\t" \
    "s_add_u32 s46, s44, 4456448\n\t" \
    "s_addc_u32 s47, s45, 0\n\t" \
    "s_add_u32 m0, s82, 139264\n\t" \
    "s_nop 0\n\t" \
    "global_load_lds_dwordx4 v200, s[46:47]\n\t" \
    "s_add_u32 s46, s44, 4587520\n\t" \
    "s_addc_u32 s47, s45, 0\n\t" \
    "s_add_u32 m0, s82, 143360\n\t" \
    "s_nop 0\n\t" \
    "global_load_lds_dwordx4 v200, s[46:47]\n\t" \
    "global_load_dwordx4 v[120:123], v203, s[50:51]\n\t" \
    "global_load_dwordx4 v[124:127], v204, s[50:51]\n\t" \
    "global_load_dwordx4 v[128:131], v205, s[50:51]\n\t" \
    "global_load_dwordx4 v[132:135], v206, s[50:51]\n\t" \
    "global_load_dwordx4 v[136:139], v207, s[50:51]\n\t" \
    "global_load_dwordx4 v[140:143], v208, s[50:51]\n\t" \
    "global_load_dwordx4 v[144:147], v209, s[50:51]\n\t" \
    "global_load_dwordx4 v[148:151], v210, s[50:51]\n\t" \
    "global_load_dwordx4 v[152:155], v211, s[50:51]\n\t" \
    "s_add_u32 s52, s50, 331776\n\t" \
    "s_addc_u32 s53, s51, 0\n\t" \
    "global_load_dwordx4 v[72:75], v203, s[52:53]\n\t" \
    "global_load_dwordx4 v[76:79], v204, s[52:53]\n\t" \
    "global_load_dwordx4 v[80:83], v205, s[52:53]\n\t" \
    "global_load_dwordx4 v[84:87], v206, s[52:53]\n\t" \
    "global_load_dwordx4 v[88:91], v207, s[52:53]\n\t" \
    "global_load_dwordx4 v[92:95], v208, s[52:53]\n\t" \
    "global_load_dwordx4 v[96:99], v209, s[52:53]\n\t" \
    "global_load_dwordx4 v[100:103], v210, s[52:53]\n\t" \
    "global_load_dwordx4 v[104:107], v211, s[52:53]\n\t" \
    "s_waitcnt vmcnt(50) lgkmcnt(0)\n\t" \
    "s_barrier\n\t" \
    "ds_read_b64_tr_b16 a[0:1], v201 offset:0\n\t" \
    "ds_read_b64_tr_b16 a[2:3], v201 offset:1024\n\t" \
    "ds_read_b64_tr_b16 a[4:5], v201 offset:8192\n\t" \
    "ds_read_b64_tr_b16 a[6:7], v201 offset:9216\n\t" \
    "ds_read_b64_tr_b16 a[72:73], v202 offset:0\n\t" \
    "ds_read_b64_tr_b16 a[74:75], v202 offset:1024\n\t" \
    "ds_read_b64_tr_b16 a[76:77], v202 offset:8192\n\t" \
    "ds_read_b64_tr_b16 a[78:79], v202 offset:9216\n\t" \
    "s_waitcnt vmcnt(46) lgkmcnt(0)\n\t" \
    "s_barrier\n\t" \
    "ds_read_b64_tr_b16 a[8:9], v201 offset:16384\n\t" \
    "ds_read_b64_tr_b16 a[10:11], v201 offset:17408\n\t" \
    "ds_read_b64_tr_b16 a[12:13], v201 offset:24576\n\t" \
    "ds_read_b64_tr_b16 a[14:15], v201 offset:25600\n\t" \
    "ds_read_b64_tr_b16 a[80:81], v202 offset:16384\n\t" \
    "ds_read_b64_tr_b16 a[82:83], v202 offset:17408\n\t" \
    "ds_read_b64_tr_b16 a[84:85], v202 offset:24576\n\t" \
    "ds_read_b64_tr_b16 a[86:87], v202 offset:25600\n\t" \
    "s_waitcnt vmcnt(42) lgkmcnt(0)\n\t" \
    "s_barrier\n\t" \
    "ds_read_b64_tr_b16 a[16:17], v201 offset:32768\n\t" \
    "ds_read_b64_tr_b16 a[18:19], v201 offset:33792\n\t" \
    "ds_read_b64_tr_b16 a[20:21], v201 offset:40960\n\t" \
    "ds_read_b64_tr_b16 a[22:23], v201 offset:41984\n\t" \
    "ds_read_b64_tr_b16 a[88:89], v202 offset:32768\n\t" \
    "ds_read_b64_tr_b16 a[90:91], v202 offset:33792\n\t" \
    "ds_read_b64_tr_b16 a[92:93], v202 offset:40960\n\t" \
    "ds_read_b64_tr_b16 a[94:95], v202 offset:41984\n\t" \
    "s_waitcnt vmcnt(38) lgkmcnt(0)\n\t" \
    "s_barrier\n\t" \
    "ds_read_b64_tr_b16 a[24:25], v201 offset:49152\n\t" \
    "ds_read_b64_tr_b16 a[26:27], v201 offset:50176\n\t" \
    "ds_read_b64_tr_b16 a[28:29], v201 offset:57344\n\t" \
    "ds_read_b64_tr_b16 a[30:31], v201 offset:58368\n\t" \
    "ds_read_b64_tr_b16 a[96:97], v202 offset:49152\n\t" \
    "ds_read_b64_tr_b16 a[98:99], v202 offset:50176\n\t" \
    "ds_read_b64_tr_b16 a[100:101], v202 offset:57344\n\t" \
    "ds_read_b64_tr_b16 a[102:103], v202 offset:58368\n\t" \
    "s_waitcnt vmcnt(34) lgkmcnt(0)\n\t" \
    "s_barrier\n\t" \
    "v_add_u32 v201, 0x10000, v201\n\t" \
    "v_add_u32 v202, 0x10000, v202\n\t" \
    "ds_read_b64_tr_b16 a[32:33], v201 offset:0\n\t" \
    "ds_read_b64_tr_b16 a[34:35], v201 offset:1024\n\t" \
    "ds_read_b64_tr_b16 a[36:37], v201 offset:8192\n\t" \
    "ds_read_b64_tr_b16 a[38:39], v201 offset:9216\n\t" \
    "ds_read_b64_tr_b16 a[104:105], v202 offset:0\n\t" \
    "ds_read_b64_tr_b16 a[106:107], v202 offset:1024\n\t" \
    "ds_read_b64_tr_b16 a[108:109], v202 offset:8192\n\t" \
    "ds_read_b64_tr_b16 a[110:111], v202 offset:9216\n\t" \
    "s_waitcnt vmcnt(30) lgkmcnt(0)\n\t" \
    "s_barrier\n\t" \
    "ds_read_b64_tr_b16 a[40:41], v201 offset:16384\n\t" \
    "ds_read_b64_tr_b16 a[42:43], v201 offset:17408\n\t" \
    "ds_read_b64_tr_b16 a[44:45], v201 offset:24576\n\t" \
    "ds_read_b64_tr_b16 a[46:47], v201 offset:25600\n\t" \
    "ds_read_b64_tr_b16 a[112:113], v202 offset:16384\n\t" \
    "ds_read_b64_tr_b16 a[114:115], v202 offset:17408\n\t" \
    "ds_read_b64_tr_b16 a[116:117], v202 offset:24576\n\t" \
    "ds_read_b64_tr_b16 a[118:119], v202 offset:25600\n\t" \
    "s_waitcnt vmcnt(26) lgkmcnt(0)\n\t" \
    "s_barrier\n\t" \
    "ds_read_b64_tr_b16 a[48:49], v201 offset:32768\n\t" \
    "ds_read_b64_tr_b16 a[50:51], v201 offset:33792\n\t" \
    "ds_read_b64_tr_b16 a[52:53], v201 offset:40960\n\t" \
    "ds_read_b64_tr_b16 a[54:55], v201 offset:41984\n\t" \
    "ds_read_b64_tr_b16 a[120:121], v202 offset:32768\n\t" \
    "ds_read_b64_tr_b16 a[122:123], v202 offset:33792\n\t" \
    "ds_read_b64_tr_b16 a[124:125], v202 offset:40960\n\t" \
    "ds_read_b64_tr_b16 a[126:127], v202 offset:41984\n\t" \
    "s_waitcnt vmcnt(22) lgkmcnt(0)\n\t" \
    "s_barrier\n\t" \
    "ds_read_b64_tr_b16 a[56:57], v201 offset:49152\n\t" \
    "ds_read_b64_tr_b16 a[58:59], v201 offset:50176\n\t" \
    "ds_read_b64_tr_b16 a[60:61], v201 offset:57344\n\t" \
    "ds_read_b64_tr_b16 a[62:63], v201 offset:58368\n\t" \
    "ds_read_b64_tr_b16 a[128:129], v202 offset:49152\n\t" \
    "ds_read_b64_tr_b16 a[130:131], v202 offset:50176\n\t" \
    "ds_read_b64_tr_b16 a[132:133], v202 offset:57344\n\t" \
    "ds_read_b64_tr_b16 a[134:135], v202 offset:58368\n\t" \
    "s_waitcnt vmcnt(18) lgkmcnt(0)\n\t" \
    "s_barrier\n\t" \
    "v_add_u32 v201, 0x10000, v201\n\t" \
    "v_add_u32 v202, 0x10000, v202\n\t" \
    "ds_read_b64_tr_b16 a[64:65], v201 offset:0\n\t" \
    "ds_read_b64_tr_b16 a[66:67], v201 offset:1024\n\t" \
    "ds_read_b64_tr_b16 a[68:69], v201 offset:8192\n\t" \
    "ds_read_b64_tr_b16 a[70:71], v201 offset:9216\n\t" \
    "ds_read_b64_tr_b16 a[136:137], v202 offset:0\n\t" \
    "ds_read_b64_tr_b16 a[138:139], v202 offset:1024\n\t" \
    "ds_read_b64_tr_b16 a[140:141], v202 offset:8192\n\t" \
    "ds_read_b64_tr_b16 a[142:143], v202 offset:9216\n\t" \
    "v_add_u32 v201, 0xfffe0000, v201\n\t" \
    "v_add_u32 v202, 0xfffe0000, v202\n\t" \
    "s_waitcnt lgkmcnt(0)\n\t" \
    "s_barrier\n\t" \
    "s_waitcnt vmcnt(0)\n\t" \
    "ds_write_b128 v3, v[120:123] offset:0\n\t" \
    "ds_write_b128 v3, v[124:127] offset:1024\n\t" \
    "ds_write_b128 v3, v[128:131] offset:2048\n\t" \
    "ds_write_b128 v3, v[132:135] offset:3072\n\t" \
    "ds_write_b128 v3, v[136:139] offset:4096\n\t" \
    "ds_write_b128 v3, v[140:143] offset:5120\n\t" \
    "ds_write_b128 v3, v[144:147] offset:6144\n\t" \
    "ds_write_b128 v3, v[148:151] offset:7168\n\t" \
    "ds_write_b128 v3, v[152:155] offset:8192\n\t" \
    "ds_write_b128 v3, v[72:75] offset:36864\n\t" \
    "ds_write_b128 v3, v[76:79] offset:37888\n\t" \
    "ds_write_b128 v3, v[80:83] offset:38912\n\t" \
    "ds_write_b128 v3, v[84:87] offset:39936\n\t" \
    "ds_write_b128 v3, v[88:91] offset:40960\n\t" \
    "ds_write_b128 v3, v[92:95] offset:41984\n\t" \
    "ds_write_b128 v3, v[96:99] offset:43008\n\t" \
    "ds_write_b128 v3, v[100:103] offset:44032\n\t" \
    "ds_write_b128 v3, v[104:107] offset:45056\n\t" \
    "s_mov_b32 s65, 0\n\t" \
    "v_mov_b32 v1, v0\n\t" \
    "v_xor_b32 v2, 64, v0\n\t" \
    "s_mov_b32 s90, 0\n\t" \
    "s_add_u32 s91, s89, 0x12000\n\t" \
    "s_add_u32 s85, s91, s89\n\t" \
    "s_add_u32 s52, s50, 36864\n\t" \
    "s_addc_u32 s53, s51, 0\n\t" \
    "s_mul_i32 s69, s67, 8192\n\t" \
    "s_add_u32 s69, s69, 175112192\n\t" \
    "s_add_u32 s42, s36, s69\n\t" \
    "s_addc_u32 s43, s37, 0\n\t" \
    "s_mov_b64 s[40:41], s[42:43]\n\t" \
    "s_waitcnt lgkmcnt(0)\n\t" \
    "s_barrier\n\t" \
    "ds_read_b128 v[24:27], v1 offset:0\n\t" \
    "ds_read_b128 v[28:31], v1 offset:18432\n\t" \
    "ds_read_b128 v[32:35], v2 offset:0\n\t" \
    "ds_read_b128 v[36:39], v2 offset:18432\n\t" \
    "ds_read_b128 v[40:43], v1 offset:128\n\t" \
    "ds_read_b128 v[44:47], v1 offset:18560\n\t" \
    ".p2align 6\n\t" \
    "s_nop 0\n\t" \
    "s_nop 0\n\t" \
    "gx_l28_%=:\n\t" \
    "s_waitcnt lgkmcnt(5)\n\t" \
    "v_mfma_f32_16x16x32_bf16 v[88:91], a[0:3], v[24:27], 0\n\t" \
    "ds_read_b128 v[48:51], v2 offset:128\n\t" \
    "s_waitcnt lgkmcnt(5)\n\t" \
    "v_mfma_f32_16x16x32_bf16 v[92:95], a[0:3], v[28:31], 0\n\t" \
    "ds_read_b128 v[52:55], v2 offset:18560\n\t" \
    "v_mfma_f32_16x16x32_bf16 v[96:99], a[72:75], v[24:27], 0\n\t" \
    "s_add_u32 m0, s91, 0\n\t" \
    "v_mfma_f32_16x16x32_bf16 v[100:103], a[72:75], v[28:31], 0\n\t" \
    "global_load_lds_dwordx4 v203, s[52:53]\n\t" \
    "s_waitcnt lgkmcnt(5)\n\t" \
    "v_mfma_f32_16x16x32_bf16 v[88:91], a[4:7], v[32:35], v[88:91]\n\t" \
    "ds_read_b128 v[56:59], v1 offset:256\n\t" \
    "s_waitcnt lgkmcnt(5)\n\t" \
    "v_mfma_f32_16x16x32_bf16 v[92:95], a[4:7], v[36:39], v[92:95]\n\t" \
    "ds_read_b128 v[60:63], v1 offset:18688\n\t" \
    "v_mfma_f32_16x16x32_bf16 v[96:99], a[76:79], v[32:35], v[96:99]\n\t" \
    "s_add_u32 m0, s91, 1024\n\t" \
    "v_mfma_f32_16x16x32_bf16 v[100:103], a[76:79], v[36:39], v[100:103]\n\t" \
    "global_load_lds_dwordx4 v204, s[52:53]\n\t" \
    "s_waitcnt lgkmcnt(5)\n\t" \
    "v_mfma_f32_16x16x32_bf16 v[88:91], a[8:11], v[40:43], v[88:91]\n\t" \
    "ds_read_b128 v[64:67], v2 offset:256\n\t" \
    "s_waitcnt lgkmcnt(5)\n\t" \
    "v_mfma_f32_16x16x32_bf16 v[92:95], a[8:11], v[44:47], v[92:95]\n\t" \
    "ds_read_b128 v[68:71], v2 offset:18688\n\t" \
    "v_cvt_pk_bf16_f32 v104, v72, v73\n\t" \
    "v_mfma_f32_16x16x32_bf16 v[96:99], a[80:83], v[40:43], v[96:99]\n\t" \
    "s_add_u32 m0, s91, 2048\n\t" \
    "v_cvt_pk_bf16_f32 v105, v74, v75\n\t" \
    "v_mfma_f32_16x16x32_bf16 v[100:103], a[80:83], v[44:47], v[100:103]\n\t" \
    "global_load_lds_dwordx4 v205, s[52:53]\n\t" \
    "s_waitcnt lgkmcnt(5)\n\t" \
    "v_mfma_f32_16x16x32_bf16 v[88:91], a[12:15], v[48:51], v[88:91]\n\t" \
    "ds_read_b128 v[24:27], v1 offset:384\n\t" \
    "s_waitcnt lgkmcnt(5)\n\t" \
    "v_mfma_f32_16x16x32_bf16 v[92:95], a[12:15], v[52:55], v[92:95]\n\t" \
    "ds_read_b128 v[28:31], v1 offset:18816\n\t" \
    "v_cvt_pk_bf16_f32 v106, v80, v81\n\t" \
    "v_mfma_f32_16x16x32_bf16 v[96:99], a[84:87], v[48:51], v[96:99]\n\t" \
    "s_add_u32 m0, s91, 3072\n\t" \
    "v_cvt_pk_bf16_f32 v107, v82, v83\n\t" \
    "v_mfma_f32_16x16x32_bf16 v[100:103], a[84:87], v[52:55], v[100:103]\n\t" \
    "global_load_lds_dwordx4 v206, s[52:53]\n\t" \
    "s_waitcnt lgkmcnt(5)\n\t" \
    "v_mfma_f32_16x16x32_bf16 v[88:91], a[16:19], v[56:59], v[88:91]\n\t" \
    "ds_read_b128 v[32:35], v2 offset:384\n\t" \
    "s_waitcnt lgkmcnt(5)\n\t" \
    "v_mfma_f32_16x16x32_bf16 v[92:95], a[16:19], v[60:63], v[92:95]\n\t" \
    "ds_read_b128 v[36:39], v2 offset:18816\n\t" \
    "global_store_dwordx4 v8, v[104:107], s[40:41] offset:0 sc1\n\t" \
    "v_mfma_f32_16x16x32_bf16 v[96:99], a[88:91], v[56:59], v[96:99]\n\t" \
    "s_add_u32 m0, s91, 4096\n\t" \
    "v_cvt_pk_bf16_f32 v108, v76, v77\n\t" \
    "v_mfma_f32_16x16x32_bf16 v[100:103], a[88:91], v[60:63], v[100:103]\n\t" \
    "global_load_lds_dwordx4 v207, s[52:53]\n\t" \
    "s_waitcnt lgkmcnt(5)\n\t" \
    "v_mfma_f32_16x16x32_bf16 v[88:91], a[20:23], v[64:67], v[88:91]\n\t" \
    "ds_read_b128 v[40:43], v1 offset:512\n\t" \
    "s_waitcnt lgkmcnt(5)\n\t" \
    "v_mfma_f32_16x16x32_bf16 v[92:95], a[20:23], v[68:71], v[92:95]\n\t" \
    "ds_read_b128 v[44:47], v1 offset:18944\n\t" \
    "v_cvt_pk_bf16_f32 v109, v78, v79\n\t" \
    "v_mfma_f32_16x16x32_bf16 v[96:99], a[92:95], v[64:67], v[96:99]\n\t" \
    "s_add_u32 m0, s91, 5120\n\t" \
    "v_cvt_pk_bf16_f32 v110, v84, v85\n\t" \
    "v_mfma_f32_16x16x32_bf16 v[100:103], a[92:95], v[68:71], v[100:103]\n\t" \
    "global_load_lds_dwordx4 v208, s[52:53]\n\t" \
    "s_waitcnt lgkmcnt(5)\n\t" \
    "v_mfma_f32_16x16x32_bf16 v[88:91], a[24:27], v[24:27], v[88:91]\n\t" \
    "ds_read_b128 v[48:51], v2 offset:512\n\t" \
    "s_waitcnt lgkmcnt(5)\n\t" \
    "v_mfma_f32_16x16x32_bf16 v[92:95], a[24:27], v[28:31], v[92:95]\n\t" \
    "ds_read_b128 v[52:55], v2 offset:18944\n\t" \
    "v_cvt_pk_bf16_f32 v111, v86, v87\n\t" \
    "v_mfma_f32_16x16x32_bf16 v[96:99], a[96:99], v[24:27], v[96:99]\n\t" \
    "s_add_u32 m0, s91, 6144\n\t" \
    "global_store_dwordx4 v8, v[108:111], s[40:41] offset:1024 sc1\n\t" \
    "v_mfma_f32_16x16x32_bf16 v[100:103], a[96:99], v[28:31], v[100:103]\n\t" \
    "global_load_lds_dwordx4 v209, s[52:53]\n\t" \
    "s_waitcnt lgkmcnt(5)\n\t" \
    "v_mfma_f32_16x16x32_bf16 v[88:91], a[28:31], v[32:35], v[88:91]\n\t" \
    "ds_read_b128 v[56:59], v1 offset:640\n\t" \
    "s_waitcnt lgkmcnt(5)\n\t" \
    "v_mfma_f32_16x16x32_bf16 v[92:95], a[28:31], v[36:39], v[92:95]\n\t" \
    "ds_read_b128 v[60:63], v1 offset:19072\n\t" \
    "v_mfma_f32_16x16x32_bf16 v[96:99], a[100:103], v[32:35], v[96:99]\n\t" \
    "s_add_u32 m0, s91, 7168\n\t" \
    "v_mfma_f32_16x16x32_bf16 v[100:103], a[100:103], v[36:39], v[100:103]\n\t" \
    "global_load_lds_dwordx4 v210, s[52:53]\n\t" \
    "s_waitcnt lgkmcnt(5)\n\t" \
    "v_mfma_f32_16x16x32_bf16 v[88:91], a[32:35], v[40:43], v[88:91]\n\t" \
    "ds_read_b128 v[64:67], v2 offset:640\n\t" \
    "s_waitcnt lgkmcnt(5)\n\t" \
    "v_mfma_f32_16x16x32_bf16 v[92:95], a[32:35], v[44:47], v[92:95]\n\t" \
    "ds_read_b128 v[68:71], v2 offset:19072\n\t" \
    "v_mfma_f32_16x16x32_bf16 v[96:99], a[104:107], v[40:43], v[96:99]\n\t" \
    "s_add_u32 m0, s91, 8192\n\t" \
    "v_mfma_f32_16x16x32_bf16 v[100:103], a[104:107], v[44:47], v[100:103]\n\t" \
    "global_load_lds_dwordx4 v211, s[52:53]\n\t" \
    "s_waitcnt lgkmcnt(5)\n\t" \
    "v_mfma_f32_16x16x32_bf16 v[88:91], a[36:39], v[48:51], v[88:91]\n\t" \
    "ds_read_b128 v[24:27], v1 offset:768\n\t" \
    "s_waitcnt lgkmcnt(5)\n\t" \
    "v_mfma_f32_16x16x32_bf16 v[92:95], a[36:39], v[52:55], v[92:95]\n\t" \
    "ds_read_b128 v[28:31], v1 offset:19200\n\t" \
    "v_mfma_f32_16x16x32_bf16 v[96:99], a[108:111], v[48:51], v[96:99]\n\t" \
    "v_mfma_f32_16x16x32_bf16 v[100:103], a[108:111], v[52:55], v[100:103]\n\t" \
    "s_waitcnt lgkmcnt(5)\n\t" \
    "v_mfma_f32_16x16x32_bf16 v[88:91], a[40:43], v[56:59], v[88:91]\n\t" \
    "ds_read_b128 v[32:35], v2 offset:768\n\t" \
    "s_waitcnt lgkmcnt(5)\n\t" \
    "v_mfma_f32_16x16x32_bf16 v[92:95], a[40:43], v[60:63], v[92:95]\n\t" \
    "ds_read_b128 v[36:39], v2 offset:19200\n\t" \
    "v_mfma_f32_16x16x32_bf16 v[96:99], a[112:115], v[56:59], v[96:99]\n\t" \
    "s_waitcnt vmcnt(11)\n\t" \
    "s_barrier\n\t" \
    "v_mfma_f32_16x16x32_bf16 v[100:103], a[112:115], v[60:63], v[100:103]\n\t" \
    "s_cmp_lg_u32 s66, 0\n\t" \
    "s_cbranch_scc1 gx_SF13_%=\n\t" \
    "s_lshl_b32 s69, s67, 7\n\t" \
    "s_add_u32 s69, s69, 5849088\n\t" \
    "s_add_u32 s70, s65, -1\n\t" \
    "v_mov_b32 v156, s69\n\t" \
    "v_mov_b32 v157, s70\n\t" \
    "s_mov_b64 exec, 1\n\t" \
    "global_store_dword v156, v157, s[36:37] sc1\n\t" \
    "s_mov_b64 exec, -1\n\t" \
    "gx_SF13_%=:\n\t" \
    "s_waitcnt lgkmcnt(5)\n\t" \
    "v_mfma_f32_16x16x32_bf16 v[88:91], a[44:47], v[64:67], v[88:91]\n\t" \
    "ds_read_b128 v[40:43], v1 offset:896\n\t" \
    "s_waitcnt lgkmcnt(5)\n\t" \
    "v_mfma_f32_16x16x32_bf16 v[92:95], a[44:47], v[68:71], v[92:95]\n\t" \
    "ds_read_b128 v[44:47], v1 offset:19328\n\t" \
    "v_mfma_f32_16x16x32_bf16 v[96:99], a[116:119], v[64:67], v[96:99]\n\t" \
    "v_add_u32 v22, 0x9000, v1\n\t" \
    "v_add_u32 v23, 0x9000, v2\n\t" \
    "v_mfma_f32_16x16x32_bf16 v[100:103], a[116:119], v[68:71], v[100:103]\n\t" \
    "s_add_u32 s84, s65, 10\n\t" \
    "s_min_u32 s69, s84, 63\n\t" \
    "s_waitcnt lgkmcnt(5)\n\t" \
    "v_mfma_f32_16x16x32_bf16 v[88:91], a[48:51], v[24:27], v[88:91]\n\t" \
    "ds_read_b128 v[48:51], v2 offset:896\n\t" \
    "s_waitcnt lgkmcnt(5)\n\t" \
    "v_mfma_f32_16x16x32_bf16 v[92:95], a[48:51], v[28:31], v[92:95]\n\t" \
    "ds_read_b128 v[52:55], v2 offset:19328\n\t" \
    "v_mfma_f32_16x16x32_bf16 v[96:99], a[120:123], v[24:27], v[96:99]\n\t" \
    "s_mul_i32 s69, s69, 36864\n\t" \
    "s_add_u32 s48, s50, s69\n\t" \
    "v_mfma_f32_16x16x32_bf16 v[100:103], a[120:123], v[28:31], v[100:103]\n\t" \
    "s_addc_u32 s49, s51, 0\n\t" \
    "s_mul_i32 s70, s67, 7\n\t" \
    "s_waitcnt lgkmcnt(5)\n\t" \
    "v_mfma_f32_16x16x32_bf16 v[88:91], a[52:55], v[32:35], v[88:91]\n\t" \
    "ds_read_b128 v[56:59], v1 offset:1024\n\t" \
    "s_waitcnt lgkmcnt(5)\n\t" \
    "v_mfma_f32_16x16x32_bf16 v[92:95], a[52:55], v[36:39], v[92:95]\n\t" \
    "ds_read_b128 v[60:63], v1 offset:19456\n\t" \
    "v_mfma_f32_16x16x32_bf16 v[96:99], a[124:127], v[32:35], v[96:99]\n\t" \
    "s_add_u32 s69, s65, 0\n\t" \
    "s_xor_b32 s69, s69, s70\n\t" \
    "v_mfma_f32_16x16x32_bf16 v[100:103], a[124:127], v[36:39], v[100:103]\n\t" \
    "s_and_b32 s69, s69, 63\n\t" \
    "s_lshl_b32 s69, s69, 13\n\t" \
    "s_waitcnt lgkmcnt(5)\n\t" \
    "v_mfma_f32_16x16x32_bf16 v[88:91], a[56:59], v[40:43], v[88:91]\n\t" \
    "ds_read_b128 v[64:67], v2 offset:1024\n\t" \
    "s_waitcnt lgkmcnt(5)\n\t" \
    "v_mfma_f32_16x16x32_bf16 v[92:95], a[56:59], v[44:47], v[92:95]\n\t" \
    "ds_read_b128 v[68:71], v2 offset:19456\n\t" \
    "v_mfma_f32_16x16x32_bf16 v[96:99], a[128:131], v[40:43], v[96:99]\n\t" \
    "s_mul_i32 s70, s67, 524288\n\t" \
    "s_add_u32 s69, s69, s70\n\t" \
    "v_mfma_f32_16x16x32_bf16 v[100:103], a[128:131], v[44:47], v[100:103]\n\t" \
    "s_add_u32 s69, s69, 6291456\n\t" \
    "s_add_u32 s38, s36, s69\n\t" \
    "s_waitcnt lgkmcnt(5)\n\t" \
    "v_mfma_f32_16x16x32_bf16 v[88:91], a[60:63], v[48:51], v[88:91]\n\t" \
    "ds_read_b128 v[24:27], v22 offset:0\n\t" \
    "s_waitcnt lgkmcnt(5)\n\t" \
    "v_mfma_f32_16x16x32_bf16 v[92:95], a[60:63], v[52:55], v[92:95]\n\t" \
    "ds_read_b128 v[28:31], v22 offset:18432\n\t" \
    "v_mfma_f32_16x16x32_bf16 v[96:99], a[132:135], v[48:51], v[96:99]\n\t" \
    "s_addc_u32 s39, s37, 0\n\t" \
    "v_mfma_f32_16x16x32_bf16 v[100:103], a[132:135], v[52:55], v[100:103]\n\t" \
    "s_waitcnt lgkmcnt(5)\n\t" \
    "v_mfma_f32_16x16x32_bf16 v[88:91], a[64:67], v[56:59], v[88:91]\n\t" \
    "ds_read_b128 v[32:35], v23 offset:0\n\t" \
    "s_waitcnt lgkmcnt(5)\n\t" \
    "v_mfma_f32_16x16x32_bf16 v[92:95], a[64:67], v[60:63], v[92:95]\n\t" \
    "ds_read_b128 v[36:39], v23 offset:18432\n\t" \
    "v_mfma_f32_16x16x32_bf16 v[96:99], a[136:139], v[56:59], v[96:99]\n\t" \
    "v_mfma_f32_16x16x32_bf16 v[100:103], a[136:139], v[60:63], v[100:103]\n\t" \
    "s_waitcnt lgkmcnt(5)\n\t" \
    "v_mfma_f32_16x16x32_bf16 v[88:91], a[68:71], v[64:67], v[88:91]\n\t" \
    "ds_read_b128 v[40:43], v22 offset:128\n\t" \
    "s_waitcnt lgkmcnt(5)\n\t" \
    "v_mfma_f32_16x16x32_bf16 v[92:95], a[68:71], v[68:71], v[92:95]\n\t" \
    "ds_read_b128 v[44:47], v22 offset:18560\n\t" \
    "v_mfma_f32_16x16x32_bf16 v[96:99], a[140:143], v[64:67], v[96:99]\n\t" \
    "v_mfma_f32_16x16x32_bf16 v[100:103], a[140:143], v[68:71], v[100:103]\n\t" \
    "s_waitcnt lgkmcnt(5)\n\t" \
    "v_mfma_f32_16x16x32_bf16 v[72:75], a[144:147], v[24:27], 0\n\t" \
    "ds_read_b128 v[48:51], v23 offset:128\n\t" \
    "s_waitcnt lgkmcnt(5)\n\t" \
    "v_mfma_f32_16x16x32_bf16 v[76:79], a[144:147], v[28:31], 0\n\t" \
    "ds_read_b128 v[52:55], v23 offset:18560\n\t" \
    "v_mfma_f32_16x16x32_bf16 v[80:83], a[216:219], v[24:27], 0\n\t" \
    "s_add_u32 m0, s91, 36864\n\t" \
    "v_mfma_f32_16x16x32_bf16 v[84:87], a[216:219], v[28:31], 0\n\t" \
    "global_load_lds_dwordx4 v203, s[48:49]\n\t" \
    "s_waitcnt lgkmcnt(5)\n\t" \
    "v_mfma_f32_16x16x32_bf16 v[72:75], a[148:151], v[32:35], v[72:75]\n\t" \
    "ds_read_b128 v[56:59], v22 offset:256\n\t" \
    "s_waitcnt lgkmcnt(5)\n\t" \
    "v_mfma_f32_16x16x32_bf16 v[76:79], a[148:151], v[36:39], v[76:79]\n\t" \
    "ds_read_b128 v[60:63], v22 offset:18688\n\t" \
    "v_mfma_f32_16x16x32_bf16 v[80:83], a[220:223], v[32:35], v[80:83]\n\t" \
    "s_add_u32 m0, s91, 37888\n\t" \
    "v_mfma_f32_16x16x32_bf16 v[84:87], a[220:223], v[36:39], v[84:87]\n\t" \
    "global_load_lds_dwordx4 v204, s[48:49]\n\t" \
    "s_waitcnt lgkmcnt(5)\n\t" \
    "v_mfma_f32_16x16x32_bf16 v[72:75], a[152:155], v[40:43], v[72:75]\n\t" \
    "ds_read_b128 v[64:67], v23 offset:256\n\t" \
    "s_waitcnt lgkmcnt(5)\n\t" \
    "v_mfma_f32_16x16x32_bf16 v[76:79], a[152:155], v[44:47], v[76:79]\n\t" \
    "ds_read_b128 v[68:71], v23 offset:18688\n\t" \
    "v_cvt_pk_bf16_f32 v104, v88, v89\n\t" \
    "v_mfma_f32_16x16x32_bf16 v[80:83], a[224:227], v[40:43], v[80:83]\n\t" \
    "s_add_u32 m0, s91, 38912\n\t" \
    "v_cvt_pk_bf16_f32 v105, v90, v91\n\t" \
    "v_mfma_f32_16x16x32_bf16 v[84:87], a[224:227], v[44:47], v[84:87]\n\t" \
    "global_load_lds_dwordx4 v205, s[48:49]\n\t" \
    "s_waitcnt lgkmcnt(5)\n\t" \
    "v_mfma_f32_16x16x32_bf16 v[72:75], a[156:159], v[48:51], v[72:75]\n\t" \
    "ds_read_b128 v[24:27], v22 offset:384\n\t" \
    "s_waitcnt lgkmcnt(5)\n\t" \
    "v_mfma_f32_16x16x32_bf16 v[76:79], a[156:159], v[52:55], v[76:79]\n\t" \
    "ds_read_b128 v[28:31], v22 offset:18816\n\t" \
    "v_cvt_pk_bf16_f32 v106, v96, v97\n\t" \
    "v_mfma_f32_16x16x32_bf16 v[80:83], a[228:231], v[48:51], v[80:83]\n\t" \
    "s_add_u32 m0, s91, 39936\n\t" \
    "v_cvt_pk_bf16_f32 v107, v98, v99\n\t" \
    "v_mfma_f32_16x16x32_bf16 v[84:87], a[228:231], v[52:55], v[84:87]\n\t" \
    "global_load_lds_dwordx4 v206, s[48:49]\n\t" \
    "s_waitcnt lgkmcnt(5)\n\t" \
    "v_mfma_f32_16x16x32_bf16 v[72:75], a[160:163], v[56:59], v[72:75]\n\t" \
    "ds_read_b128 v[32:35], v23 offset:384\n\t" \
    "s_waitcnt lgkmcnt(5)\n\t" \
    "v_mfma_f32_16x16x32_bf16 v[76:79], a[160:163], v[60:63], v[76:79]\n\t" \
    "ds_read_b128 v[36:39], v23 offset:18816\n\t" \
    "global_store_dwordx4 v8, v[104:107], s[38:39] offset:0 sc1\n\t" \
    "v_mfma_f32_16x16x32_bf16 v[80:83], a[232:235], v[56:59], v[80:83]\n\t" \
    "s_add_u32 m0, s91, 40960\n\t" \
    "v_cvt_pk_bf16_f32 v108, v92, v93\n\t" \
    "v_mfma_f32_16x16x32_bf16 v[84:87], a[232:235], v[60:63], v[84:87]\n\t" \
    "global_load_lds_dwordx4 v207, s[48:49]\n\t" \
    "s_waitcnt lgkmcnt(5)\n\t" \
    "v_mfma_f32_16x16x32_bf16 v[72:75], a[164:167], v[64:67], v[72:75]\n\t" \
    "ds_read_b128 v[40:43], v22 offset:512\n\t" \
    "s_waitcnt lgkmcnt(5)\n\t" \
    "v_mfma_f32_16x16x32_bf16 v[76:79], a[164:167], v[68:71], v[76:79]\n\t" \
    "ds_read_b128 v[44:47], v22 offset:18944\n\t" \
    "v_cvt_pk_bf16_f32 v109, v94, v95\n\t" \
    "v_mfma_f32_16x16x32_bf16 v[80:83], a[236:239], v[64:67], v[80:83]\n\t" \
    "s_add_u32 m0, s91, 41984\n\t" \
    "v_cvt_pk_bf16_f32 v110, v100, v101\n\t" \
    "v_mfma_f32_16x16x32_bf16 v[84:87], a[236:239], v[68:71], v[84:87]\n\t" \
    "global_load_lds_dwordx4 v208, s[48:49]\n\t" \
    "s_waitcnt lgkmcnt(5)\n\t" \
    "v_mfma_f32_16x16x32_bf16 v[72:75], a[168:171], v[24:27], v[72:75]\n\t" \
    "ds_read_b128 v[48:51], v23 offset:512\n\t" \
    "s_waitcnt lgkmcnt(5)\n\t" \
    "v_mfma_f32_16x16x32_bf16 v[76:79], a[168:171], v[28:31], v[76:79]\n\t" \
    "ds_read_b128 v[52:55], v23 offset:18944\n\t" \
    "v_cvt_pk_bf16_f32 v111, v102, v103\n\t" \
    "v_mfma_f32_16x16x32_bf16 v[80:83], a[240:243], v[24:27], v[80:83]\n\t" \
    "s_add_u32 m0, s91, 43008\n\t" \
    "global_store_dwordx4 v8, v[108:111], s[38:39] offset:1024 sc1\n\t" \
    "v_mfma_f32_16x16x32_bf16 v[84:87], a[240:243], v[28:31], v[84:87]\n\t" \
    "global_load_lds_dwordx4 v209, s[48:49]\n\t" \
    "s_waitcnt lgkmcnt(5)\n\t" \
    "v_mfma_f32_16x16x32_bf16 v[72:75], a[172:175], v[32:35], v[72:75]\n\t" \
    "ds_read_b128 v[56:59], v22 offset:640\n\t" \
    "s_waitcnt lgkmcnt(5)\n\t" \
    "v_mfma_f32_16x16x32_bf16 v[76:79], a[172:175], v[36:39], v[76:79]\n\t" \
    "ds_read_b128 v[60:63], v22 offset:19072\n\t" \
    "v_mfma_f32_16x16x32_bf16 v[80:83], a[244:247], v[32:35], v[80:83]\n\t" \
    "s_add_u32 m0, s91, 44032\n\t" \
    "v_mfma_f32_16x16x32_bf16 v[84:87], a[244:247], v[36:39], v[84:87]\n\t" \
    "global_load_lds_dwordx4 v210, s[48:49]\n\t" \
    "s_waitcnt lgkmcnt(5)\n\t" \
    "v_mfma_f32_16x16x32_bf16 v[72:75], a[176:179], v[40:43], v[72:75]\n\t" \
    "ds_read_b128 v[64:67], v23 offset:640\n\t" \
    "s_waitcnt lgkmcnt(5)\n\t" \
    "v_mfma_f32_16x16x32_bf16 v[76:79], a[176:179], v[44:47], v[76:79]\n\t" \
    "ds_read_b128 v[68:71], v23 offset:19072\n\t" \
    "v_mfma_f32_16x16x32_bf16 v[80:83], a[248:251], v[40:43], v[80:83]\n\t" \
    "s_add_u32 m0, s91, 45056\n\t" \
    "v_mfma_f32_16x16x32_bf16 v[84:87], a[248:251], v[44:47], v[84:87]\n\t" \
    "global_load_lds_dwordx4 v211, s[48:49]\n\t" \
    "s_waitcnt lgkmcnt(5)\n\t" \
    "v_mfma_f32_16x16x32_bf16 v[72:75], a[180:183], v[48:51], v[72:75]\n\t" \
    "ds_read_b128 v[24:27], v22 offset:768\n\t" \
    "s_waitcnt lgkmcnt(5)\n\t" \
    "v_mfma_f32_16x16x32_bf16 v[76:79], a[180:183], v[52:55], v[76:79]\n\t" \
    "ds_read_b128 v[28:31], v22 offset:19200\n\t" \
    "v_mfma_f32_16x16x32_bf16 v[80:83], a[252:255], v[48:51], v[80:83]\n\t" \
    "v_mfma_f32_16x16x32_bf16 v[84:87], a[252:255], v[52:55], v[84:87]\n\t" \
    "s_waitcnt lgkmcnt(5)\n\t" \
    "v_mfma_f32_16x16x32_bf16 v[72:75], a[184:187], v[56:59], v[72:75]\n\t" \
    "ds_read_b128 v[32:35], v23 offset:768\n\t" \
    "s_waitcnt lgkmcnt(5)\n\t" \
    "v_mfma_f32_16x16x32_bf16 v[76:79], a[184:187], v[60:63], v[76:79]\n\t" \
    "ds_read_b128 v[36:39], v23 offset:19200\n\t" \
    "v_mfma_f32_16x16x32_bf16 v[80:83], v[224:227], v[56:59], v[80:83]\n\t" \
    "s_waitcnt vmcnt(11)\n\t" \
    "s_barrier\n\t" \
    "v_mfma_f32_16x16x32_bf16 v[84:87], v[224:227], v[60:63], v[84:87]\n\t" \
    "s_add_u32 s71, s65, 8\n\t" \
    "s_cmp_lg_u32 s66, 0\n\t" \
    "s_cbranch_scc1 gx_SF14_%=\n\t" \
    "s_lshl_b32 s69, s67, 7\n\t" \
    "s_add_u32 s69, s69, 5771264\n\t" \
    "s_add_u32 s70, s71, 0\n\t" \
    "v_mov_b32 v156, s69\n\t" \
    "v_mov_b32 v157, s70\n\t" \
    "s_mov_b64 exec, 1\n\t" \
    "global_store_dword v156, v157, s[36:37] sc1\n\t" \
    "s_mov_b64 exec, -1\n\t" \
    "gx_SF14_%=:\n\t" \
    "s_waitcnt lgkmcnt(5)\n\t" \
    "v_mfma_f32_16x16x32_bf16 v[72:75], a[188:191], v[64:67], v[72:75]\n\t" \
    "ds_read_b128 v[40:43], v22 offset:896\n\t" \
    "s_waitcnt lgkmcnt(5)\n\t" \
    "v_mfma_f32_16x16x32_bf16 v[76:79], a[188:191], v[68:71], v[76:79]\n\t" \
    "ds_read_b128 v[44:47], v22 offset:19328\n\t" \
    "v_mfma_f32_16x16x32_bf16 v[80:83], v[228:231], v[64:67], v[80:83]\n\t" \
    "s_xor_b32 s90, s90, 0x12000\n\t" \
    "v_add_u32 v1, s90, v0\n\t" \
    "v_mfma_f32_16x16x32_bf16 v[84:87], v[228:231], v[68:71], v[84:87]\n\t" \
    "v_xor_b32 v2, 64, v1\n\t" \
    "s_add_u32 s84, s65, 2\n\t" \
    "s_waitcnt lgkmcnt(5)\n\t" \
    "v_mfma_f32_16x16x32_bf16 v[72:75], a[192:195], v[24:27], v[72:75]\n\t" \
    "ds_read_b128 v[48:51], v23 offset:896\n\t" \
    "s_waitcnt lgkmcnt(5)\n\t" \
    "v_mfma_f32_16x16x32_bf16 v[76:79], a[192:195], v[28:31], v[76:79]\n\t" \
    "ds_read_b128 v[52:55], v23 offset:19328\n\t" \
    "v_mfma_f32_16x16x32_bf16 v[80:83], v[232:235], v[24:27], v[80:83]\n\t" \
    "s_min_u32 s69, s84, 63\n\t" \
    "s_mul_i32 s69, s69, 36864\n\t" \
    "v_mfma_f32_16x16x32_bf16 v[84:87], v[232:235], v[28:31], v[84:87]\n\t" \
    "s_add_u32 s52, s50, s69\n\t" \
    "s_addc_u32 s53, s51, 0\n\t" \
    "s_waitcnt lgkmcnt(5)\n\t" \
    "v_mfma_f32_16x16x32_bf16 v[72:75], a[196:199], v[32:35], v[72:75]\n\t" \
    "ds_read_b128 v[56:59], v22 offset:1024\n\t" \
    "s_waitcnt lgkmcnt(5)\n\t" \
    "v_mfma_f32_16x16x32_bf16 v[76:79], a[196:199], v[36:39], v[76:79]\n\t" \
    "ds_read_b128 v[60:63], v22 offset:19456\n\t" \
    "v_mfma_f32_16x16x32_bf16 v[80:83], v[236:239], v[32:35], v[80:83]\n\t" \
    "s_mul_i32 s70, s67, 7\n\t" \
    "s_add_u32 s69, s65, 9\n\t" \
    "v_mfma_f32_16x16x32_bf16 v[84:87], v[236:239], v[36:39], v[84:87]\n\t" \
    "s_xor_b32 s69, s69, s70\n\t" \
    "s_and_b32 s69, s69, 63\n\t" \
    "s_waitcnt lgkmcnt(5)\n\t" \
    "v_mfma_f32_16x16x32_bf16 v[72:75], a[200:203], v[40:43], v[72:75]\n\t" \
    "ds_read_b128 v[64:67], v23 offset:1024\n\t" \
    "s_waitcnt lgkmcnt(5)\n\t" \
    "v_mfma_f32_16x16x32_bf16 v[76:79], a[200:203], v[44:47], v[76:79]\n\t" \
    "ds_read_b128 v[68:71], v23 offset:19456\n\t" \
    "v_mfma_f32_16x16x32_bf16 v[80:83], v[240:243], v[40:43], v[80:83]\n\t" \
    "s_lshl_b32 s69, s69, 13\n\t" \
    "s_mul_i32 s70, s67, 524288\n\t" \
    "v_mfma_f32_16x16x32_bf16 v[84:87], v[240:243], v[44:47], v[84:87]\n\t" \
    "s_add_u32 s69, s69, s70\n\t" \
    "s_add_u32 s69, s69, 23068672\n\t" \
    "s_waitcnt lgkmcnt(5)\n\t" \
    "v_mfma_f32_16x16x32_bf16 v[72:75], a[204:207], v[48:51], v[72:75]\n\t" \
    "ds_read_b128 v[24:27], v1 offset:0\n\t" \
    "s_waitcnt lgkmcnt(5)\n\t" \
    "v_mfma_f32_16x16x32_bf16 v[76:79], a[204:207], v[52:55], v[76:79]\n\t" \
    "ds_read_b128 v[28:31], v1 offset:18432\n\t" \
    "v_mfma_f32_16x16x32_bf16 v[80:83], v[244:247], v[48:51], v[80:83]\n\t" \
    "s_add_u32 s40, s36, s69\n\t" \
    "s_addc_u32 s41, s37, 0\n\t" \
    "v_mfma_f32_16x16x32_bf16 v[84:87], v[244:247], v[52:55], v[84:87]\n\t" \
    "s_cmp_lt_u32 s65, 55\n\t" \
    "s_cselect_b64 s[40:41], s[40:41], s[42:43]\n\t" \
    "s_waitcnt lgkmcnt(5)\n\t" \
    "v_mfma_f32_16x16x32_bf16 v[72:75], a[208:211], v[56:59], v[72:75]\n\t" \
    "ds_read_b128 v[32:35], v2 offset:0\n\t" \
    "s_waitcnt lgkmcnt(5)\n\t" \
    "v_mfma_f32_16x16x32_bf16 v[76:79], a[208:211], v[60:63], v[76:79]\n\t" \
    "ds_read_b128 v[36:39], v2 offset:18432\n\t" \
    "v_mfma_f32_16x16x32_bf16 v[80:83], v[248:251], v[56:59], v[80:83]\n\t" \
    "s_sub_u32 s91, s85, s91\n\t" \
    "s_add_u32 s65, s65, 1\n\t" \
    "v_mfma_f32_16x16x32_bf16 v[84:87], v[248:251], v[60:63], v[84:87]\n\t" \
    "s_waitcnt lgkmcnt(5)\n\t" \
    "v_mfma_f32_16x16x32_bf16 v[72:75], a[212:215], v[64:67], v[72:75]\n\t" \
    "ds_read_b128 v[40:43], v1 offset:128\n\t" \
    "s_waitcnt lgkmcnt(5)\n\t" \
    "v_mfma_f32_16x16x32_bf16 v[76:79], a[212:215], v[68:71], v[76:79]\n\t" \
    "ds_read_b128 v[44:47], v1 offset:18560\n\t" \
    "v_mfma_f32_16x16x32_bf16 v[80:83], v[252:255], v[64:67], v[80:83]\n\t" \
    "v_mfma_f32_16x16x32_bf16 v[84:87], v[252:255], v[68:71], v[84:87]\n\t" \
    "s_cmp_lt_u32 s65, 64\n\t" \
    "s_cbranch_scc1 gx_l28_%=\n\t" \
    "s_waitcnt vmcnt(0) lgkmcnt(0)\n\t" \
    "s_barrier\n\t" \
    "s_cmp_lg_u32 s66, 0\n\t" \
    "s_cbranch_scc1 gx_SF15_%=\n\t" \
    "s_lshl_b32 s69, s67, 7\n\t" \
    "s_add_u32 s69, s69, 5849088\n\t" \
    "s_add_u32 s70, s65, -1\n\t" \
    "v_mov_b32 v156, s69\n\t" \
    "v_mov_b32 v157, s70\n\t" \
    "s_mov_b64 exec, 1\n\t" \
    "global_store_dword v156, v157, s[36:37] sc1\n\t" \
    "s_mov_b64 exec, -1\n\t" \
    "gx_SF15_%=:\n\t" \
    "gx_last_%=:\n\t" \
    ""
#define ASM_GV \
    "s_load_dwordx2 s[42:43], %3, 56\n\t" \
    "s_load_dwordx2 s[44:45], %3, 64\n\t" \
    "s_load_dwordx2 s[36:37], %3, 0x58\n\t" \
    "s_load_dwordx2 s[50:51], %3, 0x48\n\t" \
    "s_mov_b32 s66, %4\n\t" \
    "s_mov_b32 s67, %5\n\t" \
    "s_mov_b32 s68, 262144\n\t" \
    "s_mov_b32 s72, 0\n\t" \
    "s_mov_b32 s83, -1\n\t" \
    "s_mov_b32 s65, 0\n\t" \
    "s_lshl_b32 s82, s66, 10\n\t" \
    "s_waitcnt lgkmcnt(0)\n\t" \
    "s_lshr_b32 s65, s67, 5\n\t" \
    "s_cselect_b32 s88, 24, 64\n\t" \
    "s_cmp_lt_u32 s65, 24\n\t" \
    "s_addc_u32 s84, s65, 1\n\t" \
    "s_cmp_lt_u32 s84, 24\n\t" \
    "s_addc_u32 s85, s84, 1\n\t" \
    "s_mov_b32 s86, -1\n\t" \
    "s_mov_b32 s87, -1\n\t" \
    "s_add_u32 s38, s36, 48234496\n\t" \
    "s_addc_u32 s39, s37, 0\n\t" \
    "s_and_b32 s69, s67, 31\n\t" \
    "s_mul_i32 s69, s69, 147456\n\t" \
    "s_add_u32 s44, s44, s69\n\t" \
    "s_addc_u32 s45, s45, 0\n\t" \
    "s_mul_i32 s89, s66, 9216\n\t" \
    "s_mov_b32 s90, 0\n\t" \
    "v_mov_b32 v196, -1\n\t" \
    "v_mov_b32 v197, -1\n\t" \
    "v_mov_b32 v198, -1\n\t" \
    "v_mov_b32 v199, -1\n\t" \
    "s_add_u32 s46, s42, 2097152\n\t" \
    "s_addc_u32 s47, s43, 0\n\t" \
    "s_add_u32 m0, s82, 65536\n\t" \
    "s_nop 0\n\t" \
    "global_load_lds_dwordx4 v200, s[46:47]\n\t" \
    "s_add_u32 s46, s42, 2228224\n\t" \
    "s_addc_u32 s47, s43, 0\n\t" \
    "s_add_u32 m0, s82, 69632\n\t" \
    "s_nop 0\n\t" \
    "global_load_lds_dwordx4 v200, s[46:47]\n\t" \
    "s_add_u32 s46, s42, 2359296\n\t" \
    "s_addc_u32 s47, s43, 0\n\t" \
    "s_add_u32 m0, s82, 73728\n\t" \
    "s_nop 0\n\t" \
    "global_load_lds_dwordx4 v200, s[46:47]\n\t" \
    "s_add_u32 s46, s42, 2490368\n\t" \
    "s_addc_u32 s47, s43, 0\n\t" \
    "s_add_u32 m0, s82, 77824\n\t" \
    "s_nop 0\n\t" \
    "global_load_lds_dwordx4 v200, s[46:47]\n\t" \
    "s_add_u32 s46, s42, 2621440\n\t" \
    "s_addc_u32 s47, s43, 0\n\t" \
    "s_add_u32 m0, s82, 81920\n\t" \
    "s_nop 0\n\t" \
    "global_load_lds_dwordx4 v200, s[46:47]\n\t" \
    "s_add_u32 s46, s42, 2752512\n\t" \
    "s_addc_u32 s47, s43, 0\n\t" \
    "s_add_u32 m0, s82, 86016\n\t" \
    "s_nop 0\n\t" \
    "global_load_lds_dwordx4 v200, s[46:47]\n\t" \
    "s_add_u32 s46, s42, 2883584\n\t" \
    "s_addc_u32 s47, s43, 0\n\t" \
    "s_add_u32 m0, s82, 90112\n\t" \
    "s_nop 0\n\t" \
    "global_load_lds_dwordx4 v200, s[46:47]\n\t" \
    "s_add_u32 s46, s42, 3014656\n\t" \
    "s_addc_u32 s47, s43, 0\n\t" \
    "s_add_u32 m0, s82, 94208\n\t" \
    "s_nop 0\n\t" \
    "global_load_lds_dwordx4 v200, s[46:47]\n\t" \
    "s_add_u32 s46, s42, 3145728\n\t" \
    "s_addc_u32 s47, s43, 0\n\t" \
    "s_add_u32 m0, s82, 98304\n\t" \
    "s_nop 0\n\t" \
    "global_load_lds_dwordx4 v200, s[46:47]\n\t" \
    "s_add_u32 s46, s42, 3276800\n\t" \
    "s_addc_u32 s47, s43, 0\n\t" \
    "s_add_u32 m0, s82, 102400\n\t" \
    "s_nop 0\n\t" \
    "global_load_lds_dwordx4 v200, s[46:47]\n\t" \
    "s_add_u32 s46, s42, 3407872\n\t" \
    "s_addc_u32 s47, s43, 0\n\t" \
    "s_add_u32 m0, s82, 106496\n\t" \
    "s_nop 0\n\t" \
    "global_load_lds_dwordx4 v200, s[46:47]\n\t" \
    "s_add_u32 s46, s42, 3538944\n\t" \
    "s_addc_u32 s47, s43, 0\n\t" \
    "s_add_u32 m0, s82, 110592\n\t" \
    "s_nop 0\n\t" \
    "global_load_lds_dwordx4 v200, s[46:47]\n\t" \
    "s_add_u32 s46, s42, 3670016\n\t" \
    "s_addc_u32 s47, s43, 0\n\t" \
    "s_add_u32 m0, s82, 114688\n\t" \
    "s_nop 0\n\t" \
    "global_load_lds_dwordx4 v200, s[46:47]\n\t" \
    "s_add_u32 s46, s42, 3801088\n\t" \
    "s_addc_u32 s47, s43, 0\n\t" \
    "s_add_u32 m0, s82, 118784\n\t" \
    "s_nop 0\n\t" \
    "global_load_lds_dwordx4 v200, s[46:47]\n\t" \
    "s_add_u32 s46, s42, 3932160\n\t" \
    "s_addc_u32 s47, s43, 0\n\t" \
    "s_add_u32 m0, s82, 122880\n\t" \
    "s_nop 0\n\t" \
    "global_load_lds_dwordx4 v200, s[46:47]\n\t" \
    "s_add_u32 s46, s42, 4063232\n\t" \
    "s_addc_u32 s47, s43, 0\n\t" \
    "s_add_u32 m0, s82, 126976\n\t" \
    "s_nop 0\n\t" \
    "global_load_lds_dwordx4 v200, s[46:47]\n\t" \
    "s_add_u32 s46, s42, 4194304\n\t" \
    "s_addc_u32 s47, s43, 0\n\t" \
    "s_add_u32 m0, s82, 131072\n\t" \
    "s_nop 0\n\t" \
    "global_load_lds_dwordx4 v200, s[46:47]\n\t" \
    "s_add_u32 s46, s42, 4325376\n\t" \
    "s_addc_u32 s47, s43, 0\n\t" \
    "s_add_u32 m0, s82, 135168\n\t" \
    "s_nop 0\n\t" \
    "global_load_lds_dwordx4 v200, s[46:47]\n\t" \
    "s_add_u32 s46, s42, 4456448\n\t" \
    "s_addc_u32 s47, s43, 0\n\t" \
    "s_add_u32 m0, s82, 139264\n\t" \
    "s_nop 0\n\t" \
    "global_load_lds_dwordx4 v200, s[46:47]\n\t" \
    "s_add_u32 s46, s42, 4587520\n\t" \
    "s_addc_u32 s47, s43, 0\n\t" \
    "s_add_u32 m0, s82, 143360\n\t" \
    "s_nop 0\n\t" \
    "global_load_lds_dwordx4 v200, s[46:47]\n\t" \
    "s_waitcnt vmcnt(32) lgkmcnt(0)\n\t" \
    "s_barrier\n\t" \
    "ds_read_b64_tr_b16 a[0:1], v201 offset:0\n\t" \
    "ds_read_b64_tr_b16 a[2:3], v201 offset:1024\n\t" \
    "ds_read_b64_tr_b16 a[4:5], v201 offset:8192\n\t" \
    "ds_read_b64_tr_b16 a[6:7], v201 offset:9216\n\t" \
    "ds_read_b64_tr_b16 a[72:73], v202 offset:0\n\t" \
    "ds_read_b64_tr_b16 a[74:75], v202 offset:1024\n\t" \
    "ds_read_b64_tr_b16 a[76:77], v202 offset:8192\n\t" \
    "ds_read_b64_tr_b16 a[78:79], v202 offset:9216\n\t" \
    "s_waitcnt vmcnt(28) lgkmcnt(0)\n\t" \
    "s_barrier\n\t" \
    "ds_read_b64_tr_b16 a[8:9], v201 offset:16384\n\t" \
    "ds_read_b64_tr_b16 a[10:11], v201 offset:17408\n\t" \
    "ds_read_b64_tr_b16 a[12:13], v201 offset:24576\n\t" \
    "ds_read_b64_tr_b16 a[14:15], v201 offset:25600\n\t" \
    "ds_read_b64_tr_b16 a[80:81], v202 offset:16384\n\t" \
    "ds_read_b64_tr_b16 a[82:83], v202 offset:17408\n\t" \
    "ds_read_b64_tr_b16 a[84:85], v202 offset:24576\n\t" \
    "ds_read_b64_tr_b16 a[86:87], v202 offset:25600\n\t" \
    "s_waitcnt vmcnt(24) lgkmcnt(0)\n\t" \
    "s_barrier\n\t" \
    "ds_read_b64_tr_b16 a[16:17], v201 offset:32768\n\t" \
    "ds_read_b64_tr_b16 a[18:19], v201 offset:33792\n\t" \
    "ds_read_b64_tr_b16 a[20:21], v201 offset:40960\n\t" \
    "ds_read_b64_tr_b16 a[22:23], v201 offset:41984\n\t" \
    "ds_read_b64_tr_b16 a[88:89], v202 offset:32768\n\t" \
    "ds_read_b64_tr_b16 a[90:91], v202 offset:33792\n\t" \
    "ds_read_b64_tr_b16 a[92:93], v202 offset:40960\n\t" \
    "ds_read_b64_tr_b16 a[94:95], v202 offset:41984\n\t" \
    "s_waitcnt vmcnt(20) lgkmcnt(0)\n\t" \
    "s_barrier\n\t" \
    "ds_read_b64_tr_b16 a[24:25], v201 offset:49152\n\t" \
    "ds_read_b64_tr_b16 a[26:27], v201 offset:50176\n\t" \
    "ds_read_b64_tr_b16 a[28:29], v201 offset:57344\n\t" \
    "ds_read_b64_tr_b16 a[30:31], v201 offset:58368\n\t" \
    "ds_read_b64_tr_b16 a[96:97], v202 offset:49152\n\t" \
    "ds_read_b64_tr_b16 a[98:99], v202 offset:50176\n\t" \
    "ds_read_b64_tr_b16 a[100:101], v202 offset:57344\n\t" \
    "ds_read_b64_tr_b16 a[102:103], v202 offset:58368\n\t" \
    "s_add_u32 s46, s44, 0\n\t" \
    "s_addc_u32 s47, s45, 0\n\t" \
    "s_mul_i32 s69, s82, 9\n\t" \
    "s_add_u32 m0, s69, 0\n\t" \
    "s_nop 0\n\t" \
    "global_load_lds_dwordx4 v203, s[46:47]\n\t" \
    "s_add_u32 m0, s69, 1024\n\t" \
    "s_nop 0\n\t" \
    "global_load_lds_dwordx4 v204, s[46:47]\n\t" \
    "s_add_u32 m0, s69, 2048\n\t" \
    "s_nop 0\n\t" \
    "global_load_lds_dwordx4 v205, s[46:47]\n\t" \
    "s_add_u32 m0, s69, 3072\n\t" \
    "s_nop 0\n\t" \
    "global_load_lds_dwordx4 v206, s[46:47]\n\t" \
    "s_add_u32 m0, s69, 4096\n\t" \
    "s_nop 0\n\t" \
    "global_load_lds_dwordx4 v207, s[46:47]\n\t" \
    "s_add_u32 m0, s69, 5120\n\t" \
    "s_nop 0\n\t" \
    "global_load_lds_dwordx4 v208, s[46:47]\n\t" \
    "s_add_u32 m0, s69, 6144\n\t" \
    "s_nop 0\n\t" \
    "global_load_lds_dwordx4 v209, s[46:47]\n\t" \
    "s_add_u32 m0, s69, 7168\n\t" \
    "s_nop 0\n\t" \
    "global_load_lds_dwordx4 v210, s[46:47]\n\t" \
    "s_add_u32 m0, s69, 8192\n\t" \
    "s_nop 0\n\t" \
    "global_load_lds_dwordx4 v211, s[46:47]\n\t" \
    "s_min_u32 s69, s65, 63\n\t" \
    "s_mul_i32 s69, s69, 36864\n\t" \
    "s_add_u32 s52, s50, s69\n\t" \
    "s_addc_u32 s53, s51, 0\n\t" \
    "global_load_dwordx4 v[120:123], v203, s[52:53]\n\t" \
    "global_load_dwordx4 v[124:127], v204, s[52:53]\n\t" \
    "global_load_dwordx4 v[128:131], v205, s[52:53]\n\t" \
    "global_load_dwordx4 v[132:135], v206, s[52:53]\n\t" \
    "global_load_dwordx4 v[136:139], v207, s[52:53]\n\t" \
    "global_load_dwordx4 v[140:143], v208, s[52:53]\n\t" \
    "global_load_dwordx4 v[144:147], v209, s[52:53]\n\t" \
    "global_load_dwordx4 v[148:151], v210, s[52:53]\n\t" \
    "global_load_dwordx4 v[152:155], v211, s[52:53]\n\t" \
    "s_waitcnt vmcnt(34) lgkmcnt(0)\n\t" \
    "s_barrier\n\t" \
    "v_add_u32 v201, 0x10000, v201\n\t" \
    "v_add_u32 v202, 0x10000, v202\n\t" \
    "ds_read_b64_tr_b16 a[32:33], v201 offset:0\n\t" \
    "ds_read_b64_tr_b16 a[34:35], v201 offset:1024\n\t" \
    "ds_read_b64_tr_b16 a[36:37], v201 offset:8192\n\t" \
    "ds_read_b64_tr_b16 a[38:39], v201 offset:9216\n\t" \
    "ds_read_b64_tr_b16 a[104:105], v202 offset:0\n\t" \
    "ds_read_b64_tr_b16 a[106:107], v202 offset:1024\n\t" \
    "ds_read_b64_tr_b16 a[108:109], v202 offset:8192\n\t" \
    "ds_read_b64_tr_b16 a[110:111], v202 offset:9216\n\t" \
    "s_waitcnt vmcnt(30) lgkmcnt(0)\n\t" \
    "s_barrier\n\t" \
    "ds_read_b64_tr_b16 a[40:41], v201 offset:16384\n\t" \
    "ds_read_b64_tr_b16 a[42:43], v201 offset:17408\n\t" \
    "ds_read_b64_tr_b16 a[44:45], v201 offset:24576\n\t" \
    "ds_read_b64_tr_b16 a[46:47], v201 offset:25600\n\t" \
    "ds_read_b64_tr_b16 a[112:113], v202 offset:16384\n\t" \
    "ds_read_b64_tr_b16 a[114:115], v202 offset:17408\n\t" \
    "ds_read_b64_tr_b16 a[116:117], v202 offset:24576\n\t" \
    "ds_read_b64_tr_b16 a[118:119], v202 offset:25600\n\t" \
    "s_add_u32 s46, s44, 36864\n\t" \
    "s_addc_u32 s47, s45, 0\n\t" \
    "s_mul_i32 s69, s82, 9\n\t" \
    "s_add_u32 m0, s69, 36864\n\t" \
    "s_nop 0\n\t" \
    "global_load_lds_dwordx4 v203, s[46:47]\n\t" \
    "s_add_u32 m0, s69, 37888\n\t" \
    "s_nop 0\n\t" \
    "global_load_lds_dwordx4 v204, s[46:47]\n\t" \
    "s_add_u32 m0, s69, 38912\n\t" \
    "s_nop 0\n\t" \
    "global_load_lds_dwordx4 v205, s[46:47]\n\t" \
    "s_add_u32 m0, s69, 39936\n\t" \
    "s_nop 0\n\t" \
    "global_load_lds_dwordx4 v206, s[46:47]\n\t" \
    "s_add_u32 m0, s69, 40960\n\t" \
    "s_nop 0\n\t" \
    "global_load_lds_dwordx4 v207, s[46:47]\n\t" \
    "s_add_u32 m0, s69, 41984\n\t" \
    "s_nop 0\n\t" \
    "global_load_lds_dwordx4 v208, s[46:47]\n\t" \
    "s_add_u32 m0, s69, 43008\n\t" \
    "s_nop 0\n\t" \
    "global_load_lds_dwordx4 v209, s[46:47]\n\t" \
    "s_add_u32 m0, s69, 44032\n\t" \
    "s_nop 0\n\t" \
    "global_load_lds_dwordx4 v210, s[46:47]\n\t" \
    "s_add_u32 m0, s69, 45056\n\t" \
    "s_nop 0\n\t" \
    "global_load_lds_dwordx4 v211, s[46:47]\n\t" \
    "s_waitcnt vmcnt(35) lgkmcnt(0)\n\t" \
    "s_barrier\n\t" \
    "ds_read_b64_tr_b16 a[48:49], v201 offset:32768\n\t" \
    "ds_read_b64_tr_b16 a[50:51], v201 offset:33792\n\t" \
    "ds_read_b64_tr_b16 a[52:53], v201 offset:40960\n\t" \
    "ds_read_b64_tr_b16 a[54:55], v201 offset:41984\n\t" \
    "ds_read_b64_tr_b16 a[120:121], v202 offset:32768\n\t" \
    "ds_read_b64_tr_b16 a[122:123], v202 offset:33792\n\t" \
    "ds_read_b64_tr_b16 a[124:125], v202 offset:40960\n\t" \
    "ds_read_b64_tr_b16 a[126:127], v202 offset:41984\n\t" \
    "s_waitcnt vmcnt(31) lgkmcnt(0)\n\t" \
    "s_barrier\n\t" \
    "ds_read_b64_tr_b16 a[56:57], v201 offset:49152\n\t" \
    "ds_read_b64_tr_b16 a[58:59], v201 offset:50176\n\t" \
    "ds_read_b64_tr_b16 a[60:61], v201 offset:57344\n\t" \
    "ds_read_b64_tr_b16 a[62:63], v201 offset:58368\n\t" \
    "ds_read_b64_tr_b16 a[128:129], v202 offset:49152\n\t" \
    "ds_read_b64_tr_b16 a[130:131], v202 offset:50176\n\t" \
    "ds_read_b64_tr_b16 a[132:133], v202 offset:57344\n\t" \
    "ds_read_b64_tr_b16 a[134:135], v202 offset:58368\n\t" \
    "s_add_u32 s46, s44, 73728\n\t" \
    "s_addc_u32 s47, s45, 0\n\t" \
    "s_mul_i32 s69, s82, 9\n\t" \
    "s_add_u32 m0, s69, 73728\n\t" \
    "s_nop 0\n\t" \
    "global_load_lds_dwordx4 v203, s[46:47]\n\t" \
    "s_add_u32 m0, s69, 74752\n\t" \
    "s_nop 0\n\t" \
    "global_load_lds_dwordx4 v204, s[46:47]\n\t" \
    "s_add_u32 m0, s69, 75776\n\t" \
    "s_nop 0\n\t" \
    "global_load_lds_dwordx4 v205, s[46:47]\n\t" \
    "s_add_u32 m0, s69, 76800\n\t" \
    "s_nop 0\n\t" \
    "global_load_lds_dwordx4 v206, s[46:47]\n\t" \
    "s_add_u32 m0, s69, 77824\n\t" \
    "s_nop 0\n\t" \
    "global_load_lds_dwordx4 v207, s[46:47]\n\t" \
    "s_add_u32 m0, s69, 78848\n\t" \
    "s_nop 0\n\t" \
    "global_load_lds_dwordx4 v208, s[46:47]\n\t" \
    "s_add_u32 m0, s69, 79872\n\t" \
    "s_nop 0\n\t" \
    "global_load_lds_dwordx4 v209, s[46:47]\n\t" \
    "s_add_u32 m0, s69, 80896\n\t" \
    "s_nop 0\n\t" \
    "global_load_lds_dwordx4 v210, s[46:47]\n\t" \
    "s_add_u32 m0, s69, 81920\n\t" \
    "s_nop 0\n\t" \
    "global_load_lds_dwordx4 v211, s[46:47]\n\t" \
    "v_mbcnt_lo_u32_b32 v164, -1, 0\n\t" \
    "v_mbcnt_hi_u32_b32 v164, -1, v164\n\t" \
    "v_lshlrev_b32 v164, 6, v164\n\t" \
    "v_add_u32 v164, 6225920, v164\n\t" \
    "v_mov_b32 v165, 6234112\n\t" \
    "global_load_dword v165, v165, s[36:37] sc1\n\t" \
    "global_load_dwordx2 v[166:167], v164, s[36:37] sc1\n\t" \
    "global_load_dword v168, v164, s[36:37] offset:2048 sc1\n\t" \
    "s_waitcnt vmcnt(39) lgkmcnt(0)\n\t" \
    "s_barrier\n\t" \
    "v_add_u32 v201, 0x10000, v201\n\t" \
    "v_add_u32 v202, 0x10000, v202\n\t" \
    "ds_read_b64_tr_b16 a[64:65], v201 offset:0\n\t" \
    "ds_read_b64_tr_b16 a[66:67], v201 offset:1024\n\t" \
    "ds_read_b64_tr_b16 a[68:69], v201 offset:8192\n\t" \
    "ds_read_b64_tr_b16 a[70:71], v201 offset:9216\n\t" \
    "ds_read_b64_tr_b16 a[136:137], v202 offset:0\n\t" \
    "ds_read_b64_tr_b16 a[138:139], v202 offset:1024\n\t" \
    "ds_read_b64_tr_b16 a[140:141], v202 offset:8192\n\t" \
    "ds_read_b64_tr_b16 a[142:143], v202 offset:9216\n\t" \
    "v_add_u32 v201, 0xfffe0000, v201\n\t" \
    "v_add_u32 v202, 0xfffe0000, v202\n\t" \
    "gv_jn2_%=:\n\t" \
    "s_waitcnt lgkmcnt(0)\n\t" \
    "s_barrier\n\t" \
    "s_add_u32 s46, s44, 110592\n\t" \
    "s_addc_u32 s47, s45, 0\n\t" \
    "s_mul_i32 s69, s82, 9\n\t" \
    "s_add_u32 m0, s69, 110592\n\t" \
    "s_nop 0\n\t" \
    "global_load_lds_dwordx4 v203, s[46:47]\n\t" \
    "s_add_u32 m0, s69, 111616\n\t" \
    "s_nop 0\n\t" \
    "global_load_lds_dwordx4 v204, s[46:47]\n\t" \
    "s_add_u32 m0, s69, 112640\n\t" \
    "s_nop 0\n\t" \
    "global_load_lds_dwordx4 v205, s[46:47]\n\t" \
    "s_add_u32 m0, s69, 113664\n\t" \
    "s_nop 0\n\t" \
    "global_load_lds_dwordx4 v206, s[46:47]\n\t" \
    "s_add_u32 m0, s69, 114688\n\t" \
    "s_nop 0\n\t" \
    "global_load_lds_dwordx4 v207, s[46:47]\n\t" \
    "s_add_u32 m0, s69, 115712\n\t" \
    "s_nop 0\n\t" \
    "global_load_lds_dwordx4 v208, s[46:47]\n\t" \
    "s_add_u32 m0, s69, 116736\n\t" \
    "s_nop 0\n\t" \
    "global_load_lds_dwordx4 v209, s[46:47]\n\t" \
    "s_add_u32 m0, s69, 117760\n\t" \
    "s_nop 0\n\t" \
    "global_load_lds_dwordx4 v210, s[46:47]\n\t" \
    "s_add_u32 m0, s69, 118784\n\t" \
    "s_nop 0\n\t" \
    "global_load_lds_dwordx4 v211, s[46:47]\n\t" \
    "s_waitcnt vmcnt(39) lgkmcnt(0)\n\t" \
    "s_barrier\n\t" \
    "ds_read_b64_tr_b16 a[144:145], v212 offset:0\n\t" \
    "ds_read_b64_tr_b16 a[146:147], v212 offset:18432\n\t" \
    "ds_read_b64_tr_b16 a[160:161], v213 offset:0\n\t" \
    "ds_read_b64_tr_b16 a[162:163], v213 offset:18432\n\t" \
    "ds_read_b64_tr_b16 a[176:177], v214 offset:0\n\t" \
    "ds_read_b64_tr_b16 a[178:179], v214 offset:18432\n\t" \
    "s_waitcnt lgkmcnt(6)\n\t" \
    "ds_read_b64_tr_b16 a[192:193], v215 offset:0\n\t" \
    "ds_read_b64_tr_b16 a[194:195], v215 offset:18432\n\t" \
    "ds_read_b64_tr_b16 a[208:209], v212 offset:128\n\t" \
    "ds_read_b64_tr_b16 a[210:211], v212 offset:18560\n\t" \
    "ds_read_b64_tr_b16 a[224:225], v213 offset:128\n\t" \
    "ds_read_b64_tr_b16 a[226:227], v213 offset:18560\n\t" \
    "s_waitcnt lgkmcnt(6)\n\t" \
    "ds_read_b64_tr_b16 a[240:241], v214 offset:128\n\t" \
    "ds_read_b64_tr_b16 a[242:243], v214 offset:18560\n\t" \
    "ds_read_b64_tr_b16 v[224:225], v215 offset:128\n\t" \
    "ds_read_b64_tr_b16 v[226:227], v215 offset:18560\n\t" \
    "ds_read_b64_tr_b16 v[240:241], v212 offset:256\n\t" \
    "ds_read_b64_tr_b16 v[242:243], v212 offset:18688\n\t" \
    "s_waitcnt lgkmcnt(6)\n\t" \
    "v_add_u32 v212, 0x9000, v212\n\t" \
    "v_add_u32 v213, 0x9000, v213\n\t" \
    "v_add_u32 v214, 0x9000, v214\n\t" \
    "v_add_u32 v215, 0x9000, v215\n\t" \
    "s_waitcnt vmcnt(21) lgkmcnt(0)\n\t" \
    "s_barrier\n\t" \
    "ds_read_b64_tr_b16 a[148:149], v212 offset:0\n\t" \
    "ds_read_b64_tr_b16 a[150:151], v212 offset:18432\n\t" \
    "ds_read_b64_tr_b16 a[164:165], v213 offset:0\n\t" \
    "ds_read_b64_tr_b16 a[166:167], v213 offset:18432\n\t" \
    "ds_read_b64_tr_b16 a[180:181], v214 offset:0\n\t" \
    "ds_read_b64_tr_b16 a[182:183], v214 offset:18432\n\t" \
    "s_waitcnt lgkmcnt(6)\n\t" \
    "ds_read_b64_tr_b16 a[196:197], v215 offset:0\n\t" \
    "ds_read_b64_tr_b16 a[198:199], v215 offset:18432\n\t" \
    "ds_read_b64_tr_b16 a[212:213], v212 offset:128\n\t" \
    "ds_read_b64_tr_b16 a[214:215], v212 offset:18560\n\t" \
    "ds_read_b64_tr_b16 a[228:229], v213 offset:128\n\t" \
    "ds_read_b64_tr_b16 a[230:231], v213 offset:18560\n\t" \
    "s_waitcnt lgkmcnt(6)\n\t" \
    "ds_read_b64_tr_b16 a[244:245], v214 offset:128\n\t" \
    "ds_read_b64_tr_b16 a[246:247], v214 offset:18560\n\t" \
    "ds_read_b64_tr_b16 v[228:229], v215 offset:128\n\t" \
    "ds_read_b64_tr_b16 v[230:231], v215 offset:18560\n\t" \
    "ds_read_b64_tr_b16 v[244:245], v212 offset:256\n\t" \
    "ds_read_b64_tr_b16 v[246:247], v212 offset:18688\n\t" \
    "s_waitcnt lgkmcnt(6)\n\t" \
    "v_add_u32 v212, 0x9000, v212\n\t" \
    "v_add_u32 v213, 0x9000, v213\n\t" \
    "v_add_u32 v214, 0x9000, v214\n\t" \
    "v_add_u32 v215, 0x9000, v215\n\t" \
    "ds_write_b128 v3, v[120:123] offset:0\n\t" \
    "ds_write_b128 v3, v[124:127] offset:1024\n\t" \
    "ds_write_b128 v3, v[128:131] offset:2048\n\t" \
    "ds_write_b128 v3, v[132:135] offset:3072\n\t" \
    "ds_write_b128 v3, v[136:139] offset:4096\n\t" \
    "ds_write_b128 v3, v[140:143] offset:5120\n\t" \
    "ds_write_b128 v3, v[144:147] offset:6144\n\t" \
    "ds_write_b128 v3, v[148:151] offset:7168\n\t" \
    "ds_write_b128 v3, v[152:155] offset:8192\n\t" \
    "s_waitcnt vmcnt(12) lgkmcnt(0)\n\t" \
    "s_barrier\n\t" \
    "ds_read_b64_tr_b16 a[152:153], v212 offset:0\n\t" \
    "ds_read_b64_tr_b16 a[154:155], v212 offset:18432\n\t" \
    "ds_read_b64_tr_b16 a[168:169], v213 offset:0\n\t" \
    "ds_read_b64_tr_b16 a[170:171], v213 offset:18432\n\t" \
    "ds_read_b64_tr_b16 a[184:185], v214 offset:0\n\t" \
    "ds_read_b64_tr_b16 a[186:187], v214 offset:18432\n\t" \
    "s_waitcnt lgkmcnt(6)\n\t" \
    "ds_read_b64_tr_b16 a[200:201], v215 offset:0\n\t" \
    "ds_read_b64_tr_b16 a[202:203], v215 offset:18432\n\t" \
    "ds_read_b64_tr_b16 a[216:217], v212 offset:128\n\t" \
    "ds_read_b64_tr_b16 a[218:219], v212 offset:18560\n\t" \
    "ds_read_b64_tr_b16 a[232:233], v213 offset:128\n\t" \
    "ds_read_b64_tr_b16 a[234:235], v213 offset:18560\n\t" \
    "s_waitcnt lgkmcnt(6)\n\t" \
    "ds_read_b64_tr_b16 a[248:249], v214 offset:128\n\t" \
    "ds_read_b64_tr_b16 a[250:251], v214 offset:18560\n\t" \
    "ds_read_b64_tr_b16 v[232:233], v215 offset:128\n\t" \
    "ds_read_b64_tr_b16 v[234:235], v215 offset:18560\n\t" \
    "ds_read_b64_tr_b16 v[248:249], v212 offset:256\n\t" \
    "ds_read_b64_tr_b16 v[250:251], v212 offset:18688\n\t" \
    "s_waitcnt lgkmcnt(6)\n\t" \
    "v_add_u32 v212, 0x9000, v212\n\t" \
    "v_add_u32 v213, 0x9000, v213\n\t" \
    "v_add_u32 v214, 0x9000, v214\n\t" \
    "v_add_u32 v215, 0x9000, v215\n\t" \
    "s_min_u32 s69, s84, 63\n\t" \
    "s_mul_i32 s69, s69, 36864\n\t" \
    "s_add_u32 s52, s50, s69\n\t" \
    "s_addc_u32 s53, s51, 0\n\t" \
    "s_add_u32 m0, s89, 36864\n\t" \
    "s_nop 0\n\t" \
    "global_load_lds_dwordx4 v203, s[52:53]\n\t" \
    "s_add_u32 m0, s89, 37888\n\t" \
    "s_nop 0\n\t" \
    "global_load_lds_dwordx4 v204, s[52:53]\n\t" \
    "s_add_u32 m0, s89, 38912\n\t" \
    "s_nop 0\n\t" \
    "global_load_lds_dwordx4 v205, s[52:53]\n\t" \
    "s_add_u32 m0, s89, 39936\n\t" \
    "s_nop 0\n\t" \
    "global_load_lds_dwordx4 v206, s[52:53]\n\t" \
    "s_add_u32 m0, s89, 40960\n\t" \
    "s_nop 0\n\t" \
    "global_load_lds_dwordx4 v207, s[52:53]\n\t" \
    "s_add_u32 m0, s89, 41984\n\t" \
    "s_nop 0\n\t" \
    "global_load_lds_dwordx4 v208, s[52:53]\n\t" \
    "s_add_u32 m0, s89, 43008\n\t" \
    "s_nop 0\n\t" \
    "global_load_lds_dwordx4 v209, s[52:53]\n\t" \
    "s_add_u32 m0, s89, 44032\n\t" \
    "s_nop 0\n\t" \
    "global_load_lds_dwordx4 v210, s[52:53]\n\t" \
    "s_add_u32 m0, s89, 45056\n\t" \
    "s_nop 0\n\t" \
    "global_load_lds_dwordx4 v211, s[52:53]\n\t" \
    "s_waitcnt vmcnt(18)\n\t" \
    "gv_C3_%=:\n\t" \
    "v_cmp_ne_u32 s[54:55], v166, v168\n\t" \
    "v_sub_u32 v166, v166, v165\n\t" \
    "v_cmp_ne_u32 vcc, 1, v166\n\t" \
    "s_nop 1\n\t" \
    "s_or_b64 vcc, vcc, s[54:55]\n\t" \
    "s_cbranch_scc0 gv_Cok4_%=\n\t" \
    "s_cmp_eq_u32 s68, 0\n\t" \
    "s_cbranch_scc1 gv_F6_%=\n\t" \
    "s_sub_u32 s68, s68, 1\n\t" \
    "s_sleep 6\n\t" \
    "s_branch gv_Cag5_%=\n\t" \
    "gv_F6_%=:\n\t" \
    "v_mov_b32 v168, 5767424\n\t" \
    "v_mov_b32 v169, 4\n\t" \
    "global_store_dword v168, v169, s[36:37] sc1\n\t" \
    "s_waitcnt vmcnt(0)\n\t" \
    "s_branch gv_Cok4_%=\n\t" \
    "gv_Cag5_%=:\n\t" \
    "global_load_dwordx2 v[166:167], v164, s[36:37] sc1\n\t" \
    "global_load_dword v168, v164, s[36:37] offset:2048 sc1\n\t" \
    "s_waitcnt vmcnt(0)\n\t" \
    "s_branch gv_C3_%=\n\t" \
    "gv_Cok4_%=:\n\t" \
    "s_cmp_lg_u32 s66, 0\n\t" \
    "s_cbranch_scc1 gv_Ca7_%=\n\t" \
    "v_mov_b32 v164, 5836800\n\t" \
    "s_mov_b64 exec, 1\n\t" \
    "global_atomic_add v164, v166, s[36:37] sc1\n\t" \
    "s_mov_b64 exec, -1\n\t" \
    "gv_Ca7_%=:\n\t" \
    "v_mov_b32_dpp v164, v167 quad_perm:[1,0,3,2] row_mask:0xf bank_mask:0xf\n\t" \
    "v_cmp_lt_u32 s[44:45], v164, v167\n\t" \
    "s_nop 1\n\t" \
    "s_waitcnt lgkmcnt(0)\n\t" \
    "s_barrier\n\t" \
    "ds_read_b128 v[24:27], v1 offset:0\n\t" \
    "ds_read_b128 v[28:31], v1 offset:18432\n\t" \
    "ds_read_b128 v[32:35], v2 offset:0\n\t" \
    "ds_read_b128 v[36:39], v2 offset:18432\n\t" \
    "ds_read_b128 v[40:43], v1 offset:128\n\t" \
    "ds_read_b128 v[44:47], v1 offset:18560\n\t" \
    ".p2align 6\n\t" \
    "s_nop 0\n\t" \
    "s_nop 0\n\t" \
    "gv_loop8_%=:\n\t" \
    "s_min_u32 s69, s85, 63\n\t" \
    "s_mul_i32 s69, s69, 36864\n\t" \
    "s_add_u32 s52, s50, s69\n\t" \
    "s_addc_u32 s53, s51, 0\n\t" \
    "s_add_u32 s90, s90, 36864\n\t" \
    "s_cmp_eq_u32 s90, 110592\n\t" \
    "s_cselect_b32 s90, 0, s90\n\t" \
    "s_add_u32 s91, s90, 36864\n\t" \
    "s_cmp_eq_u32 s91, 110592\n\t" \
    "s_cselect_b32 s91, 0, s91\n\t" \
    "s_add_u32 s91, s91, s89\n\t" \
    "v_add_u32 v22, s90, v0\n\t" \
    "v_xor_b32 v23, 64, v22\n\t" \
    "s_lshl_b32 s40, s65, 15\n\t" \
    "s_cselect_b32 s70, s65, 64\n\t" \
    "s_mul_i32 s69, s70, 171\n\t" \
    "s_lshr_b32 s69, s69, 9\n\t" \
    "s_mul_i32 s70, s70, 589824\n\t" \
    "s_mul_i32 s71, s69, 327680\n\t" \
    "s_add_u32 s70, s70, s71\n\t" \
    "s_lshl1_add_u32 s69, s69, 4\n\t" \
    "s_bitcmp1_b64 s[44:45], s69\n\t" \
    "s_cselect_b32 s69, 8192, 0\n\t" \
    "s_add_u32 s69, s69, s70\n\t" \
    "s_add_u32 s69, s69, 123731968\n\t" \
    "s_add_u32 s48, s36, s69\n\t" \
    "s_addc_u32 s49, s37, 0\n\t" \
    "s_waitcnt lgkmcnt(5)\n\t" \
    "v_mfma_f32_16x16x32_bf16 v[72:75], a[0:3], v[24:27], 0\n\t" \
    "ds_read_b128 v[48:51], v2 offset:128\n\t" \
    "s_waitcnt lgkmcnt(5)\n\t" \
    "v_mfma_f32_16x16x32_bf16 v[76:79], a[0:3], v[28:31], 0\n\t" \
    "ds_read_b128 v[52:55], v2 offset:18560\n\t" \
    "v_mfma_f32_16x16x32_bf16 v[80:83], a[72:75], v[24:27], 0\n\t" \
    "s_add_u32 m0, s91, 0\n\t" \
    "v_mfma_f32_16x16x32_bf16 v[84:87], a[72:75], v[28:31], 0\n\t" \
    "global_load_lds_dwordx4 v203, s[52:53]\n\t" \
    "s_waitcnt lgkmcnt(5)\n\t" \
    "v_mfma_f32_16x16x32_bf16 v[72:75], a[4:7], v[32:35], v[72:75]\n\t" \
    "ds_read_b128 v[56:59], v1 offset:256\n\t" \
    "s_waitcnt lgkmcnt(5)\n\t" \
    "v_mfma_f32_16x16x32_bf16 v[76:79], a[4:7], v[36:39], v[76:79]\n\t" \
    "ds_read_b128 v[60:63], v1 offset:18688\n\t" \
    "v_mfma_f32_16x16x32_bf16 v[80:83], a[76:79], v[32:35], v[80:83]\n\t" \
    "s_add_u32 m0, s91, 1024\n\t" \
    "v_mfma_f32_16x16x32_bf16 v[84:87], a[76:79], v[36:39], v[84:87]\n\t" \
    "global_load_lds_dwordx4 v204, s[52:53]\n\t" \
    "s_waitcnt lgkmcnt(5)\n\t" \
    "v_mfma_f32_16x16x32_bf16 v[72:75], a[8:11], v[40:43], v[72:75]\n\t" \
    "ds_read_b128 v[64:67], v2 offset:256\n\t" \
    "s_waitcnt lgkmcnt(5)\n\t" \
    "v_mfma_f32_16x16x32_bf16 v[76:79], a[8:11], v[44:47], v[76:79]\n\t" \
    "ds_read_b128 v[68:71], v2 offset:18688\n\t" \
    "v_mfma_f32_16x16x32_bf16 v[80:83], a[80:83], v[40:43], v[80:83]\n\t" \
    "s_add_u32 m0, s91, 2048\n\t" \
    "v_mfma_f32_16x16x32_bf16 v[84:87], a[80:83], v[44:47], v[84:87]\n\t" \
    "global_load_lds_dwordx4 v205, s[52:53]\n\t" \
    "s_waitcnt lgkmcnt(5)\n\t" \
    "v_mfma_f32_16x16x32_bf16 v[72:75], a[12:15], v[48:51], v[72:75]\n\t" \
    "ds_read_b128 v[24:27], v1 offset:384\n\t" \
    "s_waitcnt lgkmcnt(5)\n\t" \
    "v_mfma_f32_16x16x32_bf16 v[76:79], a[12:15], v[52:55], v[76:79]\n\t" \
    "ds_read_b128 v[28:31], v1 offset:18816\n\t" \
    "v_mfma_f32_16x16x32_bf16 v[80:83], a[84:87], v[48:51], v[80:83]\n\t" \
    "s_add_u32 m0, s91, 3072\n\t" \
    "v_mfma_f32_16x16x32_bf16 v[84:87], a[84:87], v[52:55], v[84:87]\n\t" \
    "global_load_lds_dwordx4 v206, s[52:53]\n\t" \
    "s_waitcnt lgkmcnt(5)\n\t" \
    "v_mfma_f32_16x16x32_bf16 v[72:75], a[16:19], v[56:59], v[72:75]\n\t" \
    "ds_read_b128 v[32:35], v2 offset:384\n\t" \
    "s_waitcnt lgkmcnt(5)\n\t" \
    "v_mfma_f32_16x16x32_bf16 v[76:79], a[16:19], v[60:63], v[76:79]\n\t" \
    "ds_read_b128 v[36:39], v2 offset:18816\n\t" \
    "v_mfma_f32_16x16x32_bf16 v[80:83], a[88:91], v[56:59], v[80:83]\n\t" \
    "s_add_u32 m0, s91, 4096\n\t" \
    "v_mfma_f32_16x16x32_bf16 v[84:87], a[88:91], v[60:63], v[84:87]\n\t" \
    "global_load_lds_dwordx4 v207, s[52:53]\n\t" \
    "s_waitcnt lgkmcnt(5)\n\t" \
    "v_mfma_f32_16x16x32_bf16 v[72:75], a[20:23], v[64:67], v[72:75]\n\t" \
    "ds_read_b128 v[40:43], v1 offset:512\n\t" \
    "s_waitcnt lgkmcnt(5)\n\t" \
    "v_mfma_f32_16x16x32_bf16 v[76:79], a[20:23], v[68:71], v[76:79]\n\t" \
    "ds_read_b128 v[44:47], v1 offset:18944\n\t" \
    "v_mfma_f32_16x16x32_bf16 v[80:83], a[92:95], v[64:67], v[80:83]\n\t" \
    "s_add_u32 m0, s91, 5120\n\t" \
    "v_mfma_f32_16x16x32_bf16 v[84:87], a[92:95], v[68:71], v[84:87]\n\t" \
    "global_load_lds_dwordx4 v208, s[52:53]\n\t" \
    "s_waitcnt lgkmcnt(5)\n\t" \
    "v_mfma_f32_16x16x32_bf16 v[72:75], a[24:27], v[24:27], v[72:75]\n\t" \
    "ds_read_b128 v[48:51], v2 offset:512\n\t" \
    "s_waitcnt lgkmcnt(5)\n\t" \
    "v_mfma_f32_16x16x32_bf16 v[76:79], a[24:27], v[28:31], v[76:79]\n\t" \
    "ds_read_b128 v[52:55], v2 offset:18944\n\t" \
    "v_mfma_f32_16x16x32_bf16 v[80:83], a[96:99], v[24:27], v[80:83]\n\t" \
    "s_add_u32 m0, s91, 6144\n\t" \
    "v_mfma_f32_16x16x32_bf16 v[84:87], a[96:99], v[28:31], v[84:87]\n\t" \
    "global_load_lds_dwordx4 v209, s[52:53]\n\t" \
    "s_waitcnt lgkmcnt(5)\n\t" \
    "v_mfma_f32_16x16x32_bf16 v[72:75], a[28:31], v[32:35], v[72:75]\n\t" \
    "ds_read_b128 v[56:59], v1 offset:640\n\t" \
    "s_waitcnt lgkmcnt(5)\n\t" \
    "v_mfma_f32_16x16x32_bf16 v[76:79], a[28:31], v[36:39], v[76:79]\n\t" \
    "ds_read_b128 v[60:63], v1 offset:19072\n\t" \
    "v_mfma_f32_16x16x32_bf16 v[80:83], a[100:103], v[32:35], v[80:83]\n\t" \
    "s_add_u32 m0, s91, 7168\n\t" \
    "v_mfma_f32_16x16x32_bf16 v[84:87], a[100:103], v[36:39], v[84:87]\n\t" \
    "global_load_lds_dwordx4 v210, s[52:53]\n\t" \
    "s_waitcnt lgkmcnt(5)\n\t" \
    "v_mfma_f32_16x16x32_bf16 v[72:75], a[32:35], v[40:43], v[72:75]\n\t" \
    "ds_read_b128 v[64:67], v2 offset:640\n\t" \
    "s_waitcnt lgkmcnt(5)\n\t" \
    "v_mfma_f32_16x16x32_bf16 v[76:79], a[32:35], v[44:47], v[76:79]\n\t" \
    "ds_read_b128 v[68:71], v2 offset:19072\n\t" \
    "v_mfma_f32_16x16x32_bf16 v[80:83], a[104:107], v[40:43], v[80:83]\n\t" \
    "s_add_u32 m0, s91, 8192\n\t" \
    "v_mfma_f32_16x16x32_bf16 v[84:87], a[104:107], v[44:47], v[84:87]\n\t" \
    "global_load_lds_dwordx4 v211, s[52:53]\n\t" \
    "s_waitcnt lgkmcnt(5)\n\t" \
    "v_mfma_f32_16x16x32_bf16 v[72:75], a[36:39], v[48:51], v[72:75]\n\t" \
    "ds_read_b128 v[24:27], v1 offset:768\n\t" \
    "s_waitcnt lgkmcnt(5)\n\t" \
    "v_mfma_f32_16x16x32_bf16 v[76:79], a[36:39], v[52:55], v[76:79]\n\t" \
    "ds_read_b128 v[28:31], v1 offset:19200\n\t" \
    "v_mfma_f32_16x16x32_bf16 v[80:83], a[108:111], v[48:51], v[80:83]\n\t" \
    "v_mfma_f32_16x16x32_bf16 v[84:87], a[108:111], v[52:55], v[84:87]\n\t" \
    "s_waitcnt lgkmcnt(5)\n\t" \
    "v_mfma_f32_16x16x32_bf16 v[72:75], a[40:43], v[56:59], v[72:75]\n\t" \
    "ds_read_b128 v[32:35], v2 offset:768\n\t" \
    "s_waitcnt lgkmcnt(5)\n\t" \
    "v_mfma_f32_16x16x32_bf16 v[76:79], a[40:43], v[60:63], v[76:79]\n\t" \
    "ds_read_b128 v[36:39], v2 offset:19200\n\t" \
    "v_mfma_f32_16x16x32_bf16 v[80:83], a[112:115], v[56:59], v[80:83]\n\t" \
    "v_mfma_f32_16x16x32_bf16 v[84:87], a[112:115], v[60:63], v[84:87]\n\t" \
    "global_store_dwordx4 v19, v[196:199], s[48:49] sc1\n\t" \
    "s_waitcnt lgkmcnt(5)\n\t" \
    "v_mfma_f32_16x16x32_bf16 v[72:75], a[44:47], v[64:67], v[72:75]\n\t" \
    "ds_read_b128 v[40:43], v1 offset:896\n\t" \
    "s_waitcnt lgkmcnt(5)\n\t" \
    "v_mfma_f32_16x16x32_bf16 v[76:79], a[44:47], v[68:71], v[76:79]\n\t" \
    "ds_read_b128 v[44:47], v1 offset:19328\n\t" \
    "v_mfma_f32_16x16x32_bf16 v[80:83], a[116:119], v[64:67], v[80:83]\n\t" \
    "v_mfma_f32_16x16x32_bf16 v[84:87], a[116:119], v[68:71], v[84:87]\n\t" \
    "s_waitcnt lgkmcnt(5)\n\t" \
    "v_mfma_f32_16x16x32_bf16 v[72:75], a[48:51], v[24:27], v[72:75]\n\t" \
    "ds_read_b128 v[48:51], v2 offset:896\n\t" \
    "s_waitcnt lgkmcnt(5)\n\t" \
    "v_mfma_f32_16x16x32_bf16 v[76:79], a[48:51], v[28:31], v[76:79]\n\t" \
    "ds_read_b128 v[52:55], v2 offset:19328\n\t" \
    "v_mfma_f32_16x16x32_bf16 v[80:83], a[120:123], v[24:27], v[80:83]\n\t" \
    "v_mfma_f32_16x16x32_bf16 v[84:87], a[120:123], v[28:31], v[84:87]\n\t" \
    "s_waitcnt lgkmcnt(5)\n\t" \
    "v_mfma_f32_16x16x32_bf16 v[72:75], a[52:55], v[32:35], v[72:75]\n\t" \
    "ds_read_b128 v[56:59], v1 offset:1024\n\t" \
    "s_waitcnt lgkmcnt(5)\n\t" \
    "v_mfma_f32_16x16x32_bf16 v[76:79], a[52:55], v[36:39], v[76:79]\n\t" \
    "ds_read_b128 v[60:63], v1 offset:19456\n\t" \
    "v_mfma_f32_16x16x32_bf16 v[80:83], a[124:127], v[32:35], v[80:83]\n\t" \
    "v_mfma_f32_16x16x32_bf16 v[84:87], a[124:127], v[36:39], v[84:87]\n\t" \
    "global_store_dwordx4 v20, v[196:199], s[48:49] sc1\n\t" \
    "s_waitcnt lgkmcnt(5)\n\t" \
    "v_mfma_f32_16x16x32_bf16 v[72:75], a[56:59], v[40:43], v[72:75]\n\t" \
    "ds_read_b128 v[64:67], v2 offset:1024\n\t" \
    "s_waitcnt lgkmcnt(5)\n\t" \
    "v_mfma_f32_16x16x32_bf16 v[76:79], a[56:59], v[44:47], v[76:79]\n\t" \
    "ds_read_b128 v[68:71], v2 offset:19456\n\t" \
    "v_mfma_f32_16x16x32_bf16 v[80:83], a[128:131], v[40:43], v[80:83]\n\t" \
    "v_mfma_f32_16x16x32_bf16 v[84:87], a[128:131], v[44:47], v[84:87]\n\t" \
    "s_waitcnt lgkmcnt(5)\n\t" \
    "v_mfma_f32_16x16x32_bf16 v[72:75], a[60:63], v[48:51], v[72:75]\n\t" \
    "s_waitcnt lgkmcnt(4)\n\t" \
    "v_mfma_f32_16x16x32_bf16 v[76:79], a[60:63], v[52:55], v[76:79]\n\t" \
    "v_mfma_f32_16x16x32_bf16 v[80:83], a[132:135], v[48:51], v[80:83]\n\t" \
    "v_mfma_f32_16x16x32_bf16 v[84:87], a[132:135], v[52:55], v[84:87]\n\t" \
    "s_waitcnt lgkmcnt(3)\n\t" \
    "v_mfma_f32_16x16x32_bf16 v[72:75], a[64:67], v[56:59], v[72:75]\n\t" \
    "s_waitcnt lgkmcnt(2)\n\t" \
    "v_mfma_f32_16x16x32_bf16 v[76:79], a[64:67], v[60:63], v[76:79]\n\t" \
    "v_mfma_f32_16x16x32_bf16 v[80:83], a[136:139], v[56:59], v[80:83]\n\t" \
    "v_mfma_f32_16x16x32_bf16 v[84:87], a[136:139], v[60:63], v[84:87]\n\t" \
    "global_store_dword v21, v196, s[48:49] sc1\n\t" \
    "s_waitcnt lgkmcnt(1)\n\t" \
    "v_mfma_f32_16x16x32_bf16 v[72:75], a[68:71], v[64:67], v[72:75]\n\t" \
    "s_waitcnt lgkmcnt(0)\n\t" \
    "v_mfma_f32_16x16x32_bf16 v[76:79], a[68:71], v[68:71], v[76:79]\n\t" \
    "v_mfma_f32_16x16x32_bf16 v[80:83], a[140:143], v[64:67], v[80:83]\n\t" \
    "v_mfma_f32_16x16x32_bf16 v[84:87], a[140:143], v[68:71], v[84:87]\n\t" \
    "v_mov_b32 v1, v22\n\t" \
    "v_mov_b32 v2, v23\n\t" \
    "s_nop 1\n\t" \
    "v_cvt_pk_bf16_f32 v104, v72, v73\n\t" \
    "v_cvt_pk_bf16_f32 v105, v74, v75\n\t" \
    "s_nop 0\n\t" \
    "v_cvt_pk_bf16_f32 v106, v80, v81\n\t" \
    "v_cvt_pk_bf16_f32 v107, v82, v83\n\t" \
    "ds_write_b128 v4, v[104:107] offset:0\n\t" \
    "v_cvt_pk_bf16_f32 v108, v76, v77\n\t" \
    "v_cvt_pk_bf16_f32 v109, v78, v79\n\t" \
    "v_cvt_pk_bf16_f32 v110, v84, v85\n\t" \
    "v_cvt_pk_bf16_f32 v111, v86, v87\n\t" \
    "ds_write_b128 v4, v[108:111] offset:1024\n\t" \
    "s_cmp_lt_i32 s86, 0\n\t" \
    "s_cbranch_scc1 gv_rest10_%=\n\t" \
    "gv_back11_%=:\n\t" \
    "s_waitcnt vmcnt(21) lgkmcnt(0)\n\t" \
    "s_barrier\n\t" \
    "ds_read_b128 v[164:167], v5 offset:0\n\t" \
    "ds_read_b128 v[168:171], v5 offset:1024\n\t" \
    "ds_read_b128 v[172:175], v5 offset:2048\n\t" \
    "ds_read_b128 v[176:179], v5 offset:3072\n\t" \
    "ds_read_b128 v[180:183], v5 offset:4096\n\t" \
    "ds_read_b128 v[184:187], v5 offset:5120\n\t" \
    "ds_read_b128 v[188:191], v5 offset:6144\n\t" \
    "ds_read_b128 v[192:195], v5 offset:7168\n\t" \
    "s_cmp_lt_i32 s87, 16\n\t" \
    "s_cbranch_scc1 gv_fl12_%=\n\t" \
    "s_cmp_lg_u32 s66, 0\n\t" \
    "s_cbranch_scc1 gv_SF13_%=\n\t" \
    "s_lshl_b32 s69, s67, 7\n\t" \
    "s_add_u32 s69, s69, 5783552\n\t" \
    "s_add_u32 s70, s87, 0\n\t" \
    "v_mov_b32 v156, s69\n\t" \
    "v_mov_b32 v157, s70\n\t" \
    "s_mov_b64 exec, 1\n\t" \
    "global_store_dword v156, v157, s[36:37] sc1\n\t" \
    "s_mov_b64 exec, -1\n\t" \
    "gv_SF13_%=:\n\t" \
    "gv_fl12_%=:\n\t" \
    "v_xor_b32 v4, 0x2000, v4\n\t" \
    "v_xor_b32 v5, 0x2000, v5\n\t" \
    "s_waitcnt lgkmcnt(7)\n\t" \
    "v_mfma_f32_16x16x32_bf16 v[72:75], v[164:167], a[144:147], 0\n\t" \
    "s_waitcnt lgkmcnt(6)\n\t" \
    "v_mfma_f32_16x16x32_bf16 v[76:79], v[168:171], a[144:147], 0\n\t" \
    "s_waitcnt lgkmcnt(5)\n\t" \
    "v_mfma_f32_16x16x32_bf16 v[72:75], v[172:175], a[148:151], v[72:75]\n\t" \
    "s_waitcnt lgkmcnt(4)\n\t" \
    "v_mfma_f32_16x16x32_bf16 v[76:79], v[176:179], a[148:151], v[76:79]\n\t" \
    "s_waitcnt lgkmcnt(3)\n\t" \
    "v_mfma_f32_16x16x32_bf16 v[72:75], v[180:183], a[152:155], v[72:75]\n\t" \
    "s_waitcnt lgkmcnt(2)\n\t" \
    "v_mfma_f32_16x16x32_bf16 v[76:79], v[184:187], a[152:155], v[76:79]\n\t" \
    "s_waitcnt lgkmcnt(1)\n\t" \
    "v_mfma_f32_16x16x32_bf16 v[72:75], v[188:191], a[156:159], v[72:75]\n\t" \
    "s_waitcnt lgkmcnt(0)\n\t" \
    "v_mfma_f32_16x16x32_bf16 v[76:79], v[192:195], a[156:159], v[76:79]\n\t" \
    "v_mfma_f32_16x16x32_bf16 v[80:83], v[164:167], a[160:163], 0\n\t" \
    "v_mfma_f32_16x16x32_bf16 v[84:87], v[168:171], a[160:163], 0\n\t" \
    "v_mfma_f32_16x16x32_bf16 v[80:83], v[172:175], a[164:167], v[80:83]\n\t" \
    "v_mfma_f32_16x16x32_bf16 v[84:87], v[176:179], a[164:167], v[84:87]\n\t" \
    "v_mfma_f32_16x16x32_bf16 v[80:83], v[180:183], a[168:171], v[80:83]\n\t" \
    "v_mfma_f32_16x16x32_bf16 v[84:87], v[184:187], a[168:171], v[84:87]\n\t" \
    "v_cvt_pk_bf16_f32 v104, v72, v73\n\t" \
    "v_mfma_f32_16x16x32_bf16 v[80:83], v[188:191], a[172:175], v[80:83]\n\t" \
    "v_cvt_pk_bf16_f32 v105, v74, v75\n\t" \
    "v_mfma_f32_16x16x32_bf16 v[84:87], v[192:195], a[172:175], v[84:87]\n\t" \
    "v_cvt_pk_bf16_f32 v106, v76, v77\n\t" \
    "v_mfma_f32_16x16x32_bf16 v[88:91], v[164:167], a[176:179], 0\n\t" \
    "v_cvt_pk_bf16_f32 v107, v78, v79\n\t" \
    "v_mfma_f32_16x16x32_bf16 v[92:95], v[168:171], a[176:179], 0\n\t" \
    "v_xor_b32 v18, s40, v8\n\t" \
    "v_mfma_f32_16x16x32_bf16 v[88:91], v[172:175], a[180:183], v[88:91]\n\t" \
    "global_store_dwordx4 v18, v[104:107], s[38:39] sc1\n\t" \
    "v_mfma_f32_16x16x32_bf16 v[92:95], v[176:179], a[180:183], v[92:95]\n\t" \
    "v_mfma_f32_16x16x32_bf16 v[88:91], v[180:183], a[184:187], v[88:91]\n\t" \
    "v_mfma_f32_16x16x32_bf16 v[92:95], v[184:187], a[184:187], v[92:95]\n\t" \
    "v_cvt_pk_bf16_f32 v108, v80, v81\n\t" \
    "v_mfma_f32_16x16x32_bf16 v[88:91], v[188:191], a[188:191], v[88:91]\n\t" \
    "v_cvt_pk_bf16_f32 v109, v82, v83\n\t" \
    "v_mfma_f32_16x16x32_bf16 v[92:95], v[192:195], a[188:191], v[92:95]\n\t" \
    "v_cvt_pk_bf16_f32 v110, v84, v85\n\t" \
    "v_mfma_f32_16x16x32_bf16 v[72:75], v[164:167], a[192:195], 0\n\t" \
    "v_cvt_pk_bf16_f32 v111, v86, v87\n\t" \
    "v_mfma_f32_16x16x32_bf16 v[76:79], v[168:171], a[192:195], 0\n\t" \
    "v_xor_b32 v18, s40, v9\n\t" \
    "v_mfma_f32_16x16x32_bf16 v[72:75], v[172:175], a[196:199], v[72:75]\n\t" \
    "global_store_dwordx4 v18, v[108:111], s[38:39] sc1\n\t" \
    "v_mfma_f32_16x16x32_bf16 v[76:79], v[176:179], a[196:199], v[76:79]\n\t" \
    "v_mfma_f32_16x16x32_bf16 v[72:75], v[180:183], a[200:203], v[72:75]\n\t" \
    "v_mfma_f32_16x16x32_bf16 v[76:79], v[184:187], a[200:203], v[76:79]\n\t" \
    "v_cvt_pk_bf16_f32 v104, v88, v89\n\t" \
    "v_mfma_f32_16x16x32_bf16 v[72:75], v[188:191], a[204:207], v[72:75]\n\t" \
    "v_cvt_pk_bf16_f32 v105, v90, v91\n\t" \
    "v_mfma_f32_16x16x32_bf16 v[76:79], v[192:195], a[204:207], v[76:79]\n\t" \
    "v_cvt_pk_bf16_f32 v106, v92, v93\n\t" \
    "v_mfma_f32_16x16x32_bf16 v[80:83], v[164:167], a[208:211], 0\n\t" \
    "v_cvt_pk_bf16_f32 v107, v94, v95\n\t" \
    "v_mfma_f32_16x16x32_bf16 v[84:87], v[168:171], a[208:211], 0\n\t" \
    "v_xor_b32 v18, s40, v10\n\t" \
    "v_mfma_f32_16x16x32_bf16 v[80:83], v[172:175], a[212:215], v[80:83]\n\t" \
    "global_store_dwordx4 v18, v[104:107], s[38:39] sc1\n\t" \
    "v_mfma_f32_16x16x32_bf16 v[84:87], v[176:179], a[212:215], v[84:87]\n\t" \
    "v_mfma_f32_16x16x32_bf16 v[80:83], v[180:183], a[216:219], v[80:83]\n\t" \
    "v_mfma_f32_16x16x32_bf16 v[84:87], v[184:187], a[216:219], v[84:87]\n\t" \
    "v_cvt_pk_bf16_f32 v108, v72, v73\n\t" \
    "v_mfma_f32_16x16x32_bf16 v[80:83], v[188:191], a[220:223], v[80:83]\n\t" \
    "v_cvt_pk_bf16_f32 v109, v74, v75\n\t" \
    "v_mfma_f32_16x16x32_bf16 v[84:87], v[192:195], a[220:223], v[84:87]\n\t" \
    "v_cvt_pk_bf16_f32 v110, v76, v77\n\t" \
    "v_mfma_f32_16x16x32_bf16 v[88:91], v[164:167], a[224:227], 0\n\t" \
    "v_cvt_pk_bf16_f32 v111, v78, v79\n\t" \
    "v_mfma_f32_16x16x32_bf16 v[92:95], v[168:171], a[224:227], 0\n\t" \
    "v_xor_b32 v18, s40, v11\n\t" \
    "v_mfma_f32_16x16x32_bf16 v[88:91], v[172:175], a[228:231], v[88:91]\n\t" \
    "global_store_dwordx4 v18, v[108:111], s[38:39] sc1\n\t" \
    "v_mfma_f32_16x16x32_bf16 v[92:95], v[176:179], a[228:231], v[92:95]\n\t" \
    "v_mfma_f32_16x16x32_bf16 v[88:91], v[180:183], a[232:235], v[88:91]\n\t" \
    "v_mfma_f32_16x16x32_bf16 v[92:95], v[184:187], a[232:235], v[92:95]\n\t" \
    "v_cvt_pk_bf16_f32 v104, v80, v81\n\t" \
    "v_mfma_f32_16x16x32_bf16 v[88:91], v[188:191], a[236:239], v[88:91]\n\t" \
    "v_cvt_pk_bf16_f32 v105, v82, v83\n\t" \
    "v_mfma_f32_16x16x32_bf16 v[92:95], v[192:195], a[236:239], v[92:95]\n\t" \
    "v_cvt_pk_bf16_f32 v106, v84, v85\n\t" \
    "v_mfma_f32_16x16x32_bf16 v[72:75], v[164:167], a[240:243], 0\n\t" \
    "v_cvt_pk_bf16_f32 v107, v86, v87\n\t" \
    "v_mfma_f32_16x16x32_bf16 v[76:79], v[168:171], a[240:243], 0\n\t" \
    "v_xor_b32 v18, s40, v12\n\t" \
    "v_mfma_f32_16x16x32_bf16 v[72:75], v[172:175], a[244:247], v[72:75]\n\t" \
    "global_store_dwordx4 v18, v[104:107], s[38:39] sc1\n\t" \
    "v_mfma_f32_16x16x32_bf16 v[76:79], v[176:179], a[244:247], v[76:79]\n\t" \
    "v_mfma_f32_16x16x32_bf16 v[72:75], v[180:183], a[248:251], v[72:75]\n\t" \
    "v_mfma_f32_16x16x32_bf16 v[76:79], v[184:187], a[248:251], v[76:79]\n\t" \
    "v_cvt_pk_bf16_f32 v108, v88, v89\n\t" \
    "v_mfma_f32_16x16x32_bf16 v[72:75], v[188:191], a[252:255], v[72:75]\n\t" \
    "v_cvt_pk_bf16_f32 v109, v90, v91\n\t" \
    "v_mfma_f32_16x16x32_bf16 v[76:79], v[192:195], a[252:255], v[76:79]\n\t" \
    "v_cvt_pk_bf16_f32 v110, v92, v93\n\t" \
    "v_mfma_f32_16x16x32_bf16 v[80:83], v[164:167], v[224:227], 0\n\t" \
    "v_cvt_pk_bf16_f32 v111, v94, v95\n\t" \
    "v_mfma_f32_16x16x32_bf16 v[84:87], v[168:171], v[224:227], 0\n\t" \
    "v_xor_b32 v18, s40, v13\n\t" \
    "v_mfma_f32_16x16x32_bf16 v[80:83], v[172:175], v[228:231], v[80:83]\n\t" \
    "global_store_dwordx4 v18, v[108:111], s[38:39] sc1\n\t" \
    "v_mfma_f32_16x16x32_bf16 v[84:87], v[176:179], v[228:231], v[84:87]\n\t" \
    "v_mfma_f32_16x16x32_bf16 v[80:83], v[180:183], v[232:235], v[80:83]\n\t" \
    "v_mfma_f32_16x16x32_bf16 v[84:87], v[184:187], v[232:235], v[84:87]\n\t" \
    "v_cvt_pk_bf16_f32 v104, v72, v73\n\t" \
    "v_mfma_f32_16x16x32_bf16 v[80:83], v[188:191], v[236:239], v[80:83]\n\t" \
    "v_cvt_pk_bf16_f32 v105, v74, v75\n\t" \
    "v_mfma_f32_16x16x32_bf16 v[84:87], v[192:195], v[236:239], v[84:87]\n\t" \
    "v_cvt_pk_bf16_f32 v106, v76, v77\n\t" \
    "v_mfma_f32_16x16x32_bf16 v[88:91], v[164:167], v[240:243], 0\n\t" \
    "v_cvt_pk_bf16_f32 v107, v78, v79\n\t" \
    "ds_read_b128 v[24:27], v22 offset:0\n\t" \
    "v_mfma_f32_16x16x32_bf16 v[92:95], v[168:171], v[240:243], 0\n\t" \
    "v_xor_b32 v18, s40, v14\n\t" \
    "ds_read_b128 v[28:31], v22 offset:18432\n\t" \
    "v_mfma_f32_16x16x32_bf16 v[88:91], v[172:175], v[244:247], v[88:91]\n\t" \
    "global_store_dwordx4 v18, v[104:107], s[38:39] sc1\n\t" \
    "ds_read_b128 v[32:35], v23 offset:0\n\t" \
    "v_mfma_f32_16x16x32_bf16 v[92:95], v[176:179], v[244:247], v[92:95]\n\t" \
    "ds_read_b128 v[36:39], v23 offset:18432\n\t" \
    "v_mfma_f32_16x16x32_bf16 v[88:91], v[180:183], v[248:251], v[88:91]\n\t" \
    "ds_read_b128 v[40:43], v22 offset:128\n\t" \
    "v_mfma_f32_16x16x32_bf16 v[92:95], v[184:187], v[248:251], v[92:95]\n\t" \
    "v_cvt_pk_bf16_f32 v108, v80, v81\n\t" \
    "ds_read_b128 v[44:47], v22 offset:18560\n\t" \
    "v_mfma_f32_16x16x32_bf16 v[88:91], v[188:191], v[252:255], v[88:91]\n\t" \
    "v_cvt_pk_bf16_f32 v109, v82, v83\n\t" \
    "v_mfma_f32_16x16x32_bf16 v[92:95], v[192:195], v[252:255], v[92:95]\n\t" \
    "v_cvt_pk_bf16_f32 v110, v84, v85\n\t" \
    "v_cvt_pk_bf16_f32 v111, v86, v87\n\t" \
    "v_xor_b32 v18, s40, v15\n\t" \
    "global_store_dwordx4 v18, v[108:111], s[38:39] sc1\n\t" \
    "s_nop 1\n\t" \
    "v_cvt_pk_bf16_f32 v104, v88, v89\n\t" \
    "v_cvt_pk_bf16_f32 v105, v90, v91\n\t" \
    "v_cvt_pk_bf16_f32 v106, v92, v93\n\t" \
    "v_cvt_pk_bf16_f32 v107, v94, v95\n\t" \
    "v_xor_b32 v18, s40, v16\n\t" \
    "global_store_dwordx4 v18, v[104:107], s[38:39] sc1\n\t" \
    "s_cmp_ge_u32 s65, 16\n\t" \
    "s_cbranch_scc1 gv_nofast9_%=\n\t" \
    "s_waitcnt vmcnt(0)\n\t" \
    "s_barrier\n\t" \
    "s_cmp_lg_u32 s66, 0\n\t" \
    "s_cbranch_scc1 gv_SF14_%=\n\t" \
    "s_lshl_b32 s69, s67, 7\n\t" \
    "s_add_u32 s69, s69, 5783552\n\t" \
    "s_add_u32 s70, s65, 0\n\t" \
    "v_mov_b32 v156, s69\n\t" \
    "v_mov_b32 v157, s70\n\t" \
    "s_mov_b64 exec, 1\n\t" \
    "global_store_dword v156, v157, s[36:37] sc1\n\t" \
    "s_mov_b64 exec, -1\n\t" \
    "gv_SF14_%=:\n\t" \
    "s_xor_b32 s69, s66, 1\n\t" \
    "s_lshr_b32 s70, s65, 1\n\t" \
    "s_or_b32 s69, s69, s70\n\t" \
    "s_cbranch_scc1 gv_nofast9_%=\n\t" \
    "v_mov_b32 v156, 5838848\n\t" \
    "v_mov_b32 v157, 1\n\t" \
    "s_mov_b64 exec, 1\n\t" \
    "global_atomic_add v158, v156, v157, s[36:37] sc0 sc1\n\t" \
    "s_mov_b64 exec, -1\n\t" \
    "v_mbcnt_lo_u32_b32 v156, -1, 0\n\t" \
    "v_and_b32 v157, 3, v156\n\t" \
    "v_lshlrev_b32 v157, 16, v157\n\t" \
    "v_bfe_u32 v159, v156, 2, 2\n\t" \
    "v_lshl_add_u32 v157, v159, 11, v157\n\t" \
    "v_bfe_u32 v159, v156, 4, 1\n\t" \
    "v_lshl_add_u32 v157, v159, 14, v157\n\t" \
    "s_bitcmp1_b32 s44, 2\n\t" \
    "s_cselect_b32 s63, 8192, 0\n\t" \
    "s_or_b32 s63, s63, 0x200000\n\t" \
    "v_add_u32 v157, s63, v157\n\t" \
    "s_waitcnt vmcnt(0)\n\t" \
    "v_readfirstlane_b32 s69, v158\n\t" \
    "s_nop 1\n\t" \
    "s_cmp_lg_u32 s69, 126\n\t" \
    "s_cbranch_scc1 gv_go15_%=\n\t" \
    "s_memrealtime s[56:57]\n\t" \
    "s_waitcnt lgkmcnt(0)\n\t" \
    "s_lshl_b32 s69, s56, 4\n\t" \
    "s_add_u32 s69, s69, 2480\n\t" \
    "s_add_u32 s70, s56, 24\n\t" \
    "s_lshr_b32 s70, s70, 4\n\t" \
    "v_lshrrev_b32 v159, 2, v156\n\t" \
    "v_add_u32 v159, s70, v159\n\t" \
    "v_and_b32 v159, 63, v159\n\t" \
    "v_lshlrev_b32 v159, 6, v159\n\t" \
    "v_and_b32 v158, 3, v156\n\t" \
    "v_lshl_add_u32 v159, v158, 12, v159\n\t" \
    "v_add_u32 v159, 5865472, v159\n\t" \
    "v_mov_b32 v156, s69\n\t" \
    "s_mov_b32 exec_hi, 0\n\t" \
    "global_store_dword v159, v156, s[36:37] sc1\n\t" \
    "global_store_dword v157, v156, s[36:37] sc1\n\t" \
    "s_mov_b32 exec_hi, -1\n\t" \
    "gv_go15_%=:\n\t" \
    "gv_nofast9_%=:\n\t" \
    "s_mov_b32 s87, s86\n\t" \
    "s_mov_b32 s86, s65\n\t" \
    "s_mov_b32 s65, s84\n\t" \
    "s_mov_b32 s84, s85\n\t" \
    "s_cmp_lt_u32 s84, 24\n\t" \
    "s_addc_u32 s85, s84, 1\n\t" \
    "s_cmp_lt_u32 s65, s88\n\t" \
    "s_cbranch_scc1 gv_loop8_%=\n\t" \
    "s_waitcnt vmcnt(0) lgkmcnt(0)\n\t" \
    "s_barrier\n\t" \
    "s_mov_b32 s65, 99\n\t" \
    "s_cmp_lg_u32 s66, 0\n\t" \
    "s_cbranch_scc1 gv_SF16_%=\n\t" \
    "s_lshl_b32 s69, s67, 7\n\t" \
    "s_add_u32 s69, s69, 5783552\n\t" \
    "s_add_u32 s70, s65, 0\n\t" \
    "v_mov_b32 v156, s69\n\t" \
    "v_mov_b32 v157, s70\n\t" \
    "s_mov_b64 exec, 1\n\t" \
    "global_store_dword v156, v157, s[36:37] sc1\n\t" \
    "s_mov_b64 exec, -1\n\t" \
    "gv_SF16_%=:\n\t" \
    "s_branch gv_end17_%=\n\t" \
    "gv_rest10_%=:\n\t" \
    "s_waitcnt vmcnt(12) lgkmcnt(0)\n\t" \
    "s_barrier\n\t" \
    "ds_read_b64_tr_b16 a[156:157], v212 offset:0\n\t" \
    "ds_read_b64_tr_b16 a[158:159], v212 offset:18432\n\t" \
    "ds_read_b64_tr_b16 a[172:173], v213 offset:0\n\t" \
    "ds_read_b64_tr_b16 a[174:175], v213 offset:18432\n\t" \
    "ds_read_b64_tr_b16 a[188:189], v214 offset:0\n\t" \
    "ds_read_b64_tr_b16 a[190:191], v214 offset:18432\n\t" \
    "s_waitcnt lgkmcnt(6)\n\t" \
    "ds_read_b64_tr_b16 a[204:205], v215 offset:0\n\t" \
    "ds_read_b64_tr_b16 a[206:207], v215 offset:18432\n\t" \
    "ds_read_b64_tr_b16 a[220:221], v212 offset:128\n\t" \
    "ds_read_b64_tr_b16 a[222:223], v212 offset:18560\n\t" \
    "ds_read_b64_tr_b16 a[236:237], v213 offset:128\n\t" \
    "ds_read_b64_tr_b16 a[238:239], v213 offset:18560\n\t" \
    "s_waitcnt lgkmcnt(6)\n\t" \
    "ds_read_b64_tr_b16 a[252:253], v214 offset:128\n\t" \
    "ds_read_b64_tr_b16 a[254:255], v214 offset:18560\n\t" \
    "ds_read_b64_tr_b16 v[236:237], v215 offset:128\n\t" \
    "ds_read_b64_tr_b16 v[238:239], v215 offset:18560\n\t" \
    "ds_read_b64_tr_b16 v[252:253], v212 offset:256\n\t" \
    "ds_read_b64_tr_b16 v[254:255], v212 offset:18688\n\t" \
    "s_waitcnt lgkmcnt(6)\n\t" \
    "v_add_u32 v212, 0x9000, v212\n\t" \
    "v_add_u32 v213, 0x9000, v213\n\t" \
    "v_add_u32 v214, 0x9000, v214\n\t" \
    "v_add_u32 v215, 0x9000, v215\n\t" \
    "s_branch gv_back11_%=\n\t" \
    "gv_end17_%=:\n\t" \
    "gv_last_%=:\n\t" \
    ""
#define ASM_CV \
    "v_mov_b32 v0, %0\n\t" \
    "v_mov_b32 v2, %2\n\t" \
    "v_subrev_u32 v1, 768, %1\n\t" \
    "v_mov_b32 v4, 0\n\t" \
    "v_mov_b32 v5, 0\n\t" \
    "v_mov_b32 v6, 0\n\t" \
    "v_mov_b32 v7, 0\n\t" \
    "v_mov_b32 v8, 0\n\t" \
    "v_mov_b32 v9, 0\n\t" \
    "v_mov_b32 v10, 0\n\t" \
    "v_mov_b32 v11, 0\n\t" \
    "v_mov_b32 v12, 0\n\t" \
    "v_mov_b32 v13, 0\n\t" \
    "v_mov_b32 v14, 0\n\t" \
    "v_mov_b32 v15, 0\n\t" \
    "v_mov_b32 v16, 0\n\t" \
    "v_mov_b32 v17, 0\n\t" \
    "v_mov_b32 v18, 0\n\t" \
    "v_mov_b32 v19, 0\n\t" \
    "v_mov_b32 v20, 0\n\t" \
    "v_mov_b32 v21, 0\n\t" \
    "v_mov_b32 v22, 0\n\t" \
    "v_mov_b32 v23, 0\n\t" \
    "v_mov_b32 v24, 0\n\t" \
    "v_mov_b32 v25, 0\n\t" \
    "v_mov_b32 v26, 0\n\t" \
    "v_mov_b32 v27, 0\n\t" \
    "v_mov_b32 v28, 0\n\t" \
    "v_mov_b32 v29, 0\n\t" \
    "v_mov_b32 v30, 0\n\t" \
    "v_mov_b32 v31, 0\n\t" \
    "v_mov_b32 v32, 0\n\t" \
    "v_mov_b32 v33, 0\n\t" \
    "v_mov_b32 v34, 0\n\t" \
    "v_mov_b32 v35, 0\n\t" \
    "s_waitcnt lgkmcnt(0)\n\t" \
    "ds_read_b64 v[36:37], v0 offset:0\n\t" \
    "ds_read_b64 v[38:39], v0 offset:8\n\t" \
    "ds_read_b64 v[40:41], v0 offset:32\n\t" \
    "ds_read_b64 v[42:43], v0 offset:40\n\t" \
    "ds_read_b128 v[44:47], v1 offset:768\n\t" \
    "ds_read_b128 v[48:51], v1 offset:2304\n\t" \
    "ds_read_b128 v[52:55], v1 offset:3840\n\t" \
    "ds_read_b128 v[56:59], v1 offset:5376\n\t" \
    "ds_read_b128 v[60:63], v1 offset:8448\n\t" \
    "ds_read_b128 v[64:67], v1 offset:9984\n\t" \
    "ds_read_b128 v[68:71], v1 offset:11520\n\t" \
    "ds_read_b128 v[72:75], v1 offset:13056\n\t" \
    "s_mov_b32 s69, 8\n\t" \
    "cv_c1_%=:\n\t" \
    "s_waitcnt lgkmcnt(7)\n\t" \
    "v_mfma_f32_16x16x32_bf16 v[4:7], v[36:39], v[44:47], v[4:7]\n\t" \
    "ds_read_b64 v[76:77], v0 offset:64\n\t" \
    "ds_read_b64 v[78:79], v0 offset:72\n\t" \
    "v_mfma_f32_16x16x32_bf16 v[20:23], v[40:43], v[44:47], v[20:23]\n\t" \
    "ds_read_b64 v[80:81], v0 offset:96\n\t" \
    "ds_read_b64 v[82:83], v0 offset:104\n\t" \
    "s_waitcnt lgkmcnt(10)\n\t" \
    "v_mfma_f32_16x16x32_bf16 v[8:11], v[36:39], v[48:51], v[8:11]\n\t" \
    "ds_read_b128 v[84:87], v1 offset:672\n\t" \
    "ds_read_b128 v[88:91], v1 offset:2208\n\t" \
    "v_mfma_f32_16x16x32_bf16 v[24:27], v[40:43], v[48:51], v[24:27]\n\t" \
    "ds_read_b128 v[92:95], v1 offset:3744\n\t" \
    "ds_read_b128 v[96:99], v1 offset:5280\n\t" \
    "s_waitcnt lgkmcnt(13)\n\t" \
    "v_mfma_f32_16x16x32_bf16 v[12:15], v[36:39], v[52:55], v[12:15]\n\t" \
    "ds_read_b128 v[100:103], v1 offset:8352\n\t" \
    "ds_read_b128 v[104:107], v1 offset:9888\n\t" \
    "v_mfma_f32_16x16x32_bf16 v[28:31], v[40:43], v[52:55], v[28:31]\n\t" \
    "s_waitcnt lgkmcnt(14)\n\t" \
    "v_mfma_f32_16x16x32_bf16 v[16:19], v[36:39], v[56:59], v[16:19]\n\t" \
    "ds_read_b128 v[108:111], v1 offset:11424\n\t" \
    "v_mfma_f32_16x16x32_bf16 v[32:35], v[40:43], v[56:59], v[32:35]\n\t" \
    "s_waitcnt lgkmcnt(14)\n\t" \
    "v_mfma_f32_16x16x32_bf16 v[4:7], v[36:39], v[60:63], v[4:7]\n\t" \
    "ds_read_b128 v[112:115], v1 offset:12960\n\t" \
    "v_mfma_f32_16x16x32_bf16 v[20:23], v[40:43], v[60:63], v[20:23]\n\t" \
    "s_waitcnt lgkmcnt(14)\n\t" \
    "v_mfma_f32_16x16x32_bf16 v[8:11], v[36:39], v[64:67], v[8:11]\n\t" \
    "v_mfma_f32_16x16x32_bf16 v[24:27], v[40:43], v[64:67], v[24:27]\n\t" \
    "s_waitcnt lgkmcnt(13)\n\t" \
    "v_mfma_f32_16x16x32_bf16 v[12:15], v[36:39], v[68:71], v[12:15]\n\t" \
    "v_mfma_f32_16x16x32_bf16 v[28:31], v[40:43], v[68:71], v[28:31]\n\t" \
    "s_waitcnt lgkmcnt(12)\n\t" \
    "v_mfma_f32_16x16x32_bf16 v[16:19], v[36:39], v[72:75], v[16:19]\n\t" \
    "v_mfma_f32_16x16x32_bf16 v[32:35], v[40:43], v[72:75], v[32:35]\n\t" \
    "s_waitcnt lgkmcnt(7)\n\t" \
    "v_mfma_f32_16x16x32_bf16 v[4:7], v[76:79], v[84:87], v[4:7]\n\t" \
    "ds_read_b64 v[36:37], v0 offset:128\n\t" \
    "ds_read_b64 v[38:39], v0 offset:136\n\t" \
    "v_mfma_f32_16x16x32_bf16 v[20:23], v[80:83], v[84:87], v[20:23]\n\t" \
    "ds_read_b64 v[40:41], v0 offset:160\n\t" \
    "ds_read_b64 v[42:43], v0 offset:168\n\t" \
    "s_waitcnt lgkmcnt(10)\n\t" \
    "v_mfma_f32_16x16x32_bf16 v[8:11], v[76:79], v[88:91], v[8:11]\n\t" \
    "ds_read_b128 v[44:47], v1 offset:576\n\t" \
    "ds_read_b128 v[48:51], v1 offset:2112\n\t" \
    "v_mfma_f32_16x16x32_bf16 v[24:27], v[80:83], v[88:91], v[24:27]\n\t" \
    "ds_read_b128 v[52:55], v1 offset:3648\n\t" \
    "ds_read_b128 v[56:59], v1 offset:5184\n\t" \
    "s_waitcnt lgkmcnt(13)\n\t" \
    "v_mfma_f32_16x16x32_bf16 v[12:15], v[76:79], v[92:95], v[12:15]\n\t" \
    "ds_read_b128 v[60:63], v1 offset:8256\n\t" \
    "ds_read_b128 v[64:67], v1 offset:9792\n\t" \
    "v_mfma_f32_16x16x32_bf16 v[28:31], v[80:83], v[92:95], v[28:31]\n\t" \
    "s_waitcnt lgkmcnt(14)\n\t" \
    "v_mfma_f32_16x16x32_bf16 v[16:19], v[76:79], v[96:99], v[16:19]\n\t" \
    "ds_read_b128 v[68:71], v1 offset:11328\n\t" \
    "v_mfma_f32_16x16x32_bf16 v[32:35], v[80:83], v[96:99], v[32:35]\n\t" \
    "s_waitcnt lgkmcnt(14)\n\t" \
    "v_mfma_f32_16x16x32_bf16 v[4:7], v[76:79], v[100:103], v[4:7]\n\t" \
    "ds_read_b128 v[72:75], v1 offset:12864\n\t" \
    "v_mfma_f32_16x16x32_bf16 v[20:23], v[80:83], v[100:103], v[20:23]\n\t" \
    "s_waitcnt lgkmcnt(14)\n\t" \
    "v_mfma_f32_16x16x32_bf16 v[8:11], v[76:79], v[104:107], v[8:11]\n\t" \
    "v_mfma_f32_16x16x32_bf16 v[24:27], v[80:83], v[104:107], v[24:27]\n\t" \
    "s_waitcnt lgkmcnt(13)\n\t" \
    "v_mfma_f32_16x16x32_bf16 v[12:15], v[76:79], v[108:111], v[12:15]\n\t" \
    "v_mfma_f32_16x16x32_bf16 v[28:31], v[80:83], v[108:111], v[28:31]\n\t" \
    "s_waitcnt lgkmcnt(12)\n\t" \
    "v_mfma_f32_16x16x32_bf16 v[16:19], v[76:79], v[112:115], v[16:19]\n\t" \
    "v_mfma_f32_16x16x32_bf16 v[32:35], v[80:83], v[112:115], v[32:35]\n\t" \
    "v_add_u32 v0, 128, v0\n\t" \
    "v_subrev_u32 v1, 192, v1\n\t" \
    "s_sub_u32 s69, s69, 1\n\t" \
    "s_cmp_lg_u32 s69, 0\n\t" \
    "s_cbranch_scc1 cv_c1_%=\n\t" \
    "s_waitcnt lgkmcnt(0)\n\t" \
    "s_mov_b32 s69, 8\n\t" \
    "cv_c2_%=:\n\t" \
    "s_waitcnt lgkmcnt(5)\n\t" \
    "v_mfma_f32_16x16x32_bf16 v[8:11], v[36:39], v[48:51], v[8:11]\n\t" \
    "ds_read_b64 v[76:77], v0 offset:64\n\t" \
    "ds_read_b64 v[78:79], v0 offset:72\n\t" \
    "v_mfma_f32_16x16x32_bf16 v[24:27], v[40:43], v[48:51], v[24:27]\n\t" \
    "ds_read_b64 v[80:81], v0 offset:96\n\t" \
    "ds_read_b64 v[82:83], v0 offset:104\n\t" \
    "s_waitcnt lgkmcnt(8)\n\t" \
    "v_mfma_f32_16x16x32_bf16 v[12:15], v[36:39], v[52:55], v[12:15]\n\t" \
    "ds_read_b128 v[88:91], v1 offset:2208\n\t" \
    "ds_read_b128 v[92:95], v1 offset:3744\n\t" \
    "v_mfma_f32_16x16x32_bf16 v[28:31], v[40:43], v[52:55], v[28:31]\n\t" \
    "ds_read_b128 v[96:99], v1 offset:5280\n\t" \
    "ds_read_b128 v[104:107], v1 offset:9888\n\t" \
    "s_waitcnt lgkmcnt(11)\n\t" \
    "v_mfma_f32_16x16x32_bf16 v[16:19], v[36:39], v[56:59], v[16:19]\n\t" \
    "ds_read_b128 v[108:111], v1 offset:11424\n\t" \
    "ds_read_b128 v[112:115], v1 offset:12960\n\t" \
    "v_mfma_f32_16x16x32_bf16 v[32:35], v[40:43], v[56:59], v[32:35]\n\t" \
    "s_waitcnt lgkmcnt(12)\n\t" \
    "v_mfma_f32_16x16x32_bf16 v[8:11], v[36:39], v[64:67], v[8:11]\n\t" \
    "v_mfma_f32_16x16x32_bf16 v[24:27], v[40:43], v[64:67], v[24:27]\n\t" \
    "s_waitcnt lgkmcnt(11)\n\t" \
    "v_mfma_f32_16x16x32_bf16 v[12:15], v[36:39], v[68:71], v[12:15]\n\t" \
    "v_mfma_f32_16x16x32_bf16 v[28:31], v[40:43], v[68:71], v[28:31]\n\t" \
    "s_waitcnt lgkmcnt(10)\n\t" \
    "v_mfma_f32_16x16x32_bf16 v[16:19], v[36:39], v[72:75], v[16:19]\n\t" \
    "v_mfma_f32_16x16x32_bf16 v[32:35], v[40:43], v[72:75], v[32:35]\n\t" \
    "s_waitcnt lgkmcnt(5)\n\t" \
    "v_mfma_f32_16x16x32_bf16 v[8:11], v[76:79], v[88:91], v[8:11]\n\t" \
    "ds_read_b64 v[36:37], v0 offset:128\n\t" \
    "ds_read_b64 v[38:39], v0 offset:136\n\t" \
    "v_mfma_f32_16x16x32_bf16 v[24:27], v[80:83], v[88:91], v[24:27]\n\t" \
    "ds_read_b64 v[40:41], v0 offset:160\n\t" \
    "ds_read_b64 v[42:43], v0 offset:168\n\t" \
    "s_waitcnt lgkmcnt(8)\n\t" \
    "v_mfma_f32_16x16x32_bf16 v[12:15], v[76:79], v[92:95], v[12:15]\n\t" \
    "ds_read_b128 v[48:51], v1 offset:2112\n\t" \
    "ds_read_b128 v[52:55], v1 offset:3648\n\t" \
    "v_mfma_f32_16x16x32_bf16 v[28:31], v[80:83], v[92:95], v[28:31]\n\t" \
    "ds_read_b128 v[56:59], v1 offset:5184\n\t" \
    "ds_read_b128 v[64:67], v1 offset:9792\n\t" \
    "s_waitcnt lgkmcnt(11)\n\t" \
    "v_mfma_f32_16x16x32_bf16 v[16:19], v[76:79], v[96:99], v[16:19]\n\t" \
    "ds_read_b128 v[68:71], v1 offset:11328\n\t" \
    "ds_read_b128 v[72:75], v1 offset:12864\n\t" \
    "v_mfma_f32_16x16x32_bf16 v[32:35], v[80:83], v[96:99], v[32:35]\n\t" \
    "s_waitcnt lgkmcnt(12)\n\t" \
    "v_mfma_f32_16x16x32_bf16 v[8:11], v[76:79], v[104:107], v[8:11]\n\t" \
    "v_mfma_f32_16x16x32_bf16 v[24:27], v[80:83], v[104:107], v[24:27]\n\t" \
    "s_waitcnt lgkmcnt(11)\n\t" \
    "v_mfma_f32_16x16x32_bf16 v[12:15], v[76:79], v[108:111], v[12:15]\n\t" \
    "v_mfma_f32_16x16x32_bf16 v[28:31], v[80:83], v[108:111], v[28:31]\n\t" \
    "s_waitcnt lgkmcnt(10)\n\t" \
    "v_mfma_f32_16x16x32_bf16 v[16:19], v[76:79], v[112:115], v[16:19]\n\t" \
    "v_mfma_f32_16x16x32_bf16 v[32:35], v[80:83], v[112:115], v[32:35]\n\t" \
    "v_add_u32 v0, 128, v0\n\t" \
    "v_subrev_u32 v1, 192, v1\n\t" \
    "s_sub_u32 s69, s69, 1\n\t" \
    "s_cmp_lg_u32 s69, 0\n\t" \
    "s_cbranch_scc1 cv_c2_%=\n\t" \
    "s_waitcnt lgkmcnt(0)\n\t" \
    "s_mov_b32 s69, 8\n\t" \
    "cv_c3_%=:\n\t" \
    "s_waitcnt lgkmcnt(3)\n\t" \
    "v_mfma_f32_16x16x32_bf16 v[12:15], v[36:39], v[52:55], v[12:15]\n\t" \
    "ds_read_b64 v[76:77], v0 offset:64\n\t" \
    "ds_read_b64 v[78:79], v0 offset:72\n\t" \
    "v_mfma_f32_16x16x32_bf16 v[28:31], v[40:43], v[52:55], v[28:31]\n\t" \
    "ds_read_b64 v[80:81], v0 offset:96\n\t" \
    "ds_read_b64 v[82:83], v0 offset:104\n\t" \
    "s_waitcnt lgkmcnt(6)\n\t" \
    "v_mfma_f32_16x16x32_bf16 v[16:19], v[36:39], v[56:59], v[16:19]\n\t" \
    "ds_read_b128 v[92:95], v1 offset:3744\n\t" \
    "ds_read_b128 v[96:99], v1 offset:5280\n\t" \
    "v_mfma_f32_16x16x32_bf16 v[32:35], v[40:43], v[56:59], v[32:35]\n\t" \
    "ds_read_b128 v[108:111], v1 offset:11424\n\t" \
    "ds_read_b128 v[112:115], v1 offset:12960\n\t" \
    "s_waitcnt lgkmcnt(9)\n\t" \
    "v_mfma_f32_16x16x32_bf16 v[12:15], v[36:39], v[68:71], v[12:15]\n\t" \
    "v_mfma_f32_16x16x32_bf16 v[28:31], v[40:43], v[68:71], v[28:31]\n\t" \
    "s_waitcnt lgkmcnt(8)\n\t" \
    "v_mfma_f32_16x16x32_bf16 v[16:19], v[36:39], v[72:75], v[16:19]\n\t" \
    "v_mfma_f32_16x16x32_bf16 v[32:35], v[40:43], v[72:75], v[32:35]\n\t" \
    "s_waitcnt lgkmcnt(3)\n\t" \
    "v_mfma_f32_16x16x32_bf16 v[12:15], v[76:79], v[92:95], v[12:15]\n\t" \
    "ds_read_b64 v[36:37], v0 offset:128\n\t" \
    "ds_read_b64 v[38:39], v0 offset:136\n\t" \
    "v_mfma_f32_16x16x32_bf16 v[28:31], v[80:83], v[92:95], v[28:31]\n\t" \
    "ds_read_b64 v[40:41], v0 offset:160\n\t" \
    "ds_read_b64 v[42:43], v0 offset:168\n\t" \
    "s_waitcnt lgkmcnt(6)\n\t" \
    "v_mfma_f32_16x16x32_bf16 v[16:19], v[76:79], v[96:99], v[16:19]\n\t" \
    "ds_read_b128 v[52:55], v1 offset:3648\n\t" \
    "ds_read_b128 v[56:59], v1 offset:5184\n\t" \
    "v_mfma_f32_16x16x32_bf16 v[32:35], v[80:83], v[96:99], v[32:35]\n\t" \
    "ds_read_b128 v[68:71], v1 offset:11328\n\t" \
    "ds_read_b128 v[72:75], v1 offset:12864\n\t" \
    "s_waitcnt lgkmcnt(9)\n\t" \
    "v_mfma_f32_16x16x32_bf16 v[12:15], v[76:79], v[108:111], v[12:15]\n\t" \
    "v_mfma_f32_16x16x32_bf16 v[28:31], v[80:83], v[108:111], v[28:31]\n\t" \
    "s_waitcnt lgkmcnt(8)\n\t" \
    "v_mfma_f32_16x16x32_bf16 v[16:19], v[76:79], v[112:115], v[16:19]\n\t" \
    "v_mfma_f32_16x16x32_bf16 v[32:35], v[80:83], v[112:115], v[32:35]\n\t" \
    "v_add_u32 v0, 128, v0\n\t" \
    "v_subrev_u32 v1, 192, v1\n\t" \
    "s_sub_u32 s69, s69, 1\n\t" \
    "s_cmp_lg_u32 s69, 0\n\t" \
    "s_cbranch_scc1 cv_c3_%=\n\t" \
    "s_waitcnt lgkmcnt(0)\n\t" \
    "ds_read_b64 v[36:37], v0 offset:0\n\t" \
    "ds_read_b64 v[38:39], v0 offset:8\n\t" \
    "ds_read_b64 v[40:41], v0 offset:32\n\t" \
    "ds_read_b64 v[42:43], v0 offset:40\n\t" \
    "ds_read_b128 v[56:59], v1 offset:5376\n\t" \
    "ds_read_b128 v[72:75], v1 offset:13056\n\t" \
    "ds_read_b64 v[76:77], v0 offset:64\n\t" \
    "ds_read_b64 v[78:79], v0 offset:72\n\t" \
    "ds_read_b64 v[80:81], v0 offset:96\n\t" \
    "ds_read_b64 v[82:83], v0 offset:104\n\t" \
    "ds_read_b128 v[96:99], v1 offset:5280\n\t" \
    "ds_read_b128 v[112:115], v1 offset:12960\n\t" \
    "s_mov_b32 s69, 4\n\t" \
    "cv_c4_%=:\n\t" \
    "s_waitcnt lgkmcnt(7)\n\t" \
    "v_mfma_f32_16x16x32_bf16 v[16:19], v[36:39], v[56:59], v[16:19]\n\t" \
    "ds_read_b64 v[116:117], v0 offset:128\n\t" \
    "ds_read_b64 v[118:119], v0 offset:136\n\t" \
    "v_mfma_f32_16x16x32_bf16 v[32:35], v[40:43], v[56:59], v[32:35]\n\t" \
    "ds_read_b64 v[120:121], v0 offset:160\n\t" \
    "ds_read_b64 v[122:123], v0 offset:168\n\t" \
    "s_waitcnt lgkmcnt(10)\n\t" \
    "v_mfma_f32_16x16x32_bf16 v[16:19], v[36:39], v[72:75], v[16:19]\n\t" \
    "ds_read_b128 v[136:139], v1 offset:5184\n\t" \
    "ds_read_b128 v[152:155], v1 offset:12864\n\t" \
    "v_mfma_f32_16x16x32_bf16 v[32:35], v[40:43], v[72:75], v[32:35]\n\t" \
    "ds_read_b64 v[156:157], v0 offset:192\n\t" \
    "ds_read_b64 v[158:159], v0 offset:200\n\t" \
    "s_waitcnt lgkmcnt(9)\n\t" \
    "v_mfma_f32_16x16x32_bf16 v[16:19], v[76:79], v[96:99], v[16:19]\n\t" \
    "ds_read_b64 v[160:161], v0 offset:224\n\t" \
    "ds_read_b64 v[162:163], v0 offset:232\n\t" \
    "v_mfma_f32_16x16x32_bf16 v[32:35], v[80:83], v[96:99], v[32:35]\n\t" \
    "ds_read_b128 v[176:179], v1 offset:5088\n\t" \
    "ds_read_b128 v[192:195], v1 offset:12768\n\t" \
    "s_waitcnt lgkmcnt(12)\n\t" \
    "v_mfma_f32_16x16x32_bf16 v[16:19], v[76:79], v[112:115], v[16:19]\n\t" \
    "v_mfma_f32_16x16x32_bf16 v[32:35], v[80:83], v[112:115], v[32:35]\n\t" \
    "s_waitcnt lgkmcnt(7)\n\t" \
    "v_mfma_f32_16x16x32_bf16 v[16:19], v[116:119], v[136:139], v[16:19]\n\t" \
    "ds_read_b64 v[36:37], v0 offset:256\n\t" \
    "ds_read_b64 v[38:39], v0 offset:264\n\t" \
    "v_mfma_f32_16x16x32_bf16 v[32:35], v[120:123], v[136:139], v[32:35]\n\t" \
    "ds_read_b64 v[40:41], v0 offset:288\n\t" \
    "ds_read_b64 v[42:43], v0 offset:296\n\t" \
    "s_waitcnt lgkmcnt(10)\n\t" \
    "v_mfma_f32_16x16x32_bf16 v[16:19], v[116:119], v[152:155], v[16:19]\n\t" \
    "ds_read_b128 v[56:59], v1 offset:4992\n\t" \
    "ds_read_b128 v[72:75], v1 offset:12672\n\t" \
    "v_mfma_f32_16x16x32_bf16 v[32:35], v[120:123], v[152:155], v[32:35]\n\t" \
    "ds_read_b64 v[76:77], v0 offset:320\n\t" \
    "ds_read_b64 v[78:79], v0 offset:328\n\t" \
    "s_waitcnt lgkmcnt(9)\n\t" \
    "v_mfma_f32_16x16x32_bf16 v[16:19], v[156:159], v[176:179], v[16:19]\n\t" \
    "ds_read_b64 v[80:81], v0 offset:352\n\t" \
    "ds_read_b64 v[82:83], v0 offset:360\n\t" \
    "v_mfma_f32_16x16x32_bf16 v[32:35], v[160:163], v[176:179], v[32:35]\n\t" \
    "ds_read_b128 v[96:99], v1 offset:4896\n\t" \
    "ds_read_b128 v[112:115], v1 offset:12576\n\t" \
    "s_waitcnt lgkmcnt(12)\n\t" \
    "v_mfma_f32_16x16x32_bf16 v[16:19], v[156:159], v[192:195], v[16:19]\n\t" \
    "v_mfma_f32_16x16x32_bf16 v[32:35], v[160:163], v[192:195], v[32:35]\n\t" \
    "v_add_u32 v0, 256, v0\n\t" \
    "v_subrev_u32 v1, 384, v1\n\t" \
    "s_sub_u32 s69, s69, 1\n\t" \
    "s_cmp_lg_u32 s69, 0\n\t" \
    "s_cbranch_scc1 cv_c4_%=\n\t" \
    "s_waitcnt lgkmcnt(0)\n\t" \
    "s_nop 7\n\t" \
    "ds_write_b128 v2, v[4:7] offset:0\n\t" \
    "ds_write_b128 v2, v[8:11] offset:1024\n\t" \
    "ds_write_b128 v2, v[12:15] offset:2048\n\t" \
    "ds_write_b128 v2, v[16:19] offset:3072\n\t" \
    "ds_write_b128 v2, v[20:23] offset:4096\n\t" \
    "ds_write_b128 v2, v[24:27] offset:5120\n\t" \
    "ds_write_b128 v2, v[28:31] offset:6144\n\t" \
    "ds_write_b128 v2, v[32:35] offset:7168\n\t" \
    "s_waitcnt lgkmcnt(0)\n\t" \
    "cv_last_%=:\n\t" \
    ""
#define ASM_EA \
    "s_bitcmp1_b32 %1, 2\n\t" \
    "s_cselect_b32 s40, 0x30, 0x28\n\t" \
    "s_cmp_lt_u32 %1, 64\n\t" \
    "s_cselect_b32 s40, s40, 0x38\n\t" \
    "s_load_dwordx2 s[42:43], %0, s40\n\t" \
    "s_load_dword s48, %0, 0x40\n\t" \
    "s_getpc_b64 s[44:45]\n\t" \
    "ea_pc_%=:\n\t" \
    "s_bfe_u32 s40, %1, 0x20003\n\t" \
    "s_lshl_b32 s40, s40, 15\n\t" \
    "v_lshl_add_u32 v1, %2, 7, s40\n\t" \
    "v_min_u32 v1, code_end_marker-ea_pc_%=, v1\n\t" \
    "s_mov_b32 m0, 155648\n\t" \
    "v_readfirstlane_b32 s41, %2\n\t" \
    "global_load_lds_dword v1, s[44:45]\n\t" \
    "s_bitcmp1_b32 %1, 2\n\t" \
    "s_cselect_b32 s40, 24, 16\n\t" \
    "s_lshr_b32 s47, %1, 3\n\t" \
    "s_cmp_ge_u32 s47, s40\n\t" \
    "s_cbranch_scc1 ea_done1_%=\n\t" \
    "s_mov_b32 s46, pad_qk-ea_pc_%=\n\t" \
    "s_mov_b32 s40, pad_vv-ea_pc_%=\n\t" \
    "s_bitcmp1_b32 %1, 2\n\t" \
    "s_cselect_b32 s46, pad_gx-ea_pc_%=, s46\n\t" \
    "s_cselect_b32 s40, pad_gv-ea_pc_%=, s40\n\t" \
    "s_cmp_lt_u32 %1, 64\n\t" \
    "s_cselect_b32 s40, s46, s40\n\t" \
    "s_add_u32 s44, s44, s40\n\t" \
    "s_addc_u32 s45, s45, 0\n\t" \
    "s_swappc_b64 s[46:47], s[44:45]\n\t" \
    "s_lshl_b32 m0, s41, 4\n\t" \
    "s_and_b32 s40, %1, 3\n\t" \
    "s_lshl_b32 s40, s40, 11\n\t" \
    "s_bfe_u32 s46, %1, 0x30003\n\t" \
    "s_lshl_b32 s46, s46, 8\n\t" \
    "s_add_u32 s40, s40, s46\n\t" \
    "s_and_b32 s41, s41, 0x80\n\t" \
    "s_xor_b32 s40, s40, s41\n\t" \
    "v_lshrrev_b32 v1, 3, %2\n\t" \
    "v_and_b32 v1, 6, v1\n\t" \
    "v_xor_b32 v1, v1, %2\n\t" \
    "v_and_b32 v1, 15, v1\n\t" \
    "v_lshlrev_b32 v1, 4, v1\n\t" \
    "v_xor_b32 v1, s40, v1\n\t" \
    "v_lshrrev_b32 v2, 4, %2\n\t" \
    "v_lshl_add_u32 v1, v2, 13, v1\n\t" \
    "s_mov_b32 s40, 16\n\t" \
    "s_waitcnt lgkmcnt(0)\n\t" \
    "ea_lp2_%=:\n\t" \
    "global_load_lds_dwordx4 v1, s[42:43]\n\t" \
    "v_add_u32 v1, 0x20000, v1\n\t" \
    "s_add_u32 m0, m0, 4096\n\t" \
    "s_sub_u32 s40, s40, 1\n\t" \
    "s_cmp_lg_u32 s40, 0\n\t" \
    "s_cbranch_scc1 ea_lp2_%=\n\t" \
    "ea_done1_%=:\n\t" \
    "s_waitcnt lgkmcnt(0)\n\t" \
    "ea_last_%=:\n\t" \
    ""
#define ASM_CLOB "v0", "v1", "v2", "v3", "v4", "v5", "v6", "v7", "v8", "v9", "v10", "v11", "v12", "v13", "v14", "v15", "v16", "v17", "v18", "v19", "v20", "v21", "v22", "v23", "v24", "v25", "v26", "v27", "v28", "v29", "v30", "v31", "v32", "v33", "v34", "v35", "v36", "v37", "v38", "v39", "v40", "v41", "v42", "v43", "v44", "v45", "v46", "v47", "v48", "v49", "v50", "v51", "v52", "v53", "v54", "v55", "v56", "v57", "v58", "v59", "v60", "v61", "v62", "v63", "v64", "v65", "v66", "v67", "v68", "v69", "v70", "v71", "v72", "v73", "v74", "v75", "v76", "v77", "v78", "v79", "v80", "v81", "v82", "v83", "v84", "v85", "v86", "v87", "v88", "v89", "v90", "v91", "v92", "v93", "v94", "v95", "v96", "v97", "v98", "v99", "v100", "v101", "v102", "v103", "v104", "v105", "v106", "v107", "v108", "v109", "v110", "v111", "v112", "v113", "v114", "v115", "v116", "v117", "v118", "v119", "v120", "v121", "v122", "v123", "v124", "v125", "v126", "v127", "v128", "v129", "v130", "v131", "v132", "v133", "v134", "v135", "v136", "v137", "v138", "v139", "v140", "v141", "v142", "v143", "v144", "v145", "v146", "v147", "v148", "v149", "v150", "v151", "v152", "v153", "v154", "v155", "v156", "v157", "v158", "v159", "v160", "v161", "v162", "v163", "v164", "v165", "v166", "v167", "v168", "v169", "v170", "v171", "v172", "v173", "v174", "v175", "v176", "v177", "v178", "v179", "v180", "v181", "v182", "v183", "v184", "v185", "v186", "v187", "v188", "v189", "v190", "v191", "v192", "v193", "v194", "v195", "v196", "v197", "v198", "v199", "v200", "v201", "v202", "v203", "v204", "v205", "v206", "v207", "v208", "v209", "v210", "v211", "v212", "v213", "v214", "v215", "v222", "v223", "v224", "v225", "v226", "v227", "v228", "v229", "v230", "v231", "v232", "v233", "v234", "v235", "v236", "v237", "v238", "v239", "v240", "v241", "v242", "v243", "v244", "v245", "v246", "v247", "v248", "v249", "v250", "v251", "v252", "v253", "v254", "v255", "a0", "a1", "a2", "a3", "a4", "a5", "a6", "a7", "a8", "a9", "a10", "a11", "a12", "a13", "a14", "a15", "a16", "a17", "a18", "a19", "a20", "a21", "a22", "a23", "a24", "a25", "a26", "a27", "a28", "a29", "a30", "a31", "a32", "a33", "a34", "a35", "a36", "a37", "a38", "a39", "a40", "a41", "a42", "a43", "a44", "a45", "a46", "a47", "a48", "a49", "a50", "a51", "a52", "a53", "a54", "a55", "a56", "a57", "a58", "a59", "a60", "a61", "a62", "a63", "a64", "a65", "a66", "a67", "a68", "a69", "a70", "a71", "a72", "a73", "a74", "a75", "a76", "a77", "a78", "a79", "a80", "a81", "a82", "a83", "a84", "a85", "a86", "a87", "a88", "a89", "a90", "a91", "a92", "a93", "a94", "a95", "a96", "a97", "a98", "a99", "a100", "a101", "a102", "a103", "a104", "a105", "a106", "a107", "a108", "a109", "a110", "a111", "a112", "a113", "a114", "a115", "a116", "a117", "a118", "a119", "a120", "a121", "a122", "a123", "a124", "a125", "a126", "a127", "a128", "a129", "a130", "a131", "a132", "a133", "a134", "a135", "a136", "a137", "a138", "a139", "a140", "a141", "a142", "a143", "a144", "a145", "a146", "a147", "a148", "a149", "a150", "a151", "a152", "a153", "a154", "a155", "a156", "a157", "a158", "a159", "a160", "a161", "a162", "a163", "a164", "a165", "a166", "a167", "a168", "a169", "a170", "a171", "a172", "a173", "a174", "a175", "a176", "a177", "a178", "a179", "a180", "a181", "a182", "a183", "a184", "a185", "a186", "a187", "a188", "a189", "a190", "a191", "a192", "a193", "a194", "a195", "a196", "a197", "a198", "a199", "a200", "a201", "a202", "a203", "a204", "a205", "a206", "a207", "a208", "a209", "a210", "a211", "a212", "a213", "a214", "a215", "a216", "a217", "a218", "a219", "a220", "a221", "a222", "a223", "a224", "a225", "a226", "a227", "a228", "a229", "a230", "a231", "a232", "a233", "a234", "a235", "a236", "a237", "a238", "a239", "a240", "a241", "a242", "a243", "a244", "a245", "a246", "a247", "a248", "a249", "a250", "a251", "a252", "a253", "a254", "a255", "s33", "s34", "s35", "s36", "s37", "s38", "s39", "s40", "s41", "s42", "s43", "s44", "s45", "s46", "s47", "s48", "s49", "s50", "s51", "s52", "s53", "s54", "s55", "s56", "s57", "s58", "s59", "s60", "s61", "s62", "s63", "s64", "s65", "s66", "s67", "s68", "s69", "s70", "s71", "s72", "s73", "s74", "s75", "s76", "s77", "s78", "s79", "s80", "s81", "s82", "s83", "s84", "s85", "s86", "s87", "s88", "s89", "s90", "s91", "s92", "s93", "s94", "s95", "s96", "s97", "s98", "s99", "vcc", "scc", "memory"
#define ASM_CLOBW "v32", "v33", "v34", "v35", "v36", "v37", "v38", "v39", "v40", "v41", "v42", "v43", "v44", "v45", "v46", "v47", "v48", "v49", "v50", "v51", "v52", "v53", "v54", "v55", "v56", "v57", "v58", "v59", "v60", "v61", "v62", "v63", "v64", "v65", "v66", "v67", "v68", "v69", "v70", "v71", "v72", "v73", "v74", "v75", "v76", "v77", "v78", "v79", "v80", "v81", "v82", "v83", "v84", "v85", "v86", "v87", "v88", "v89", "v90", "v91", "v92", "v93", "v94", "v95", "v96", "v97", "v98", "v99", "v100", "v101", "v102", "v103", "v104", "v105", "v106", "v107", "v108", "v109", "v110", "v111", "v112", "v113", "v114", "v115", "v116", "v117", "v118", "v119", "v120", "v121", "v122", "v123", "v124", "v125", "v126", "v127", "v128", "v129", "v130", "v131", "v132", "v133", "v134", "v135", "v136", "v137", "v138", "v139", "v140", "v141", "v142", "v143", "v144", "v145", "v146", "v147", "v148", "v149", "v150", "v151", "v152", "v153", "v154", "v155", "v156", "v157", "v158", "v159", "v160", "v161", "v162", "v163", "v164", "v165", "v166", "v167", "v168", "v169", "v170", "v171", "v172", "v173", "v174", "v175", "v176", "v177", "v178", "v179", "v180", "v181", "v182", "v183", "v184", "v185", "v186", "v187", "v188", "v189", "v190", "v191", "v192", "v193", "v194", "v195", "v196", "v197", "v198", "v199", "v222", "v223", "v224", "v225", "v226", "v227", "v228", "v229", "v230", "v231", "v232", "v233", "v234", "v235", "v236", "v237", "v238", "v239", "v240", "v241", "v242", "v243", "v244", "v245", "v246", "v247", "v248", "v249", "v250", "v251", "v252", "v253", "v254", "v255", "a0", "a1", "a2", "a3", "a4", "a5", "a6", "a7", "a8", "a9", "a10", "a11", "a12", "a13", "a14", "a15", "a16", "a17", "a18", "a19", "a20", "a21", "a22", "a23", "a24", "a25", "a26", "a27", "a28", "a29", "a30", "a31", "a32", "a33", "a34", "a35", "a36", "a37", "a38", "a39", "a40", "a41", "a42", "a43", "a44", "a45", "a46", "a47", "a48", "a49", "a50", "a51", "a52", "a53", "a54", "a55", "a56", "a57", "a58", "a59", "a60", "a61", "a62", "a63", "a64", "a65", "a66", "a67", "a68", "a69", "a70", "a71", "a72", "a73", "a74", "a75", "a76", "a77", "a78", "a79", "a80", "a81", "a82", "a83", "a84", "a85", "a86", "a87", "a88", "a89", "a90", "a91", "a92", "a93", "a94", "a95", "a96", "a97", "a98", "a99", "a100", "a101", "a102", "a103", "a104", "a105", "a106", "a107", "a108", "a109", "a110", "a111", "a112", "a113", "a114", "a115", "a116", "a117", "a118", "a119", "a120", "a121", "a122", "a123", "a124", "a125", "a126", "a127", "a128", "a129", "a130", "a131", "a132", "a133", "a134", "a135", "a136", "a137", "a138", "a139", "a140", "a141", "a142", "a143", "a144", "a145", "a146", "a147", "a148", "a149", "a150", "a151", "a152", "a153", "a154", "a155", "a156", "a157", "a158", "a159", "a160", "a161", "a162", "a163", "a164", "a165", "a166", "a167", "a168", "a169", "a170", "a171", "a172", "a173", "a174", "a175", "a176", "a177", "a178", "a179", "a180", "a181", "a182", "a183", "a184", "a185", "a186", "a187", "a188", "a189", "a190", "a191", "a192", "a193", "a194", "a195", "a196", "a197", "a198", "a199", "a200", "a201", "a202", "a203", "a204", "a205", "a206", "a207", "a208", "a209", "a210", "a211", "a212", "a213", "a214", "a215", "a216", "a217", "a218", "a219", "a220", "a221", "a222", "a223", "a224", "a225", "a226", "a227", "a228", "a229", "a230", "a231", "a232", "a233", "a234", "a235", "a236", "a237", "a238", "a239", "a240", "a241", "a242", "a243", "a244", "a245", "a246", "a247", "a248", "a249", "a250", "a251", "a252", "a253", "a254", "a255", "s33", "s34", "s35", "s36", "s37", "s38", "s39", "s40", "s41", "s42", "s43", "s44", "s45", "s46", "s47", "s48", "s49", "s50", "s51", "s52", "s53", "s54", "s55", "s56", "s57", "s58", "s59", "s60", "s61", "s62", "s63", "s64", "s65", "s66", "s67", "s68", "s69", "s70", "s71", "s72", "s73", "s74", "s75", "s76", "s77", "s78", "s79", "s80", "s81", "s82", "s83", "s84", "s85", "s86", "s87", "s88", "s89", "s90", "s91", "s92", "s93", "s94", "s95", "s96", "s97", "s98", "s99", "vcc", "scc", "memory"
#define CV_CLOB "v0", "v1", "v2", "v3", "v4", "v5", "v6", "v7", "v8", "v9", "v10", "v11", "v12", "v13", "v14", "v15", "v16", "v17", "v18", "v19", "v20", "v21", "v22", "v23", "v24", "v25", "v26", "v27", "v28", "v29", "v30", "v31", "v32", "v33", "v34", "v35", "v36", "v37", "v38", "v39", "v40", "v41", "v42", "v43", "v44", "v45", "v46", "v47", "v48", "v49", "v50", "v51", "v52", "v53", "v54", "v55", "v56", "v57", "v58", "v59", "v60", "v61", "v62", "v63", "v64", "v65", "v66", "v67", "v68", "v69", "v70", "v71", "v72", "v73", "v74", "v75", "v76", "v77", "v78", "v79", "v80", "v81", "v82", "v83", "v84", "v85", "v86", "v87", "v88", "v89", "v90", "v91", "v92", "v93", "v94", "v95", "v96", "v97", "v98", "v99", "v100", "v101", "v102", "v103", "v104", "v105", "v106", "v107", "v108", "v109", "v110", "v111", "v112", "v113", "v114", "v115", "v116", "v117", "v118", "v119", "v120", "v121", "v122", "v123", "v124", "v125", "v126", "v127", "v128", "v129", "v130", "v131", "v132", "v133", "v134", "v135", "v136", "v137", "v138", "v139", "v140", "v141", "v142", "v143", "v144", "v145", "v146", "v147", "v148", "v149", "v150", "v151", "v152", "v153", "v154", "v155", "v156", "v157", "v158", "v159", "v160", "v161", "v162", "v163", "v164", "v165", "v166", "v167", "v168", "v169", "v170", "v171", "v172", "v173", "v174", "v175", "v176", "v177", "v178", "v179", "v180", "v181", "v182", "v183", "v184", "v185", "v186", "v187", "v188", "v189", "v190", "v191", "v192", "v193", "v194", "v195", "s69", "scc", "memory"
static constexpr unsigned G_STEP = 0x10000;
static constexpr unsigned G_WS_CTL = 0x580000;
static constexpr unsigned G_WS_DONE = 0x5f0000;
static constexpr unsigned G_WS_EP = 0x5f2000;
static constexpr unsigned G_WS_STAMPS = 0xc000000;
static constexpr unsigned G_C_CAL = 0x0;
static constexpr unsigned G_C_ERR = 0x100;
static constexpr unsigned G_C_TASK = 0x200;
static constexpr unsigned G_C_GX = 0x1000;
static constexpr unsigned G_C_GV = 0x4000;
static constexpr unsigned G_C_KM = 0x8000;
static constexpr unsigned G_C_UM = 0x9000;
static constexpr unsigned G_C_PC = 0xc000;
static constexpr unsigned G_C_UT = 0xd000;
static constexpr unsigned G_C_YC = 0x10000;
static constexpr unsigned G_C_ARR = 0x11000;
static constexpr unsigned G_C_KG = 0x12000;
static constexpr unsigned G_C_GQ = 0x14000;
static constexpr unsigned G_C_RDY = 0x15000;
static constexpr unsigned G_C_GT = 0x18000;
static constexpr unsigned G_C_GO = 0x11800;
static constexpr unsigned G_C_RW = 0xf000;
static constexpr unsigned G_WS_H = 0x0;
static constexpr unsigned G_HS8 = 0x80000;
static constexpr unsigned G_HS4 = 0x40000;
static constexpr unsigned G_UXS = 0x200000;
static constexpr unsigned G_UMS = 0x90000;
static constexpr unsigned G_PROBE = 0x1bd400;
static constexpr unsigned G_D_QX = 0x600000;
static constexpr unsigned G_D_KX = 0x1600000;
static constexpr unsigned G_D_KM = 0x2600000;
static constexpr unsigned G_D_UX = 0x2e00000;
static constexpr unsigned G_D_UM = 0x7600000;
static constexpr unsigned G_D_PX = 0xa200000;
static constexpr unsigned G_D_PM = 0xa600000;
static constexpr unsigned G_D_KT = 0xa700000;
static constexpr unsigned G_D_UT = 0xac00000;
static constexpr unsigned G_D_YB = 0xaf00000;
static constexpr unsigned G_D_END = 0xb200000;
static constexpr unsigned G_SKM = 0x7;
static constexpr unsigned G_S1 = 0x18;
static constexpr unsigned G_S0 = 0x6;
static constexpr unsigned G_GVN = 0x2;
static constexpr unsigned G_STG = 0x9000;
static constexpr unsigned G_KROW = 0x30;
#define FLG(x_) (kDefault[5])
#define FLH(x_) (kDefault[7])
static constexpr int kDefault[8] = {PRM_DEFAULT};
#define MFMA32(a_, b_, c_) __builtin_amdgcn_mfma_f32_16x16x32_bf16(__builtin_bit_cast(bf16x8, a_), __builtin_bit_cast(bf16x8, b_), c_, 0, 0, 0)
#define MFMA16(a_, b_, c_) __builtin_amdgcn_mfma_f32_16x16x16bf16_1k(__builtin_bit_cast(s16x4, a_), __builtin_bit_cast(s16x4, b_), c_, 0, 0, 0)
#define WLD8(d_, voff_, base_, off_) asm volatile("s_nop 4\n\tglobal_load_dwordx2 %0, %1, %2 offset:%3 sc1" : "=v"(d_) : "v"(voff_), "s"(base_), "i"(off_) : "memory")
#define WLD16(d_, voff_, base_, off_) asm volatile("s_nop 4\n\tglobal_load_dwordx4 %0, %1, %2 offset:%3 sc1" : "=v"(d_) : "v"(voff_), "s"(base_), "i"(off_) : "memory")
#define WST16(voff_, d_, base_, off_) asm volatile("s_nop 4\n\tglobal_store_dwordx4 %0, %1, %2 offset:%3 sc1\n\ts_nop 1" :: "v"(voff_), "v"(d_), "s"(base_), "i"(off_) : "memory")
#define WLD16X4(d0, d1, d2, d3, voff_, base_) asm volatile("s_nop 4\n\tglobal_load_dwordx4 %0, %4, %5 sc1\n\tglobal_load_dwordx4 %1, %4, %5 offset:1024 sc1\n\t" \
    "global_load_dwordx4 %2, %4, %5 offset:2048 sc1\n\tglobal_load_dwordx4 %3, %4, %5 offset:3072 sc1" \
    : "=&v"(d0), "=&v"(d1), "=&v"(d2), "=&v"(d3) : "v"(voff_), "s"(base_) : "memory")

__device__ __forceinline__ u32x4 pack8(const f32x4& a, const f32x4& b) {
    return u32x4{pk2bf(a[0], a[1]), pk2bf(a[2], a[3]), pk2bf(b[0], b[1]), pk2bf(b[2], b[3])};
}
template <class T> __device__ __forceinline__ T* uni(T* q) {
    const u64 v = (u64)q;
    unsigned lo = __builtin_amdgcn_readfirstlane((unsigned)v), hi = __builtin_amdgcn_readfirstlane((unsigned)(v >> 32));
    asm volatile("s_nop 4" : "+s"(lo), "+s"(hi));
    return (T*)(((u64)hi << 32) | lo);
}
template <bool FAST = false>
__device__ __forceinline__ void wait_word(const MainP& p, const char* f, int want, unsigned code) {
    unsigned tries = 0;
    while (__any((int)__hip_atomic_load((const gu32*)f, RLX_AGENT) < want)) {
        if (++tries > kWaitCap) { __hip_atomic_store((gu32*)(p.ws + G_WS_CTL + G_C_ERR), code, RLX_AGENT); break; }
        if ((tries & 1023u) == 0u && __hip_atomic_load((const gu32*)(p.ws + G_WS_CTL + G_C_ERR), RLX_AGENT) != 0xffffffffu) break;
        if (FAST) __builtin_amdgcn_s_sleep(1); else __builtin_amdgcn_s_sleep(10);
    }
}

__device__ __forceinline__ int skew(int s, int id) { return (s ^ (G_SKM * id)) & 63; }
__device__ __forceinline__ unsigned um_base(int s, unsigned nm) { return G_D_UM + (s / 3) * 0x200000 + (s % 3) * G_UMS + ((nm >> (2 + s / 3)) & 1) * 8192; }
typedef GAS unsigned long long gu64;
#define IST(k_, slot_)
__device__ __forceinline__ void init_done(char* ws, int slot, unsigned val) {
    const unsigned e = __hip_atomic_load((const gu32*)(ws + G_WS_EP), RLX_AGENT);
    __hip_atomic_store((gu64*)(ws + G_WS_DONE + slot * 64), ((u64)val << 32) | (e + 1u), RLX_AGENT);
}
__device__ __forceinline__ void init_wait(char* ws, int w, int lane) {
    const unsigned e = __hip_atomic_load((const gu32*)(ws + G_WS_EP), RLX_AGENT);
    if (w == 0) for (unsigned tries = 0; tries < kWaitCap; ++tries) {
        if (!__any(__hip_atomic_load((const gu32*)(ws + G_WS_DONE + lane * 64), RLX_AGENT) != e + 1u || __hip_atomic_load((const gu32*)(ws + G_WS_DONE + 2048 + lane * 64), RLX_AGENT) != e + 1u)) break;
        __builtin_amdgcn_s_sleep(8);
    }
    __syncthreads();
    if (w == 0 && lane == 0) __hip_atomic_fetch_add((gu32*)(ws + G_WS_CTL + G_C_ARR), 1u, RLX_AGENT);
}
__device__ __forceinline__ void fill_block(char* ws, int b, int tid, const u32x4& v0, const u32x4& v1) {
    const unsigned o = b * 8192 + tid * 16, o2 = o + 4096;
    asm volatile("global_store_dwordx4 %0, %1, %2 sc1\n\ts_nop 1" :: "v"(o), "v"(v0), "s"(ws) : "memory");
    if (b >= 512 || (b & 6) != 6 || tid < 64)
        asm volatile("global_store_dwordx4 %0, %1, %2 sc1\n\ts_nop 1" :: "v"(o2), "v"(v1), "s"(ws) : "memory");
}
__device__ __forceinline__ void init_fill(char* ws, const u16* mem0, int f, int tid) {
    const u32x4 sent = {0xffffffffu, 0xffffffffu, 0xffffffffu, 0xffffffffu};
    u32x4 v0 = sent, v1 = sent;
    if (f < 32 && (f & 4)) {
#pragma unroll
        for (int k = 0; k < 2; ++k) {
            const int q = tid + 256 * k + ((f & 2) ? 256 : 0), m = q & 15;
            if (!(f & 2) || (q >= 512 && q < 576)) {
                const unsigned* src = (const unsigned*)(mem0 + (2 * (m >> 2) + (m & 1)) * DM + 16 * (q >> 4) + (m & 2));
                const u32x4 o = {min(src[0], 0xfffffffeu), min(src[2], 0xfffffffeu), min(src[4], 0xfffffffeu), min(src[6], 0xfffffffeu)};
                if (k) v1 = o; else v0 = o;
            }
        }
    }
#pragma unroll 1
    for (int q = f; q < 182; q += 48) {
        if (q >= 136 && q < 152 && (tid & 127)) { v0 = u32x4{0u, 0u, 0u, 0u}; v1 = v0; }
        fill_block(ws, q < 64 ? q : q < 136 ? q - 64 + G_D_UM / 8192 : q < 168 ? 256 + ((q - 136) >> 2) * 8 + (q & 3) : q - 168 + G_WS_CTL / 8192, tid, v0, v1);
        v0 = sent; v1 = sent;
    }
    const unsigned e = __hip_atomic_load((const gu32*)(ws + G_WS_EP), RLX_AGENT);
    asm volatile("s_waitcnt vmcnt(0)" :: "v"(e) : "memory");
    __syncthreads();
    if (tid == 0) __hip_atomic_store((gu64*)(ws + G_WS_DONE + (48 + f) * 64), (u64)(e + 1u), RLX_AGENT);
    if (tid == 0) IST(0, 48 + f);
}
__device__ __forceinline__ void init_cal(char* ws, int id, int tid, int n) {
    if (tid < 64) {
        const int pg = id >> 1;
        char* a = ws + (pg < 2 ? pg * 0x200000u : G_D_UM + (pg - 2) * 0x200000u) + G_PROBE + (id & 1) * 8192;
        const u32x4 z = {0u, 0u, 0u, 0u};
        asm volatile("global_store_dwordx4 %0, %1, off sc1\n\tglobal_store_dwordx4 %0, %1, off offset:1024 sc1\n\ts_waitcnt vmcnt(0)" :: "v"(a + tid * 16), "v"(z) : "memory");
        if (tid == 0) {
            unsigned best = 0xffffu;
            const unsigned ts1 = (unsigned)__builtin_amdgcn_s_memrealtime() << 4;
#pragma unroll 1
            for (int it = 0; it < n; ++it) {
                const unsigned c0 = (unsigned)__builtin_amdgcn_s_memtime();
                const unsigned v = __hip_atomic_load((const gu32*)(a + it * 128), RLX_AGENT);
                asm volatile("s_waitcnt vmcnt(0)" :: "v"(v) : "memory");
                best = min(best, (unsigned)__builtin_amdgcn_s_memtime() - c0);
            }
            init_done(ws, id, best);
            IST(0, id);
        }
    }
    __syncthreads();
}
__device__ __forceinline__ unsigned near_mask(char* ws, int lane) {
    const gu32* q = (const gu32*)(ws + G_WS_DONE + (lane & 31) * 128 + 4);
    return (unsigned)__ballot(__hip_atomic_load(q + 16, RLX_AGENT) < __hip_atomic_load(q, RLX_AGENT));
}

#define TST(slot_, i_)
#define PIN8(a) asm volatile("" : "+v"(a[0]), "+v"(a[1]), "+v"(a[2]), "+v"(a[3]), "+v"(a[4]), "+v"(a[5]), "+v"(a[6]), "+v"(a[7]))
struct AOps { u32x4 q[4], k[8]; int fl; };
struct APtr { const char* q; const char* k; const char* m; unsigned voff, vkm, voffm, vfl; };
__device__ __forceinline__ void attn_batch(const MainP& p, AOps& o, const APtr& a) {
    const char* q2 = a.q + 4096; const char* k2 = a.k + 4096;
    WLD16(o.q[0], a.voff, a.q, 0); WLD16(o.q[1], a.voff, a.q, 2048); WLD16(o.q[2], a.voff, q2, 0); WLD16(o.q[3], a.voff, q2, 2048);
    WLD16X4(o.k[0], o.k[1], o.k[2], o.k[3], a.voff, a.k); WLD16X4(o.k[4], o.k[5], o.k[6], o.k[7], a.voff, k2);
    asm volatile("global_load_dword %0, %1, %2 sc1" : "=v"(o.fl) : "v"(a.vfl), "s"(p.ws) : "memory");
}
__device__ __forceinline__ void attn_pin(AOps& o) {
    asm volatile("" : "+v"(o.fl), "+v"(o.q[0]), "+v"(o.q[1]), "+v"(o.q[2]), "+v"(o.q[3])); PIN8(o.k);
}
static constexpr int NFINE = 6;
static constexpr int NG1 = 64 - NFINE;
__device__ __forceinline__ void score_round4(float* r, const f32x4& x) {
    asm volatile("v_cvt_pk_bf16_f32 %0, 0, %4\n\tv_cvt_pk_bf16_f32 %1, 0, %5\n\tv_cvt_pk_bf16_f32 %2, 0, %6\n\tv_cvt_pk_bf16_f32 %3, 0, %7\n\t"
                 "v_mul_f32 %0, 0x3db504f3, %0\n\tv_mul_f32 %1, 0x3db504f3, %1\n\tv_mul_f32 %2, 0x3db504f3, %2\n\tv_mul_f32 %3, 0x3db504f3, %3\n\t"
                 "v_cvt_pk_bf16_f32 %0, 0, %0\n\tv_cvt_pk_bf16_f32 %1, 0, %1\n\tv_cvt_pk_bf16_f32 %2, 0, %2\n\tv_cvt_pk_bf16_f32 %3, 0, %3"
                 : "=&v"(r[0]), "=&v"(r[1]), "=&v"(r[2]), "=&v"(r[3]) : "v"(x[0]), "v"(x[1]), "v"(x[2]), "v"(x[3]));
}
__device__ __forceinline__ float vmax3(float a, float b, float c) { float r; asm("v_max3_f32 %0, %1, %2, %3" : "=v"(r) : "v"(a), "v"(b), "v"(c)); return r; }
template <int N>
__device__ __forceinline__ void attn_soft(float (&q)[12], u32x2& Pm, u32x4& Px) {
    float m = vmax3(q[0], q[1], q[2]);
#pragma unroll
    for (int i = 3; i < N; i += 2) m = vmax3(m, q[i], q[i + 1 < N ? i + 1 : i]);
    { auto a = __builtin_amdgcn_permlane16_swap(__float_as_uint(m), __float_as_uint(m), false, false); m = vmax3(m, __uint_as_float(a[0]), __uint_as_float(a[1])); }
    { auto a = __builtin_amdgcn_permlane32_swap(__float_as_uint(m), __float_as_uint(m), false, false); m = vmax3(m, __uint_as_float(a[0]), __uint_as_float(a[1])); }
    const float ml = -m * 1.4426950408889634f;
#pragma unroll
    for (int i = 0; i < N; ++i) q[i] = __builtin_amdgcn_exp2f(fmaf(q[i], 1.4426950408889634f, ml));
    float sum = 0.5f * ((q[0] + q[1]) + (q[2] + q[3])) + ((q[4] + q[5]) + (q[6] + q[7]));
    if (N > 8) sum += (q[8] + q[9]) + (q[10] + q[11]);
    sum = rowsum4(sum);
    const float inv = __builtin_amdgcn_rcpf(sum);
    Pm = u32x2{pk2bf(q[0] * inv, q[1] * inv), pk2bf(q[2] * inv, q[3] * inv)};
    Px = u32x4{pk2bf(q[4] * inv, q[5] * inv), pk2bf(q[6] * inv, q[7] * inv), N > 8 ? pk2bf(q[8] * inv, q[9] * inv) : 0u, N > 8 ? pk2bf(q[10] * inv, q[11] * inv) : 0u};
}
#define NASPLIT 12
#define HSL 1
#define NAG 2
static constexpr int NA = NASPLIT;
__device__ __forceinline__ int attn_next(int s) { return s < NA ? s + 2 : s + NAG; }
__device__ __forceinline__ unsigned attn_flag(int s, int hq) { return G_WS_CTL + G_C_PC + (s < NA ? s & 1 : (s - NA) % NAG) * 1024 + hq * 128; }
struct BOps { u32x4 q[8], k[8]; };
#define WLD16X4A(d0, d1, d2, d3, voff_, base_) asm volatile("s_nop 4\n\tglobal_load_dwordx4 %0, %4, %5 sc1\n\tglobal_load_dwordx4 %1, %4, %5 offset:1024 sc1\n\t" \
    "global_load_dwordx4 %2, %4, %5 offset:2048 sc1\n\tglobal_load_dwordx4 %3, %4, %5 offset:3072 sc1" \
    : "=&a"(d0), "=&a"(d1), "=&a"(d2), "=&a"(d3) : "v"(voff_), "s"(base_) : "memory")
#define PIN8A(a) asm volatile("" : "+a"(a[0]), "+a"(a[1]), "+a"(a[2]), "+a"(a[3]), "+a"(a[4]), "+a"(a[5]), "+a"(a[6]), "+a"(a[7]))
#define MFMA_AV0(acc, a_, b_) asm volatile("v_mfma_f32_16x16x32_bf16 %0, %1, %2, 0" : "=&v"(acc) : "a"(a_), "v"(b_))
#define MFMA_AV(acc, a_, b_) asm volatile("v_mfma_f32_16x16x32_bf16 %0, %1, %2, %0" : "+v"(acc) : "a"(a_), "v"(b_))
__device__ __forceinline__ void attn_batch8(const MainP& p, BOps& o, const APtr& a) {
    const char* q2 = a.q + 4096; const char* k2 = a.k + 4096;
    WLD16X4(o.q[0], o.q[1], o.q[2], o.q[3], a.voff, a.q); WLD16X4(o.q[4], o.q[5], o.q[6], o.q[7], a.voff, q2);
    WLD16X4A(o.k[0], o.k[1], o.k[2], o.k[3], a.voff, a.k); WLD16X4A(o.k[4], o.k[5], o.k[6], o.k[7], a.voff, k2);
}
__device__ __forceinline__ void attn_step(const MainP& p, BOps& o, BOps& n1, BOps& n2, APtr& a, int& kg, int& kk, int s, int h, int w, int lane) {
    const int g = lane >> 4, c = lane & 15;
    const float scale = 0.08838834764831845f;
    TST(s * 64 + h * 2, 0);
    const int s2 = min(attn_next(attn_next(s)), NSEG - 1);
    if (kg < s2 || kk < s) {
        VMC0();
        if (s < 50) wait_word(p, p.ws + a.vfl, (lane & 1) ? s : s2, 13u); else wait_word<true>(p, p.ws + a.vfl, (lane & 1) ? s : s2, 13u);
    }
    TST(s * 64 + h * 2, 1);
    a.q = p.ws + G_D_QX + (size_t)h * G_HS8 + skew(s2, h) * 8192; a.k = a.q + (G_D_KX - G_D_QX); a.m = p.ws + G_D_KM + (size_t)h * G_HS4 + skew(s, h) * 4096;
    __builtin_amdgcn_sched_barrier(0);
    u32x4 km[4];
    int fl;
    WLD16X4A(km[0], km[1], km[2], km[3], a.vkm, a.m);
    asm volatile("global_load_dword %0, %1, %2 sc1" : "=v"(fl) : "v"(a.vfl), "s"(p.ws) : "memory");
    attn_batch8(p, n2, a);
    f32x4 y[5];
#pragma unroll
    for (int wc = 0; wc < 4; ++wc)
#pragma unroll
        for (int i = 0; i < 3; ++i) { if (wc == 0) MFMA_AV0(y[i], o.k[i >> 1], o.q[(i + 1) >> 1]); else MFMA_AV(y[i], o.k[wc * 2 + (i >> 1)], o.q[wc * 2 + ((i + 1) >> 1)]); }
    asm volatile("s_nop 7" : "+v"(y[0]), "+v"(y[1]), "+v"(y[2]));
    float q0[12], q1[12];
    score_round4(q0 + 4, y[0]); score_round4(q1 + 4, y[1]); score_round4(q1 + 8, y[2]);
#pragma unroll
    for (int i = 0; i < 4; ++i) {
        const bool vis = 4 * g + i <= c;
        q0[4 + i] = vis ? q0[4 + i] : -INFINITY; q1[8 + i] = vis ? q1[8 + i] : -INFINITY;
    }
    __builtin_amdgcn_sched_barrier(0);
    asm volatile("s_waitcnt vmcnt(16)\n\ts_barrier" : "+a"(km[0]), "+a"(km[1]), "+a"(km[2]), "+a"(km[3]), "+v"(fl) :: "memory");
    PIN8(n1.q); PIN8A(n1.k);
    { const unsigned fo = attn_flag(s, h >> 2); const int fv = s - 1;
      asm volatile("s_cmp_lg_u32 %3, 0\n\ts_cbranch_scc1 1f\n\tglobal_store_dword %0, %1, %2 sc1\n1:" :: "v"(fo), "v"(fv), "s"(p.ws), "s"(w) : "scc", "memory"); }
#pragma unroll
    for (int wc = 0; wc < 4; ++wc)
#pragma unroll
        for (int i = 0; i < 2; ++i) { if (wc == 0) MFMA_AV0(y[3 + i], km[0], o.q[i]); else MFMA_AV(y[3 + i], km[wc], o.q[wc * 2 + i]); }
    asm volatile("s_nop 7" : "+v"(y[3]), "+v"(y[4]));
    kg = __builtin_amdgcn_readlane(fl, 0); kk = __builtin_amdgcn_readlane(fl, 1);
    score_round4(q0, y[3]); score_round4(q1, y[4]);
    u32x2 Pm0, Pm1; u32x4 Px0, Px1;
    attn_soft<8>(q0, Pm0, Px0); attn_soft<12>(q1, Pm1, Px1);
    char* pxb = p.ws + G_D_PX + (size_t)(s * 32 + h) * 2048;
    char* pmb = p.ws + G_D_PM + (size_t)s * 16384 + h * 256; char* pm2 = pmb + 8192;
    WST16(a.voff, Px0, pxb, 0); WST16(a.voff, Px1, pxb, 1024);
    asm volatile("global_store_dwordx2 %0, %1, %3 sc1\n\tglobal_store_dwordx2 %0, %2, %4 sc1" :: "v"(a.voffm), "v"(Pm0), "v"(Pm1), "s"(pmb), "s"(pm2) : "memory");
    { const unsigned fo = attn_flag(s, h >> 2);
      asm volatile("s_cmp_lt_u32 %4, 46\n\ts_cbranch_scc1 1f\n\ts_waitcnt vmcnt(0)\n\ts_barrier\n\ts_cmp_lg_u32 %3, 0\n\ts_cbranch_scc1 1f\n\tglobal_store_dword %0, %1, %2 sc1\n1:"
                   :: "v"(fo), "v"(s), "s"(p.ws), "s"(w), "s"(s) : "scc", "memory"); }
    TST(s * 64 + h * 2, 2);
}
__device__ __forceinline__ void attn_role(const MainP& p, int s, int h, int w, int lane) {
    APtr a;
    a.q = p.ws + G_D_QX + (size_t)h * G_HS8 + skew(s, h) * 8192; a.k = a.q + (G_D_KX - G_D_QX);
    a.voff = lane * 16; a.vkm = (lane & 55) * 16; a.voffm = (lane & 15) * 16 + ((lane >> 4) & 1) * 8;
    a.vfl = G_WS_CTL + ((lane & 1) ? G_C_KM : G_C_GQ) + h * 128;
    BOps o0, o1, o2;
    wait_word(p, p.ws + G_WS_CTL + G_C_GQ + h * 128, attn_next(s), 10u);
    attn_batch8(p, o0, a);
    a.q = p.ws + G_D_QX + (size_t)h * G_HS8 + skew(attn_next(s), h) * 8192; a.k = a.q + (G_D_KX - G_D_QX);
    attn_batch8(p, o1, a);
    VMC0();
    PIN8(o0.q); PIN8A(o0.k); PIN8(o1.q); PIN8A(o1.k);
    int kg = -1, kk = -1;
    static_assert(NA % 2 == 0, "ranges");
#pragma unroll 1
    for (;;) {
        attn_step(p, o0, o1, o2, a, kg, kk, s, h, w, lane);
        if ((s = attn_next(s)) >= NG1) break;
        attn_step(p, o1, o2, o0, a, kg, kk, s, h, w, lane);
        if ((s = attn_next(s)) >= NG1) break;
        attn_step(p, o2, o0, o1, a, kg, kk, s, h, w, lane);
        if ((s = attn_next(s)) >= NG1) break;
    }
    VMC0();
}
__device__ __forceinline__ unsigned umax4(const u32x4& v) { return max(max(v[0], v[1]), max(v[2], v[3])); }
__device__ __forceinline__ bool give_up(const MainP& p, unsigned& tries, unsigned code) {
    if (++tries <= kWaitCap / 4) return false;
    __hip_atomic_store((gu32*)(p.ws + G_WS_CTL + G_C_ERR), code, RLX_AGENT);
    return true;
}
__device__ __forceinline__ void task_attn_tail(const MainP& p, int s, int h, int w, int lane) {
    const int g = lane >> 4, c = lane & 15, qt = w & 1;
    const float scale = 0.08838834764831845f;
    APtr a;
    a.q = p.ws + G_D_QX + (size_t)h * G_HS8 + skew(s, h) * 8192 + qt * 1024; a.k = p.ws + G_D_KX + (size_t)h * G_HS8 + skew(s, h) * 8192; a.m = p.ws + G_D_KM + (size_t)h * G_HS4 + skew(s, h) * 4096;
    a.voff = lane * 16; a.vkm = (lane & 55) * 16; a.voffm = c * 16 + (g & 1) * 8; a.vfl = G_WS_CTL;
    AOps o;
    wait_word(p, p.ws + G_WS_CTL + G_C_GQ + h * 128, s, 14u);
    attn_batch(p, o, a);
    VMC0();
    attn_pin(o);
    float q[12];
    u32x4 km[4];
#pragma unroll
    for (int kt = 1; kt >= -1; --kt) {
        if (kt < 0) {
            TST(s * 64 + h * 2 + qt, 0);
            wait_word(p, p.ws + G_WS_CTL + G_C_KM + h * 128, s - 2, 15u);
            if (FLG(p.prm[5]) & 2) wait_word<true>(p, p.ws + G_WS_CTL + G_C_KM + h * 128, s - 1, 15u);
            for (unsigned tries = 0;;) {
                WLD16X4(km[0], km[1], km[2], km[3], a.vkm, a.m);
                asm volatile("s_waitcnt vmcnt(0)" : "+v"(km[0]), "+v"(km[1]), "+v"(km[2]), "+v"(km[3]) :: "memory");
                if (!__any(max(max(umax4(km[0]), umax4(km[1])), max(umax4(km[2]), umax4(km[3]))) == 0xffffffffu) || give_up(p, tries, 16u)) break;
            }
            TST(s * 64 + h * 2 + qt, 1);
        }
        f32x4 y = {0.f, 0.f, 0.f, 0.f};
#pragma unroll
        for (int wc = 0; wc < 4; ++wc) y = MFMA32(kt < 0 ? km[wc] : o.k[wc * 2 + (kt < 0 ? 0 : kt)], o.q[wc], y);
#pragma unroll
        for (int i = 0; i < 4; ++i) q[4 + 4 * kt + i] = (16 * kt + 4 * g + i <= 16 * qt + c) ? bfr(bfr(y[i]) * scale) : -INFINITY;
    }
    u32x2 Pm; u32x4 Px;
    attn_soft<12>(q, Pm, Px);
    char* pxb = p.ws + G_D_PX + (size_t)(s * 32 + h) * 2048 + qt * 1024;
    char* pmb = p.ws + G_D_PM + (size_t)s * 16384 + h * 256 + qt * 8192;
    WST16(a.voff, Px, pxb, 0);
    asm volatile("global_store_dwordx2 %0, %1, %2 sc1" :: "v"(a.voffm), "v"(Pm), "s"(pmb) : "memory");
    TST(s * 64 + h * 2 + qt, 2);
}

#define RLD(d_, voff_, base_, off_) asm volatile("global_load_dwordx4 %0, %1, %2 offset:%3 sc1" : "=v"(d_) : "v"(voff_), "s"(base_), "i"(off_) : "memory")
#define RLDA(d_, voff_, base_, off_) asm volatile("global_load_dwordx4 %0, %1, %2 offset:%3 sc1" : "=a"(d_) : "v"(voff_), "s"(base_), "i"(off_) : "memory")
#define MFMA_ACC_A(acc, a_, b_) asm volatile("v_mfma_f32_16x16x32_bf16 %0, %1, %2, %0" : "+v"(acc) : "a"(a_), "v"(b_))
#define VMW(n_) asm volatile("s_waitcnt vmcnt(%0)" :: "i"(n_) : "memory")
template <int NT, int DEPTH, bool TAIL>
__device__ __forceinline__ void task_out(const MainP& p, char* smem, int s, int ct0, int w, int lane) {
    const int g = lane >> 4, c = lane & 15;
    const unsigned voff = lane * 16;
    f32x4 acc[NT][2];
#pragma unroll
    for (int t = 0; t < NT; ++t) { acc[t][0] = f32x4{0.f, 0.f, 0.f, 0.f}; acc[t][1] = acc[t][0]; asm volatile("" : "+v"(acc[t][0]), "+v"(acc[t][1])); }
    const char* ub[NT];
#pragma unroll
    for (int t = 0; t < NT; ++t) ub[t] = uni(p.ws + G_D_UX + (size_t)(ct0 + t) * G_UXS + skew(s, ct0 + t) * 32768 + w * 8192);
    const char* pb = uni(p.ws + G_D_PX + (size_t)(s * 32 + w * 8) * 2048);
    const char* um = uni(p.ws + um_base(s, p.prm[0]) + ct0 * 16384);
    unsigned vu[NT];
#pragma unroll
    for (int t = 0; t < NT; ++t) { vu[t] = t * 16384 + w * 2048 + voff; asm volatile("" : "+v"(vu[t])); }
    const char* pm = uni(p.ws + G_D_PM + (size_t)s * 16384 + w * 2048);
    constexpr int NR = DEPTH + 1 > 10 ? 10 : DEPTH + 1;
    u32x4 a[NR][NT], b[10][2];
    unsigned vb = voff, va = voff;
    TST(4096 + (s * 36 + ct0) * 4 + w, 0);
    const int f0 = (int)__hip_atomic_load((const gu32*)(p.ws + G_WS_CTL + (lane < 8 ? G_C_UM + (w * 8 + lane) * 128 : lane < 16 ? G_C_GV + ((s < (int)G_S1 ? s & 1 : 0) * 32 + w * 8 + lane - 8) * 128
                                                                                                                                : attn_flag(s, w * 2 + (lane & 1)) - G_WS_CTL)), RLX_AGENT);
    const bool early = !TAIL && !__any(f0 < s);
    if (!early) wait_word(p, p.ws + G_WS_CTL + (lane < 8 ? G_C_GV + ((s < (int)G_S1 ? s & 1 : 0) * 32 + w * 8 + lane) * 128 : G_C_UM + w * 8 * 128), lane < 8 ? s : s - 3, 21u);
#define ALOAD(q) do { if ((q) < 8) { _Pragma("unroll") for (int t = 0; t < NT; ++t) RLDA(a[(q) % NR][t], va, ub[t], 0);  \
                                     asm volatile("v_add_u32 %0, 0x400, %0" : "+v"(va)); }                                            \
                      else { _Pragma("unroll") for (int t = 0; t < NT; ++t) RLDA(a[(q) % NR][t], vu[t], um, ((q) & 1) * 1024); } } while (0)
    constexpr int D0 = DEPTH < 8 ? DEPTH : 8;
    asm volatile("s_nop 4");
#pragma unroll
    for (int q = 0; q < D0; ++q) ALOAD(q);
    asm volatile("s_cmp_lg_u32 %0, 0\n\ts_cbranch_scc1 1f\n\ts_waitcnt vmcnt(0)\n1:\n\ts_waitcnt vmcnt(30)" :: "s"(__builtin_amdgcn_readfirstlane((int)early)) : "scc", "memory");
    unsigned tries = 0;
    if (TAIL) {
        const char* sp = pm + (lane & 7) * 256 + ((lane >> 3) & 1) * 8192;
        const int dl = (FLG(p.prm[5]) >> 26) & 31;
        if (dl) {
            wait_word<true>(p, p.ws + G_WS_CTL + G_C_KM + ((ct0 >> 1) + w) * 128, s - 1, 25u);
            for (int i = 0; i < dl + (s < 63 ? 5 : 0); ++i) __builtin_amdgcn_s_sleep(2);
        } else
        while (__any(__hip_atomic_load((const gu32*)sp, RLX_AGENT) == 0xffffffffu) && !give_up(p, tries, 23u)) __builtin_amdgcn_s_sleep(1);
    } else if (!early) wait_word(p, p.ws + G_WS_CTL + (lane < 8 ? G_C_UM + (w * 8 + lane) * 128 : attn_flag(s, w * 2 + (lane & 1)) - G_WS_CTL), s, 22u);
    if (!early) TST(4096 + (s * 36 + ct0) * 4 + w, 1);
    for (;;) {
    asm volatile("s_nop 4");
#pragma unroll
    for (int q = 0; q < 8; ++q) { RLD(b[q][0], vb, pb, 0); RLD(b[q][1], vb, pb, 1024); asm volatile("v_add_u32 %0, 0x800, %0" : "+v"(vb)); }
#pragma unroll
    for (int q = 8; q < 10; ++q) { RLD(b[q][0], voff, pm, (q & 1) * 1024); RLD(b[q][1], voff, pm + 8192, (q & 1) * 1024); }
#pragma unroll
    for (int q = D0; q < NR; ++q) ALOAD(q);
#pragma unroll
    for (int q = 0; q < 10; ++q) {
        const int issuedA = (NR + q < 10 ? NR + q : 10) - D0;
        if (q < D0) VMW(2 * (9 - q) + issuedA * NT); else VMW((issuedA - (q - D0 + 1)) * NT);
#pragma unroll
        for (int t = 0; t < NT; ++t) { MFMA_ACC_A(acc[t][0], a[q % NR][t], b[q][0]); MFMA_ACC_A(acc[t][1], a[q % NR][t], b[q][1]); }
        if (NR + q < 10) ALOAD(NR + q);
    }
    asm volatile("s_nop 7" ::: "memory");
#pragma unroll
    for (int t = 0; t < NT; ++t) asm volatile("" : "+v"(acc[t][0]), "+v"(acc[t][1]));
    if (!TAIL) break;
    f32x4 chk = acc[0][0];
#pragma unroll
    for (int t = 0; t < NT; ++t) chk += acc[t][1] + acc[t][0];
    const float ck = (chk[0] + chk[1]) + (chk[2] + chk[3]);
    if (!__any(ck != ck)) break;
    unsigned mx = 0;
#pragma unroll
    for (int q = 0; q < 10; ++q) { asm volatile("" : "+v"(b[q][0]), "+v"(b[q][1])); mx = max(mx, max(umax4(b[q][0]), umax4(b[q][1]))); }
#pragma unroll
    for (int t = 0; t < NT; ++t) { asm volatile("" : "+a"(a[8 % NR][t]), "+a"(a[9 % NR][t])); mx = max(mx, max(umax4(a[8 % NR][t]), umax4(a[9 % NR][t]))); }
    if (!__any(mx == 0xffffffffu) || give_up(p, tries, 24u)) break;
    vb = voff;
#pragma unroll
    for (int t = 0; t < NT; ++t) { acc[t][0] = f32x4{0.f, 0.f, 0.f, 0.f}; acc[t][1] = acc[t][0]; asm volatile("" : "+v"(acc[t][0]), "+v"(acc[t][1])); }
    }
    TST(4096 + (s * 36 + ct0) * 4 + w, 2);
    f32x4* red = (f32x4*)smem;
#pragma unroll
    for (int t = 0; t < NT; ++t) { red[((w * NT + t) * 2 + 0) * 64 + lane] = acc[t][0]; red[((w * NT + t) * 2 + 1) * 64 + lane] = acc[t][1]; }
    __syncthreads();
    u16* orow = p.out + (size_t)(32 * s + 16 * (w & 1) + c) * OUTW + 16 * ct0 + 4 * g;
#pragma unroll
    for (int t = 0; t < NT; ++t)
        if ((t & 1) == (w >> 1)) {
            const int m = w & 1;
            const f32x4 v = (red[((0 * NT + t) * 2 + m) * 64 + lane] + red[((1 * NT + t) * 2 + m) * 64 + lane]) +
                            (red[((2 * NT + t) * 2 + m) * 64 + lane] + red[((3 * NT + t) * 2 + m) * 64 + lane]);
            *(u32x2*)(orow + 16 * t) = u32x2{pk2bf(v[0], v[1]), pk2bf(v[2], v[3])};
        }
}

__device__ __forceinline__ int index_of_l(int l) {
    const int pv = (int)bfr((float)l);
    if (pv <= 256) return pv;
    if (pv <= 512) return 256 + ((pv - 256) >> 1);
    if (pv <= 1024) return 384 + ((pv - 512) >> 2);
    return 512 + ((pv - 1024) >> 3);
}
static constexpr int KROW = G_KROW;
static constexpr int KARR = 80 * KROW;
static constexpr int UCS = 2208;
struct BCh {
    u16 uc[4][UCS];
    f32x4 cst[NST];
    unsigned Kd[648];
    u16 Kh[KARR];
    u16 Kl[KARR];
};
static_assert(4 * sizeof(BCh) <= LDS_TOTAL - 32, "LDS");
__device__ __forceinline__ unsigned prm(unsigned hi, unsigned lo, unsigned sel) { return __builtin_amdgcn_perm(hi, lo, sel); }
#define BST(i) asm volatile("" ::: "memory")
#define UT_LOAD(p_) __builtin_nontemporal_load(p_)
template <class F>
__device__ __forceinline__ void task_ut(const MainP& p, char* smem, int rb, int w, int lane, F more) {
    const int tid = w * 64 + lane, g = lane >> 4, c = lane & 15;
    { char* ws = p.ws; if (tid == 0) IST(4, rb); }
    const u32x4* src = (const u32x4*)(p.U + (size_t)rb * 64 * DM);
    u32x4 v[18];
#pragma unroll
    for (int i = 0; i < 18; ++i) v[i] = UT_LOAD(src + tid + 256 * i);
#pragma unroll
    for (int i = 0; i < 18; ++i) { const int ch = tid + 256 * i, row = ch / 72; *(u32x4*)(smem + row * 1184 + (ch - 72 * row) * 16) = v[i]; }
    __syncthreads();
    { char* ws = p.ws; if (tid == 0) IST(3, rb); }
    const unsigned ad = (unsigned)(uintptr_t)smem + (16 * g + (c >> 2)) * 1184 + (w * 144 + 4 * (c & 3)) * 2;
    char* tw = smem + 77824 + w * 20736;
#pragma unroll
    for (int i = 0; i < 9; ++i) {
        u32x2 x0, x1, x2, x3;
        asm volatile("ds_read_b64_tr_b16 %0, %4 offset:%5\n\tds_read_b64_tr_b16 %1, %4 offset:%6\n\tds_read_b64_tr_b16 %2, %4 offset:%7\n\tds_read_b64_tr_b16 %3, %4 offset:%8\n\ts_waitcnt lgkmcnt(0)"
                     : "=&v"(x0), "=&v"(x1), "=&v"(x2), "=&v"(x3) : "v"(ad), "i"(i * 32), "i"(4 * 1184 + i * 32), "i"(8 * 1184 + i * 32), "i"(12 * 1184 + i * 32) : "memory");
        u32x4* o = (u32x4*)(tw + (16 * i + c) * 144 + g * 32);
        o[0] = u32x4{x0[0], x0[1], x1[0], x1[1]}; o[1] = u32x4{x2[0], x2[1], x3[0], x3[1]};
    }
    asm volatile("" ::: "memory");
    const unsigned vo = (w * 144 + (lane >> 3)) * (SL * 2) + rb * 128 + (lane & 7) * 16;
    const char* dst = p.ws + G_D_UT;
#pragma unroll
    for (int i = 0; i < 18; ++i) {
        const u32x4 a = *(const u32x4*)(tw + (8 * i + (lane >> 3)) * 144 + (lane & 7) * 16);
        const unsigned vi = vo + i * 8 * (SL * 2);
        WST16(vi, a, dst, 0);
    }
    VMC0();
    init_wait(p.ws, w, lane);
    if (tid == 0) __hip_atomic_store((gu32*)(p.ws + G_WS_CTL + G_C_UT + rb * 128), 1u, RLX_AGENT);
    { char* ws = p.ws; if (tid == 0) IST(2, rb); }
    more();
    VMC0();
    __syncthreads();
    if (tid == 0) __hip_atomic_store((gu32*)(p.ws + G_WS_CTL + G_C_UT + rb * 128), 2u, RLX_AGENT);
}
struct KPar { u16 ldt, pa[2][4]; };
__device__ __forceinline__ KPar kgen_par(const MainP& p, int d, int lane) {
    KPar k;
    k.ldt = p.logdt[d];
#pragma unroll
    for (int q = 0; q < 2; ++q) { const int i = d * NST + lane + 64 * q; k.pa[q][0] = p.logA[i]; k.pa[q][1] = p.Aim[i]; k.pa[q][2] = p.Cre[i]; k.pa[q][3] = p.Cim[i]; }
    return k;
}
__device__ __forceinline__ void task_s4d(const MainP& p, BCh& L, int d, int lane, bool first, int& nb) {
    const int g = lane >> 4, c = lane & 15;
    BST(0);
    const KPar kp = kgen_par(p, d, lane);
    const float dsk = bf2f(p.Dskip[d]);
    const auto& pa = kp.pa;
    const float dt = bfr(expf(bf2f(kp.ldt)));
#pragma unroll
    for (int q = 0; q < 2; ++q) {
        const float ar = -bfr(expf(bf2f(pa[q][0])));
        const float cr = bf2f(pa[q][2]) * dt, ci = bf2f(pa[q][3]) * dt;
        L.cst[lane + 64 * q] = f32x4{ar * dt * 1.4426950408889634f, bf2f(pa[q][1]) * dt * 0.15915494309189535f,
                                     __log2f(hypotf(cr, ci)), atan2f(ci, cr) * 0.15915494309189535f};
    }
    BST(1);
    {
        f32x4 acc[3];
#pragma unroll
        for (int v = 0; v < 3; ++v) acc[v] = f32x4{0.f, 0.f, 0.f, 0.f};
        auto base_of = [](int v, int r) { return v == 0 ? (r & 7) + 128 * (r >> 3) : v == 1 ? (r < 8 ? 256 + 2 * (r & 3) + 128 * (r >> 2) : 512 + 4 * (r & 1) + 128 * ((r - 8) >> 1))
                                                                                           : 1024 + 128 * min(r, 8); };
        const float b0 = (float)base_of(0, c), b1 = (float)base_of(1, c), b2 = (float)base_of(2, c), off = (float)(8 * c);
#define KG_POLAR(h_, l_, lg_, f_, a_, b_) do { const float m_ = __builtin_amdgcn_exp2f(fmaf(cc[0], b_, lg_)), p_ = __builtin_amdgcn_fractf(fmaf(a_, b_, f_)); \
            const float re_ = m_ * __builtin_amdgcn_cosf(p_), im_ = m_ * __builtin_amdgcn_sinf(p_);                                                            \
            h_ = pk2bf(re_, im_); l_ = pk2bf(re_ - lo2f(h_), im_ - hi2f(h_)); } while (0)
#pragma unroll 1
        for (int t = 0; t < NST / 16; ++t) {
            u32x4 fh, fl, ah[3], al[3];
#pragma unroll
            for (int j = 0; j < 4; ++j) {
                const f32x4 cc = L.cst[16 * t + 4 * g + j];
                KG_POLAR(fh[j], fl[j], 0.f, 0.f, -cc[1], off);
                KG_POLAR(ah[0][j], al[0][j], cc[2], cc[3], cc[1], b0); KG_POLAR(ah[1][j], al[1][j], cc[2], cc[3], cc[1], b1); KG_POLAR(ah[2][j], al[2][j], cc[2], cc[3], cc[1], b2);
            }
#pragma unroll
            for (int v = 0; v < 3; ++v) { acc[v] = MFMA32(al[v], fh, acc[v]); acc[v] = MFMA32(ah[v], fl, acc[v]); acc[v] = MFMA32(ah[v], fh, acc[v]); }
        }
        BST(2);
        auto split = [](float k) { const unsigned hi = pk2bf(k, 0.f) & 0xffffu; return hi | (pk2bf(0.f, k - __uint_as_float(hi << 16)) & 0xffff0000u); };
#pragma unroll
        for (int v = 0; v < 3; ++v)
#pragma unroll
            for (int i = 0; i < 4; ++i) {
                const int pos = base_of(v, 4 * g + i) + 8 * c;
                if (pos <= 2048) L.Kd[index_of_l(pos)] = split(2.0f * acc[v][i]);
            }
    }
    if (first) wait_word(p, p.ws + G_WS_CTL + G_C_UT + (lane & 31) * 128, 1, 30u);
    const char* ut = uni(p.ws + G_D_UT + (size_t)d * 4096);
    const unsigned voff = lane * 16;
    u32x4 uu[4];
    WLD16X4(uu[0], uu[1], uu[2], uu[3], voff, ut);
    const u32x4 z4 = {0u, 0u, 0u, 0u};
    *(u32x4*)&L.Kh[8 * lane] = z4; *(u32x4*)&L.Kl[8 * lane] = z4;
    *(u32x4*)&L.Kh[512 + 8 * (lane & 31)] = z4; *(u32x4*)&L.Kl[512 + 8 * (lane & 31)] = z4;
    *(u32x4*)&L.uc[0][64 + SL] = z4;
    *(u32x4*)&L.uc[(lane >> 3) & 3][8 * (lane & 7)] = z4;
    asm volatile("" ::: "memory");
#pragma unroll
    for (int q = 0; q < 4; ++q) {
        const int e = lane + 64 * q, at = (16 + (e >> 2)) * KROW + 24 - 8 * (e & 3);
        unsigned k[8];
#pragma unroll
        for (int j = 0; j < 8; ++j) k[j] = L.Kd[index_of_l(8 * e + 7 - j)];
        u32x4 kh, kl;
#pragma unroll
        for (int j = 0; j < 4; ++j) { kh[j] = prm(k[2 * j + 1], k[2 * j], 0x05040100u); kl[j] = prm(k[2 * j + 1], k[2 * j], 0x07060302u); }
        *(u32x4*)&L.Kh[at] = kh; *(u32x4*)&L.Kl[at] = kl;
    }
    __builtin_amdgcn_sched_barrier(0);
    asm volatile("s_waitcnt vmcnt(0)" : "+v"(uu[0]), "+v"(uu[1]), "+v"(uu[2]), "+v"(uu[3]) :: "memory");
    BST(3);
#pragma unroll
    for (int q = 0; q < 4; ++q) *(u32x4*)&L.uc[0][64 + 8 * (lane + 64 * q)] = uu[q];
    asm volatile("" ::: "memory");
#pragma unroll
    for (int q = 0; q < 4; ++q) {
        const int e = lane + 64 * q;
        const int ue = 56 + 8 * e;
        const u32x4 a = *(const u32x4*)&L.uc[0][ue];
        const u32x2 b = *(const u32x2*)&L.uc[0][ue + 8];
        const unsigned s0 = __builtin_amdgcn_alignbit(a[1], a[0], 16), s1 = __builtin_amdgcn_alignbit(a[2], a[1], 16), s2 = __builtin_amdgcn_alignbit(a[3], a[2], 16),
                       s3 = __builtin_amdgcn_alignbit(b[0], a[3], 16), s4 = __builtin_amdgcn_alignbit(b[1], b[0], 16);
        *(u32x4*)&L.uc[1][ue] = u32x4{s0, s1, s2, s3};
        *(u32x4*)&L.uc[2][ue] = u32x4{a[1], a[2], a[3], b[0]};
        *(u32x4*)&L.uc[3][ue] = u32x4{s1, s2, s3, s4};
    }
    if (lane == 0) {
        const u32x4 a = *(const u32x4*)&L.uc[0][56 + SL];
        const unsigned s0 = __builtin_amdgcn_alignbit(a[1], a[0], 16), s1 = __builtin_amdgcn_alignbit(a[2], a[1], 16), s2 = __builtin_amdgcn_alignbit(a[3], a[2], 16), s3 = a[3] >> 16;
        *(u32x4*)&L.uc[1][56 + SL] = u32x4{s0, s1, s2, s3};
        *(u32x4*)&L.uc[2][56 + SL] = u32x4{a[1], a[2], a[3], 0u};
        *(u32x4*)&L.uc[3][56 + SL] = u32x4{s1, s2, s3, 0u};
    }
    BST(4);
    {
        const int o = 33 + c + 8 * g;
        const unsigned ua = (unsigned)(uintptr_t)&L.uc[o & 3][o & ~3], ka = (unsigned)(uintptr_t)&L.Kh[(16 + c) * KROW + 8 * g];
        const f32x4* res = (const f32x4*)L.Kh + lane;
        asm volatile(ASM_CV :: "v"(ua), "v"(ka), "v"((unsigned)(uintptr_t)res) : CV_CLOB);
        BST(5);
#pragma unroll
        for (int par = 0; par < 2; ++par)
#pragma unroll
            for (int t = 0; t < 4; ++t) {
                const int n = 16 * (par + 2 * (16 * t + c)) + 4 * g;
                const u32x2 uu = *(const u32x2*)&L.uc[0][64 + n];
                const f32x4 y4 = res[(par * 4 + t) * 64];
                const float r0 = bfr(y4[0]) + bfr(lo2f(uu[0]) * dsk), r1 = bfr(y4[1]) + bfr(hi2f(uu[0]) * dsk);
                const float r2 = bfr(y4[2]) + bfr(lo2f(uu[1]) * dsk), r3 = bfr(y4[3]) + bfr(hi2f(uu[1]) * dsk);
                *(u32x2*)&L.uc[1][n] = u32x2{pk2bf(r0, r1), pk2bf(r2, r3)};
            }
    }
    BST(6);
    char* yb = uni(p.ws + G_D_YB + (size_t)d * 4096);
#pragma unroll
    for (int q = 0; q < 4; ++q) { const u32x4 o = *(const u32x4*)&L.uc[1][8 * (lane + 64 * q)]; WST16(voff, o, yb, q * 1024); }
    if (lane == 0) nb = (int)(__hip_atomic_fetch_add((gu32*)(p.ws + G_WS_CTL + G_C_TASK + 128), nb >= 0 ? 1u : 0u, RLX_AGENT) + 1u);
    VMC0();
    if (lane == 0) __hip_atomic_fetch_add((gu32*)(p.ws + G_WS_CTL + G_C_YC + (d >> 6) * 128), 1u, RLX_AGENT);
    BST(7);
}
__device__ __forceinline__ void s4d_wave(const MainP& p, char* smem, int w, int lane, int quota) {
    int nb = 0;
    if (lane == 0) nb = (int)(__hip_atomic_fetch_add((gu32*)(p.ws + G_WS_CTL + G_C_TASK + 128), 1u, RLX_AGENT) + 1u);
#pragma unroll 1
    for (int n = 0; n < quota; ++n) {
        const int d = __builtin_amdgcn_readfirstlane(nb);
        if (d >= DM) break;
        nb = n + 1 < quota ? 0 : -1;
        task_s4d(p, reinterpret_cast<BCh*>(smem)[w], d, lane, n == 0, nb);
    }
}
__device__ __forceinline__ void watch_role(const MainP& p, int lane) {
    const char* c = p.ws + G_WS_CTL;
#pragma unroll 1
    for (int s = G_S0; s < NSEG; ++s) {
        const int n = min(s + 3, NSEG - 1);
        wait_word(p, c + G_C_GV + lane * 128, n - (n < (int)G_S1 ? (n ^ (lane >> 5)) & 1 : 0), 60u);
        wait_word(p, c + (lane < 32 ? G_C_GX : G_C_UT - 4096) + lane * 128, lane < 32 ? min(s + 2, NSEG - 1) : s < 40 ? -1 : 2, 61u);
        if (lane == 0) __hip_atomic_store((gu32*)(p.ws + G_WS_CTL + G_C_RDY + s * 128), 1u, RLX_AGENT);
    }
}
__device__ __forceinline__ void task_merge(const MainP& p, char* smem, int gr, int i, int w, int lane) {
    const int g = lane >> 4, c = lane & 15;
    wait_word(p, p.ws + G_WS_CTL + G_C_YC + gr * 128, 63, 31u);
    const char* sb = uni(p.ws + G_D_YB + (size_t)gr * 262144 + i * 1024 + w * 256);
    const unsigned vt = g * 4096 + c * 16;
    u32x4 z[16];
#pragma unroll
    for (int j = 0; j < 16; ++j) { const char* sj = sb + j * 16384; WLD16(z[j], vt, sj, 0); }
    asm volatile("s_waitcnt vmcnt(0)" : "+v"(z[0]), "+v"(z[1]), "+v"(z[2]), "+v"(z[3]), "+v"(z[4]), "+v"(z[5]), "+v"(z[6]), "+v"(z[7]),
                 "+v"(z[8]), "+v"(z[9]), "+v"(z[10]), "+v"(z[11]), "+v"(z[12]), "+v"(z[13]), "+v"(z[14]), "+v"(z[15]) :: "memory");
    char* tw = smem + w * 17408;
#pragma unroll
    for (int j = 0; j < 16; ++j) *(u32x4*)(tw + (4 * j + g) * 272 + c * 16) = z[j];
    const unsigned ad = (unsigned)(uintptr_t)tw + (8 * g + (c >> 2)) * 272 + (c & 3) * 8;
    u16* orow = p.out + (size_t)(512 * i + 128 * w + c) * OUTW + DM + 64 * gr + 8 * g;
#pragma unroll
    for (int k = 0; k < 8; ++k) {
        u32x2 x0, x1, x2, x3;
        asm volatile("ds_read_b64_tr_b16 %0, %4 offset:%5\n\tds_read_b64_tr_b16 %1, %4 offset:%6\n\tds_read_b64_tr_b16 %2, %4 offset:%7\n\tds_read_b64_tr_b16 %3, %4 offset:%8\n\ts_waitcnt lgkmcnt(0)"
                     : "=&v"(x0), "=&v"(x1), "=&v"(x2), "=&v"(x3) : "v"(ad), "i"(k * 32), "i"(4 * 272 + k * 32), "i"(32 * 272 + k * 32), "i"(36 * 272 + k * 32) : "memory");
        *(u32x4*)(orow + (size_t)16 * k * OUTW) = u32x4{x0[0], x0[1], x1[0], x1[1]};
        *(u32x4*)(orow + (size_t)16 * k * OUTW + 32) = u32x4{x2[0], x2[1], x3[0], x3[1]};
    }
}

static constexpr int Q1 = NG1 * 6 + 36, Q2 = Q1 + (NFINE - 2) * 25;
static constexpr int NTASK = Q2 + 68;
__host__ __device__ __forceinline__ void task_of(int u, int m0, int& sg, int& r) {
    if (u < 6 * m0) { sg = u / 6; r = u % 6; }
    else if (u < 6 * m0 + 108) { sg = m0 + (u - 6 * m0) / 9; r = (u - 6 * m0) % 9; }
    else if (u < Q1) { sg = (u - 36) / 6; r = (u - 36) % 6; }
    else if (u < Q2) { sg = NG1 + (u - Q1) / 25; r = (u - Q1) % 25; }
    else { sg = 62 + (u - Q2) / 34; r = (u - Q2) % 34; }
}
__device__ __forceinline__ void worker_body(const MainP& p, char* smem, const int w, const int lane) {
    const int tid = w * 64 + lane;
    int* slot = (int*)(smem + LDS_TOTAL - 16);
    int nst = 0, nxt = -1;
    gu32* qa = (gu32*)(p.ws + G_WS_CTL + G_C_TASK);
    const int m0 = (FLH(p.prm[7]) >> 4) & 63;
    for (;;) {
        if (tid == 0) {
            *slot = nxt >= 0 ? nxt : (int)(__hip_atomic_fetch_add(qa, 1u, RLX_AGENT) + 1u);
            nxt = -1;
        }
        __syncthreads();
        const int u = __builtin_amdgcn_readfirstlane(*slot);
        if (u >= NTASK) break;
        if (tid == 0 && u < Q1 - 140) nxt = (int)(__hip_atomic_fetch_add(qa, 1u, RLX_AGENT) + 1u);
        const bool last2 = u >= Q2;
        int sg, r;
        task_of(u, m0, sg, r);
        if (u < Q1) {
            if (r < 6) task_out<6, 6, false>(p, smem, sg, 6 * r, w, lane);
            else { const int m = 3 * (sg - m0) + r - 6; task_merge(p, smem, m >> 2, m & 3, w, lane); }
        }
        else if (r < 16) task_attn_tail(p, sg, 2 * r + (w >> 1), w, lane);
        else if (last2) task_out<2, 9, true>(p, smem, sg, 2 * (r - 16), w, lane);
        else task_out<4, 9, true>(p, smem, sg, 4 * (r - 16), w, lane);
        if (last2) break;
        __syncthreads();
    }
}

__host__ __device__ constexpr unsigned phys(unsigned x) { return (x >> 13) * 16384 + (x & 8191); }
__host__ __device__ constexpr unsigned aMB(int f, int b) { return phys(f < 8 ? 16384 + f * 1024 + b : 28672 + b); }
__host__ __device__ constexpr unsigned aPX(int pr, int b) { return phys(pr * 1024 + b); }
__host__ __device__ constexpr unsigned aPM(int u, int b) { return phys(24576 + u * 1024 + b); }
static constexpr int L_KI = 8192, L_QI = 16384, L_VI = 147456;

__device__ __forceinline__ int lane_id() { int l; asm volatile("v_mbcnt_lo_u32_b32 %0, -1, 0\n\tv_mbcnt_hi_u32_b32 %0, -1, %0" : "=v"(l)); return l; }
typedef const __attribute__((address_space(4))) MainP* KArg;

__global__ __launch_bounds__(256, 1) void main_kernel(MainP p_) {
    __shared__ __attribute__((aligned(16))) char smem[LDS_TOTAL];
    KArg ka = (KArg)__builtin_amdgcn_kernarg_segment_ptr();
    static_assert(offsetof(MainP, W) == 0x28, "argument layout");
    asm volatile(ASM_EA :: "s"(ka), "s"(blockIdx.x), "v"(threadIdx.x) : "v1", "v2", "s40", "s41", "s42", "s43", "s44", "s45", "s46", "s47", "s48", "s49", "scc", "memory");
    asm volatile("" : "+s"(ka));
    const int b = blockIdx.x, x = b & 7, ix = b >> 3;
    const int w = __builtin_amdgcn_readfirstlane(threadIdx.x >> 6);
    const bool isr = x < 4 && ix >= 16;
    constexpr int NRF = 8 + 8 * G_GVN;
    if (__builtin_expect(ix < (x < 4 ? 16 : NRF), 1)) {
        const int h = (x & 3) * 8 + (ix & 7), id = h + (ix >> 4) * 32;
        const unsigned stb = 0;
#define UNR _Pragma("unroll")
#define CW(k, val) do { if ((k) < 16) ca[(k) & 15] = (unsigned)(val); else if ((k) < 32) cb[(k) & 15] = (unsigned)(val); else cl[((k) - 40) & 15] = (unsigned)(val); } while (0)
#define ROLEW(A_) asm volatile(A_ : "+{v[0:15]}"(ca), "+{v[16:31]}"(cb), "+{v[200:215]}"(cl) : "s"(ka), "s"(w), "s"(id), "s"(stb) : ASM_CLOBW)
#define PAD(n_) asm volatile("s_branch skip_" n_ "\npad_" n_ ":\n\ts_setpc_b64 s[46:47]\nskip_" n_ ":")
#define CW_ALL const int lane = lane_id(), g = lane >> 4, c = lane & 15, q = c >> 2; u32x16 ca = {}, cb = {}, cl = {};                                      \
        CW(0, lane * 16); CW(40, (4 * w + g) * 8192 + h * 256 + (c ^ (2 * g + 8 * (w >> 1))) * 16);                                                          \
        UNR for (int k = 0; k < 2; ++k) CW(41 + k, (8 * g + q) * 256 + ((4 * w + 2 * k + ((c >> 1) & 1)) ^ (2 * q + 8 * (g & 1))) * 16 + (c & 1) * 8)
#define CW_SRC(b_, key_) UNR for (int k = 0; k < 9; ++k) { const int pp = 64 * k + lane, r = pp / 72, pc = pp - 72 * r; CW(43 + k, (b_) + (8 * w + r) * 1152 + (pc ^ (key_)) * 16); }
#define UNIT(u_, key_) (((u_) >> 2) * 128 + (((u_) & 3) ^ (key_)) * 32 + (c & 3) * 8)
#define CW_NEAR CW(1, x * 65536 + aMB(2 * w, lane * 16)); CW(2, x * 65536 + aMB(8, w * 256 + lane * 4)); CW(3, 2 * w * 1024 + lane * 16);                            \
        UNR for (int i = 0; i < 6; ++i) CW(4 + i, ((2 * w + 2 + i) & 7) * 1024 + lane * 16);                                                                 \
        CW(10, (c & 2) ? 0xEEEE : 0x4444);                                                                                                                   \
        CW(20, x * 65536 + 22528 + (lane & 31) * 64); CW(21, lane < 32 ? (lane & 3) * 65536 + phys((lane >> 2) * 2048) + h * 64 : 32768 + h * 128);          \
        CW(22, lane * 4); CW(23, x * 65536 + aMB(2 * w + 1, lane * 16))
#define CW_FAR CW(0, c * 1152 + (g ^ (c >> 1)) * 16); CW(1, ca[0]); CW(2, ca[0] ^ 64); CW(3, w * 9216 + lane * 16); CW_SRC(0, (r >> 1) + 4 * (w & 1))                                   \
        CW(4, L_VI + w * 2048 + lane * 16); CW(5, L_VI + lane * 16); CW(6, lane * 4)
        if (x >= 4 && ix >= 8) {
            PAD("gv"); CW_ALL; CW_FAR;
            UNR for (int t = 0; t < 9; ++t) CW(8 + t, (9 * w + t) * G_UXS + skew(0, 9 * w + t) * 32768 + h * 1024 + lane * 16);
            UNR for (int k = 0; k < 2; ++k) CW(19 + k, (9 * w + 4 * k + g) * 16384 + h * 256 + c * 16);
            CW(21, (9 * w + 8) * 16384 + h * 256 + lane * 4);
            UNR for (int k = 0; k < 4; ++k) CW(52 + k, (4 * g + q) * 1152 + ((9 * w + k) >> 2) * 128 + ((2 * ((9 * w + k) & 3) + ((c >> 1) & 1)) ^ (2 * g + (q >> 1))) * 16 + (c & 1) * 8);
            ROLEW(ASM_GV);
        } else if (x >= 4) {
            PAD("gx"); CW_ALL; CW_FAR;
            CW(8, w * 2048 + lane * 16);
            ROLEW(ASM_GX);
        } else if (ix < 8) {
            PAD("qk"); CW_ALL; CW_NEAR;
            CW(12, L_QI + (16 * w + (c & 13)) * 32 + 8 * g + 4 * ((c >> 1) & 1));
            CW(13, L_QI + (8 * g + q) * 32 + (c & 3) * 8);
            CW(14, G_WS_CTL + G_C_KM + h * 128); CW(15, lane * 16 + 4096);
            CW(16, L_KI + (32 * w + c) * 16 + 4 * g);
            CW(17, L_KI + (4 * g + q) * 16 + (c & 1) * 8);
            const int mm = 8 * (w & 1) + 4 * (g >> 1) + (g & 1);
            CW(18, (x ^ 3) * 65536 + aPX(h >> 1, (h & 1) * 512 + (w & 1) * 256 + g * 64 + (c >> 3) * 32 + ((c >> 1) & 1) * 16 + (2 * (q & 1) + (c & 1)) * 4));
            CW(19, ((c & 1) + 2 * (c >> 3)) * 65536 + aPM(h >> 3, ((h >> 1) & 3) * 256 + (mm + 2 * ((c >> 1) & 1)) * 16 + (h & 1) * 8 + (q & 1) * 4));
            CW(24, G_WS_CTL + G_C_GX + h * 128);
            ROLEW(ASM_QK);
        } else {
            PAD("vv"); CW_ALL; CW_NEAR; CW_SRC(h * 147456, r & 4);
            const int tq = 9 * w + 2 * (g & 1) + (g >> 1);
            CW(14, tq * 16384 + h * 256 + c * 16);
            CW(15, (tq + 4) * 16384 + h * 256 + c * 16);
            CW(16, (9 * w + 8) * 16384 + h * 256 + c * 16 + (g & 1) * 8);
            CW(19, (c & 8) ? 0xEEEE : 0x4444);
            CW(12, L_QI + w * 576 + c * 16 + 4 * g);
            CW(13, L_QI + (8 * g + q) * 16 + (c & 1) * 8 + ((c >> 1) & 1) * 576);
            CW(25, L_QI + (8 * g + q) * 16 + (c & 1) * 8 + (1 - ((c >> 1) & 1)) * 576);
            UNR for (int k = 0; k < 4; ++k) CW(52 + k, ((k >> 1) ^ (q >> 1)) * 36864 + (4 * g + (q & 1)) * 1152 + UNIT(9 * w + 2 * (k & 1) + (q >> 1), 2 * (g & 1)));
            CW(24, (q >> 1) * 36864 + (4 * g + (q & 1)) * 1152 + UNIT(9 * w + 8, 2 * (g & 1)));
            ROLEW(ASM_V);
        }
    } else {
    if (isr && (ix < 25 || ix >= 29)) {
        const int id = x * 12 + (ix < 25 ? ix - 13 : ix - 29);
        init_cal(ka->ws, id, threadIdx.x, (FLG(ka->prm[5]) >> 16) & 15);
        if (FLG(ka->prm[5]) & 4) {
            const unsigned o = (id * 384 + threadIdx.x) * 128, o2 = o + (threadIdx.x & 128 ? 16384 : 32768);
            const char* a = (const char*)ka->X + o; const char* b2 = (const char*)ka->U + o;
            const char* c2 = (const char*)(w < 2 ? ka->X : ka->U) + o2;
            const char* ps = (const char*)(w == 0 ? ka->logA : w == 1 ? ka->Aim : w == 2 ? ka->Cre : ka->Cim) + id * 3072 + min((int)(threadIdx.x & 63), 23) * 128;
            asm volatile("s_mov_b32 m0, %4\n\ts_nop 0\n\tglobal_load_lds_dword %0, off\n\tglobal_load_lds_dword %1, off\n\tglobal_load_lds_dword %2, off\n\tglobal_load_lds_dword %3, off"
                         :: "v"(a), "v"(c2), "v"(b2), "v"(ps), "s"(163072) : "memory");
        }
    }
    if (x >= 4 ? ix >= 24 : (ix >= 25 && ix < 29)) init_fill(ka->ws, ka->mem0, x >= 4 ? (ix - 24) * 4 + (x & 3) : 32 + (ix - 25) * 4 + x, threadIdx.x);
    if (x >= 4 && !(FLG(ka->prm[5]) & 1)) {
        const char* src = (const char*)ka->W[3] + (size_t)((x & 3) * 8 + (ix & 7)) * 147456 + threadIdx.x * 128;
#pragma unroll
        for (int i = 0; i < 5; ++i)
            if (i < 4 || w < 2) asm volatile("s_mov_b32 m0, %1\n\ts_nop 0\n\tglobal_load_lds_dword %0, off" :: "v"(src + i * 32768), "s"(155648) : "memory");
    }
    if (isr && ix >= 25) {
        for (int i = 0; i < (ix < 29 ? 3 : 0) + HSL; ++i) __builtin_amdgcn_s_sleep(40);
        init_wait(ka->ws, w, threadIdx.x & 63);
    }
    asm volatile("" : "+s"(ka));
    if (x < 4 && (ix < 25 || w == 0)) {
        const int lane = threadIdx.x & 63, g = lane >> 4, c = lane & 15;
        const unsigned lb = w * G_STG;
        unsigned* ct = (unsigned*)(smem + lb) + lane;
        asm volatile("" :: "v"(ct) : "memory");
#define CTW(k, val) ct[(k) * 64] = (unsigned)(val)
        const u64 ws = (u64)ka->ws, prm = (u64)ka;
        const u64 stb = 0;
        CTW(0, lane * 16);
        const int j = x * 9 + ix - 16;
        if (ix >= 25) {
            CTW(1, x * 65536 + 22528 + (lane & 31) * 64);
            for (int k = 0; k < 7; ++k) {
                const int q = min(ix - 25 + 7 * k, 43), id = q < 36 ? 9 * x + (q >> 2) : 8 * x + q - 36;
                CTW(4 + k, (q < 36 ? G_D_UX + id * G_UXS + skew(0, id) * 32768 + (q & 3) * 8192 : G_D_KX + id * G_HS8 + skew(0, id) * 8192) + lane * 128);
                CTW(11 + k, q < 36 ? 32768 : 8192);
            }
        } else {
            const int rot = (FLG(ka->prm[5]) & 16) ? 0 : (x ^ 3) * 4;
            if (w >= 2) {
                return;
            }
            CTW(1, lane * 16 + 4096);
            CTW(2, x * 65536 + aMB(j >> 2, (j & 3) * 256 + (4 * g + 2 * ((c >> 1) & 1) + (c & 1)) * 16 + (c >> 2) * 4));
            for (int k = 0; k < 5; ++k) { const int q = 9 * k + ix - 16; CTW(46 + k, x * 65536 + lane * 16 + (q < 29 ? phys(q * 1024) : 0x200000 + phys((q - 29) * 1024))); }
            CTW(4, (c & 2) ? 0xEEEE : 0x4444); CTW(5, (c & 1) ? 0x03020706u : 0x05040100u);
            CTW(6, G_WS_CTL + G_C_GV + ((w & (G_GVN - 1)) * 32 + ((lane & 31) ^ (2 * rot))) * 128); CTW(7, x * 65536 + (lane & 31) * 64); CTW(8, lane * 4); CTW(9, 131072);
            for (int k = 0; k < 16; ++k) { const int i = k ^ rot; CTW(10 + k, x * 65536 + aPX(i, (g >> 1) * 512 + (2 * (c >> 2) + (c & 1)) * 64 + (g & 1) * 32 + ((c >> 1) & 1) * 16)); CTW(26 + k, i * 2048 + lane * 16); }
            for (int k = 0; k < 4; ++k) CTW(42 + k, x * 65536 + aPM(k, lane * 16));
        }
        const int id = ix >= 25 ? ix - 25 : j;
#define ROLE(A_) asm volatile(A_ :: "s"(ws), "s"(ws), "s"(ws), "s"(w), "s"(id), "s"(ws), "s"(prm), "s"(lb), "s"(stb) : ASM_CLOB)
        if (ix >= 25) ROLE(ASM_H);
        else ROLE(ASM_R);
    }
    else if (x >= 4 && ix >= 24) {
        if (ix >= 25) for (int i = 0; i < ((FLG(ka->prm[5]) >> 5) & 7); ++i) __builtin_amdgcn_s_sleep(36);
        const char* xs = (const char*)ka->X + (size_t)((ix - 24) * 4 + (x & 3)) * 73728 + threadIdx.x * 128;
        const int f = (ix - 24) * 4 + (x & 3);
        const u16* ps = (w == 0 ? ka->logA : w == 1 ? ka->Aim : w == 2 ? ka->Cre : ka->Cim) + f * 2304 + min((int)(threadIdx.x & 63), 35) * 64;
        if (ix < 25 || !(FLG(ka->prm[5]) & 4))
        asm volatile("s_mov_b32 m0, %4\n\ts_nop 0\n\tglobal_load_lds_dword %0, off\n\tglobal_load_lds_dword %1, off\n\tglobal_load_lds_dword %2, off\n\tglobal_load_lds_dword %3, off"
                     :: "v"(xs), "v"(xs + 32768), "v"(xs + 65536 - w * 8192), "v"(ps), "s"(163072) : "memory");
        if (ix < 25) for (int i = 0; i < ((FLG(ka->prm[5]) >> 5) & 7); ++i) __builtin_amdgcn_s_sleep(36);
    }
    }
    asm volatile("" : "+s"(ka));
    if (ix >= 24) {
        unsigned co = (w * 64 + lane_id()) * 128 + (x < 4 ? -8192 : 0);
        asm volatile("s_getpc_b64 s[44:45]\ncp2_%=:\n\ts_mov_b32 m0, 163072\n\tv_min_u32 %0, code_end_marker-cp2_%=, %0\n\tglobal_load_lds_dword %0, s[44:45]\n\t"
                     "v_add_u32 %0, 0x8000, %0\n\tv_min_u32 %0, code_end_marker-cp2_%=, %0\n\tglobal_load_lds_dword %0, s[44:45]" : "+v"(co) :: "s44", "s45", "memory");
    }
    MainP p;
    p.prm[5] = ka->prm[5]; p.prm[7] = ka->prm[7];
    p.U = ka->U; p.ws = ka->ws; p.logA = ka->logA; p.Aim = ka->Aim; p.Cre = ka->Cre; p.Cim = ka->Cim; p.logdt = ka->logdt; p.Dskip = ka->Dskip; p.out = ka->out;
    if (x >= 4 && ix >= 24) {
        const u32x4 sent = {0xffffffffu, 0xffffffffu, 0xffffffffu, 0xffffffffu};
        const int tid = w * 64 + lane_id(), h = (ix - 24) * 4 + (x & 3);
        task_ut(p, smem, h, w, lane_id(), [&] {
#pragma unroll 1
            for (int sg = NG1; sg < 64; ++sg) {
                const unsigned ok = G_D_KM + h * G_HS4 + skew(sg, h) * 4096 + tid * 16;
                const unsigned op = tid < 128 ? G_D_PX + (sg * 32 + h) * 2048 + tid * 16 : G_D_PM + sg * 16384 + ((tid >> 4) & 1) * 8192 + h * 256 + (tid & 15) * 16;
                asm volatile("global_store_dwordx4 %0, %2, %3 sc1\n\tglobal_store_dwordx4 %1, %2, %3 sc1\n\ts_nop 1" :: "v"(ok), "v"(op), "v"(sent), "s"(p.ws) : "memory");
            }
        });
    }
    {
        const int nq = x < 4 ? ((FLH(p.prm[7]) & 2) ? 0 : 1000) : ix < 8 ? (FLG(p.prm[5]) >> 8) & 3 : ix < 16 ? (FLG(p.prm[5]) >> 10) & 3 : (ix & 2) && ((31 - ix) >> 2) < NAG ? 0 : ix < 24 ? (FLG(p.prm[5]) >> 12) & 3 : (FLG(p.prm[5]) >> 14) & 3;
        if (b == 239 && w == 0) watch_role(p, lane_id());
        else if (nq || b == 239) {
            for (int i = 0; i < (int)(((ix & 7) * 4 + w) * ((FLG(p.prm[5]) >> 23) & 7)); ++i) __builtin_amdgcn_s_sleep(3);
            s4d_wave(p, smem, w, lane_id(), nq == 3 || b == 239 ? 1000 : nq);
        }
        if (x < 4) return;
        __syncthreads();
    }
    if ((ix & 18) == 18 && ((31 - ix) >> 2) < NAG) { attn_role(p, ((31 - ix) >> 2) + (ix < 24 ? NA : 0), (x & 3) * 8 + (ix & 1) * 4 + w, w, lane_id());
        if (b == 255) {
            wait_word(p, p.ws + G_WS_CTL + G_C_ARR, 255, 50u);
            if (w == 0 && lane_id() == 0) __hip_atomic_fetch_add((gu32*)(p.ws + G_WS_EP), 1u, RLX_AGENT);
        }
        __syncthreads();
    }
    p.prm[0] = near_mask(p.ws, lane_id());
    worker_body(p, smem, w, lane_id());
}

extern "C" __global__ void code_end_marker() {}

static constexpr int g_prm[8] = {PRM_DEFAULT};

extern "C" void kernel_launch(void* const* d_in, const int* in_sizes, int n_in, void* d_out, int out_size,
                              void* d_ws, size_t ws_size, hipStream_t stream) {
    char* ws = (char*)(((uintptr_t)d_ws + 0x1FFFFF) & ~(uintptr_t)0x1FFFFF);
    MainP p;
    for (int i = 0; i < 8; ++i) p.prm[i] = g_prm[i];
    p.mem0 = (const u16*)d_in[1];
    for (int i = 0; i < 4; ++i) p.W[i] = (const u16*)d_in[2 + i];
    p.X = (const u16*)d_in[0]; p.U = (const u16*)d_in[7]; p.ws = ws;
    p.logA = (const u16*)d_in[8]; p.Aim = (const u16*)d_in[9]; p.Cre = (const u16*)d_in[10]; p.Cim = (const u16*)d_in[11];
    p.logdt = (const u16*)d_in[12]; p.Dskip = (const u16*)d_in[13]; p.out = (u16*)d_out;
    main_kernel<<<256, 256, 0, stream>>>(p);
}
```

```cpp
#include <hip/hip_runtime.h>
#include <hip/hip_bf16.h>
#include <stdint.h>
#include <stddef.h>

typedef __attribute__((ext_vector_type(8))) __bf16 bf16x8;
typedef __attribute__((ext_vector_type(4))) short s16x4;
typedef __attribute__((ext_vector_type(4))) float f32x4;
typedef __attribute__((ext_vector_type(4))) unsigned u32x4;
typedef __attribute__((ext_vector_type(2))) unsigned u32x2;
typedef __attribute__((ext_vector_type(2))) float f32x2;
typedef __attribute__((ext_vector_type(16))) unsigned u32x16;
typedef unsigned short u16;
typedef unsigned long long u64;

#define GAS __attribute__((address_space(1)))
typedef GAS unsigned gu32;
#define RLX_AGENT __ATOMIC_RELAXED, __HIP_MEMORY_SCOPE_AGENT

static constexpr int DM = 576, NH = 32, DH = 128, MM = 8, SEG = 32, NSEG = 64, SL = 2048;
static constexpr int HD = NH * DH;
static constexpr int OUTW = 2 * DM;
static constexpr int NST = 128;

__device__ __forceinline__ float bf2f(u16 h) { return __uint_as_float(((unsigned)h) << 16); }
typedef __attribute__((ext_vector_type(2))) __bf16 bf16x2_t;
__device__ __forceinline__ unsigned pk2bf(float a, float b) {
    bf16x2_t v; v[0] = (__bf16)a; v[1] = (__bf16)b;
    return __builtin_bit_cast(unsigned, v);
}
__device__ __forceinline__ float bfr(float f) { return __uint_as_float(pk2bf(f, f) << 16); }
__device__ __forceinline__ float lo2f(unsigned u) { return __uint_as_float(u << 16); }
__device__ __forceinline__ float hi2f(unsigned u) { return __uint_as_float(u & 0xffff0000u); }
__device__ __forceinline__ float rowmax4(float x) {
    auto a = __builtin_amdgcn_permlane16_swap(__float_as_uint(x), __float_as_uint(x), false, false);
    x = fmaxf(__uint_as_float(a[0]), __uint_as_float(a[1]));
    auto b = __builtin_amdgcn_permlane32_swap(__float_as_uint(x), __float_as_uint(x), false, false);
    return fmaxf(__uint_as_float(b[0]), __uint_as_float(b[1]));
}
__device__ __forceinline__ float rowsum4(float x) {
    auto a = __builtin_amdgcn_permlane16_swap(__float_as_uint(x), __float_as_uint(x), false, false);
    x = __uint_as_float(a[0]) + __uint_as_float(a[1]);
    auto b = __builtin_amdgcn_permlane32_swap(__float_as_uint(x), __float_as_uint(x), false, false);
    return __uint_as_float(b[0]) + __uint_as_float(b[1]);
}

struct MainP {
    int prm[8];
    const u16* mem0; const u16* W[4]; const u16* X; const u16* U;
    char* ws;
    const u16* logA; const u16* Aim; const u16* Cre; const u16* Cim; const u16* logdt; const u16* Dskip;
    u16* out;
};
static constexpr int LDS_TOTAL = 163840;
static constexpr unsigned kWaitCap = 1u << 18;
#define VMC0() asm volatile("s_waitcnt vmcnt(0)" ::: "memory")
#define PRM_DEFAULT 1060, 600, 120, 680, 752, 1317806283, 3145984, 3777
#define ASM_QK \
    "s_load_dwordx2 s[42:43], %3, 40\n\t" \
    "s_load_dwordx2 s[44:45], %3, 48\n\t" \
    "s_load_dwordx2 s[36:37], %3, 0x58\n\t" \
    "s_load_dwordx2 s[50:51], %3, 0x48\n\t" \
    "s_load_dwordx8 s[84:91], %3, 0\n\t" \
    "s_mov_b32 s66, %4\n\t" \
    "s_mov_b32 s67, %5\n\t" \
    "s_mov_b32 s68, 262144\n\t" \
    "s_mov_b32 s72, 0\n\t" \
    "s_mov_b32 s83, -1\n\t" \
    "s_mov_b32 s65, 0\n\t" \
    "s_lshl_b32 s82, s66, 10\n\t" \
    "s_waitcnt lgkmcnt(0)\n\t" \
    "s_add_u32 s46, s42, 2097152\n\t" \
    "s_addc_u32 s47, s43, 0\n\t" \
    "s_add_u32 m0, s82, 65536\n\t" \
    "s_nop 0\n\t" \
    "global_load_lds_dwordx4 v200, s[46:47]\n\t" \
    "s_add_u32 s46, s42, 2228224\n\t" \
    "s_addc_u32 s47, s43, 0\n\t" \
    "s_add_u32 m0, s82, 69632\n\t" \
    "s_nop 0\n\t" \
    "global_load_lds_dwordx4 v200, s[46:47]\n\t" \
    "s_add_u32 s46, s42, 2359296\n\t" \
    "s_addc_u32 s47, s43, 0\n\t" \
    "s_add_u32 m0, s82, 73728\n\t" \
    "s_nop 0\n\t" \
    "global_load_lds_dwordx4 v200, s[46:47]\n\t" \
    "s_add_u32 s46, s42, 2490368\n\t" \
    "s_addc_u32 s47, s43, 0\n\t" \
    "s_add_u32 m0, s82, 77824\n\t" \
    "s_nop 0\n\t" \
    "global_load_lds_dwordx4 v200, s[46:47]\n\t" \
    "s_add_u32 s46, s42, 2621440\n\t" \
    "s_addc_u32 s47, s43, 0\n\t" \
    "s_add_u32 m0, s82, 81920\n\t" \
    "s_nop 0\n\t" \
    "global_load_lds_dwordx4 v200, s[46:47]\n\t" \
    "s_add_u32 s46, s42, 2752512\n\t" \
    "s_addc_u32 s47, s43, 0\n\t" \
    "s_add_u32 m0, s82, 86016\n\t" \
    "s_nop 0\n\t" \
    "global_load_lds_dwordx4 v200, s[46:47]\n\t" \
    "s_add_u32 s46, s42, 2883584\n\t" \
    "s_addc_u32 s47, s43, 0\n\t" \
    "s_add_u32 m0, s82, 90112\n\t" \
    "s_nop 0\n\t" \
    "global_load_lds_dwordx4 v200, s[46:47]\n\t" \
    "s_add_u32 s46, s42, 3014656\n\t" \
    "s_addc_u32 s47, s43, 0\n\t" \
    "s_add_u32 m0, s82, 94208\n\t" \
    "s_nop 0\n\t" \
    "global_load_lds_dwordx4 v200, s[46:47]\n\t" \
    "s_add_u32 s46, s42, 3145728\n\t" \
    "s_addc_u32 s47, s43, 0\n\t" \
    "s_add_u32 m0, s82, 98304\n\t" \
    "s_nop 0\n\t" \
    "global_load_lds_dwordx4 v200, s[46:47]\n\t" \
    "s_add_u32 s46, s42, 3276800\n\t" \
    "s_addc_u32 s47, s43, 0\n\t" \
    "s_add_u32 m0, s82, 102400\n\t" \
    "s_nop 0\n\t" \
    "global_load_lds_dwordx4 v200, s[46:47]\n\t" \
    "s_add_u32 s46, s42, 3407872\n\t" \
    "s_addc_u32 s47, s43, 0\n\t" \
    "s_add_u32 m0, s82, 106496\n\t" \
    "s_nop 0\n\t" \
    "global_load_lds_dwordx4 v200, s[46:47]\n\t" \
    "s_add_u32 s46, s42, 3538944\n\t" \
    "s_addc_u32 s47, s43, 0\n\t" \
    "s_add_u32 m0, s82, 110592\n\t" \
    "s_nop 0\n\t" \
    "global_load_lds_dwordx4 v200, s[46:47]\n\t" \
    "s_add_u32 s46, s42, 3670016\n\t" \
    "s_addc_u32 s47, s43, 0\n\t" \
    "s_add_u32 m0, s82, 114688\n\t" \
    "s_nop 0\n\t" \
    "global_load_lds_dwordx4 v200, s[46:47]\n\t" \
    "s_add_u32 s46, s42, 3801088\n\t" \
    "s_addc_u32 s47, s43, 0\n\t" \
    "s_add_u32 m0, s82, 118784\n\t" \
    "s_nop 0\n\t" \
    "global_load_lds_dwordx4 v200, s[46:47]\n\t" \
    "s_add_u32 s46, s42, 3932160\n\t" \
    "s_addc_u32 s47, s43, 0\n\t" \
    "s_add_u32 m0, s82, 122880\n\t" \
    "s_nop 0\n\t" \
    "global_load_lds_dwordx4 v200, s[46:47]\n\t" \
    "s_add_u32 s46, s42, 4063232\n\t" \
    "s_addc_u32 s47, s43, 0\n\t" \
    "s_add_u32 m0, s82, 126976\n\t" \
    "s_nop 0\n\t" \
    "global_load_lds_dwordx4 v200, s[46:47]\n\t" \
    "s_add_u32 s46, s42, 4194304\n\t" \
    "s_addc_u32 s47, s43, 0\n\t" \
    "s_add_u32 m0, s82, 131072\n\t" \
    "s_nop 0\n\t" \
    "global_load_lds_dwordx4 v200, s[46:47]\n\t" \
    "s_add_u32 s46, s42, 4325376\n\t" \
    "s_addc_u32 s47, s43, 0\n\t" \
    "s_add_u32 m0, s82, 135168\n\t" \
    "s_nop 0\n\t" \
    "global_load_lds_dwordx4 v200, s[46:47]\n\t" \
    "s_add_u32 s46, s42, 4456448\n\t" \
    "s_addc_u32 s47, s43, 0\n\t" \
    "s_add_u32 m0, s82, 139264\n\t" \
    "s_nop 0\n\t" \
    "global_load_lds_dwordx4 v200, s[46:47]\n\t" \
    "s_add_u32 s46, s42, 4587520\n\t" \
    "s_addc_u32 s47, s43, 0\n\t" \
    "s_add_u32 m0, s82, 143360\n\t" \
    "s_nop 0\n\t" \
    "global_load_lds_dwordx4 v200, s[46:47]\n\t" \
    "s_mov_b32 s77, 3019898880\n\t" \
    "s_mov_b32 s78, 3054198966\n\t" \
    "s_waitcnt lgkmcnt(0)\n\t" \
    "s_mov_b32 s70, 116\n\t" \
    "qk_E1_%=:\n\t" \
    "s_memtime s[56:57]\n\t" \
    "s_memrealtime s[2:3]\n\t" \
    "s_nop 0\n\t" \
    "s_memrealtime s[4:5]\n\t" \
    "s_nop 0\n\t" \
    "s_memrealtime s[6:7]\n\t" \
    "s_nop 0\n\t" \
    "s_memrealtime s[8:9]\n\t" \
    "s_nop 0\n\t" \
    "s_memrealtime s[10:11]\n\t" \
    "s_nop 0\n\t" \
    "s_memrealtime s[12:13]\n\t" \
    "s_nop 0\n\t" \
    "s_memrealtime s[14:15]\n\t" \
    "s_nop 0\n\t" \
    "s_memrealtime s[16:17]\n\t" \
    "s_nop 0\n\t" \
    "s_memrealtime s[18:19]\n\t" \
    "s_nop 0\n\t" \
    "s_memrealtime s[20:21]\n\t" \
    "s_nop 0\n\t" \
    "s_memrealtime s[22:23]\n\t" \
    "s_nop 0\n\t" \
    "s_memrealtime s[24:25]\n\t" \
    "s_nop 0\n\t" \
    "s_memrealtime s[26:27]\n\t" \
    "s_nop 0\n\t" \
    "s_memrealtime s[28:29]\n\t" \
    "s_memtime s[54:55]\n\t" \
    "s_add_u32 s70, s70, 8\n\t" \
    "s_waitcnt lgkmcnt(0)\n\t" \
    "s_sub_u32 s69, s54, s56\n\t" \
    "s_cmp_gt_u32 s69, s70\n\t" \
    "s_cbranch_scc1 qk_E1_%=\n\t" \
    "s_add_u32 s2, s2, s4\n\t" \
    "s_add_u32 s6, s6, s8\n\t" \
    "s_add_u32 s10, s10, s12\n\t" \
    "s_add_u32 s14, s14, s16\n\t" \
    "s_add_u32 s18, s18, s20\n\t" \
    "s_add_u32 s22, s22, s24\n\t" \
    "s_add_u32 s2, s2, s6\n\t" \
    "s_add_u32 s10, s10, s14\n\t" \
    "s_add_u32 s18, s18, s22\n\t" \
    "s_add_u32 s2, s2, s26\n\t" \
    "s_add_u32 s10, s10, s18\n\t" \
    "s_add_u32 s2, s2, s10\n\t" \
    "s_mul_i32 s70, s28, 13\n\t" \
    "s_sub_u32 s70, s70, s2\n\t" \
    "s_min_u32 s70, s70, 52\n\t" \
    "s_mul_i32 s69, s69, s70\n\t" \
    "s_mul_hi_u32 s69, s69, 76695844\n\t" \
    "s_mov_b32 s75, s73\n\t" \
    "s_mov_b32 s76, s74\n\t" \
    "s_add_u32 s73, s56, s69\n\t" \
    "s_lshl_b32 s74, s28, 4\n\t" \
    "s_waitcnt vmcnt(32) lgkmcnt(0)\n\t" \
    "s_barrier\n\t" \
    "ds_read_b64_tr_b16 a[0:1], v201 offset:0\n\t" \
    "ds_read_b64_tr_b16 a[2:3], v201 offset:1024\n\t" \
    "ds_read_b64_tr_b16 a[4:5], v201 offset:8192\n\t" \
    "ds_read_b64_tr_b16 a[6:7], v201 offset:9216\n\t" \
    "ds_read_b64_tr_b16 a[72:73], v202 offset:0\n\t" \
    "ds_read_b64_tr_b16 a[74:75], v202 offset:1024\n\t" \
    "ds_read_b64_tr_b16 a[76:77], v202 offset:8192\n\t" \
    "ds_read_b64_tr_b16 a[78:79], v202 offset:9216\n\t" \
    "s_waitcnt vmcnt(28) lgkmcnt(0)\n\t" \
    "s_barrier\n\t" \
    "ds_read_b64_tr_b16 a[8:9], v201 offset:16384\n\t" \
    "ds_read_b64_tr_b16 a[10:11], v201 offset:17408\n\t" \
    "ds_read_b64_tr_b16 a[12:13], v201 offset:24576\n\t" \
    "ds_read_b64_tr_b16 a[14:15], v201 offset:25600\n\t" \
    "ds_read_b64_tr_b16 a[80:81], v202 offset:16384\n\t" \
    "ds_read_b64_tr_b16 a[82:83], v202 offset:17408\n\t" \
    "ds_read_b64_tr_b16 a[84:85], v202 offset:24576\n\t" \
    "ds_read_b64_tr_b16 a[86:87], v202 offset:25600\n\t" \
    "s_add_u32 s46, s44, 0\n\t" \
    "s_addc_u32 s47, s45, 0\n\t" \
    "s_add_u32 m0, s82, 0\n\t" \
    "s_nop 0\n\t" \
    "global_load_lds_dwordx4 v200, s[46:47]\n\t" \
    "s_add_u32 s46, s44, 131072\n\t" \
    "s_addc_u32 s47, s45, 0\n\t" \
    "s_add_u32 m0, s82, 4096\n\t" \
    "s_nop 0\n\t" \
    "global_load_lds_dwordx4 v200, s[46:47]\n\t" \
    "s_add_u32 s46, s44, 262144\n\t" \
    "s_addc_u32 s47, s45, 0\n\t" \
    "s_add_u32 m0, s82, 8192\n\t" \
    "s_nop 0\n\t" \
    "global_load_lds_dwordx4 v200, s[46:47]\n\t" \
    "s_add_u32 s46, s44, 393216\n\t" \
    "s_addc_u32 s47, s45, 0\n\t" \
    "s_add_u32 m0, s82, 12288\n\t" \
    "s_nop 0\n\t" \
    "global_load_lds_dwordx4 v200, s[46:47]\n\t" \
    "s_waitcnt vmcnt(28) lgkmcnt(0)\n\t" \
    "s_barrier\n\t" \
    "ds_read_b64_tr_b16 a[16:17], v201 offset:32768\n\t" \
    "ds_read_b64_tr_b16 a[18:19], v201 offset:33792\n\t" \
    "ds_read_b64_tr_b16 a[20:21], v201 offset:40960\n\t" \
    "ds_read_b64_tr_b16 a[22:23], v201 offset:41984\n\t" \
    "ds_read_b64_tr_b16 a[88:89], v202 offset:32768\n\t" \
    "ds_read_b64_tr_b16 a[90:91], v202 offset:33792\n\t" \
    "ds_read_b64_tr_b16 a[92:93], v202 offset:40960\n\t" \
    "ds_read_b64_tr_b16 a[94:95], v202 offset:41984\n\t" \
    "s_add_u32 s46, s44, 524288\n\t" \
    "s_addc_u32 s47, s45, 0\n\t" \
    "s_add_u32 m0, s82, 16384\n\t" \
    "s_nop 0\n\t" \
    "global_load_lds_dwordx4 v200, s[46:47]\n\t" \
    "s_add_u32 s46, s44, 655360\n\t" \
    "s_addc_u32 s47, s45, 0\n\t" \
    "s_add_u32 m0, s82, 20480\n\t" \
    "s_nop 0\n\t" \
    "global_load_lds_dwordx4 v200, s[46:47]\n\t" \
    "s_add_u32 s46, s44, 786432\n\t" \
    "s_addc_u32 s47, s45, 0\n\t" \
    "s_add_u32 m0, s82, 24576\n\t" \
    "s_nop 0\n\t" \
    "global_load_lds_dwordx4 v200, s[46:47]\n\t" \
    "s_add_u32 s46, s44, 917504\n\t" \
    "s_addc_u32 s47, s45, 0\n\t" \
    "s_add_u32 m0, s82, 28672\n\t" \
    "s_nop 0\n\t" \
    "global_load_lds_dwordx4 v200, s[46:47]\n\t" \
    "s_waitcnt vmcnt(28) lgkmcnt(0)\n\t" \
    "s_barrier\n\t" \
    "ds_read_b64_tr_b16 a[24:25], v201 offset:49152\n\t" \
    "ds_read_b64_tr_b16 a[26:27], v201 offset:50176\n\t" \
    "ds_read_b64_tr_b16 a[28:29], v201 offset:57344\n\t" \
    "ds_read_b64_tr_b16 a[30:31], v201 offset:58368\n\t" \
    "ds_read_b64_tr_b16 a[96:97], v202 offset:49152\n\t" \
    "ds_read_b64_tr_b16 a[98:99], v202 offset:50176\n\t" \
    "ds_read_b64_tr_b16 a[100:101], v202 offset:57344\n\t" \
    "ds_read_b64_tr_b16 a[102:103], v202 offset:58368\n\t" \
    "s_add_u32 s46, s44, 1048576\n\t" \
    "s_addc_u32 s47, s45, 0\n\t" \
    "s_add_u32 m0, s82, 32768\n\t" \
    "s_nop 0\n\t" \
    "global_load_lds_dwordx4 v200, s[46:47]\n\t" \
    "s_add_u32 s46, s44, 1179648\n\t" \
    "s_addc_u32 s47, s45, 0\n\t" \
    "s_add_u32 m0, s82, 36864\n\t" \
    "s_nop 0\n\t" \
    "global_load_lds_dwordx4 v200, s[46:47]\n\t" \
    "s_add_u32 s46, s44, 1310720\n\t" \
    "s_addc_u32 s47, s45, 0\n\t" \
    "s_add_u32 m0, s82, 40960\n\t" \
    "s_nop 0\n\t" \
    "global_load_lds_dwordx4 v200, s[46:47]\n\t" \
    "s_add_u32 s46, s44, 1441792\n\t" \
    "s_addc_u32 s47, s45, 0\n\t" \
    "s_add_u32 m0, s82, 45056\n\t" \
    "s_nop 0\n\t" \
    "global_load_lds_dwordx4 v200, s[46:47]\n\t" \
    "s_waitcnt vmcnt(28) lgkmcnt(0)\n\t" \
    "s_barrier\n\t" \
    "v_add_u32 v201, 0x10000, v201\n\t" \
    "v_add_u32 v202, 0x10000, v202\n\t" \
    "ds_read_b64_tr_b16 a[32:33], v201 offset:0\n\t" \
    "ds_read_b64_tr_b16 a[34:35], v201 offset:1024\n\t" \
    "ds_read_b64_tr_b16 a[36:37], v201 offset:8192\n\t" \
    "ds_read_b64_tr_b16 a[38:39], v201 offset:9216\n\t" \
    "ds_read_b64_tr_b16 a[104:105], v202 offset:0\n\t" \
    "ds_read_b64_tr_b16 a[106:107], v202 offset:1024\n\t" \
    "ds_read_b64_tr_b16 a[108:109], v202 offset:8192\n\t" \
    "ds_read_b64_tr_b16 a[110:111], v202 offset:9216\n\t" \
    "s_add_u32 s46, s44, 1572864\n\t" \
    "s_addc_u32 s47, s45, 0\n\t" \
    "s_add_u32 m0, s82, 49152\n\t" \
    "s_nop 0\n\t" \
    "global_load_lds_dwordx4 v200, s[46:47]\n\t" \
    "s_add_u32 s46, s44, 1703936\n\t" \
    "s_addc_u32 s47, s45, 0\n\t" \
    "s_add_u32 m0, s82, 53248\n\t" \
    "s_nop 0\n\t" \
    "global_load_lds_dwordx4 v200, s[46:47]\n\t" \
    "s_add_u32 s46, s44, 1835008\n\t" \
    "s_addc_u32 s47, s45, 0\n\t" \
    "s_add_u32 m0, s82, 57344\n\t" \
    "s_nop 0\n\t" \
    "global_load_lds_dwordx4 v200, s[46:47]\n\t" \
    "s_add_u32 s46, s44, 1966080\n\t" \
    "s_addc_u32 s47, s45, 0\n\t" \
    "s_add_u32 m0, s82, 61440\n\t" \
    "s_nop 0\n\t" \
    "global_load_lds_dwordx4 v200, s[46:47]\n\t" \
    "s_waitcnt vmcnt(28) lgkmcnt(0)\n\t" \
    "s_barrier\n\t" \
    "ds_read_b64_tr_b16 a[40:41], v201 offset:16384\n\t" \
    "ds_read_b64_tr_b16 a[42:43], v201 offset:17408\n\t" \
    "ds_read_b64_tr_b16 a[44:45], v201 offset:24576\n\t" \
    "ds_read_b64_tr_b16 a[46:47], v201 offset:25600\n\t" \
    "ds_read_b64_tr_b16 a[112:113], v202 offset:16384\n\t" \
    "ds_read_b64_tr_b16 a[114:115], v202 offset:17408\n\t" \
    "ds_read_b64_tr_b16 a[116:117], v202 offset:24576\n\t" \
    "ds_read_b64_tr_b16 a[118:119], v202 offset:25600\n\t" \
    "s_add_u32 s46, s44, 2097152\n\t" \
    "s_addc_u32 s47, s45, 0\n\t" \
    "s_add_u32 m0, s82, 65536\n\t" \
    "s_nop 0\n\t" \
    "global_load_lds_dwordx4 v200, s[46:47]\n\t" \
    "s_add_u32 s46, s44, 2228224\n\t" \
    "s_addc_u32 s47, s45, 0\n\t" \
    "s_add_u32 m0, s82, 69632\n\t" \
    "s_nop 0\n\t" \
    "global_load_lds_dwordx4 v200, s[46:47]\n\t" \
    "s_add_u32 s46, s44, 2359296\n\t" \
    "s_addc_u32 s47, s45, 0\n\t" \
    "s_add_u32 m0, s82, 73728\n\t" \
    "s_nop 0\n\t" \
    "global_load_lds_dwordx4 v200, s[46:47]\n\t" \
    "s_add_u32 s46, s44, 2490368\n\t" \
    "s_addc_u32 s47, s45, 0\n\t" \
    "s_add_u32 m0, s82, 77824\n\t" \
    "s_nop 0\n\t" \
    "global_load_lds_dwordx4 v200, s[46:47]\n\t" \
    "s_waitcnt vmcnt(28) lgkmcnt(0)\n\t" \
    "s_barrier\n\t" \
    "ds_read_b64_tr_b16 a[48:49], v201 offset:32768\n\t" \
    "ds_read_b64_tr_b16 a[50:51], v201 offset:33792\n\t" \
    "ds_read_b64_tr_b16 a[52:53], v201 offset:40960\n\t" \
    "ds_read_b64_tr_b16 a[54:55], v201 offset:41984\n\t" \
    "ds_read_b64_tr_b16 a[120:121], v202 offset:32768\n\t" \
    "ds_read_b64_tr_b16 a[122:123], v202 offset:33792\n\t" \
    "ds_read_b64_tr_b16 a[124:125], v202 offset:40960\n\t" \
    "ds_read_b64_tr_b16 a[126:127], v202 offset:41984\n\t" \
    "s_add_u32 s46, s44, 2621440\n\t" \
    "s_addc_u32 s47, s45, 0\n\t" \
    "s_add_u32 m0, s82, 81920\n\t" \
    "s_nop 0\n\t" \
    "global_load_lds_dwordx4 v200, s[46:47]\n\t" \
    "s_add_u32 s46, s44, 2752512\n\t" \
    "s_addc_u32 s47, s45, 0\n\t" \
    "s_add_u32 m0, s82, 86016\n\t" \
    "s_nop 0\n\t" \
    "global_load_lds_dwordx4 v200, s[46:47]\n\t" \
    "s_add_u32 s46, s44, 2883584\n\t" \
    "s_addc_u32 s47, s45, 0\n\t" \
    "s_add_u32 m0, s82, 90112\n\t" \
    "s_nop 0\n\t" \
    "global_load_lds_dwordx4 v200, s[46:47]\n\t" \
    "s_add_u32 s46, s44, 3014656\n\t" \
    "s_addc_u32 s47, s45, 0\n\t" \
    "s_add_u32 m0, s82, 94208\n\t" \
    "s_nop 0\n\t" \
    "global_load_lds_dwordx4 v200, s[46:47]\n\t" \
    "s_waitcnt vmcnt(28) lgkmcnt(0)\n\t" \
    "s_barrier\n\t" \
    "ds_read_b64_tr_b16 a[56:57], v201 offset:49152\n\t" \
    "ds_read_b64_tr_b16 a[58:59], v201 offset:50176\n\t" \
    "ds_read_b64_tr_b16 a[60:61], v201 offset:57344\n\t" \
    "ds_read_b64_tr_b16 a[62:63], v201 offset:58368\n\t" \
    "ds_read_b64_tr_b16 a[128:129], v202 offset:49152\n\t" \
    "ds_read_b64_tr_b16 a[130:131], v202 offset:50176\n\t" \
    "ds_read_b64_tr_b16 a[132:133], v202 offset:57344\n\t" \
    "ds_read_b64_tr_b16 a[134:135], v202 offset:58368\n\t" \
    "s_add_u32 s46, s44, 3145728\n\t" \
    "s_addc_u32 s47, s45, 0\n\t" \
    "s_add_u32 m0, s82, 98304\n\t" \
    "s_nop 0\n\t" \
    "global_load_lds_dwordx4 v200, s[46:47]\n\t" \
    "s_add_u32 s46, s44, 3276800\n\t" \
    "s_addc_u32 s47, s45, 0\n\t" \
    "s_add_u32 m0, s82, 102400\n\t" \
    "s_nop 0\n\t" \
    "global_load_lds_dwordx4 v200, s[46:47]\n\t" \
    "s_add_u32 s46, s44, 3407872\n\t" \
    "s_addc_u32 s47, s45, 0\n\t" \
    "s_add_u32 m0, s82, 106496\n\t" \
    "s_nop 0\n\t" \
    "global_load_lds_dwordx4 v200, s[46:47]\n\t" \
    "s_add_u32 s46, s44, 3538944\n\t" \
    "s_addc_u32 s47, s45, 0\n\t" \
    "s_add_u32 m0, s82, 110592\n\t" \
    "s_nop 0\n\t" \
    "global_load_lds_dwordx4 v200, s[46:47]\n\t" \
    "v_mbcnt_lo_u32_b32 v160, -1, 0\n\t" \
    "v_mbcnt_hi_u32_b32 v160, -1, v160\n\t" \
    "v_lshlrev_b32 v160, 6, v160\n\t" \
    "v_add_u32 v160, 6225920, v160\n\t" \
    "v_mov_b32 v161, 6234112\n\t" \
    "global_load_dword v161, v161, s[36:37] sc1\n\t" \
    "global_load_dwordx2 v[162:163], v160, s[36:37] sc1\n\t" \
    "global_load_dword v164, v160, s[36:37] offset:2048 sc1\n\t" \
    "s_waitcnt vmcnt(31) lgkmcnt(0)\n\t" \
    "s_barrier\n\t" \
    "v_add_u32 v201, 0x10000, v201\n\t" \
    "v_add_u32 v202, 0x10000, v202\n\t" \
    "ds_read_b64_tr_b16 a[64:65], v201 offset:0\n\t" \
    "ds_read_b64_tr_b16 a[66:67], v201 offset:1024\n\t" \
    "ds_read_b64_tr_b16 a[68:69], v201 offset:8192\n\t" \
    "ds_read_b64_tr_b16 a[70:71], v201 offset:9216\n\t" \
    "ds_read_b64_tr_b16 a[136:137], v202 offset:0\n\t" \
    "ds_read_b64_tr_b16 a[138:139], v202 offset:1024\n\t" \
    "ds_read_b64_tr_b16 a[140:141], v202 offset:8192\n\t" \
    "ds_read_b64_tr_b16 a[142:143], v202 offset:9216\n\t" \
    "v_add_u32 v201, 0xfffe0000, v201\n\t" \
    "v_add_u32 v202, 0xfffe0000, v202\n\t" \
    "s_add_u32 s46, s44, 3670016\n\t" \
    "s_addc_u32 s47, s45, 0\n\t" \
    "s_add_u32 m0, s82, 114688\n\t" \
    "s_nop 0\n\t" \
    "global_load_lds_dwordx4 v200, s[46:47]\n\t" \
    "s_add_u32 s46, s44, 3801088\n\t" \
    "s_addc_u32 s47, s45, 0\n\t" \
    "s_add_u32 m0, s82, 118784\n\t" \
    "s_nop 0\n\t" \
    "global_load_lds_dwordx4 v200, s[46:47]\n\t" \
    "s_add_u32 s46, s44, 3932160\n\t" \
    "s_addc_u32 s47, s45, 0\n\t" \
    "s_add_u32 m0, s82, 122880\n\t" \
    "s_nop 0\n\t" \
    "global_load_lds_dwordx4 v200, s[46:47]\n\t" \
    "s_add_u32 s46, s44, 4063232\n\t" \
    "s_addc_u32 s47, s45, 0\n\t" \
    "s_add_u32 m0, s82, 126976\n\t" \
    "s_nop 0\n\t" \
    "global_load_lds_dwordx4 v200, s[46:47]\n\t" \
    "s_waitcnt lgkmcnt(0)\n\t" \
    "s_barrier\n\t" \
    "s_add_u32 s46, s44, 4194304\n\t" \
    "s_addc_u32 s47, s45, 0\n\t" \
    "s_add_u32 m0, s82, 131072\n\t" \
    "s_nop 0\n\t" \
    "global_load_lds_dwordx4 v200, s[46:47]\n\t" \
    "s_add_u32 s46, s44, 4325376\n\t" \
    "s_addc_u32 s47, s45, 0\n\t" \
    "s_add_u32 m0, s82, 135168\n\t" \
    "s_nop 0\n\t" \
    "global_load_lds_dwordx4 v200, s[46:47]\n\t" \
    "s_add_u32 s46, s44, 4456448\n\t" \
    "s_addc_u32 s47, s45, 0\n\t" \
    "s_add_u32 m0, s82, 139264\n\t" \
    "s_nop 0\n\t" \
    "global_load_lds_dwordx4 v200, s[46:47]\n\t" \
    "s_add_u32 s46, s44, 4587520\n\t" \
    "s_addc_u32 s47, s45, 0\n\t" \
    "s_add_u32 m0, s82, 143360\n\t" \
    "s_nop 0\n\t" \
    "global_load_lds_dwordx4 v200, s[46:47]\n\t" \
    "s_waitcnt vmcnt(35) lgkmcnt(0)\n\t" \
    "s_barrier\n\t" \
    "ds_read_b64_tr_b16 a[144:145], v201 offset:0\n\t" \
    "ds_read_b64_tr_b16 a[146:147], v201 offset:1024\n\t" \
    "ds_read_b64_tr_b16 a[148:149], v201 offset:8192\n\t" \
    "ds_read_b64_tr_b16 a[150:151], v201 offset:9216\n\t" \
    "ds_read_b64_tr_b16 a[216:217], v202 offset:0\n\t" \
    "ds_read_b64_tr_b16 a[218:219], v202 offset:1024\n\t" \
    "ds_read_b64_tr_b16 a[220:221], v202 offset:8192\n\t" \
    "ds_read_b64_tr_b16 a[222:223], v202 offset:9216\n\t" \
    "s_waitcnt vmcnt(31) lgkmcnt(0)\n\t" \
    "s_barrier\n\t" \
    "ds_read_b64_tr_b16 a[152:153], v201 offset:16384\n\t" \
    "ds_read_b64_tr_b16 a[154:155], v201 offset:17408\n\t" \
    "ds_read_b64_tr_b16 a[156:157], v201 offset:24576\n\t" \
    "ds_read_b64_tr_b16 a[158:159], v201 offset:25600\n\t" \
    "ds_read_b64_tr_b16 a[224:225], v202 offset:16384\n\t" \
    "ds_read_b64_tr_b16 a[226:227], v202 offset:17408\n\t" \
    "ds_read_b64_tr_b16 a[228:229], v202 offset:24576\n\t" \
    "ds_read_b64_tr_b16 a[230:231], v202 offset:25600\n\t" \
    "s_waitcnt vmcnt(27) lgkmcnt(0)\n\t" \
    "s_barrier\n\t" \
    "ds_read_b64_tr_b16 a[160:161], v201 offset:32768\n\t" \
    "ds_read_b64_tr_b16 a[162:163], v201 offset:33792\n\t" \
    "ds_read_b64_tr_b16 a[164:165], v201 offset:40960\n\t" \
    "ds_read_b64_tr_b16 a[166:167], v201 offset:41984\n\t" \
    "ds_read_b64_tr_b16 a[232:233], v202 offset:32768\n\t" \
    "ds_read_b64_tr_b16 a[234:235], v202 offset:33792\n\t" \
    "ds_read_b64_tr_b16 a[236:237], v202 offset:40960\n\t" \
    "ds_read_b64_tr_b16 a[238:239], v202 offset:41984\n\t" \
    "s_waitcnt vmcnt(23) lgkmcnt(0)\n\t" \
    "s_barrier\n\t" \
    "ds_read_b64_tr_b16 a[168:169], v201 offset:49152\n\t" \
    "ds_read_b64_tr_b16 a[170:171], v201 offset:50176\n\t" \
    "ds_read_b64_tr_b16 a[172:173], v201 offset:57344\n\t" \
    "ds_read_b64_tr_b16 a[174:175], v201 offset:58368\n\t" \
    "ds_read_b64_tr_b16 a[240:241], v202 offset:49152\n\t" \
    "ds_read_b64_tr_b16 a[242:243], v202 offset:50176\n\t" \
    "ds_read_b64_tr_b16 a[244:245], v202 offset:57344\n\t" \
    "ds_read_b64_tr_b16 a[246:247], v202 offset:58368\n\t" \
    "s_waitcnt vmcnt(19) lgkmcnt(0)\n\t" \
    "s_barrier\n\t" \
    "v_add_u32 v201, 0x10000, v201\n\t" \
    "v_add_u32 v202, 0x10000, v202\n\t" \
    "ds_read_b64_tr_b16 a[176:177], v201 offset:0\n\t" \
    "ds_read_b64_tr_b16 a[178:179], v201 offset:1024\n\t" \
    "ds_read_b64_tr_b16 a[180:181], v201 offset:8192\n\t" \
    "ds_read_b64_tr_b16 a[182:183], v201 offset:9216\n\t" \
    "ds_read_b64_tr_b16 a[248:249], v202 offset:0\n\t" \
    "ds_read_b64_tr_b16 a[250:251], v202 offset:1024\n\t" \
    "ds_read_b64_tr_b16 a[252:253], v202 offset:8192\n\t" \
    "ds_read_b64_tr_b16 a[254:255], v202 offset:9216\n\t" \
    "s_waitcnt vmcnt(15) lgkmcnt(0)\n\t" \
    "s_barrier\n\t" \
    "ds_read_b64_tr_b16 a[184:185], v201 offset:16384\n\t" \
    "ds_read_b64_tr_b16 a[186:187], v201 offset:17408\n\t" \
    "ds_read_b64_tr_b16 a[188:189], v201 offset:24576\n\t" \
    "ds_read_b64_tr_b16 a[190:191], v201 offset:25600\n\t" \
    "ds_read_b64_tr_b16 v[224:225], v202 offset:16384\n\t" \
    "ds_read_b64_tr_b16 v[226:227], v202 offset:17408\n\t" \
    "ds_read_b64_tr_b16 v[228:229], v202 offset:24576\n\t" \
    "ds_read_b64_tr_b16 v[230:231], v202 offset:25600\n\t" \
    "s_waitcnt vmcnt(11) lgkmcnt(0)\n\t" \
    "s_barrier\n\t" \
    "ds_read_b64_tr_b16 a[192:193], v201 offset:32768\n\t" \
    "ds_read_b64_tr_b16 a[194:195], v201 offset:33792\n\t" \
    "ds_read_b64_tr_b16 a[196:197], v201 offset:40960\n\t" \
    "ds_read_b64_tr_b16 a[198:199], v201 offset:41984\n\t" \
    "ds_read_b64_tr_b16 v[232:233], v202 offset:32768\n\t" \
    "ds_read_b64_tr_b16 v[234:235], v202 offset:33792\n\t" \
    "ds_read_b64_tr_b16 v[236:237], v202 offset:40960\n\t" \
    "ds_read_b64_tr_b16 v[238:239], v202 offset:41984\n\t" \
    "s_waitcnt vmcnt(8)\n\t" \
    "qk_C2_%=:\n\t" \
    "v_cmp_ne_u32 s[54:55], v162, v164\n\t" \
    "v_sub_u32 v162, v162, v161\n\t" \
    "v_cmp_ne_u32 vcc, 1, v162\n\t" \
    "s_nop 1\n\t" \
    "s_or_b64 vcc, vcc, s[54:55]\n\t" \
    "s_cbranch_scc0 qk_Cok3_%=\n\t" \
    "s_cmp_eq_u32 s68, 0\n\t" \
    "s_cbranch_scc1 qk_F5_%=\n\t" \
    "s_sub_u32 s68, s68, 1\n\t" \
    "s_sleep 6\n\t" \
    "s_branch qk_Cag4_%=\n\t" \
    "qk_F5_%=:\n\t" \
    "v_mov_b32 v164, 5767424\n\t" \
    "v_mov_b32 v165, 4\n\t" \
    "global_store_dword v164, v165, s[36:37] sc1\n\t" \
    "s_waitcnt vmcnt(0)\n\t" \
    "s_branch qk_Cok3_%=\n\t" \
    "qk_Cag4_%=:\n\t" \
    "global_load_dwordx2 v[162:163], v160, s[36:37] sc1\n\t" \
    "global_load_dword v164, v160, s[36:37] offset:2048 sc1\n\t" \
    "s_waitcnt vmcnt(0)\n\t" \
    "s_branch qk_C2_%=\n\t" \
    "qk_Cok3_%=:\n\t" \
    "s_cmp_lg_u32 s66, 0\n\t" \
    "s_cbranch_scc1 qk_Ca6_%=\n\t" \
    "v_mov_b32 v160, 5836800\n\t" \
    "s_mov_b64 exec, 1\n\t" \
    "global_atomic_add v160, v162, s[36:37] sc1\n\t" \
    "s_mov_b64 exec, -1\n\t" \
    "qk_Ca6_%=:\n\t" \
    "v_mov_b32_dpp v160, v163 quad_perm:[1,0,3,2] row_mask:0xf bank_mask:0xf\n\t" \
    "v_cmp_lt_u32 s[44:45], v160, v163\n\t" \
    "s_nop 1\n\t" \
    "s_bitcmp1_b32 s44, 0\n\t" \
    "s_cselect_b32 s62, 8192, 0\n\t" \
    "s_bitcmp1_b32 s44, 2\n\t" \
    "s_cselect_b32 s63, 8192, 0\n\t" \
    "s_or_b32 s63, s63, 0x200000\n\t" \
    "v_min_u32 v160, v160, v163\n\t" \
    "v_cmp_lt_u32 vcc, 660, v160\n\t" \
    "s_nop 1\n\t" \
    "s_cmp_lg_u64 vcc, 0\n\t" \
    "s_cselect_b32 s69, 1, 0\n\t" \
    "s_mul_i32 s70, s69, 160\n\t" \
    "s_add_u32 s86, s86, s70\n\t" \
    "s_mul_i32 s70, s69, 536\n\t" \
    "s_add_u32 s87, s87, s70\n\t" \
    "s_mul_i32 s70, s69, 256\n\t" \
    "s_add_u32 s88, s88, s70\n\t" \
    "global_load_dword v25, v24, s[36:37] sc1\n\t" \
    "s_waitcnt vmcnt(5) lgkmcnt(0)\n\t" \
    "s_barrier\n\t" \
    "ds_read_b64_tr_b16 a[200:201], v201 offset:49152\n\t" \
    "ds_read_b64_tr_b16 a[202:203], v201 offset:50176\n\t" \
    "ds_read_b64_tr_b16 a[204:205], v201 offset:57344\n\t" \
    "ds_read_b64_tr_b16 a[206:207], v201 offset:58368\n\t" \
    "ds_read_b64_tr_b16 v[240:241], v202 offset:49152\n\t" \
    "ds_read_b64_tr_b16 v[242:243], v202 offset:50176\n\t" \
    "ds_read_b64_tr_b16 v[244:245], v202 offset:57344\n\t" \
    "ds_read_b64_tr_b16 v[246:247], v202 offset:58368\n\t" \
    "s_waitcnt vmcnt(1) lgkmcnt(0)\n\t" \
    "s_barrier\n\t" \
    "v_add_u32 v201, 0x10000, v201\n\t" \
    "v_add_u32 v202, 0x10000, v202\n\t" \
    "ds_read_b64_tr_b16 a[208:209], v201 offset:0\n\t" \
    "ds_read_b64_tr_b16 a[210:211], v201 offset:1024\n\t" \
    "ds_read_b64_tr_b16 a[212:213], v201 offset:8192\n\t" \
    "ds_read_b64_tr_b16 a[214:215], v201 offset:9216\n\t" \
    "ds_read_b64_tr_b16 v[248:249], v202 offset:0\n\t" \
    "ds_read_b64_tr_b16 v[250:251], v202 offset:1024\n\t" \
    "ds_read_b64_tr_b16 v[252:253], v202 offset:8192\n\t" \
    "ds_read_b64_tr_b16 v[254:255], v202 offset:9216\n\t" \
    "v_add_u32 v201, 0xfffe0000, v201\n\t" \
    "v_add_u32 v202, 0xfffe0000, v202\n\t" \
    "s_waitcnt lgkmcnt(0)\n\t" \
    "s_barrier\n\t" \
    "s_mul_i32 s70, s67, 7\n\t" \
    "s_add_u32 s69, s65, 0\n\t" \
    "s_xor_b32 s69, s69, s70\n\t" \
    "s_and_b32 s69, s69, 63\n\t" \
    "s_lshl_b32 s69, s69, 13\n\t" \
    "s_mul_i32 s70, s67, 524288\n\t" \
    "s_add_u32 s69, s69, s70\n\t" \
    "s_add_u32 s69, s69, 23068672\n\t" \
    "s_add_u32 s38, s36, s69\n\t" \
    "s_addc_u32 s39, s37, 0\n\t" \
    "v_mov_b32 v26, 0x3db504f3\n\t" \
    "v_mov_b32 v28, 0x3fb8aa3b\n\t" \
    "v_mov_b32 v27, 0x3db504f3\n\t" \
    "v_mov_b32 v29, 0x3fb8aa3b\n\t" \
    "v_xor_b32 v180, 65536, v18\n\t" \
    "v_xor_b32 v181, 131072, v18\n\t" \
    "v_xor_b32 v182, 196608, v18\n\t" \
    "s_mov_b32 s48, 0xaaaaaaaa\n\t" \
    "s_mov_b32 s49, 0xaaaaaaaa\n\t" \
    "s_mov_b32 s58, 0x00ff00ff\n\t" \
    "s_mov_b32 s59, 0x00ff00ff\n\t" \
    "v_mov_b32 v31, -1\n\t" \
    "s_mov_b32 s91, -1\n\t" \
    "ds_write_b32 v12, v31 offset:0\n\t" \
    "ds_write_b32 v12, v31 offset:64\n\t" \
    "s_waitcnt vmcnt(0)\n\t" \
    "s_cmp_gt_u32 s66, 1\n\t" \
    "s_cbranch_scc1 qk_nk7_%=\n\t" \
    "v_readfirstlane_b32 s83, v25\n\t" \
    "s_nop 1\n\t" \
    "qk_WF8_%=:\n\t" \
    "s_cmp_ge_i32 s83, s65\n\t" \
    "s_cbranch_scc1 qk_WFok9_%=\n\t" \
    "global_load_dword v160, v24, s[36:37] sc1\n\t" \
    "s_waitcnt vmcnt(0)\n\t" \
    "v_readfirstlane_b32 s83, v160\n\t" \
    "s_nop 1\n\t" \
    "s_cmp_ge_i32 s83, s65\n\t" \
    "s_cbranch_scc1 qk_WFok9_%=\n\t" \
    "s_cmp_eq_u32 s68, 0\n\t" \
    "s_cbranch_scc1 qk_F10_%=\n\t" \
    "s_sub_u32 s68, s68, 1\n\t" \
    "s_sleep 2\n\t" \
    "s_branch qk_WF8_%=\n\t" \
    "qk_F10_%=:\n\t" \
    "v_mov_b32 v160, 5767424\n\t" \
    "v_mov_b32 v161, 8\n\t" \
    "global_store_dword v160, v161, s[36:37] sc1\n\t" \
    "s_waitcnt vmcnt(0)\n\t" \
    "s_branch qk_WFok9_%=\n\t" \
    "qk_WFok9_%=:\n\t" \
    "global_load_dwordx4 v[100:103], v0, s[38:39] offset:0 sc1\n\t" \
    "global_load_dwordx4 v[104:107], v0, s[38:39] offset:2048 sc1\n\t" \
    "global_load_dwordx4 v[108:111], v15, s[38:39] offset:0 sc1\n\t" \
    "global_load_dwordx4 v[112:115], v15, s[38:39] offset:2048 sc1\n\t" \
    "global_load_dwordx4 v[116:119], v0, s[38:39] offset:1024 sc1\n\t" \
    "global_load_dwordx4 v[120:123], v0, s[38:39] offset:3072 sc1\n\t" \
    "global_load_dwordx4 v[124:127], v15, s[38:39] offset:1024 sc1\n\t" \
    "global_load_dwordx4 v[128:131], v15, s[38:39] offset:3072 sc1\n\t" \
    "qk_nk7_%=:\n\t" \
    "v_mov_b32 v33, v21\n\t" \
    "v_readlane_b32 s69, v21, 63\n\t" \
    "s_nop 1\n\t" \
    "v_mov_b32 v21, s69\n\t" \
    "s_mov_b32 s79, s74\n\t" \
    "s_mov_b32 s80, s74\n\t" \
    "s_cmp_eq_u32 s66, 1\n\t" \
    "s_cbranch_scc1 qk_loop12_%=\n\t" \
    "s_cmp_eq_u32 s66, 2\n\t" \
    "s_cbranch_scc1 qk_loop13_%=\n\t" \
    "s_cmp_eq_u32 s66, 3\n\t" \
    "s_cbranch_scc1 qk_loop14_%=\n\t" \
    "qk_loop11_%=:\n\t" \
    "v_mov_b32 v72, 0\n\t" \
    "v_mov_b32 v73, 0\n\t" \
    "v_mov_b32 v74, 0\n\t" \
    "v_mov_b32 v75, 0\n\t" \
    "v_mov_b32 v76, 0\n\t" \
    "v_mov_b32 v77, 0\n\t" \
    "v_mov_b32 v78, 0\n\t" \
    "v_mov_b32 v79, 0\n\t" \
    "v_mov_b32 v80, 0\n\t" \
    "v_mov_b32 v81, 0\n\t" \
    "v_mov_b32 v82, 0\n\t" \
    "v_mov_b32 v83, 0\n\t" \
    "v_mov_b32 v84, 0\n\t" \
    "v_mov_b32 v85, 0\n\t" \
    "v_mov_b32 v86, 0\n\t" \
    "v_mov_b32 v87, 0\n\t" \
    "v_mov_b32 v88, 0\n\t" \
    "v_mov_b32 v89, 0\n\t" \
    "v_mov_b32 v90, 0\n\t" \
    "v_mov_b32 v91, 0\n\t" \
    "v_mov_b32 v92, 0\n\t" \
    "v_mov_b32 v93, 0\n\t" \
    "v_mov_b32 v94, 0\n\t" \
    "v_mov_b32 v95, 0\n\t" \
    "v_mov_b32 v96, 0\n\t" \
    "v_mov_b32 v97, 0\n\t" \
    "v_mov_b32 v98, 0\n\t" \
    "v_mov_b32 v99, 0\n\t" \
    "s_add_u32 s69, s65, 0\n\t" \
    "s_and_b32 s69, s69, 7\n\t" \
    "s_lshl_b32 s69, s69, 18\n\t" \
    "s_add_u32 s69, s69, s62\n\t" \
    "s_add_u32 s46, s36, s69\n\t" \
    "s_addc_u32 s47, s37, 0\n\t" \
    "s_sub_u32 s70, s79, s74\n\t" \
    "s_max_i32 s70, s70, 0\n\t" \
    "s_min_i32 s70, s70, 16000\n\t" \
    "s_lshl_b32 s70, s70, 1\n\t" \
    "s_mul_hi_u32 s70, s70, s77\n\t" \
    "s_add_u32 s70, s70, s73\n\t" \
    ".p2align 6\n\t" \
    "s_nop 0\n\t" \
    "s_nop 0\n\t" \
    "s_nop 0\n\t" \
    "s_nop 0\n\t" \
    "s_nop 0\n\t" \
    "s_nop 0\n\t" \
    "qk_go16_%=:\n\t" \
    "s_getpc_b64 s[30:31]\n\t" \
    "qk_Wp18_%=:\n\t" \
    "s_add_u32 s30, s30, qk_We19_%=-qk_Wp18_%=\n\t" \
    "s_addc_u32 s31, s31, 0\n\t" \
    "s_sub_u32 s70, s70, 96\n\t" \
    "qk_W17_%=:\n\t" \
    "s_memtime s[56:57]\n\t" \
    "s_waitcnt lgkmcnt(0)\n\t" \
    "s_sub_u32 s69, s70, s56\n\t" \
    "s_cmp_gt_i32 s69, 80\n\t" \
    "s_cbranch_scc1 qk_W17_%=\n\t" \
    "s_max_i32 s69, s69, 0\n\t" \
    "s_and_b32 s69, s69, -4\n\t" \
    "s_sub_u32 s30, s30, s69\n\t" \
    "s_subb_u32 s31, s31, 0\n\t" \
    "s_setpc_b64 s[30:31]\n\t" \
    "s_nop 0\n\t" \
    "s_nop 0\n\t" \
    "s_nop 0\n\t" \
    "s_nop 0\n\t" \
    "s_nop 0\n\t" \
    "s_nop 0\n\t" \
    "s_nop 0\n\t" \
    "s_nop 0\n\t" \
    "s_nop 0\n\t" \
    "s_nop 0\n\t" \
    "s_nop 0\n\t" \
    "s_nop 0\n\t" \
    "s_nop 0\n\t" \
    "s_nop 0\n\t" \
    "s_nop 0\n\t" \
    "s_nop 0\n\t" \
    "s_nop 0\n\t" \
    "s_nop 0\n\t" \
    "s_nop 0\n\t" \
    "s_nop 0\n\t" \
    "qk_We19_%=:\n\t" \
    "qk_G20_%=:\n\t" \
    "global_load_dwordx4 v[36:39], v1, s[46:47] sc1\n\t" \
    "global_load_dword v161, v2, s[46:47] sc1\n\t" \
    "global_load_dwordx4 v[40:43], v23, s[46:47] sc1\n\t" \
    "s_waitcnt vmcnt(2)\n\t" \
    "ds_write_b128 v3, v[36:39]\n\t" \
    "v_smfmac_f32_16x16x64_bf16 v[72:75], v[36:39], a[0:7], v10\n\t" \
    "s_waitcnt vmcnt(1)\n\t" \
    "ds_write_b32 v22, v161 offset:19456\n\t" \
    "v_smfmac_f32_16x16x64_bf16 v[76:79], v[36:39], a[72:79], v10\n\t" \
    "s_waitcnt vmcnt(0)\n\t" \
    "ds_write_b128 v3, v[40:43] offset:1024\n\t" \
    "v_max3_u32 v160, v36, v40, v161\n\t" \
    "v_cmp_eq_u32 vcc, -1, v160\n\t" \
    "v_smfmac_f32_16x16x64_bf16 v[80:83], v[36:39], a[144:151], v10\n\t" \
    "v_smfmac_f32_16x16x64_bf16 v[84:87], v[36:39], a[216:223], v10\n\t" \
    "v_smfmac_f32_16x16x64_bf16 v[72:75], v[40:43], a[8:15], v10\n\t" \
    "s_cbranch_vccnz qk_Gbad21_%=\n\t" \
    "qk_Gc22_%=:\n\t" \
    "s_waitcnt lgkmcnt(0)\n\t" \
    "s_barrier\n\t" \
    "ds_read_b128 v[48:51], v4\n\t" \
    "ds_read_b128 v[52:55], v5\n\t" \
    "v_smfmac_f32_16x16x64_bf16 v[76:79], v[40:43], a[80:87], v10\n\t" \
    "ds_read_b128 v[56:59], v6\n\t" \
    "ds_read_b128 v[60:63], v7\n\t" \
    "v_smfmac_f32_16x16x64_bf16 v[80:83], v[40:43], a[152:159], v10\n\t" \
    "ds_read_b128 v[64:67], v8\n\t" \
    "v_smfmac_f32_16x16x64_bf16 v[84:87], v[40:43], a[224:231], v10\n\t" \
    "ds_read_b128 v[68:71], v9\n\t" \
    "s_waitcnt lgkmcnt(5)\n\t" \
    "v_smfmac_f32_16x16x64_bf16 v[72:75], v[48:51], a[16:23], v10\n\t" \
    "ds_read_b128 v[44:47], v0 offset:19456\n\t" \
    "s_waitcnt lgkmcnt(5)\n\t" \
    "v_smfmac_f32_16x16x64_bf16 v[72:75], v[52:55], a[24:31], v10\n\t" \
    "s_waitcnt lgkmcnt(4)\n\t" \
    "v_smfmac_f32_16x16x64_bf16 v[72:75], v[56:59], a[32:39], v10\n\t" \
    "s_waitcnt lgkmcnt(3)\n\t" \
    "v_smfmac_f32_16x16x64_bf16 v[72:75], v[60:63], a[40:47], v10\n\t" \
    "s_waitcnt lgkmcnt(2)\n\t" \
    "v_smfmac_f32_16x16x64_bf16 v[72:75], v[64:67], a[48:55], v10\n\t" \
    "s_waitcnt lgkmcnt(1)\n\t" \
    "v_smfmac_f32_16x16x64_bf16 v[72:75], v[68:71], a[56:63], v10\n\t" \
    "s_waitcnt lgkmcnt(0)\n\t" \
    "v_smfmac_f32_16x16x64_bf16 v[72:75], v[44:47], a[64:71], v10\n\t" \
    "s_nop 2\n\t" \
    "v_smfmac_f32_16x16x64_bf16 v[76:79], v[48:51], a[88:95], v10\n\t" \
    "s_nop 2\n\t" \
    "v_smfmac_f32_16x16x64_bf16 v[76:79], v[52:55], a[96:103], v10\n\t" \
    "v_add_f32 v72, v72, v74\n\t" \
    "v_smfmac_f32_16x16x64_bf16 v[76:79], v[56:59], a[104:111], v10\n\t" \
    "v_add_f32 v73, v73, v75\n\t" \
    "v_smfmac_f32_16x16x64_bf16 v[76:79], v[60:63], a[112:119], v10\n\t" \
    "v_cvt_pk_bf16_f32 v156, v72, v73\n\t" \
    "v_smfmac_f32_16x16x64_bf16 v[76:79], v[64:67], a[120:127], v10\n\t" \
    "ds_write_b32 v12, v156 offset:0\n\t" \
    "v_smfmac_f32_16x16x64_bf16 v[76:79], v[68:71], a[128:135], v10\n\t" \
    "v_smfmac_f32_16x16x64_bf16 v[76:79], v[44:47], a[136:143], v10\n\t" \
    "s_nop 2\n\t" \
    "v_smfmac_f32_16x16x64_bf16 v[80:83], v[48:51], a[160:167], v10\n\t" \
    "s_nop 2\n\t" \
    "v_smfmac_f32_16x16x64_bf16 v[80:83], v[52:55], a[168:175], v10\n\t" \
    "v_add_f32 v76, v76, v78\n\t" \
    "v_smfmac_f32_16x16x64_bf16 v[80:83], v[56:59], a[176:183], v10\n\t" \
    "v_add_f32 v77, v77, v79\n\t" \
    "v_smfmac_f32_16x16x64_bf16 v[80:83], v[60:63], a[184:191], v10\n\t" \
    "v_cvt_pk_bf16_f32 v157, v76, v77\n\t" \
    "v_smfmac_f32_16x16x64_bf16 v[80:83], v[64:67], a[192:199], v10\n\t" \
    "ds_write_b32 v12, v157 offset:64\n\t" \
    "v_smfmac_f32_16x16x64_bf16 v[80:83], v[68:71], a[200:207], v10\n\t" \
    "v_smfmac_f32_16x16x64_bf16 v[80:83], v[44:47], a[208:215], v10\n\t" \
    "s_nop 2\n\t" \
    "v_smfmac_f32_16x16x64_bf16 v[84:87], v[48:51], a[232:239], v10\n\t" \
    "s_nop 2\n\t" \
    "v_smfmac_f32_16x16x64_bf16 v[84:87], v[52:55], a[240:247], v10\n\t" \
    "v_add_f32 v80, v80, v82\n\t" \
    "v_smfmac_f32_16x16x64_bf16 v[84:87], v[56:59], a[248:255], v10\n\t" \
    "v_add_f32 v81, v81, v83\n\t" \
    "v_smfmac_f32_16x16x64_bf16 v[84:87], v[60:63], v[224:231], v10\n\t" \
    "v_cvt_pk_bf16_f32 v158, v80, v81\n\t" \
    "v_smfmac_f32_16x16x64_bf16 v[84:87], v[64:67], v[232:239], v10\n\t" \
    "ds_write_b32 v16, v158 offset:0\n\t" \
    "v_smfmac_f32_16x16x64_bf16 v[84:87], v[68:71], v[240:247], v10\n\t" \
    "ds_read_b64_tr_b16 v[132:133], v13 offset:0\n\t" \
    "ds_read_b64_tr_b16 v[134:135], v13 offset:128\n\t" \
    "v_smfmac_f32_16x16x64_bf16 v[84:87], v[44:47], v[248:255], v10\n\t" \
    "ds_read_b64_tr_b16 v[136:137], v13 offset:1024\n\t" \
    "ds_read_b64_tr_b16 v[138:139], v13 offset:1152\n\t" \
    "s_nop 4\n\t" \
    "v_cmp_u_f32 vcc, v72, v73\n\t" \
    "v_pk_add_f32 v[84:85], v[84:85], v[86:87]\n\t" \
    "v_cvt_pk_bf16_f32 v159, v84, v85\n\t" \
    "ds_write_b32 v16, v159 offset:256\n\t" \
    "s_cbranch_vccnz qk_Gslow23_%=\n\t" \
    "qk_Gback24_%=:\n\t" \
    "s_waitcnt lgkmcnt(3)\n\t" \
    "v_smfmac_f32_16x16x64_bf16 v[88:91], v[132:135], v[100:107], v10\n\t" \
    "s_waitcnt lgkmcnt(1)\n\t" \
    "v_smfmac_f32_16x16x64_bf16 v[88:91], v[136:139], v[108:115], v10\n\t" \
    "s_waitcnt lgkmcnt(0)\n\t" \
    "s_barrier\n\t" \
    "ds_read_b64_tr_b16 v[140:141], v17 offset:0\n\t" \
    "v_smfmac_f32_16x16x64_bf16 v[92:95], v[132:135], v[116:123], v10\n\t" \
    "ds_read_b64_tr_b16 v[142:143], v17 offset:256\n\t" \
    "ds_read_b64_tr_b16 v[144:145], v17 offset:512\n\t" \
    "ds_read_b64_tr_b16 v[146:147], v17 offset:768\n\t" \
    "ds_read_b64_tr_b16 v[148:149], v17 offset:1024\n\t" \
    "v_smfmac_f32_16x16x64_bf16 v[92:95], v[136:139], v[124:131], v10\n\t" \
    "ds_read_b64_tr_b16 v[150:151], v17 offset:1280\n\t" \
    "ds_read_b64_tr_b16 v[152:153], v17 offset:1536\n\t" \
    "ds_read_b64_tr_b16 v[154:155], v17 offset:1792\n\t" \
    "v_smfmac_f32_16x16x64_bf16 v[184:187], v[132:135], v[100:107], v10\n\t" \
    "qk_qb26_%=:\n\t" \
    "s_waitcnt lgkmcnt(4)\n\t" \
    "v_smfmac_f32_16x16x64_bf16 v[96:99], v[132:135], v[140:147], v10\n\t" \
    "s_waitcnt lgkmcnt(0)\n\t" \
    "v_smfmac_f32_16x16x64_bf16 v[96:99], v[136:139], v[148:155], v10\n\t" \
    "v_add_f32 v168, v88, v90\n\t" \
    "v_add_f32 v169, v92, v94\n\t" \
    "v_cmp_u_f32 vcc, v168, v169\n\t" \
    "v_cvt_pk_bf16_f32 v168, 0, v168\n\t" \
    "v_cvt_pk_bf16_f32 v169, 0, v169\n\t" \
    "v_mul_f32 v168, v168, v26\n\t" \
    "v_mul_f32 v169, v169, v26\n\t" \
    "v_cvt_pk_bf16_f32 v168, 0, v168\n\t" \
    "v_add_f32 v172, v96, v98\n\t" \
    "v_cvt_pk_bf16_f32 v169, 0, v169\n\t" \
    "v_max_f32 v178, v168, v169\n\t" \
    "v_cvt_pk_bf16_f32 v172, 0, v172\n\t" \
    "v_mul_f32 v172, v172, v26\n\t" \
    "v_max_f32_dpp v178, v178, v178 row_mirror row_mask:0xf bank_mask:0xf\n\t" \
    "v_cvt_pk_bf16_f32 v172, 0, v172\n\t" \
    "v_max_f32 v173, v178, v172\n\t" \
    "v_mul_f32 v168, v168, v28\n\t" \
    "v_mul_f32 v169, v169, v28\n\t" \
    "v_max_f32_dpp v173, v173, v173 quad_perm:[1,0,3,2] row_mask:0xf bank_mask:0xf\n\t" \
    "v_mul_f32 v172, v172, v28\n\t" \
    "s_cbranch_vccnz qk_qs25_%=\n\t" \
    "qk_qb227_%=:\n\t" \
    "v_max_f32_dpp v173, v173, v173 quad_perm:[2,3,0,1] row_mask:0xf bank_mask:0xf\n\t" \
    "s_nop 1\n\t" \
    "v_max_f32_dpp v173, v173, v173 row_half_mirror row_mask:0xf bank_mask:0xf\n\t" \
    "v_fma_f32 v172, -v173, v28, v172\n\t" \
    "v_fma_f32 v168, -v173, v28, v168\n\t" \
    "v_fma_f32 v169, -v173, v28, v169\n\t" \
    "v_exp_f32 v172, v172\n\t" \
    "v_exp_f32 v168, v168\n\t" \
    "v_exp_f32 v169, v169\n\t" \
    "v_fma_f32 v173, v172, 0.5, v168\n\t" \
    "v_add_f32 v173, v173, v169\n\t" \
    "v_mov_b32_dpp v174, v172 quad_perm:[0,0,2,2] row_mask:0xf bank_mask:0xf\n\t" \
    "v_mov_b32_dpp v175, v172 quad_perm:[1,1,3,3] row_mask:0xf bank_mask:0xf\n\t" \
    "v_add_f32_dpp v173, v173, v173 quad_perm:[1,0,3,2] row_mask:0xf bank_mask:0xf\n\t" \
    "v_mov_b32_dpp v176, v168 quad_perm:[1,0,3,2] row_mask:0xf bank_mask:0xf\n\t" \
    "v_mov_b32_dpp v177, v169 quad_perm:[1,0,3,2] row_mask:0xf bank_mask:0xf\n\t" \
    "v_add_f32_dpp v173, v173, v173 quad_perm:[2,3,0,1] row_mask:0xf bank_mask:0xf\n\t" \
    "v_cndmask_b32 v168, v168, v177, s[48:49]\n\t" \
    "v_cndmask_b32 v169, v176, v169, s[48:49]\n\t" \
    "v_add_f32_dpp v173, v173, v173 row_half_mirror row_mask:0xf bank_mask:0xf\n\t" \
    "s_nop 1\n\t" \
    "v_add_f32_dpp v173, v173, v173 row_mirror row_mask:0xf bank_mask:0xf\n\t" \
    "v_rcp_f32 v173, v173\n\t" \
    "s_nop 0\n\t" \
    "v_pk_mul_f32 v[168:169], v[168:169], v[172:173] op_sel:[0,1] op_sel_hi:[1,1]\n\t" \
    "v_cvt_pk_bf16_f32 v176, v168, v169\n\t" \
    "global_store_dword v18, v176, s[46:47] sc1\n\t" \
    "global_store_dword v180, v176, s[46:47] sc1\n\t" \
    "global_store_dword v181, v176, s[46:47] sc1\n\t" \
    "global_store_dword v182, v176, s[46:47] sc1\n\t" \
    "v_pk_mul_f32 v[174:175], v[174:175], v[172:173] op_sel:[0,1] op_sel_hi:[1,1]\n\t" \
    "v_cvt_pk_bf16_f32 v177, v174, v175\n\t" \
    "global_store_dword v19, v177, s[46:47] sc1\n\t" \
    "s_cmp_eq_u32 s65, 63\n\t" \
    "s_cbranch_scc1 qk_last15_%=\n\t" \
    "v_readfirstlane_b32 s69, v25\n\t" \
    "s_add_u32 s65, s65, 1\n\t" \
    "s_max_i32 s83, s83, s69\n\t" \
    "s_cmp_gt_u32 s65, 6\n\t" \
    "s_cselect_b32 s83, 99, s83\n\t" \
    "v_mov_b32 v72, 0\n\t" \
    "v_mov_b32 v73, 0\n\t" \
    "v_mov_b32 v74, 0\n\t" \
    "v_mov_b32 v75, 0\n\t" \
    "v_mov_b32 v76, 0\n\t" \
    "v_mov_b32 v77, 0\n\t" \
    "v_mov_b32 v78, 0\n\t" \
    "v_mov_b32 v79, 0\n\t" \
    "v_mov_b32 v80, 0\n\t" \
    "v_mov_b32 v81, 0\n\t" \
    "v_mov_b32 v82, 0\n\t" \
    "v_mov_b32 v83, 0\n\t" \
    "v_mov_b32 v84, 0\n\t" \
    "v_mov_b32 v85, 0\n\t" \
    "v_mov_b32 v86, 0\n\t" \
    "v_mov_b32 v87, 0\n\t" \
    "v_mov_b32 v88, 0\n\t" \
    "v_mov_b32 v89, 0\n\t" \
    "v_mov_b32 v90, 0\n\t" \
    "v_mov_b32 v91, 0\n\t" \
    "v_mov_b32 v92, 0\n\t" \
    "v_mov_b32 v93, 0\n\t" \
    "v_mov_b32 v94, 0\n\t" \
    "v_mov_b32 v95, 0\n\t" \
    "v_mov_b32 v96, 0\n\t" \
    "v_mov_b32 v97, 0\n\t" \
    "v_mov_b32 v98, 0\n\t" \
    "v_mov_b32 v99, 0\n\t" \
    "s_add_u32 s69, s65, 0\n\t" \
    "s_and_b32 s69, s69, 7\n\t" \
    "s_lshl_b32 s69, s69, 18\n\t" \
    "s_add_u32 s69, s69, s62\n\t" \
    "s_add_u32 s46, s36, s69\n\t" \
    "s_addc_u32 s47, s37, 0\n\t" \
    "s_mul_i32 s70, s67, 7\n\t" \
    "s_add_u32 s69, s65, 0\n\t" \
    "s_xor_b32 s69, s69, s70\n\t" \
    "s_and_b32 s69, s69, 63\n\t" \
    "s_lshl_b32 s69, s69, 13\n\t" \
    "s_mul_i32 s70, s67, 524288\n\t" \
    "s_add_u32 s69, s69, s70\n\t" \
    "s_add_u32 s69, s69, 23068672\n\t" \
    "s_add_u32 s38, s36, s69\n\t" \
    "s_addc_u32 s39, s37, 0\n\t" \
    "qk_WF28_%=:\n\t" \
    "s_cmp_ge_i32 s83, s65\n\t" \
    "s_cbranch_scc1 qk_WFok29_%=\n\t" \
    "global_load_dword v160, v24, s[36:37] sc1\n\t" \
    "s_waitcnt vmcnt(0)\n\t" \
    "v_readfirstlane_b32 s83, v160\n\t" \
    "s_nop 1\n\t" \
    "s_cmp_ge_i32 s83, s65\n\t" \
    "s_cbranch_scc1 qk_WFok29_%=\n\t" \
    "s_cmp_eq_u32 s68, 0\n\t" \
    "s_cbranch_scc1 qk_F30_%=\n\t" \
    "s_sub_u32 s68, s68, 1\n\t" \
    "s_sleep 2\n\t" \
    "s_branch qk_WF28_%=\n\t" \
    "qk_F30_%=:\n\t" \
    "v_mov_b32 v160, 5767424\n\t" \
    "v_mov_b32 v161, 9\n\t" \
    "global_store_dword v160, v161, s[36:37] sc1\n\t" \
    "s_waitcnt vmcnt(0)\n\t" \
    "s_branch qk_WFok29_%=\n\t" \
    "qk_WFok29_%=:\n\t" \
    "global_load_dwordx4 v[100:103], v0, s[38:39] offset:0 sc1\n\t" \
    "global_load_dwordx4 v[104:107], v0, s[38:39] offset:2048 sc1\n\t" \
    "global_load_dwordx4 v[108:111], v15, s[38:39] offset:0 sc1\n\t" \
    "global_load_dwordx4 v[112:115], v15, s[38:39] offset:2048 sc1\n\t" \
    "global_load_dwordx4 v[116:119], v0, s[38:39] offset:1024 sc1\n\t" \
    "global_load_dwordx4 v[120:123], v0, s[38:39] offset:3072 sc1\n\t" \
    "global_load_dwordx4 v[124:127], v15, s[38:39] offset:1024 sc1\n\t" \
    "global_load_dwordx4 v[128:131], v15, s[38:39] offset:3072 sc1\n\t" \
    "s_cmp_gt_i32 s83, s65\n\t" \
    "s_cbranch_scc1 qk_nf31_%=\n\t" \
    "global_load_dword v25, v24, s[36:37] sc1\n\t" \
    "qk_nf31_%=:\n\t" \
    "s_barrier\n\t" \
    "ds_read_b32 v168, v11 offset:20480\n\t" \
    "s_waitcnt lgkmcnt(0)\n\t" \
    "v_readfirstlane_b32 s70, v168\n\t" \
    "ds_write_b32 v12, v31 offset:0\n\t" \
    "ds_write_b32 v12, v31 offset:64\n\t" \
    "s_branch qk_go16_%=\n\t" \
    "qk_Gbad21_%=:\n\t" \
    "s_waitcnt lgkmcnt(0)\n\t" \
    "s_nop 7\n\t" \
    "v_mov_b32 v72, 0\n\t" \
    "v_mov_b32 v73, 0\n\t" \
    "v_mov_b32 v74, 0\n\t" \
    "v_mov_b32 v75, 0\n\t" \
    "v_mov_b32 v76, 0\n\t" \
    "v_mov_b32 v77, 0\n\t" \
    "v_mov_b32 v78, 0\n\t" \
    "v_mov_b32 v79, 0\n\t" \
    "v_mov_b32 v80, 0\n\t" \
    "v_mov_b32 v81, 0\n\t" \
    "v_mov_b32 v82, 0\n\t" \
    "v_mov_b32 v83, 0\n\t" \
    "v_mov_b32 v84, 0\n\t" \
    "v_mov_b32 v85, 0\n\t" \
    "v_mov_b32 v86, 0\n\t" \
    "v_mov_b32 v87, 0\n\t" \
    "s_cmp_eq_u32 s68, 0\n\t" \
    "s_cbranch_scc1 qk_F32_%=\n\t" \
    "s_sub_u32 s68, s68, 1\n\t" \
    "s_branch qk_G20_%=\n\t" \
    "qk_F32_%=:\n\t" \
    "v_mov_b32 v160, 5767424\n\t" \
    "v_mov_b32 v161, 1\n\t" \
    "global_store_dword v160, v161, s[36:37] sc1\n\t" \
    "s_waitcnt vmcnt(0)\n\t" \
    "s_branch qk_Gc22_%=\n\t" \
    "qk_Gslow23_%=:\n\t" \
    "v_max_u32 v160, v36, v37\n\t" \
    "v_max3_u32 v160, v160, v38, v39\n\t" \
    "v_max3_u32 v160, v160, v40, v41\n\t" \
    "v_max3_u32 v160, v160, v42, v43\n\t" \
    "v_max3_u32 v160, v160, v44, v45\n\t" \
    "v_max3_u32 v160, v160, v46, v47\n\t" \
    "v_max3_u32 v160, v160, v48, v49\n\t" \
    "v_max3_u32 v160, v160, v50, v51\n\t" \
    "v_max3_u32 v160, v160, v52, v53\n\t" \
    "v_max3_u32 v160, v160, v54, v55\n\t" \
    "v_max3_u32 v160, v160, v56, v57\n\t" \
    "v_max3_u32 v160, v160, v58, v59\n\t" \
    "v_max3_u32 v160, v160, v60, v61\n\t" \
    "v_max3_u32 v160, v160, v62, v63\n\t" \
    "v_max3_u32 v160, v160, v64, v65\n\t" \
    "v_max3_u32 v160, v160, v66, v67\n\t" \
    "v_max3_u32 v160, v160, v68, v69\n\t" \
    "v_max3_u32 v160, v160, v70, v71\n\t" \
    "v_cmp_eq_u32 vcc, -1, v160\n\t" \
    "s_cbranch_vccz qk_Gback24_%=\n\t" \
    "s_waitcnt lgkmcnt(0)\n\t" \
    "v_sub_u32 v160, v1, v3\n\t" \
    "s_nop 0\n\t" \
    "v_readfirstlane_b32 s69, v160\n\t" \
    "v_add_u32 v163, 20480, v0\n\t" \
    "s_nop 7\n\t" \
    "s_add_u32 s30, s46, s69\n\t" \
    "s_addc_u32 s31, s47, 0\n\t" \
    "qk_Gag33_%=:\n\t" \
    "global_load_dwordx4 v[36:39], v1, s[46:47] sc1\n\t" \
    "global_load_dwordx4 v[40:43], v23, s[46:47] sc1\n\t" \
    "global_load_dwordx4 v[44:47], v163, s[30:31] sc1\n\t" \
    "global_load_dwordx4 v[48:51], v4, s[30:31] sc1\n\t" \
    "global_load_dwordx4 v[52:55], v5, s[30:31] sc1\n\t" \
    "global_load_dwordx4 v[56:59], v6, s[30:31] sc1\n\t" \
    "global_load_dwordx4 v[60:63], v7, s[30:31] sc1\n\t" \
    "global_load_dwordx4 v[64:67], v8, s[30:31] sc1\n\t" \
    "global_load_dwordx4 v[68:71], v9, s[30:31] sc1\n\t" \
    "s_waitcnt vmcnt(0)\n\t" \
    "v_max_u32 v160, v36, v37\n\t" \
    "v_max3_u32 v160, v160, v38, v39\n\t" \
    "v_max3_u32 v160, v160, v40, v41\n\t" \
    "v_max3_u32 v160, v160, v42, v43\n\t" \
    "v_max3_u32 v160, v160, v44, v45\n\t" \
    "v_max3_u32 v160, v160, v46, v47\n\t" \
    "v_max3_u32 v160, v160, v48, v49\n\t" \
    "v_max3_u32 v160, v160, v50, v51\n\t" \
    "v_max3_u32 v160, v160, v52, v53\n\t" \
    "v_max3_u32 v160, v160, v54, v55\n\t" \
    "v_max3_u32 v160, v160, v56, v57\n\t" \
    "v_max3_u32 v160, v160, v58, v59\n\t" \
    "v_max3_u32 v160, v160, v60, v61\n\t" \
    "v_max3_u32 v160, v160, v62, v63\n\t" \
    "v_max3_u32 v160, v160, v64, v65\n\t" \
    "v_max3_u32 v160, v160, v66, v67\n\t" \
    "v_max3_u32 v160, v160, v68, v69\n\t" \
    "v_max3_u32 v160, v160, v70, v71\n\t" \
    "v_cmp_eq_u32 vcc, -1, v160\n\t" \
    "s_cbranch_vccz qk_Gredo34_%=\n\t" \
    "s_cmp_eq_u32 s68, 0\n\t" \
    "s_cbranch_scc1 qk_F35_%=\n\t" \
    "s_sub_u32 s68, s68, 1\n\t" \
    "s_sleep 1\n\t" \
    "s_branch qk_Gag33_%=\n\t" \
    "qk_F35_%=:\n\t" \
    "v_mov_b32 v160, 5767424\n\t" \
    "v_mov_b32 v161, 2\n\t" \
    "global_store_dword v160, v161, s[36:37] sc1\n\t" \
    "s_waitcnt vmcnt(0)\n\t" \
    "s_branch qk_Gredo34_%=\n\t" \
    "qk_Gredo34_%=:\n\t" \
    "v_mov_b32 v72, 0\n\t" \
    "v_mov_b32 v73, 0\n\t" \
    "v_mov_b32 v74, 0\n\t" \
    "v_mov_b32 v75, 0\n\t" \
    "v_mov_b32 v76, 0\n\t" \
    "v_mov_b32 v77, 0\n\t" \
    "v_mov_b32 v78, 0\n\t" \
    "v_mov_b32 v79, 0\n\t" \
    "v_mov_b32 v80, 0\n\t" \
    "v_mov_b32 v81, 0\n\t" \
    "v_mov_b32 v82, 0\n\t" \
    "v_mov_b32 v83, 0\n\t" \
    "v_mov_b32 v84, 0\n\t" \
    "v_mov_b32 v85, 0\n\t" \
    "v_mov_b32 v86, 0\n\t" \
    "v_mov_b32 v87, 0\n\t" \
    "v_smfmac_f32_16x16x64_bf16 v[72:75], v[36:39], a[0:7], v10\n\t" \
    "v_smfmac_f32_16x16x64_bf16 v[72:75], v[40:43], a[8:15], v10\n\t" \
    "v_smfmac_f32_16x16x64_bf16 v[72:75], v[44:47], a[64:71], v10\n\t" \
    "v_smfmac_f32_16x16x64_bf16 v[72:75], v[48:51], a[16:23], v10\n\t" \
    "v_smfmac_f32_16x16x64_bf16 v[72:75], v[52:55], a[24:31], v10\n\t" \
    "v_smfmac_f32_16x16x64_bf16 v[72:75], v[56:59], a[32:39], v10\n\t" \
    "v_smfmac_f32_16x16x64_bf16 v[72:75], v[60:63], a[40:47], v10\n\t" \
    "v_smfmac_f32_16x16x64_bf16 v[72:75], v[64:67], a[48:55], v10\n\t" \
    "v_smfmac_f32_16x16x64_bf16 v[72:75], v[68:71], a[56:63], v10\n\t" \
    "v_smfmac_f32_16x16x64_bf16 v[76:79], v[36:39], a[72:79], v10\n\t" \
    "v_smfmac_f32_16x16x64_bf16 v[76:79], v[40:43], a[80:87], v10\n\t" \
    "v_smfmac_f32_16x16x64_bf16 v[76:79], v[44:47], a[136:143], v10\n\t" \
    "v_smfmac_f32_16x16x64_bf16 v[76:79], v[48:51], a[88:95], v10\n\t" \
    "v_smfmac_f32_16x16x64_bf16 v[76:79], v[52:55], a[96:103], v10\n\t" \
    "v_smfmac_f32_16x16x64_bf16 v[76:79], v[56:59], a[104:111], v10\n\t" \
    "v_smfmac_f32_16x16x64_bf16 v[76:79], v[60:63], a[112:119], v10\n\t" \
    "v_smfmac_f32_16x16x64_bf16 v[76:79], v[64:67], a[120:127], v10\n\t" \
    "v_smfmac_f32_16x16x64_bf16 v[76:79], v[68:71], a[128:135], v10\n\t" \
    "v_smfmac_f32_16x16x64_bf16 v[80:83], v[36:39], a[144:151], v10\n\t" \
    "v_smfmac_f32_16x16x64_bf16 v[80:83], v[40:43], a[152:159], v10\n\t" \
    "v_smfmac_f32_16x16x64_bf16 v[80:83], v[44:47], a[208:215], v10\n\t" \
    "v_smfmac_f32_16x16x64_bf16 v[80:83], v[48:51], a[160:167], v10\n\t" \
    "v_smfmac_f32_16x16x64_bf16 v[80:83], v[52:55], a[168:175], v10\n\t" \
    "v_smfmac_f32_16x16x64_bf16 v[80:83], v[56:59], a[176:183], v10\n\t" \
    "v_smfmac_f32_16x16x64_bf16 v[80:83], v[60:63], a[184:191], v10\n\t" \
    "v_smfmac_f32_16x16x64_bf16 v[80:83], v[64:67], a[192:199], v10\n\t" \
    "v_smfmac_f32_16x16x64_bf16 v[80:83], v[68:71], a[200:207], v10\n\t" \
    "v_smfmac_f32_16x16x64_bf16 v[84:87], v[36:39], a[216:223], v10\n\t" \
    "v_smfmac_f32_16x16x64_bf16 v[84:87], v[40:43], a[224:231], v10\n\t" \
    "v_smfmac_f32_16x16x64_bf16 v[84:87], v[44:47], v[248:255], v10\n\t" \
    "v_smfmac_f32_16x16x64_bf16 v[84:87], v[48:51], a[232:239], v10\n\t" \
    "v_smfmac_f32_16x16x64_bf16 v[84:87], v[52:55], a[240:247], v10\n\t" \
    "v_smfmac_f32_16x16x64_bf16 v[84:87], v[56:59], a[248:255], v10\n\t" \
    "v_smfmac_f32_16x16x64_bf16 v[84:87], v[60:63], v[224:231], v10\n\t" \
    "v_smfmac_f32_16x16x64_bf16 v[84:87], v[64:67], v[232:239], v10\n\t" \
    "v_smfmac_f32_16x16x64_bf16 v[84:87], v[68:71], v[240:247], v10\n\t" \
    "v_add_f32 v72, v72, v74\n\t" \
    "v_add_f32 v73, v73, v75\n\t" \
    "v_cvt_pk_bf16_f32 v156, v72, v73\n\t" \
    "ds_write_b32 v12, v156 offset:0\n\t" \
    "v_add_f32 v76, v76, v78\n\t" \
    "v_add_f32 v77, v77, v79\n\t" \
    "v_cvt_pk_bf16_f32 v157, v76, v77\n\t" \
    "ds_write_b32 v12, v157 offset:64\n\t" \
    "v_add_f32 v80, v80, v82\n\t" \
    "v_add_f32 v81, v81, v83\n\t" \
    "v_cvt_pk_bf16_f32 v158, v80, v81\n\t" \
    "ds_write_b32 v16, v158 offset:0\n\t" \
    "v_pk_add_f32 v[84:85], v[84:85], v[86:87]\n\t" \
    "v_cvt_pk_bf16_f32 v159, v84, v85\n\t" \
    "ds_write_b32 v16, v159 offset:256\n\t" \
    "s_branch qk_Gback24_%=\n\t" \
    "qk_qs25_%=:\n\t" \
    "s_cmp_eq_u32 s91, s65\n\t" \
    "s_cbranch_scc1 qk_qb227_%=\n\t" \
    "s_mov_b32 s91, s65\n\t" \
    "s_nop 7\n\t" \
    "ds_read_b64_tr_b16 v[132:133], v13 offset:0\n\t" \
    "ds_read_b64_tr_b16 v[134:135], v13 offset:128\n\t" \
    "ds_read_b64_tr_b16 v[136:137], v13 offset:1024\n\t" \
    "ds_read_b64_tr_b16 v[138:139], v13 offset:1152\n\t" \
    "v_mov_b32 v88, 0\n\t" \
    "v_mov_b32 v89, 0\n\t" \
    "v_mov_b32 v90, 0\n\t" \
    "v_mov_b32 v91, 0\n\t" \
    "v_mov_b32 v92, 0\n\t" \
    "v_mov_b32 v93, 0\n\t" \
    "v_mov_b32 v94, 0\n\t" \
    "v_mov_b32 v95, 0\n\t" \
    "v_mov_b32 v96, 0\n\t" \
    "v_mov_b32 v97, 0\n\t" \
    "v_mov_b32 v98, 0\n\t" \
    "v_mov_b32 v99, 0\n\t" \
    "s_waitcnt lgkmcnt(0)\n\t" \
    "v_smfmac_f32_16x16x64_bf16 v[88:91], v[132:135], v[100:107], v10\n\t" \
    "v_smfmac_f32_16x16x64_bf16 v[92:95], v[132:135], v[116:123], v10\n\t" \
    "v_smfmac_f32_16x16x64_bf16 v[88:91], v[136:139], v[108:115], v10\n\t" \
    "v_smfmac_f32_16x16x64_bf16 v[92:95], v[136:139], v[124:131], v10\n\t" \
    "s_nop 7\n\t" \
    "s_branch qk_qb26_%=\n\t" \
    "qk_last15_%=:\n\t" \
    "s_endpgm\n\t" \
    "qk_loop12_%=:\n\t" \
    "v_mov_b32 v72, 0\n\t" \
    "v_mov_b32 v73, 0\n\t" \
    "v_mov_b32 v74, 0\n\t" \
    "v_mov_b32 v75, 0\n\t" \
    "v_mov_b32 v76, 0\n\t" \
    "v_mov_b32 v77, 0\n\t" \
    "v_mov_b32 v78, 0\n\t" \
    "v_mov_b32 v79, 0\n\t" \
    "v_mov_b32 v80, 0\n\t" \
    "v_mov_b32 v81, 0\n\t" \
    "v_mov_b32 v82, 0\n\t" \
    "v_mov_b32 v83, 0\n\t" \
    "v_mov_b32 v84, 0\n\t" \
    "v_mov_b32 v85, 0\n\t" \
    "v_mov_b32 v86, 0\n\t" \
    "v_mov_b32 v87, 0\n\t" \
    "v_mov_b32 v88, 0\n\t" \
    "v_mov_b32 v89, 0\n\t" \
    "v_mov_b32 v90, 0\n\t" \
    "v_mov_b32 v91, 0\n\t" \
    "v_mov_b32 v92, 0\n\t" \
    "v_mov_b32 v93, 0\n\t" \
    "v_mov_b32 v94, 0\n\t" \
    "v_mov_b32 v95, 0\n\t" \
    "v_mov_b32 v96, 0\n\t" \
    "v_mov_b32 v97, 0\n\t" \
    "v_mov_b32 v98, 0\n\t" \
    "v_mov_b32 v99, 0\n\t" \
    "s_add_u32 s69, s65, 0\n\t" \
    "s_and_b32 s69, s69, 7\n\t" \
    "s_lshl_b32 s69, s69, 18\n\t" \
    "s_add_u32 s69, s69, s62\n\t" \
    "s_add_u32 s46, s36, s69\n\t" \
    "s_addc_u32 s47, s37, 0\n\t" \
    "s_sub_u32 s70, s79, s74\n\t" \
    "s_max_i32 s70, s70, 0\n\t" \
    "s_min_i32 s70, s70, 16000\n\t" \
    "s_lshl_b32 s70, s70, 1\n\t" \
    "s_mul_hi_u32 s70, s70, s77\n\t" \
    "s_add_u32 s70, s70, s73\n\t" \
    ".p2align 6\n\t" \
    "s_nop 0\n\t" \
    "s_nop 0\n\t" \
    "s_nop 0\n\t" \
    "s_nop 0\n\t" \
    "s_nop 0\n\t" \
    "s_nop 0\n\t" \
    "qk_go37_%=:\n\t" \
    "s_getpc_b64 s[30:31]\n\t" \
    "qk_Wp39_%=:\n\t" \
    "s_add_u32 s30, s30, qk_We40_%=-qk_Wp39_%=\n\t" \
    "s_addc_u32 s31, s31, 0\n\t" \
    "s_sub_u32 s70, s70, 96\n\t" \
    "qk_W38_%=:\n\t" \
    "s_memtime s[56:57]\n\t" \
    "s_waitcnt lgkmcnt(0)\n\t" \
    "s_sub_u32 s69, s70, s56\n\t" \
    "s_cmp_gt_i32 s69, 80\n\t" \
    "s_cbranch_scc1 qk_W38_%=\n\t" \
    "s_max_i32 s69, s69, 0\n\t" \
    "s_and_b32 s69, s69, -4\n\t" \
    "s_sub_u32 s30, s30, s69\n\t" \
    "s_subb_u32 s31, s31, 0\n\t" \
    "s_setpc_b64 s[30:31]\n\t" \
    "s_nop 0\n\t" \
    "s_nop 0\n\t" \
    "s_nop 0\n\t" \
    "s_nop 0\n\t" \
    "s_nop 0\n\t" \
    "s_nop 0\n\t" \
    "s_nop 0\n\t" \
    "s_nop 0\n\t" \
    "s_nop 0\n\t" \
    "s_nop 0\n\t" \
    "s_nop 0\n\t" \
    "s_nop 0\n\t" \
    "s_nop 0\n\t" \
    "s_nop 0\n\t" \
    "s_nop 0\n\t" \
    "s_nop 0\n\t" \
    "s_nop 0\n\t" \
    "s_nop 0\n\t" \
    "s_nop 0\n\t" \
    "s_nop 0\n\t" \
    "qk_We40_%=:\n\t" \
    "qk_G41_%=:\n\t" \
    "global_load_dwordx4 v[36:39], v1, s[46:47] sc1\n\t" \
    "global_load_dword v161, v2, s[46:47] sc1\n\t" \
    "global_load_dwordx4 v[40:43], v23, s[46:47] sc1\n\t" \
    "s_waitcnt vmcnt(2)\n\t" \
    "ds_write_b128 v3, v[36:39]\n\t" \
    "v_smfmac_f32_16x16x64_bf16 v[72:75], v[36:39], a[16:23], v10\n\t" \
    "s_waitcnt vmcnt(1)\n\t" \
    "ds_write_b32 v22, v161 offset:19712\n\t" \
    "v_smfmac_f32_16x16x64_bf16 v[76:79], v[36:39], a[88:95], v10\n\t" \
    "s_waitcnt vmcnt(0)\n\t" \
    "ds_write_b128 v3, v[40:43] offset:1024\n\t" \
    "v_max3_u32 v160, v36, v40, v161\n\t" \
    "v_cmp_eq_u32 vcc, -1, v160\n\t" \
    "v_smfmac_f32_16x16x64_bf16 v[80:83], v[36:39], a[160:167], v10\n\t" \
    "v_smfmac_f32_16x16x64_bf16 v[84:87], v[36:39], a[232:239], v10\n\t" \
    "v_smfmac_f32_16x16x64_bf16 v[72:75], v[40:43], a[24:31], v10\n\t" \
    "s_cbranch_vccnz qk_Gbad42_%=\n\t" \
    "qk_Gc43_%=:\n\t" \
    "s_waitcnt lgkmcnt(0)\n\t" \
    "s_barrier\n\t" \
    "ds_read_b128 v[48:51], v4\n\t" \
    "ds_read_b128 v[52:55], v5\n\t" \
    "v_smfmac_f32_16x16x64_bf16 v[76:79], v[40:43], a[96:103], v10\n\t" \
    "ds_read_b128 v[56:59], v6\n\t" \
    "ds_read_b128 v[60:63], v7\n\t" \
    "v_smfmac_f32_16x16x64_bf16 v[80:83], v[40:43], a[168:175], v10\n\t" \
    "ds_read_b128 v[64:67], v8\n\t" \
    "v_smfmac_f32_16x16x64_bf16 v[84:87], v[40:43], a[240:247], v10\n\t" \
    "ds_read_b128 v[68:71], v9\n\t" \
    "s_waitcnt lgkmcnt(5)\n\t" \
    "v_smfmac_f32_16x16x64_bf16 v[72:75], v[48:51], a[32:39], v10\n\t" \
    "ds_read_b128 v[44:47], v0 offset:19456\n\t" \
    "s_waitcnt lgkmcnt(5)\n\t" \
    "v_smfmac_f32_16x16x64_bf16 v[72:75], v[52:55], a[40:47], v10\n\t" \
    "s_waitcnt lgkmcnt(4)\n\t" \
    "v_smfmac_f32_16x16x64_bf16 v[72:75], v[56:59], a[48:55], v10\n\t" \
    "s_waitcnt lgkmcnt(3)\n\t" \
    "v_smfmac_f32_16x16x64_bf16 v[72:75], v[60:63], a[56:63], v10\n\t" \
    "s_waitcnt lgkmcnt(2)\n\t" \
    "v_smfmac_f32_16x16x64_bf16 v[72:75], v[64:67], a[0:7], v10\n\t" \
    "s_waitcnt lgkmcnt(1)\n\t" \
    "v_smfmac_f32_16x16x64_bf16 v[72:75], v[68:71], a[8:15], v10\n\t" \
    "s_waitcnt lgkmcnt(0)\n\t" \
    "v_smfmac_f32_16x16x64_bf16 v[72:75], v[44:47], a[64:71], v10\n\t" \
    "s_nop 2\n\t" \
    "v_smfmac_f32_16x16x64_bf16 v[76:79], v[48:51], a[104:111], v10\n\t" \
    "s_nop 2\n\t" \
    "v_smfmac_f32_16x16x64_bf16 v[76:79], v[52:55], a[112:119], v10\n\t" \
    "v_add_f32 v72, v72, v74\n\t" \
    "v_smfmac_f32_16x16x64_bf16 v[76:79], v[56:59], a[120:127], v10\n\t" \
    "v_add_f32 v73, v73, v75\n\t" \
    "v_smfmac_f32_16x16x64_bf16 v[76:79], v[60:63], a[128:135], v10\n\t" \
    "v_cvt_pk_bf16_f32 v156, v72, v73\n\t" \
    "v_smfmac_f32_16x16x64_bf16 v[76:79], v[64:67], a[72:79], v10\n\t" \
    "ds_write_b32 v12, v156 offset:0\n\t" \
    "v_smfmac_f32_16x16x64_bf16 v[76:79], v[68:71], a[80:87], v10\n\t" \
    "v_smfmac_f32_16x16x64_bf16 v[76:79], v[44:47], a[136:143], v10\n\t" \
    "s_nop 2\n\t" \
    "v_smfmac_f32_16x16x64_bf16 v[80:83], v[48:51], a[176:183], v10\n\t" \
    "s_nop 2\n\t" \
    "v_smfmac_f32_16x16x64_bf16 v[80:83], v[52:55], a[184:191], v10\n\t" \
    "v_add_f32 v76, v76, v78\n\t" \
    "v_smfmac_f32_16x16x64_bf16 v[80:83], v[56:59], a[192:199], v10\n\t" \
    "v_add_f32 v77, v77, v79\n\t" \
    "v_smfmac_f32_16x16x64_bf16 v[80:83], v[60:63], a[200:207], v10\n\t" \
    "v_cvt_pk_bf16_f32 v157, v76, v77\n\t" \
    "v_smfmac_f32_16x16x64_bf16 v[80:83], v[64:67], a[144:151], v10\n\t" \
    "ds_write_b32 v12, v157 offset:64\n\t" \
    "v_smfmac_f32_16x16x64_bf16 v[80:83], v[68:71], a[152:159], v10\n\t" \
    "v_smfmac_f32_16x16x64_bf16 v[80:83], v[44:47], a[208:215], v10\n\t" \
    "s_nop 2\n\t" \
    "v_smfmac_f32_16x16x64_bf16 v[84:87], v[48:51], a[248:255], v10\n\t" \
    "s_nop 2\n\t" \
    "v_smfmac_f32_16x16x64_bf16 v[84:87], v[52:55], v[224:231], v10\n\t" \
    "v_add_f32 v80, v80, v82\n\t" \
    "v_smfmac_f32_16x16x64_bf16 v[84:87], v[56:59], v[232:239], v10\n\t" \
    "v_add_f32 v81, v81, v83\n\t" \
    "v_smfmac_f32_16x16x64_bf16 v[84:87], v[60:63], v[240:247], v10\n\t" \
    "v_cvt_pk_bf16_f32 v158, v80, v81\n\t" \
    "v_smfmac_f32_16x16x64_bf16 v[84:87], v[64:67], a[216:223], v10\n\t" \
    "ds_write_b32 v16, v158 offset:0\n\t" \
    "v_smfmac_f32_16x16x64_bf16 v[84:87], v[68:71], a[224:231], v10\n\t" \
    "ds_read_b64_tr_b16 v[132:133], v13 offset:0\n\t" \
    "ds_read_b64_tr_b16 v[134:135], v13 offset:128\n\t" \
    "v_smfmac_f32_16x16x64_bf16 v[84:87], v[44:47], v[248:255], v10\n\t" \
    "ds_read_b64_tr_b16 v[136:137], v13 offset:1024\n\t" \
    "ds_read_b64_tr_b16 v[138:139], v13 offset:1152\n\t" \
    "s_nop 4\n\t" \
    "v_cmp_u_f32 vcc, v72, v73\n\t" \
    "v_pk_add_f32 v[84:85], v[84:85], v[86:87]\n\t" \
    "v_cvt_pk_bf16_f32 v159, v84, v85\n\t" \
    "ds_write_b32 v16, v159 offset:256\n\t" \
    "s_cbranch_vccnz qk_Gslow44_%=\n\t" \
    "qk_Gback45_%=:\n\t" \
    "s_waitcnt lgkmcnt(3)\n\t" \
    "v_smfmac_f32_16x16x64_bf16 v[88:91], v[132:135], v[100:107], v10\n\t" \
    "s_waitcnt lgkmcnt(1)\n\t" \
    "v_smfmac_f32_16x16x64_bf16 v[88:91], v[136:139], v[108:115], v10\n\t" \
    "s_waitcnt lgkmcnt(0)\n\t" \
    "s_barrier\n\t" \
    "ds_read_b64_tr_b16 v[140:141], v17 offset:0\n\t" \
    "v_smfmac_f32_16x16x64_bf16 v[92:95], v[132:135], v[116:123], v10\n\t" \
    "ds_read_b64_tr_b16 v[142:143], v17 offset:256\n\t" \
    "ds_read_b64_tr_b16 v[144:145], v17 offset:512\n\t" \
    "ds_read_b64_tr_b16 v[146:147], v17 offset:768\n\t" \
    "ds_read_b64_tr_b16 v[148:149], v17 offset:1024\n\t" \
    "v_smfmac_f32_16x16x64_bf16 v[92:95], v[136:139], v[124:131], v10\n\t" \
    "ds_read_b64_tr_b16 v[150:151], v17 offset:1280\n\t" \
    "ds_read_b64_tr_b16 v[152:153], v17 offset:1536\n\t" \
    "ds_read_b64_tr_b16 v[154:155], v17 offset:1792\n\t" \
    "v_smfmac_f32_16x16x64_bf16 v[184:187], v[132:135], v[100:107], v10\n\t" \
    "qk_qb47_%=:\n\t" \
    "s_waitcnt lgkmcnt(4)\n\t" \
    "v_smfmac_f32_16x16x64_bf16 v[96:99], v[132:135], v[140:147], v10\n\t" \
    "s_waitcnt lgkmcnt(0)\n\t" \
    "v_smfmac_f32_16x16x64_bf16 v[96:99], v[136:139], v[148:155], v10\n\t" \
    "v_add_f32 v168, v89, v91\n\t" \
    "v_add_f32 v169, v93, v95\n\t" \
    "v_cmp_u_f32 vcc, v168, v169\n\t" \
    "v_cvt_pk_bf16_f32 v168, 0, v168\n\t" \
    "v_cvt_pk_bf16_f32 v169, 0, v169\n\t" \
    "v_mul_f32 v168, v168, v26\n\t" \
    "v_mul_f32 v169, v169, v26\n\t" \
    "v_cvt_pk_bf16_f32 v168, 0, v168\n\t" \
    "v_add_f32 v172, v97, v99\n\t" \
    "v_cvt_pk_bf16_f32 v169, 0, v169\n\t" \
    "v_max_f32 v178, v168, v169\n\t" \
    "v_cvt_pk_bf16_f32 v172, 0, v172\n\t" \
    "v_mul_f32 v172, v172, v26\n\t" \
    "v_max_f32_dpp v178, v178, v178 row_mirror row_mask:0xf bank_mask:0xf\n\t" \
    "v_cvt_pk_bf16_f32 v172, 0, v172\n\t" \
    "v_max_f32 v173, v178, v172\n\t" \
    "v_mul_f32 v168, v168, v28\n\t" \
    "v_mul_f32 v169, v169, v28\n\t" \
    "v_max_f32_dpp v173, v173, v173 quad_perm:[1,0,3,2] row_mask:0xf bank_mask:0xf\n\t" \
    "v_mul_f32 v172, v172, v28\n\t" \
    "s_cbranch_vccnz qk_qs46_%=\n\t" \
    "qk_qb248_%=:\n\t" \
    "v_max_f32_dpp v173, v173, v173 quad_perm:[2,3,0,1] row_mask:0xf bank_mask:0xf\n\t" \
    "s_nop 1\n\t" \
    "v_max_f32_dpp v173, v173, v173 row_half_mirror row_mask:0xf bank_mask:0xf\n\t" \
    "v_fma_f32 v172, -v173, v28, v172\n\t" \
    "v_fma_f32 v168, -v173, v28, v168\n\t" \
    "v_fma_f32 v169, -v173, v28, v169\n\t" \
    "v_exp_f32 v172, v172\n\t" \
    "v_exp_f32 v168, v168\n\t" \
    "v_exp_f32 v169, v169\n\t" \
    "v_fma_f32 v173, v172, 0.5, v168\n\t" \
    "v_add_f32 v173, v173, v169\n\t" \
    "v_mov_b32_dpp v174, v172 quad_perm:[0,0,2,2] row_mask:0xf bank_mask:0xf\n\t" \
    "v_mov_b32_dpp v175, v172 quad_perm:[1,1,3,3] row_mask:0xf bank_mask:0xf\n\t" \
    "v_add_f32_dpp v173, v173, v173 quad_perm:[1,0,3,2] row_mask:0xf bank_mask:0xf\n\t" \
    "v_mov_b32_dpp v176, v168 quad_perm:[1,0,3,2] row_mask:0xf bank_mask:0xf\n\t" \
    "v_mov_b32_dpp v177, v169 quad_perm:[1,0,3,2] row_mask:0xf bank_mask:0xf\n\t" \
    "v_add_f32_dpp v173, v173, v173 quad_perm:[2,3,0,1] row_mask:0xf bank_mask:0xf\n\t" \
    "v_cndmask_b32 v168, v168, v177, s[48:49]\n\t" \
    "v_cndmask_b32 v169, v176, v169, s[48:49]\n\t" \
    "v_add_f32_dpp v173, v173, v173 row_half_mirror row_mask:0xf bank_mask:0xf\n\t" \
    "s_nop 1\n\t" \
    "v_add_f32_dpp v173, v173, v173 row_mirror row_mask:0xf bank_mask:0xf\n\t" \
    "v_rcp_f32 v173, v173\n\t" \
    "s_nop 0\n\t" \
    "v_pk_mul_f32 v[168:169], v[168:169], v[172:173] op_sel:[0,1] op_sel_hi:[1,1]\n\t" \
    "v_cvt_pk_bf16_f32 v176, v168, v169\n\t" \
    "global_store_dword v18, v176, s[46:47] sc1\n\t" \
    "global_store_dword v180, v176, s[46:47] sc1\n\t" \
    "global_store_dword v181, v176, s[46:47] sc1\n\t" \
    "global_store_dword v182, v176, s[46:47] sc1\n\t" \
    "v_pk_mul_f32 v[174:175], v[174:175], v[172:173] op_sel:[0,1] op_sel_hi:[1,1]\n\t" \
    "v_cvt_pk_bf16_f32 v177, v174, v175\n\t" \
    "global_store_dword v19, v177, s[46:47] sc1\n\t" \
    "s_cmp_eq_u32 s65, 63\n\t" \
    "s_cbranch_scc1 qk_last36_%=\n\t" \
    "v_readfirstlane_b32 s69, v25\n\t" \
    "s_add_u32 s65, s65, 1\n\t" \
    "s_max_i32 s83, s83, s69\n\t" \
    "s_cmp_gt_u32 s65, 6\n\t" \
    "s_cselect_b32 s83, 99, s83\n\t" \
    "v_mov_b32 v72, 0\n\t" \
    "v_mov_b32 v73, 0\n\t" \
    "v_mov_b32 v74, 0\n\t" \
    "v_mov_b32 v75, 0\n\t" \
    "v_mov_b32 v76, 0\n\t" \
    "v_mov_b32 v77, 0\n\t" \
    "v_mov_b32 v78, 0\n\t" \
    "v_mov_b32 v79, 0\n\t" \
    "v_mov_b32 v80, 0\n\t" \
    "v_mov_b32 v81, 0\n\t" \
    "v_mov_b32 v82, 0\n\t" \
    "v_mov_b32 v83, 0\n\t" \
    "v_mov_b32 v84, 0\n\t" \
    "v_mov_b32 v85, 0\n\t" \
    "v_mov_b32 v86, 0\n\t" \
    "v_mov_b32 v87, 0\n\t" \
    "v_mov_b32 v88, 0\n\t" \
    "v_mov_b32 v89, 0\n\t" \
    "v_mov_b32 v90, 0\n\t" \
    "v_mov_b32 v91, 0\n\t" \
    "v_mov_b32 v92, 0\n\t" \
    "v_mov_b32 v93, 0\n\t" \
    "v_mov_b32 v94, 0\n\t" \
    "v_mov_b32 v95, 0\n\t" \
    "v_mov_b32 v96, 0\n\t" \
    "v_mov_b32 v97, 0\n\t" \
    "v_mov_b32 v98, 0\n\t" \
    "v_mov_b32 v99, 0\n\t" \
    "s_add_u32 s69, s65, 0\n\t" \
    "s_and_b32 s69, s69, 7\n\t" \
    "s_lshl_b32 s69, s69, 18\n\t" \
    "s_add_u32 s69, s69, s62\n\t" \
    "s_add_u32 s46, s36, s69\n\t" \
    "s_addc_u32 s47, s37, 0\n\t" \
    "s_mul_i32 s70, s67, 7\n\t" \
    "s_add_u32 s69, s65, 0\n\t" \
    "s_xor_b32 s69, s69, s70\n\t" \
    "s_and_b32 s69, s69, 63\n\t" \
    "s_lshl_b32 s69, s69, 13\n\t" \
    "s_mul_i32 s70, s67, 524288\n\t" \
    "s_add_u32 s69, s69, s70\n\t" \
    "s_add_u32 s69, s69, 23068672\n\t" \
    "s_add_u32 s38, s36, s69\n\t" \
    "s_addc_u32 s39, s37, 0\n\t" \
    "qk_WF49_%=:\n\t" \
    "s_cmp_ge_i32 s83, s65\n\t" \
    "s_cbranch_scc1 qk_WFok50_%=\n\t" \
    "global_load_dword v160, v24, s[36:37] sc1\n\t" \
    "s_waitcnt vmcnt(0)\n\t" \
    "v_readfirstlane_b32 s83, v160\n\t" \
    "s_nop 1\n\t" \
    "s_cmp_ge_i32 s83, s65\n\t" \
    "s_cbranch_scc1 qk_WFok50_%=\n\t" \
    "s_cmp_eq_u32 s68, 0\n\t" \
    "s_cbranch_scc1 qk_F51_%=\n\t" \
    "s_sub_u32 s68, s68, 1\n\t" \
    "s_sleep 2\n\t" \
    "s_branch qk_WF49_%=\n\t" \
    "qk_F51_%=:\n\t" \
    "v_mov_b32 v160, 5767424\n\t" \
    "v_mov_b32 v161, 9\n\t" \
    "global_store_dword v160, v161, s[36:37] sc1\n\t" \
    "s_waitcnt vmcnt(0)\n\t" \
    "s_branch qk_WFok50_%=\n\t" \
    "qk_WFok50_%=:\n\t" \
    "global_load_dwordx4 v[100:103], v0, s[38:39] offset:0 sc1\n\t" \
    "global_load_dwordx4 v[104:107], v0, s[38:39] offset:2048 sc1\n\t" \
    "global_load_dwordx4 v[108:111], v15, s[38:39] offset:0 sc1\n\t" \
    "global_load_dwordx4 v[112:115], v15, s[38:39] offset:2048 sc1\n\t" \
    "global_load_dwordx4 v[116:119], v0, s[38:39] offset:1024 sc1\n\t" \
    "global_load_dwordx4 v[120:123], v0, s[38:39] offset:3072 sc1\n\t" \
    "global_load_dwordx4 v[124:127], v15, s[38:39] offset:1024 sc1\n\t" \
    "global_load_dwordx4 v[128:131], v15, s[38:39] offset:3072 sc1\n\t" \
    "s_cmp_gt_i32 s83, s65\n\t" \
    "s_cbranch_scc1 qk_nf52_%=\n\t" \
    "global_load_dword v25, v24, s[36:37] sc1\n\t" \
    "qk_nf52_%=:\n\t" \
    "s_barrier\n\t" \
    "ds_read_b32 v168, v11 offset:20480\n\t" \
    "s_waitcnt lgkmcnt(0)\n\t" \
    "v_readfirstlane_b32 s70, v168\n\t" \
    "ds_write_b32 v12, v31 offset:0\n\t" \
    "ds_write_b32 v12, v31 offset:64\n\t" \
    "s_branch qk_go37_%=\n\t" \
    "qk_Gbad42_%=:\n\t" \
    "s_waitcnt lgkmcnt(0)\n\t" \
    "s_nop 7\n\t" \
    "v_mov_b32 v72, 0\n\t" \
    "v_mov_b32 v73, 0\n\t" \
    "v_mov_b32 v74, 0\n\t" \
    "v_mov_b32 v75, 0\n\t" \
    "v_mov_b32 v76, 0\n\t" \
    "v_mov_b32 v77, 0\n\t" \
    "v_mov_b32 v78, 0\n\t" \
    "v_mov_b32 v79, 0\n\t" \
    "v_mov_b32 v80, 0\n\t" \
    "v_mov_b32 v81, 0\n\t" \
    "v_mov_b32 v82, 0\n\t" \
    "v_mov_b32 v83, 0\n\t" \
    "v_mov_b32 v84, 0\n\t" \
    "v_mov_b32 v85, 0\n\t" \
    "v_mov_b32 v86, 0\n\t" \
    "v_mov_b32 v87, 0\n\t" \
    "s_cmp_eq_u32 s68, 0\n\t" \
    "s_cbranch_scc1 qk_F53_%=\n\t" \
    "s_sub_u32 s68, s68, 1\n\t" \
    "s_branch qk_G41_%=\n\t" \
    "qk_F53_%=:\n\t" \
    "v_mov_b32 v160, 5767424\n\t" \
    "v_mov_b32 v161, 1\n\t" \
    "global_store_dword v160, v161, s[36:37] sc1\n\t" \
    "s_waitcnt vmcnt(0)\n\t" \
    "s_branch qk_Gc43_%=\n\t" \
    "qk_Gslow44_%=:\n\t" \
    "v_max_u32 v160, v36, v37\n\t" \
    "v_max3_u32 v160, v160, v38, v39\n\t" \
    "v_max3_u32 v160, v160, v40, v41\n\t" \
    "v_max3_u32 v160, v160, v42, v43\n\t" \
    "v_max3_u32 v160, v160, v44, v45\n\t" \
    "v_max3_u32 v160, v160, v46, v47\n\t" \
    "v_max3_u32 v160, v160, v48, v49\n\t" \
    "v_max3_u32 v160, v160, v50, v51\n\t" \
    "v_max3_u32 v160, v160, v52, v53\n\t" \
    "v_max3_u32 v160, v160, v54, v55\n\t" \
    "v_max3_u32 v160, v160, v56, v57\n\t" \
    "v_max3_u32 v160, v160, v58, v59\n\t" \
    "v_max3_u32 v160, v160, v60, v61\n\t" \
    "v_max3_u32 v160, v160, v62, v63\n\t" \
    "v_max3_u32 v160, v160, v64, v65\n\t" \
    "v_max3_u32 v160, v160, v66, v67\n\t" \
    "v_max3_u32 v160, v160, v68, v69\n\t" \
    "v_max3_u32 v160, v160, v70, v71\n\t" \
    "v_cmp_eq_u32 vcc, -1, v160\n\t" \
    "s_cbranch_vccz qk_Gback45_%=\n\t" \
    "s_waitcnt lgkmcnt(0)\n\t" \
    "v_sub_u32 v160, v1, v3\n\t" \
    "s_nop 0\n\t" \
    "v_readfirstlane_b32 s69, v160\n\t" \
    "v_add_u32 v163, 20480, v0\n\t" \
    "s_nop 7\n\t" \
    "s_add_u32 s30, s46, s69\n\t" \
    "s_addc_u32 s31, s47, 0\n\t" \
    "qk_Gag54_%=:\n\t" \
    "global_load_dwordx4 v[36:39], v1, s[46:47] sc1\n\t" \
    "global_load_dwordx4 v[40:43], v23, s[46:47] sc1\n\t" \
    "global_load_dwordx4 v[44:47], v163, s[30:31] sc1\n\t" \
    "global_load_dwordx4 v[48:51], v4, s[30:31] sc1\n\t" \
    "global_load_dwordx4 v[52:55], v5, s[30:31] sc1\n\t" \
    "global_load_dwordx4 v[56:59], v6, s[30:31] sc1\n\t" \
    "global_load_dwordx4 v[60:63], v7, s[30:31] sc1\n\t" \
    "global_load_dwordx4 v[64:67], v8, s[30:31] sc1\n\t" \
    "global_load_dwordx4 v[68:71], v9, s[30:31] sc1\n\t" \
    "s_waitcnt vmcnt(0)\n\t" \
    "v_max_u32 v160, v36, v37\n\t" \
    "v_max3_u32 v160, v160, v38, v39\n\t" \
    "v_max3_u32 v160, v160, v40, v41\n\t" \
    "v_max3_u32 v160, v160, v42, v43\n\t" \
    "v_max3_u32 v160, v160, v44, v45\n\t" \
    "v_max3_u32 v160, v160, v46, v47\n\t" \
    "v_max3_u32 v160, v160, v48, v49\n\t" \
    "v_max3_u32 v160, v160, v50, v51\n\t" \
    "v_max3_u32 v160, v160, v52, v53\n\t" \
    "v_max3_u32 v160, v160, v54, v55\n\t" \
    "v_max3_u32 v160, v160, v56, v57\n\t" \
    "v_max3_u32 v160, v160, v58, v59\n\t" \
    "v_max3_u32 v160, v160, v60, v61\n\t" \
    "v_max3_u32 v160, v160, v62, v63\n\t" \
    "v_max3_u32 v160, v160, v64, v65\n\t" \
    "v_max3_u32 v160, v160, v66, v67\n\t" \
    "v_max3_u32 v160, v160, v68, v69\n\t" \
    "v_max3_u32 v160, v160, v70, v71\n\t" \
    "v_cmp_eq_u32 vcc, -1, v160\n\t" \
    "s_cbranch_vccz qk_Gredo55_%=\n\t" \
    "s_cmp_eq_u32 s68, 0\n\t" \
    "s_cbranch_scc1 qk_F56_%=\n\t" \
    "s_sub_u32 s68, s68, 1\n\t" \
    "s_sleep 1\n\t" \
    "s_branch qk_Gag54_%=\n\t" \
    "qk_F56_%=:\n\t" \
    "v_mov_b32 v160, 5767424\n\t" \
    "v_mov_b32 v161, 2\n\t" \
    "global_store_dword v160, v161, s[36:37] sc1\n\t" \
    "s_waitcnt vmcnt(0)\n\t" \
    "s_branch qk_Gredo55_%=\n\t" \
    "qk_Gredo55_%=:\n\t" \
    "v_mov_b32 v72, 0\n\t" \
    "v_mov_b32 v73, 0\n\t" \
    "v_mov_b32 v74, 0\n\t" \
    "v_mov_b32 v75, 0\n\t" \
    "v_mov_b32 v76, 0\n\t" \
    "v_mov_b32 v77, 0\n\t" \
    "v_mov_b32 v78, 0\n\t" \
    "v_mov_b32 v79, 0\n\t" \
    "v_mov_b32 v80, 0\n\t" \
    "v_mov_b32 v81, 0\n\t" \
    "v_mov_b32 v82, 0\n\t" \
    "v_mov_b32 v83, 0\n\t" \
    "v_mov_b32 v84, 0\n\t" \
    "v_mov_b32 v85, 0\n\t" \
    "v_mov_b32 v86, 0\n\t" \
    "v_mov_b32 v87, 0\n\t" \
    "v_smfmac_f32_16x16x64_bf16 v[72:75], v[36:39], a[16:23], v10\n\t" \
    "v_smfmac_f32_16x16x64_bf16 v[72:75], v[40:43], a[24:31], v10\n\t" \
    "v_smfmac_f32_16x16x64_bf16 v[72:75], v[44:47], a[64:71], v10\n\t" \
    "v_smfmac_f32_16x16x64_bf16 v[72:75], v[48:51], a[32:39], v10\n\t" \
    "v_smfmac_f32_16x16x64_bf16 v[72:75], v[52:55], a[40:47], v10\n\t" \
    "v_smfmac_f32_16x16x64_bf16 v[72:75], v[56:59], a[48:55], v10\n\t" \
    "v_smfmac_f32_16x16x64_bf16 v[72:75], v[60:63], a[56:63], v10\n\t" \
    "v_smfmac_f32_16x16x64_bf16 v[72:75], v[64:67], a[0:7], v10\n\t" \
    "v_smfmac_f32_16x16x64_bf16 v[72:75], v[68:71], a[8:15], v10\n\t" \
    "v_smfmac_f32_16x16x64_bf16 v[76:79], v[36:39], a[88:95], v10\n\t" \
    "v_smfmac_f32_16x16x64_bf16 v[76:79], v[40:43], a[96:103], v10\n\t" \
    "v_smfmac_f32_16x16x64_bf16 v[76:79], v[44:47], a[136:143], v10\n\t" \
    "v_smfmac_f32_16x16x64_bf16 v[76:79], v[48:51], a[104:111], v10\n\t" \
    "v_smfmac_f32_16x16x64_bf16 v[76:79], v[52:55], a[112:119], v10\n\t" \
    "v_smfmac_f32_16x16x64_bf16 v[76:79], v[56:59], a[120:127], v10\n\t" \
    "v_smfmac_f32_16x16x64_bf16 v[76:79], v[60:63], a[128:135], v10\n\t" \
    "v_smfmac_f32_16x16x64_bf16 v[76:79], v[64:67], a[72:79], v10\n\t" \
    "v_smfmac_f32_16x16x64_bf16 v[76:79], v[68:71], a[80:87], v10\n\t" \
    "v_smfmac_f32_16x16x64_bf16 v[80:83], v[36:39], a[160:167], v10\n\t" \
    "v_smfmac_f32_16x16x64_bf16 v[80:83], v[40:43], a[168:175], v10\n\t" \
    "v_smfmac_f32_16x16x64_bf16 v[80:83], v[44:47], a[208:215], v10\n\t" \
    "v_smfmac_f32_16x16x64_bf16 v[80:83], v[48:51], a[176:183], v10\n\t" \
    "v_smfmac_f32_16x16x64_bf16 v[80:83], v[52:55], a[184:191], v10\n\t" \
    "v_smfmac_f32_16x16x64_bf16 v[80:83], v[56:59], a[192:199], v10\n\t" \
    "v_smfmac_f32_16x16x64_bf16 v[80:83], v[60:63], a[200:207], v10\n\t" \
    "v_smfmac_f32_16x16x64_bf16 v[80:83], v[64:67], a[144:151], v10\n\t" \
    "v_smfmac_f32_16x16x64_bf16 v[80:83], v[68:71], a[152:159], v10\n\t" \
    "v_smfmac_f32_16x16x64_bf16 v[84:87], v[36:39], a[232:239], v10\n\t" \
    "v_smfmac_f32_16x16x64_bf16 v[84:87], v[40:43], a[240:247], v10\n\t" \
    "v_smfmac_f32_16x16x64_bf16 v[84:87], v[44:47], v[248:255], v10\n\t" \
    "v_smfmac_f32_16x16x64_bf16 v[84:87], v[48:51], a[248:255], v10\n\t" \
    "v_smfmac_f32_16x16x64_bf16 v[84:87], v[52:55], v[224:231], v10\n\t" \
    "v_smfmac_f32_16x16x64_bf16 v[84:87], v[56:59], v[232:239], v10\n\t" \
    "v_smfmac_f32_16x16x64_bf16 v[84:87], v[60:63], v[240:247], v10\n\t" \
    "v_smfmac_f32_16x16x64_bf16 v[84:87], v[64:67], a[216:223], v10\n\t" \
    "v_smfmac_f32_16x16x64_bf16 v[84:87], v[68:71], a[224:231], v10\n\t" \
    "v_add_f32 v72, v72, v74\n\t" \
    "v_add_f32 v73, v73, v75\n\t" \
    "v_cvt_pk_bf16_f32 v156, v72, v73\n\t" \
    "ds_write_b32 v12, v156 offset:0\n\t" \
    "v_add_f32 v76, v76, v78\n\t" \
    "v_add_f32 v77, v77, v79\n\t" \
    "v_cvt_pk_bf16_f32 v157, v76, v77\n\t" \
    "ds_write_b32 v12, v157 offset:64\n\t" \
    "v_add_f32 v80, v80, v82\n\t" \
    "v_add_f32 v81, v81, v83\n\t" \
    "v_cvt_pk_bf16_f32 v158, v80, v81\n\t" \
    "ds_write_b32 v16, v158 offset:0\n\t" \
    "v_pk_add_f32 v[84:85], v[84:85], v[86:87]\n\t" \
    "v_cvt_pk_bf16_f32 v159, v84, v85\n\t" \
    "ds_write_b32 v16, v159 offset:256\n\t" \
    "s_branch qk_Gback45_%=\n\t" \
    "qk_qs46_%=:\n\t" \
    "s_cmp_eq_u32 s91, s65\n\t" \
    "s_cbranch_scc1 qk_qb248_%=\n\t" \
    "s_mov_b32 s91, s65\n\t" \
    "s_nop 7\n\t" \
    "ds_read_b64_tr_b16 v[132:133], v13 offset:0\n\t" \
    "ds_read_b64_tr_b16 v[134:135], v13 offset:128\n\t" \
    "ds_read_b64_tr_b16 v[136:137], v13 offset:1024\n\t" \
    "ds_read_b64_tr_b16 v[138:139], v13 offset:1152\n\t" \
    "v_mov_b32 v88, 0\n\t" \
    "v_mov_b32 v89, 0\n\t" \
    "v_mov_b32 v90, 0\n\t" \
    "v_mov_b32 v91, 0\n\t" \
    "v_mov_b32 v92, 0\n\t" \
    "v_mov_b32 v93, 0\n\t" \
    "v_mov_b32 v94, 0\n\t" \
    "v_mov_b32 v95, 0\n\t" \
    "v_mov_b32 v96, 0\n\t" \
    "v_mov_b32 v97, 0\n\t" \
    "v_mov_b32 v98, 0\n\t" \
    "v_mov_b32 v99, 0\n\t" \
    "s_waitcnt lgkmcnt(0)\n\t" \
    "v_smfmac_f32_16x16x64_bf16 v[88:91], v[132:135], v[100:107], v10\n\t" \
    "v_smfmac_f32_16x16x64_bf16 v[92:95], v[132:135], v[116:123], v10\n\t" \
    "v_smfmac_f32_16x16x64_bf16 v[88:91], v[136:139], v[108:115], v10\n\t" \
    "v_smfmac_f32_16x16x64_bf16 v[92:95], v[136:139], v[124:131], v10\n\t" \
    "s_nop 7\n\t" \
    "s_branch qk_qb47_%=\n\t" \
    "qk_last36_%=:\n\t" \
    "s_endpgm\n\t" \
    "qk_loop13_%=:\n\t" \
    "v_mov_b32 v72, 0\n\t" \
    "v_mov_b32 v73, 0\n\t" \
    "v_mov_b32 v74, 0\n\t" \
    "v_mov_b32 v75, 0\n\t" \
    "v_mov_b32 v76, 0\n\t" \
    "v_mov_b32 v77, 0\n\t" \
    "v_mov_b32 v78, 0\n\t" \
    "v_mov_b32 v79, 0\n\t" \
    "v_mov_b32 v80, 0\n\t" \
    "v_mov_b32 v81, 0\n\t" \
    "v_mov_b32 v82, 0\n\t" \
    "v_mov_b32 v83, 0\n\t" \
    "v_mov_b32 v84, 0\n\t" \
    "v_mov_b32 v85, 0\n\t" \
    "v_mov_b32 v86, 0\n\t" \
    "v_mov_b32 v87, 0\n\t" \
    "s_add_u32 s69, s65, 0\n\t" \
    "s_and_b32 s69, s69, 7\n\t" \
    "s_lshl_b32 s69, s69, 18\n\t" \
    "s_add_u32 s69, s69, s62\n\t" \
    "s_add_u32 s46, s36, s69\n\t" \
    "s_addc_u32 s47, s37, 0\n\t" \
    "s_mul_i32 s70, s67, 7\n\t" \
    "s_add_u32 s69, s65, 0\n\t" \
    "s_xor_b32 s69, s69, s70\n\t" \
    "s_and_b32 s69, s69, 63\n\t" \
    "s_lshl_b32 s69, s69, 12\n\t" \
    "s_mul_i32 s70, s67, 262144\n\t" \
    "s_add_u32 s69, s69, s70\n\t" \
    "s_add_u32 s69, s69, 39845888\n\t" \
    "s_add_u32 s40, s36, s69\n\t" \
    "s_addc_u32 s41, s37, 0\n\t" \
    "s_sub_u32 s91, s65, 1\n\t" \
    "v_mov_b32 v25, s91\n\t" \
    "s_sub_u32 s70, s79, s74\n\t" \
    "s_max_i32 s70, s70, 0\n\t" \
    "s_min_i32 s70, s70, 16000\n\t" \
    "s_lshl_b32 s70, s70, 1\n\t" \
    "s_mul_hi_u32 s70, s70, s77\n\t" \
    "s_add_u32 s70, s70, s73\n\t" \
    ".p2align 6\n\t" \
    "s_nop 0\n\t" \
    "s_nop 0\n\t" \
    "s_nop 0\n\t" \
    "s_nop 0\n\t" \
    "s_nop 0\n\t" \
    "s_nop 0\n\t" \
    "qk_go58_%=:\n\t" \
    "s_getpc_b64 s[30:31]\n\t" \
    "qk_Wp62_%=:\n\t" \
    "s_add_u32 s30, s30, qk_We63_%=-qk_Wp62_%=\n\t" \
    "s_addc_u32 s31, s31, 0\n\t" \
    "s_sub_u32 s70, s70, 96\n\t" \
    "qk_W61_%=:\n\t" \
    "s_memtime s[56:57]\n\t" \
    "s_waitcnt lgkmcnt(0)\n\t" \
    "s_sub_u32 s69, s70, s56\n\t" \
    "s_cmp_gt_i32 s69, 80\n\t" \
    "s_cbranch_scc1 qk_W61_%=\n\t" \
    "s_max_i32 s69, s69, 0\n\t" \
    "s_and_b32 s69, s69, -4\n\t" \
    "s_sub_u32 s30, s30, s69\n\t" \
    "s_subb_u32 s31, s31, 0\n\t" \
    "s_setpc_b64 s[30:31]\n\t" \
    "s_nop 0\n\t" \
    "s_nop 0\n\t" \
    "s_nop 0\n\t" \
    "s_nop 0\n\t" \
    "s_nop 0\n\t" \
    "s_nop 0\n\t" \
    "s_nop 0\n\t" \
    "s_nop 0\n\t" \
    "s_nop 0\n\t" \
    "s_nop 0\n\t" \
    "s_nop 0\n\t" \
    "s_nop 0\n\t" \
    "s_nop 0\n\t" \
    "s_nop 0\n\t" \
    "s_nop 0\n\t" \
    "s_nop 0\n\t" \
    "s_nop 0\n\t" \
    "s_nop 0\n\t" \
    "s_nop 0\n\t" \
    "s_nop 0\n\t" \
    "qk_We63_%=:\n\t" \
    "qk_G64_%=:\n\t" \
    "global_load_dwordx4 v[36:39], v1, s[46:47] sc1\n\t" \
    "global_load_dword v161, v2, s[46:47] sc1\n\t" \
    "global_load_dwordx4 v[40:43], v23, s[46:47] sc1\n\t" \
    "s_waitcnt vmcnt(2)\n\t" \
    "ds_write_b128 v3, v[36:39]\n\t" \
    "v_smfmac_f32_16x16x64_bf16 v[72:75], v[36:39], a[32:39], v10\n\t" \
    "s_waitcnt vmcnt(1)\n\t" \
    "ds_write_b32 v22, v161 offset:19968\n\t" \
    "v_smfmac_f32_16x16x64_bf16 v[76:79], v[36:39], a[104:111], v10\n\t" \
    "s_waitcnt vmcnt(0)\n\t" \
    "ds_write_b128 v3, v[40:43] offset:1024\n\t" \
    "v_max3_u32 v160, v36, v40, v161\n\t" \
    "v_cmp_eq_u32 vcc, -1, v160\n\t" \
    "v_smfmac_f32_16x16x64_bf16 v[80:83], v[36:39], a[176:183], v10\n\t" \
    "v_smfmac_f32_16x16x64_bf16 v[84:87], v[36:39], a[248:255], v10\n\t" \
    "v_smfmac_f32_16x16x64_bf16 v[72:75], v[40:43], a[40:47], v10\n\t" \
    "s_cbranch_vccnz qk_Gbad65_%=\n\t" \
    "qk_Gc66_%=:\n\t" \
    "s_waitcnt lgkmcnt(0)\n\t" \
    "s_barrier\n\t" \
    "ds_read_b128 v[48:51], v4\n\t" \
    "ds_read_b128 v[52:55], v5\n\t" \
    "v_smfmac_f32_16x16x64_bf16 v[76:79], v[40:43], a[112:119], v10\n\t" \
    "ds_read_b128 v[56:59], v6\n\t" \
    "ds_read_b128 v[60:63], v7\n\t" \
    "v_smfmac_f32_16x16x64_bf16 v[80:83], v[40:43], a[184:191], v10\n\t" \
    "ds_read_b128 v[64:67], v8\n\t" \
    "v_smfmac_f32_16x16x64_bf16 v[84:87], v[40:43], v[224:231], v10\n\t" \
    "ds_read_b128 v[68:71], v9\n\t" \
    "s_waitcnt lgkmcnt(5)\n\t" \
    "v_smfmac_f32_16x16x64_bf16 v[72:75], v[48:51], a[48:55], v10\n\t" \
    "ds_read_b128 v[44:47], v0 offset:19456\n\t" \
    "s_waitcnt lgkmcnt(5)\n\t" \
    "v_smfmac_f32_16x16x64_bf16 v[72:75], v[52:55], a[56:63], v10\n\t" \
    "s_waitcnt lgkmcnt(4)\n\t" \
    "v_smfmac_f32_16x16x64_bf16 v[72:75], v[56:59], a[0:7], v10\n\t" \
    "s_waitcnt lgkmcnt(3)\n\t" \
    "v_smfmac_f32_16x16x64_bf16 v[72:75], v[60:63], a[8:15], v10\n\t" \
    "s_waitcnt lgkmcnt(2)\n\t" \
    "v_smfmac_f32_16x16x64_bf16 v[72:75], v[64:67], a[16:23], v10\n\t" \
    "s_waitcnt lgkmcnt(1)\n\t" \
    "v_smfmac_f32_16x16x64_bf16 v[72:75], v[68:71], a[24:31], v10\n\t" \
    "s_waitcnt lgkmcnt(0)\n\t" \
    "v_smfmac_f32_16x16x64_bf16 v[72:75], v[44:47], a[64:71], v10\n\t" \
    "s_nop 2\n\t" \
    "v_smfmac_f32_16x16x64_bf16 v[76:79], v[48:51], a[120:127], v10\n\t" \
    "s_cmp_ge_u32 s91, 57\n\t" \
    "s_cbranch_scc1 qk_kf59_%=\n\t" \
    "qk_kb60_%=:\n\t" \
    "v_smfmac_f32_16x16x64_bf16 v[76:79], v[52:55], a[128:135], v10\n\t" \
    "s_nop 0\n\t" \
    "v_add_f32 v72, v72, v74\n\t" \
    "v_smfmac_f32_16x16x64_bf16 v[76:79], v[56:59], a[72:79], v10\n\t" \
    "v_add_f32 v73, v73, v75\n\t" \
    "v_smfmac_f32_16x16x64_bf16 v[76:79], v[60:63], a[80:87], v10\n\t" \
    "v_cvt_pk_bf16_f32 v156, v72, v73\n\t" \
    "v_smfmac_f32_16x16x64_bf16 v[76:79], v[64:67], a[88:95], v10\n\t" \
    "ds_write_b32 v12, v156 offset:0\n\t" \
    "v_smfmac_f32_16x16x64_bf16 v[76:79], v[68:71], a[96:103], v10\n\t" \
    "v_smfmac_f32_16x16x64_bf16 v[76:79], v[44:47], a[136:143], v10\n\t" \
    "s_nop 2\n\t" \
    "v_smfmac_f32_16x16x64_bf16 v[80:83], v[48:51], a[192:199], v10\n\t" \
    "s_nop 2\n\t" \
    "v_smfmac_f32_16x16x64_bf16 v[80:83], v[52:55], a[200:207], v10\n\t" \
    "v_add_f32 v76, v76, v78\n\t" \
    "v_smfmac_f32_16x16x64_bf16 v[80:83], v[56:59], a[144:151], v10\n\t" \
    "v_add_f32 v77, v77, v79\n\t" \
    "v_smfmac_f32_16x16x64_bf16 v[80:83], v[60:63], a[152:159], v10\n\t" \
    "v_cvt_pk_bf16_f32 v157, v76, v77\n\t" \
    "v_smfmac_f32_16x16x64_bf16 v[80:83], v[64:67], a[160:167], v10\n\t" \
    "ds_write_b32 v12, v157 offset:64\n\t" \
    "v_smfmac_f32_16x16x64_bf16 v[80:83], v[68:71], a[168:175], v10\n\t" \
    "v_smfmac_f32_16x16x64_bf16 v[80:83], v[44:47], a[208:215], v10\n\t" \
    "s_nop 2\n\t" \
    "v_smfmac_f32_16x16x64_bf16 v[84:87], v[48:51], v[232:239], v10\n\t" \
    "s_nop 2\n\t" \
    "v_smfmac_f32_16x16x64_bf16 v[84:87], v[52:55], v[240:247], v10\n\t" \
    "v_add_f32 v80, v80, v82\n\t" \
    "v_smfmac_f32_16x16x64_bf16 v[84:87], v[56:59], a[216:223], v10\n\t" \
    "v_add_f32 v81, v81, v83\n\t" \
    "v_smfmac_f32_16x16x64_bf16 v[84:87], v[60:63], a[224:231], v10\n\t" \
    "v_cvt_pk_bf16_f32 v158, v80, v81\n\t" \
    "v_smfmac_f32_16x16x64_bf16 v[84:87], v[64:67], a[232:239], v10\n\t" \
    "ds_write_b32 v16, v158 offset:0\n\t" \
    "v_smfmac_f32_16x16x64_bf16 v[84:87], v[68:71], a[240:247], v10\n\t" \
    "v_smfmac_f32_16x16x64_bf16 v[84:87], v[44:47], v[248:255], v10\n\t" \
    "s_nop 6\n\t" \
    "v_cmp_u_f32 vcc, v72, v73\n\t" \
    "v_pk_add_f32 v[84:85], v[84:85], v[86:87]\n\t" \
    "v_cvt_pk_bf16_f32 v159, v84, v85\n\t" \
    "ds_write_b32 v16, v159 offset:256\n\t" \
    "s_cbranch_vccnz qk_Gslow67_%=\n\t" \
    "qk_Gback68_%=:\n\t" \
    "s_waitcnt lgkmcnt(0)\n\t" \
    "s_barrier\n\t" \
    "s_sleep 1\n\t" \
    "ds_read_b64_tr_b16 v[140:141], v17 offset:0\n\t" \
    "ds_read_b64_tr_b16 v[142:143], v17 offset:256\n\t" \
    "ds_read_b64_tr_b16 v[144:145], v17 offset:512\n\t" \
    "ds_read_b64_tr_b16 v[146:147], v17 offset:768\n\t" \
    "ds_read_b64_tr_b16 v[148:149], v17 offset:1024\n\t" \
    "ds_read_b64_tr_b16 v[150:151], v17 offset:1280\n\t" \
    "ds_read_b64_tr_b16 v[152:153], v17 offset:1536\n\t" \
    "ds_read_b64_tr_b16 v[154:155], v17 offset:1792\n\t" \
    "s_waitcnt lgkmcnt(0)\n\t" \
    "s_cmp_eq_u32 s65, 63\n\t" \
    "s_cbranch_scc1 qk_last57_%=\n\t" \
    "s_cmp_ge_u32 s65, 58\n\t" \
    "s_cbranch_scc1 qk_nsl69_%=\n\t" \
    "s_sleep 6\n\t" \
    "global_store_dword v14, v25, s[36:37] sc1\n\t" \
    "qk_nsl69_%=:\n\t" \
    "s_mov_b64 exec, s[58:59]\n\t" \
    "global_store_dwordx4 v0, v[140:143], s[40:41] offset:0 sc1\n\t" \
    "global_store_dwordx4 v0, v[144:147], s[40:41] offset:1024 sc1\n\t" \
    "global_store_dwordx4 v0, v[148:151], s[40:41] offset:2048 sc1\n\t" \
    "global_store_dwordx4 v0, v[152:155], s[40:41] offset:3072 sc1\n\t" \
    "s_mov_b64 exec, -1\n\t" \
    "s_add_u32 s65, s65, 1\n\t" \
    "v_mov_b32 v72, 0\n\t" \
    "v_mov_b32 v73, 0\n\t" \
    "v_mov_b32 v74, 0\n\t" \
    "v_mov_b32 v75, 0\n\t" \
    "v_mov_b32 v76, 0\n\t" \
    "v_mov_b32 v77, 0\n\t" \
    "v_mov_b32 v78, 0\n\t" \
    "v_mov_b32 v79, 0\n\t" \
    "v_mov_b32 v80, 0\n\t" \
    "v_mov_b32 v81, 0\n\t" \
    "v_mov_b32 v82, 0\n\t" \
    "v_mov_b32 v83, 0\n\t" \
    "v_mov_b32 v84, 0\n\t" \
    "v_mov_b32 v85, 0\n\t" \
    "v_mov_b32 v86, 0\n\t" \
    "v_mov_b32 v87, 0\n\t" \
    "s_add_u32 s69, s65, 0\n\t" \
    "s_and_b32 s69, s69, 7\n\t" \
    "s_lshl_b32 s69, s69, 18\n\t" \
    "s_add_u32 s69, s69, s62\n\t" \
    "s_add_u32 s46, s36, s69\n\t" \
    "s_addc_u32 s47, s37, 0\n\t" \
    "s_mul_i32 s70, s67, 7\n\t" \
    "s_add_u32 s69, s65, 0\n\t" \
    "s_xor_b32 s69, s69, s70\n\t" \
    "s_and_b32 s69, s69, 63\n\t" \
    "s_lshl_b32 s69, s69, 12\n\t" \
    "s_mul_i32 s70, s67, 262144\n\t" \
    "s_add_u32 s69, s69, s70\n\t" \
    "s_add_u32 s69, s69, 39845888\n\t" \
    "s_add_u32 s40, s36, s69\n\t" \
    "s_addc_u32 s41, s37, 0\n\t" \
    "s_sub_u32 s91, s65, 1\n\t" \
    "v_mov_b32 v25, s91\n\t" \
    "s_barrier\n\t" \
    "ds_read_b32 v168, v11 offset:20480\n\t" \
    "s_waitcnt lgkmcnt(0)\n\t" \
    "v_readfirstlane_b32 s70, v168\n\t" \
    "ds_write_b32 v12, v31 offset:0\n\t" \
    "ds_write_b32 v12, v31 offset:64\n\t" \
    "s_branch qk_go58_%=\n\t" \
    "qk_Gbad65_%=:\n\t" \
    "s_waitcnt lgkmcnt(0)\n\t" \
    "s_nop 7\n\t" \
    "v_mov_b32 v72, 0\n\t" \
    "v_mov_b32 v73, 0\n\t" \
    "v_mov_b32 v74, 0\n\t" \
    "v_mov_b32 v75, 0\n\t" \
    "v_mov_b32 v76, 0\n\t" \
    "v_mov_b32 v77, 0\n\t" \
    "v_mov_b32 v78, 0\n\t" \
    "v_mov_b32 v79, 0\n\t" \
    "v_mov_b32 v80, 0\n\t" \
    "v_mov_b32 v81, 0\n\t" \
    "v_mov_b32 v82, 0\n\t" \
    "v_mov_b32 v83, 0\n\t" \
    "v_mov_b32 v84, 0\n\t" \
    "v_mov_b32 v85, 0\n\t" \
    "v_mov_b32 v86, 0\n\t" \
    "v_mov_b32 v87, 0\n\t" \
    "s_cmp_eq_u32 s68, 0\n\t" \
    "s_cbranch_scc1 qk_F70_%=\n\t" \
    "s_sub_u32 s68, s68, 1\n\t" \
    "s_branch qk_G64_%=\n\t" \
    "qk_F70_%=:\n\t" \
    "v_mov_b32 v160, 5767424\n\t" \
    "v_mov_b32 v161, 1\n\t" \
    "global_store_dword v160, v161, s[36:37] sc1\n\t" \
    "s_waitcnt vmcnt(0)\n\t" \
    "s_branch qk_Gc66_%=\n\t" \
    "qk_Gslow67_%=:\n\t" \
    "v_max_u32 v160, v36, v37\n\t" \
    "v_max3_u32 v160, v160, v38, v39\n\t" \
    "v_max3_u32 v160, v160, v40, v41\n\t" \
    "v_max3_u32 v160, v160, v42, v43\n\t" \
    "v_max3_u32 v160, v160, v44, v45\n\t" \
    "v_max3_u32 v160, v160, v46, v47\n\t" \
    "v_max3_u32 v160, v160, v48, v49\n\t" \
    "v_max3_u32 v160, v160, v50, v51\n\t" \
    "v_max3_u32 v160, v160, v52, v53\n\t" \
    "v_max3_u32 v160, v160, v54, v55\n\t" \
    "v_max3_u32 v160, v160, v56, v57\n\t" \
    "v_max3_u32 v160, v160, v58, v59\n\t" \
    "v_max3_u32 v160, v160, v60, v61\n\t" \
    "v_max3_u32 v160, v160, v62, v63\n\t" \
    "v_max3_u32 v160, v160, v64, v65\n\t" \
    "v_max3_u32 v160, v160, v66, v67\n\t" \
    "v_max3_u32 v160, v160, v68, v69\n\t" \
    "v_max3_u32 v160, v160, v70, v71\n\t" \
    "v_cmp_eq_u32 vcc, -1, v160\n\t" \
    "s_cbranch_vccz qk_Gback68_%=\n\t" \
    "s_waitcnt lgkmcnt(0)\n\t" \
    "v_sub_u32 v160, v1, v3\n\t" \
    "s_nop 0\n\t" \
    "v_readfirstlane_b32 s69, v160\n\t" \
    "v_add_u32 v163, 20480, v0\n\t" \
    "s_nop 7\n\t" \
    "s_add_u32 s30, s46, s69\n\t" \
    "s_addc_u32 s31, s47, 0\n\t" \
    "qk_Gag71_%=:\n\t" \
    "global_load_dwordx4 v[36:39], v1, s[46:47] sc1\n\t" \
    "global_load_dwordx4 v[40:43], v23, s[46:47] sc1\n\t" \
    "global_load_dwordx4 v[44:47], v163, s[30:31] sc1\n\t" \
    "global_load_dwordx4 v[48:51], v4, s[30:31] sc1\n\t" \
    "global_load_dwordx4 v[52:55], v5, s[30:31] sc1\n\t" \
    "global_load_dwordx4 v[56:59], v6, s[30:31] sc1\n\t" \
    "global_load_dwordx4 v[60:63], v7, s[30:31] sc1\n\t" \
    "global_load_dwordx4 v[64:67], v8, s[30:31] sc1\n\t" \
    "global_load_dwordx4 v[68:71], v9, s[30:31] sc1\n\t" \
    "s_waitcnt vmcnt(0)\n\t" \
    "v_max_u32 v160, v36, v37\n\t" \
    "v_max3_u32 v160, v160, v38, v39\n\t" \
    "v_max3_u32 v160, v160, v40, v41\n\t" \
    "v_max3_u32 v160, v160, v42, v43\n\t" \
    "v_max3_u32 v160, v160, v44, v45\n\t" \
    "v_max3_u32 v160, v160, v46, v47\n\t" \
    "v_max3_u32 v160, v160, v48, v49\n\t" \
    "v_max3_u32 v160, v160, v50, v51\n\t" \
    "v_max3_u32 v160, v160, v52, v53\n\t" \
    "v_max3_u32 v160, v160, v54, v55\n\t" \
    "v_max3_u32 v160, v160, v56, v57\n\t" \
    "v_max3_u32 v160, v160, v58, v59\n\t" \
    "v_max3_u32 v160, v160, v60, v61\n\t" \
    "v_max3_u32 v160, v160, v62, v63\n\t" \
    "v_max3_u32 v160, v160, v64, v65\n\t" \
    "v_max3_u32 v160, v160, v66, v67\n\t" \
    "v_max3_u32 v160, v160, v68, v69\n\t" \
    "v_max3_u32 v160, v160, v70, v71\n\t" \
    "v_cmp_eq_u32 vcc, -1, v160\n\t" \
    "s_cbranch_vccz qk_Gredo72_%=\n\t" \
    "s_cmp_eq_u32 s68, 0\n\t" \
    "s_cbranch_scc1 qk_F73_%=\n\t" \
    "s_sub_u32 s68, s68, 1\n\t" \
    "s_sleep 1\n\t" \
    "s_branch qk_Gag71_%=\n\t" \
    "qk_F73_%=:\n\t" \
    "v_mov_b32 v160, 5767424\n\t" \
    "v_mov_b32 v161, 2\n\t" \
    "global_store_dword v160, v161, s[36:37] sc1\n\t" \
    "s_waitcnt vmcnt(0)\n\t" \
    "s_branch qk_Gredo72_%=\n\t" \
    "qk_Gredo72_%=:\n\t" \
    "v_mov_b32 v72, 0\n\t" \
    "v_mov_b32 v73, 0\n\t" \
    "v_mov_b32 v74, 0\n\t" \
    "v_mov_b32 v75, 0\n\t" \
    "v_mov_b32 v76, 0\n\t" \
    "v_mov_b32 v77, 0\n\t" \
    "v_mov_b32 v78, 0\n\t" \
    "v_mov_b32 v79, 0\n\t" \
    "v_mov_b32 v80, 0\n\t" \
    "v_mov_b32 v81, 0\n\t" \
    "v_mov_b32 v82, 0\n\t" \
    "v_mov_b32 v83, 0\n\t" \
    "v_mov_b32 v84, 0\n\t" \
    "v_mov_b32 v85, 0\n\t" \
    "v_mov_b32 v86, 0\n\t" \
    "v_mov_b32 v87, 0\n\t" \
    "v_smfmac_f32_16x16x64_bf16 v[72:75], v[36:39], a[32:39], v10\n\t" \
    "v_smfmac_f32_16x16x64_bf16 v[72:75], v[40:43], a[40:47], v10\n\t" \
    "v_smfmac_f32_16x16x64_bf16 v[72:75], v[44:47], a[64:71], v10\n\t" \
    "v_smfmac_f32_16x16x64_bf16 v[72:75], v[48:51], a[48:55], v10\n\t" \
    "v_smfmac_f32_16x16x64_bf16 v[72:75], v[52:55], a[56:63], v10\n\t" \
    "v_smfmac_f32_16x16x64_bf16 v[72:75], v[56:59], a[0:7], v10\n\t" \
    "v_smfmac_f32_16x16x64_bf16 v[72:75], v[60:63], a[8:15], v10\n\t" \
    "v_smfmac_f32_16x16x64_bf16 v[72:75], v[64:67], a[16:23], v10\n\t" \
    "v_smfmac_f32_16x16x64_bf16 v[72:75], v[68:71], a[24:31], v10\n\t" \
    "v_smfmac_f32_16x16x64_bf16 v[76:79], v[36:39], a[104:111], v10\n\t" \
    "v_smfmac_f32_16x16x64_bf16 v[76:79], v[40:43], a[112:119], v10\n\t" \
    "v_smfmac_f32_16x16x64_bf16 v[76:79], v[44:47], a[136:143], v10\n\t" \
    "v_smfmac_f32_16x16x64_bf16 v[76:79], v[48:51], a[120:127], v10\n\t" \
    "v_smfmac_f32_16x16x64_bf16 v[76:79], v[52:55], a[128:135], v10\n\t" \
    "v_smfmac_f32_16x16x64_bf16 v[76:79], v[56:59], a[72:79], v10\n\t" \
    "v_smfmac_f32_16x16x64_bf16 v[76:79], v[60:63], a[80:87], v10\n\t" \
    "v_smfmac_f32_16x16x64_bf16 v[76:79], v[64:67], a[88:95], v10\n\t" \
    "v_smfmac_f32_16x16x64_bf16 v[76:79], v[68:71], a[96:103], v10\n\t" \
    "v_smfmac_f32_16x16x64_bf16 v[80:83], v[36:39], a[176:183], v10\n\t" \
    "v_smfmac_f32_16x16x64_bf16 v[80:83], v[40:43], a[184:191], v10\n\t" \
    "v_smfmac_f32_16x16x64_bf16 v[80:83], v[44:47], a[208:215], v10\n\t" \
    "v_smfmac_f32_16x16x64_bf16 v[80:83], v[48:51], a[192:199], v10\n\t" \
    "v_smfmac_f32_16x16x64_bf16 v[80:83], v[52:55], a[200:207], v10\n\t" \
    "v_smfmac_f32_16x16x64_bf16 v[80:83], v[56:59], a[144:151], v10\n\t" \
    "v_smfmac_f32_16x16x64_bf16 v[80:83], v[60:63], a[152:159], v10\n\t" \
    "v_smfmac_f32_16x16x64_bf16 v[80:83], v[64:67], a[160:167], v10\n\t" \
    "v_smfmac_f32_16x16x64_bf16 v[80:83], v[68:71], a[168:175], v10\n\t" \
    "v_smfmac_f32_16x16x64_bf16 v[84:87], v[36:39], a[248:255], v10\n\t" \
    "v_smfmac_f32_16x16x64_bf16 v[84:87], v[40:43], v[224:231], v10\n\t" \
    "v_smfmac_f32_16x16x64_bf16 v[84:87], v[44:47], v[248:255], v10\n\t" \
    "v_smfmac_f32_16x16x64_bf16 v[84:87], v[48:51], v[232:239], v10\n\t" \
    "v_smfmac_f32_16x16x64_bf16 v[84:87], v[52:55], v[240:247], v10\n\t" \
    "v_smfmac_f32_16x16x64_bf16 v[84:87], v[56:59], a[216:223], v10\n\t" \
    "v_smfmac_f32_16x16x64_bf16 v[84:87], v[60:63], a[224:231], v10\n\t" \
    "v_smfmac_f32_16x16x64_bf16 v[84:87], v[64:67], a[232:239], v10\n\t" \
    "v_smfmac_f32_16x16x64_bf16 v[84:87], v[68:71], a[240:247], v10\n\t" \
    "v_add_f32 v72, v72, v74\n\t" \
    "v_add_f32 v73, v73, v75\n\t" \
    "v_cvt_pk_bf16_f32 v156, v72, v73\n\t" \
    "ds_write_b32 v12, v156 offset:0\n\t" \
    "v_add_f32 v76, v76, v78\n\t" \
    "v_add_f32 v77, v77, v79\n\t" \
    "v_cvt_pk_bf16_f32 v157, v76, v77\n\t" \
    "ds_write_b32 v12, v157 offset:64\n\t" \
    "v_add_f32 v80, v80, v82\n\t" \
    "v_add_f32 v81, v81, v83\n\t" \
    "v_cvt_pk_bf16_f32 v158, v80, v81\n\t" \
    "ds_write_b32 v16, v158 offset:0\n\t" \
    "v_pk_add_f32 v[84:85], v[84:85], v[86:87]\n\t" \
    "v_cvt_pk_bf16_f32 v159, v84, v85\n\t" \
    "ds_write_b32 v16, v159 offset:256\n\t" \
    "s_branch qk_Gback68_%=\n\t" \
    "qk_kf59_%=:\n\t" \
    "global_store_dword v14, v25, s[36:37] sc1\n\t" \
    "s_branch qk_kb60_%=\n\t" \
    "qk_last57_%=:\n\t" \
    "s_mov_b64 exec, s[58:59]\n\t" \
    "global_store_dwordx4 v0, v[140:143], s[40:41] offset:0 sc1\n\t" \
    "global_store_dwordx4 v0, v[144:147], s[40:41] offset:1024 sc1\n\t" \
    "global_store_dwordx4 v0, v[148:151], s[40:41] offset:2048 sc1\n\t" \
    "global_store_dwordx4 v0, v[152:155], s[40:41] offset:3072 sc1\n\t" \
    "s_mov_b64 exec, -1\n\t" \
    "s_endpgm\n\t" \
    "qk_loop14_%=:\n\t" \
    "s_mov_b32 s38, 0x22226a00\n\t" \
    "s_mov_b32 s39, 0x2b\n\t" \
    "v_mov_b32 v72, 0\n\t" \
    "v_mov_b32 v73, 0\n\t" \
    "v_mov_b32 v74, 0\n\t" \
    "v_mov_b32 v75, 0\n\t" \
    "v_mov_b32 v76, 0\n\t" \
    "v_mov_b32 v77, 0\n\t" \
    "v_mov_b32 v78, 0\n\t" \
    "v_mov_b32 v79, 0\n\t" \
    "v_mov_b32 v80, 0\n\t" \
    "v_mov_b32 v81, 0\n\t" \
    "v_mov_b32 v82, 0\n\t" \
    "v_mov_b32 v83, 0\n\t" \
    "v_mov_b32 v84, 0\n\t" \
    "v_mov_b32 v85, 0\n\t" \
    "v_mov_b32 v86, 0\n\t" \
    "v_mov_b32 v87, 0\n\t" \
    "s_add_u32 s69, s65, 0\n\t" \
    "s_and_b32 s69, s69, 7\n\t" \
    "s_lshl_b32 s69, s69, 18\n\t" \
    "s_add_u32 s69, s69, s62\n\t" \
    "s_add_u32 s46, s36, s69\n\t" \
    "s_addc_u32 s47, s37, 0\n\t" \
    "s_lshr_b64 s[38:39], s[38:39], 4\n\t" \
    "s_and_b32 s97, s38, 15\n\t" \
    "s_lshl_b32 s97, s97, 6\n\t" \
    "s_and_b32 s69, s65, 7\n\t" \
    "s_lshl_b32 s69, s69, 18\n\t" \
    "s_add_u32 s69, s69, s63\n\t" \
    "s_add_u32 s52, s36, s69\n\t" \
    "s_addc_u32 s53, s37, 0\n\t" \
    "s_sub_u32 s70, s79, s74\n\t" \
    "s_max_i32 s70, s70, 0\n\t" \
    "s_min_i32 s70, s70, 16000\n\t" \
    "s_lshl_b32 s70, s70, 1\n\t" \
    "s_mul_hi_u32 s70, s70, s77\n\t" \
    "s_add_u32 s70, s70, s73\n\t" \
    ".p2align 6\n\t" \
    "s_nop 0\n\t" \
    "s_nop 0\n\t" \
    "s_nop 0\n\t" \
    "s_nop 0\n\t" \
    "s_nop 0\n\t" \
    "s_nop 0\n\t" \
    "qk_go75_%=:\n\t" \
    "s_getpc_b64 s[30:31]\n\t" \
    "qk_Wp77_%=:\n\t" \
    "s_add_u32 s30, s30, qk_We78_%=-qk_Wp77_%=\n\t" \
    "s_addc_u32 s31, s31, 0\n\t" \
    "s_sub_u32 s70, s70, 96\n\t" \
    "qk_W76_%=:\n\t" \
    "s_memtime s[56:57]\n\t" \
    "s_waitcnt lgkmcnt(0)\n\t" \
    "s_sub_u32 s69, s70, s56\n\t" \
    "s_cmp_gt_i32 s69, 80\n\t" \
    "s_cbranch_scc1 qk_W76_%=\n\t" \
    "s_max_i32 s69, s69, 0\n\t" \
    "s_and_b32 s69, s69, -4\n\t" \
    "s_sub_u32 s30, s30, s69\n\t" \
    "s_subb_u32 s31, s31, 0\n\t" \
    "s_setpc_b64 s[30:31]\n\t" \
    "s_nop 0\n\t" \
    "s_nop 0\n\t" \
    "s_nop 0\n\t" \
    "s_nop 0\n\t" \
    "s_nop 0\n\t" \
    "s_nop 0\n\t" \
    "s_nop 0\n\t" \
    "s_nop 0\n\t" \
    "s_nop 0\n\t" \
    "s_nop 0\n\t" \
    "s_nop 0\n\t" \
    "s_nop 0\n\t" \
    "s_nop 0\n\t" \
    "s_nop 0\n\t" \
    "s_nop 0\n\t" \
    "s_nop 0\n\t" \
    "s_nop 0\n\t" \
    "s_nop 0\n\t" \
    "s_nop 0\n\t" \
    "s_nop 0\n\t" \
    "qk_We78_%=:\n\t" \
    "qk_G79_%=:\n\t" \
    "global_load_dwordx4 v[36:39], v1, s[46:47] sc1\n\t" \
    "global_load_dword v161, v2, s[46:47] sc1\n\t" \
    "global_load_dwordx4 v[40:43], v23, s[46:47] sc1\n\t" \
    "s_mul_hi_u32 s69, s84, s78\n\t" \
    "s_add_u32 s33, s74, s69\n\t" \
    "s_add_u32 s33, s33, s97\n\t" \
    "s_lshr_b32 s69, s90, 16\n\t" \
    "s_add_u32 s34, s33, s69\n\t" \
    "s_and_b32 s69, s90, 0xffff\n\t" \
    "s_sub_u32 s35, s80, s74\n\t" \
    "s_sub_u32 s35, s35, s69\n\t" \
    "s_add_u32 s99, s35, 240\n\t" \
    "s_add_u32 s98, s33, 448\n\t" \
    "s_add_u32 s99, s35, 716\n\t" \
    "s_mul_i32 s69, s35, 10\n\t" \
    "s_ashr_i32 s69, s69, 4\n\t" \
    "s_sub_u32 s69, s33, s69\n\t" \
    "v_lshrrev_b32_e64 v163, 1, s69\n\t" \
    "s_sub_u32 s96, s79, s74\n\t" \
    "s_add_u32 s96, s96, 448\n\t" \
    "s_waitcnt vmcnt(2)\n\t" \
    "ds_write_b128 v3, v[36:39]\n\t" \
    "v_smfmac_f32_16x16x64_bf16 v[72:75], v[36:39], a[48:55], v10\n\t" \
    "s_waitcnt vmcnt(1)\n\t" \
    "ds_write_b32 v22, v161 offset:20224\n\t" \
    "v_smfmac_f32_16x16x64_bf16 v[76:79], v[36:39], a[120:127], v10\n\t" \
    "s_waitcnt vmcnt(0)\n\t" \
    "ds_write_b128 v3, v[40:43] offset:1024\n\t" \
    "v_max3_u32 v160, v36, v40, v161\n\t" \
    "v_cmp_eq_u32 vcc, -1, v160\n\t" \
    "v_smfmac_f32_16x16x64_bf16 v[80:83], v[36:39], a[192:199], v10\n\t" \
    "v_smfmac_f32_16x16x64_bf16 v[84:87], v[36:39], v[232:239], v10\n\t" \
    "v_smfmac_f32_16x16x64_bf16 v[72:75], v[40:43], a[56:63], v10\n\t" \
    "s_cbranch_vccnz qk_Gbad80_%=\n\t" \
    "qk_Gc81_%=:\n\t" \
    "s_waitcnt lgkmcnt(0)\n\t" \
    "s_barrier\n\t" \
    "s_memtime s[56:57]\n\t" \
    "ds_read_b128 v[48:51], v4\n\t" \
    "ds_read_b128 v[52:55], v5\n\t" \
    "v_smfmac_f32_16x16x64_bf16 v[76:79], v[40:43], a[128:135], v10\n\t" \
    "ds_read_b128 v[56:59], v6\n\t" \
    "ds_read_b128 v[60:63], v7\n\t" \
    "v_smfmac_f32_16x16x64_bf16 v[80:83], v[40:43], a[200:207], v10\n\t" \
    "ds_read_b128 v[64:67], v8\n\t" \
    "ds_read_b128 v[68:71], v9\n\t" \
    "v_smfmac_f32_16x16x64_bf16 v[84:87], v[40:43], v[240:247], v10\n\t" \
    "ds_read_b128 v[44:47], v0 offset:19456\n\t" \
    "s_waitcnt lgkmcnt(6)\n\t" \
    "v_smfmac_f32_16x16x64_bf16 v[72:75], v[48:51], a[0:7], v10\n\t" \
    "s_sub_u32 s69, s56, s73\n\t" \
    "s_mul_hi_u32 s69, s69, s78\n\t" \
    "s_max_i32 s69, s69, s96\n\t" \
    "s_waitcnt lgkmcnt(5)\n\t" \
    "v_smfmac_f32_16x16x64_bf16 v[72:75], v[52:55], a[8:15], v10\n\t" \
    "s_max_i32 s70, s69, s35\n\t" \
    "s_min_i32 s70, s70, s99\n\t" \
    "s_mul_i32 s70, s70, 10\n\t" \
    "s_waitcnt lgkmcnt(4)\n\t" \
    "v_smfmac_f32_16x16x64_bf16 v[72:75], v[56:59], a[16:23], v10\n\t" \
    "s_lshl4_add_u32 s70, s69, s70\n\t" \
    "s_ashr_i32 s70, s70, 5\n\t" \
    "v_add_lshl_u32 v162, s70, v163, 1\n\t" \
    "s_waitcnt lgkmcnt(3)\n\t" \
    "v_smfmac_f32_16x16x64_bf16 v[72:75], v[60:63], a[24:31], v10\n\t" \
    "global_store_dword v21, v162, s[52:53] sc1\n\t" \
    "s_waitcnt lgkmcnt(2)\n\t" \
    "v_smfmac_f32_16x16x64_bf16 v[72:75], v[64:67], a[32:39], v10\n\t" \
    "s_waitcnt lgkmcnt(1)\n\t" \
    "v_smfmac_f32_16x16x64_bf16 v[72:75], v[68:71], a[40:47], v10\n\t" \
    "s_waitcnt lgkmcnt(0)\n\t" \
    "v_smfmac_f32_16x16x64_bf16 v[72:75], v[44:47], a[64:71], v10\n\t" \
    "s_nop 2\n\t" \
    "v_smfmac_f32_16x16x64_bf16 v[76:79], v[48:51], a[72:79], v10\n\t" \
    "s_nop 2\n\t" \
    "v_smfmac_f32_16x16x64_bf16 v[76:79], v[52:55], a[80:87], v10\n\t" \
    "v_add_f32 v72, v72, v74\n\t" \
    "v_smfmac_f32_16x16x64_bf16 v[76:79], v[56:59], a[88:95], v10\n\t" \
    "v_add_f32 v73, v73, v75\n\t" \
    "v_smfmac_f32_16x16x64_bf16 v[76:79], v[60:63], a[96:103], v10\n\t" \
    "v_cvt_pk_bf16_f32 v156, v72, v73\n\t" \
    "v_smfmac_f32_16x16x64_bf16 v[76:79], v[64:67], a[104:111], v10\n\t" \
    "ds_write_b32 v12, v156 offset:0\n\t" \
    "v_smfmac_f32_16x16x64_bf16 v[76:79], v[68:71], a[112:119], v10\n\t" \
    "v_smfmac_f32_16x16x64_bf16 v[76:79], v[44:47], a[136:143], v10\n\t" \
    "s_nop 2\n\t" \
    "v_smfmac_f32_16x16x64_bf16 v[80:83], v[48:51], a[144:151], v10\n\t" \
    "s_nop 2\n\t" \
    "v_smfmac_f32_16x16x64_bf16 v[80:83], v[52:55], a[152:159], v10\n\t" \
    "v_add_f32 v76, v76, v78\n\t" \
    "v_smfmac_f32_16x16x64_bf16 v[80:83], v[56:59], a[160:167], v10\n\t" \
    "v_add_f32 v77, v77, v79\n\t" \
    "v_smfmac_f32_16x16x64_bf16 v[80:83], v[60:63], a[168:175], v10\n\t" \
    "v_cvt_pk_bf16_f32 v157, v76, v77\n\t" \
    "v_smfmac_f32_16x16x64_bf16 v[80:83], v[64:67], a[176:183], v10\n\t" \
    "ds_write_b32 v12, v157 offset:64\n\t" \
    "v_smfmac_f32_16x16x64_bf16 v[80:83], v[68:71], a[184:191], v10\n\t" \
    "v_smfmac_f32_16x16x64_bf16 v[80:83], v[44:47], a[208:215], v10\n\t" \
    "s_nop 2\n\t" \
    "v_smfmac_f32_16x16x64_bf16 v[84:87], v[48:51], a[216:223], v10\n\t" \
    "s_nop 2\n\t" \
    "v_smfmac_f32_16x16x64_bf16 v[84:87], v[52:55], a[224:231], v10\n\t" \
    "v_add_f32 v80, v80, v82\n\t" \
    "v_smfmac_f32_16x16x64_bf16 v[84:87], v[56:59], a[232:239], v10\n\t" \
    "v_add_f32 v81, v81, v83\n\t" \
    "v_smfmac_f32_16x16x64_bf16 v[84:87], v[60:63], a[240:247], v10\n\t" \
    "v_cvt_pk_bf16_f32 v158, v80, v81\n\t" \
    "v_smfmac_f32_16x16x64_bf16 v[84:87], v[64:67], a[248:255], v10\n\t" \
    "ds_write_b32 v16, v158 offset:0\n\t" \
    "v_smfmac_f32_16x16x64_bf16 v[84:87], v[68:71], v[224:231], v10\n\t" \
    "v_smfmac_f32_16x16x64_bf16 v[84:87], v[44:47], v[248:255], v10\n\t" \
    "s_nop 6\n\t" \
    "v_cmp_u_f32 vcc, v72, v73\n\t" \
    "v_pk_add_f32 v[84:85], v[84:85], v[86:87]\n\t" \
    "v_cvt_pk_bf16_f32 v159, v84, v85\n\t" \
    "ds_write_b32 v16, v159 offset:256\n\t" \
    "s_cbranch_vccnz qk_Gslow82_%=\n\t" \
    "qk_Gback83_%=:\n\t" \
    "s_waitcnt lgkmcnt(0)\n\t" \
    "s_barrier\n\t" \
    "s_cmp_eq_u32 s65, 63\n\t" \
    "s_cbranch_scc1 qk_last74_%=\n\t" \
    "s_cmp_lg_u32 s65, 0\n\t" \
    "s_cbranch_scc1 qk_ng84_%=\n\t" \
    "v_mov_b32 v168, 5838848\n\t" \
    "v_mov_b32 v169, 1\n\t" \
    "s_mov_b64 exec, 1\n\t" \
    "global_atomic_add v170, v168, v169, s[36:37] sc0 sc1\n\t" \
    "s_mov_b64 exec, -1\n\t" \
    "v_mbcnt_lo_u32_b32 v168, -1, 0\n\t" \
    "v_and_b32 v169, 3, v168\n\t" \
    "v_lshlrev_b32 v169, 16, v169\n\t" \
    "v_bfe_u32 v171, v168, 2, 2\n\t" \
    "v_lshl_add_u32 v169, v171, 11, v169\n\t" \
    "v_bfe_u32 v171, v168, 4, 1\n\t" \
    "v_lshl_add_u32 v169, v171, 14, v169\n\t" \
    "v_add_u32 v169, s63, v169\n\t" \
    "s_waitcnt vmcnt(0)\n\t" \
    "v_readfirstlane_b32 s69, v170\n\t" \
    "s_nop 1\n\t" \
    "s_cmp_lg_u32 s69, 126\n\t" \
    "s_cbranch_scc1 qk_go85_%=\n\t" \
    "s_memrealtime s[56:57]\n\t" \
    "s_waitcnt lgkmcnt(0)\n\t" \
    "s_lshl_b32 s69, s56, 4\n\t" \
    "s_add_u32 s69, s69, 2800\n\t" \
    "s_add_u32 s70, s56, 24\n\t" \
    "s_lshr_b32 s70, s70, 4\n\t" \
    "v_lshrrev_b32 v171, 2, v168\n\t" \
    "v_add_u32 v171, s70, v171\n\t" \
    "v_and_b32 v171, 63, v171\n\t" \
    "v_lshlrev_b32 v171, 6, v171\n\t" \
    "v_and_b32 v170, 3, v168\n\t" \
    "v_lshl_add_u32 v171, v170, 12, v171\n\t" \
    "v_add_u32 v171, 5865472, v171\n\t" \
    "v_mov_b32 v168, s69\n\t" \
    "s_mov_b32 exec_hi, 0\n\t" \
    "global_store_dword v171, v168, s[36:37] sc1\n\t" \
    "global_store_dword v169, v168, s[36:37] sc1\n\t" \
    "s_mov_b32 exec_hi, -1\n\t" \
    "qk_go85_%=:\n\t" \
    "v_mov_b32 v21, v33\n\t" \
    "qk_ng84_%=:\n\t" \
    "s_sub_u32 s70, s80, s74\n\t" \
    "s_max_i32 s70, s70, 0\n\t" \
    "s_min_i32 s70, s70, 16000\n\t" \
    "s_lshl_b32 s70, s70, 1\n\t" \
    "s_mul_hi_u32 s70, s70, s77\n\t" \
    "s_add_u32 s70, s70, s73\n\t" \
    "qk_W86_%=:\n\t" \
    "s_memtime s[56:57]\n\t" \
    "s_waitcnt lgkmcnt(0)\n\t" \
    "s_sub_u32 s69, s70, s56\n\t" \
    "s_cmp_gt_i32 s69, 24\n\t" \
    "s_cbranch_scc1 qk_W86_%=\n\t" \
    "global_load_dword v160, v20, s[52:53] sc1\n\t" \
    "s_waitcnt lgkmcnt(0)\n\t" \
    "s_mov_b32 s70, 116\n\t" \
    "qk_E90_%=:\n\t" \
    "s_memtime s[56:57]\n\t" \
    "s_memrealtime s[2:3]\n\t" \
    "s_nop 0\n\t" \
    "s_memrealtime s[4:5]\n\t" \
    "s_nop 0\n\t" \
    "s_memrealtime s[6:7]\n\t" \
    "s_nop 0\n\t" \
    "s_memrealtime s[8:9]\n\t" \
    "s_nop 0\n\t" \
    "s_memrealtime s[10:11]\n\t" \
    "s_nop 0\n\t" \
    "s_memrealtime s[12:13]\n\t" \
    "s_nop 0\n\t" \
    "s_memrealtime s[14:15]\n\t" \
    "s_nop 0\n\t" \
    "s_memrealtime s[16:17]\n\t" \
    "s_nop 0\n\t" \
    "s_memrealtime s[18:19]\n\t" \
    "s_nop 0\n\t" \
    "s_memrealtime s[20:21]\n\t" \
    "s_nop 0\n\t" \
    "s_memrealtime s[22:23]\n\t" \
    "s_nop 0\n\t" \
    "s_memrealtime s[24:25]\n\t" \
    "s_nop 0\n\t" \
    "s_memrealtime s[26:27]\n\t" \
    "s_nop 0\n\t" \
    "s_memrealtime s[28:29]\n\t" \
    "s_memtime s[54:55]\n\t" \
    "s_add_u32 s70, s70, 8\n\t" \
    "s_waitcnt lgkmcnt(0)\n\t" \
    "s_sub_u32 s69, s54, s56\n\t" \
    "s_cmp_gt_u32 s69, s70\n\t" \
    "s_cbranch_scc1 qk_E90_%=\n\t" \
    "s_add_u32 s2, s2, s4\n\t" \
    "s_add_u32 s6, s6, s8\n\t" \
    "s_add_u32 s10, s10, s12\n\t" \
    "s_add_u32 s14, s14, s16\n\t" \
    "s_add_u32 s18, s18, s20\n\t" \
    "s_add_u32 s22, s22, s24\n\t" \
    "s_add_u32 s2, s2, s6\n\t" \
    "s_add_u32 s10, s10, s14\n\t" \
    "s_add_u32 s18, s18, s22\n\t" \
    "s_add_u32 s2, s2, s26\n\t" \
    "s_add_u32 s10, s10, s18\n\t" \
    "s_add_u32 s2, s2, s10\n\t" \
    "s_mul_i32 s70, s28, 13\n\t" \
    "s_sub_u32 s70, s70, s2\n\t" \
    "s_min_u32 s70, s70, 52\n\t" \
    "s_mul_i32 s69, s69, s70\n\t" \
    "s_mul_hi_u32 s69, s69, 76695844\n\t" \
    "s_mov_b32 s75, s73\n\t" \
    "s_mov_b32 s76, s74\n\t" \
    "s_add_u32 s73, s56, s69\n\t" \
    "s_lshl_b32 s74, s28, 4\n\t" \
    "s_sub_u32 s69, s73, s75\n\t" \
    "s_sub_u32 s70, s74, s76\n\t" \
    "s_sub_u32 s71, s70, 1600\n\t" \
    "s_cmp_lt_u32 s71, 400000\n\t" \
    "s_cbranch_scc0 qk_K91_%=\n\t" \
    "v_cvt_f32_u32 v168, s69\n\t" \
    "v_cvt_f32_u32 v169, s70\n\t" \
    "v_rcp_f32 v170, v168\n\t" \
    "v_rcp_f32 v171, v169\n\t" \
    "v_mul_f32 v170, v170, v169\n\t" \
    "v_mul_f32 v171, v171, v168\n\t" \
    "v_mul_f32 v170, 0x4f800000, v170\n\t" \
    "v_mul_f32 v171, 0x4f000000, v171\n\t" \
    "v_cvt_u32_f32 v170, v170\n\t" \
    "v_cvt_u32_f32 v171, v171\n\t" \
    "v_readfirstlane_b32 s69, v170\n\t" \
    "v_readfirstlane_b32 s70, v171\n\t" \
    "s_nop 1\n\t" \
    "s_min_u32 s71, s65, 1\n\t" \
    "s_sub_u32 s69, s69, s78\n\t" \
    "s_ashr_i32 s69, s69, s71\n\t" \
    "s_add_u32 s78, s78, s69\n\t" \
    "s_sub_u32 s70, s70, s77\n\t" \
    "s_ashr_i32 s70, s70, s71\n\t" \
    "s_add_u32 s77, s77, s70\n\t" \
    "qk_K91_%=:\n\t" \
    "s_add_u32 s65, s65, 1\n\t" \
    "v_mov_b32 v72, 0\n\t" \
    "v_mov_b32 v73, 0\n\t" \
    "v_mov_b32 v74, 0\n\t" \
    "v_mov_b32 v75, 0\n\t" \
    "v_mov_b32 v76, 0\n\t" \
    "v_mov_b32 v77, 0\n\t" \
    "v_mov_b32 v78, 0\n\t" \
    "v_mov_b32 v79, 0\n\t" \
    "v_mov_b32 v80, 0\n\t" \
    "v_mov_b32 v81, 0\n\t" \
    "v_mov_b32 v82, 0\n\t" \
    "v_mov_b32 v83, 0\n\t" \
    "v_mov_b32 v84, 0\n\t" \
    "v_mov_b32 v85, 0\n\t" \
    "v_mov_b32 v86, 0\n\t" \
    "v_mov_b32 v87, 0\n\t" \
    "s_add_u32 s69, s65, 0\n\t" \
    "s_and_b32 s69, s69, 7\n\t" \
    "s_lshl_b32 s69, s69, 18\n\t" \
    "s_add_u32 s69, s69, s62\n\t" \
    "s_add_u32 s46, s36, s69\n\t" \
    "s_addc_u32 s47, s37, 0\n\t" \
    "s_lshr_b64 s[38:39], s[38:39], 4\n\t" \
    "s_and_b32 s97, s38, 15\n\t" \
    "s_lshl_b32 s97, s97, 6\n\t" \
    "s_waitcnt vmcnt(0)\n\t" \
    "v_cmp_eq_u32 vcc, -1, v160\n\t" \
    "s_cbranch_vccz qk_Hok88_%=\n\t" \
    "qk_H87_%=:\n\t" \
    "global_load_dword v160, v20, s[52:53] sc1\n\t" \
    "s_waitcnt vmcnt(0)\n\t" \
    "v_cmp_eq_u32 vcc, -1, v160\n\t" \
    "s_cbranch_vccz qk_Hc89_%=\n\t" \
    "s_cmp_eq_u32 s68, 0\n\t" \
    "s_cbranch_scc1 qk_F92_%=\n\t" \
    "s_sub_u32 s68, s68, 1\n\t" \
    "s_sleep 2\n\t" \
    "s_branch qk_H87_%=\n\t" \
    "qk_F92_%=:\n\t" \
    "v_mov_b32 v161, 5767424\n\t" \
    "v_mov_b32 v162, 5\n\t" \
    "global_store_dword v161, v162, s[36:37] sc1\n\t" \
    "s_waitcnt vmcnt(0)\n\t" \
    "s_branch qk_Hc89_%=\n\t" \
    "qk_Hc89_%=:\n\t" \
    "s_memtime s[56:57]\n\t" \
    "s_waitcnt lgkmcnt(0)\n\t" \
    "s_sub_u32 s69, s56, s73\n\t" \
    "s_cmp_lt_u32 s69, 3000\n\t" \
    "s_cbranch_scc1 qk_St93_%=\n\t" \
    "s_waitcnt lgkmcnt(0)\n\t" \
    "s_mov_b32 s70, 116\n\t" \
    "qk_E94_%=:\n\t" \
    "s_memtime s[56:57]\n\t" \
    "s_memrealtime s[2:3]\n\t" \
    "s_nop 0\n\t" \
    "s_memrealtime s[4:5]\n\t" \
    "s_nop 0\n\t" \
    "s_memrealtime s[6:7]\n\t" \
    "s_nop 0\n\t" \
    "s_memrealtime s[8:9]\n\t" \
    "s_nop 0\n\t" \
    "s_memrealtime s[10:11]\n\t" \
    "s_nop 0\n\t" \
    "s_memrealtime s[12:13]\n\t" \
    "s_nop 0\n\t" \
    "s_memrealtime s[14:15]\n\t" \
    "s_nop 0\n\t" \
    "s_memrealtime s[16:17]\n\t" \
    "s_nop 0\n\t" \
    "s_memrealtime s[18:19]\n\t" \
    "s_nop 0\n\t" \
    "s_memrealtime s[20:21]\n\t" \
    "s_nop 0\n\t" \
    "s_memrealtime s[22:23]\n\t" \
    "s_nop 0\n\t" \
    "s_memrealtime s[24:25]\n\t" \
    "s_nop 0\n\t" \
    "s_memrealtime s[26:27]\n\t" \
    "s_nop 0\n\t" \
    "s_memrealtime s[28:29]\n\t" \
    "s_memtime s[54:55]\n\t" \
    "s_add_u32 s70, s70, 8\n\t" \
    "s_waitcnt lgkmcnt(0)\n\t" \
    "s_sub_u32 s69, s54, s56\n\t" \
    "s_cmp_gt_u32 s69, s70\n\t" \
    "s_cbranch_scc1 qk_E94_%=\n\t" \
    "s_add_u32 s2, s2, s4\n\t" \
    "s_add_u32 s6, s6, s8\n\t" \
    "s_add_u32 s10, s10, s12\n\t" \
    "s_add_u32 s14, s14, s16\n\t" \
    "s_add_u32 s18, s18, s20\n\t" \
    "s_add_u32 s22, s22, s24\n\t" \
    "s_add_u32 s2, s2, s6\n\t" \
    "s_add_u32 s10, s10, s14\n\t" \
    "s_add_u32 s18, s18, s22\n\t" \
    "s_add_u32 s2, s2, s26\n\t" \
    "s_add_u32 s10, s10, s18\n\t" \
    "s_add_u32 s2, s2, s10\n\t" \
    "s_mul_i32 s70, s28, 13\n\t" \
    "s_sub_u32 s70, s70, s2\n\t" \
    "s_min_u32 s70, s70, 52\n\t" \
    "s_mul_i32 s69, s69, s70\n\t" \
    "s_mul_hi_u32 s69, s69, 76695844\n\t" \
    "s_mov_b32 s75, s73\n\t" \
    "s_mov_b32 s76, s74\n\t" \
    "s_add_u32 s73, s56, s69\n\t" \
    "s_lshl_b32 s74, s28, 4\n\t" \
    "qk_St93_%=:\n\t" \
    "qk_Hok88_%=:\n\t" \
    "v_max_u32_dpp v160, v160, v160 row_ror:1 row_mask:0xf bank_mask:0xf\n\t" \
    "s_nop 1\n\t" \
    "v_max_u32_dpp v160, v160, v160 row_ror:2 row_mask:0xf bank_mask:0xf\n\t" \
    "s_nop 1\n\t" \
    "v_max_u32_dpp v160, v160, v160 row_ror:4 row_mask:0xf bank_mask:0xf\n\t" \
    "s_nop 1\n\t" \
    "v_max_u32_dpp v160, v160, v160 row_ror:8 row_mask:0xf bank_mask:0xf\n\t" \
    "s_nop 0\n\t" \
    "v_readlane_b32 s69, v160, 0\n\t" \
    "v_readlane_b32 s71, v160, 16\n\t" \
    "s_nop 1\n\t" \
    "s_max_u32 s81, s69, s71\n\t" \
    "s_mul_hi_u32 s69, s85, s78\n\t" \
    "s_add_u32 s79, s81, s69\n\t" \
    "s_add_u32 s79, s79, s87\n\t" \
    "s_cmp_ge_u32 s65, 53\n\t" \
    "s_cselect_b32 s69, 32, 0\n\t" \
    "s_sub_u32 s79, s79, s69\n\t" \
    "s_cmp_lt_u32 s65, 10\n\t" \
    "s_cselect_b32 s69, 64, 0\n\t" \
    "s_add_u32 s79, s79, s69\n\t" \
    "s_add_u32 s80, s79, s88\n\t" \
    "s_sub_u32 s70, s79, s74\n\t" \
    "s_max_i32 s70, s70, 0\n\t" \
    "s_min_i32 s70, s70, 16000\n\t" \
    "s_lshl_b32 s70, s70, 1\n\t" \
    "s_mul_hi_u32 s70, s70, s77\n\t" \
    "s_add_u32 s70, s70, s73\n\t" \
    "v_mov_b32 v168, s70\n\t" \
    "ds_write_b32 v11, v168 offset:20480\n\t" \
    "s_and_b32 s69, s65, 7\n\t" \
    "s_lshl_b32 s69, s69, 18\n\t" \
    "s_add_u32 s69, s69, s63\n\t" \
    "s_add_u32 s52, s36, s69\n\t" \
    "s_addc_u32 s53, s37, 0\n\t" \
    "s_waitcnt lgkmcnt(0)\n\t" \
    "s_barrier\n\t" \
    "ds_write_b32 v12, v31 offset:0\n\t" \
    "ds_write_b32 v12, v31 offset:64\n\t" \
    "s_branch qk_go75_%=\n\t" \
    "qk_Gbad80_%=:\n\t" \
    "s_waitcnt lgkmcnt(0)\n\t" \
    "s_nop 7\n\t" \
    "v_mov_b32 v72, 0\n\t" \
    "v_mov_b32 v73, 0\n\t" \
    "v_mov_b32 v74, 0\n\t" \
    "v_mov_b32 v75, 0\n\t" \
    "v_mov_b32 v76, 0\n\t" \
    "v_mov_b32 v77, 0\n\t" \
    "v_mov_b32 v78, 0\n\t" \
    "v_mov_b32 v79, 0\n\t" \
    "v_mov_b32 v80, 0\n\t" \
    "v_mov_b32 v81, 0\n\t" \
    "v_mov_b32 v82, 0\n\t" \
    "v_mov_b32 v83, 0\n\t" \
    "v_mov_b32 v84, 0\n\t" \
    "v_mov_b32 v85, 0\n\t" \
    "v_mov_b32 v86, 0\n\t" \
    "v_mov_b32 v87, 0\n\t" \
    "s_cmp_eq_u32 s68, 0\n\t" \
    "s_cbranch_scc1 qk_F95_%=\n\t" \
    "s_sub_u32 s68, s68, 1\n\t" \
    "s_branch qk_G79_%=\n\t" \
    "qk_F95_%=:\n\t" \
    "v_mov_b32 v160, 5767424\n\t" \
    "v_mov_b32 v161, 1\n\t" \
    "global_store_dword v160, v161, s[36:37] sc1\n\t" \
    "s_waitcnt vmcnt(0)\n\t" \
    "s_branch qk_Gc81_%=\n\t" \
    "qk_Gslow82_%=:\n\t" \
    "v_max_u32 v160, v36, v37\n\t" \
    "v_max3_u32 v160, v160, v38, v39\n\t" \
    "v_max3_u32 v160, v160, v40, v41\n\t" \
    "v_max3_u32 v160, v160, v42, v43\n\t" \
    "v_max3_u32 v160, v160, v44, v45\n\t" \
    "v_max3_u32 v160, v160, v46, v47\n\t" \
    "v_max3_u32 v160, v160, v48, v49\n\t" \
    "v_max3_u32 v160, v160, v50, v51\n\t" \
    "v_max3_u32 v160, v160, v52, v53\n\t" \
    "v_max3_u32 v160, v160, v54, v55\n\t" \
    "v_max3_u32 v160, v160, v56, v57\n\t" \
    "v_max3_u32 v160, v160, v58, v59\n\t" \
    "v_max3_u32 v160, v160, v60, v61\n\t" \
    "v_max3_u32 v160, v160, v62, v63\n\t" \
    "v_max3_u32 v160, v160, v64, v65\n\t" \
    "v_max3_u32 v160, v160, v66, v67\n\t" \
    "v_max3_u32 v160, v160, v68, v69\n\t" \
    "v_max3_u32 v160, v160, v70, v71\n\t" \
    "v_cmp_eq_u32 vcc, -1, v160\n\t" \
    "s_cbranch_vccz qk_Gback83_%=\n\t" \
    "s_waitcnt lgkmcnt(0)\n\t" \
    "v_sub_u32 v160, v1, v3\n\t" \
    "s_nop 0\n\t" \
    "v_readfirstlane_b32 s69, v160\n\t" \
    "v_add_u32 v163, 20480, v0\n\t" \
    "s_nop 7\n\t" \
    "s_add_u32 s30, s46, s69\n\t" \
    "s_addc_u32 s31, s47, 0\n\t" \
    "qk_Gag96_%=:\n\t" \
    "global_load_dwordx4 v[36:39], v1, s[46:47] sc1\n\t" \
    "global_load_dwordx4 v[40:43], v23, s[46:47] sc1\n\t" \
    "global_load_dwordx4 v[44:47], v163, s[30:31] sc1\n\t" \
    "global_load_dwordx4 v[48:51], v4, s[30:31] sc1\n\t" \
    "global_load_dwordx4 v[52:55], v5, s[30:31] sc1\n\t" \
    "global_load_dwordx4 v[56:59], v6, s[30:31] sc1\n\t" \
    "global_load_dwordx4 v[60:63], v7, s[30:31] sc1\n\t" \
    "global_load_dwordx4 v[64:67], v8, s[30:31] sc1\n\t" \
    "global_load_dwordx4 v[68:71], v9, s[30:31] sc1\n\t" \
    "s_waitcnt vmcnt(0)\n\t" \
    "v_max_u32 v160, v36, v37\n\t" \
    "v_max3_u32 v160, v160, v38, v39\n\t" \
    "v_max3_u32 v160, v160, v40, v41\n\t" \
    "v_max3_u32 v160, v160, v42, v43\n\t" \
    "v_max3_u32 v160, v160, v44, v45\n\t" \
    "v_max3_u32 v160, v160, v46, v47\n\t" \
    "v_max3_u32 v160, v160, v48, v49\n\t" \
    "v_max3_u32 v160, v160, v50, v51\n\t" \
    "v_max3_u32 v160, v160, v52, v53\n\t" \
    "v_max3_u32 v160, v160, v54, v55\n\t" \
    "v_max3_u32 v160, v160, v56, v57\n\t" \
    "v_max3_u32 v160, v160, v58, v59\n\t" \
    "v_max3_u32 v160, v160, v60, v61\n\t" \
    "v_max3_u32 v160, v160, v62, v63\n\t" \
    "v_max3_u32 v160, v160, v64, v65\n\t" \
    "v_max3_u32 v160, v160, v66, v67\n\t" \
    "v_max3_u32 v160, v160, v68, v69\n\t" \
    "v_max3_u32 v160, v160, v70, v71\n\t" \
    "v_cmp_eq_u32 vcc, -1, v160\n\t" \
    "s_cbranch_vccz qk_Gredo97_%=\n\t" \
    "s_cmp_eq_u32 s68, 0\n\t" \
    "s_cbranch_scc1 qk_F98_%=\n\t" \
    "s_sub_u32 s68, s68, 1\n\t" \
    "s_sleep 1\n\t" \
    "s_branch qk_Gag96_%=\n\t" \
    "qk_F98_%=:\n\t" \
    "v_mov_b32 v160, 5767424\n\t" \
    "v_mov_b32 v161, 2\n\t" \
    "global_store_dword v160, v161, s[36:37] sc1\n\t" \
    "s_waitcnt vmcnt(0)\n\t" \
    "s_branch qk_Gredo97_%=\n\t" \
    "qk_Gredo97_%=:\n\t" \
    "v_mov_b32 v72, 0\n\t" \
    "v_mov_b32 v73, 0\n\t" \
    "v_mov_b32 v74, 0\n\t" \
    "v_mov_b32 v75, 0\n\t" \
    "v_mov_b32 v76, 0\n\t" \
    "v_mov_b32 v77, 0\n\t" \
    "v_mov_b32 v78, 0\n\t" \
    "v_mov_b32 v79, 0\n\t" \
    "v_mov_b32 v80, 0\n\t" \
    "v_mov_b32 v81, 0\n\t" \
    "v_mov_b32 v82, 0\n\t" \
    "v_mov_b32 v83, 0\n\t" \
    "v_mov_b32 v84, 0\n\t" \
    "v_mov_b32 v85, 0\n\t" \
    "v_mov_b32 v86, 0\n\t" \
    "v_mov_b32 v87, 0\n\t" \
    "v_smfmac_f32_16x16x64_bf16 v[72:75], v[36:39], a[48:55], v10\n\t" \
    "v_smfmac_f32_16x16x64_bf16 v[72:75], v[40:43], a[56:63], v10\n\t" \
    "v_smfmac_f32_16x16x64_bf16 v[72:75], v[44:47], a[64:71], v10\n\t" \
    "v_smfmac_f32_16x16x64_bf16 v[72:75], v[48:51], a[0:7], v10\n\t" \
    "v_smfmac_f32_16x16x64_bf16 v[72:75], v[52:55], a[8:15], v10\n\t" \
    "v_smfmac_f32_16x16x64_bf16 v[72:75], v[56:59], a[16:23], v10\n\t" \
    "v_smfmac_f32_16x16x64_bf16 v[72:75], v[60:63], a[24:31], v10\n\t" \
    "v_smfmac_f32_16x16x64_bf16 v[72:75], v[64:67], a[32:39], v10\n\t" \
    "v_smfmac_f32_16x16x64_bf16 v[72:75], v[68:71], a[40:47], v10\n\t" \
    "v_smfmac_f32_16x16x64_bf16 v[76:79], v[36:39], a[120:127], v10\n\t" \
    "v_smfmac_f32_16x16x64_bf16 v[76:79], v[40:43], a[128:135], v10\n\t" \
    "v_smfmac_f32_16x16x64_bf16 v[76:79], v[44:47], a[136:143], v10\n\t" \
    "v_smfmac_f32_16x16x64_bf16 v[76:79], v[48:51], a[72:79], v10\n\t" \
    "v_smfmac_f32_16x16x64_bf16 v[76:79], v[52:55], a[80:87], v10\n\t" \
    "v_smfmac_f32_16x16x64_bf16 v[76:79], v[56:59], a[88:95], v10\n\t" \
    "v_smfmac_f32_16x16x64_bf16 v[76:79], v[60:63], a[96:103], v10\n\t" \
    "v_smfmac_f32_16x16x64_bf16 v[76:79], v[64:67], a[104:111], v10\n\t" \
    "v_smfmac_f32_16x16x64_bf16 v[76:79], v[68:71], a[112:119], v10\n\t" \
    "v_smfmac_f32_16x16x64_bf16 v[80:83], v[36:39], a[192:199], v10\n\t" \
    "v_smfmac_f32_16x16x64_bf16 v[80:83], v[40:43], a[200:207], v10\n\t" \
    "v_smfmac_f32_16x16x64_bf16 v[80:83], v[44:47], a[208:215], v10\n\t" \
    "v_smfmac_f32_16x16x64_bf16 v[80:83], v[48:51], a[144:151], v10\n\t" \
    "v_smfmac_f32_16x16x64_bf16 v[80:83], v[52:55], a[152:159], v10\n\t" \
    "v_smfmac_f32_16x16x64_bf16 v[80:83], v[56:59], a[160:167], v10\n\t" \
    "v_smfmac_f32_16x16x64_bf16 v[80:83], v[60:63], a[168:175], v10\n\t" \
    "v_smfmac_f32_16x16x64_bf16 v[80:83], v[64:67], a[176:183], v10\n\t" \
    "v_smfmac_f32_16x16x64_bf16 v[80:83], v[68:71], a[184:191], v10\n\t" \
    "v_smfmac_f32_16x16x64_bf16 v[84:87], v[36:39], v[232:239], v10\n\t" \
    "v_smfmac_f32_16x16x64_bf16 v[84:87], v[40:43], v[240:247], v10\n\t" \
    "v_smfmac_f32_16x16x64_bf16 v[84:87], v[44:47], v[248:255], v10\n\t" \
    "v_smfmac_f32_16x16x64_bf16 v[84:87], v[48:51], a[216:223], v10\n\t" \
    "v_smfmac_f32_16x16x64_bf16 v[84:87], v[52:55], a[224:231], v10\n\t" \
    "v_smfmac_f32_16x16x64_bf16 v[84:87], v[56:59], a[232:239], v10\n\t" \
    "v_smfmac_f32_16x16x64_bf16 v[84:87], v[60:63], a[240:247], v10\n\t" \
    "v_smfmac_f32_16x16x64_bf16 v[84:87], v[64:67], a[248:255], v10\n\t" \
    "v_smfmac_f32_16x16x64_bf16 v[84:87], v[68:71], v[224:231], v10\n\t" \
    "v_add_f32 v72, v72, v74\n\t" \
    "v_add_f32 v73, v73, v75\n\t" \
    "v_cvt_pk_bf16_f32 v156, v72, v73\n\t" \
    "ds_write_b32 v12, v156 offset:0\n\t" \
    "v_add_f32 v76, v76, v78\n\t" \
    "v_add_f32 v77, v77, v79\n\t" \
    "v_cvt_pk_bf16_f32 v157, v76, v77\n\t" \
    "ds_write_b32 v12, v157 offset:64\n\t" \
    "v_add_f32 v80, v80, v82\n\t" \
    "v_add_f32 v81, v81, v83\n\t" \
    "v_cvt_pk_bf16_f32 v158, v80, v81\n\t" \
    "ds_write_b32 v16, v158 offset:0\n\t" \
    "v_pk_add_f32 v[84:85], v[84:85], v[86:87]\n\t" \
    "v_cvt_pk_bf16_f32 v159, v84, v85\n\t" \
    "ds_write_b32 v16, v159 offset:256\n\t" \
    "s_branch qk_Gback83_%=\n\t" \
    "qk_last74_%=:\n\t" \
    "s_endpgm\n\t" \
    "qk_last_%=:\n\t" \
    ""
#define ASM_V \
    "s_load_dwordx2 s[42:43], %3, 56\n\t" \
    "s_load_dwordx2 s[44:45], %3, 64\n\t" \
    "s_load_dwordx2 s[36:37], %3, 0x58\n\t" \
    "s_load_dwordx2 s[50:51], %3, 0x48\n\t" \
    "s_load_dwordx8 s[84:91], %3, 0\n\t" \
    "s_mov_b32 s66, %4\n\t" \
    "s_mov_b32 s67, %5\n\t" \
    "s_mov_b32 s68, 262144\n\t" \
    "s_mov_b32 s72, 0\n\t" \
    "s_mov_b32 s83, -1\n\t" \
    "s_mov_b32 s65, 0\n\t" \
    "s_lshl_b32 s82, s66, 10\n\t" \
    "s_waitcnt lgkmcnt(0)\n\t" \
    "s_add_u32 s46, s42, 2097152\n\t" \
    "s_addc_u32 s47, s43, 0\n\t" \
    "s_add_u32 m0, s82, 65536\n\t" \
    "s_nop 0\n\t" \
    "global_load_lds_dwordx4 v200, s[46:47]\n\t" \
    "s_add_u32 s46, s42, 2228224\n\t" \
    "s_addc_u32 s47, s43, 0\n\t" \
    "s_add_u32 m0, s82, 69632\n\t" \
    "s_nop 0\n\t" \
    "global_load_lds_dwordx4 v200, s[46:47]\n\t" \
    "s_add_u32 s46, s42, 2359296\n\t" \
    "s_addc_u32 s47, s43, 0\n\t" \
    "s_add_u32 m0, s82, 73728\n\t" \
    "s_nop 0\n\t" \
    "global_load_lds_dwordx4 v200, s[46:47]\n\t" \
    "s_add_u32 s46, s42, 2490368\n\t" \
    "s_addc_u32 s47, s43, 0\n\t" \
    "s_add_u32 m0, s82, 77824\n\t" \
    "s_nop 0\n\t" \
    "global_load_lds_dwordx4 v200, s[46:47]\n\t" \
    "s_add_u32 s46, s42, 2621440\n\t" \
    "s_addc_u32 s47, s43, 0\n\t" \
    "s_add_u32 m0, s82, 81920\n\t" \
    "s_nop 0\n\t" \
    "global_load_lds_dwordx4 v200, s[46:47]\n\t" \
    "s_add_u32 s46, s42, 2752512\n\t" \
    "s_addc_u32 s47, s43, 0\n\t" \
    "s_add_u32 m0, s82, 86016\n\t" \
    "s_nop 0\n\t" \
    "global_load_lds_dwordx4 v200, s[46:47]\n\t" \
    "s_add_u32 s46, s42, 2883584\n\t" \
    "s_addc_u32 s47, s43, 0\n\t" \
    "s_add_u32 m0, s82, 90112\n\t" \
    "s_nop 0\n\t" \
    "global_load_lds_dwordx4 v200, s[46:47]\n\t" \
    "s_add_u32 s46, s42, 3014656\n\t" \
    "s_addc_u32 s47, s43, 0\n\t" \
    "s_add_u32 m0, s82, 94208\n\t" \
    "s_nop 0\n\t" \
    "global_load_lds_dwordx4 v200, s[46:47]\n\t" \
    "s_add_u32 s46, s42, 3145728\n\t" \
    "s_addc_u32 s47, s43, 0\n\t" \
    "s_add_u32 m0, s82, 98304\n\t" \
    "s_nop 0\n\t" \
    "global_load_lds_dwordx4 v200, s[46:47]\n\t" \
    "s_add_u32 s46, s42, 3276800\n\t" \
    "s_addc_u32 s47, s43, 0\n\t" \
    "s_add_u32 m0, s82, 102400\n\t" \
    "s_nop 0\n\t" \
    "global_load_lds_dwordx4 v200, s[46:47]\n\t" \
    "s_add_u32 s46, s42, 3407872\n\t" \
    "s_addc_u32 s47, s43, 0\n\t" \
    "s_add_u32 m0, s82, 106496\n\t" \
    "s_nop 0\n\t" \
    "global_load_lds_dwordx4 v200, s[46:47]\n\t" \
    "s_add_u32 s46, s42, 3538944\n\t" \
    "s_addc_u32 s47, s43, 0\n\t" \
    "s_add_u32 m0, s82, 110592\n\t" \
    "s_nop 0\n\t" \
    "global_load_lds_dwordx4 v200, s[46:47]\n\t" \
    "s_add_u32 s46, s42, 3670016\n\t" \
    "s_addc_u32 s47, s43, 0\n\t" \
    "s_add_u32 m0, s82, 114688\n\t" \
    "s_nop 0\n\t" \
    "global_load_lds_dwordx4 v200, s[46:47]\n\t" \
    "s_add_u32 s46, s42, 3801088\n\t" \
    "s_addc_u32 s47, s43, 0\n\t" \
    "s_add_u32 m0, s82, 118784\n\t" \
    "s_nop 0\n\t" \
    "global_load_lds_dwordx4 v200, s[46:47]\n\t" \
    "s_add_u32 s46, s42, 3932160\n\t" \
    "s_addc_u32 s47, s43, 0\n\t" \
    "s_add_u32 m0, s82, 122880\n\t" \
    "s_nop 0\n\t" \
    "global_load_lds_dwordx4 v200, s[46:47]\n\t" \
    "s_add_u32 s46, s42, 4063232\n\t" \
    "s_addc_u32 s47, s43, 0\n\t" \
    "s_add_u32 m0, s82, 126976\n\t" \
    "s_nop 0\n\t" \
    "global_load_lds_dwordx4 v200, s[46:47]\n\t" \
    "s_add_u32 s46, s42, 4194304\n\t" \
    "s_addc_u32 s47, s43, 0\n\t" \
    "s_add_u32 m0, s82, 131072\n\t" \
    "s_nop 0\n\t" \
    "global_load_lds_dwordx4 v200, s[46:47]\n\t" \
    "s_add_u32 s46, s42, 4325376\n\t" \
    "s_addc_u32 s47, s43, 0\n\t" \
    "s_add_u32 m0, s82, 135168\n\t" \
    "s_nop 0\n\t" \
    "global_load_lds_dwordx4 v200, s[46:47]\n\t" \
    "s_add_u32 s46, s42, 4456448\n\t" \
    "s_addc_u32 s47, s43, 0\n\t" \
    "s_add_u32 m0, s82, 139264\n\t" \
    "s_nop 0\n\t" \
    "global_load_lds_dwordx4 v200, s[46:47]\n\t" \
    "s_add_u32 s46, s42, 4587520\n\t" \
    "s_addc_u32 s47, s43, 0\n\t" \
    "s_add_u32 m0, s82, 143360\n\t" \
    "s_nop 0\n\t" \
    "global_load_lds_dwordx4 v200, s[46:47]\n\t" \
    "s_mov_b32 s77, 3019898880\n\t" \
    "s_mov_b32 s78, 3054198966\n\t" \
    "s_waitcnt lgkmcnt(0)\n\t" \
    "s_mov_b32 s70, 116\n\t" \
    "vv_E1_%=:\n\t" \
    "s_memtime s[56:57]\n\t" \
    "s_memrealtime s[2:3]\n\t" \
    "s_nop 0\n\t" \
    "s_memrealtime s[4:5]\n\t" \
    "s_nop 0\n\t" \
    "s_memrealtime s[6:7]\n\t" \
    "s_nop 0\n\t" \
    "s_memrealtime s[8:9]\n\t" \
    "s_nop 0\n\t" \
    "s_memrealtime s[10:11]\n\t" \
    "s_nop 0\n\t" \
    "s_memrealtime s[12:13]\n\t" \
    "s_nop 0\n\t" \
    "s_memrealtime s[14:15]\n\t" \
    "s_nop 0\n\t" \
    "s_memrealtime s[16:17]\n\t" \
    "s_nop 0\n\t" \
    "s_memrealtime s[18:19]\n\t" \
    "s_nop 0\n\t" \
    "s_memrealtime s[20:21]\n\t" \
    "s_nop 0\n\t" \
    "s_memrealtime s[22:23]\n\t" \
    "s_nop 0\n\t" \
    "s_memrealtime s[24:25]\n\t" \
    "s_nop 0\n\t" \
    "s_memrealtime s[26:27]\n\t" \
    "s_nop 0\n\t" \
    "s_memrealtime s[28:29]\n\t" \
    "s_memtime s[54:55]\n\t" \
    "s_add_u32 s70, s70, 8\n\t" \
    "s_waitcnt lgkmcnt(0)\n\t" \
    "s_sub_u32 s69, s54, s56\n\t" \
    "s_cmp_gt_u32 s69, s70\n\t" \
    "s_cbranch_scc1 vv_E1_%=\n\t" \
    "s_add_u32 s2, s2, s4\n\t" \
    "s_add_u32 s6, s6, s8\n\t" \
    "s_add_u32 s10, s10, s12\n\t" \
    "s_add_u32 s14, s14, s16\n\t" \
    "s_add_u32 s18, s18, s20\n\t" \
    "s_add_u32 s22, s22, s24\n\t" \
    "s_add_u32 s2, s2, s6\n\t" \
    "s_add_u32 s10, s10, s14\n\t" \
    "s_add_u32 s18, s18, s22\n\t" \
    "s_add_u32 s2, s2, s26\n\t" \
    "s_add_u32 s10, s10, s18\n\t" \
    "s_add_u32 s2, s2, s10\n\t" \
    "s_mul_i32 s70, s28, 13\n\t" \
    "s_sub_u32 s70, s70, s2\n\t" \
    "s_min_u32 s70, s70, 52\n\t" \
    "s_mul_i32 s69, s69, s70\n\t" \
    "s_mul_hi_u32 s69, s69, 76695844\n\t" \
    "s_mov_b32 s75, s73\n\t" \
    "s_mov_b32 s76, s74\n\t" \
    "s_add_u32 s73, s56, s69\n\t" \
    "s_lshl_b32 s74, s28, 4\n\t" \
    "s_waitcnt vmcnt(32) lgkmcnt(0)\n\t" \
    "s_barrier\n\t" \
    "ds_read_b64_tr_b16 a[0:1], v201 offset:0\n\t" \
    "ds_read_b64_tr_b16 a[2:3], v201 offset:1024\n\t" \
    "ds_read_b64_tr_b16 a[4:5], v201 offset:8192\n\t" \
    "ds_read_b64_tr_b16 a[6:7], v201 offset:9216\n\t" \
    "ds_read_b64_tr_b16 a[72:73], v202 offset:0\n\t" \
    "ds_read_b64_tr_b16 a[74:75], v202 offset:1024\n\t" \
    "ds_read_b64_tr_b16 a[76:77], v202 offset:8192\n\t" \
    "ds_read_b64_tr_b16 a[78:79], v202 offset:9216\n\t" \
    "s_waitcnt vmcnt(28) lgkmcnt(0)\n\t" \
    "s_barrier\n\t" \
    "ds_read_b64_tr_b16 a[8:9], v201 offset:16384\n\t" \
    "ds_read_b64_tr_b16 a[10:11], v201 offset:17408\n\t" \
    "ds_read_b64_tr_b16 a[12:13], v201 offset:24576\n\t" \
    "ds_read_b64_tr_b16 a[14:15], v201 offset:25600\n\t" \
    "ds_read_b64_tr_b16 a[80:81], v202 offset:16384\n\t" \
    "ds_read_b64_tr_b16 a[82:83], v202 offset:17408\n\t" \
    "ds_read_b64_tr_b16 a[84:85], v202 offset:24576\n\t" \
    "ds_read_b64_tr_b16 a[86:87], v202 offset:25600\n\t" \
    "s_waitcnt vmcnt(24) lgkmcnt(0)\n\t" \
    "s_barrier\n\t" \
    "ds_read_b64_tr_b16 a[16:17], v201 offset:32768\n\t" \
    "ds_read_b64_tr_b16 a[18:19], v201 offset:33792\n\t" \
    "ds_read_b64_tr_b16 a[20:21], v201 offset:40960\n\t" \
    "ds_read_b64_tr_b16 a[22:23], v201 offset:41984\n\t" \
    "ds_read_b64_tr_b16 a[88:89], v202 offset:32768\n\t" \
    "ds_read_b64_tr_b16 a[90:91], v202 offset:33792\n\t" \
    "ds_read_b64_tr_b16 a[92:93], v202 offset:40960\n\t" \
    "ds_read_b64_tr_b16 a[94:95], v202 offset:41984\n\t" \
    "s_waitcnt vmcnt(20) lgkmcnt(0)\n\t" \
    "s_barrier\n\t" \
    "ds_read_b64_tr_b16 a[24:25], v201 offset:49152\n\t" \
    "ds_read_b64_tr_b16 a[26:27], v201 offset:50176\n\t" \
    "ds_read_b64_tr_b16 a[28:29], v201 offset:57344\n\t" \
    "ds_read_b64_tr_b16 a[30:31], v201 offset:58368\n\t" \
    "ds_read_b64_tr_b16 a[96:97], v202 offset:49152\n\t" \
    "ds_read_b64_tr_b16 a[98:99], v202 offset:50176\n\t" \
    "ds_read_b64_tr_b16 a[100:101], v202 offset:57344\n\t" \
    "ds_read_b64_tr_b16 a[102:103], v202 offset:58368\n\t" \
    "s_add_u32 s46, s44, 0\n\t" \
    "s_addc_u32 s47, s45, 0\n\t" \
    "s_mul_i32 s69, s82, 9\n\t" \
    "s_add_u32 m0, s69, 0\n\t" \
    "s_nop 0\n\t" \
    "global_load_lds_dwordx4 v203, s[46:47]\n\t" \
    "s_add_u32 m0, s69, 1024\n\t" \
    "s_nop 0\n\t" \
    "global_load_lds_dwordx4 v204, s[46:47]\n\t" \
    "s_add_u32 m0, s69, 2048\n\t" \
    "s_nop 0\n\t" \
    "global_load_lds_dwordx4 v205, s[46:47]\n\t" \
    "s_add_u32 m0, s69, 3072\n\t" \
    "s_nop 0\n\t" \
    "global_load_lds_dwordx4 v206, s[46:47]\n\t" \
    "s_add_u32 m0, s69, 4096\n\t" \
    "s_nop 0\n\t" \
    "global_load_lds_dwordx4 v207, s[46:47]\n\t" \
    "s_add_u32 m0, s69, 5120\n\t" \
    "s_nop 0\n\t" \
    "global_load_lds_dwordx4 v208, s[46:47]\n\t" \
    "s_add_u32 m0, s69, 6144\n\t" \
    "s_nop 0\n\t" \
    "global_load_lds_dwordx4 v209, s[46:47]\n\t" \
    "s_add_u32 m0, s69, 7168\n\t" \
    "s_nop 0\n\t" \
    "global_load_lds_dwordx4 v210, s[46:47]\n\t" \
    "s_add_u32 m0, s69, 8192\n\t" \
    "s_nop 0\n\t" \
    "global_load_lds_dwordx4 v211, s[46:47]\n\t" \
    "s_waitcnt vmcnt(25) lgkmcnt(0)\n\t" \
    "s_barrier\n\t" \
    "v_add_u32 v201, 0x10000, v201\n\t" \
    "v_add_u32 v202, 0x10000, v202\n\t" \
    "ds_read_b64_tr_b16 a[32:33], v201 offset:0\n\t" \
    "ds_read_b64_tr_b16 a[34:35], v201 offset:1024\n\t" \
    "ds_read_b64_tr_b16 a[36:37], v201 offset:8192\n\t" \
    "ds_read_b64_tr_b16 a[38:39], v201 offset:9216\n\t" \
    "ds_read_b64_tr_b16 a[104:105], v202 offset:0\n\t" \
    "ds_read_b64_tr_b16 a[106:107], v202 offset:1024\n\t" \
    "ds_read_b64_tr_b16 a[108:109], v202 offset:8192\n\t" \
    "ds_read_b64_tr_b16 a[110:111], v202 offset:9216\n\t" \
    "s_waitcnt vmcnt(21) lgkmcnt(0)\n\t" \
    "s_barrier\n\t" \
    "ds_read_b64_tr_b16 a[40:41], v201 offset:16384\n\t" \
    "ds_read_b64_tr_b16 a[42:43], v201 offset:17408\n\t" \
    "ds_read_b64_tr_b16 a[44:45], v201 offset:24576\n\t" \
    "ds_read_b64_tr_b16 a[46:47], v201 offset:25600\n\t" \
    "ds_read_b64_tr_b16 a[112:113], v202 offset:16384\n\t" \
    "ds_read_b64_tr_b16 a[114:115], v202 offset:17408\n\t" \
    "ds_read_b64_tr_b16 a[116:117], v202 offset:24576\n\t" \
    "ds_read_b64_tr_b16 a[118:119], v202 offset:25600\n\t" \
    "s_add_u32 s46, s44, 36864\n\t" \
    "s_addc_u32 s47, s45, 0\n\t" \
    "s_mul_i32 s69, s82, 9\n\t" \
    "s_add_u32 m0, s69, 36864\n\t" \
    "s_nop 0\n\t" \
    "global_load_lds_dwordx4 v203, s[46:47]\n\t" \
    "s_add_u32 m0, s69, 37888\n\t" \
    "s_nop 0\n\t" \
    "global_load_lds_dwordx4 v204, s[46:47]\n\t" \
    "s_add_u32 m0, s69, 38912\n\t" \
    "s_nop 0\n\t" \
    "global_load_lds_dwordx4 v205, s[46:47]\n\t" \
    "s_add_u32 m0, s69, 39936\n\t" \
    "s_nop 0\n\t" \
    "global_load_lds_dwordx4 v206, s[46:47]\n\t" \
    "s_add_u32 m0, s69, 40960\n\t" \
    "s_nop 0\n\t" \
    "global_load_lds_dwordx4 v207, s[46:47]\n\t" \
    "s_add_u32 m0, s69, 41984\n\t" \
    "s_nop 0\n\t" \
    "global_load_lds_dwordx4 v208, s[46:47]\n\t" \
    "s_add_u32 m0, s69, 43008\n\t" \
    "s_nop 0\n\t" \
    "global_load_lds_dwordx4 v209, s[46:47]\n\t" \
    "s_add_u32 m0, s69, 44032\n\t" \
    "s_nop 0\n\t" \
    "global_load_lds_dwordx4 v210, s[46:47]\n\t" \
    "s_add_u32 m0, s69, 45056\n\t" \
    "s_nop 0\n\t" \
    "global_load_lds_dwordx4 v211, s[46:47]\n\t" \
    "s_waitcnt vmcnt(26) lgkmcnt(0)\n\t" \
    "s_barrier\n\t" \
    "ds_read_b64_tr_b16 a[48:49], v201 offset:32768\n\t" \
    "ds_read_b64_tr_b16 a[50:51], v201 offset:33792\n\t" \
    "ds_read_b64_tr_b16 a[52:53], v201 offset:40960\n\t" \
    "ds_read_b64_tr_b16 a[54:55], v201 offset:41984\n\t" \
    "ds_read_b64_tr_b16 a[120:121], v202 offset:32768\n\t" \
    "ds_read_b64_tr_b16 a[122:123], v202 offset:33792\n\t" \
    "ds_read_b64_tr_b16 a[124:125], v202 offset:40960\n\t" \
    "ds_read_b64_tr_b16 a[126:127], v202 offset:41984\n\t" \
    "s_waitcnt vmcnt(22) lgkmcnt(0)\n\t" \
    "s_barrier\n\t" \
    "ds_read_b64_tr_b16 a[56:57], v201 offset:49152\n\t" \
    "ds_read_b64_tr_b16 a[58:59], v201 offset:50176\n\t" \
    "ds_read_b64_tr_b16 a[60:61], v201 offset:57344\n\t" \
    "ds_read_b64_tr_b16 a[62:63], v201 offset:58368\n\t" \
    "ds_read_b64_tr_b16 a[128:129], v202 offset:49152\n\t" \
    "ds_read_b64_tr_b16 a[130:131], v202 offset:50176\n\t" \
    "ds_read_b64_tr_b16 a[132:133], v202 offset:57344\n\t" \
    "ds_read_b64_tr_b16 a[134:135], v202 offset:58368\n\t" \
    "s_add_u32 s46, s44, 73728\n\t" \
    "s_addc_u32 s47, s45, 0\n\t" \
    "s_mul_i32 s69, s82, 9\n\t" \
    "s_add_u32 m0, s69, 73728\n\t" \
    "s_nop 0\n\t" \
    "global_load_lds_dwordx4 v203, s[46:47]\n\t" \
    "s_add_u32 m0, s69, 74752\n\t" \
    "s_nop 0\n\t" \
    "global_load_lds_dwordx4 v204, s[46:47]\n\t" \
    "s_add_u32 m0, s69, 75776\n\t" \
    "s_nop 0\n\t" \
    "global_load_lds_dwordx4 v205, s[46:47]\n\t" \
    "s_add_u32 m0, s69, 76800\n\t" \
    "s_nop 0\n\t" \
    "global_load_lds_dwordx4 v206, s[46:47]\n\t" \
    "s_add_u32 m0, s69, 77824\n\t" \
    "s_nop 0\n\t" \
    "global_load_lds_dwordx4 v207, s[46:47]\n\t" \
    "s_add_u32 m0, s69, 78848\n\t" \
    "s_nop 0\n\t" \
    "global_load_lds_dwordx4 v208, s[46:47]\n\t" \
    "s_add_u32 m0, s69, 79872\n\t" \
    "s_nop 0\n\t" \
    "global_load_lds_dwordx4 v209, s[46:47]\n\t" \
    "s_add_u32 m0, s69, 80896\n\t" \
    "s_nop 0\n\t" \
    "global_load_lds_dwordx4 v210, s[46:47]\n\t" \
    "s_add_u32 m0, s69, 81920\n\t" \
    "s_nop 0\n\t" \
    "global_load_lds_dwordx4 v211, s[46:47]\n\t" \
    "v_mbcnt_lo_u32_b32 v140, -1, 0\n\t" \
    "v_mbcnt_hi_u32_b32 v140, -1, v140\n\t" \
    "v_lshlrev_b32 v140, 6, v140\n\t" \
    "v_add_u32 v140, 6225920, v140\n\t" \
    "v_mov_b32 v141, 6234112\n\t" \
    "global_load_dword v141, v141, s[36:37] sc1\n\t" \
    "global_load_dwordx2 v[142:143], v140, s[36:37] sc1\n\t" \
    "global_load_dword v144, v140, s[36:37] offset:2048 sc1\n\t" \
    "s_waitcnt vmcnt(30) lgkmcnt(0)\n\t" \
    "s_barrier\n\t" \
    "v_add_u32 v201, 0x10000, v201\n\t" \
    "v_add_u32 v202, 0x10000, v202\n\t" \
    "ds_read_b64_tr_b16 a[64:65], v201 offset:0\n\t" \
    "ds_read_b64_tr_b16 a[66:67], v201 offset:1024\n\t" \
    "ds_read_b64_tr_b16 a[68:69], v201 offset:8192\n\t" \
    "ds_read_b64_tr_b16 a[70:71], v201 offset:9216\n\t" \
    "ds_read_b64_tr_b16 a[136:137], v202 offset:0\n\t" \
    "ds_read_b64_tr_b16 a[138:139], v202 offset:1024\n\t" \
    "ds_read_b64_tr_b16 a[140:141], v202 offset:8192\n\t" \
    "ds_read_b64_tr_b16 a[142:143], v202 offset:9216\n\t" \
    "v_add_u32 v201, 0xfffe0000, v201\n\t" \
    "v_add_u32 v202, 0xfffe0000, v202\n\t" \
    "s_waitcnt lgkmcnt(0)\n\t" \
    "s_barrier\n\t" \
    "s_add_u32 s46, s44, 110592\n\t" \
    "s_addc_u32 s47, s45, 0\n\t" \
    "s_mul_i32 s69, s82, 9\n\t" \
    "s_add_u32 m0, s69, 110592\n\t" \
    "s_nop 0\n\t" \
    "global_load_lds_dwordx4 v203, s[46:47]\n\t" \
    "s_add_u32 m0, s69, 111616\n\t" \
    "s_nop 0\n\t" \
    "global_load_lds_dwordx4 v204, s[46:47]\n\t" \
    "s_add_u32 m0, s69, 112640\n\t" \
    "s_nop 0\n\t" \
    "global_load_lds_dwordx4 v205, s[46:47]\n\t" \
    "s_add_u32 m0, s69, 113664\n\t" \
    "s_nop 0\n\t" \
    "global_load_lds_dwordx4 v206, s[46:47]\n\t" \
    "s_add_u32 m0, s69, 114688\n\t" \
    "s_nop 0\n\t" \
    "global_load_lds_dwordx4 v207, s[46:47]\n\t" \
    "s_add_u32 m0, s69, 115712\n\t" \
    "s_nop 0\n\t" \
    "global_load_lds_dwordx4 v208, s[46:47]\n\t" \
    "s_add_u32 m0, s69, 116736\n\t" \
    "s_nop 0\n\t" \
    "global_load_lds_dwordx4 v209, s[46:47]\n\t" \
    "s_add_u32 m0, s69, 117760\n\t" \
    "s_nop 0\n\t" \
    "global_load_lds_dwordx4 v210, s[46:47]\n\t" \
    "s_add_u32 m0, s69, 118784\n\t" \
    "s_nop 0\n\t" \
    "global_load_lds_dwordx4 v211, s[46:47]\n\t" \
    "s_waitcnt vmcnt(21) lgkmcnt(0)\n\t" \
    "s_barrier\n\t" \
    "ds_read_b64_tr_b16 a[144:145], v212 offset:0\n\t" \
    "ds_read_b64_tr_b16 a[146:147], v212 offset:2304\n\t" \
    "ds_read_b64_tr_b16 a[148:149], v212 offset:18432\n\t" \
    "ds_read_b64_tr_b16 a[150:151], v212 offset:20736\n\t" \
    "s_waitcnt lgkmcnt(6)\n\t" \
    "ds_read_b64_tr_b16 a[176:177], v213 offset:0\n\t" \
    "ds_read_b64_tr_b16 a[178:179], v213 offset:2304\n\t" \
    "ds_read_b64_tr_b16 a[180:181], v213 offset:18432\n\t" \
    "ds_read_b64_tr_b16 a[182:183], v213 offset:20736\n\t" \
    "s_waitcnt lgkmcnt(6)\n\t" \
    "ds_read_b64_tr_b16 a[208:209], v212 offset:128\n\t" \
    "ds_read_b64_tr_b16 a[210:211], v212 offset:2432\n\t" \
    "ds_read_b64_tr_b16 a[212:213], v212 offset:18560\n\t" \
    "ds_read_b64_tr_b16 a[214:215], v212 offset:20864\n\t" \
    "s_waitcnt lgkmcnt(6)\n\t" \
    "ds_read_b64_tr_b16 a[240:241], v213 offset:128\n\t" \
    "ds_read_b64_tr_b16 a[242:243], v213 offset:2432\n\t" \
    "ds_read_b64_tr_b16 a[244:245], v213 offset:18560\n\t" \
    "ds_read_b64_tr_b16 a[246:247], v213 offset:20864\n\t" \
    "s_waitcnt lgkmcnt(6)\n\t" \
    "ds_read_b64_tr_b16 a[152:153], v214 offset:0\n\t" \
    "ds_read_b64_tr_b16 a[154:155], v214 offset:2304\n\t" \
    "ds_read_b64_tr_b16 a[156:157], v214 offset:18432\n\t" \
    "ds_read_b64_tr_b16 a[158:159], v214 offset:20736\n\t" \
    "s_waitcnt lgkmcnt(6)\n\t" \
    "ds_read_b64_tr_b16 a[184:185], v215 offset:0\n\t" \
    "ds_read_b64_tr_b16 a[186:187], v215 offset:2304\n\t" \
    "ds_read_b64_tr_b16 a[188:189], v215 offset:18432\n\t" \
    "ds_read_b64_tr_b16 a[190:191], v215 offset:20736\n\t" \
    "s_waitcnt lgkmcnt(6)\n\t" \
    "ds_read_b64_tr_b16 a[216:217], v214 offset:128\n\t" \
    "ds_read_b64_tr_b16 a[218:219], v214 offset:2432\n\t" \
    "ds_read_b64_tr_b16 a[220:221], v214 offset:18560\n\t" \
    "ds_read_b64_tr_b16 a[222:223], v214 offset:20864\n\t" \
    "s_waitcnt lgkmcnt(6)\n\t" \
    "ds_read_b64_tr_b16 a[248:249], v215 offset:128\n\t" \
    "ds_read_b64_tr_b16 a[250:251], v215 offset:2432\n\t" \
    "ds_read_b64_tr_b16 a[252:253], v215 offset:18560\n\t" \
    "ds_read_b64_tr_b16 a[254:255], v215 offset:20864\n\t" \
    "s_waitcnt lgkmcnt(6)\n\t" \
    "ds_read_b64_tr_b16 v[240:241], v24 offset:0\n\t" \
    "ds_read_b64_tr_b16 v[242:243], v24 offset:2304\n\t" \
    "ds_read_b64_tr_b16 v[244:245], v24 offset:18432\n\t" \
    "ds_read_b64_tr_b16 v[246:247], v24 offset:20736\n\t" \
    "v_add_u32 v212, 0x12000, v212\n\t" \
    "v_add_u32 v213, 0x12000, v213\n\t" \
    "v_add_u32 v214, 0x12000, v214\n\t" \
    "v_add_u32 v215, 0x12000, v215\n\t" \
    "v_add_u32 v24, 0x12000, v24\n\t" \
    "s_waitcnt vmcnt(9)\n\t" \
    "vv_C2_%=:\n\t" \
    "v_cmp_ne_u32 s[54:55], v142, v144\n\t" \
    "v_sub_u32 v142, v142, v141\n\t" \
    "v_cmp_ne_u32 vcc, 1, v142\n\t" \
    "s_nop 1\n\t" \
    "s_or_b64 vcc, vcc, s[54:55]\n\t" \
    "s_cbranch_scc0 vv_Cok3_%=\n\t" \
    "s_cmp_eq_u32 s68, 0\n\t" \
    "s_cbranch_scc1 vv_F5_%=\n\t" \
    "s_sub_u32 s68, s68, 1\n\t" \
    "s_sleep 6\n\t" \
    "s_branch vv_Cag4_%=\n\t" \
    "vv_F5_%=:\n\t" \
    "v_mov_b32 v144, 5767424\n\t" \
    "v_mov_b32 v145, 4\n\t" \
    "global_store_dword v144, v145, s[36:37] sc1\n\t" \
    "s_waitcnt vmcnt(0)\n\t" \
    "s_branch vv_Cok3_%=\n\t" \
    "vv_Cag4_%=:\n\t" \
    "global_load_dwordx2 v[142:143], v140, s[36:37] sc1\n\t" \
    "global_load_dword v144, v140, s[36:37] offset:2048 sc1\n\t" \
    "s_waitcnt vmcnt(0)\n\t" \
    "s_branch vv_C2_%=\n\t" \
    "vv_Cok3_%=:\n\t" \
    "s_cmp_lg_u32 s66, 0\n\t" \
    "s_cbranch_scc1 vv_Ca6_%=\n\t" \
    "v_mov_b32 v140, 5836800\n\t" \
    "s_mov_b64 exec, 1\n\t" \
    "global_atomic_add v140, v142, s[36:37] sc1\n\t" \
    "s_mov_b64 exec, -1\n\t" \
    "vv_Ca6_%=:\n\t" \
    "v_mov_b32_dpp v140, v143 quad_perm:[1,0,3,2] row_mask:0xf bank_mask:0xf\n\t" \
    "v_cmp_lt_u32 s[44:45], v140, v143\n\t" \
    "s_nop 1\n\t" \
    "s_bitcmp1_b32 s44, 0\n\t" \
    "s_cselect_b32 s62, 8192, 0\n\t" \
    "s_bitcmp1_b32 s44, 2\n\t" \
    "s_cselect_b32 s63, 8192, 0\n\t" \
    "s_or_b32 s63, s63, 0x200000\n\t" \
    "v_min_u32 v140, v140, v143\n\t" \
    "v_cmp_lt_u32 vcc, 660, v140\n\t" \
    "s_nop 1\n\t" \
    "s_cmp_lg_u64 vcc, 0\n\t" \
    "s_cselect_b32 s69, 1, 0\n\t" \
    "s_mul_i32 s70, s69, 160\n\t" \
    "s_add_u32 s86, s86, s70\n\t" \
    "s_mul_i32 s70, s69, 536\n\t" \
    "s_add_u32 s87, s87, s70\n\t" \
    "s_mul_i32 s70, s69, 256\n\t" \
    "s_add_u32 s88, s88, s70\n\t" \
    "s_waitcnt vmcnt(0) lgkmcnt(0)\n\t" \
    "s_barrier\n\t" \
    "ds_read_b64_tr_b16 a[160:161], v212 offset:0\n\t" \
    "ds_read_b64_tr_b16 a[162:163], v212 offset:2304\n\t" \
    "ds_read_b64_tr_b16 a[164:165], v212 offset:18432\n\t" \
    "ds_read_b64_tr_b16 a[166:167], v212 offset:20736\n\t" \
    "s_waitcnt lgkmcnt(6)\n\t" \
    "ds_read_b64_tr_b16 a[192:193], v213 offset:0\n\t" \
    "ds_read_b64_tr_b16 a[194:195], v213 offset:2304\n\t" \
    "ds_read_b64_tr_b16 a[196:197], v213 offset:18432\n\t" \
    "ds_read_b64_tr_b16 a[198:199], v213 offset:20736\n\t" \
    "s_waitcnt lgkmcnt(6)\n\t" \
    "ds_read_b64_tr_b16 a[224:225], v212 offset:128\n\t" \
    "ds_read_b64_tr_b16 a[226:227], v212 offset:2432\n\t" \
    "ds_read_b64_tr_b16 a[228:229], v212 offset:18560\n\t" \
    "ds_read_b64_tr_b16 a[230:231], v212 offset:20864\n\t" \
    "s_waitcnt lgkmcnt(6)\n\t" \
    "ds_read_b64_tr_b16 v[224:225], v213 offset:128\n\t" \
    "ds_read_b64_tr_b16 v[226:227], v213 offset:2432\n\t" \
    "ds_read_b64_tr_b16 v[228:229], v213 offset:18560\n\t" \
    "ds_read_b64_tr_b16 v[230:231], v213 offset:20864\n\t" \
    "s_waitcnt lgkmcnt(6)\n\t" \
    "ds_read_b64_tr_b16 a[168:169], v214 offset:0\n\t" \
    "ds_read_b64_tr_b16 a[170:171], v214 offset:2304\n\t" \
    "ds_read_b64_tr_b16 a[172:173], v214 offset:18432\n\t" \
    "ds_read_b64_tr_b16 a[174:175], v214 offset:20736\n\t" \
    "s_waitcnt lgkmcnt(6)\n\t" \
    "ds_read_b64_tr_b16 a[200:201], v215 offset:0\n\t" \
    "ds_read_b64_tr_b16 a[202:203], v215 offset:2304\n\t" \
    "ds_read_b64_tr_b16 a[204:205], v215 offset:18432\n\t" \
    "ds_read_b64_tr_b16 a[206:207], v215 offset:20736\n\t" \
    "s_waitcnt lgkmcnt(6)\n\t" \
    "ds_read_b64_tr_b16 a[232:233], v214 offset:128\n\t" \
    "ds_read_b64_tr_b16 a[234:235], v214 offset:2432\n\t" \
    "ds_read_b64_tr_b16 a[236:237], v214 offset:18560\n\t" \
    "ds_read_b64_tr_b16 a[238:239], v214 offset:20864\n\t" \
    "s_waitcnt lgkmcnt(6)\n\t" \
    "ds_read_b64_tr_b16 v[232:233], v215 offset:128\n\t" \
    "ds_read_b64_tr_b16 v[234:235], v215 offset:2432\n\t" \
    "ds_read_b64_tr_b16 v[236:237], v215 offset:18560\n\t" \
    "ds_read_b64_tr_b16 v[238:239], v215 offset:20864\n\t" \
    "s_waitcnt lgkmcnt(6)\n\t" \
    "ds_read_b64_tr_b16 v[248:249], v24 offset:0\n\t" \
    "ds_read_b64_tr_b16 v[250:251], v24 offset:2304\n\t" \
    "ds_read_b64_tr_b16 v[252:253], v24 offset:18432\n\t" \
    "ds_read_b64_tr_b16 v[254:255], v24 offset:20736\n\t" \
    "v_add_u32 v212, 0x12000, v212\n\t" \
    "v_add_u32 v213, 0x12000, v213\n\t" \
    "v_add_u32 v214, 0x12000, v214\n\t" \
    "v_add_u32 v215, 0x12000, v215\n\t" \
    "v_add_u32 v24, 0x12000, v24\n\t" \
    "s_waitcnt lgkmcnt(0)\n\t" \
    "s_barrier\n\t" \
    "s_mov_b32 s79, s74\n\t" \
    "s_sub_u32 s80, s74, 32000\n\t" \
    "v_mov_b32 v72, 0\n\t" \
    "v_mov_b32 v73, 0\n\t" \
    "v_mov_b32 v74, 0\n\t" \
    "v_mov_b32 v75, 0\n\t" \
    "v_mov_b32 v76, 0\n\t" \
    "v_mov_b32 v77, 0\n\t" \
    "v_mov_b32 v78, 0\n\t" \
    "v_mov_b32 v79, 0\n\t" \
    "v_mov_b32 v108, 0\n\t" \
    "v_mov_b32 v109, 0\n\t" \
    "v_mov_b32 v110, 0\n\t" \
    "v_mov_b32 v111, 0\n\t" \
    "v_mov_b32 v112, 0\n\t" \
    "v_mov_b32 v113, 0\n\t" \
    "v_mov_b32 v114, 0\n\t" \
    "v_mov_b32 v115, 0\n\t" \
    "v_mov_b32 v116, 0\n\t" \
    "v_mov_b32 v117, 0\n\t" \
    "v_mov_b32 v118, 0\n\t" \
    "v_mov_b32 v119, 0\n\t" \
    "v_mov_b32 v120, 0\n\t" \
    "v_mov_b32 v121, 0\n\t" \
    "v_mov_b32 v122, 0\n\t" \
    "v_mov_b32 v123, 0\n\t" \
    "v_mov_b32 v124, 0\n\t" \
    "v_mov_b32 v125, 0\n\t" \
    "v_mov_b32 v126, 0\n\t" \
    "v_mov_b32 v127, 0\n\t" \
    "s_add_u32 s69, s65, 0\n\t" \
    "s_and_b32 s69, s69, 7\n\t" \
    "s_lshl_b32 s69, s69, 18\n\t" \
    "s_add_u32 s69, s69, s62\n\t" \
    "s_add_u32 s46, s36, s69\n\t" \
    "s_addc_u32 s47, s37, 0\n\t" \
    "s_add_u32 s70, s65, 0\n\t" \
    "s_mul_i32 s69, s70, 171\n\t" \
    "s_lshr_b32 s69, s69, 9\n\t" \
    "s_mul_i32 s70, s70, 589824\n\t" \
    "s_mul_i32 s71, s69, 327680\n\t" \
    "s_add_u32 s70, s70, s71\n\t" \
    "s_lshl1_add_u32 s69, s69, 4\n\t" \
    "s_bitcmp1_b64 s[44:45], s69\n\t" \
    "s_cselect_b32 s69, 8192, 0\n\t" \
    "s_add_u32 s69, s69, s70\n\t" \
    "s_add_u32 s69, s69, 123731968\n\t" \
    "s_add_u32 s38, s36, s69\n\t" \
    "s_addc_u32 s39, s37, 0\n\t" \
    "s_and_b32 s69, s69, 0xffe00000\n\t" \
    "s_add_u32 s69, s69, 1823744\n\t" \
    "v_mov_b32 v17, s69\n\t" \
    "global_load_dword v17, v17, s[36:37]\n\t" \
    "s_and_b32 s69, s65, 7\n\t" \
    "s_lshl_b32 s69, s69, 18\n\t" \
    "s_add_u32 s69, s69, s63\n\t" \
    "s_add_u32 s52, s36, s69\n\t" \
    "s_addc_u32 s53, s37, 0\n\t" \
    "v_mov_b32 v100, 0\n\t" \
    "v_mov_b32 v101, 0\n\t" \
    "v_mov_b32 v102, 0\n\t" \
    "v_mov_b32 v103, 0\n\t" \
    "s_sub_u32 s70, s79, s74\n\t" \
    "s_max_i32 s70, s70, 0\n\t" \
    "s_min_i32 s70, s70, 16000\n\t" \
    "s_lshl_b32 s70, s70, 1\n\t" \
    "s_mul_hi_u32 s70, s70, s77\n\t" \
    "s_add_u32 s70, s70, s73\n\t" \
    "s_cmp_eq_u32 s66, 1\n\t" \
    "s_cbranch_scc1 vv_loop10_%=\n\t" \
    "s_cmp_eq_u32 s66, 2\n\t" \
    "s_cbranch_scc1 vv_loop11_%=\n\t" \
    "s_cmp_eq_u32 s66, 3\n\t" \
    "s_cbranch_scc1 vv_loop12_%=\n\t" \
    ".p2align 6\n\t" \
    "s_nop 0\n\t" \
    "s_nop 0\n\t" \
    "s_nop 0\n\t" \
    "s_nop 0\n\t" \
    "s_nop 0\n\t" \
    "s_nop 0\n\t" \
    "vv_loop9_%=:\n\t" \
    "s_getpc_b64 s[30:31]\n\t" \
    "vv_Wp15_%=:\n\t" \
    "s_add_u32 s30, s30, vv_We16_%=-vv_Wp15_%=\n\t" \
    "s_addc_u32 s31, s31, 0\n\t" \
    "s_sub_u32 s70, s70, 96\n\t" \
    "vv_W14_%=:\n\t" \
    "s_memtime s[56:57]\n\t" \
    "s_waitcnt lgkmcnt(0)\n\t" \
    "s_sub_u32 s69, s70, s56\n\t" \
    "s_cmp_gt_i32 s69, 80\n\t" \
    "s_cbranch_scc1 vv_W14_%=\n\t" \
    "s_max_i32 s69, s69, 0\n\t" \
    "s_and_b32 s69, s69, -4\n\t" \
    "s_sub_u32 s30, s30, s69\n\t" \
    "s_subb_u32 s31, s31, 0\n\t" \
    "s_setpc_b64 s[30:31]\n\t" \
    "s_nop 0\n\t" \
    "s_nop 0\n\t" \
    "s_nop 0\n\t" \
    "s_nop 0\n\t" \
    "s_nop 0\n\t" \
    "s_nop 0\n\t" \
    "s_nop 0\n\t" \
    "s_nop 0\n\t" \
    "s_nop 0\n\t" \
    "s_nop 0\n\t" \
    "s_nop 0\n\t" \
    "s_nop 0\n\t" \
    "s_nop 0\n\t" \
    "s_nop 0\n\t" \
    "s_nop 0\n\t" \
    "s_nop 0\n\t" \
    "s_nop 0\n\t" \
    "s_nop 0\n\t" \
    "s_nop 0\n\t" \
    "s_nop 0\n\t" \
    "vv_We16_%=:\n\t" \
    "vv_G17_%=:\n\t" \
    "global_load_dwordx4 v[36:39], v1, s[46:47] sc1\n\t" \
    "global_load_dword v141, v2, s[46:47] sc1\n\t" \
    "global_load_dwordx4 v[40:43], v23, s[46:47] sc1\n\t" \
    "s_waitcnt vmcnt(2)\n\t" \
    "ds_write_b128 v3, v[36:39]\n\t" \
    "v_smfmac_f32_16x16x64_bf16 v[72:75], v[36:39], a[0:7], v10\n\t" \
    "s_waitcnt vmcnt(1)\n\t" \
    "ds_write_b32 v22, v141 offset:19456\n\t" \
    "s_waitcnt vmcnt(0)\n\t" \
    "ds_write_b128 v3, v[40:43] offset:1024\n\t" \
    "v_max3_u32 v140, v36, v40, v141\n\t" \
    "v_cmp_eq_u32 vcc, -1, v140\n\t" \
    "s_cbranch_vccnz vv_Gbad18_%=\n\t" \
    "vv_Gc19_%=:\n\t" \
    "s_waitcnt lgkmcnt(0)\n\t" \
    "s_barrier\n\t" \
    "ds_read_b128 v[48:51], v4\n\t" \
    "ds_read_b128 v[52:55], v5\n\t" \
    "v_smfmac_f32_16x16x64_bf16 v[76:79], v[36:39], a[72:79], v10\n\t" \
    "ds_read_b128 v[56:59], v6\n\t" \
    "ds_read_b128 v[60:63], v7\n\t" \
    "v_smfmac_f32_16x16x64_bf16 v[72:75], v[40:43], a[8:15], v10\n\t" \
    "ds_read_b128 v[64:67], v8\n\t" \
    "v_smfmac_f32_16x16x64_bf16 v[76:79], v[40:43], a[80:87], v10\n\t" \
    "ds_read_b128 v[68:71], v9\n\t" \
    "s_waitcnt lgkmcnt(5)\n\t" \
    "v_smfmac_f32_16x16x64_bf16 v[72:75], v[48:51], a[16:23], v10\n\t" \
    "ds_read_b128 v[44:47], v0 offset:19456\n\t" \
    "s_waitcnt lgkmcnt(5)\n\t" \
    "v_smfmac_f32_16x16x64_bf16 v[72:75], v[52:55], a[24:31], v10\n\t" \
    "s_waitcnt lgkmcnt(4)\n\t" \
    "v_smfmac_f32_16x16x64_bf16 v[72:75], v[56:59], a[32:39], v10\n\t" \
    "s_waitcnt lgkmcnt(3)\n\t" \
    "v_smfmac_f32_16x16x64_bf16 v[72:75], v[60:63], a[40:47], v10\n\t" \
    "s_waitcnt lgkmcnt(2)\n\t" \
    "v_smfmac_f32_16x16x64_bf16 v[72:75], v[64:67], a[48:55], v10\n\t" \
    "s_waitcnt lgkmcnt(1)\n\t" \
    "v_smfmac_f32_16x16x64_bf16 v[72:75], v[68:71], a[56:63], v10\n\t" \
    "s_waitcnt lgkmcnt(0)\n\t" \
    "v_smfmac_f32_16x16x64_bf16 v[72:75], v[44:47], a[64:71], v10\n\t" \
    "s_nop 2\n\t" \
    "v_smfmac_f32_16x16x64_bf16 v[76:79], v[48:51], a[88:95], v10\n\t" \
    "s_nop 2\n\t" \
    "v_smfmac_f32_16x16x64_bf16 v[76:79], v[52:55], a[96:103], v10\n\t" \
    "v_add_f32 v72, v72, v74\n\t" \
    "v_smfmac_f32_16x16x64_bf16 v[76:79], v[56:59], a[104:111], v10\n\t" \
    "v_add_f32 v73, v73, v75\n\t" \
    "v_smfmac_f32_16x16x64_bf16 v[76:79], v[60:63], a[112:119], v10\n\t" \
    "v_cvt_pk_bf16_f32 v80, v72, v73\n\t" \
    "v_smfmac_f32_16x16x64_bf16 v[76:79], v[64:67], a[120:127], v10\n\t" \
    "ds_write_b32 v12, v80 offset:0\n\t" \
    "v_smfmac_f32_16x16x64_bf16 v[76:79], v[68:71], a[128:135], v10\n\t" \
    "v_smfmac_f32_16x16x64_bf16 v[76:79], v[44:47], a[136:143], v10\n\t" \
    "s_nop 6\n\t" \
    "v_cmp_u_f32 vcc, v72, v73\n\t" \
    "v_pk_add_f32 v[76:77], v[76:77], v[78:79]\n\t" \
    "v_cvt_pk_bf16_f32 v81, v76, v77\n\t" \
    "ds_write_b32 v12, v81 offset:256\n\t" \
    "s_cbranch_vccnz vv_Gslow20_%=\n\t" \
    "vv_Gback21_%=:\n\t" \
    "s_waitcnt lgkmcnt(0)\n\t" \
    "s_barrier\n\t" \
    "ds_read_b64_tr_b16 v[84:85], v13 offset:0\n\t" \
    "v_smfmac_f32_16x16x64_bf16 v[100:103], v[36:39], a[0:7], v10\n\t" \
    "ds_read_b64_tr_b16 v[86:87], v13 offset:64\n\t" \
    "ds_read_b64_tr_b16 v[92:93], v13 offset:1152\n\t" \
    "v_smfmac_f32_16x16x64_bf16 v[100:103], v[36:39], a[0:7], v10\n\t" \
    "ds_read_b64_tr_b16 v[94:95], v13 offset:1216\n\t" \
    "ds_read_b64_tr_b16 v[88:89], v25 offset:0\n\t" \
    "v_smfmac_f32_16x16x64_bf16 v[100:103], v[36:39], a[0:7], v10\n\t" \
    "ds_read_b64_tr_b16 v[90:91], v25 offset:64\n\t" \
    "s_waitcnt lgkmcnt(4)\n\t" \
    "v_smfmac_f32_16x16x64_bf16 v[124:127], v[84:87], v[240:247], v19\n\t" \
    "ds_read_b64_tr_b16 v[96:97], v25 offset:1152\n\t" \
    "ds_read_b64_tr_b16 v[98:99], v25 offset:1216\n\t" \
    "s_waitcnt lgkmcnt(4)\n\t" \
    "v_smfmac_f32_16x16x64_bf16 v[124:127], v[92:95], v[248:255], v19\n\t" \
    "s_nop 2\n\t" \
    "v_smfmac_f32_16x16x64_bf16 v[108:111], v[84:87], a[144:151], v19\n\t" \
    "s_nop 2\n\t" \
    "v_smfmac_f32_16x16x64_bf16 v[108:111], v[92:95], a[160:167], v19\n\t" \
    "s_nop 1\n\t" \
    "v_permlane32_swap_b32 v124, v126\n\t" \
    "v_smfmac_f32_16x16x64_bf16 v[112:115], v[84:87], a[176:183], v19\n\t" \
    "s_nop 1\n\t" \
    "v_permlane32_swap_b32 v125, v127\n\t" \
    "v_smfmac_f32_16x16x64_bf16 v[112:115], v[92:95], a[192:199], v19\n\t" \
    "s_nop 0\n\t" \
    "v_add_f32 v124, v124, v126\n\t" \
    "s_waitcnt lgkmcnt(2)\n\t" \
    "v_smfmac_f32_16x16x64_bf16 v[108:111], v[88:91], a[152:159], v19\n\t" \
    "s_nop 1\n\t" \
    "v_add_f32 v125, v125, v127\n\t" \
    "v_smfmac_f32_16x16x64_bf16 v[112:115], v[88:91], a[184:191], v19\n\t" \
    "s_nop 0\n\t" \
    "v_cvt_pk_bf16_f32 v136, v124, v125\n\t" \
    "s_waitcnt lgkmcnt(0)\n\t" \
    "v_smfmac_f32_16x16x64_bf16 v[108:111], v[96:99], a[168:175], v19\n\t" \
    "s_nop 1\n\t" \
    "v_cvt_pk_bf16_f32 v137, v124, v125\n\t" \
    "v_smfmac_f32_16x16x64_bf16 v[112:115], v[96:99], a[200:207], v19\n\t" \
    "s_nop 1\n\t" \
    "v_permlane32_swap_b32 v136, v137\n\t" \
    "v_smfmac_f32_16x16x64_bf16 v[116:119], v[84:87], a[208:215], v19\n\t" \
    "s_mov_b32 exec_hi, 0\n\t" \
    "global_store_dwordx2 v16, v[136:137], s[38:39] sc1\n\t" \
    "s_mov_b32 exec_hi, -1\n\t" \
    "v_cvt_pk_bf16_f32 v128, v108, v109\n\t" \
    "v_smfmac_f32_16x16x64_bf16 v[116:119], v[88:91], a[216:223], v19\n\t" \
    "s_nop 1\n\t" \
    "v_cvt_pk_bf16_f32 v129, v110, v111\n\t" \
    "v_smfmac_f32_16x16x64_bf16 v[116:119], v[92:95], a[224:231], v19\n\t" \
    "s_nop 1\n\t" \
    "v_cvt_pk_bf16_f32 v130, v112, v113\n\t" \
    "v_smfmac_f32_16x16x64_bf16 v[116:119], v[96:99], a[232:239], v19\n\t" \
    "s_nop 1\n\t" \
    "v_cvt_pk_bf16_f32 v131, v114, v115\n\t" \
    "v_smfmac_f32_16x16x64_bf16 v[120:123], v[84:87], a[240:247], v19\n\t" \
    "s_nop 1\n\t" \
    "v_permlane16_swap_b32 v128, v130\n\t" \
    "v_smfmac_f32_16x16x64_bf16 v[120:123], v[88:91], a[248:255], v19\n\t" \
    "s_nop 1\n\t" \
    "v_permlane16_swap_b32 v129, v131\n\t" \
    "v_smfmac_f32_16x16x64_bf16 v[120:123], v[92:95], v[224:231], v19\n\t" \
    "s_nop 0\n\t" \
    "global_store_dwordx4 v14, v[128:131], s[38:39] sc1\n\t" \
    "v_cvt_pk_bf16_f32 v132, v116, v117\n\t" \
    "v_smfmac_f32_16x16x64_bf16 v[120:123], v[96:99], v[232:239], v19\n\t" \
    "s_nop 1\n\t" \
    "v_cvt_pk_bf16_f32 v133, v118, v119\n\t" \
    "s_nop 4\n\t" \
    "v_cvt_pk_bf16_f32 v134, v120, v121\n\t" \
    "v_cvt_pk_bf16_f32 v135, v122, v123\n\t" \
    "s_nop 0\n\t" \
    "v_permlane16_swap_b32 v132, v134\n\t" \
    "v_permlane16_swap_b32 v133, v135\n\t" \
    "global_store_dwordx4 v15, v[132:135], s[38:39] sc1\n\t" \
    "s_cmp_eq_u32 s65, 63\n\t" \
    "s_cbranch_scc1 vv_end13_%=\n\t" \
    "s_cmp_lg_u32 s66, 0\n\t" \
    "s_cbranch_scc1 vv_SF22_%=\n\t" \
    "s_lshl_b32 s69, s67, 7\n\t" \
    "s_add_u32 s69, s69, 5804032\n\t" \
    "s_add_u32 s70, s65, -1\n\t" \
    "v_mov_b32 v142, s69\n\t" \
    "v_mov_b32 v143, s70\n\t" \
    "s_mov_b64 exec, 1\n\t" \
    "global_store_dword v142, v143, s[36:37] sc1\n\t" \
    "s_mov_b64 exec, -1\n\t" \
    "vv_SF22_%=:\n\t" \
    "s_add_u32 s65, s65, 1\n\t" \
    "v_mov_b32 v72, 0\n\t" \
    "v_mov_b32 v73, 0\n\t" \
    "v_mov_b32 v74, 0\n\t" \
    "v_mov_b32 v75, 0\n\t" \
    "v_mov_b32 v76, 0\n\t" \
    "v_mov_b32 v77, 0\n\t" \
    "v_mov_b32 v78, 0\n\t" \
    "v_mov_b32 v79, 0\n\t" \
    "v_mov_b32 v108, 0\n\t" \
    "v_mov_b32 v109, 0\n\t" \
    "v_mov_b32 v110, 0\n\t" \
    "v_mov_b32 v111, 0\n\t" \
    "v_mov_b32 v112, 0\n\t" \
    "v_mov_b32 v113, 0\n\t" \
    "v_mov_b32 v114, 0\n\t" \
    "v_mov_b32 v115, 0\n\t" \
    "v_mov_b32 v116, 0\n\t" \
    "v_mov_b32 v117, 0\n\t" \
    "v_mov_b32 v118, 0\n\t" \
    "v_mov_b32 v119, 0\n\t" \
    "v_mov_b32 v120, 0\n\t" \
    "v_mov_b32 v121, 0\n\t" \
    "v_mov_b32 v122, 0\n\t" \
    "v_mov_b32 v123, 0\n\t" \
    "v_mov_b32 v124, 0\n\t" \
    "v_mov_b32 v125, 0\n\t" \
    "v_mov_b32 v126, 0\n\t" \
    "v_mov_b32 v127, 0\n\t" \
    "s_add_u32 s69, s65, 0\n\t" \
    "s_and_b32 s69, s69, 7\n\t" \
    "s_lshl_b32 s69, s69, 18\n\t" \
    "s_add_u32 s69, s69, s62\n\t" \
    "s_add_u32 s46, s36, s69\n\t" \
    "s_addc_u32 s47, s37, 0\n\t" \
    "s_add_u32 s70, s65, 0\n\t" \
    "s_mul_i32 s69, s70, 171\n\t" \
    "s_lshr_b32 s69, s69, 9\n\t" \
    "s_mul_i32 s70, s70, 589824\n\t" \
    "s_mul_i32 s71, s69, 327680\n\t" \
    "s_add_u32 s70, s70, s71\n\t" \
    "s_lshl1_add_u32 s69, s69, 4\n\t" \
    "s_bitcmp1_b64 s[44:45], s69\n\t" \
    "s_cselect_b32 s69, 8192, 0\n\t" \
    "s_add_u32 s69, s69, s70\n\t" \
    "s_add_u32 s69, s69, 123731968\n\t" \
    "s_add_u32 s38, s36, s69\n\t" \
    "s_addc_u32 s39, s37, 0\n\t" \
    "s_and_b32 s69, s69, 0xffe00000\n\t" \
    "s_add_u32 s69, s69, 1823744\n\t" \
    "v_mov_b32 v17, s69\n\t" \
    "global_load_dword v17, v17, s[36:37]\n\t" \
    "s_barrier\n\t" \
    "ds_read_b32 v160, v11 offset:20480\n\t" \
    "s_waitcnt lgkmcnt(0)\n\t" \
    "v_readfirstlane_b32 s70, v160\n\t" \
    "s_branch vv_loop9_%=\n\t" \
    "vv_Gbad18_%=:\n\t" \
    "s_waitcnt lgkmcnt(0)\n\t" \
    "s_nop 7\n\t" \
    "v_mov_b32 v72, 0\n\t" \
    "v_mov_b32 v73, 0\n\t" \
    "v_mov_b32 v74, 0\n\t" \
    "v_mov_b32 v75, 0\n\t" \
    "v_mov_b32 v76, 0\n\t" \
    "v_mov_b32 v77, 0\n\t" \
    "v_mov_b32 v78, 0\n\t" \
    "v_mov_b32 v79, 0\n\t" \
    "s_cmp_eq_u32 s68, 0\n\t" \
    "s_cbranch_scc1 vv_F23_%=\n\t" \
    "s_sub_u32 s68, s68, 1\n\t" \
    "s_branch vv_G17_%=\n\t" \
    "vv_F23_%=:\n\t" \
    "v_mov_b32 v140, 5767424\n\t" \
    "v_mov_b32 v141, 1\n\t" \
    "global_store_dword v140, v141, s[36:37] sc1\n\t" \
    "s_waitcnt vmcnt(0)\n\t" \
    "s_branch vv_Gc19_%=\n\t" \
    "vv_Gslow20_%=:\n\t" \
    "v_max_u32 v140, v36, v37\n\t" \
    "v_max3_u32 v140, v140, v38, v39\n\t" \
    "v_max3_u32 v140, v140, v40, v41\n\t" \
    "v_max3_u32 v140, v140, v42, v43\n\t" \
    "v_max3_u32 v140, v140, v44, v45\n\t" \
    "v_max3_u32 v140, v140, v46, v47\n\t" \
    "v_max3_u32 v140, v140, v48, v49\n\t" \
    "v_max3_u32 v140, v140, v50, v51\n\t" \
    "v_max3_u32 v140, v140, v52, v53\n\t" \
    "v_max3_u32 v140, v140, v54, v55\n\t" \
    "v_max3_u32 v140, v140, v56, v57\n\t" \
    "v_max3_u32 v140, v140, v58, v59\n\t" \
    "v_max3_u32 v140, v140, v60, v61\n\t" \
    "v_max3_u32 v140, v140, v62, v63\n\t" \
    "v_max3_u32 v140, v140, v64, v65\n\t" \
    "v_max3_u32 v140, v140, v66, v67\n\t" \
    "v_max3_u32 v140, v140, v68, v69\n\t" \
    "v_max3_u32 v140, v140, v70, v71\n\t" \
    "v_cmp_eq_u32 vcc, -1, v140\n\t" \
    "s_cbranch_vccz vv_Gback21_%=\n\t" \
    "s_waitcnt lgkmcnt(0)\n\t" \
    "v_sub_u32 v140, v1, v3\n\t" \
    "s_nop 0\n\t" \
    "v_readfirstlane_b32 s69, v140\n\t" \
    "v_add_u32 v143, 20480, v0\n\t" \
    "s_nop 7\n\t" \
    "s_add_u32 s30, s46, s69\n\t" \
    "s_addc_u32 s31, s47, 0\n\t" \
    "vv_Gag24_%=:\n\t" \
    "global_load_dwordx4 v[36:39], v1, s[46:47] sc1\n\t" \
    "global_load_dwordx4 v[40:43], v23, s[46:47] sc1\n\t" \
    "global_load_dwordx4 v[44:47], v143, s[30:31] sc1\n\t" \
    "global_load_dwordx4 v[48:51], v4, s[30:31] sc1\n\t" \
    "global_load_dwordx4 v[52:55], v5, s[30:31] sc1\n\t" \
    "global_load_dwordx4 v[56:59], v6, s[30:31] sc1\n\t" \
    "global_load_dwordx4 v[60:63], v7, s[30:31] sc1\n\t" \
    "global_load_dwordx4 v[64:67], v8, s[30:31] sc1\n\t" \
    "global_load_dwordx4 v[68:71], v9, s[30:31] sc1\n\t" \
    "s_waitcnt vmcnt(0)\n\t" \
    "v_max_u32 v140, v36, v37\n\t" \
    "v_max3_u32 v140, v140, v38, v39\n\t" \
    "v_max3_u32 v140, v140, v40, v41\n\t" \
    "v_max3_u32 v140, v140, v42, v43\n\t" \
    "v_max3_u32 v140, v140, v44, v45\n\t" \
    "v_max3_u32 v140, v140, v46, v47\n\t" \
    "v_max3_u32 v140, v140, v48, v49\n\t" \
    "v_max3_u32 v140, v140, v50, v51\n\t" \
    "v_max3_u32 v140, v140, v52, v53\n\t" \
    "v_max3_u32 v140, v140, v54, v55\n\t" \
    "v_max3_u32 v140, v140, v56, v57\n\t" \
    "v_max3_u32 v140, v140, v58, v59\n\t" \
    "v_max3_u32 v140, v140, v60, v61\n\t" \
    "v_max3_u32 v140, v140, v62, v63\n\t" \
    "v_max3_u32 v140, v140, v64, v65\n\t" \
    "v_max3_u32 v140, v140, v66, v67\n\t" \
    "v_max3_u32 v140, v140, v68, v69\n\t" \
    "v_max3_u32 v140, v140, v70, v71\n\t" \
    "v_cmp_eq_u32 vcc, -1, v140\n\t" \
    "s_cbranch_vccz vv_Gredo25_%=\n\t" \
    "s_cmp_eq_u32 s68, 0\n\t" \
    "s_cbranch_scc1 vv_F26_%=\n\t" \
    "s_sub_u32 s68, s68, 1\n\t" \
    "s_sleep 1\n\t" \
    "s_branch vv_Gag24_%=\n\t" \
    "vv_F26_%=:\n\t" \
    "v_mov_b32 v140, 5767424\n\t" \
    "v_mov_b32 v141, 2\n\t" \
    "global_store_dword v140, v141, s[36:37] sc1\n\t" \
    "s_waitcnt vmcnt(0)\n\t" \
    "s_branch vv_Gredo25_%=\n\t" \
    "vv_Gredo25_%=:\n\t" \
    "v_mov_b32 v72, 0\n\t" \
    "v_mov_b32 v73, 0\n\t" \
    "v_mov_b32 v74, 0\n\t" \
    "v_mov_b32 v75, 0\n\t" \
    "v_mov_b32 v76, 0\n\t" \
    "v_mov_b32 v77, 0\n\t" \
    "v_mov_b32 v78, 0\n\t" \
    "v_mov_b32 v79, 0\n\t" \
    "v_smfmac_f32_16x16x64_bf16 v[72:75], v[36:39], a[0:7], v10\n\t" \
    "v_smfmac_f32_16x16x64_bf16 v[72:75], v[40:43], a[8:15], v10\n\t" \
    "v_smfmac_f32_16x16x64_bf16 v[72:75], v[44:47], a[64:71], v10\n\t" \
    "v_smfmac_f32_16x16x64_bf16 v[72:75], v[48:51], a[16:23], v10\n\t" \
    "v_smfmac_f32_16x16x64_bf16 v[72:75], v[52:55], a[24:31], v10\n\t" \
    "v_smfmac_f32_16x16x64_bf16 v[72:75], v[56:59], a[32:39], v10\n\t" \
    "v_smfmac_f32_16x16x64_bf16 v[72:75], v[60:63], a[40:47], v10\n\t" \
    "v_smfmac_f32_16x16x64_bf16 v[72:75], v[64:67], a[48:55], v10\n\t" \
    "v_smfmac_f32_16x16x64_bf16 v[72:75], v[68:71], a[56:63], v10\n\t" \
    "v_smfmac_f32_16x16x64_bf16 v[76:79], v[36:39], a[72:79], v10\n\t" \
    "v_smfmac_f32_16x16x64_bf16 v[76:79], v[40:43], a[80:87], v10\n\t" \
    "v_smfmac_f32_16x16x64_bf16 v[76:79], v[44:47], a[136:143], v10\n\t" \
    "v_smfmac_f32_16x16x64_bf16 v[76:79], v[48:51], a[88:95], v10\n\t" \
    "v_smfmac_f32_16x16x64_bf16 v[76:79], v[52:55], a[96:103], v10\n\t" \
    "v_smfmac_f32_16x16x64_bf16 v[76:79], v[56:59], a[104:111], v10\n\t" \
    "v_smfmac_f32_16x16x64_bf16 v[76:79], v[60:63], a[112:119], v10\n\t" \
    "v_smfmac_f32_16x16x64_bf16 v[76:79], v[64:67], a[120:127], v10\n\t" \
    "v_smfmac_f32_16x16x64_bf16 v[76:79], v[68:71], a[128:135], v10\n\t" \
    "v_add_f32 v72, v72, v74\n\t" \
    "v_add_f32 v73, v73, v75\n\t" \
    "v_cvt_pk_bf16_f32 v80, v72, v73\n\t" \
    "ds_write_b32 v12, v80 offset:0\n\t" \
    "s_nop 3\n\t" \
    "v_pk_add_f32 v[76:77], v[76:77], v[78:79]\n\t" \
    "v_cvt_pk_bf16_f32 v81, v76, v77\n\t" \
    "ds_write_b32 v12, v81 offset:256\n\t" \
    "s_branch vv_Gback21_%=\n\t" \
    "vv_end13_%=:\n\t" \
    "s_endpgm\n\t" \
    ".p2align 6\n\t" \
    "s_nop 0\n\t" \
    "s_nop 0\n\t" \
    "s_nop 0\n\t" \
    "s_nop 0\n\t" \
    "s_nop 0\n\t" \
    "s_nop 0\n\t" \
    "vv_loop10_%=:\n\t" \
    "s_getpc_b64 s[30:31]\n\t" \
    "vv_Wp29_%=:\n\t" \
    "s_add_u32 s30, s30, vv_We30_%=-vv_Wp29_%=\n\t" \
    "s_addc_u32 s31, s31, 0\n\t" \
    "s_sub_u32 s70, s70, 96\n\t" \
    "vv_W28_%=:\n\t" \
    "s_memtime s[56:57]\n\t" \
    "s_waitcnt lgkmcnt(0)\n\t" \
    "s_sub_u32 s69, s70, s56\n\t" \
    "s_cmp_gt_i32 s69, 80\n\t" \
    "s_cbranch_scc1 vv_W28_%=\n\t" \
    "s_max_i32 s69, s69, 0\n\t" \
    "s_and_b32 s69, s69, -4\n\t" \
    "s_sub_u32 s30, s30, s69\n\t" \
    "s_subb_u32 s31, s31, 0\n\t" \
    "s_setpc_b64 s[30:31]\n\t" \
    "s_nop 0\n\t" \
    "s_nop 0\n\t" \
    "s_nop 0\n\t" \
    "s_nop 0\n\t" \
    "s_nop 0\n\t" \
    "s_nop 0\n\t" \
    "s_nop 0\n\t" \
    "s_nop 0\n\t" \
    "s_nop 0\n\t" \
    "s_nop 0\n\t" \
    "s_nop 0\n\t" \
    "s_nop 0\n\t" \
    "s_nop 0\n\t" \
    "s_nop 0\n\t" \
    "s_nop 0\n\t" \
    "s_nop 0\n\t" \
    "s_nop 0\n\t" \
    "s_nop 0\n\t" \
    "s_nop 0\n\t" \
    "s_nop 0\n\t" \
    "vv_We30_%=:\n\t" \
    "vv_G31_%=:\n\t" \
    "global_load_dwordx4 v[36:39], v1, s[46:47] sc1\n\t" \
    "global_load_dword v141, v2, s[46:47] sc1\n\t" \
    "global_load_dwordx4 v[40:43], v23, s[46:47] sc1\n\t" \
    "s_waitcnt vmcnt(2)\n\t" \
    "ds_write_b128 v3, v[36:39]\n\t" \
    "v_smfmac_f32_16x16x64_bf16 v[72:75], v[36:39], a[16:23], v10\n\t" \
    "s_waitcnt vmcnt(1)\n\t" \
    "ds_write_b32 v22, v141 offset:19712\n\t" \
    "s_waitcnt vmcnt(0)\n\t" \
    "ds_write_b128 v3, v[40:43] offset:1024\n\t" \
    "v_max3_u32 v140, v36, v40, v141\n\t" \
    "v_cmp_eq_u32 vcc, -1, v140\n\t" \
    "s_cbranch_vccnz vv_Gbad32_%=\n\t" \
    "vv_Gc33_%=:\n\t" \
    "s_waitcnt lgkmcnt(0)\n\t" \
    "s_barrier\n\t" \
    "ds_read_b128 v[48:51], v4\n\t" \
    "ds_read_b128 v[52:55], v5\n\t" \
    "v_smfmac_f32_16x16x64_bf16 v[76:79], v[36:39], a[88:95], v10\n\t" \
    "ds_read_b128 v[56:59], v6\n\t" \
    "ds_read_b128 v[60:63], v7\n\t" \
    "v_smfmac_f32_16x16x64_bf16 v[72:75], v[40:43], a[24:31], v10\n\t" \
    "ds_read_b128 v[64:67], v8\n\t" \
    "v_smfmac_f32_16x16x64_bf16 v[76:79], v[40:43], a[96:103], v10\n\t" \
    "ds_read_b128 v[68:71], v9\n\t" \
    "s_waitcnt lgkmcnt(5)\n\t" \
    "v_smfmac_f32_16x16x64_bf16 v[72:75], v[48:51], a[32:39], v10\n\t" \
    "ds_read_b128 v[44:47], v0 offset:19456\n\t" \
    "s_waitcnt lgkmcnt(5)\n\t" \
    "v_smfmac_f32_16x16x64_bf16 v[72:75], v[52:55], a[40:47], v10\n\t" \
    "s_waitcnt lgkmcnt(4)\n\t" \
    "v_smfmac_f32_16x16x64_bf16 v[72:75], v[56:59], a[48:55], v10\n\t" \
    "s_waitcnt lgkmcnt(3)\n\t" \
    "v_smfmac_f32_16x16x64_bf16 v[72:75], v[60:63], a[56:63], v10\n\t" \
    "s_waitcnt lgkmcnt(2)\n\t" \
    "v_smfmac_f32_16x16x64_bf16 v[72:75], v[64:67], a[0:7], v10\n\t" \
    "s_waitcnt lgkmcnt(1)\n\t" \
    "v_smfmac_f32_16x16x64_bf16 v[72:75], v[68:71], a[8:15], v10\n\t" \
    "s_waitcnt lgkmcnt(0)\n\t" \
    "v_smfmac_f32_16x16x64_bf16 v[72:75], v[44:47], a[64:71], v10\n\t" \
    "s_nop 2\n\t" \
    "v_smfmac_f32_16x16x64_bf16 v[76:79], v[48:51], a[104:111], v10\n\t" \
    "s_nop 2\n\t" \
    "v_smfmac_f32_16x16x64_bf16 v[76:79], v[52:55], a[112:119], v10\n\t" \
    "v_add_f32 v72, v72, v74\n\t" \
    "v_smfmac_f32_16x16x64_bf16 v[76:79], v[56:59], a[120:127], v10\n\t" \
    "v_add_f32 v73, v73, v75\n\t" \
    "v_smfmac_f32_16x16x64_bf16 v[76:79], v[60:63], a[128:135], v10\n\t" \
    "v_cvt_pk_bf16_f32 v80, v72, v73\n\t" \
    "v_smfmac_f32_16x16x64_bf16 v[76:79], v[64:67], a[72:79], v10\n\t" \
    "ds_write_b32 v12, v80 offset:0\n\t" \
    "v_smfmac_f32_16x16x64_bf16 v[76:79], v[68:71], a[80:87], v10\n\t" \
    "v_smfmac_f32_16x16x64_bf16 v[76:79], v[44:47], a[136:143], v10\n\t" \
    "s_nop 6\n\t" \
    "v_cmp_u_f32 vcc, v72, v73\n\t" \
    "v_pk_add_f32 v[76:77], v[76:77], v[78:79]\n\t" \
    "v_cvt_pk_bf16_f32 v81, v76, v77\n\t" \
    "ds_write_b32 v12, v81 offset:256\n\t" \
    "s_cbranch_vccnz vv_Gslow34_%=\n\t" \
    "vv_Gback35_%=:\n\t" \
    "s_waitcnt lgkmcnt(0)\n\t" \
    "s_barrier\n\t" \
    "ds_read_b64_tr_b16 v[84:85], v13 offset:0\n\t" \
    "v_smfmac_f32_16x16x64_bf16 v[100:103], v[36:39], a[0:7], v10\n\t" \
    "ds_read_b64_tr_b16 v[86:87], v13 offset:64\n\t" \
    "ds_read_b64_tr_b16 v[92:93], v13 offset:1152\n\t" \
    "v_smfmac_f32_16x16x64_bf16 v[100:103], v[36:39], a[0:7], v10\n\t" \
    "ds_read_b64_tr_b16 v[94:95], v13 offset:1216\n\t" \
    "ds_read_b64_tr_b16 v[88:89], v25 offset:0\n\t" \
    "v_smfmac_f32_16x16x64_bf16 v[100:103], v[36:39], a[0:7], v10\n\t" \
    "ds_read_b64_tr_b16 v[90:91], v25 offset:64\n\t" \
    "s_waitcnt lgkmcnt(4)\n\t" \
    "v_smfmac_f32_16x16x64_bf16 v[124:127], v[84:87], v[240:247], v19\n\t" \
    "ds_read_b64_tr_b16 v[96:97], v25 offset:1152\n\t" \
    "ds_read_b64_tr_b16 v[98:99], v25 offset:1216\n\t" \
    "s_waitcnt lgkmcnt(4)\n\t" \
    "v_smfmac_f32_16x16x64_bf16 v[124:127], v[92:95], v[248:255], v19\n\t" \
    "s_nop 2\n\t" \
    "v_smfmac_f32_16x16x64_bf16 v[108:111], v[84:87], a[144:151], v19\n\t" \
    "s_nop 2\n\t" \
    "v_smfmac_f32_16x16x64_bf16 v[108:111], v[92:95], a[160:167], v19\n\t" \
    "s_nop 1\n\t" \
    "v_permlane32_swap_b32 v124, v126\n\t" \
    "v_smfmac_f32_16x16x64_bf16 v[112:115], v[84:87], a[176:183], v19\n\t" \
    "s_nop 1\n\t" \
    "v_permlane32_swap_b32 v125, v127\n\t" \
    "v_smfmac_f32_16x16x64_bf16 v[112:115], v[92:95], a[192:199], v19\n\t" \
    "s_nop 0\n\t" \
    "v_add_f32 v124, v124, v126\n\t" \
    "s_waitcnt lgkmcnt(2)\n\t" \
    "v_smfmac_f32_16x16x64_bf16 v[108:111], v[88:91], a[152:159], v19\n\t" \
    "s_nop 1\n\t" \
    "v_add_f32 v125, v125, v127\n\t" \
    "v_smfmac_f32_16x16x64_bf16 v[112:115], v[88:91], a[184:191], v19\n\t" \
    "s_nop 0\n\t" \
    "v_cvt_pk_bf16_f32 v136, v124, v125\n\t" \
    "s_waitcnt lgkmcnt(0)\n\t" \
    "v_smfmac_f32_16x16x64_bf16 v[108:111], v[96:99], a[168:175], v19\n\t" \
    "s_nop 1\n\t" \
    "v_cvt_pk_bf16_f32 v137, v124, v125\n\t" \
    "v_smfmac_f32_16x16x64_bf16 v[112:115], v[96:99], a[200:207], v19\n\t" \
    "s_nop 1\n\t" \
    "v_permlane32_swap_b32 v136, v137\n\t" \
    "v_smfmac_f32_16x16x64_bf16 v[116:119], v[84:87], a[208:215], v19\n\t" \
    "s_mov_b32 exec_hi, 0\n\t" \
    "global_store_dwordx2 v16, v[136:137], s[38:39] sc1\n\t" \
    "s_mov_b32 exec_hi, -1\n\t" \
    "v_cvt_pk_bf16_f32 v128, v108, v109\n\t" \
    "v_smfmac_f32_16x16x64_bf16 v[116:119], v[88:91], a[216:223], v19\n\t" \
    "s_nop 1\n\t" \
    "v_cvt_pk_bf16_f32 v129, v110, v111\n\t" \
    "v_smfmac_f32_16x16x64_bf16 v[116:119], v[92:95], a[224:231], v19\n\t" \
    "s_nop 1\n\t" \
    "v_cvt_pk_bf16_f32 v130, v112, v113\n\t" \
    "v_smfmac_f32_16x16x64_bf16 v[116:119], v[96:99], a[232:239], v19\n\t" \
    "s_nop 1\n\t" \
    "v_cvt_pk_bf16_f32 v131, v114, v115\n\t" \
    "v_smfmac_f32_16x16x64_bf16 v[120:123], v[84:87], a[240:247], v19\n\t" \
    "s_nop 1\n\t" \
    "v_permlane16_swap_b32 v128, v130\n\t" \
    "v_smfmac_f32_16x16x64_bf16 v[120:123], v[88:91], a[248:255], v19\n\t" \
    "s_nop 1\n\t" \
    "v_permlane16_swap_b32 v129, v131\n\t" \
    "v_smfmac_f32_16x16x64_bf16 v[120:123], v[92:95], v[224:231], v19\n\t" \
    "s_nop 0\n\t" \
    "global_store_dwordx4 v14, v[128:131], s[38:39] sc1\n\t" \
    "v_cvt_pk_bf16_f32 v132, v116, v117\n\t" \
    "v_smfmac_f32_16x16x64_bf16 v[120:123], v[96:99], v[232:239], v19\n\t" \
    "s_nop 1\n\t" \
    "v_cvt_pk_bf16_f32 v133, v118, v119\n\t" \
    "s_nop 4\n\t" \
    "v_cvt_pk_bf16_f32 v134, v120, v121\n\t" \
    "v_cvt_pk_bf16_f32 v135, v122, v123\n\t" \
    "s_nop 0\n\t" \
    "v_permlane16_swap_b32 v132, v134\n\t" \
    "v_permlane16_swap_b32 v133, v135\n\t" \
    "global_store_dwordx4 v15, v[132:135], s[38:39] sc1\n\t" \
    "s_cmp_eq_u32 s65, 63\n\t" \
    "s_cbranch_scc1 vv_end27_%=\n\t" \
    "s_add_u32 s65, s65, 1\n\t" \
    "v_mov_b32 v72, 0\n\t" \
    "v_mov_b32 v73, 0\n\t" \
    "v_mov_b32 v74, 0\n\t" \
    "v_mov_b32 v75, 0\n\t" \
    "v_mov_b32 v76, 0\n\t" \
    "v_mov_b32 v77, 0\n\t" \
    "v_mov_b32 v78, 0\n\t" \
    "v_mov_b32 v79, 0\n\t" \
    "v_mov_b32 v108, 0\n\t" \
    "v_mov_b32 v109, 0\n\t" \
    "v_mov_b32 v110, 0\n\t" \
    "v_mov_b32 v111, 0\n\t" \
    "v_mov_b32 v112, 0\n\t" \
    "v_mov_b32 v113, 0\n\t" \
    "v_mov_b32 v114, 0\n\t" \
    "v_mov_b32 v115, 0\n\t" \
    "v_mov_b32 v116, 0\n\t" \
    "v_mov_b32 v117, 0\n\t" \
    "v_mov_b32 v118, 0\n\t" \
    "v_mov_b32 v119, 0\n\t" \
    "v_mov_b32 v120, 0\n\t" \
    "v_mov_b32 v121, 0\n\t" \
    "v_mov_b32 v122, 0\n\t" \
    "v_mov_b32 v123, 0\n\t" \
    "v_mov_b32 v124, 0\n\t" \
    "v_mov_b32 v125, 0\n\t" \
    "v_mov_b32 v126, 0\n\t" \
    "v_mov_b32 v127, 0\n\t" \
    "s_add_u32 s69, s65, 0\n\t" \
    "s_and_b32 s69, s69, 7\n\t" \
    "s_lshl_b32 s69, s69, 18\n\t" \
    "s_add_u32 s69, s69, s62\n\t" \
    "s_add_u32 s46, s36, s69\n\t" \
    "s_addc_u32 s47, s37, 0\n\t" \
    "s_add_u32 s70, s65, 0\n\t" \
    "s_mul_i32 s69, s70, 171\n\t" \
    "s_lshr_b32 s69, s69, 9\n\t" \
    "s_mul_i32 s70, s70, 589824\n\t" \
    "s_mul_i32 s71, s69, 327680\n\t" \
    "s_add_u32 s70, s70, s71\n\t" \
    "s_lshl1_add_u32 s69, s69, 4\n\t" \
    "s_bitcmp1_b64 s[44:45], s69\n\t" \
    "s_cselect_b32 s69, 8192, 0\n\t" \
    "s_add_u32 s69, s69, s70\n\t" \
    "s_add_u32 s69, s69, 123731968\n\t" \
    "s_add_u32 s38, s36, s69\n\t" \
    "s_addc_u32 s39, s37, 0\n\t" \
    "s_and_b32 s69, s69, 0xffe00000\n\t" \
    "s_add_u32 s69, s69, 1823744\n\t" \
    "v_mov_b32 v17, s69\n\t" \
    "global_load_dword v17, v17, s[36:37]\n\t" \
    "s_barrier\n\t" \
    "ds_read_b32 v160, v11 offset:20480\n\t" \
    "s_waitcnt lgkmcnt(0)\n\t" \
    "v_readfirstlane_b32 s70, v160\n\t" \
    "s_branch vv_loop10_%=\n\t" \
    "vv_Gbad32_%=:\n\t" \
    "s_waitcnt lgkmcnt(0)\n\t" \
    "s_nop 7\n\t" \
    "v_mov_b32 v72, 0\n\t" \
    "v_mov_b32 v73, 0\n\t" \
    "v_mov_b32 v74, 0\n\t" \
    "v_mov_b32 v75, 0\n\t" \
    "v_mov_b32 v76, 0\n\t" \
    "v_mov_b32 v77, 0\n\t" \
    "v_mov_b32 v78, 0\n\t" \
    "v_mov_b32 v79, 0\n\t" \
    "s_cmp_eq_u32 s68, 0\n\t" \
    "s_cbranch_scc1 vv_F36_%=\n\t" \
    "s_sub_u32 s68, s68, 1\n\t" \
    "s_branch vv_G31_%=\n\t" \
    "vv_F36_%=:\n\t" \
    "v_mov_b32 v140, 5767424\n\t" \
    "v_mov_b32 v141, 1\n\t" \
    "global_store_dword v140, v141, s[36:37] sc1\n\t" \
    "s_waitcnt vmcnt(0)\n\t" \
    "s_branch vv_Gc33_%=\n\t" \
    "vv_Gslow34_%=:\n\t" \
    "v_max_u32 v140, v36, v37\n\t" \
    "v_max3_u32 v140, v140, v38, v39\n\t" \
    "v_max3_u32 v140, v140, v40, v41\n\t" \
    "v_max3_u32 v140, v140, v42, v43\n\t" \
    "v_max3_u32 v140, v140, v44, v45\n\t" \
    "v_max3_u32 v140, v140, v46, v47\n\t" \
    "v_max3_u32 v140, v140, v48, v49\n\t" \
    "v_max3_u32 v140, v140, v50, v51\n\t" \
    "v_max3_u32 v140, v140, v52, v53\n\t" \
    "v_max3_u32 v140, v140, v54, v55\n\t" \
    "v_max3_u32 v140, v140, v56, v57\n\t" \
    "v_max3_u32 v140, v140, v58, v59\n\t" \
    "v_max3_u32 v140, v140, v60, v61\n\t" \
    "v_max3_u32 v140, v140, v62, v63\n\t" \
    "v_max3_u32 v140, v140, v64, v65\n\t" \
    "v_max3_u32 v140, v140, v66, v67\n\t" \
    "v_max3_u32 v140, v140, v68, v69\n\t" \
    "v_max3_u32 v140, v140, v70, v71\n\t" \
    "v_cmp_eq_u32 vcc, -1, v140\n\t" \
    "s_cbranch_vccz vv_Gback35_%=\n\t" \
    "s_waitcnt lgkmcnt(0)\n\t" \
    "v_sub_u32 v140, v1, v3\n\t" \
    "s_nop 0\n\t" \
    "v_readfirstlane_b32 s69, v140\n\t" \
    "v_add_u32 v143, 20480, v0\n\t" \
    "s_nop 7\n\t" \
    "s_add_u32 s30, s46, s69\n\t" \
    "s_addc_u32 s31, s47, 0\n\t" \
    "vv_Gag37_%=:\n\t" \
    "global_load_dwordx4 v[36:39], v1, s[46:47] sc1\n\t" \
    "global_load_dwordx4 v[40:43], v23, s[46:47] sc1\n\t" \
    "global_load_dwordx4 v[44:47], v143, s[30:31] sc1\n\t" \
    "global_load_dwordx4 v[48:51], v4, s[30:31] sc1\n\t" \
    "global_load_dwordx4 v[52:55], v5, s[30:31] sc1\n\t" \
    "global_load_dwordx4 v[56:59], v6, s[30:31] sc1\n\t" \
    "global_load_dwordx4 v[60:63], v7, s[30:31] sc1\n\t" \
    "global_load_dwordx4 v[64:67], v8, s[30:31] sc1\n\t" \
    "global_load_dwordx4 v[68:71], v9, s[30:31] sc1\n\t" \
    "s_waitcnt vmcnt(0)\n\t" \
    "v_max_u32 v140, v36, v37\n\t" \
    "v_max3_u32 v140, v140, v38, v39\n\t" \
    "v_max3_u32 v140, v140, v40, v41\n\t" \
    "v_max3_u32 v140, v140, v42, v43\n\t" \
    "v_max3_u32 v140, v140, v44, v45\n\t" \
    "v_max3_u32 v140, v140, v46, v47\n\t" \
    "v_max3_u32 v140, v140, v48, v49\n\t" \
    "v_max3_u32 v140, v140, v50, v51\n\t" \
    "v_max3_u32 v140, v140, v52, v53\n\t" \
    "v_max3_u32 v140, v140, v54, v55\n\t" \
    "v_max3_u32 v140, v140, v56, v57\n\t" \
    "v_max3_u32 v140, v140, v58, v59\n\t" \
    "v_max3_u32 v140, v140, v60, v61\n\t" \
    "v_max3_u32 v140, v140, v62, v63\n\t" \
    "v_max3_u32 v140, v140, v64, v65\n\t" \
    "v_max3_u32 v140, v140, v66, v67\n\t" \
    "v_max3_u32 v140, v140, v68, v69\n\t" \
    "v_max3_u32 v140, v140, v70, v71\n\t" \
    "v_cmp_eq_u32 vcc, -1, v140\n\t" \
    "s_cbranch_vccz vv_Gredo38_%=\n\t" \
    "s_cmp_eq_u32 s68, 0\n\t" \
    "s_cbranch_scc1 vv_F39_%=\n\t" \
    "s_sub_u32 s68, s68, 1\n\t" \
    "s_sleep 1\n\t" \
    "s_branch vv_Gag37_%=\n\t" \
    "vv_F39_%=:\n\t" \
    "v_mov_b32 v140, 5767424\n\t" \
    "v_mov_b32 v141, 2\n\t" \
    "global_store_dword v140, v141, s[36:37] sc1\n\t" \
    "s_waitcnt vmcnt(0)\n\t" \
    "s_branch vv_Gredo38_%=\n\t" \
    "vv_Gredo38_%=:\n\t" \
    "v_mov_b32 v72, 0\n\t" \
    "v_mov_b32 v73, 0\n\t" \
    "v_mov_b32 v74, 0\n\t" \
    "v_mov_b32 v75, 0\n\t" \
    "v_mov_b32 v76, 0\n\t" \
    "v_mov_b32 v77, 0\n\t" \
    "v_mov_b32 v78, 0\n\t" \
    "v_mov_b32 v79, 0\n\t" \
    "v_smfmac_f32_16x16x64_bf16 v[72:75], v[36:39], a[16:23], v10\n\t" \
    "v_smfmac_f32_16x16x64_bf16 v[72:75], v[40:43], a[24:31], v10\n\t" \
    "v_smfmac_f32_16x16x64_bf16 v[72:75], v[44:47], a[64:71], v10\n\t" \
    "v_smfmac_f32_16x16x64_bf16 v[72:75], v[48:51], a[32:39], v10\n\t" \
    "v_smfmac_f32_16x16x64_bf16 v[72:75], v[52:55], a[40:47], v10\n\t" \
    "v_smfmac_f32_16x16x64_bf16 v[72:75], v[56:59], a[48:55], v10\n\t" \
    "v_smfmac_f32_16x16x64_bf16 v[72:75], v[60:63], a[56:63], v10\n\t" \
    "v_smfmac_f32_16x16x64_bf16 v[72:75], v[64:67], a[0:7], v10\n\t" \
    "v_smfmac_f32_16x16x64_bf16 v[72:75], v[68:71], a[8:15], v10\n\t" \
    "v_smfmac_f32_16x16x64_bf16 v[76:79], v[36:39], a[88:95], v10\n\t" \
    "v_smfmac_f32_16x16x64_bf16 v[76:79], v[40:43], a[96:103], v10\n\t" \
    "v_smfmac_f32_16x16x64_bf16 v[76:79], v[44:47], a[136:143], v10\n\t" \
    "v_smfmac_f32_16x16x64_bf16 v[76:79], v[48:51], a[104:111], v10\n\t" \
    "v_smfmac_f32_16x16x64_bf16 v[76:79], v[52:55], a[112:119], v10\n\t" \
    "v_smfmac_f32_16x16x64_bf16 v[76:79], v[56:59], a[120:127], v10\n\t" \
    "v_smfmac_f32_16x16x64_bf16 v[76:79], v[60:63], a[128:135], v10\n\t" \
    "v_smfmac_f32_16x16x64_bf16 v[76:79], v[64:67], a[72:79], v10\n\t" \
    "v_smfmac_f32_16x16x64_bf16 v[76:79], v[68:71], a[80:87], v10\n\t" \
    "v_add_f32 v72, v72, v74\n\t" \
    "v_add_f32 v73, v73, v75\n\t" \
    "v_cvt_pk_bf16_f32 v80, v72, v73\n\t" \
    "ds_write_b32 v12, v80 offset:0\n\t" \
    "s_nop 3\n\t" \
    "v_pk_add_f32 v[76:77], v[76:77], v[78:79]\n\t" \
    "v_cvt_pk_bf16_f32 v81, v76, v77\n\t" \
    "ds_write_b32 v12, v81 offset:256\n\t" \
    "s_branch vv_Gback35_%=\n\t" \
    "vv_end27_%=:\n\t" \
    "s_endpgm\n\t" \
    ".p2align 6\n\t" \
    "s_nop 0\n\t" \
    "s_nop 0\n\t" \
    "s_nop 0\n\t" \
    "s_nop 0\n\t" \
    "s_nop 0\n\t" \
    "s_nop 0\n\t" \
    "vv_loop11_%=:\n\t" \
    "s_getpc_b64 s[30:31]\n\t" \
    "vv_Wp42_%=:\n\t" \
    "s_add_u32 s30, s30, vv_We43_%=-vv_Wp42_%=\n\t" \
    "s_addc_u32 s31, s31, 0\n\t" \
    "s_sub_u32 s70, s70, 96\n\t" \
    "vv_W41_%=:\n\t" \
    "s_memtime s[56:57]\n\t" \
    "s_waitcnt lgkmcnt(0)\n\t" \
    "s_sub_u32 s69, s70, s56\n\t" \
    "s_cmp_gt_i32 s69, 80\n\t" \
    "s_cbranch_scc1 vv_W41_%=\n\t" \
    "s_max_i32 s69, s69, 0\n\t" \
    "s_and_b32 s69, s69, -4\n\t" \
    "s_sub_u32 s30, s30, s69\n\t" \
    "s_subb_u32 s31, s31, 0\n\t" \
    "s_setpc_b64 s[30:31]\n\t" \
    "s_nop 0\n\t" \
    "s_nop 0\n\t" \
    "s_nop 0\n\t" \
    "s_nop 0\n\t" \
    "s_nop 0\n\t" \
    "s_nop 0\n\t" \
    "s_nop 0\n\t" \
    "s_nop 0\n\t" \
    "s_nop 0\n\t" \
    "s_nop 0\n\t" \
    "s_nop 0\n\t" \
    "s_nop 0\n\t" \
    "s_nop 0\n\t" \
    "s_nop 0\n\t" \
    "s_nop 0\n\t" \
    "s_nop 0\n\t" \
    "s_nop 0\n\t" \
    "s_nop 0\n\t" \
    "s_nop 0\n\t" \
    "s_nop 0\n\t" \
    "vv_We43_%=:\n\t" \
    "vv_G44_%=:\n\t" \
    "global_load_dwordx4 v[36:39], v1, s[46:47] sc1\n\t" \
    "global_load_dword v141, v2, s[46:47] sc1\n\t" \
    "global_load_dwordx4 v[40:43], v23, s[46:47] sc1\n\t" \
    "s_waitcnt vmcnt(2)\n\t" \
    "ds_write_b128 v3, v[36:39]\n\t" \
    "v_smfmac_f32_16x16x64_bf16 v[72:75], v[36:39], a[32:39], v10\n\t" \
    "s_waitcnt vmcnt(1)\n\t" \
    "ds_write_b32 v22, v141 offset:19968\n\t" \
    "s_waitcnt vmcnt(0)\n\t" \
    "ds_write_b128 v3, v[40:43] offset:1024\n\t" \
    "v_max3_u32 v140, v36, v40, v141\n\t" \
    "v_cmp_eq_u32 vcc, -1, v140\n\t" \
    "s_cbranch_vccnz vv_Gbad45_%=\n\t" \
    "vv_Gc46_%=:\n\t" \
    "s_waitcnt lgkmcnt(0)\n\t" \
    "s_barrier\n\t" \
    "ds_read_b128 v[48:51], v4\n\t" \
    "ds_read_b128 v[52:55], v5\n\t" \
    "v_smfmac_f32_16x16x64_bf16 v[76:79], v[36:39], a[104:111], v10\n\t" \
    "ds_read_b128 v[56:59], v6\n\t" \
    "ds_read_b128 v[60:63], v7\n\t" \
    "v_smfmac_f32_16x16x64_bf16 v[72:75], v[40:43], a[40:47], v10\n\t" \
    "ds_read_b128 v[64:67], v8\n\t" \
    "v_smfmac_f32_16x16x64_bf16 v[76:79], v[40:43], a[112:119], v10\n\t" \
    "ds_read_b128 v[68:71], v9\n\t" \
    "s_waitcnt lgkmcnt(5)\n\t" \
    "v_smfmac_f32_16x16x64_bf16 v[72:75], v[48:51], a[48:55], v10\n\t" \
    "ds_read_b128 v[44:47], v0 offset:19456\n\t" \
    "s_waitcnt lgkmcnt(5)\n\t" \
    "v_smfmac_f32_16x16x64_bf16 v[72:75], v[52:55], a[56:63], v10\n\t" \
    "s_waitcnt lgkmcnt(4)\n\t" \
    "v_smfmac_f32_16x16x64_bf16 v[72:75], v[56:59], a[0:7], v10\n\t" \
    "s_waitcnt lgkmcnt(3)\n\t" \
    "v_smfmac_f32_16x16x64_bf16 v[72:75], v[60:63], a[8:15], v10\n\t" \
    "s_waitcnt lgkmcnt(2)\n\t" \
    "v_smfmac_f32_16x16x64_bf16 v[72:75], v[64:67], a[16:23], v10\n\t" \
    "s_waitcnt lgkmcnt(1)\n\t" \
    "v_smfmac_f32_16x16x64_bf16 v[72:75], v[68:71], a[24:31], v10\n\t" \
    "s_waitcnt lgkmcnt(0)\n\t" \
    "v_smfmac_f32_16x16x64_bf16 v[72:75], v[44:47], a[64:71], v10\n\t" \
    "s_nop 2\n\t" \
    "v_smfmac_f32_16x16x64_bf16 v[76:79], v[48:51], a[120:127], v10\n\t" \
    "s_nop 2\n\t" \
    "v_smfmac_f32_16x16x64_bf16 v[76:79], v[52:55], a[128:135], v10\n\t" \
    "v_add_f32 v72, v72, v74\n\t" \
    "v_smfmac_f32_16x16x64_bf16 v[76:79], v[56:59], a[72:79], v10\n\t" \
    "v_add_f32 v73, v73, v75\n\t" \
    "v_smfmac_f32_16x16x64_bf16 v[76:79], v[60:63], a[80:87], v10\n\t" \
    "v_cvt_pk_bf16_f32 v80, v72, v73\n\t" \
    "v_smfmac_f32_16x16x64_bf16 v[76:79], v[64:67], a[88:95], v10\n\t" \
    "ds_write_b32 v12, v80 offset:0\n\t" \
    "v_smfmac_f32_16x16x64_bf16 v[76:79], v[68:71], a[96:103], v10\n\t" \
    "v_smfmac_f32_16x16x64_bf16 v[76:79], v[44:47], a[136:143], v10\n\t" \
    "s_nop 6\n\t" \
    "v_cmp_u_f32 vcc, v72, v73\n\t" \
    "v_pk_add_f32 v[76:77], v[76:77], v[78:79]\n\t" \
    "v_cvt_pk_bf16_f32 v81, v76, v77\n\t" \
    "ds_write_b32 v12, v81 offset:256\n\t" \
    "s_cbranch_vccnz vv_Gslow47_%=\n\t" \
    "vv_Gback48_%=:\n\t" \
    "s_waitcnt lgkmcnt(0)\n\t" \
    "s_barrier\n\t" \
    "ds_read_b64_tr_b16 v[84:85], v13 offset:0\n\t" \
    "v_smfmac_f32_16x16x64_bf16 v[100:103], v[36:39], a[0:7], v10\n\t" \
    "ds_read_b64_tr_b16 v[86:87], v13 offset:64\n\t" \
    "ds_read_b64_tr_b16 v[92:93], v13 offset:1152\n\t" \
    "v_smfmac_f32_16x16x64_bf16 v[100:103], v[36:39], a[0:7], v10\n\t" \
    "ds_read_b64_tr_b16 v[94:95], v13 offset:1216\n\t" \
    "ds_read_b64_tr_b16 v[88:89], v25 offset:0\n\t" \
    "v_smfmac_f32_16x16x64_bf16 v[100:103], v[36:39], a[0:7], v10\n\t" \
    "ds_read_b64_tr_b16 v[90:91], v25 offset:64\n\t" \
    "s_waitcnt lgkmcnt(4)\n\t" \
    "v_smfmac_f32_16x16x64_bf16 v[124:127], v[84:87], v[240:247], v19\n\t" \
    "ds_read_b64_tr_b16 v[96:97], v25 offset:1152\n\t" \
    "ds_read_b64_tr_b16 v[98:99], v25 offset:1216\n\t" \
    "s_waitcnt lgkmcnt(4)\n\t" \
    "v_smfmac_f32_16x16x64_bf16 v[124:127], v[92:95], v[248:255], v19\n\t" \
    "s_nop 2\n\t" \
    "v_smfmac_f32_16x16x64_bf16 v[108:111], v[84:87], a[144:151], v19\n\t" \
    "s_nop 2\n\t" \
    "v_smfmac_f32_16x16x64_bf16 v[108:111], v[92:95], a[160:167], v19\n\t" \
    "s_nop 1\n\t" \
    "v_permlane32_swap_b32 v124, v126\n\t" \
    "v_smfmac_f32_16x16x64_bf16 v[112:115], v[84:87], a[176:183], v19\n\t" \
    "s_nop 1\n\t" \
    "v_permlane32_swap_b32 v125, v127\n\t" \
    "v_smfmac_f32_16x16x64_bf16 v[112:115], v[92:95], a[192:199], v19\n\t" \
    "s_nop 0\n\t" \
    "v_add_f32 v124, v124, v126\n\t" \
    "s_waitcnt lgkmcnt(2)\n\t" \
    "v_smfmac_f32_16x16x64_bf16 v[108:111], v[88:91], a[152:159], v19\n\t" \
    "s_nop 1\n\t" \
    "v_add_f32 v125, v125, v127\n\t" \
    "v_smfmac_f32_16x16x64_bf16 v[112:115], v[88:91], a[184:191], v19\n\t" \
    "s_nop 0\n\t" \
    "v_cvt_pk_bf16_f32 v136, v124, v125\n\t" \
    "s_waitcnt lgkmcnt(0)\n\t" \
    "v_smfmac_f32_16x16x64_bf16 v[108:111], v[96:99], a[168:175], v19\n\t" \
    "s_nop 1\n\t" \
    "v_cvt_pk_bf16_f32 v137, v124, v125\n\t" \
    "v_smfmac_f32_16x16x64_bf16 v[112:115], v[96:99], a[200:207], v19\n\t" \
    "s_nop 1\n\t" \
    "v_permlane32_swap_b32 v136, v137\n\t" \
    "v_smfmac_f32_16x16x64_bf16 v[116:119], v[84:87], a[208:215], v19\n\t" \
    "s_mov_b32 exec_hi, 0\n\t" \
    "global_store_dwordx2 v16, v[136:137], s[38:39] sc1\n\t" \
    "s_mov_b32 exec_hi, -1\n\t" \
    "v_cvt_pk_bf16_f32 v128, v108, v109\n\t" \
    "v_smfmac_f32_16x16x64_bf16 v[116:119], v[88:91], a[216:223], v19\n\t" \
    "s_nop 1\n\t" \
    "v_cvt_pk_bf16_f32 v129, v110, v111\n\t" \
    "v_smfmac_f32_16x16x64_bf16 v[116:119], v[92:95], a[224:231], v19\n\t" \
    "s_nop 1\n\t" \
    "v_cvt_pk_bf16_f32 v130, v112, v113\n\t" \
    "v_smfmac_f32_16x16x64_bf16 v[116:119], v[96:99], a[232:239], v19\n\t" \
    "s_nop 1\n\t" \
    "v_cvt_pk_bf16_f32 v131, v114, v115\n\t" \
    "v_smfmac_f32_16x16x64_bf16 v[120:123], v[84:87], a[240:247], v19\n\t" \
    "s_nop 1\n\t" \
    "v_permlane16_swap_b32 v128, v130\n\t" \
    "v_smfmac_f32_16x16x64_bf16 v[120:123], v[88:91], a[248:255], v19\n\t" \
    "s_nop 1\n\t" \
    "v_permlane16_swap_b32 v129, v131\n\t" \
    "v_smfmac_f32_16x16x64_bf16 v[120:123], v[92:95], v[224:231], v19\n\t" \
    "s_nop 0\n\t" \
    "global_store_dwordx4 v14, v[128:131], s[38:39] sc1\n\t" \
    "v_cvt_pk_bf16_f32 v132, v116, v117\n\t" \
    "v_smfmac_f32_16x16x64_bf16 v[120:123], v[96:99], v[232:239], v19\n\t" \
    "s_nop 1\n\t" \
    "v_cvt_pk_bf16_f32 v133, v118, v119\n\t" \
    "s_nop 4\n\t" \
    "v_cvt_pk_bf16_f32 v134, v120, v121\n\t" \
    "v_cvt_pk_bf16_f32 v135, v122, v123\n\t" \
    "s_nop 0\n\t" \
    "v_permlane16_swap_b32 v132, v134\n\t" \
    "v_permlane16_swap_b32 v133, v135\n\t" \
    "global_store_dwordx4 v15, v[132:135], s[38:39] sc1\n\t" \
    "s_cmp_eq_u32 s65, 63\n\t" \
    "s_cbranch_scc1 vv_end40_%=\n\t" \
    "s_add_u32 s65, s65, 1\n\t" \
    "v_mov_b32 v72, 0\n\t" \
    "v_mov_b32 v73, 0\n\t" \
    "v_mov_b32 v74, 0\n\t" \
    "v_mov_b32 v75, 0\n\t" \
    "v_mov_b32 v76, 0\n\t" \
    "v_mov_b32 v77, 0\n\t" \
    "v_mov_b32 v78, 0\n\t" \
    "v_mov_b32 v79, 0\n\t" \
    "v_mov_b32 v108, 0\n\t" \
    "v_mov_b32 v109, 0\n\t" \
    "v_mov_b32 v110, 0\n\t" \
    "v_mov_b32 v111, 0\n\t" \
    "v_mov_b32 v112, 0\n\t" \
    "v_mov_b32 v113, 0\n\t" \
    "v_mov_b32 v114, 0\n\t" \
    "v_mov_b32 v115, 0\n\t" \
    "v_mov_b32 v116, 0\n\t" \
    "v_mov_b32 v117, 0\n\t" \
    "v_mov_b32 v118, 0\n\t" \
    "v_mov_b32 v119, 0\n\t" \
    "v_mov_b32 v120, 0\n\t" \
    "v_mov_b32 v121, 0\n\t" \
    "v_mov_b32 v122, 0\n\t" \
    "v_mov_b32 v123, 0\n\t" \
    "v_mov_b32 v124, 0\n\t" \
    "v_mov_b32 v125, 0\n\t" \
    "v_mov_b32 v126, 0\n\t" \
    "v_mov_b32 v127, 0\n\t" \
    "s_add_u32 s69, s65, 0\n\t" \
    "s_and_b32 s69, s69, 7\n\t" \
    "s_lshl_b32 s69, s69, 18\n\t" \
    "s_add_u32 s69, s69, s62\n\t" \
    "s_add_u32 s46, s36, s69\n\t" \
    "s_addc_u32 s47, s37, 0\n\t" \
    "s_add_u32 s70, s65, 0\n\t" \
    "s_mul_i32 s69, s70, 171\n\t" \
    "s_lshr_b32 s69, s69, 9\n\t" \
    "s_mul_i32 s70, s70, 589824\n\t" \
    "s_mul_i32 s71, s69, 327680\n\t" \
    "s_add_u32 s70, s70, s71\n\t" \
    "s_lshl1_add_u32 s69, s69, 4\n\t" \
    "s_bitcmp1_b64 s[44:45], s69\n\t" \
    "s_cselect_b32 s69, 8192, 0\n\t" \
    "s_add_u32 s69, s69, s70\n\t" \
    "s_add_u32 s69, s69, 123731968\n\t" \
    "s_add_u32 s38, s36, s69\n\t" \
    "s_addc_u32 s39, s37, 0\n\t" \
    "s_and_b32 s69, s69, 0xffe00000\n\t" \
    "s_add_u32 s69, s69, 1823744\n\t" \
    "v_mov_b32 v17, s69\n\t" \
    "global_load_dword v17, v17, s[36:37]\n\t" \
    "s_barrier\n\t" \
    "ds_read_b32 v160, v11 offset:20480\n\t" \
    "s_waitcnt lgkmcnt(0)\n\t" \
    "v_readfirstlane_b32 s70, v160\n\t" \
    "s_branch vv_loop11_%=\n\t" \
    "vv_Gbad45_%=:\n\t" \
    "s_waitcnt lgkmcnt(0)\n\t" \
    "s_nop 7\n\t" \
    "v_mov_b32 v72, 0\n\t" \
    "v_mov_b32 v73, 0\n\t" \
    "v_mov_b32 v74, 0\n\t" \
    "v_mov_b32 v75, 0\n\t" \
    "v_mov_b32 v76, 0\n\t" \
    "v_mov_b32 v77, 0\n\t" \
    "v_mov_b32 v78, 0\n\t" \
    "v_mov_b32 v79, 0\n\t" \
    "s_cmp_eq_u32 s68, 0\n\t" \
    "s_cbranch_scc1 vv_F49_%=\n\t" \
    "s_sub_u32 s68, s68, 1\n\t" \
    "s_branch vv_G44_%=\n\t" \
    "vv_F49_%=:\n\t" \
    "v_mov_b32 v140, 5767424\n\t" \
    "v_mov_b32 v141, 1\n\t" \
    "global_store_dword v140, v141, s[36:37] sc1\n\t" \
    "s_waitcnt vmcnt(0)\n\t" \
    "s_branch vv_Gc46_%=\n\t" \
    "vv_Gslow47_%=:\n\t" \
    "v_max_u32 v140, v36, v37\n\t" \
    "v_max3_u32 v140, v140, v38, v39\n\t" \
    "v_max3_u32 v140, v140, v40, v41\n\t" \
    "v_max3_u32 v140, v140, v42, v43\n\t" \
    "v_max3_u32 v140, v140, v44, v45\n\t" \
    "v_max3_u32 v140, v140, v46, v47\n\t" \
    "v_max3_u32 v140, v140, v48, v49\n\t" \
    "v_max3_u32 v140, v140, v50, v51\n\t" \
    "v_max3_u32 v140, v140, v52, v53\n\t" \
    "v_max3_u32 v140, v140, v54, v55\n\t" \
    "v_max3_u32 v140, v140, v56, v57\n\t" \
    "v_max3_u32 v140, v140, v58, v59\n\t" \
    "v_max3_u32 v140, v140, v60, v61\n\t" \
    "v_max3_u32 v140, v140, v62, v63\n\t" \
    "v_max3_u32 v140, v140, v64, v65\n\t" \
    "v_max3_u32 v140, v140, v66, v67\n\t" \
    "v_max3_u32 v140, v140, v68, v69\n\t" \
    "v_max3_u32 v140, v140, v70, v71\n\t" \
    "v_cmp_eq_u32 vcc, -1, v140\n\t" \
    "s_cbranch_vccz vv_Gback48_%=\n\t" \
    "s_waitcnt lgkmcnt(0)\n\t" \
    "v_sub_u32 v140, v1, v3\n\t" \
    "s_nop 0\n\t" \
    "v_readfirstlane_b32 s69, v140\n\t" \
    "v_add_u32 v143, 20480, v0\n\t" \
    "s_nop 7\n\t" \
    "s_add_u32 s30, s46, s69\n\t" \
    "s_addc_u32 s31, s47, 0\n\t" \
    "vv_Gag50_%=:\n\t" \
    "global_load_dwordx4 v[36:39], v1, s[46:47] sc1\n\t" \
    "global_load_dwordx4 v[40:43], v23, s[46:47] sc1\n\t" \
    "global_load_dwordx4 v[44:47], v143, s[30:31] sc1\n\t" \
    "global_load_dwordx4 v[48:51], v4, s[30:31] sc1\n\t" \
    "global_load_dwordx4 v[52:55], v5, s[30:31] sc1\n\t" \
    "global_load_dwordx4 v[56:59], v6, s[30:31] sc1\n\t" \
    "global_load_dwordx4 v[60:63], v7, s[30:31] sc1\n\t" \
    "global_load_dwordx4 v[64:67], v8, s[30:31] sc1\n\t" \
    "global_load_dwordx4 v[68:71], v9, s[30:31] sc1\n\t" \
    "s_waitcnt vmcnt(0)\n\t" \
    "v_max_u32 v140, v36, v37\n\t" \
    "v_max3_u32 v140, v140, v38, v39\n\t" \
    "v_max3_u32 v140, v140, v40, v41\n\t" \
    "v_max3_u32 v140, v140, v42, v43\n\t" \
    "v_max3_u32 v140, v140, v44, v45\n\t" \
    "v_max3_u32 v140, v140, v46, v47\n\t" \
    "v_max3_u32 v140, v140, v48, v49\n\t" \
    "v_max3_u32 v140, v140, v50, v51\n\t" \
    "v_max3_u32 v140, v140, v52, v53\n\t" \
    "v_max3_u32 v140, v140, v54, v55\n\t" \
    "v_max3_u32 v140, v140, v56, v57\n\t" \
    "v_max3_u32 v140, v140, v58, v59\n\t" \
    "v_max3_u32 v140, v140, v60, v61\n\t" \
    "v_max3_u32 v140, v140, v62, v63\n\t" \
    "v_max3_u32 v140, v140, v64, v65\n\t" \
    "v_max3_u32 v140, v140, v66, v67\n\t" \
    "v_max3_u32 v140, v140, v68, v69\n\t" \
    "v_max3_u32 v140, v140, v70, v71\n\t" \
    "v_cmp_eq_u32 vcc, -1, v140\n\t" \
    "s_cbranch_vccz vv_Gredo51_%=\n\t" \
    "s_cmp_eq_u32 s68, 0\n\t" \
    "s_cbranch_scc1 vv_F52_%=\n\t" \
    "s_sub_u32 s68, s68, 1\n\t" \
    "s_sleep 1\n\t" \
    "s_branch vv_Gag50_%=\n\t" \
    "vv_F52_%=:\n\t" \
    "v_mov_b32 v140, 5767424\n\t" \
    "v_mov_b32 v141, 2\n\t" \
    "global_store_dword v140, v141, s[36:37] sc1\n\t" \
    "s_waitcnt vmcnt(0)\n\t" \
    "s_branch vv_Gredo51_%=\n\t" \
    "vv_Gredo51_%=:\n\t" \
    "v_mov_b32 v72, 0\n\t" \
    "v_mov_b32 v73, 0\n\t" \
    "v_mov_b32 v74, 0\n\t" \
    "v_mov_b32 v75, 0\n\t" \
    "v_mov_b32 v76, 0\n\t" \
    "v_mov_b32 v77, 0\n\t" \
    "v_mov_b32 v78, 0\n\t" \
    "v_mov_b32 v79, 0\n\t" \
    "v_smfmac_f32_16x16x64_bf16 v[72:75], v[36:39], a[32:39], v10\n\t" \
    "v_smfmac_f32_16x16x64_bf16 v[72:75], v[40:43], a[40:47], v10\n\t" \
    "v_smfmac_f32_16x16x64_bf16 v[72:75], v[44:47], a[64:71], v10\n\t" \
    "v_smfmac_f32_16x16x64_bf16 v[72:75], v[48:51], a[48:55], v10\n\t" \
    "v_smfmac_f32_16x16x64_bf16 v[72:75], v[52:55], a[56:63], v10\n\t" \
    "v_smfmac_f32_16x16x64_bf16 v[72:75], v[56:59], a[0:7], v10\n\t" \
    "v_smfmac_f32_16x16x64_bf16 v[72:75], v[60:63], a[8:15], v10\n\t" \
    "v_smfmac_f32_16x16x64_bf16 v[72:75], v[64:67], a[16:23], v10\n\t" \
    "v_smfmac_f32_16x16x64_bf16 v[72:75], v[68:71], a[24:31], v10\n\t" \
    "v_smfmac_f32_16x16x64_bf16 v[76:79], v[36:39], a[104:111], v10\n\t" \
    "v_smfmac_f32_16x16x64_bf16 v[76:79], v[40:43], a[112:119], v10\n\t" \
    "v_smfmac_f32_16x16x64_bf16 v[76:79], v[44:47], a[136:143], v10\n\t" \
    "v_smfmac_f32_16x16x64_bf16 v[76:79], v[48:51], a[120:127], v10\n\t" \
    "v_smfmac_f32_16x16x64_bf16 v[76:79], v[52:55], a[128:135], v10\n\t" \
    "v_smfmac_f32_16x16x64_bf16 v[76:79], v[56:59], a[72:79], v10\n\t" \
    "v_smfmac_f32_16x16x64_bf16 v[76:79], v[60:63], a[80:87], v10\n\t" \
    "v_smfmac_f32_16x16x64_bf16 v[76:79], v[64:67], a[88:95], v10\n\t" \
    "v_smfmac_f32_16x16x64_bf16 v[76:79], v[68:71], a[96:103], v10\n\t" \
    "v_add_f32 v72, v72, v74\n\t" \
    "v_add_f32 v73, v73, v75\n\t" \
    "v_cvt_pk_bf16_f32 v80, v72, v73\n\t" \
    "ds_write_b32 v12, v80 offset:0\n\t" \
    "s_nop 3\n\t" \
    "v_pk_add_f32 v[76:77], v[76:77], v[78:79]\n\t" \
    "v_cvt_pk_bf16_f32 v81, v76, v77\n\t" \
    "ds_write_b32 v12, v81 offset:256\n\t" \
    "s_branch vv_Gback48_%=\n\t" \
    "vv_end40_%=:\n\t" \
    "s_endpgm\n\t" \
    ".p2align 6\n\t" \
    "s_nop 0\n\t" \
    "s_nop 0\n\t" \
    "s_nop 0\n\t" \
    "s_nop 0\n\t" \
    "s_nop 0\n\t" \
    "s_nop 0\n\t" \
    "vv_loop12_%=:\n\t" \
    "s_getpc_b64 s[30:31]\n\t" \
    "vv_Wp55_%=:\n\t" \
    "s_add_u32 s30, s30, vv_We56_%=-vv_Wp55_%=\n\t" \
    "s_addc_u32 s31, s31, 0\n\t" \
    "s_sub_u32 s70, s70, 96\n\t" \
    "vv_W54_%=:\n\t" \
    "s_memtime s[56:57]\n\t" \
    "s_waitcnt lgkmcnt(0)\n\t" \
    "s_sub_u32 s69, s70, s56\n\t" \
    "s_cmp_gt_i32 s69, 80\n\t" \
    "s_cbranch_scc1 vv_W54_%=\n\t" \
    "s_max_i32 s69, s69, 0\n\t" \
    "s_and_b32 s69, s69, -4\n\t" \
    "s_sub_u32 s30, s30, s69\n\t" \
    "s_subb_u32 s31, s31, 0\n\t" \
    "s_setpc_b64 s[30:31]\n\t" \
    "s_nop 0\n\t" \
    "s_nop 0\n\t" \
    "s_nop 0\n\t" \
    "s_nop 0\n\t" \
    "s_nop 0\n\t" \
    "s_nop 0\n\t" \
    "s_nop 0\n\t" \
    "s_nop 0\n\t" \
    "s_nop 0\n\t" \
    "s_nop 0\n\t" \
    "s_nop 0\n\t" \
    "s_nop 0\n\t" \
    "s_nop 0\n\t" \
    "s_nop 0\n\t" \
    "s_nop 0\n\t" \
    "s_nop 0\n\t" \
    "s_nop 0\n\t" \
    "s_nop 0\n\t" \
    "s_nop 0\n\t" \
    "s_nop 0\n\t" \
    "vv_We56_%=:\n\t" \
    "vv_G57_%=:\n\t" \
    "global_load_dwordx4 v[36:39], v1, s[46:47] sc1\n\t" \
    "global_load_dword v141, v2, s[46:47] sc1\n\t" \
    "global_load_dwordx4 v[40:43], v23, s[46:47] sc1\n\t" \
    "s_waitcnt vmcnt(2)\n\t" \
    "ds_write_b128 v3, v[36:39]\n\t" \
    "v_smfmac_f32_16x16x64_bf16 v[72:75], v[36:39], a[48:55], v10\n\t" \
    "s_waitcnt vmcnt(1)\n\t" \
    "ds_write_b32 v22, v141 offset:20224\n\t" \
    "s_waitcnt vmcnt(0)\n\t" \
    "ds_write_b128 v3, v[40:43] offset:1024\n\t" \
    "v_max3_u32 v140, v36, v40, v141\n\t" \
    "v_cmp_eq_u32 vcc, -1, v140\n\t" \
    "s_cbranch_vccnz vv_Gbad58_%=\n\t" \
    "vv_Gc59_%=:\n\t" \
    "s_waitcnt lgkmcnt(0)\n\t" \
    "s_barrier\n\t" \
    "ds_read_b128 v[48:51], v4\n\t" \
    "ds_read_b128 v[52:55], v5\n\t" \
    "v_smfmac_f32_16x16x64_bf16 v[76:79], v[36:39], a[120:127], v10\n\t" \
    "ds_read_b128 v[56:59], v6\n\t" \
    "ds_read_b128 v[60:63], v7\n\t" \
    "v_smfmac_f32_16x16x64_bf16 v[72:75], v[40:43], a[56:63], v10\n\t" \
    "ds_read_b128 v[64:67], v8\n\t" \
    "v_smfmac_f32_16x16x64_bf16 v[76:79], v[40:43], a[128:135], v10\n\t" \
    "ds_read_b128 v[68:71], v9\n\t" \
    "s_waitcnt lgkmcnt(5)\n\t" \
    "v_smfmac_f32_16x16x64_bf16 v[72:75], v[48:51], a[0:7], v10\n\t" \
    "ds_read_b128 v[44:47], v0 offset:19456\n\t" \
    "s_waitcnt lgkmcnt(5)\n\t" \
    "v_smfmac_f32_16x16x64_bf16 v[72:75], v[52:55], a[8:15], v10\n\t" \
    "s_waitcnt lgkmcnt(4)\n\t" \
    "v_smfmac_f32_16x16x64_bf16 v[72:75], v[56:59], a[16:23], v10\n\t" \
    "s_waitcnt lgkmcnt(3)\n\t" \
    "v_smfmac_f32_16x16x64_bf16 v[72:75], v[60:63], a[24:31], v10\n\t" \
    "s_waitcnt lgkmcnt(2)\n\t" \
    "v_smfmac_f32_16x16x64_bf16 v[72:75], v[64:67], a[32:39], v10\n\t" \
    "s_waitcnt lgkmcnt(1)\n\t" \
    "v_smfmac_f32_16x16x64_bf16 v[72:75], v[68:71], a[40:47], v10\n\t" \
    "s_waitcnt lgkmcnt(0)\n\t" \
    "v_smfmac_f32_16x16x64_bf16 v[72:75], v[44:47], a[64:71], v10\n\t" \
    "s_nop 2\n\t" \
    "v_smfmac_f32_16x16x64_bf16 v[76:79], v[48:51], a[72:79], v10\n\t" \
    "s_nop 2\n\t" \
    "v_smfmac_f32_16x16x64_bf16 v[76:79], v[52:55], a[80:87], v10\n\t" \
    "v_add_f32 v72, v72, v74\n\t" \
    "v_smfmac_f32_16x16x64_bf16 v[76:79], v[56:59], a[88:95], v10\n\t" \
    "v_add_f32 v73, v73, v75\n\t" \
    "v_smfmac_f32_16x16x64_bf16 v[76:79], v[60:63], a[96:103], v10\n\t" \
    "v_cvt_pk_bf16_f32 v80, v72, v73\n\t" \
    "v_smfmac_f32_16x16x64_bf16 v[76:79], v[64:67], a[104:111], v10\n\t" \
    "ds_write_b32 v12, v80 offset:0\n\t" \
    "v_smfmac_f32_16x16x64_bf16 v[76:79], v[68:71], a[112:119], v10\n\t" \
    "v_smfmac_f32_16x16x64_bf16 v[76:79], v[44:47], a[136:143], v10\n\t" \
    "s_nop 6\n\t" \
    "v_cmp_u_f32 vcc, v72, v73\n\t" \
    "v_pk_add_f32 v[76:77], v[76:77], v[78:79]\n\t" \
    "v_cvt_pk_bf16_f32 v81, v76, v77\n\t" \
    "ds_write_b32 v12, v81 offset:256\n\t" \
    "s_cbranch_vccnz vv_Gslow60_%=\n\t" \
    "vv_Gback61_%=:\n\t" \
    "s_waitcnt lgkmcnt(0)\n\t" \
    "s_barrier\n\t" \
    "ds_read_b64_tr_b16 v[84:85], v13 offset:0\n\t" \
    "v_smfmac_f32_16x16x64_bf16 v[100:103], v[36:39], a[0:7], v10\n\t" \
    "ds_read_b64_tr_b16 v[86:87], v13 offset:64\n\t" \
    "ds_read_b64_tr_b16 v[92:93], v13 offset:1152\n\t" \
    "v_smfmac_f32_16x16x64_bf16 v[100:103], v[36:39], a[0:7], v10\n\t" \
    "ds_read_b64_tr_b16 v[94:95], v13 offset:1216\n\t" \
    "ds_read_b64_tr_b16 v[88:89], v25 offset:0\n\t" \
    "v_smfmac_f32_16x16x64_bf16 v[100:103], v[36:39], a[0:7], v10\n\t" \
    "ds_read_b64_tr_b16 v[90:91], v25 offset:64\n\t" \
    "s_waitcnt lgkmcnt(4)\n\t" \
    "v_smfmac_f32_16x16x64_bf16 v[124:127], v[84:87], v[240:247], v19\n\t" \
    "ds_read_b64_tr_b16 v[96:97], v25 offset:1152\n\t" \
    "ds_read_b64_tr_b16 v[98:99], v25 offset:1216\n\t" \
    "s_waitcnt lgkmcnt(4)\n\t" \
    "v_smfmac_f32_16x16x64_bf16 v[124:127], v[92:95], v[248:255], v19\n\t" \
    "s_nop 2\n\t" \
    "v_smfmac_f32_16x16x64_bf16 v[108:111], v[84:87], a[144:151], v19\n\t" \
    "s_nop 2\n\t" \
    "v_smfmac_f32_16x16x64_bf16 v[108:111], v[92:95], a[160:167], v19\n\t" \
    "s_nop 1\n\t" \
    "v_permlane32_swap_b32 v124, v126\n\t" \
    "v_smfmac_f32_16x16x64_bf16 v[112:115], v[84:87], a[176:183], v19\n\t" \
    "s_nop 1\n\t" \
    "v_permlane32_swap_b32 v125, v127\n\t" \
    "v_smfmac_f32_16x16x64_bf16 v[112:115], v[92:95], a[192:199], v19\n\t" \
    "s_nop 0\n\t" \
    "v_add_f32 v124, v124, v126\n\t" \
    "s_waitcnt lgkmcnt(2)\n\t" \
    "v_smfmac_f32_16x16x64_bf16 v[108:111], v[88:91], a[152:159], v19\n\t" \
    "s_nop 1\n\t" \
    "v_add_f32 v125, v125, v127\n\t" \
    "v_smfmac_f32_16x16x64_bf16 v[112:115], v[88:91], a[184:191], v19\n\t" \
    "s_nop 0\n\t" \
    "v_cvt_pk_bf16_f32 v136, v124, v125\n\t" \
    "s_waitcnt lgkmcnt(0)\n\t" \
    "v_smfmac_f32_16x16x64_bf16 v[108:111], v[96:99], a[168:175], v19\n\t" \
    "s_nop 1\n\t" \
    "v_cvt_pk_bf16_f32 v137, v124, v125\n\t" \
    "v_smfmac_f32_16x16x64_bf16 v[112:115], v[96:99], a[200:207], v19\n\t" \
    "s_nop 1\n\t" \
    "v_permlane32_swap_b32 v136, v137\n\t" \
    "v_smfmac_f32_16x16x64_bf16 v[116:119], v[84:87], a[208:215], v19\n\t" \
    "s_mov_b32 exec_hi, 0\n\t" \
    "global_store_dwordx2 v16, v[136:137], s[38:39] sc1\n\t" \
    "s_mov_b32 exec_hi, -1\n\t" \
    "v_cvt_pk_bf16_f32 v128, v108, v109\n\t" \
    "v_smfmac_f32_16x16x64_bf16 v[116:119], v[88:91], a[216:223], v19\n\t" \
    "s_nop 1\n\t" \
    "v_cvt_pk_bf16_f32 v129, v110, v111\n\t" \
    "v_smfmac_f32_16x16x64_bf16 v[116:119], v[92:95], a[224:231], v19\n\t" \
    "s_nop 1\n\t" \
    "v_cvt_pk_bf16_f32 v130, v112, v113\n\t" \
    "v_smfmac_f32_16x16x64_bf16 v[116:119], v[96:99], a[232:239], v19\n\t" \
    "s_nop 1\n\t" \
    "v_cvt_pk_bf16_f32 v131, v114, v115\n\t" \
    "v_smfmac_f32_16x16x64_bf16 v[120:123], v[84:87], a[240:247], v19\n\t" \
    "s_nop 1\n\t" \
    "v_permlane16_swap_b32 v128, v130\n\t" \
    "v_smfmac_f32_16x16x64_bf16 v[120:123], v[88:91], a[248:255], v19\n\t" \
    "s_nop 1\n\t" \
    "v_permlane16_swap_b32 v129, v131\n\t" \
    "v_smfmac_f32_16x16x64_bf16 v[120:123], v[92:95], v[224:231], v19\n\t" \
    "s_nop 0\n\t" \
    "global_store_dwordx4 v14, v[128:131], s[38:39] sc1\n\t" \
    "v_cvt_pk_bf16_f32 v132, v116, v117\n\t" \
    "v_smfmac_f32_16x16x64_bf16 v[120:123], v[96:99], v[232:239], v19\n\t" \
    "s_nop 1\n\t" \
    "v_cvt_pk_bf16_f32 v133, v118, v119\n\t" \
    "s_nop 4\n\t" \
    "v_cvt_pk_bf16_f32 v134, v120, v121\n\t" \
    "v_cvt_pk_bf16_f32 v135, v122, v123\n\t" \
    "s_nop 0\n\t" \
    "v_permlane16_swap_b32 v132, v134\n\t" \
    "v_permlane16_swap_b32 v133, v135\n\t" \
    "global_store_dwordx4 v15, v[132:135], s[38:39] sc1\n\t" \
    "global_load_dword v140, v20, s[52:53] sc1\n\t" \
    "s_cmp_eq_u32 s65, 63\n\t" \
    "s_cbranch_scc1 vv_end53_%=\n\t" \
    "s_cmp_lg_u32 s65, 0\n\t" \
    "s_cbranch_scc1 vv_ng62_%=\n\t" \
    "v_mov_b32 v160, 5838848\n\t" \
    "v_mov_b32 v161, 1\n\t" \
    "s_mov_b64 exec, 1\n\t" \
    "global_atomic_add v162, v160, v161, s[36:37] sc0 sc1\n\t" \
    "s_mov_b64 exec, -1\n\t" \
    "v_mbcnt_lo_u32_b32 v160, -1, 0\n\t" \
    "v_and_b32 v161, 3, v160\n\t" \
    "v_lshlrev_b32 v161, 16, v161\n\t" \
    "v_bfe_u32 v163, v160, 2, 2\n\t" \
    "v_lshl_add_u32 v161, v163, 11, v161\n\t" \
    "v_bfe_u32 v163, v160, 4, 1\n\t" \
    "v_lshl_add_u32 v161, v163, 14, v161\n\t" \
    "v_add_u32 v161, s63, v161\n\t" \
    "s_waitcnt vmcnt(0)\n\t" \
    "v_readfirstlane_b32 s69, v162\n\t" \
    "s_nop 1\n\t" \
    "s_cmp_lg_u32 s69, 126\n\t" \
    "s_cbranch_scc1 vv_go63_%=\n\t" \
    "s_memrealtime s[56:57]\n\t" \
    "s_waitcnt lgkmcnt(0)\n\t" \
    "s_lshl_b32 s69, s56, 4\n\t" \
    "s_add_u32 s69, s69, 2800\n\t" \
    "s_add_u32 s70, s56, 24\n\t" \
    "s_lshr_b32 s70, s70, 4\n\t" \
    "v_lshrrev_b32 v163, 2, v160\n\t" \
    "v_add_u32 v163, s70, v163\n\t" \
    "v_and_b32 v163, 63, v163\n\t" \
    "v_lshlrev_b32 v163, 6, v163\n\t" \
    "v_and_b32 v162, 3, v160\n\t" \
    "v_lshl_add_u32 v163, v162, 12, v163\n\t" \
    "v_add_u32 v163, 5865472, v163\n\t" \
    "v_mov_b32 v160, s69\n\t" \
    "s_mov_b32 exec_hi, 0\n\t" \
    "global_store_dword v163, v160, s[36:37] sc1\n\t" \
    "global_store_dword v161, v160, s[36:37] sc1\n\t" \
    "s_mov_b32 exec_hi, -1\n\t" \
    "vv_go63_%=:\n\t" \
    "vv_ng62_%=:\n\t" \
    "s_waitcnt lgkmcnt(0)\n\t" \
    "s_mov_b32 s70, 116\n\t" \
    "vv_E67_%=:\n\t" \
    "s_memtime s[56:57]\n\t" \
    "s_memrealtime s[2:3]\n\t" \
    "s_nop 0\n\t" \
    "s_memrealtime s[4:5]\n\t" \
    "s_nop 0\n\t" \
    "s_memrealtime s[6:7]\n\t" \
    "s_nop 0\n\t" \
    "s_memrealtime s[8:9]\n\t" \
    "s_nop 0\n\t" \
    "s_memrealtime s[10:11]\n\t" \
    "s_nop 0\n\t" \
    "s_memrealtime s[12:13]\n\t" \
    "s_nop 0\n\t" \
    "s_memrealtime s[14:15]\n\t" \
    "s_nop 0\n\t" \
    "s_memrealtime s[16:17]\n\t" \
    "s_nop 0\n\t" \
    "s_memrealtime s[18:19]\n\t" \
    "s_nop 0\n\t" \
    "s_memrealtime s[20:21]\n\t" \
    "s_nop 0\n\t" \
    "s_memrealtime s[22:23]\n\t" \
    "s_nop 0\n\t" \
    "s_memrealtime s[24:25]\n\t" \
    "s_nop 0\n\t" \
    "s_memrealtime s[26:27]\n\t" \
    "s_nop 0\n\t" \
    "s_memrealtime s[28:29]\n\t" \
    "s_memtime s[54:55]\n\t" \
    "s_add_u32 s70, s70, 8\n\t" \
    "s_waitcnt lgkmcnt(0)\n\t" \
    "s_sub_u32 s69, s54, s56\n\t" \
    "s_cmp_gt_u32 s69, s70\n\t" \
    "s_cbranch_scc1 vv_E67_%=\n\t" \
    "s_add_u32 s2, s2, s4\n\t" \
    "s_add_u32 s6, s6, s8\n\t" \
    "s_add_u32 s10, s10, s12\n\t" \
    "s_add_u32 s14, s14, s16\n\t" \
    "s_add_u32 s18, s18, s20\n\t" \
    "s_add_u32 s22, s22, s24\n\t" \
    "s_add_u32 s2, s2, s6\n\t" \
    "s_add_u32 s10, s10, s14\n\t" \
    "s_add_u32 s18, s18, s22\n\t" \
    "s_add_u32 s2, s2, s26\n\t" \
    "s_add_u32 s10, s10, s18\n\t" \
    "s_add_u32 s2, s2, s10\n\t" \
    "s_mul_i32 s70, s28, 13\n\t" \
    "s_sub_u32 s70, s70, s2\n\t" \
    "s_min_u32 s70, s70, 52\n\t" \
    "s_mul_i32 s69, s69, s70\n\t" \
    "s_mul_hi_u32 s69, s69, 76695844\n\t" \
    "s_mov_b32 s75, s73\n\t" \
    "s_mov_b32 s76, s74\n\t" \
    "s_add_u32 s73, s56, s69\n\t" \
    "s_lshl_b32 s74, s28, 4\n\t" \
    "s_sub_u32 s69, s73, s75\n\t" \
    "s_sub_u32 s70, s74, s76\n\t" \
    "s_sub_u32 s71, s70, 1600\n\t" \
    "s_cmp_lt_u32 s71, 400000\n\t" \
    "s_cbranch_scc0 vv_K68_%=\n\t" \
    "v_cvt_f32_u32 v160, s69\n\t" \
    "v_cvt_f32_u32 v161, s70\n\t" \
    "v_rcp_f32 v162, v160\n\t" \
    "v_rcp_f32 v163, v161\n\t" \
    "v_mul_f32 v162, v162, v161\n\t" \
    "v_mul_f32 v163, v163, v160\n\t" \
    "v_mul_f32 v162, 0x4f800000, v162\n\t" \
    "v_mul_f32 v163, 0x4f000000, v163\n\t" \
    "v_cvt_u32_f32 v162, v162\n\t" \
    "v_cvt_u32_f32 v163, v163\n\t" \
    "v_readfirstlane_b32 s69, v162\n\t" \
    "v_readfirstlane_b32 s70, v163\n\t" \
    "s_nop 1\n\t" \
    "s_min_u32 s71, s65, 1\n\t" \
    "s_sub_u32 s69, s69, s78\n\t" \
    "s_ashr_i32 s69, s69, s71\n\t" \
    "s_add_u32 s78, s78, s69\n\t" \
    "s_sub_u32 s70, s70, s77\n\t" \
    "s_ashr_i32 s70, s70, s71\n\t" \
    "s_add_u32 s77, s77, s70\n\t" \
    "vv_K68_%=:\n\t" \
    "s_add_u32 s65, s65, 1\n\t" \
    "v_mov_b32 v72, 0\n\t" \
    "v_mov_b32 v73, 0\n\t" \
    "v_mov_b32 v74, 0\n\t" \
    "v_mov_b32 v75, 0\n\t" \
    "v_mov_b32 v76, 0\n\t" \
    "v_mov_b32 v77, 0\n\t" \
    "v_mov_b32 v78, 0\n\t" \
    "v_mov_b32 v79, 0\n\t" \
    "v_mov_b32 v108, 0\n\t" \
    "v_mov_b32 v109, 0\n\t" \
    "v_mov_b32 v110, 0\n\t" \
    "v_mov_b32 v111, 0\n\t" \
    "v_mov_b32 v112, 0\n\t" \
    "v_mov_b32 v113, 0\n\t" \
    "v_mov_b32 v114, 0\n\t" \
    "v_mov_b32 v115, 0\n\t" \
    "v_mov_b32 v116, 0\n\t" \
    "v_mov_b32 v117, 0\n\t" \
    "v_mov_b32 v118, 0\n\t" \
    "v_mov_b32 v119, 0\n\t" \
    "v_mov_b32 v120, 0\n\t" \
    "v_mov_b32 v121, 0\n\t" \
    "v_mov_b32 v122, 0\n\t" \
    "v_mov_b32 v123, 0\n\t" \
    "v_mov_b32 v124, 0\n\t" \
    "v_mov_b32 v125, 0\n\t" \
    "v_mov_b32 v126, 0\n\t" \
    "v_mov_b32 v127, 0\n\t" \
    "s_add_u32 s69, s65, 0\n\t" \
    "s_and_b32 s69, s69, 7\n\t" \
    "s_lshl_b32 s69, s69, 18\n\t" \
    "s_add_u32 s69, s69, s62\n\t" \
    "s_add_u32 s46, s36, s69\n\t" \
    "s_addc_u32 s47, s37, 0\n\t" \
    "s_add_u32 s70, s65, 0\n\t" \
    "s_mul_i32 s69, s70, 171\n\t" \
    "s_lshr_b32 s69, s69, 9\n\t" \
    "s_mul_i32 s70, s70, 589824\n\t" \
    "s_mul_i32 s71, s69, 327680\n\t" \
    "s_add_u32 s70, s70, s71\n\t" \
    "s_lshl1_add_u32 s69, s69, 4\n\t" \
    "s_bitcmp1_b64 s[44:45], s69\n\t" \
    "s_cselect_b32 s69, 8192, 0\n\t" \
    "s_add_u32 s69, s69, s70\n\t" \
    "s_add_u32 s69, s69, 123731968\n\t" \
    "s_add_u32 s38, s36, s69\n\t" \
    "s_addc_u32 s39, s37, 0\n\t" \
    "s_and_b32 s69, s69, 0xffe00000\n\t" \
    "s_add_u32 s69, s69, 1823744\n\t" \
    "v_mov_b32 v17, s69\n\t" \
    "global_load_dword v17, v17, s[36:37]\n\t" \
    "s_waitcnt vmcnt(0)\n\t" \
    "v_cmp_eq_u32 vcc, -1, v140\n\t" \
    "s_cbranch_vccz vv_Hok65_%=\n\t" \
    "vv_H64_%=:\n\t" \
    "global_load_dword v140, v20, s[52:53] sc1\n\t" \
    "s_waitcnt vmcnt(0)\n\t" \
    "v_cmp_eq_u32 vcc, -1, v140\n\t" \
    "s_cbranch_vccz vv_Hc66_%=\n\t" \
    "s_cmp_eq_u32 s68, 0\n\t" \
    "s_cbranch_scc1 vv_F69_%=\n\t" \
    "s_sub_u32 s68, s68, 1\n\t" \
    "s_sleep 2\n\t" \
    "s_branch vv_H64_%=\n\t" \
    "vv_F69_%=:\n\t" \
    "v_mov_b32 v141, 5767424\n\t" \
    "v_mov_b32 v142, 6\n\t" \
    "global_store_dword v141, v142, s[36:37] sc1\n\t" \
    "s_waitcnt vmcnt(0)\n\t" \
    "s_branch vv_Hc66_%=\n\t" \
    "vv_Hc66_%=:\n\t" \
    "s_memtime s[56:57]\n\t" \
    "s_waitcnt lgkmcnt(0)\n\t" \
    "s_sub_u32 s69, s56, s73\n\t" \
    "s_cmp_lt_u32 s69, 3000\n\t" \
    "s_cbranch_scc1 vv_St70_%=\n\t" \
    "s_waitcnt lgkmcnt(0)\n\t" \
    "s_mov_b32 s70, 116\n\t" \
    "vv_E71_%=:\n\t" \
    "s_memtime s[56:57]\n\t" \
    "s_memrealtime s[2:3]\n\t" \
    "s_nop 0\n\t" \
    "s_memrealtime s[4:5]\n\t" \
    "s_nop 0\n\t" \
    "s_memrealtime s[6:7]\n\t" \
    "s_nop 0\n\t" \
    "s_memrealtime s[8:9]\n\t" \
    "s_nop 0\n\t" \
    "s_memrealtime s[10:11]\n\t" \
    "s_nop 0\n\t" \
    "s_memrealtime s[12:13]\n\t" \
    "s_nop 0\n\t" \
    "s_memrealtime s[14:15]\n\t" \
    "s_nop 0\n\t" \
    "s_memrealtime s[16:17]\n\t" \
    "s_nop 0\n\t" \
    "s_memrealtime s[18:19]\n\t" \
    "s_nop 0\n\t" \
    "s_memrealtime s[20:21]\n\t" \
    "s_nop 0\n\t" \
    "s_memrealtime s[22:23]\n\t" \
    "s_nop 0\n\t" \
    "s_memrealtime s[24:25]\n\t" \
    "s_nop 0\n\t" \
    "s_memrealtime s[26:27]\n\t" \
    "s_nop 0\n\t" \
    "s_memrealtime s[28:29]\n\t" \
    "s_memtime s[54:55]\n\t" \
    "s_add_u32 s70, s70, 8\n\t" \
    "s_waitcnt lgkmcnt(0)\n\t" \
    "s_sub_u32 s69, s54, s56\n\t" \
    "s_cmp_gt_u32 s69, s70\n\t" \
    "s_cbranch_scc1 vv_E71_%=\n\t" \
    "s_add_u32 s2, s2, s4\n\t" \
    "s_add_u32 s6, s6, s8\n\t" \
    "s_add_u32 s10, s10, s12\n\t" \
    "s_add_u32 s14, s14, s16\n\t" \
    "s_add_u32 s18, s18, s20\n\t" \
    "s_add_u32 s22, s22, s24\n\t" \
    "s_add_u32 s2, s2, s6\n\t" \
    "s_add_u32 s10, s10, s14\n\t" \
    "s_add_u32 s18, s18, s22\n\t" \
    "s_add_u32 s2, s2, s26\n\t" \
    "s_add_u32 s10, s10, s18\n\t" \
    "s_add_u32 s2, s2, s10\n\t" \
    "s_mul_i32 s70, s28, 13\n\t" \
    "s_sub_u32 s70, s70, s2\n\t" \
    "s_min_u32 s70, s70, 52\n\t" \
    "s_mul_i32 s69, s69, s70\n\t" \
    "s_mul_hi_u32 s69, s69, 76695844\n\t" \
    "s_mov_b32 s75, s73\n\t" \
    "s_mov_b32 s76, s74\n\t" \
    "s_add_u32 s73, s56, s69\n\t" \
    "s_lshl_b32 s74, s28, 4\n\t" \
    "vv_St70_%=:\n\t" \
    "vv_Hok65_%=:\n\t" \
    "v_max_u32_dpp v140, v140, v140 row_ror:1 row_mask:0xf bank_mask:0xf\n\t" \
    "s_nop 1\n\t" \
    "v_max_u32_dpp v140, v140, v140 row_ror:2 row_mask:0xf bank_mask:0xf\n\t" \
    "s_nop 1\n\t" \
    "v_max_u32_dpp v140, v140, v140 row_ror:4 row_mask:0xf bank_mask:0xf\n\t" \
    "s_nop 1\n\t" \
    "v_max_u32_dpp v140, v140, v140 row_ror:8 row_mask:0xf bank_mask:0xf\n\t" \
    "s_nop 0\n\t" \
    "v_readlane_b32 s69, v140, 0\n\t" \
    "v_readlane_b32 s71, v140, 16\n\t" \
    "s_nop 1\n\t" \
    "s_max_u32 s81, s69, s71\n\t" \
    "s_mul_hi_u32 s69, s85, s78\n\t" \
    "s_add_u32 s79, s81, s69\n\t" \
    "s_add_u32 s79, s79, s87\n\t" \
    "s_cmp_ge_u32 s65, 53\n\t" \
    "s_cselect_b32 s69, 32, 0\n\t" \
    "s_sub_u32 s79, s79, s69\n\t" \
    "s_cmp_lt_u32 s65, 10\n\t" \
    "s_cselect_b32 s69, 64, 0\n\t" \
    "s_add_u32 s79, s79, s69\n\t" \
    "s_add_u32 s80, s79, s88\n\t" \
    "s_sub_u32 s70, s79, s74\n\t" \
    "s_max_i32 s70, s70, 0\n\t" \
    "s_min_i32 s70, s70, 16000\n\t" \
    "s_lshl_b32 s70, s70, 1\n\t" \
    "s_mul_hi_u32 s70, s70, s77\n\t" \
    "s_add_u32 s70, s70, s73\n\t" \
    "v_mov_b32 v160, s70\n\t" \
    "ds_write_b32 v11, v160 offset:20480\n\t" \
    "s_and_b32 s69, s65, 7\n\t" \
    "s_lshl_b32 s69, s69, 18\n\t" \
    "s_add_u32 s69, s69, s63\n\t" \
    "s_add_u32 s52, s36, s69\n\t" \
    "s_addc_u32 s53, s37, 0\n\t" \
    "s_waitcnt lgkmcnt(0)\n\t" \
    "s_barrier\n\t" \
    "s_branch vv_loop12_%=\n\t" \
    "vv_Gbad58_%=:\n\t" \
    "s_waitcnt lgkmcnt(0)\n\t" \
    "s_nop 7\n\t" \
    "v_mov_b32 v72, 0\n\t" \
    "v_mov_b32 v73, 0\n\t" \
    "v_mov_b32 v74, 0\n\t" \
    "v_mov_b32 v75, 0\n\t" \
    "v_mov_b32 v76, 0\n\t" \
    "v_mov_b32 v77, 0\n\t" \
    "v_mov_b32 v78, 0\n\t" \
    "v_mov_b32 v79, 0\n\t" \
    "s_cmp_eq_u32 s68, 0\n\t" \
    "s_cbranch_scc1 vv_F72_%=\n\t" \
    "s_sub_u32 s68, s68, 1\n\t" \
    "s_branch vv_G57_%=\n\t" \
    "vv_F72_%=:\n\t" \
    "v_mov_b32 v140, 5767424\n\t" \
    "v_mov_b32 v141, 1\n\t" \
    "global_store_dword v140, v141, s[36:37] sc1\n\t" \
    "s_waitcnt vmcnt(0)\n\t" \
    "s_branch vv_Gc59_%=\n\t" \
    "vv_Gslow60_%=:\n\t" \
    "v_max_u32 v140, v36, v37\n\t" \
    "v_max3_u32 v140, v140, v38, v39\n\t" \
    "v_max3_u32 v140, v140, v40, v41\n\t" \
    "v_max3_u32 v140, v140, v42, v43\n\t" \
    "v_max3_u32 v140, v140, v44, v45\n\t" \
    "v_max3_u32 v140, v140, v46, v47\n\t" \
    "v_max3_u32 v140, v140, v48, v49\n\t" \
    "v_max3_u32 v140, v140, v50, v51\n\t" \
    "v_max3_u32 v140, v140, v52, v53\n\t" \
    "v_max3_u32 v140, v140, v54, v55\n\t" \
    "v_max3_u32 v140, v140, v56, v57\n\t" \
    "v_max3_u32 v140, v140, v58, v59\n\t" \
    "v_max3_u32 v140, v140, v60, v61\n\t" \
    "v_max3_u32 v140, v140, v62, v63\n\t" \
    "v_max3_u32 v140, v140, v64, v65\n\t" \
    "v_max3_u32 v140, v140, v66, v67\n\t" \
    "v_max3_u32 v140, v140, v68, v69\n\t" \
    "v_max3_u32 v140, v140, v70, v71\n\t" \
    "v_cmp_eq_u32 vcc, -1, v140\n\t" \
    "s_cbranch_vccz vv_Gback61_%=\n\t" \
    "s_waitcnt lgkmcnt(0)\n\t" \
    "v_sub_u32 v140, v1, v3\n\t" \
    "s_nop 0\n\t" \
    "v_readfirstlane_b32 s69, v140\n\t" \
    "v_add_u32 v143, 20480, v0\n\t" \
    "s_nop 7\n\t" \
    "s_add_u32 s30, s46, s69\n\t" \
    "s_addc_u32 s31, s47, 0\n\t" \
    "vv_Gag73_%=:\n\t" \
    "global_load_dwordx4 v[36:39], v1, s[46:47] sc1\n\t" \
    "global_load_dwordx4 v[40:43], v23, s[46:47] sc1\n\t" \
    "global_load_dwordx4 v[44:47], v143, s[30:31] sc1\n\t" \
    "global_load_dwordx4 v[48:51], v4, s[30:31] sc1\n\t" \
    "global_load_dwordx4 v[52:55], v5, s[30:31] sc1\n\t" \
    "global_load_dwordx4 v[56:59], v6, s[30:31] sc1\n\t" \
    "global_load_dwordx4 v[60:63], v7, s[30:31] sc1\n\t" \
    "global_load_dwordx4 v[64:67], v8, s[30:31] sc1\n\t" \
    "global_load_dwordx4 v[68:71], v9, s[30:31] sc1\n\t" \
    "s_waitcnt vmcnt(0)\n\t" \
    "v_max_u32 v140, v36, v37\n\t" \
    "v_max3_u32 v140, v140, v38, v39\n\t" \
    "v_max3_u32 v140, v140, v40, v41\n\t" \
    "v_max3_u32 v140, v140, v42, v43\n\t" \
    "v_max3_u32 v140, v140, v44, v45\n\t" \
    "v_max3_u32 v140, v140, v46, v47\n\t" \
    "v_max3_u32 v140, v140, v48, v49\n\t" \
    "v_max3_u32 v140, v140, v50, v51\n\t" \
    "v_max3_u32 v140, v140, v52, v53\n\t" \
    "v_max3_u32 v140, v140, v54, v55\n\t" \
    "v_max3_u32 v140, v140, v56, v57\n\t" \
    "v_max3_u32 v140, v140, v58, v59\n\t" \
    "v_max3_u32 v140, v140, v60, v61\n\t" \
    "v_max3_u32 v140, v140, v62, v63\n\t" \
    "v_max3_u32 v140, v140, v64, v65\n\t" \
    "v_max3_u32 v140, v140, v66, v67\n\t" \
    "v_max3_u32 v140, v140, v68, v69\n\t" \
    "v_max3_u32 v140, v140, v70, v71\n\t" \
    "v_cmp_eq_u32 vcc, -1, v140\n\t" \
    "s_cbranch_vccz vv_Gredo74_%=\n\t" \
    "s_cmp_eq_u32 s68, 0\n\t" \
    "s_cbranch_scc1 vv_F75_%=\n\t" \
    "s_sub_u32 s68, s68, 1\n\t" \
    "s_sleep 1\n\t" \
    "s_branch vv_Gag73_%=\n\t" \
    "vv_F75_%=:\n\t" \
    "v_mov_b32 v140, 5767424\n\t" \
    "v_mov_b32 v141, 2\n\t" \
    "global_store_dword v140, v141, s[36:37] sc1\n\t" \
    "s_waitcnt vmcnt(0)\n\t" \
    "s_branch vv_Gredo74_%=\n\t" \
    "vv_Gredo74_%=:\n\t" \
    "v_mov_b32 v72, 0\n\t" \
    "v_mov_b32 v73, 0\n\t" \
    "v_mov_b32 v74, 0\n\t" \
    "v_mov_b32 v75, 0\n\t" \
    "v_mov_b32 v76, 0\n\t" \
    "v_mov_b32 v77, 0\n\t" \
    "v_mov_b32 v78, 0\n\t" \
    "v_mov_b32 v79, 0\n\t" \
    "v_smfmac_f32_16x16x64_bf16 v[72:75], v[36:39], a[48:55], v10\n\t" \
    "v_smfmac_f32_16x16x64_bf16 v[72:75], v[40:43], a[56:63], v10\n\t" \
    "v_smfmac_f32_16x16x64_bf16 v[72:75], v[44:47], a[64:71], v10\n\t" \
    "v_smfmac_f32_16x16x64_bf16 v[72:75], v[48:51], a[0:7], v10\n\t" \
    "v_smfmac_f32_16x16x64_bf16 v[72:75], v[52:55], a[8:15], v10\n\t" \
    "v_smfmac_f32_16x16x64_bf16 v[72:75], v[56:59], a[16:23], v10\n\t" \
    "v_smfmac_f32_16x16x64_bf16 v[72:75], v[60:63], a[24:31], v10\n\t" \
    "v_smfmac_f32_16x16x64_bf16 v[72:75], v[64:67], a[32:39], v10\n\t" \
    "v_smfmac_f32_16x16x64_bf16 v[72:75], v[68:71], a[40:47], v10\n\t" \
    "v_smfmac_f32_16x16x64_bf16 v[76:79], v[36:39], a[120:127], v10\n\t" \
    "v_smfmac_f32_16x16x64_bf16 v[76:79], v[40:43], a[128:135], v10\n\t" \
    "v_smfmac_f32_16x16x64_bf16 v[76:79], v[44:47], a[136:143], v10\n\t" \
    "v_smfmac_f32_16x16x64_bf16 v[76:79], v[48:51], a[72:79], v10\n\t" \
    "v_smfmac_f32_16x16x64_bf16 v[76:79], v[52:55], a[80:87], v10\n\t" \
    "v_smfmac_f32_16x16x64_bf16 v[76:79], v[56:59], a[88:95], v10\n\t" \
    "v_smfmac_f32_16x16x64_bf16 v[76:79], v[60:63], a[96:103], v10\n\t" \
    "v_smfmac_f32_16x16x64_bf16 v[76:79], v[64:67], a[104:111], v10\n\t" \
    "v_smfmac_f32_16x16x64_bf16 v[76:79], v[68:71], a[112:119], v10\n\t" \
    "v_add_f32 v72, v72, v74\n\t" \
    "v_add_f32 v73, v73, v75\n\t" \
    "v_cvt_pk_bf16_f32 v80, v72, v73\n\t" \
    "ds_write_b32 v12, v80 offset:0\n\t" \
    "s_nop 3\n\t" \
    "v_pk_add_f32 v[76:77], v[76:77], v[78:79]\n\t" \
    "v_cvt_pk_bf16_f32 v81, v76, v77\n\t" \
    "ds_write_b32 v12, v81 offset:256\n\t" \
    "s_branch vv_Gback61_%=\n\t" \
    "vv_end53_%=:\n\t" \
    "s_endpgm\n\t" \
    "vv_last_%=:\n\t" \
    ""
#define ASM_R \
    "s_mov_b64 s[36:37], %0\n\t" \
    "s_mov_b64 s[42:43], %1\n\t" \
    "s_mov_b64 s[44:45], %2\n\t" \
    "s_mov_b32 s66, %3\n\t" \
    "s_mov_b32 s67, %4\n\t" \
    "s_mov_b64 s[50:51], %5\n\t" \
    "s_mov_b32 s82, %7\n\t" \
    "s_load_dwordx8 s[84:91], %6, 0\n\t" \
    "v_mbcnt_lo_u32_b32 v223, -1, 0\n\t" \
    "v_mbcnt_hi_u32_b32 v223, -1, v223\n\t" \
    "s_getpc_b64 s[46:47]\n\t" \
    "rr_pc_%=:\n\t" \
    "s_lshl_b32 s69, s66, 13\n\t" \
    "v_lshl_add_u32 v222, v223, 7, s69\n\t" \
    "v_min_u32 v222, rr_last_%=-rr_pc_%=-4, v222\n\t" \
    "global_load_dword v222, v222, s[46:47]\n\t" \
    "v_lshlrev_b32 v223, 2, v223\n\t" \
    "v_add_u32 v223, s82, v223\n\t" \
    "ds_read_b32 v0, v223 offset:0\n\t" \
    "ds_read_b32 v1, v223 offset:256\n\t" \
    "ds_read_b32 v2, v223 offset:512\n\t" \
    "ds_read_b32 v3, v223 offset:768\n\t" \
    "ds_read_b32 v4, v223 offset:1024\n\t" \
    "ds_read_b32 v5, v223 offset:1280\n\t" \
    "ds_read_b32 v6, v223 offset:1536\n\t" \
    "ds_read_b32 v7, v223 offset:1792\n\t" \
    "ds_read_b32 v8, v223 offset:2048\n\t" \
    "ds_read_b32 v9, v223 offset:2304\n\t" \
    "ds_read_b32 v10, v223 offset:2560\n\t" \
    "ds_read_b32 v11, v223 offset:2816\n\t" \
    "s_waitcnt lgkmcnt(0)\n\t" \
    "ds_read_b32 v12, v223 offset:3072\n\t" \
    "ds_read_b32 v13, v223 offset:3328\n\t" \
    "ds_read_b32 v14, v223 offset:3584\n\t" \
    "ds_read_b32 v15, v223 offset:3840\n\t" \
    "ds_read_b32 v16, v223 offset:4096\n\t" \
    "ds_read_b32 v17, v223 offset:4352\n\t" \
    "ds_read_b32 v18, v223 offset:4608\n\t" \
    "ds_read_b32 v19, v223 offset:4864\n\t" \
    "ds_read_b32 v20, v223 offset:5120\n\t" \
    "ds_read_b32 v21, v223 offset:5376\n\t" \
    "ds_read_b32 v22, v223 offset:5632\n\t" \
    "ds_read_b32 v23, v223 offset:5888\n\t" \
    "s_waitcnt lgkmcnt(0)\n\t" \
    "ds_read_b32 v24, v223 offset:6144\n\t" \
    "ds_read_b32 v25, v223 offset:6400\n\t" \
    "ds_read_b32 v26, v223 offset:6656\n\t" \
    "ds_read_b32 v27, v223 offset:6912\n\t" \
    "ds_read_b32 v28, v223 offset:7168\n\t" \
    "ds_read_b32 v29, v223 offset:7424\n\t" \
    "ds_read_b32 v30, v223 offset:7680\n\t" \
    "ds_read_b32 v31, v223 offset:7936\n\t" \
    "ds_read_b32 v32, v223 offset:8192\n\t" \
    "ds_read_b32 v33, v223 offset:8448\n\t" \
    "ds_read_b32 v34, v223 offset:8704\n\t" \
    "ds_read_b32 v35, v223 offset:8960\n\t" \
    "s_waitcnt lgkmcnt(0)\n\t" \
    "ds_read_b32 v36, v223 offset:9216\n\t" \
    "ds_read_b32 v37, v223 offset:9472\n\t" \
    "ds_read_b32 v38, v223 offset:9728\n\t" \
    "ds_read_b32 v39, v223 offset:9984\n\t" \
    "ds_read_b32 v40, v223 offset:10240\n\t" \
    "ds_read_b32 v41, v223 offset:10496\n\t" \
    "ds_read_b32 v42, v223 offset:10752\n\t" \
    "ds_read_b32 v43, v223 offset:11008\n\t" \
    "ds_read_b32 v44, v223 offset:11264\n\t" \
    "ds_read_b32 v45, v223 offset:11520\n\t" \
    "ds_read_b32 v184, v223 offset:11776\n\t" \
    "ds_read_b32 v185, v223 offset:12032\n\t" \
    "s_waitcnt lgkmcnt(0)\n\t" \
    "ds_read_b32 v186, v223 offset:12288\n\t" \
    "ds_read_b32 v187, v223 offset:12544\n\t" \
    "ds_read_b32 v188, v223 offset:12800\n\t" \
    "s_waitcnt lgkmcnt(0)\n\t" \
    "s_mov_b32 s68, 262144\n\t" \
    "s_mov_b32 s72, 0\n\t" \
    "s_mov_b32 s83, -1\n\t" \
    "s_mov_b32 s65, 0\n\t" \
    "v_xor_b32 v180, 65536, v2\n\t" \
    "v_xor_b32 v181, 131072, v2\n\t" \
    "v_xor_b32 v182, 196608, v2\n\t" \
    "v_add_u32 v189, 4096, v7\n\t" \
    "v_add_u32 v190, 16384, v7\n\t" \
    "v_add_u32 v191, 20480, v7\n\t" \
    "s_mul_i32 s69, s67, 57\n\t" \
    "s_lshr_b32 s69, s69, 9\n\t" \
    "s_mul_i32 s69, s69, 9\n\t" \
    "s_sub_u32 s69, s67, s69\n\t" \
    "s_cmp_lt_u32 s69, 4\n\t" \
    "s_cselect_b32 s70, 21, 16\n\t" \
    "s_cmp_eq_u32 s69, 8\n\t" \
    "s_cselect_b32 s70, 26, s70\n\t" \
    "s_lshr_b32 s97, s91, s70\n\t" \
    "s_and_b32 s97, s97, 31\n\t" \
    "s_min_u32 s97, s97, 0\n\t" \
    "s_lshl_b32 s97, s97, 4\n\t" \
    "s_mov_b32 s77, 3019898880\n\t" \
    "s_mov_b32 s78, 3054198966\n\t" \
    "s_waitcnt lgkmcnt(0)\n\t" \
    "s_mov_b32 s70, 116\n\t" \
    "rr_E1_%=:\n\t" \
    "s_memtime s[56:57]\n\t" \
    "s_memrealtime s[2:3]\n\t" \
    "s_nop 0\n\t" \
    "s_memrealtime s[4:5]\n\t" \
    "s_nop 0\n\t" \
    "s_memrealtime s[6:7]\n\t" \
    "s_nop 0\n\t" \
    "s_memrealtime s[8:9]\n\t" \
    "s_nop 0\n\t" \
    "s_memrealtime s[10:11]\n\t" \
    "s_nop 0\n\t" \
    "s_memrealtime s[12:13]\n\t" \
    "s_nop 0\n\t" \
    "s_memrealtime s[14:15]\n\t" \
    "s_nop 0\n\t" \
    "s_memrealtime s[16:17]\n\t" \
    "s_nop 0\n\t" \
    "s_memrealtime s[18:19]\n\t" \
    "s_nop 0\n\t" \
    "s_memrealtime s[20:21]\n\t" \
    "s_nop 0\n\t" \
    "s_memrealtime s[22:23]\n\t" \
    "s_nop 0\n\t" \
    "s_memrealtime s[24:25]\n\t" \
    "s_nop 0\n\t" \
    "s_memrealtime s[26:27]\n\t" \
    "s_nop 0\n\t" \
    "s_memrealtime s[28:29]\n\t" \
    "s_memtime s[54:55]\n\t" \
    "s_add_u32 s70, s70, 8\n\t" \
    "s_waitcnt lgkmcnt(0)\n\t" \
    "s_sub_u32 s69, s54, s56\n\t" \
    "s_cmp_gt_u32 s69, s70\n\t" \
    "s_cbranch_scc1 rr_E1_%=\n\t" \
    "s_add_u32 s2, s2, s4\n\t" \
    "s_add_u32 s6, s6, s8\n\t" \
    "s_add_u32 s10, s10, s12\n\t" \
    "s_add_u32 s14, s14, s16\n\t" \
    "s_add_u32 s18, s18, s20\n\t" \
    "s_add_u32 s22, s22, s24\n\t" \
    "s_add_u32 s2, s2, s6\n\t" \
    "s_add_u32 s10, s10, s14\n\t" \
    "s_add_u32 s18, s18, s22\n\t" \
    "s_add_u32 s2, s2, s26\n\t" \
    "s_add_u32 s10, s10, s18\n\t" \
    "s_add_u32 s2, s2, s10\n\t" \
    "s_mul_i32 s70, s28, 13\n\t" \
    "s_sub_u32 s70, s70, s2\n\t" \
    "s_min_u32 s70, s70, 52\n\t" \
    "s_mul_i32 s69, s69, s70\n\t" \
    "s_mul_hi_u32 s69, s69, 76695844\n\t" \
    "s_mov_b32 s75, s73\n\t" \
    "s_mov_b32 s76, s74\n\t" \
    "s_add_u32 s73, s56, s69\n\t" \
    "s_lshl_b32 s74, s28, 4\n\t" \
    "v_mov_b32 v172, 0xd800\n\t" \
    "v_mov_b32 v173, 0x20d800\n\t" \
    "global_load_dword v172, v172, s[36:37]\n\t" \
    "global_load_dword v173, v173, s[36:37]\n\t" \
    "s_mov_b32 s62, 0\n\t" \
    "s_mov_b32 s63, 0x200000\n\t" \
    "s_mov_b64 s[44:45], 0\n\t" \
    "v_mov_b32 v170, 0\n\t" \
    "s_mov_b32 s65, s66\n\t" \
    "s_lshl_b32 s58, s67, 14\n\t" \
    "s_sub_u32 s80, s74, 32000\n\t" \
    "s_mov_b32 s64, 1\n\t" \
    "s_mov_b32 s65, 5\n\t" \
    "s_mov_b32 s83, 99\n\t" \
    "s_add_u32 s70, s65, 59\n\t" \
    "s_mul_i32 s69, s70, 171\n\t" \
    "s_lshr_b32 s69, s69, 9\n\t" \
    "s_mul_i32 s70, s70, 589824\n\t" \
    "s_mul_i32 s71, s69, 327680\n\t" \
    "s_add_u32 s70, s70, s71\n\t" \
    "s_lshl1_add_u32 s69, s69, 4\n\t" \
    "s_bitcmp1_b64 s[44:45], s69\n\t" \
    "s_cselect_b32 s69, 8192, 0\n\t" \
    "s_add_u32 s69, s69, s70\n\t" \
    "s_add_u32 s69, s69, 123731968\n\t" \
    "s_add_u32 s69, s69, s58\n\t" \
    "s_add_u32 s40, s36, s69\n\t" \
    "s_addc_u32 s41, s37, 0\n\t" \
    "v_mov_b32 v192, 0\n\t" \
    "s_mov_b64 exec, 1\n\t" \
    "s_branch rr_act3_%=\n\t" \
    "rr_g214_%=:\n\t" \
    "s_mov_b64 exec, -1\n\t" \
    "s_waitcnt vmcnt(0)\n\t" \
    "v_mbcnt_lo_u32_b32 v164, -1, 0\n\t" \
    "v_mbcnt_hi_u32_b32 v164, -1, v164\n\t" \
    "v_lshlrev_b32 v164, 6, v164\n\t" \
    "v_add_u32 v164, 6225920, v164\n\t" \
    "v_mov_b32 v165, 6234112\n\t" \
    "global_load_dword v165, v165, s[36:37] sc1\n\t" \
    "global_load_dwordx2 v[166:167], v164, s[36:37] sc1\n\t" \
    "global_load_dword v168, v164, s[36:37] offset:2048 sc1\n\t" \
    "s_waitcnt vmcnt(0)\n\t" \
    "rr_C15_%=:\n\t" \
    "v_cmp_ne_u32 s[54:55], v166, v168\n\t" \
    "v_sub_u32 v166, v166, v165\n\t" \
    "v_cmp_ne_u32 vcc, 1, v166\n\t" \
    "s_nop 1\n\t" \
    "s_or_b64 vcc, vcc, s[54:55]\n\t" \
    "s_cbranch_scc0 rr_Cok16_%=\n\t" \
    "s_cmp_eq_u32 s68, 0\n\t" \
    "s_cbranch_scc1 rr_F18_%=\n\t" \
    "s_sub_u32 s68, s68, 1\n\t" \
    "s_sleep 6\n\t" \
    "s_branch rr_Cag17_%=\n\t" \
    "rr_F18_%=:\n\t" \
    "v_mov_b32 v168, 5767424\n\t" \
    "v_mov_b32 v169, 4\n\t" \
    "global_store_dword v168, v169, s[36:37] sc1\n\t" \
    "s_waitcnt vmcnt(0)\n\t" \
    "s_branch rr_Cok16_%=\n\t" \
    "rr_Cag17_%=:\n\t" \
    "global_load_dwordx2 v[166:167], v164, s[36:37] sc1\n\t" \
    "global_load_dword v168, v164, s[36:37] offset:2048 sc1\n\t" \
    "s_waitcnt vmcnt(0)\n\t" \
    "s_branch rr_C15_%=\n\t" \
    "rr_Cok16_%=:\n\t" \
    "s_cmp_lg_u32 s66, 0\n\t" \
    "s_cbranch_scc1 rr_Ca19_%=\n\t" \
    "v_mov_b32 v164, 5836800\n\t" \
    "s_mov_b64 exec, 1\n\t" \
    "global_atomic_add v164, v166, s[36:37] sc1\n\t" \
    "s_mov_b64 exec, -1\n\t" \
    "rr_Ca19_%=:\n\t" \
    "v_mov_b32_dpp v164, v167 quad_perm:[1,0,3,2] row_mask:0xf bank_mask:0xf\n\t" \
    "v_cmp_lt_u32 s[44:45], v164, v167\n\t" \
    "s_nop 1\n\t" \
    "s_bitcmp1_b32 s44, 0\n\t" \
    "s_cselect_b32 s62, 8192, 0\n\t" \
    "s_bitcmp1_b32 s44, 2\n\t" \
    "s_cselect_b32 s63, 8192, 0\n\t" \
    "s_or_b32 s63, s63, 0x200000\n\t" \
    "v_min_u32 v164, v164, v167\n\t" \
    "v_cmp_lt_u32 vcc, 660, v164\n\t" \
    "s_nop 1\n\t" \
    "s_cmp_lg_u64 vcc, 0\n\t" \
    "s_cselect_b32 s69, 1, 0\n\t" \
    "s_mul_i32 s70, s69, 160\n\t" \
    "s_add_u32 s86, s86, s70\n\t" \
    "s_mul_i32 s70, s69, 536\n\t" \
    "s_add_u32 s87, s87, s70\n\t" \
    "s_mul_i32 s70, s69, 256\n\t" \
    "s_add_u32 s88, s88, s70\n\t" \
    "v_mov_b32 v172, -1\n\t" \
    "v_mov_b32 v173, -1\n\t" \
    "v_mov_b32 v174, -1\n\t" \
    "v_mov_b32 v175, -1\n\t" \
    "v_mov_b32 v176, 32000\n\t" \
    "s_sub_u32 s69, s63, s62\n\t" \
    "s_sub_u32 s69, s69, 0x200000\n\t" \
    "v_bfe_u32 v164, v187, 21, 1\n\t" \
    "v_mad_i32_i24 v187, v164, s69, v187\n\t" \
    "v_bfe_u32 v164, v188, 21, 1\n\t" \
    "v_mad_i32_i24 v188, v164, s69, v188\n\t" \
    "s_mov_b32 s64, 0\n\t" \
    "s_mov_b32 s65, s66\n\t" \
    "s_mov_b32 s83, 1\n\t" \
    "s_mov_b32 s77, 3154116608\n\t" \
    "rr_pf9_%=:\n\t" \
    "s_cmp_gt_u32 s65, 7\n\t" \
    "s_cselect_b32 s83, 99, s83\n\t" \
    "rr_WF20_%=:\n\t" \
    "s_cmp_ge_i32 s83, s65\n\t" \
    "s_cbranch_scc1 rr_WFok21_%=\n\t" \
    "global_load_dword v164, v6, s[36:37] sc1\n\t" \
    "s_waitcnt vmcnt(0)\n\t" \
    "v_min_i32_dpp v164, v164, v164 row_ror:1 row_mask:0xf bank_mask:0xf\n\t" \
    "s_nop 1\n\t" \
    "v_min_i32_dpp v164, v164, v164 row_ror:2 row_mask:0xf bank_mask:0xf\n\t" \
    "s_nop 1\n\t" \
    "v_min_i32_dpp v164, v164, v164 row_ror:4 row_mask:0xf bank_mask:0xf\n\t" \
    "s_nop 1\n\t" \
    "v_min_i32_dpp v164, v164, v164 row_ror:8 row_mask:0xf bank_mask:0xf\n\t" \
    "s_nop 0\n\t" \
    "v_readlane_b32 s69, v164, 0\n\t" \
    "v_readlane_b32 s70, v164, 16\n\t" \
    "s_nop 1\n\t" \
    "s_min_i32 s83, s69, s70\n\t" \
    "s_cmp_ge_i32 s83, s65\n\t" \
    "s_cbranch_scc1 rr_WFok21_%=\n\t" \
    "s_cmp_eq_u32 s68, 0\n\t" \
    "s_cbranch_scc1 rr_F22_%=\n\t" \
    "s_sub_u32 s68, s68, 1\n\t" \
    "s_sleep 2\n\t" \
    "s_branch rr_WF20_%=\n\t" \
    "rr_F22_%=:\n\t" \
    "v_mov_b32 v164, 5767424\n\t" \
    "v_mov_b32 v165, 10\n\t" \
    "global_store_dword v164, v165, s[36:37] sc1\n\t" \
    "s_waitcnt vmcnt(0)\n\t" \
    "s_branch rr_WFok21_%=\n\t" \
    "rr_WFok21_%=:\n\t" \
    "s_mul_i32 s70, s67, 7\n\t" \
    "s_add_u32 s69, s65, 0\n\t" \
    "s_xor_b32 s69, s69, s70\n\t" \
    "s_and_b32 s69, s69, 63\n\t" \
    "s_lshl_b32 s69, s69, 15\n\t" \
    "s_mul_i32 s70, s67, 2097152\n\t" \
    "s_add_u32 s69, s69, s70\n\t" \
    "s_add_u32 s69, s69, 48234496\n\t" \
    "s_add_u32 s38, s36, s69\n\t" \
    "s_addc_u32 s39, s37, 0\n\t" \
    "s_add_u32 s70, s65, 0\n\t" \
    "s_mul_i32 s69, s70, 171\n\t" \
    "s_lshr_b32 s69, s69, 9\n\t" \
    "s_mul_i32 s70, s70, 589824\n\t" \
    "s_mul_i32 s71, s69, 327680\n\t" \
    "s_add_u32 s70, s70, s71\n\t" \
    "s_lshl1_add_u32 s69, s69, 4\n\t" \
    "s_bitcmp1_b64 s[44:45], s69\n\t" \
    "s_cselect_b32 s69, 8192, 0\n\t" \
    "s_add_u32 s69, s69, s70\n\t" \
    "s_add_u32 s69, s69, 123731968\n\t" \
    "s_add_u32 s69, s69, s58\n\t" \
    "s_add_u32 s40, s36, s69\n\t" \
    "s_addc_u32 s41, s37, 0\n\t" \
    "s_and_b32 s69, s69, 0xffe00000\n\t" \
    "s_add_u32 s69, s69, 1823744\n\t" \
    "v_mov_b32 v168, s69\n\t" \
    "global_load_dword v168, v168, s[36:37]\n\t" \
    "v_mov_b32 v164, s83\n\t" \
    "s_add_u32 s69, s65, 2\n\t" \
    "s_cmp_ge_i32 s83, s69\n\t" \
    "s_cbranch_scc1 rr_nq23_%=\n\t" \
    "global_load_dword v164, v6, s[36:37] sc1\n\t" \
    "rr_nq23_%=:\n\t" \
    "s_cmp_gt_u32 s65, 61\n\t" \
    "s_cbranch_scc1 rr_idle2_%=\n\t" \
    "s_add_u32 s69, s65, 2\n\t" \
    "s_and_b32 s69, s69, 7\n\t" \
    "s_lshl_b32 s69, s69, 18\n\t" \
    "s_add_u32 s69, s69, s62\n\t" \
    "s_add_u32 s46, s36, s69\n\t" \
    "s_addc_u32 s47, s37, 0\n\t" \
    "global_store_dwordx4 v184, v[172:175], s[46:47] sc1\n\t" \
    "global_store_dwordx4 v185, v[172:175], s[46:47] sc1\n\t" \
    "global_store_dwordx4 v186, v[172:175], s[46:47] sc1\n\t" \
    "global_store_dwordx4 v187, v[172:175], s[46:47] sc1\n\t" \
    "global_store_dwordx4 v188, v[172:175], s[46:47] sc1\n\t" \
    "rr_idle2_%=:\n\t" \
    "v_mov_b32 v200, 0\n\t" \
    "s_cmp_lg_u32 s67, 35\n\t" \
    "s_cbranch_scc1 rr_cke24_%=\n\t" \
    "s_cmp_lt_u32 s65, 6\n\t" \
    "s_cbranch_scc1 rr_cke24_%=\n\t" \
    "s_lshl_b32 s69, s65, 7\n\t" \
    "s_add_u32 s69, s69, 5853184\n\t" \
    "v_mov_b32 v201, s69\n\t" \
    "global_load_dword v200, v201, s[36:37] sc1\n\t" \
    "rr_cke24_%=:\n\t" \
    "s_cmp_gt_u32 s65, 62\n\t" \
    "s_cbranch_scc1 rr_n225_%=\n\t" \
    "s_cmp_lt_u32 s65, 2\n\t" \
    "s_cbranch_scc1 rr_nw226_%=\n\t" \
    "s_mul_hi_u32 s69, s85, s78\n\t" \
    "s_add_u32 s79, s81, s69\n\t" \
    "s_add_u32 s79, s79, s87\n\t" \
    "s_cmp_ge_u32 s65, 54\n\t" \
    "s_cselect_b32 s69, 32, 0\n\t" \
    "s_sub_u32 s79, s79, s69\n\t" \
    "s_cmp_lt_u32 s65, 11\n\t" \
    "s_cselect_b32 s69, 64, 0\n\t" \
    "s_add_u32 s79, s79, s69\n\t" \
    "s_add_u32 s80, s79, s88\n\t" \
    "s_cmp_gt_u32 s65, 8\n\t" \
    "s_cselect_b32 s71, 240, 64\n\t" \
    "s_add_u32 s71, s71, s79\n\t" \
    "s_sub_u32 s70, s71, s74\n\t" \
    "s_max_i32 s70, s70, 0\n\t" \
    "s_min_i32 s70, s70, 16000\n\t" \
    "s_lshl_b32 s70, s70, 1\n\t" \
    "s_mul_hi_u32 s70, s70, s77\n\t" \
    "s_add_u32 s70, s70, s73\n\t" \
    "rr_W27_%=:\n\t" \
    "s_memtime s[56:57]\n\t" \
    "s_waitcnt lgkmcnt(0)\n\t" \
    "s_sub_u32 s69, s70, s56\n\t" \
    "s_cmp_gt_i32 s69, 24\n\t" \
    "s_cbranch_scc1 rr_W27_%=\n\t" \
    "s_branch rr_g113_%=\n\t" \
    "rr_nw226_%=:\n\t" \
    "s_mov_b32 s96, 0xaaaaaaaa\n\t" \
    "v_mov_b32 v166, -1\n\t" \
    "v_readfirstlane_b32 s71, v7\n\t" \
    "s_nop 1\n\t" \
    "s_lshr_b32 s71, s71, 4\n\t" \
    "s_add_u32 s71, s71, 5865472\n\t" \
    "rr_g012_%=:\n\t" \
    "s_memrealtime s[54:55]\n\t" \
    "s_waitcnt lgkmcnt(0)\n\t" \
    "s_bfe_u32 s69, s54, 0x60004\n\t" \
    "s_lshl_b32 s69, s69, 6\n\t" \
    "s_add_u32 s69, s69, s71\n\t" \
    "v_mov_b32 v165, s69\n\t" \
    "global_load_dword v169, v165, s[36:37] sc1\n\t" \
    "s_and_b32 s69, s68, 63\n\t" \
    "s_cbranch_scc1 rr_gf28_%=\n\t" \
    "global_load_dword v166, v6, s[36:37] sc1\n\t" \
    "rr_gf28_%=:\n\t" \
    "s_waitcnt vmcnt(0)\n\t" \
    "v_cmp_ne_u32 s[30:31], -1, v169\n\t" \
    "v_cmp_le_i32 vcc, s65, v166\n\t" \
    "s_nop 1\n\t" \
    "s_lshr_b32 s69, vcc_lo, 1\n\t" \
    "s_and_b32 s69, s69, vcc_lo\n\t" \
    "s_or_b32 s69, s69, s30\n\t" \
    "s_andn2_b32 s69, s69, s96\n\t" \
    "s_cbranch_scc0 rr_gs29_%=\n\t" \
    "s_or_b32 s96, s96, s69\n\t" \
    "s_cmp_eq_u32 s66, 0\n\t" \
    "s_cbranch_scc1 rr_w030_%=\n\t" \
    "s_sleep 8\n\t" \
    "rr_w030_%=:\n\t" \
    "s_bitcmp0_b32 s69, 0\n\t" \
    "s_cbranch_scc1 rr_gk31_%=\n\t" \
    "global_load_dwordx4 a[0:3], v26, s[38:39] offset:0 sc1\n\t" \
    "global_load_dwordx4 a[4:7], v26, s[38:39] offset:1024 sc1\n\t" \
    "rr_gk31_%=:\n\t" \
    "s_bitcmp0_b32 s69, 2\n\t" \
    "s_cbranch_scc1 rr_gk32_%=\n\t" \
    "global_load_dwordx4 a[8:11], v27, s[38:39] offset:0 sc1\n\t" \
    "global_load_dwordx4 a[12:15], v27, s[38:39] offset:1024 sc1\n\t" \
    "rr_gk32_%=:\n\t" \
    "s_bitcmp0_b32 s69, 4\n\t" \
    "s_cbranch_scc1 rr_gk33_%=\n\t" \
    "global_load_dwordx4 a[16:19], v28, s[38:39] offset:0 sc1\n\t" \
    "global_load_dwordx4 a[20:23], v28, s[38:39] offset:1024 sc1\n\t" \
    "rr_gk33_%=:\n\t" \
    "s_bitcmp0_b32 s69, 6\n\t" \
    "s_cbranch_scc1 rr_gk34_%=\n\t" \
    "global_load_dwordx4 a[24:27], v29, s[38:39] offset:0 sc1\n\t" \
    "global_load_dwordx4 a[28:31], v29, s[38:39] offset:1024 sc1\n\t" \
    "rr_gk34_%=:\n\t" \
    "s_bitcmp0_b32 s69, 8\n\t" \
    "s_cbranch_scc1 rr_gk35_%=\n\t" \
    "global_load_dwordx4 a[32:35], v30, s[38:39] offset:0 sc1\n\t" \
    "global_load_dwordx4 a[36:39], v30, s[38:39] offset:1024 sc1\n\t" \
    "rr_gk35_%=:\n\t" \
    "s_bitcmp0_b32 s69, 10\n\t" \
    "s_cbranch_scc1 rr_gk36_%=\n\t" \
    "global_load_dwordx4 a[40:43], v31, s[38:39] offset:0 sc1\n\t" \
    "global_load_dwordx4 a[44:47], v31, s[38:39] offset:1024 sc1\n\t" \
    "rr_gk36_%=:\n\t" \
    "s_bitcmp0_b32 s69, 12\n\t" \
    "s_cbranch_scc1 rr_gk37_%=\n\t" \
    "global_load_dwordx4 a[48:51], v32, s[38:39] offset:0 sc1\n\t" \
    "global_load_dwordx4 a[52:55], v32, s[38:39] offset:1024 sc1\n\t" \
    "rr_gk37_%=:\n\t" \
    "s_bitcmp0_b32 s69, 14\n\t" \
    "s_cbranch_scc1 rr_gk38_%=\n\t" \
    "global_load_dwordx4 a[56:59], v33, s[38:39] offset:0 sc1\n\t" \
    "global_load_dwordx4 a[60:63], v33, s[38:39] offset:1024 sc1\n\t" \
    "rr_gk38_%=:\n\t" \
    "s_bitcmp0_b32 s69, 16\n\t" \
    "s_cbranch_scc1 rr_gk39_%=\n\t" \
    "global_load_dwordx4 a[64:67], v34, s[38:39] offset:0 sc1\n\t" \
    "global_load_dwordx4 a[68:71], v34, s[38:39] offset:1024 sc1\n\t" \
    "rr_gk39_%=:\n\t" \
    "s_bitcmp0_b32 s69, 18\n\t" \
    "s_cbranch_scc1 rr_gk40_%=\n\t" \
    "global_load_dwordx4 a[72:75], v35, s[38:39] offset:0 sc1\n\t" \
    "global_load_dwordx4 a[76:79], v35, s[38:39] offset:1024 sc1\n\t" \
    "rr_gk40_%=:\n\t" \
    "s_bitcmp0_b32 s69, 20\n\t" \
    "s_cbranch_scc1 rr_gk41_%=\n\t" \
    "global_load_dwordx4 a[80:83], v36, s[38:39] offset:0 sc1\n\t" \
    "global_load_dwordx4 a[84:87], v36, s[38:39] offset:1024 sc1\n\t" \
    "rr_gk41_%=:\n\t" \
    "s_bitcmp0_b32 s69, 22\n\t" \
    "s_cbranch_scc1 rr_gk42_%=\n\t" \
    "global_load_dwordx4 a[88:91], v37, s[38:39] offset:0 sc1\n\t" \
    "global_load_dwordx4 a[92:95], v37, s[38:39] offset:1024 sc1\n\t" \
    "rr_gk42_%=:\n\t" \
    "s_bitcmp0_b32 s69, 24\n\t" \
    "s_cbranch_scc1 rr_gk43_%=\n\t" \
    "global_load_dwordx4 a[96:99], v38, s[38:39] offset:0 sc1\n\t" \
    "global_load_dwordx4 a[100:103], v38, s[38:39] offset:1024 sc1\n\t" \
    "rr_gk43_%=:\n\t" \
    "s_bitcmp0_b32 s69, 26\n\t" \
    "s_cbranch_scc1 rr_gk44_%=\n\t" \
    "global_load_dwordx4 a[104:107], v39, s[38:39] offset:0 sc1\n\t" \
    "global_load_dwordx4 a[108:111], v39, s[38:39] offset:1024 sc1\n\t" \
    "rr_gk44_%=:\n\t" \
    "s_bitcmp0_b32 s69, 28\n\t" \
    "s_cbranch_scc1 rr_gk45_%=\n\t" \
    "global_load_dwordx4 a[112:115], v40, s[38:39] offset:0 sc1\n\t" \
    "global_load_dwordx4 a[116:119], v40, s[38:39] offset:1024 sc1\n\t" \
    "rr_gk45_%=:\n\t" \
    "s_bitcmp0_b32 s69, 30\n\t" \
    "s_cbranch_scc1 rr_gk46_%=\n\t" \
    "global_load_dwordx4 a[120:123], v41, s[38:39] offset:0 sc1\n\t" \
    "global_load_dwordx4 a[124:127], v41, s[38:39] offset:1024 sc1\n\t" \
    "rr_gk46_%=:\n\t" \
    "s_cmp_eq_u32 s96, -1\n\t" \
    "s_cbranch_scc1 rr_n225_%=\n\t" \
    "rr_gs29_%=:\n\t" \
    "s_cmp_eq_u32 s68, 0\n\t" \
    "s_cbranch_scc1 rr_F47_%=\n\t" \
    "s_sub_u32 s68, s68, 1\n\t" \
    "s_sleep 1\n\t" \
    "s_branch rr_g012_%=\n\t" \
    "rr_F47_%=:\n\t" \
    "v_mov_b32 v167, 5767424\n\t" \
    "v_mov_b32 v168, 16\n\t" \
    "global_store_dword v167, v168, s[36:37] sc1\n\t" \
    "s_waitcnt vmcnt(0)\n\t" \
    "s_branch rr_n225_%=\n\t" \
    "rr_g113_%=:\n\t" \
    "global_load_dwordx4 a[0:3], v26, s[38:39] offset:0 sc1\n\t" \
    "global_load_dwordx4 a[4:7], v26, s[38:39] offset:1024 sc1\n\t" \
    "global_load_dwordx4 a[8:11], v27, s[38:39] offset:0 sc1\n\t" \
    "global_load_dwordx4 a[12:15], v27, s[38:39] offset:1024 sc1\n\t" \
    "global_load_dwordx4 a[16:19], v28, s[38:39] offset:0 sc1\n\t" \
    "global_load_dwordx4 a[20:23], v28, s[38:39] offset:1024 sc1\n\t" \
    "global_load_dwordx4 a[24:27], v29, s[38:39] offset:0 sc1\n\t" \
    "global_load_dwordx4 a[28:31], v29, s[38:39] offset:1024 sc1\n\t" \
    "global_load_dwordx4 a[32:35], v30, s[38:39] offset:0 sc1\n\t" \
    "global_load_dwordx4 a[36:39], v30, s[38:39] offset:1024 sc1\n\t" \
    "global_load_dwordx4 a[40:43], v31, s[38:39] offset:0 sc1\n\t" \
    "global_load_dwordx4 a[44:47], v31, s[38:39] offset:1024 sc1\n\t" \
    "global_load_dwordx4 a[48:51], v32, s[38:39] offset:0 sc1\n\t" \
    "global_load_dwordx4 a[52:55], v32, s[38:39] offset:1024 sc1\n\t" \
    "global_load_dwordx4 a[56:59], v33, s[38:39] offset:0 sc1\n\t" \
    "global_load_dwordx4 a[60:63], v33, s[38:39] offset:1024 sc1\n\t" \
    "global_load_dwordx4 a[64:67], v34, s[38:39] offset:0 sc1\n\t" \
    "global_load_dwordx4 a[68:71], v34, s[38:39] offset:1024 sc1\n\t" \
    "global_load_dwordx4 a[72:75], v35, s[38:39] offset:0 sc1\n\t" \
    "global_load_dwordx4 a[76:79], v35, s[38:39] offset:1024 sc1\n\t" \
    "global_load_dwordx4 a[80:83], v36, s[38:39] offset:0 sc1\n\t" \
    "global_load_dwordx4 a[84:87], v36, s[38:39] offset:1024 sc1\n\t" \
    "global_load_dwordx4 a[88:91], v37, s[38:39] offset:0 sc1\n\t" \
    "global_load_dwordx4 a[92:95], v37, s[38:39] offset:1024 sc1\n\t" \
    "global_load_dwordx4 a[96:99], v38, s[38:39] offset:0 sc1\n\t" \
    "global_load_dwordx4 a[100:103], v38, s[38:39] offset:1024 sc1\n\t" \
    "global_load_dwordx4 a[104:107], v39, s[38:39] offset:0 sc1\n\t" \
    "global_load_dwordx4 a[108:111], v39, s[38:39] offset:1024 sc1\n\t" \
    "global_load_dwordx4 a[112:115], v40, s[38:39] offset:0 sc1\n\t" \
    "global_load_dwordx4 a[116:119], v40, s[38:39] offset:1024 sc1\n\t" \
    "global_load_dwordx4 a[120:123], v41, s[38:39] offset:0 sc1\n\t" \
    "global_load_dwordx4 a[124:127], v41, s[38:39] offset:1024 sc1\n\t" \
    "rr_n225_%=:\n\t" \
    "s_cmp_eq_u32 s65, 0\n\t" \
    "s_cbranch_scc1 rr_act3_%=\n\t" \
    "s_waitcnt vmcnt(0)\n\t" \
    "rr_ckl49_%=:\n\t" \
    "v_cmp_eq_u32 vcc, -1, v200\n\t" \
    "s_cbranch_vccz rr_ck48_%=\n\t" \
    "global_load_dword v200, v201, s[36:37] sc1\n\t" \
    "s_waitcnt vmcnt(0)\n\t" \
    "s_cmp_eq_u32 s68, 0\n\t" \
    "s_cbranch_scc1 rr_F50_%=\n\t" \
    "s_sub_u32 s68, s68, 1\n\t" \
    "s_sleep 1\n\t" \
    "s_branch rr_ckl49_%=\n\t" \
    "rr_F50_%=:\n\t" \
    "v_mov_b32 v166, 5767424\n\t" \
    "v_mov_b32 v167, 15\n\t" \
    "global_store_dword v166, v167, s[36:37] sc1\n\t" \
    "s_waitcnt vmcnt(0)\n\t" \
    "s_branch rr_ck48_%=\n\t" \
    "rr_ck48_%=:\n\t" \
    "v_min_i32_dpp v164, v164, v164 row_ror:1 row_mask:0xf bank_mask:0xf\n\t" \
    "s_nop 1\n\t" \
    "v_min_i32_dpp v164, v164, v164 row_ror:2 row_mask:0xf bank_mask:0xf\n\t" \
    "s_nop 1\n\t" \
    "v_min_i32_dpp v164, v164, v164 row_ror:4 row_mask:0xf bank_mask:0xf\n\t" \
    "s_nop 1\n\t" \
    "v_min_i32_dpp v164, v164, v164 row_ror:8 row_mask:0xf bank_mask:0xf\n\t" \
    "s_nop 0\n\t" \
    "v_readlane_b32 s69, v164, 0\n\t" \
    "v_readlane_b32 s70, v164, 16\n\t" \
    "s_nop 1\n\t" \
    "s_min_i32 s69, s69, s70\n\t" \
    "s_max_i32 s83, s83, s69\n\t" \
    "s_cmp_eq_u32 s65, 0\n\t" \
    "s_cbranch_scc1 rr_act3_%=\n\t" \
    "s_barrier\n\t" \
    "s_cmp_gt_u32 s65, 62\n\t" \
    "s_cbranch_scc1 rr_end4_%=\n\t" \
    "ds_read_b32 v164, v9\n\t" \
    "s_waitcnt lgkmcnt(0)\n\t" \
    "v_readfirstlane_b32 s81, v164\n\t" \
    "s_nop 1\n\t" \
    "s_cmp_lg_u32 s65, 1\n\t" \
    "s_cbranch_scc1 rr_n151_%=\n\t" \
    "s_waitcnt lgkmcnt(0)\n\t" \
    "s_mov_b32 s70, 116\n\t" \
    "rr_E52_%=:\n\t" \
    "s_memtime s[56:57]\n\t" \
    "s_memrealtime s[2:3]\n\t" \
    "s_nop 0\n\t" \
    "s_memrealtime s[4:5]\n\t" \
    "s_nop 0\n\t" \
    "s_memrealtime s[6:7]\n\t" \
    "s_nop 0\n\t" \
    "s_memrealtime s[8:9]\n\t" \
    "s_nop 0\n\t" \
    "s_memrealtime s[10:11]\n\t" \
    "s_nop 0\n\t" \
    "s_memrealtime s[12:13]\n\t" \
    "s_nop 0\n\t" \
    "s_memrealtime s[14:15]\n\t" \
    "s_nop 0\n\t" \
    "s_memrealtime s[16:17]\n\t" \
    "s_nop 0\n\t" \
    "s_memrealtime s[18:19]\n\t" \
    "s_nop 0\n\t" \
    "s_memrealtime s[20:21]\n\t" \
    "s_nop 0\n\t" \
    "s_memrealtime s[22:23]\n\t" \
    "s_nop 0\n\t" \
    "s_memrealtime s[24:25]\n\t" \
    "s_nop 0\n\t" \
    "s_memrealtime s[26:27]\n\t" \
    "s_nop 0\n\t" \
    "s_memrealtime s[28:29]\n\t" \
    "s_memtime s[54:55]\n\t" \
    "s_add_u32 s70, s70, 8\n\t" \
    "s_waitcnt lgkmcnt(0)\n\t" \
    "s_sub_u32 s69, s54, s56\n\t" \
    "s_cmp_gt_u32 s69, s70\n\t" \
    "s_cbranch_scc1 rr_E52_%=\n\t" \
    "s_add_u32 s2, s2, s4\n\t" \
    "s_add_u32 s6, s6, s8\n\t" \
    "s_add_u32 s10, s10, s12\n\t" \
    "s_add_u32 s14, s14, s16\n\t" \
    "s_add_u32 s18, s18, s20\n\t" \
    "s_add_u32 s22, s22, s24\n\t" \
    "s_add_u32 s2, s2, s6\n\t" \
    "s_add_u32 s10, s10, s14\n\t" \
    "s_add_u32 s18, s18, s22\n\t" \
    "s_add_u32 s2, s2, s26\n\t" \
    "s_add_u32 s10, s10, s18\n\t" \
    "s_add_u32 s2, s2, s10\n\t" \
    "s_mul_i32 s70, s28, 13\n\t" \
    "s_sub_u32 s70, s70, s2\n\t" \
    "s_min_u32 s70, s70, 52\n\t" \
    "s_mul_i32 s69, s69, s70\n\t" \
    "s_mul_hi_u32 s69, s69, 76695844\n\t" \
    "s_mov_b32 s75, s73\n\t" \
    "s_mov_b32 s76, s74\n\t" \
    "s_add_u32 s73, s56, s69\n\t" \
    "s_lshl_b32 s74, s28, 4\n\t" \
    "rr_n151_%=:\n\t" \
    "s_mul_hi_u32 s69, s85, s78\n\t" \
    "s_add_u32 s79, s81, s69\n\t" \
    "s_add_u32 s79, s79, s87\n\t" \
    "s_cmp_ge_u32 s65, 53\n\t" \
    "s_cselect_b32 s69, 32, 0\n\t" \
    "s_sub_u32 s79, s79, s69\n\t" \
    "s_cmp_lt_u32 s65, 10\n\t" \
    "s_cselect_b32 s69, 64, 0\n\t" \
    "s_add_u32 s79, s79, s69\n\t" \
    "s_add_u32 s80, s79, s88\n\t" \
    "s_sub_u32 s70, s79, s74\n\t" \
    "s_max_i32 s70, s70, 0\n\t" \
    "s_min_i32 s70, s70, 16000\n\t" \
    "s_lshl_b32 s70, s70, 1\n\t" \
    "s_mul_hi_u32 s70, s70, s77\n\t" \
    "s_add_u32 s70, s70, s73\n\t" \
    "rr_W53_%=:\n\t" \
    "s_memtime s[56:57]\n\t" \
    "s_waitcnt lgkmcnt(0)\n\t" \
    "s_sub_u32 s69, s70, s56\n\t" \
    "s_cmp_gt_i32 s69, 24\n\t" \
    "s_cbranch_scc1 rr_W53_%=\n\t" \
    "rr_act3_%=:\n\t" \
    "s_add_u32 s69, s65, 0\n\t" \
    "s_and_b32 s69, s69, 7\n\t" \
    "s_lshl_b32 s69, s69, 18\n\t" \
    "s_add_u32 s69, s69, s62\n\t" \
    "s_add_u32 s46, s36, s69\n\t" \
    "s_addc_u32 s47, s37, 0\n\t" \
    "s_add_u32 s69, s65, 1\n\t" \
    "s_and_b32 s69, s69, 7\n\t" \
    "s_lshl_b32 s69, s69, 18\n\t" \
    "s_add_u32 s69, s69, s62\n\t" \
    "s_add_u32 s48, s36, s69\n\t" \
    "s_addc_u32 s49, s37, 0\n\t" \
    "s_and_b32 s69, s65, 7\n\t" \
    "s_lshl_b32 s69, s69, 18\n\t" \
    "s_add_u32 s69, s69, s63\n\t" \
    "s_add_u32 s52, s36, s69\n\t" \
    "s_addc_u32 s53, s37, 0\n\t" \
    "s_cmp_eq_u32 s65, 0\n\t" \
    "s_cbranch_scc1 rr_nf54_%=\n\t" \
    "s_add_u32 s69, s65, 2\n\t" \
    "s_cmp_ge_i32 s83, s69\n\t" \
    "s_cbranch_scc1 rr_nf54_%=\n\t" \
    "global_load_dword v164, v6, s[36:37] sc1\n\t" \
    "s_waitcnt vmcnt(0)\n\t" \
    "v_min_i32_dpp v164, v164, v164 row_ror:1 row_mask:0xf bank_mask:0xf\n\t" \
    "s_nop 1\n\t" \
    "v_min_i32_dpp v164, v164, v164 row_ror:2 row_mask:0xf bank_mask:0xf\n\t" \
    "s_nop 1\n\t" \
    "v_min_i32_dpp v164, v164, v164 row_ror:4 row_mask:0xf bank_mask:0xf\n\t" \
    "s_nop 1\n\t" \
    "v_min_i32_dpp v164, v164, v164 row_ror:8 row_mask:0xf bank_mask:0xf\n\t" \
    "s_nop 0\n\t" \
    "v_readlane_b32 s69, v164, 0\n\t" \
    "v_readlane_b32 s70, v164, 16\n\t" \
    "s_nop 1\n\t" \
    "s_min_i32 s83, s69, s70\n\t" \
    "rr_nf54_%=:\n\t" \
    "s_waitcnt lgkmcnt(0)\n\t" \
    "s_mov_b32 s70, 116\n\t" \
    "rr_E55_%=:\n\t" \
    "s_memtime s[56:57]\n\t" \
    "s_memrealtime s[2:3]\n\t" \
    "s_nop 0\n\t" \
    "s_memrealtime s[4:5]\n\t" \
    "s_nop 0\n\t" \
    "s_memrealtime s[6:7]\n\t" \
    "s_nop 0\n\t" \
    "s_memrealtime s[8:9]\n\t" \
    "s_nop 0\n\t" \
    "s_memrealtime s[10:11]\n\t" \
    "s_nop 0\n\t" \
    "s_memrealtime s[12:13]\n\t" \
    "s_nop 0\n\t" \
    "s_memrealtime s[14:15]\n\t" \
    "s_nop 0\n\t" \
    "s_memrealtime s[16:17]\n\t" \
    "s_nop 0\n\t" \
    "s_memrealtime s[18:19]\n\t" \
    "s_nop 0\n\t" \
    "s_memrealtime s[20:21]\n\t" \
    "s_nop 0\n\t" \
    "s_memrealtime s[22:23]\n\t" \
    "s_nop 0\n\t" \
    "s_memrealtime s[24:25]\n\t" \
    "s_nop 0\n\t" \
    "s_memrealtime s[26:27]\n\t" \
    "s_nop 0\n\t" \
    "s_memrealtime s[28:29]\n\t" \
    "s_memtime s[54:55]\n\t" \
    "s_add_u32 s70, s70, 8\n\t" \
    "s_waitcnt lgkmcnt(0)\n\t" \
    "s_sub_u32 s69, s54, s56\n\t" \
    "s_cmp_gt_u32 s69, s70\n\t" \
    "s_cbranch_scc1 rr_E55_%=\n\t" \
    "s_add_u32 s2, s2, s4\n\t" \
    "s_add_u32 s6, s6, s8\n\t" \
    "s_add_u32 s10, s10, s12\n\t" \
    "s_add_u32 s14, s14, s16\n\t" \
    "s_add_u32 s18, s18, s20\n\t" \
    "s_add_u32 s22, s22, s24\n\t" \
    "s_add_u32 s2, s2, s6\n\t" \
    "s_add_u32 s10, s10, s14\n\t" \
    "s_add_u32 s18, s18, s22\n\t" \
    "s_add_u32 s2, s2, s26\n\t" \
    "s_add_u32 s10, s10, s18\n\t" \
    "s_add_u32 s2, s2, s10\n\t" \
    "s_mul_i32 s70, s28, 13\n\t" \
    "s_sub_u32 s70, s70, s2\n\t" \
    "s_min_u32 s70, s70, 52\n\t" \
    "s_mul_i32 s69, s69, s70\n\t" \
    "s_mul_hi_u32 s69, s69, 76695844\n\t" \
    "s_mov_b32 s75, s73\n\t" \
    "s_mov_b32 s76, s74\n\t" \
    "s_add_u32 s73, s56, s69\n\t" \
    "s_lshl_b32 s74, s28, 4\n\t" \
    "s_sub_u32 s69, s73, s75\n\t" \
    "s_sub_u32 s70, s74, s76\n\t" \
    "s_sub_u32 s71, s70, 1600\n\t" \
    "s_cmp_lt_u32 s71, 400000\n\t" \
    "s_cbranch_scc0 rr_K56_%=\n\t" \
    "v_cvt_f32_u32 v164, s69\n\t" \
    "v_cvt_f32_u32 v165, s70\n\t" \
    "v_rcp_f32 v166, v164\n\t" \
    "v_rcp_f32 v167, v165\n\t" \
    "v_mul_f32 v166, v166, v165\n\t" \
    "v_mul_f32 v167, v167, v164\n\t" \
    "v_mul_f32 v166, 0x4f800000, v166\n\t" \
    "v_mul_f32 v167, 0x4f000000, v167\n\t" \
    "v_cvt_u32_f32 v166, v166\n\t" \
    "v_cvt_u32_f32 v167, v167\n\t" \
    "v_readfirstlane_b32 s69, v166\n\t" \
    "v_readfirstlane_b32 s70, v167\n\t" \
    "s_nop 1\n\t" \
    "s_lshr_b32 s71, s65, 1\n\t" \
    "s_min_u32 s71, s71, 1\n\t" \
    "s_sub_u32 s69, s69, s78\n\t" \
    "s_ashr_i32 s69, s69, s71\n\t" \
    "s_add_u32 s78, s78, s69\n\t" \
    "s_sub_u32 s70, s70, s77\n\t" \
    "s_ashr_i32 s70, s70, s71\n\t" \
    "s_add_u32 s77, s77, s70\n\t" \
    "rr_K56_%=:\n\t" \
    "s_mov_b32 s91, s64\n\t" \
    "s_cmp_lg_u32 s64, 0\n\t" \
    "s_cselect_b32 s77, 0, s77\n\t" \
    "v_mov_b32 v160, 0\n\t" \
    "v_mov_b32 v161, 0\n\t" \
    "v_mov_b32 v162, 0\n\t" \
    "v_mov_b32 v163, 0\n\t" \
    ".p2align 6\n\t" \
    "s_nop 0\n\t" \
    "s_nop 0\n\t" \
    "s_sub_u32 s70, s80, s74\n\t" \
    "s_max_i32 s70, s70, 0\n\t" \
    "s_min_i32 s70, s70, 16000\n\t" \
    "s_lshl_b32 s70, s70, 1\n\t" \
    "s_mul_hi_u32 s70, s70, s77\n\t" \
    "s_add_u32 s70, s70, s73\n\t" \
    "s_getpc_b64 s[30:31]\n\t" \
    "rr_Wp59_%=:\n\t" \
    "s_add_u32 s30, s30, rr_We60_%=-rr_Wp59_%=\n\t" \
    "s_addc_u32 s31, s31, 0\n\t" \
    "s_sub_u32 s70, s70, 96\n\t" \
    "rr_W58_%=:\n\t" \
    "s_memtime s[56:57]\n\t" \
    "s_waitcnt lgkmcnt(0)\n\t" \
    "s_sub_u32 s69, s70, s56\n\t" \
    "s_cmp_gt_i32 s69, 80\n\t" \
    "s_cbranch_scc1 rr_W58_%=\n\t" \
    "s_max_i32 s69, s69, 0\n\t" \
    "s_and_b32 s69, s69, -4\n\t" \
    "s_sub_u32 s30, s30, s69\n\t" \
    "s_subb_u32 s31, s31, 0\n\t" \
    "s_setpc_b64 s[30:31]\n\t" \
    "s_nop 0\n\t" \
    "s_nop 0\n\t" \
    "s_nop 0\n\t" \
    "s_nop 0\n\t" \
    "s_nop 0\n\t" \
    "s_nop 0\n\t" \
    "s_nop 0\n\t" \
    "s_nop 0\n\t" \
    "s_nop 0\n\t" \
    "s_nop 0\n\t" \
    "s_nop 0\n\t" \
    "s_nop 0\n\t" \
    "s_nop 0\n\t" \
    "s_nop 0\n\t" \
    "s_nop 0\n\t" \
    "s_nop 0\n\t" \
    "s_nop 0\n\t" \
    "s_nop 0\n\t" \
    "s_nop 0\n\t" \
    "s_nop 0\n\t" \
    "rr_We60_%=:\n\t" \
    "global_load_dword v192, v7, s[52:53] offset:0 sc1\n\t" \
    "s_sub_u32 s71, s86, s74\n\t" \
    "s_lshl_b32 s71, s71, 2\n\t" \
    "s_lshr_b32 s33, s77, 1\n\t" \
    "s_sub_u32 s34, s73, 176\n\t" \
    "s_mov_b32 s96, 0\n\t" \
    "s_getpc_b64 s[30:31]\n\t" \
    "rr_rpc64_%=:\n\t" \
    "s_add_u32 s50, s30, rr_pgo67_%=-rr_rpc64_%=\n\t" \
    "s_addc_u32 s51, s31, 0\n\t" \
    "s_add_u32 s30, s30, rr_again5_%=-rr_rpc64_%=\n\t" \
    "s_addc_u32 s31, s31, 0\n\t" \
    "s_nop 7\n\t" \
    "s_nop 7\n\t" \
    "s_nop 7\n\t" \
    "s_nop 7\n\t" \
    "s_nop 3\n\t" \
    "global_load_dword v193, v7, s[52:53] offset:2048 sc1\n\t" \
    "s_nop 7\n\t" \
    "s_nop 7\n\t" \
    "s_nop 7\n\t" \
    "s_nop 7\n\t" \
    "s_nop 7\n\t" \
    "s_nop 7\n\t" \
    "global_load_dword v194, v189, s[52:53] offset:0 sc1\n\t" \
    "s_waitcnt vmcnt(2)\n\t" \
    "rr_hok61_%=:\n\t" \
    "s_memtime s[54:55]\n\t" \
    "v_max_u32_dpp v192, v192, v192 row_ror:1 row_mask:0xf bank_mask:0xf\n\t" \
    "s_nop 1\n\t" \
    "v_max_u32_dpp v192, v192, v192 row_ror:2 row_mask:0xf bank_mask:0xf\n\t" \
    "s_nop 1\n\t" \
    "v_max_u32_dpp v192, v192, v192 row_ror:4 row_mask:0xf bank_mask:0xf\n\t" \
    "s_nop 1\n\t" \
    "v_max_u32_dpp v192, v192, v192 row_ror:8 row_mask:0xf bank_mask:0xf\n\t" \
    "s_nop 0\n\t" \
    "v_readlane_b32 s69, v192, 0\n\t" \
    "v_readlane_b32 s70, v192, 16\n\t" \
    "s_waitcnt lgkmcnt(0)\n\t" \
    "s_sub_u32 s56, s34, s54\n\t" \
    "s_max_u32 s81, s69, s70\n\t" \
    "s_cmp_eq_u32 s81, -1\n\t" \
    "s_cbranch_scc1 rr_hlate62_%=\n\t" \
    "s_lshl2_add_u32 s70, s81, s71\n\t" \
    "s_mul_hi_i32 s70, s70, s33\n\t" \
    "s_add_u32 s69, s70, s56\n\t" \
    "s_max_i32 s69, s69, 0\n\t" \
    "s_cmp_gt_i32 s69, 80\n\t" \
    "s_cbranch_scc1 rr_rpoll65_%=\n\t" \
    "rr_rback66_%=:\n\t" \
    "s_and_b32 s69, s69, -4\n\t" \
    "s_sub_u32 s30, s30, s69\n\t" \
    "s_subb_u32 s31, s31, 0\n\t" \
    "s_setpc_b64 s[30:31]\n\t" \
    "rr_rpoll65_%=:\n\t" \
    "s_min_i32 s70, s70, 24000\n\t" \
    "s_add_u32 s70, s70, s73\n\t" \
    "s_sub_u32 s70, s70, 124\n\t" \
    "s_sub_u32 s70, s70, s96\n\t" \
    "rr_wl63_%=:\n\t" \
    "s_memtime s[56:57]\n\t" \
    "s_waitcnt lgkmcnt(0)\n\t" \
    "s_sub_u32 s69, s70, s56\n\t" \
    "s_cmp_gt_i32 s69, 80\n\t" \
    "s_cbranch_scc1 rr_wl63_%=\n\t" \
    "s_max_i32 s69, s69, 0\n\t" \
    "s_branch rr_rback66_%=\n\t" \
    "s_nop 0\n\t" \
    "s_nop 0\n\t" \
    "s_nop 0\n\t" \
    "s_nop 0\n\t" \
    "s_nop 0\n\t" \
    "s_nop 0\n\t" \
    "s_nop 0\n\t" \
    "s_nop 0\n\t" \
    "s_nop 0\n\t" \
    "s_nop 0\n\t" \
    "s_nop 0\n\t" \
    "s_nop 0\n\t" \
    "s_nop 0\n\t" \
    "s_nop 0\n\t" \
    "s_nop 0\n\t" \
    "s_nop 0\n\t" \
    "s_nop 0\n\t" \
    "s_nop 0\n\t" \
    "s_nop 0\n\t" \
    "s_nop 0\n\t" \
    "rr_again5_%=:\n\t" \
    "rr_pgo67_%=:\n\t" \
    "global_load_dwordx4 v[48:51], v10, s[46:47] sc1\n\t" \
    "global_load_dwordx4 v[52:55], v11, s[46:47] sc1\n\t" \
    "global_load_dwordx4 v[56:59], v12, s[46:47] sc1\n\t" \
    "global_load_dwordx4 v[60:63], v13, s[46:47] sc1\n\t" \
    "global_load_dwordx4 v[64:67], v14, s[46:47] sc1\n\t" \
    "global_load_dwordx4 v[68:71], v15, s[46:47] sc1\n\t" \
    "global_load_dwordx4 v[72:75], v16, s[46:47] sc1\n\t" \
    "global_load_dwordx4 v[76:79], v17, s[46:47] sc1\n\t" \
    "global_load_dwordx4 v[80:83], v18, s[46:47] sc1\n\t" \
    "global_load_dwordx4 v[84:87], v19, s[46:47] sc1\n\t" \
    "global_load_dwordx4 v[88:91], v20, s[46:47] sc1\n\t" \
    "global_load_dwordx4 v[92:95], v21, s[46:47] sc1\n\t" \
    "global_load_dwordx4 v[96:99], v22, s[46:47] sc1\n\t" \
    "global_load_dwordx4 v[100:103], v23, s[46:47] sc1\n\t" \
    "global_load_dwordx4 v[104:107], v24, s[46:47] sc1\n\t" \
    "global_load_dwordx4 v[108:111], v25, s[46:47] sc1\n\t" \
    "global_load_dwordx4 v[112:115], v42, s[46:47] sc1\n\t" \
    "global_load_dwordx4 v[116:119], v43, s[46:47] sc1\n\t" \
    "global_load_dwordx4 v[120:123], v44, s[46:47] sc1\n\t" \
    "global_load_dwordx4 v[124:127], v45, s[46:47] sc1\n\t" \
    "global_load_dwordx4 v[128:131], v0, s[40:41] offset:0 sc1\n\t" \
    "global_load_dwordx4 v[132:135], v0, s[40:41] offset:1024 sc1\n\t" \
    "global_load_dwordx4 v[136:139], v0, s[40:41] offset:2048 sc1\n\t" \
    "global_load_dwordx4 v[140:143], v0, s[40:41] offset:3072 sc1\n\t" \
    "global_load_dwordx4 v[144:147], v1, s[40:41] offset:0 sc1\n\t" \
    "global_load_dwordx4 v[148:151], v1, s[40:41] offset:1024 sc1\n\t" \
    "global_load_dwordx4 v[152:155], v1, s[40:41] offset:2048 sc1\n\t" \
    "global_load_dwordx4 v[156:159], v1, s[40:41] offset:3072 sc1\n\t" \
    "s_cmp_lg_u32 s91, 0\n\t" \
    "s_cbranch_scc1 rr_nob8_%=\n\t" \
    "v_mov_b32 v164, s81\n\t" \
    "ds_write_b32 v9, v164\n\t" \
    "s_waitcnt lgkmcnt(0)\n\t" \
    "s_barrier\n\t" \
    "rr_nob8_%=:\n\t" \
    "s_waitcnt vmcnt(27)\n\t" \
    "v_smfmac_f32_16x16x64_bf16 v[160:163], v[48:51], a[0:7], v4\n\t" \
    "s_waitcnt vmcnt(26)\n\t" \
    "v_smfmac_f32_16x16x64_bf16 v[160:163], v[52:55], a[8:15], v4\n\t" \
    "s_waitcnt vmcnt(25)\n\t" \
    "v_smfmac_f32_16x16x64_bf16 v[160:163], v[56:59], a[16:23], v4\n\t" \
    "s_waitcnt vmcnt(24)\n\t" \
    "v_smfmac_f32_16x16x64_bf16 v[160:163], v[60:63], a[24:31], v4\n\t" \
    "s_waitcnt vmcnt(23)\n\t" \
    "v_smfmac_f32_16x16x64_bf16 v[160:163], v[64:67], a[32:39], v4\n\t" \
    "s_waitcnt vmcnt(22)\n\t" \
    "v_smfmac_f32_16x16x64_bf16 v[160:163], v[68:71], a[40:47], v4\n\t" \
    "s_waitcnt vmcnt(21)\n\t" \
    "v_smfmac_f32_16x16x64_bf16 v[160:163], v[72:75], a[48:55], v4\n\t" \
    "s_waitcnt vmcnt(20)\n\t" \
    "v_smfmac_f32_16x16x64_bf16 v[160:163], v[76:79], a[56:63], v4\n\t" \
    "s_waitcnt vmcnt(19)\n\t" \
    "v_smfmac_f32_16x16x64_bf16 v[160:163], v[80:83], a[64:71], v4\n\t" \
    "s_waitcnt vmcnt(18)\n\t" \
    "v_smfmac_f32_16x16x64_bf16 v[160:163], v[84:87], a[72:79], v4\n\t" \
    "s_waitcnt vmcnt(17)\n\t" \
    "v_smfmac_f32_16x16x64_bf16 v[160:163], v[88:91], a[80:87], v4\n\t" \
    "s_waitcnt vmcnt(16)\n\t" \
    "v_smfmac_f32_16x16x64_bf16 v[160:163], v[92:95], a[88:95], v4\n\t" \
    "s_waitcnt vmcnt(15)\n\t" \
    "v_smfmac_f32_16x16x64_bf16 v[160:163], v[96:99], a[96:103], v4\n\t" \
    "s_waitcnt vmcnt(14)\n\t" \
    "v_smfmac_f32_16x16x64_bf16 v[160:163], v[100:103], a[104:111], v4\n\t" \
    "s_waitcnt vmcnt(13)\n\t" \
    "v_smfmac_f32_16x16x64_bf16 v[160:163], v[104:107], a[112:119], v4\n\t" \
    "s_waitcnt vmcnt(12)\n\t" \
    "v_smfmac_f32_16x16x64_bf16 v[160:163], v[108:111], a[120:127], v4\n\t" \
    "s_waitcnt vmcnt(6)\n\t" \
    "v_smfmac_f32_16x16x64_bf16 v[160:163], v[112:115], v[128:135], v4\n\t" \
    "s_waitcnt vmcnt(4)\n\t" \
    "v_smfmac_f32_16x16x64_bf16 v[160:163], v[116:119], v[136:143], v4\n\t" \
    "s_waitcnt vmcnt(2)\n\t" \
    "v_smfmac_f32_16x16x64_bf16 v[160:163], v[120:123], v[144:151], v4\n\t" \
    "s_waitcnt vmcnt(0)\n\t" \
    "v_smfmac_f32_16x16x64_bf16 v[160:163], v[124:127], v[152:159], v4\n\t" \
    "s_nop 7\n\t" \
    "v_pk_add_f32 v[160:161], v[160:161], v[162:163]\n\t" \
    "v_cvt_pk_bf16_f32 v164, v160, v161\n\t" \
    "v_cmp_u_f32 vcc, v160, v161\n\t" \
    "s_cbranch_vccnz rr_bad6_%=\n\t" \
    "rr_cont7_%=:\n\t" \
    "v_mov_b32_dpp v165, v164 quad_perm:[1,0,3,2] row_mask:0xf bank_mask:0xf\n\t" \
    "v_perm_b32 v164, v165, v164, v5\n\t" \
    "global_store_dword v180, v164, s[48:49] sc1\n\t" \
    "global_store_dword v181, v164, s[48:49] sc1\n\t" \
    "global_store_dword v182, v164, s[48:49] sc1\n\t" \
    "global_store_dword v2, v164, s[48:49] sc1\n\t" \
    "s_cmp_lg_u32 s64, 0\n\t" \
    "s_cbranch_scc1 rr_g214_%=\n\t" \
    "s_cmp_eq_u32 s65, 62\n\t" \
    "s_cbranch_scc1 rr_end4_%=\n\t" \
    "s_add_u32 s65, s65, 2\n\t" \
    "s_branch rr_pf9_%=\n\t" \
    "rr_bad6_%=:\n\t" \
    "s_cmp_lg_u32 s64, 0\n\t" \
    "s_cbranch_scc1 rr_cont7_%=\n\t" \
    "s_waitcnt lgkmcnt(0)\n\t" \
    "v_max_u32 v165, v48, v49\n\t" \
    "v_max3_u32 v165, v165, v50, v51\n\t" \
    "v_max3_u32 v165, v165, v52, v53\n\t" \
    "v_max3_u32 v165, v165, v54, v55\n\t" \
    "v_max3_u32 v165, v165, v56, v57\n\t" \
    "v_max3_u32 v165, v165, v58, v59\n\t" \
    "v_max3_u32 v165, v165, v60, v61\n\t" \
    "v_max3_u32 v165, v165, v62, v63\n\t" \
    "v_max3_u32 v165, v165, v64, v65\n\t" \
    "v_max3_u32 v165, v165, v66, v67\n\t" \
    "v_max3_u32 v165, v165, v68, v69\n\t" \
    "v_max3_u32 v165, v165, v70, v71\n\t" \
    "v_max3_u32 v165, v165, v72, v73\n\t" \
    "v_max3_u32 v165, v165, v74, v75\n\t" \
    "v_max3_u32 v165, v165, v76, v77\n\t" \
    "v_max3_u32 v165, v165, v78, v79\n\t" \
    "v_max3_u32 v165, v165, v80, v81\n\t" \
    "v_max3_u32 v165, v165, v82, v83\n\t" \
    "v_max3_u32 v165, v165, v84, v85\n\t" \
    "v_max3_u32 v165, v165, v86, v87\n\t" \
    "v_max3_u32 v165, v165, v88, v89\n\t" \
    "v_max3_u32 v165, v165, v90, v91\n\t" \
    "v_max3_u32 v165, v165, v92, v93\n\t" \
    "v_max3_u32 v165, v165, v94, v95\n\t" \
    "v_max3_u32 v165, v165, v96, v97\n\t" \
    "v_max3_u32 v165, v165, v98, v99\n\t" \
    "v_max3_u32 v165, v165, v100, v101\n\t" \
    "v_max3_u32 v165, v165, v102, v103\n\t" \
    "v_max3_u32 v165, v165, v104, v105\n\t" \
    "v_max3_u32 v165, v165, v106, v107\n\t" \
    "v_max3_u32 v165, v165, v108, v109\n\t" \
    "v_max3_u32 v165, v165, v110, v111\n\t" \
    "v_max3_u32 v165, v165, v112, v113\n\t" \
    "v_max3_u32 v165, v165, v114, v115\n\t" \
    "v_max3_u32 v165, v165, v116, v117\n\t" \
    "v_max3_u32 v165, v165, v118, v119\n\t" \
    "v_max3_u32 v165, v165, v120, v121\n\t" \
    "v_max3_u32 v165, v165, v122, v123\n\t" \
    "v_max3_u32 v165, v165, v124, v125\n\t" \
    "v_max3_u32 v165, v165, v126, v127\n\t" \
    "v_max_u32 v166, v128, v129\n\t" \
    "v_max3_u32 v166, v166, v130, v131\n\t" \
    "v_max3_u32 v166, v166, v132, v133\n\t" \
    "v_max3_u32 v166, v166, v134, v135\n\t" \
    "v_max3_u32 v166, v166, v136, v137\n\t" \
    "v_max3_u32 v166, v166, v138, v139\n\t" \
    "v_max3_u32 v166, v166, v140, v141\n\t" \
    "v_max3_u32 v166, v166, v142, v143\n\t" \
    "v_max3_u32 v166, v166, v144, v145\n\t" \
    "v_max3_u32 v166, v166, v146, v147\n\t" \
    "v_max3_u32 v166, v166, v148, v149\n\t" \
    "v_max3_u32 v166, v166, v150, v151\n\t" \
    "v_max3_u32 v166, v166, v152, v153\n\t" \
    "v_max3_u32 v166, v166, v154, v155\n\t" \
    "v_max3_u32 v166, v166, v156, v157\n\t" \
    "v_max3_u32 v166, v166, v158, v159\n\t" \
    "v_max_u32 v165, v165, v166\n\t" \
    "v_cmp_eq_u32 vcc, -1, v165\n\t" \
    "s_cbranch_vccz rr_cont7_%=\n\t" \
    "s_mov_b32 s91, 1\n\t" \
    "s_mov_b32 s96, 0\n\t" \
    "v_mov_b32 v160, 0\n\t" \
    "v_mov_b32 v161, 0\n\t" \
    "v_mov_b32 v162, 0\n\t" \
    "v_mov_b32 v163, 0\n\t" \
    "s_cmp_eq_u32 s68, 0\n\t" \
    "s_cbranch_scc1 rr_F68_%=\n\t" \
    "s_sub_u32 s68, s68, 1\n\t" \
    "s_branch rr_again5_%=\n\t" \
    "rr_F68_%=:\n\t" \
    "v_mov_b32 v166, 5767424\n\t" \
    "v_mov_b32 v167, 3\n\t" \
    "global_store_dword v166, v167, s[36:37] sc1\n\t" \
    "s_waitcnt vmcnt(0)\n\t" \
    "s_branch rr_cont7_%=\n\t" \
    "rr_hlate62_%=:\n\t" \
    "s_cmp_lg_u32 s64, 0\n\t" \
    "s_cbranch_scc1 rr_hpd70_%=\n\t" \
    "global_load_dword v195, v189, s[52:53] offset:2048 sc1\n\t" \
    "s_waitcnt vmcnt(2)\n\t" \
    "v_mov_b32 v192, v193\n\t" \
    "v_cmp_eq_u32 vcc, -1, v193\n\t" \
    "s_cbranch_vccz rr_hok61_%=\n\t" \
    "global_load_dword v196, v190, s[52:53] offset:0 sc1\n\t" \
    "s_waitcnt vmcnt(2)\n\t" \
    "v_mov_b32 v192, v194\n\t" \
    "v_cmp_eq_u32 vcc, -1, v194\n\t" \
    "s_cbranch_vccz rr_hok61_%=\n\t" \
    "global_load_dword v197, v190, s[52:53] offset:2048 sc1\n\t" \
    "s_waitcnt vmcnt(2)\n\t" \
    "v_mov_b32 v192, v195\n\t" \
    "v_cmp_eq_u32 vcc, -1, v195\n\t" \
    "s_cbranch_vccz rr_hok61_%=\n\t" \
    "s_waitcnt vmcnt(1)\n\t" \
    "v_mov_b32 v192, v196\n\t" \
    "v_cmp_eq_u32 vcc, -1, v196\n\t" \
    "s_cbranch_vccz rr_hok61_%=\n\t" \
    "s_waitcnt vmcnt(0)\n\t" \
    "v_mov_b32 v192, v197\n\t" \
    "v_cmp_eq_u32 vcc, -1, v197\n\t" \
    "s_cbranch_vccz rr_hok61_%=\n\t" \
    "rr_hp69_%=:\n\t" \
    "global_load_dword v192, v191, s[52:53] offset:0 sc1\n\t" \
    "s_waitcnt vmcnt(0)\n\t" \
    "v_cmp_eq_u32 vcc, -1, v192\n\t" \
    "s_cbranch_vccz rr_hpd70_%=\n\t" \
    "s_cmp_eq_u32 s68, 0\n\t" \
    "s_cbranch_scc1 rr_F71_%=\n\t" \
    "s_sub_u32 s68, s68, 1\n\t" \
    "s_sleep 1\n\t" \
    "s_branch rr_hp69_%=\n\t" \
    "rr_F71_%=:\n\t" \
    "v_mov_b32 v166, 5767424\n\t" \
    "v_mov_b32 v167, 7\n\t" \
    "global_store_dword v166, v167, s[36:37] sc1\n\t" \
    "s_waitcnt vmcnt(0)\n\t" \
    "s_branch rr_hpd70_%=\n\t" \
    "rr_hpd70_%=:\n\t" \
    "v_and_b32 v192, -2, v192\n\t" \
    "s_waitcnt lgkmcnt(0)\n\t" \
    "s_mov_b32 s70, 116\n\t" \
    "rr_E72_%=:\n\t" \
    "s_memtime s[56:57]\n\t" \
    "s_memrealtime s[2:3]\n\t" \
    "s_nop 0\n\t" \
    "s_memrealtime s[4:5]\n\t" \
    "s_nop 0\n\t" \
    "s_memrealtime s[6:7]\n\t" \
    "s_nop 0\n\t" \
    "s_memrealtime s[8:9]\n\t" \
    "s_nop 0\n\t" \
    "s_memrealtime s[10:11]\n\t" \
    "s_nop 0\n\t" \
    "s_memrealtime s[12:13]\n\t" \
    "s_nop 0\n\t" \
    "s_memrealtime s[14:15]\n\t" \
    "s_nop 0\n\t" \
    "s_memrealtime s[16:17]\n\t" \
    "s_nop 0\n\t" \
    "s_memrealtime s[18:19]\n\t" \
    "s_nop 0\n\t" \
    "s_memrealtime s[20:21]\n\t" \
    "s_nop 0\n\t" \
    "s_memrealtime s[22:23]\n\t" \
    "s_nop 0\n\t" \
    "s_memrealtime s[24:25]\n\t" \
    "s_nop 0\n\t" \
    "s_memrealtime s[26:27]\n\t" \
    "s_nop 0\n\t" \
    "s_memrealtime s[28:29]\n\t" \
    "s_memtime s[54:55]\n\t" \
    "s_add_u32 s70, s70, 8\n\t" \
    "s_waitcnt lgkmcnt(0)\n\t" \
    "s_sub_u32 s69, s54, s56\n\t" \
    "s_cmp_gt_u32 s69, s70\n\t" \
    "s_cbranch_scc1 rr_E72_%=\n\t" \
    "s_add_u32 s2, s2, s4\n\t" \
    "s_add_u32 s6, s6, s8\n\t" \
    "s_add_u32 s10, s10, s12\n\t" \
    "s_add_u32 s14, s14, s16\n\t" \
    "s_add_u32 s18, s18, s20\n\t" \
    "s_add_u32 s22, s22, s24\n\t" \
    "s_add_u32 s2, s2, s6\n\t" \
    "s_add_u32 s10, s10, s14\n\t" \
    "s_add_u32 s18, s18, s22\n\t" \
    "s_add_u32 s2, s2, s26\n\t" \
    "s_add_u32 s10, s10, s18\n\t" \
    "s_add_u32 s2, s2, s10\n\t" \
    "s_mul_i32 s70, s28, 13\n\t" \
    "s_sub_u32 s70, s70, s2\n\t" \
    "s_min_u32 s70, s70, 52\n\t" \
    "s_mul_i32 s69, s69, s70\n\t" \
    "s_mul_hi_u32 s69, s69, 76695844\n\t" \
    "s_mov_b32 s75, s73\n\t" \
    "s_mov_b32 s76, s74\n\t" \
    "s_add_u32 s73, s56, s69\n\t" \
    "s_lshl_b32 s74, s28, 4\n\t" \
    "s_sub_u32 s71, s86, s74\n\t" \
    "s_lshl_b32 s71, s71, 2\n\t" \
    "s_lshr_b32 s33, s77, 1\n\t" \
    "s_sub_u32 s34, s73, 176\n\t" \
    "s_branch rr_hok61_%=\n\t" \
    "rr_end4_%=:\n\t" \
    "s_endpgm\n\t" \
    "rr_last_%=:\n\t" \
    ""
#define ASM_H \
    "s_mov_b64 s[36:37], %0\n\t" \
    "s_mov_b64 s[42:43], %1\n\t" \
    "s_mov_b64 s[44:45], %2\n\t" \
    "s_mov_b32 s66, %3\n\t" \
    "s_mov_b32 s67, %4\n\t" \
    "s_mov_b64 s[50:51], %5\n\t" \
    "s_mov_b32 s82, %7\n\t" \
    "s_load_dwordx8 s[84:91], %6, 0\n\t" \
    "v_mbcnt_lo_u32_b32 v223, -1, 0\n\t" \
    "v_mbcnt_hi_u32_b32 v223, -1, v223\n\t" \
    "s_getpc_b64 s[46:47]\n\t" \
    "hh_pc_%=:\n\t" \
    "s_lshl_b32 s69, s66, 13\n\t" \
    "v_lshl_add_u32 v222, v223, 7, s69\n\t" \
    "v_min_u32 v222, hh_last_%=-hh_pc_%=-4, v222\n\t" \
    "global_load_dword v222, v222, s[46:47]\n\t" \
    "v_lshlrev_b32 v223, 2, v223\n\t" \
    "v_add_u32 v223, s82, v223\n\t" \
    "ds_read_b32 v0, v223 offset:0\n\t" \
    "ds_read_b32 v1, v223 offset:256\n\t" \
    "ds_read_b32 v2, v223 offset:512\n\t" \
    "ds_read_b32 v3, v223 offset:768\n\t" \
    "ds_read_b32 v4, v223 offset:1024\n\t" \
    "ds_read_b32 v5, v223 offset:1280\n\t" \
    "ds_read_b32 v6, v223 offset:1536\n\t" \
    "ds_read_b32 v7, v223 offset:1792\n\t" \
    "ds_read_b32 v8, v223 offset:2048\n\t" \
    "ds_read_b32 v9, v223 offset:2304\n\t" \
    "ds_read_b32 v10, v223 offset:2560\n\t" \
    "ds_read_b32 v11, v223 offset:2816\n\t" \
    "s_waitcnt lgkmcnt(0)\n\t" \
    "ds_read_b32 v12, v223 offset:3072\n\t" \
    "ds_read_b32 v13, v223 offset:3328\n\t" \
    "ds_read_b32 v14, v223 offset:3584\n\t" \
    "ds_read_b32 v15, v223 offset:3840\n\t" \
    "ds_read_b32 v16, v223 offset:4096\n\t" \
    "ds_read_b32 v17, v223 offset:4352\n\t" \
    "s_waitcnt lgkmcnt(0)\n\t" \
    "s_mov_b32 s68, 262144\n\t" \
    "s_mov_b32 s72, 0\n\t" \
    "s_mov_b32 s83, -1\n\t" \
    "s_mov_b32 s65, 0\n\t" \
    "s_mov_b32 s77, 3019898880\n\t" \
    "s_mov_b32 s78, 3054198966\n\t" \
    "s_waitcnt lgkmcnt(0)\n\t" \
    "s_mov_b32 s70, 116\n\t" \
    "hh_E1_%=:\n\t" \
    "s_memtime s[56:57]\n\t" \
    "s_memrealtime s[2:3]\n\t" \
    "s_nop 0\n\t" \
    "s_memrealtime s[4:5]\n\t" \
    "s_nop 0\n\t" \
    "s_memrealtime s[6:7]\n\t" \
    "s_nop 0\n\t" \
    "s_memrealtime s[8:9]\n\t" \
    "s_nop 0\n\t" \
    "s_memrealtime s[10:11]\n\t" \
    "s_nop 0\n\t" \
    "s_memrealtime s[12:13]\n\t" \
    "s_nop 0\n\t" \
    "s_memrealtime s[14:15]\n\t" \
    "s_nop 0\n\t" \
    "s_memrealtime s[16:17]\n\t" \
    "s_nop 0\n\t" \
    "s_memrealtime s[18:19]\n\t" \
    "s_nop 0\n\t" \
    "s_memrealtime s[20:21]\n\t" \
    "s_nop 0\n\t" \
    "s_memrealtime s[22:23]\n\t" \
    "s_nop 0\n\t" \
    "s_memrealtime s[24:25]\n\t" \
    "s_nop 0\n\t" \
    "s_memrealtime s[26:27]\n\t" \
    "s_nop 0\n\t" \
    "s_memrealtime s[28:29]\n\t" \
    "s_memtime s[54:55]\n\t" \
    "s_add_u32 s70, s70, 8\n\t" \
    "s_waitcnt lgkmcnt(0)\n\t" \
    "s_sub_u32 s69, s54, s56\n\t" \
    "s_cmp_gt_u32 s69, s70\n\t" \
    "s_cbranch_scc1 hh_E1_%=\n\t" \
    "s_add_u32 s2, s2, s4\n\t" \
    "s_add_u32 s6, s6, s8\n\t" \
    "s_add_u32 s10, s10, s12\n\t" \
    "s_add_u32 s14, s14, s16\n\t" \
    "s_add_u32 s18, s18, s20\n\t" \
    "s_add_u32 s22, s22, s24\n\t" \
    "s_add_u32 s2, s2, s6\n\t" \
    "s_add_u32 s10, s10, s14\n\t" \
    "s_add_u32 s18, s18, s22\n\t" \
    "s_add_u32 s2, s2, s26\n\t" \
    "s_add_u32 s10, s10, s18\n\t" \
    "s_add_u32 s2, s2, s10\n\t" \
    "s_mul_i32 s70, s28, 13\n\t" \
    "s_sub_u32 s70, s70, s2\n\t" \
    "s_min_u32 s70, s70, 52\n\t" \
    "s_mul_i32 s69, s69, s70\n\t" \
    "s_mul_hi_u32 s69, s69, 76695844\n\t" \
    "s_mov_b32 s75, s73\n\t" \
    "s_mov_b32 s76, s74\n\t" \
    "s_add_u32 s73, s56, s69\n\t" \
    "s_lshl_b32 s74, s28, 4\n\t" \
    "v_mbcnt_lo_u32_b32 v30, -1, 0\n\t" \
    "v_mbcnt_hi_u32_b32 v30, -1, v30\n\t" \
    "v_lshlrev_b32 v30, 6, v30\n\t" \
    "v_add_u32 v30, 6225920, v30\n\t" \
    "v_mov_b32 v31, 6234112\n\t" \
    "global_load_dword v31, v31, s[36:37] sc1\n\t" \
    "global_load_dwordx2 v[32:33], v30, s[36:37] sc1\n\t" \
    "global_load_dword v34, v30, s[36:37] offset:2048 sc1\n\t" \
    "s_waitcnt vmcnt(0)\n\t" \
    "hh_C2_%=:\n\t" \
    "v_cmp_ne_u32 s[54:55], v32, v34\n\t" \
    "v_sub_u32 v32, v32, v31\n\t" \
    "v_cmp_ne_u32 vcc, 1, v32\n\t" \
    "s_nop 1\n\t" \
    "s_or_b64 vcc, vcc, s[54:55]\n\t" \
    "s_cbranch_scc0 hh_Cok3_%=\n\t" \
    "s_cmp_eq_u32 s68, 0\n\t" \
    "s_cbranch_scc1 hh_F5_%=\n\t" \
    "s_sub_u32 s68, s68, 1\n\t" \
    "s_sleep 6\n\t" \
    "s_branch hh_Cag4_%=\n\t" \
    "hh_F5_%=:\n\t" \
    "v_mov_b32 v34, 5767424\n\t" \
    "v_mov_b32 v35, 4\n\t" \
    "global_store_dword v34, v35, s[36:37] sc1\n\t" \
    "s_waitcnt vmcnt(0)\n\t" \
    "s_branch hh_Cok3_%=\n\t" \
    "hh_Cag4_%=:\n\t" \
    "global_load_dwordx2 v[32:33], v30, s[36:37] sc1\n\t" \
    "global_load_dword v34, v30, s[36:37] offset:2048 sc1\n\t" \
    "s_waitcnt vmcnt(0)\n\t" \
    "s_branch hh_C2_%=\n\t" \
    "hh_Cok3_%=:\n\t" \
    "v_mov_b32_dpp v30, v33 quad_perm:[1,0,3,2] row_mask:0xf bank_mask:0xf\n\t" \
    "v_cmp_lt_u32 s[44:45], v30, v33\n\t" \
    "s_nop 1\n\t" \
    "s_bitcmp1_b32 s44, 0\n\t" \
    "s_cselect_b32 s62, 8192, 0\n\t" \
    "s_bitcmp1_b32 s44, 2\n\t" \
    "s_cselect_b32 s63, 8192, 0\n\t" \
    "s_or_b32 s63, s63, 0x200000\n\t" \
    "v_min_u32 v30, v30, v33\n\t" \
    "v_cmp_lt_u32 vcc, 660, v30\n\t" \
    "s_nop 1\n\t" \
    "s_cmp_lg_u64 vcc, 0\n\t" \
    "s_cselect_b32 s69, 1, 0\n\t" \
    "s_mul_i32 s70, s69, 160\n\t" \
    "s_add_u32 s86, s86, s70\n\t" \
    "s_mul_i32 s70, s69, 536\n\t" \
    "s_add_u32 s87, s87, s70\n\t" \
    "s_mul_i32 s70, s69, 256\n\t" \
    "s_add_u32 s88, s88, s70\n\t" \
    "s_waitcnt lgkmcnt(0)\n\t" \
    "s_mov_b32 s70, 116\n\t" \
    "hh_E6_%=:\n\t" \
    "s_memtime s[56:57]\n\t" \
    "s_memrealtime s[2:3]\n\t" \
    "s_nop 0\n\t" \
    "s_memrealtime s[4:5]\n\t" \
    "s_nop 0\n\t" \
    "s_memrealtime s[6:7]\n\t" \
    "s_nop 0\n\t" \
    "s_memrealtime s[8:9]\n\t" \
    "s_nop 0\n\t" \
    "s_memrealtime s[10:11]\n\t" \
    "s_nop 0\n\t" \
    "s_memrealtime s[12:13]\n\t" \
    "s_nop 0\n\t" \
    "s_memrealtime s[14:15]\n\t" \
    "s_nop 0\n\t" \
    "s_memrealtime s[16:17]\n\t" \
    "s_nop 0\n\t" \
    "s_memrealtime s[18:19]\n\t" \
    "s_nop 0\n\t" \
    "s_memrealtime s[20:21]\n\t" \
    "s_nop 0\n\t" \
    "s_memrealtime s[22:23]\n\t" \
    "s_nop 0\n\t" \
    "s_memrealtime s[24:25]\n\t" \
    "s_nop 0\n\t" \
    "s_memrealtime s[26:27]\n\t" \
    "s_nop 0\n\t" \
    "s_memrealtime s[28:29]\n\t" \
    "s_memtime s[54:55]\n\t" \
    "s_add_u32 s70, s70, 8\n\t" \
    "s_waitcnt lgkmcnt(0)\n\t" \
    "s_sub_u32 s69, s54, s56\n\t" \
    "s_cmp_gt_u32 s69, s70\n\t" \
    "s_cbranch_scc1 hh_E6_%=\n\t" \
    "s_add_u32 s2, s2, s4\n\t" \
    "s_add_u32 s6, s6, s8\n\t" \
    "s_add_u32 s10, s10, s12\n\t" \
    "s_add_u32 s14, s14, s16\n\t" \
    "s_add_u32 s18, s18, s20\n\t" \
    "s_add_u32 s22, s22, s24\n\t" \
    "s_add_u32 s2, s2, s6\n\t" \
    "s_add_u32 s10, s10, s14\n\t" \
    "s_add_u32 s18, s18, s22\n\t" \
    "s_add_u32 s2, s2, s26\n\t" \
    "s_add_u32 s10, s10, s18\n\t" \
    "s_add_u32 s2, s2, s10\n\t" \
    "s_mul_i32 s70, s28, 13\n\t" \
    "s_sub_u32 s70, s70, s2\n\t" \
    "s_min_u32 s70, s70, 52\n\t" \
    "s_mul_i32 s69, s69, s70\n\t" \
    "s_mul_hi_u32 s69, s69, 76695844\n\t" \
    "s_mov_b32 s75, s73\n\t" \
    "s_mov_b32 s76, s74\n\t" \
    "s_add_u32 s73, s56, s69\n\t" \
    "s_lshl_b32 s74, s28, 4\n\t" \
    "s_sub_u32 s69, s73, s75\n\t" \
    "s_sub_u32 s70, s74, s76\n\t" \
    "s_sub_u32 s71, s70, 1600\n\t" \
    "s_cmp_lt_u32 s71, 400000\n\t" \
    "s_cbranch_scc0 hh_K7_%=\n\t" \
    "v_cvt_f32_u32 v30, s69\n\t" \
    "v_cvt_f32_u32 v31, s70\n\t" \
    "v_rcp_f32 v32, v30\n\t" \
    "v_rcp_f32 v33, v31\n\t" \
    "v_mul_f32 v32, v32, v31\n\t" \
    "v_mul_f32 v33, v33, v30\n\t" \
    "v_mul_f32 v32, 0x4f800000, v32\n\t" \
    "v_mul_f32 v33, 0x4f000000, v33\n\t" \
    "v_cvt_u32_f32 v32, v32\n\t" \
    "v_cvt_u32_f32 v33, v33\n\t" \
    "v_readfirstlane_b32 s69, v32\n\t" \
    "v_readfirstlane_b32 s70, v33\n\t" \
    "s_nop 1\n\t" \
    "s_mov_b32 s78, s69\n\t" \
    "s_mov_b32 s77, s70\n\t" \
    "hh_K7_%=:\n\t" \
    "s_sub_u32 s80, s74, 32000\n\t" \
    "v_mov_b32 v38, -1\n\t" \
    "v_mov_b32 v39, -1\n\t" \
    "s_mov_b32 m0, 155648\n\t" \
    "s_mul_i32 s96, s67, 16\n\t" \
    "s_add_u32 s96, s96, 64\n\t" \
    "hh_loop8_%=:\n\t" \
    "s_and_b32 s69, s65, 7\n\t" \
    "s_lshl_b32 s69, s69, 18\n\t" \
    "s_add_u32 s69, s69, s63\n\t" \
    "s_add_u32 s52, s36, s69\n\t" \
    "s_addc_u32 s53, s37, 0\n\t" \
    "s_add_u32 s71, s80, 608\n\t" \
    "s_sub_u32 s70, s71, s74\n\t" \
    "s_max_i32 s70, s70, 0\n\t" \
    "s_min_i32 s70, s70, 16000\n\t" \
    "s_lshl_b32 s70, s70, 1\n\t" \
    "s_mul_hi_u32 s70, s70, s77\n\t" \
    "s_add_u32 s70, s70, s73\n\t" \
    "hh_W11_%=:\n\t" \
    "s_memtime s[56:57]\n\t" \
    "s_waitcnt lgkmcnt(0)\n\t" \
    "s_sub_u32 s69, s70, s56\n\t" \
    "s_cmp_gt_i32 s69, 24\n\t" \
    "s_cbranch_scc1 hh_W11_%=\n\t" \
    "global_load_dword v30, v1, s[52:53] sc1\n\t" \
    "s_waitcnt lgkmcnt(0)\n\t" \
    "s_mov_b32 s70, 116\n\t" \
    "hh_E15_%=:\n\t" \
    "s_memtime s[56:57]\n\t" \
    "s_memrealtime s[2:3]\n\t" \
    "s_nop 0\n\t" \
    "s_memrealtime s[4:5]\n\t" \
    "s_nop 0\n\t" \
    "s_memrealtime s[6:7]\n\t" \
    "s_nop 0\n\t" \
    "s_memrealtime s[8:9]\n\t" \
    "s_nop 0\n\t" \
    "s_memrealtime s[10:11]\n\t" \
    "s_nop 0\n\t" \
    "s_memrealtime s[12:13]\n\t" \
    "s_nop 0\n\t" \
    "s_memrealtime s[14:15]\n\t" \
    "s_nop 0\n\t" \
    "s_memrealtime s[16:17]\n\t" \
    "s_nop 0\n\t" \
    "s_memrealtime s[18:19]\n\t" \
    "s_nop 0\n\t" \
    "s_memrealtime s[20:21]\n\t" \
    "s_nop 0\n\t" \
    "s_memrealtime s[22:23]\n\t" \
    "s_nop 0\n\t" \
    "s_memrealtime s[24:25]\n\t" \
    "s_nop 0\n\t" \
    "s_memrealtime s[26:27]\n\t" \
    "s_nop 0\n\t" \
    "s_memrealtime s[28:29]\n\t" \
    "s_memtime s[54:55]\n\t" \
    "s_add_u32 s70, s70, 8\n\t" \
    "s_waitcnt lgkmcnt(0)\n\t" \
    "s_sub_u32 s69, s54, s56\n\t" \
    "s_cmp_gt_u32 s69, s70\n\t" \
    "s_cbranch_scc1 hh_E15_%=\n\t" \
    "s_add_u32 s2, s2, s4\n\t" \
    "s_add_u32 s6, s6, s8\n\t" \
    "s_add_u32 s10, s10, s12\n\t" \
    "s_add_u32 s14, s14, s16\n\t" \
    "s_add_u32 s18, s18, s20\n\t" \
    "s_add_u32 s22, s22, s24\n\t" \
    "s_add_u32 s2, s2, s6\n\t" \
    "s_add_u32 s10, s10, s14\n\t" \
    "s_add_u32 s18, s18, s22\n\t" \
    "s_add_u32 s2, s2, s26\n\t" \
    "s_add_u32 s10, s10, s18\n\t" \
    "s_add_u32 s2, s2, s10\n\t" \
    "s_mul_i32 s70, s28, 13\n\t" \
    "s_sub_u32 s70, s70, s2\n\t" \
    "s_min_u32 s70, s70, 52\n\t" \
    "s_mul_i32 s69, s69, s70\n\t" \
    "s_mul_hi_u32 s69, s69, 76695844\n\t" \
    "s_mov_b32 s75, s73\n\t" \
    "s_mov_b32 s76, s74\n\t" \
    "s_add_u32 s73, s56, s69\n\t" \
    "s_lshl_b32 s74, s28, 4\n\t" \
    "s_sub_u32 s69, s73, s75\n\t" \
    "s_sub_u32 s70, s74, s76\n\t" \
    "s_sub_u32 s71, s70, 1600\n\t" \
    "s_cmp_lt_u32 s71, 400000\n\t" \
    "s_cbranch_scc0 hh_K16_%=\n\t" \
    "v_cvt_f32_u32 v34, s69\n\t" \
    "v_cvt_f32_u32 v35, s70\n\t" \
    "v_rcp_f32 v36, v34\n\t" \
    "v_rcp_f32 v37, v35\n\t" \
    "v_mul_f32 v36, v36, v35\n\t" \
    "v_mul_f32 v37, v37, v34\n\t" \
    "v_mul_f32 v36, 0x4f800000, v36\n\t" \
    "v_mul_f32 v37, 0x4f000000, v37\n\t" \
    "v_cvt_u32_f32 v36, v36\n\t" \
    "v_cvt_u32_f32 v37, v37\n\t" \
    "v_readfirstlane_b32 s69, v36\n\t" \
    "v_readfirstlane_b32 s70, v37\n\t" \
    "s_nop 1\n\t" \
    "s_sub_u32 s69, s69, s78\n\t" \
    "s_ashr_i32 s69, s69, 1\n\t" \
    "s_add_u32 s78, s78, s69\n\t" \
    "s_sub_u32 s70, s70, s77\n\t" \
    "s_ashr_i32 s70, s70, 1\n\t" \
    "s_add_u32 s77, s77, s70\n\t" \
    "hh_K16_%=:\n\t" \
    "s_waitcnt vmcnt(0)\n\t" \
    "v_cmp_eq_u32 vcc, -1, v30\n\t" \
    "s_cbranch_vccz hh_Hok13_%=\n\t" \
    "hh_H12_%=:\n\t" \
    "global_load_dword v30, v1, s[52:53] sc1\n\t" \
    "s_waitcnt vmcnt(0)\n\t" \
    "v_cmp_eq_u32 vcc, -1, v30\n\t" \
    "s_cbranch_vccz hh_Hc14_%=\n\t" \
    "s_cmp_eq_u32 s68, 0\n\t" \
    "s_cbranch_scc1 hh_F17_%=\n\t" \
    "s_sub_u32 s68, s68, 1\n\t" \
    "s_sleep 2\n\t" \
    "s_branch hh_H12_%=\n\t" \
    "hh_F17_%=:\n\t" \
    "v_mov_b32 v31, 5767424\n\t" \
    "v_mov_b32 v32, 14\n\t" \
    "global_store_dword v31, v32, s[36:37] sc1\n\t" \
    "s_waitcnt vmcnt(0)\n\t" \
    "s_branch hh_Hc14_%=\n\t" \
    "hh_Hc14_%=:\n\t" \
    "s_waitcnt lgkmcnt(0)\n\t" \
    "s_mov_b32 s70, 116\n\t" \
    "hh_E18_%=:\n\t" \
    "s_memtime s[56:57]\n\t" \
    "s_memrealtime s[2:3]\n\t" \
    "s_nop 0\n\t" \
    "s_memrealtime s[4:5]\n\t" \
    "s_nop 0\n\t" \
    "s_memrealtime s[6:7]\n\t" \
    "s_nop 0\n\t" \
    "s_memrealtime s[8:9]\n\t" \
    "s_nop 0\n\t" \
    "s_memrealtime s[10:11]\n\t" \
    "s_nop 0\n\t" \
    "s_memrealtime s[12:13]\n\t" \
    "s_nop 0\n\t" \
    "s_memrealtime s[14:15]\n\t" \
    "s_nop 0\n\t" \
    "s_memrealtime s[16:17]\n\t" \
    "s_nop 0\n\t" \
    "s_memrealtime s[18:19]\n\t" \
    "s_nop 0\n\t" \
    "s_memrealtime s[20:21]\n\t" \
    "s_nop 0\n\t" \
    "s_memrealtime s[22:23]\n\t" \
    "s_nop 0\n\t" \
    "s_memrealtime s[24:25]\n\t" \
    "s_nop 0\n\t" \
    "s_memrealtime s[26:27]\n\t" \
    "s_nop 0\n\t" \
    "s_memrealtime s[28:29]\n\t" \
    "s_memtime s[54:55]\n\t" \
    "s_add_u32 s70, s70, 8\n\t" \
    "s_waitcnt lgkmcnt(0)\n\t" \
    "s_sub_u32 s69, s54, s56\n\t" \
    "s_cmp_gt_u32 s69, s70\n\t" \
    "s_cbranch_scc1 hh_E18_%=\n\t" \
    "s_add_u32 s2, s2, s4\n\t" \
    "s_add_u32 s6, s6, s8\n\t" \
    "s_add_u32 s10, s10, s12\n\t" \
    "s_add_u32 s14, s14, s16\n\t" \
    "s_add_u32 s18, s18, s20\n\t" \
    "s_add_u32 s22, s22, s24\n\t" \
    "s_add_u32 s2, s2, s6\n\t" \
    "s_add_u32 s10, s10, s14\n\t" \
    "s_add_u32 s18, s18, s22\n\t" \
    "s_add_u32 s2, s2, s26\n\t" \
    "s_add_u32 s10, s10, s18\n\t" \
    "s_add_u32 s2, s2, s10\n\t" \
    "s_mul_i32 s70, s28, 13\n\t" \
    "s_sub_u32 s70, s70, s2\n\t" \
    "s_min_u32 s70, s70, 52\n\t" \
    "s_mul_i32 s69, s69, s70\n\t" \
    "s_mul_hi_u32 s69, s69, 76695844\n\t" \
    "s_mov_b32 s75, s73\n\t" \
    "s_mov_b32 s76, s74\n\t" \
    "s_add_u32 s73, s56, s69\n\t" \
    "s_lshl_b32 s74, s28, 4\n\t" \
    "s_sub_u32 s69, s73, s75\n\t" \
    "s_sub_u32 s70, s74, s76\n\t" \
    "s_sub_u32 s71, s70, 1600\n\t" \
    "s_cmp_lt_u32 s71, 400000\n\t" \
    "s_cbranch_scc0 hh_K19_%=\n\t" \
    "v_cvt_f32_u32 v34, s69\n\t" \
    "v_cvt_f32_u32 v35, s70\n\t" \
    "v_rcp_f32 v36, v34\n\t" \
    "v_rcp_f32 v37, v35\n\t" \
    "v_mul_f32 v36, v36, v35\n\t" \
    "v_mul_f32 v37, v37, v34\n\t" \
    "v_mul_f32 v36, 0x4f800000, v36\n\t" \
    "v_mul_f32 v37, 0x4f000000, v37\n\t" \
    "v_cvt_u32_f32 v36, v36\n\t" \
    "v_cvt_u32_f32 v37, v37\n\t" \
    "v_readfirstlane_b32 s69, v36\n\t" \
    "v_readfirstlane_b32 s70, v37\n\t" \
    "s_nop 1\n\t" \
    "s_sub_u32 s69, s69, s78\n\t" \
    "s_ashr_i32 s69, s69, 1\n\t" \
    "s_add_u32 s78, s78, s69\n\t" \
    "s_sub_u32 s70, s70, s77\n\t" \
    "s_ashr_i32 s70, s70, 1\n\t" \
    "s_add_u32 s77, s77, s70\n\t" \
    "hh_K19_%=:\n\t" \
    "hh_Hok13_%=:\n\t" \
    "v_max_u32_dpp v30, v30, v30 row_ror:1 row_mask:0xf bank_mask:0xf\n\t" \
    "s_nop 1\n\t" \
    "v_max_u32_dpp v30, v30, v30 row_ror:2 row_mask:0xf bank_mask:0xf\n\t" \
    "s_nop 1\n\t" \
    "v_max_u32_dpp v30, v30, v30 row_ror:4 row_mask:0xf bank_mask:0xf\n\t" \
    "s_nop 1\n\t" \
    "v_max_u32_dpp v30, v30, v30 row_ror:8 row_mask:0xf bank_mask:0xf\n\t" \
    "s_nop 0\n\t" \
    "v_readlane_b32 s69, v30, 0\n\t" \
    "v_readlane_b32 s71, v30, 16\n\t" \
    "s_nop 1\n\t" \
    "s_max_u32 s81, s69, s71\n\t" \
    "s_mul_hi_u32 s69, s85, s78\n\t" \
    "s_add_u32 s79, s81, s69\n\t" \
    "s_add_u32 s79, s79, s87\n\t" \
    "s_cmp_ge_u32 s65, 52\n\t" \
    "s_cselect_b32 s69, 32, 0\n\t" \
    "s_sub_u32 s79, s79, s69\n\t" \
    "s_cmp_lt_u32 s65, 9\n\t" \
    "s_cselect_b32 s69, 64, 0\n\t" \
    "s_add_u32 s79, s79, s69\n\t" \
    "s_add_u32 s80, s79, s88\n\t" \
    "s_add_u32 s65, s65, 1\n\t" \
    "s_cmp_gt_u32 s65, 62\n\t" \
    "s_cbranch_scc1 hh_end10_%=\n\t" \
    "s_cmp_le_u32 s65, 6\n\t" \
    "s_cbranch_scc1 hh_loop8_%=\n\t" \
    "v_lshrrev_b32 v20, 15, v11\n\t" \
    "v_add3_u32 v20, v20, s65, 1\n\t" \
    "v_min_u32 v20, 63, v20\n\t" \
    "v_mul_u32_u24 v20, v20, v11\n\t" \
    "v_xor_b32 v20, v20, v4\n\t" \
    "v_lshrrev_b32 v21, 15, v12\n\t" \
    "v_add3_u32 v21, v21, s65, 1\n\t" \
    "v_min_u32 v21, 63, v21\n\t" \
    "v_mul_u32_u24 v21, v21, v12\n\t" \
    "v_xor_b32 v21, v21, v5\n\t" \
    "v_lshrrev_b32 v22, 15, v13\n\t" \
    "v_add3_u32 v22, v22, s65, 1\n\t" \
    "v_min_u32 v22, 63, v22\n\t" \
    "v_mul_u32_u24 v22, v22, v13\n\t" \
    "v_xor_b32 v22, v22, v6\n\t" \
    "v_lshrrev_b32 v23, 15, v14\n\t" \
    "v_add3_u32 v23, v23, s65, 1\n\t" \
    "v_min_u32 v23, 63, v23\n\t" \
    "v_mul_u32_u24 v23, v23, v14\n\t" \
    "v_xor_b32 v23, v23, v7\n\t" \
    "v_lshrrev_b32 v24, 15, v15\n\t" \
    "v_add3_u32 v24, v24, s65, 1\n\t" \
    "v_min_u32 v24, 63, v24\n\t" \
    "v_mul_u32_u24 v24, v24, v15\n\t" \
    "v_xor_b32 v24, v24, v8\n\t" \
    "v_lshrrev_b32 v25, 15, v16\n\t" \
    "v_add3_u32 v25, v25, s65, 1\n\t" \
    "v_min_u32 v25, 63, v25\n\t" \
    "v_mul_u32_u24 v25, v25, v16\n\t" \
    "v_xor_b32 v25, v25, v9\n\t" \
    "v_lshrrev_b32 v26, 15, v17\n\t" \
    "v_add3_u32 v26, v26, s65, 1\n\t" \
    "v_min_u32 v26, 63, v26\n\t" \
    "v_mul_u32_u24 v26, v26, v17\n\t" \
    "v_xor_b32 v26, v26, v10\n\t" \
    "s_add_u32 s71, s79, s96\n\t" \
    "s_sub_u32 s70, s71, s74\n\t" \
    "s_max_i32 s70, s70, 0\n\t" \
    "s_min_i32 s70, s70, 16000\n\t" \
    "s_lshl_b32 s70, s70, 1\n\t" \
    "s_mul_hi_u32 s70, s70, s77\n\t" \
    "s_add_u32 s70, s70, s73\n\t" \
    "s_getpc_b64 s[30:31]\n\t" \
    "hh_Wp22_%=:\n\t" \
    "s_add_u32 s30, s30, hh_We23_%=-hh_Wp22_%=\n\t" \
    "s_addc_u32 s31, s31, 0\n\t" \
    "s_sub_u32 s70, s70, 96\n\t" \
    "hh_W21_%=:\n\t" \
    "s_memtime s[56:57]\n\t" \
    "s_waitcnt lgkmcnt(0)\n\t" \
    "s_sub_u32 s69, s70, s56\n\t" \
    "s_cmp_gt_i32 s69, 80\n\t" \
    "s_cbranch_scc1 hh_W21_%=\n\t" \
    "s_max_i32 s69, s69, 0\n\t" \
    "s_and_b32 s69, s69, -4\n\t" \
    "s_sub_u32 s30, s30, s69\n\t" \
    "s_subb_u32 s31, s31, 0\n\t" \
    "s_setpc_b64 s[30:31]\n\t" \
    "s_nop 0\n\t" \
    "s_nop 0\n\t" \
    "s_nop 0\n\t" \
    "s_nop 0\n\t" \
    "s_nop 0\n\t" \
    "s_nop 0\n\t" \
    "s_nop 0\n\t" \
    "s_nop 0\n\t" \
    "s_nop 0\n\t" \
    "s_nop 0\n\t" \
    "s_nop 0\n\t" \
    "s_nop 0\n\t" \
    "s_nop 0\n\t" \
    "s_nop 0\n\t" \
    "s_nop 0\n\t" \
    "s_nop 0\n\t" \
    "s_nop 0\n\t" \
    "s_nop 0\n\t" \
    "s_nop 0\n\t" \
    "s_nop 0\n\t" \
    "hh_We23_%=:\n\t" \
    "global_load_lds_dword v26, s[36:37] sc1\n\t" \
    "s_sleep 2\n\t" \
    "s_nop 5\n\t" \
    "global_load_lds_dword v25, s[36:37] sc1\n\t" \
    "s_sleep 2\n\t" \
    "s_nop 5\n\t" \
    "global_load_lds_dword v24, s[36:37] sc1\n\t" \
    "s_sleep 2\n\t" \
    "s_nop 5\n\t" \
    "global_load_lds_dword v23, s[36:37] sc1\n\t" \
    "s_sleep 2\n\t" \
    "s_nop 5\n\t" \
    "global_load_lds_dword v22, s[36:37] sc1\n\t" \
    "s_sleep 2\n\t" \
    "s_nop 5\n\t" \
    "global_load_lds_dword v21, s[36:37] sc1\n\t" \
    "s_sleep 2\n\t" \
    "s_nop 5\n\t" \
    "global_load_lds_dword v20, s[36:37] sc1\n\t" \
    "s_branch hh_loop8_%=\n\t" \
    "hh_end10_%=:\n\t" \
    "s_waitcnt vmcnt(0)\n\t" \
    "s_endpgm\n\t" \
    "hh_last_%=:\n\t" \
    ""
#define ASM_GX \
    "s_load_dwordx2 s[42:43], %3, 48\n\t" \
    "s_load_dwordx2 s[44:45], %3, 40\n\t" \
    "s_load_dwordx2 s[36:37], %3, 0x58\n\t" \
    "s_load_dwordx2 s[50:51], %3, 0x48\n\t" \
    "s_mov_b32 s66, %4\n\t" \
    "s_mov_b32 s67, %5\n\t" \
    "s_mov_b32 s68, 262144\n\t" \
    "s_mov_b32 s72, 0\n\t" \
    "s_mov_b32 s83, -1\n\t" \
    "s_mov_b32 s65, 0\n\t" \
    "s_lshl_b32 s82, s66, 10\n\t" \
    "s_waitcnt lgkmcnt(0)\n\t" \
    "s_mul_i32 s89, s66, 9216\n\t" \
    "s_mov_b32 s90, 0\n\t" \
    "s_add_u32 s46, s42, 2097152\n\t" \
    "s_addc_u32 s47, s43, 0\n\t" \
    "s_add_u32 m0, s82, 65536\n\t" \
    "s_nop 0\n\t" \
    "global_load_lds_dwordx4 v200, s[46:47]\n\t" \
    "s_add_u32 s46, s42, 2228224\n\t" \
    "s_addc_u32 s47, s43, 0\n\t" \
    "s_add_u32 m0, s82, 69632\n\t" \
    "s_nop 0\n\t" \
    "global_load_lds_dwordx4 v200, s[46:47]\n\t" \
    "s_add_u32 s46, s42, 2359296\n\t" \
    "s_addc_u32 s47, s43, 0\n\t" \
    "s_add_u32 m0, s82, 73728\n\t" \
    "s_nop 0\n\t" \
    "global_load_lds_dwordx4 v200, s[46:47]\n\t" \
    "s_add_u32 s46, s42, 2490368\n\t" \
    "s_addc_u32 s47, s43, 0\n\t" \
    "s_add_u32 m0, s82, 77824\n\t" \
    "s_nop 0\n\t" \
    "global_load_lds_dwordx4 v200, s[46:47]\n\t" \
    "s_add_u32 s46, s42, 2621440\n\t" \
    "s_addc_u32 s47, s43, 0\n\t" \
    "s_add_u32 m0, s82, 81920\n\t" \
    "s_nop 0\n\t" \
    "global_load_lds_dwordx4 v200, s[46:47]\n\t" \
    "s_add_u32 s46, s42, 2752512\n\t" \
    "s_addc_u32 s47, s43, 0\n\t" \
    "s_add_u32 m0, s82, 86016\n\t" \
    "s_nop 0\n\t" \
    "global_load_lds_dwordx4 v200, s[46:47]\n\t" \
    "s_add_u32 s46, s42, 2883584\n\t" \
    "s_addc_u32 s47, s43, 0\n\t" \
    "s_add_u32 m0, s82, 90112\n\t" \
    "s_nop 0\n\t" \
    "global_load_lds_dwordx4 v200, s[46:47]\n\t" \
    "s_add_u32 s46, s42, 3014656\n\t" \
    "s_addc_u32 s47, s43, 0\n\t" \
    "s_add_u32 m0, s82, 94208\n\t" \
    "s_nop 0\n\t" \
    "global_load_lds_dwordx4 v200, s[46:47]\n\t" \
    "s_add_u32 s46, s42, 3145728\n\t" \
    "s_addc_u32 s47, s43, 0\n\t" \
    "s_add_u32 m0, s82, 98304\n\t" \
    "s_nop 0\n\t" \
    "global_load_lds_dwordx4 v200, s[46:47]\n\t" \
    "s_add_u32 s46, s42, 3276800\n\t" \
    "s_addc_u32 s47, s43, 0\n\t" \
    "s_add_u32 m0, s82, 102400\n\t" \
    "s_nop 0\n\t" \
    "global_load_lds_dwordx4 v200, s[46:47]\n\t" \
    "s_add_u32 s46, s42, 3407872\n\t" \
    "s_addc_u32 s47, s43, 0\n\t" \
    "s_add_u32 m0, s82, 106496\n\t" \
    "s_nop 0\n\t" \
    "global_load_lds_dwordx4 v200, s[46:47]\n\t" \
    "s_add_u32 s46, s42, 3538944\n\t" \
    "s_addc_u32 s47, s43, 0\n\t" \
    "s_add_u32 m0, s82, 110592\n\t" \
    "s_nop 0\n\t" \
    "global_load_lds_dwordx4 v200, s[46:47]\n\t" \
    "s_add_u32 s46, s42, 3670016\n\t" \
    "s_addc_u32 s47, s43, 0\n\t" \
    "s_add_u32 m0, s82, 114688\n\t" \
    "s_nop 0\n\t" \
    "global_load_lds_dwordx4 v200, s[46:47]\n\t" \
    "s_add_u32 s46, s42, 3801088\n\t" \
    "s_addc_u32 s47, s43, 0\n\t" \
    "s_add_u32 m0, s82, 118784\n\t" \
    "s_nop 0\n\t" \
    "global_load_lds_dwordx4 v200, s[46:47]\n\t" \
    "s_add_u32 s46, s42, 3932160\n\t" \
    "s_addc_u32 s47, s43, 0\n\t" \
    "s_add_u32 m0, s82, 122880\n\t" \
    "s_nop 0\n\t" \
    "global_load_lds_dwordx4 v200, s[46:47]\n\t" \
    "s_add_u32 s46, s42, 4063232\n\t" \
    "s_addc_u32 s47, s43, 0\n\t" \
    "s_add_u32 m0, s82, 126976\n\t" \
    "s_nop 0\n\t" \
    "global_load_lds_dwordx4 v200, s[46:47]\n\t" \
    "s_add_u32 s46, s42, 4194304\n\t" \
    "s_addc_u32 s47, s43, 0\n\t" \
    "s_add_u32 m0, s82, 131072\n\t" \
    "s_nop 0\n\t" \
    "global_load_lds_dwordx4 v200, s[46:47]\n\t" \
    "s_add_u32 s46, s42, 4325376\n\t" \
    "s_addc_u32 s47, s43, 0\n\t" \
    "s_add_u32 m0, s82, 135168\n\t" \
    "s_nop 0\n\t" \
    "global_load_lds_dwordx4 v200, s[46:47]\n\t" \
    "s_add_u32 s46, s42, 4456448\n\t" \
    "s_addc_u32 s47, s43, 0\n\t" \
    "s_add_u32 m0, s82, 139264\n\t" \
    "s_nop 0\n\t" \
    "global_load_lds_dwordx4 v200, s[46:47]\n\t" \
    "s_add_u32 s46, s42, 4587520\n\t" \
    "s_addc_u32 s47, s43, 0\n\t" \
    "s_add_u32 m0, s82, 143360\n\t" \
    "s_nop 0\n\t" \
    "global_load_lds_dwordx4 v200, s[46:47]\n\t" \
    "global_load_dwordx4 v[120:123], v203, s[50:51]\n\t" \
    "global_load_dwordx4 v[124:127], v204, s[50:51]\n\t" \
    "global_load_dwordx4 v[128:131], v205, s[50:51]\n\t" \
    "global_load_dwordx4 v[132:135], v206, s[50:51]\n\t" \
    "global_load_dwordx4 v[136:139], v207, s[50:51]\n\t" \
    "global_load_dwordx4 v[140:143], v208, s[50:51]\n\t" \
    "global_load_dwordx4 v[144:147], v209, s[50:51]\n\t" \
    "global_load_dwordx4 v[148:151], v210, s[50:51]\n\t" \
    "global_load_dwordx4 v[152:155], v211, s[50:51]\n\t" \
    "s_add_u32 s52, s50, 36864\n\t" \
    "s_addc_u32 s53, s51, 0\n\t" \
    "global_load_dwordx4 v[72:75], v203, s[52:53]\n\t" \
    "global_load_dwordx4 v[76:79], v204, s[52:53]\n\t" \
    "global_load_dwordx4 v[80:83], v205, s[52:53]\n\t" \
    "global_load_dwordx4 v[84:87], v206, s[52:53]\n\t" \
    "global_load_dwordx4 v[88:91], v207, s[52:53]\n\t" \
    "global_load_dwordx4 v[92:95], v208, s[52:53]\n\t" \
    "global_load_dwordx4 v[96:99], v209, s[52:53]\n\t" \
    "global_load_dwordx4 v[100:103], v210, s[52:53]\n\t" \
    "global_load_dwordx4 v[104:107], v211, s[52:53]\n\t" \
    "s_waitcnt vmcnt(50) lgkmcnt(0)\n\t" \
    "s_barrier\n\t" \
    "ds_read_b64_tr_b16 a[144:145], v201 offset:0\n\t" \
    "ds_read_b64_tr_b16 a[146:147], v201 offset:1024\n\t" \
    "ds_read_b64_tr_b16 a[148:149], v201 offset:8192\n\t" \
    "ds_read_b64_tr_b16 a[150:151], v201 offset:9216\n\t" \
    "ds_read_b64_tr_b16 a[216:217], v202 offset:0\n\t" \
    "ds_read_b64_tr_b16 a[218:219], v202 offset:1024\n\t" \
    "ds_read_b64_tr_b16 a[220:221], v202 offset:8192\n\t" \
    "ds_read_b64_tr_b16 a[222:223], v202 offset:9216\n\t" \
    "s_waitcnt vmcnt(46) lgkmcnt(0)\n\t" \
    "s_barrier\n\t" \
    "ds_read_b64_tr_b16 a[152:153], v201 offset:16384\n\t" \
    "ds_read_b64_tr_b16 a[154:155], v201 offset:17408\n\t" \
    "ds_read_b64_tr_b16 a[156:157], v201 offset:24576\n\t" \
    "ds_read_b64_tr_b16 a[158:159], v201 offset:25600\n\t" \
    "ds_read_b64_tr_b16 a[224:225], v202 offset:16384\n\t" \
    "ds_read_b64_tr_b16 a[226:227], v202 offset:17408\n\t" \
    "ds_read_b64_tr_b16 a[228:229], v202 offset:24576\n\t" \
    "ds_read_b64_tr_b16 a[230:231], v202 offset:25600\n\t" \
    "s_waitcnt vmcnt(42) lgkmcnt(0)\n\t" \
    "s_barrier\n\t" \
    "ds_read_b64_tr_b16 a[160:161], v201 offset:32768\n\t" \
    "ds_read_b64_tr_b16 a[162:163], v201 offset:33792\n\t" \
    "ds_read_b64_tr_b16 a[164:165], v201 offset:40960\n\t" \
    "ds_read_b64_tr_b16 a[166:167], v201 offset:41984\n\t" \
    "ds_read_b64_tr_b16 a[232:233], v202 offset:32768\n\t" \
    "ds_read_b64_tr_b16 a[234:235], v202 offset:33792\n\t" \
    "ds_read_b64_tr_b16 a[236:237], v202 offset:40960\n\t" \
    "ds_read_b64_tr_b16 a[238:239], v202 offset:41984\n\t" \
    "s_waitcnt vmcnt(38) lgkmcnt(0)\n\t" \
    "s_barrier\n\t" \
    "ds_read_b64_tr_b16 a[168:169], v201 offset:49152\n\t" \
    "ds_read_b64_tr_b16 a[170:171], v201 offset:50176\n\t" \
    "ds_read_b64_tr_b16 a[172:173], v201 offset:57344\n\t" \
    "ds_read_b64_tr_b16 a[174:175], v201 offset:58368\n\t" \
    "ds_read_b64_tr_b16 a[240:241], v202 offset:49152\n\t" \
    "ds_read_b64_tr_b16 a[242:243], v202 offset:50176\n\t" \
    "ds_read_b64_tr_b16 a[244:245], v202 offset:57344\n\t" \
    "ds_read_b64_tr_b16 a[246:247], v202 offset:58368\n\t" \
    "v_mbcnt_lo_u32_b32 v164, -1, 0\n\t" \
    "v_mbcnt_hi_u32_b32 v164, -1, v164\n\t" \
    "v_lshlrev_b32 v164, 6, v164\n\t" \
    "v_add_u32 v164, 6225920, v164\n\t" \
    "v_mov_b32 v165, 6234112\n\t" \
    "global_load_dword v165, v165, s[36:37] sc1\n\t" \
    "global_load_dwordx2 v[166:167], v164, s[36:37] sc1\n\t" \
    "global_load_dword v168, v164, s[36:37] offset:2048 sc1\n\t" \
    "s_waitcnt vmcnt(37) lgkmcnt(0)\n\t" \
    "s_barrier\n\t" \
    "v_add_u32 v201, 0x10000, v201\n\t" \
    "v_add_u32 v202, 0x10000, v202\n\t" \
    "ds_read_b64_tr_b16 a[176:177], v201 offset:0\n\t" \
    "ds_read_b64_tr_b16 a[178:179], v201 offset:1024\n\t" \
    "ds_read_b64_tr_b16 a[180:181], v201 offset:8192\n\t" \
    "ds_read_b64_tr_b16 a[182:183], v201 offset:9216\n\t" \
    "ds_read_b64_tr_b16 a[248:249], v202 offset:0\n\t" \
    "ds_read_b64_tr_b16 a[250:251], v202 offset:1024\n\t" \
    "ds_read_b64_tr_b16 a[252:253], v202 offset:8192\n\t" \
    "ds_read_b64_tr_b16 a[254:255], v202 offset:9216\n\t" \
    "s_waitcnt vmcnt(33) lgkmcnt(0)\n\t" \
    "s_barrier\n\t" \
    "ds_read_b64_tr_b16 a[184:185], v201 offset:16384\n\t" \
    "ds_read_b64_tr_b16 a[186:187], v201 offset:17408\n\t" \
    "ds_read_b64_tr_b16 a[188:189], v201 offset:24576\n\t" \
    "ds_read_b64_tr_b16 a[190:191], v201 offset:25600\n\t" \
    "ds_read_b64_tr_b16 v[224:225], v202 offset:16384\n\t" \
    "ds_read_b64_tr_b16 v[226:227], v202 offset:17408\n\t" \
    "ds_read_b64_tr_b16 v[228:229], v202 offset:24576\n\t" \
    "ds_read_b64_tr_b16 v[230:231], v202 offset:25600\n\t" \
    "s_waitcnt vmcnt(29) lgkmcnt(0)\n\t" \
    "s_barrier\n\t" \
    "ds_read_b64_tr_b16 a[192:193], v201 offset:32768\n\t" \
    "ds_read_b64_tr_b16 a[194:195], v201 offset:33792\n\t" \
    "ds_read_b64_tr_b16 a[196:197], v201 offset:40960\n\t" \
    "ds_read_b64_tr_b16 a[198:199], v201 offset:41984\n\t" \
    "ds_read_b64_tr_b16 v[232:233], v202 offset:32768\n\t" \
    "ds_read_b64_tr_b16 v[234:235], v202 offset:33792\n\t" \
    "ds_read_b64_tr_b16 v[236:237], v202 offset:40960\n\t" \
    "ds_read_b64_tr_b16 v[238:239], v202 offset:41984\n\t" \
    "s_waitcnt vmcnt(25) lgkmcnt(0)\n\t" \
    "s_barrier\n\t" \
    "ds_read_b64_tr_b16 a[200:201], v201 offset:49152\n\t" \
    "ds_read_b64_tr_b16 a[202:203], v201 offset:50176\n\t" \
    "ds_read_b64_tr_b16 a[204:205], v201 offset:57344\n\t" \
    "ds_read_b64_tr_b16 a[206:207], v201 offset:58368\n\t" \
    "ds_read_b64_tr_b16 v[240:241], v202 offset:49152\n\t" \
    "ds_read_b64_tr_b16 v[242:243], v202 offset:50176\n\t" \
    "ds_read_b64_tr_b16 v[244:245], v202 offset:57344\n\t" \
    "ds_read_b64_tr_b16 v[246:247], v202 offset:58368\n\t" \
    "s_waitcnt vmcnt(21) lgkmcnt(0)\n\t" \
    "s_barrier\n\t" \
    "v_add_u32 v201, 0x10000, v201\n\t" \
    "v_add_u32 v202, 0x10000, v202\n\t" \
    "ds_read_b64_tr_b16 a[208:209], v201 offset:0\n\t" \
    "ds_read_b64_tr_b16 a[210:211], v201 offset:1024\n\t" \
    "ds_read_b64_tr_b16 a[212:213], v201 offset:8192\n\t" \
    "ds_read_b64_tr_b16 a[214:215], v201 offset:9216\n\t" \
    "ds_read_b64_tr_b16 v[248:249], v202 offset:0\n\t" \
    "ds_read_b64_tr_b16 v[250:251], v202 offset:1024\n\t" \
    "ds_read_b64_tr_b16 v[252:253], v202 offset:8192\n\t" \
    "ds_read_b64_tr_b16 v[254:255], v202 offset:9216\n\t" \
    "v_add_u32 v201, 0xfffe0000, v201\n\t" \
    "v_add_u32 v202, 0xfffe0000, v202\n\t" \
    "s_waitcnt lgkmcnt(0)\n\t" \
    "s_barrier\n\t" \
    "s_waitcnt vmcnt(0)\n\t" \
    "ds_write_b128 v3, v[120:123] offset:0\n\t" \
    "ds_write_b128 v3, v[124:127] offset:1024\n\t" \
    "ds_write_b128 v3, v[128:131] offset:2048\n\t" \
    "ds_write_b128 v3, v[132:135] offset:3072\n\t" \
    "ds_write_b128 v3, v[136:139] offset:4096\n\t" \
    "ds_write_b128 v3, v[140:143] offset:5120\n\t" \
    "ds_write_b128 v3, v[144:147] offset:6144\n\t" \
    "ds_write_b128 v3, v[148:151] offset:7168\n\t" \
    "ds_write_b128 v3, v[152:155] offset:8192\n\t" \
    "ds_write_b128 v3, v[72:75] offset:36864\n\t" \
    "ds_write_b128 v3, v[76:79] offset:37888\n\t" \
    "ds_write_b128 v3, v[80:83] offset:38912\n\t" \
    "ds_write_b128 v3, v[84:87] offset:39936\n\t" \
    "ds_write_b128 v3, v[88:91] offset:40960\n\t" \
    "ds_write_b128 v3, v[92:95] offset:41984\n\t" \
    "ds_write_b128 v3, v[96:99] offset:43008\n\t" \
    "ds_write_b128 v3, v[100:103] offset:44032\n\t" \
    "ds_write_b128 v3, v[104:107] offset:45056\n\t" \
    "gx_C1_%=:\n\t" \
    "v_cmp_ne_u32 s[54:55], v166, v168\n\t" \
    "v_sub_u32 v166, v166, v165\n\t" \
    "v_cmp_ne_u32 vcc, 1, v166\n\t" \
    "s_nop 1\n\t" \
    "s_or_b64 vcc, vcc, s[54:55]\n\t" \
    "s_cbranch_scc0 gx_Cok2_%=\n\t" \
    "s_cmp_eq_u32 s68, 0\n\t" \
    "s_cbranch_scc1 gx_F4_%=\n\t" \
    "s_sub_u32 s68, s68, 1\n\t" \
    "s_sleep 6\n\t" \
    "s_branch gx_Cag3_%=\n\t" \
    "gx_F4_%=:\n\t" \
    "v_mov_b32 v168, 5767424\n\t" \
    "v_mov_b32 v169, 4\n\t" \
    "global_store_dword v168, v169, s[36:37] sc1\n\t" \
    "s_waitcnt vmcnt(0)\n\t" \
    "s_branch gx_Cok2_%=\n\t" \
    "gx_Cag3_%=:\n\t" \
    "global_load_dwordx2 v[166:167], v164, s[36:37] sc1\n\t" \
    "global_load_dword v168, v164, s[36:37] offset:2048 sc1\n\t" \
    "s_waitcnt vmcnt(0)\n\t" \
    "s_branch gx_C1_%=\n\t" \
    "gx_Cok2_%=:\n\t" \
    "s_cmp_lg_u32 s66, 0\n\t" \
    "s_cbranch_scc1 gx_Ca5_%=\n\t" \
    "v_mov_b32 v164, 5836800\n\t" \
    "s_mov_b64 exec, 1\n\t" \
    "global_atomic_add v164, v166, s[36:37] sc1\n\t" \
    "s_mov_b64 exec, -1\n\t" \
    "gx_Ca5_%=:\n\t" \
    "s_waitcnt lgkmcnt(0)\n\t" \
    "s_barrier\n\t" \
    "ds_read_b128 v[24:27], v1 offset:0\n\t" \
    "ds_read_b128 v[28:31], v1 offset:18432\n\t" \
    "ds_read_b128 v[32:35], v2 offset:0\n\t" \
    "ds_read_b128 v[36:39], v2 offset:18432\n\t" \
    "ds_read_b128 v[40:43], v1 offset:128\n\t" \
    "ds_read_b128 v[44:47], v1 offset:18560\n\t" \
    "gx_l16_%=:\n\t" \
    "s_add_u32 s84, s65, 2\n\t" \
    "s_min_u32 s69, s84, 63\n\t" \
    "s_mul_i32 s69, s69, 36864\n\t" \
    "s_add_u32 s52, s50, s69\n\t" \
    "s_addc_u32 s53, s51, 0\n\t" \
    "s_add_u32 s90, s90, 36864\n\t" \
    "s_cmp_eq_u32 s90, 147456\n\t" \
    "s_cselect_b32 s90, 0, s90\n\t" \
    "s_add_u32 s91, s90, 36864\n\t" \
    "s_cmp_eq_u32 s91, 147456\n\t" \
    "s_cselect_b32 s91, 0, s91\n\t" \
    "s_add_u32 s91, s91, s89\n\t" \
    "v_add_u32 v22, s90, v0\n\t" \
    "v_xor_b32 v23, 64, v22\n\t" \
    "s_mul_i32 s70, s67, 7\n\t" \
    "s_add_u32 s69, s65, 0\n\t" \
    "s_xor_b32 s69, s69, s70\n\t" \
    "s_and_b32 s69, s69, 63\n\t" \
    "s_lshl_b32 s69, s69, 13\n\t" \
    "s_mul_i32 s70, s67, 524288\n\t" \
    "s_add_u32 s69, s69, s70\n\t" \
    "s_add_u32 s69, s69, 23068672\n\t" \
    "s_add_u32 s40, s36, s69\n\t" \
    "s_addc_u32 s41, s37, 0\n\t" \
    "s_waitcnt lgkmcnt(5)\n\t" \
    "v_mfma_f32_16x16x32_bf16 v[72:75], a[144:147], v[24:27], 0\n\t" \
    "ds_read_b128 v[48:51], v2 offset:128\n\t" \
    "s_waitcnt lgkmcnt(5)\n\t" \
    "v_mfma_f32_16x16x32_bf16 v[76:79], a[144:147], v[28:31], 0\n\t" \
    "ds_read_b128 v[52:55], v2 offset:18560\n\t" \
    "v_mfma_f32_16x16x32_bf16 v[80:83], a[216:219], v[24:27], 0\n\t" \
    "s_add_u32 m0, s91, 0\n\t" \
    "v_mfma_f32_16x16x32_bf16 v[84:87], a[216:219], v[28:31], 0\n\t" \
    "global_load_lds_dwordx4 v203, s[52:53]\n\t" \
    "s_waitcnt lgkmcnt(5)\n\t" \
    "v_mfma_f32_16x16x32_bf16 v[72:75], a[148:151], v[32:35], v[72:75]\n\t" \
    "ds_read_b128 v[56:59], v1 offset:256\n\t" \
    "s_waitcnt lgkmcnt(5)\n\t" \
    "v_mfma_f32_16x16x32_bf16 v[76:79], a[148:151], v[36:39], v[76:79]\n\t" \
    "ds_read_b128 v[60:63], v1 offset:18688\n\t" \
    "v_mfma_f32_16x16x32_bf16 v[80:83], a[220:223], v[32:35], v[80:83]\n\t" \
    "s_add_u32 m0, s91, 1024\n\t" \
    "v_mfma_f32_16x16x32_bf16 v[84:87], a[220:223], v[36:39], v[84:87]\n\t" \
    "global_load_lds_dwordx4 v204, s[52:53]\n\t" \
    "s_waitcnt lgkmcnt(5)\n\t" \
    "v_mfma_f32_16x16x32_bf16 v[72:75], a[152:155], v[40:43], v[72:75]\n\t" \
    "ds_read_b128 v[64:67], v2 offset:256\n\t" \
    "s_waitcnt lgkmcnt(5)\n\t" \
    "v_mfma_f32_16x16x32_bf16 v[76:79], a[152:155], v[44:47], v[76:79]\n\t" \
    "ds_read_b128 v[68:71], v2 offset:18688\n\t" \
    "v_mfma_f32_16x16x32_bf16 v[80:83], a[224:227], v[40:43], v[80:83]\n\t" \
    "s_add_u32 m0, s91, 2048\n\t" \
    "v_mfma_f32_16x16x32_bf16 v[84:87], a[224:227], v[44:47], v[84:87]\n\t" \
    "global_load_lds_dwordx4 v205, s[52:53]\n\t" \
    "s_waitcnt lgkmcnt(5)\n\t" \
    "v_mfma_f32_16x16x32_bf16 v[72:75], a[156:159], v[48:51], v[72:75]\n\t" \
    "ds_read_b128 v[24:27], v1 offset:384\n\t" \
    "s_waitcnt lgkmcnt(5)\n\t" \
    "v_mfma_f32_16x16x32_bf16 v[76:79], a[156:159], v[52:55], v[76:79]\n\t" \
    "ds_read_b128 v[28:31], v1 offset:18816\n\t" \
    "v_mfma_f32_16x16x32_bf16 v[80:83], a[228:231], v[48:51], v[80:83]\n\t" \
    "s_add_u32 m0, s91, 3072\n\t" \
    "v_mfma_f32_16x16x32_bf16 v[84:87], a[228:231], v[52:55], v[84:87]\n\t" \
    "global_load_lds_dwordx4 v206, s[52:53]\n\t" \
    "s_waitcnt lgkmcnt(5)\n\t" \
    "v_mfma_f32_16x16x32_bf16 v[72:75], a[160:163], v[56:59], v[72:75]\n\t" \
    "ds_read_b128 v[32:35], v2 offset:384\n\t" \
    "s_waitcnt lgkmcnt(5)\n\t" \
    "v_mfma_f32_16x16x32_bf16 v[76:79], a[160:163], v[60:63], v[76:79]\n\t" \
    "ds_read_b128 v[36:39], v2 offset:18816\n\t" \
    "v_mfma_f32_16x16x32_bf16 v[80:83], a[232:235], v[56:59], v[80:83]\n\t" \
    "s_add_u32 m0, s91, 4096\n\t" \
    "v_mfma_f32_16x16x32_bf16 v[84:87], a[232:235], v[60:63], v[84:87]\n\t" \
    "global_load_lds_dwordx4 v207, s[52:53]\n\t" \
    "s_waitcnt lgkmcnt(5)\n\t" \
    "v_mfma_f32_16x16x32_bf16 v[72:75], a[164:167], v[64:67], v[72:75]\n\t" \
    "ds_read_b128 v[40:43], v1 offset:512\n\t" \
    "s_waitcnt lgkmcnt(5)\n\t" \
    "v_mfma_f32_16x16x32_bf16 v[76:79], a[164:167], v[68:71], v[76:79]\n\t" \
    "ds_read_b128 v[44:47], v1 offset:18944\n\t" \
    "v_mfma_f32_16x16x32_bf16 v[80:83], a[236:239], v[64:67], v[80:83]\n\t" \
    "s_add_u32 m0, s91, 5120\n\t" \
    "v_mfma_f32_16x16x32_bf16 v[84:87], a[236:239], v[68:71], v[84:87]\n\t" \
    "global_load_lds_dwordx4 v208, s[52:53]\n\t" \
    "s_waitcnt lgkmcnt(5)\n\t" \
    "v_mfma_f32_16x16x32_bf16 v[72:75], a[168:171], v[24:27], v[72:75]\n\t" \
    "ds_read_b128 v[48:51], v2 offset:512\n\t" \
    "s_waitcnt lgkmcnt(5)\n\t" \
    "v_mfma_f32_16x16x32_bf16 v[76:79], a[168:171], v[28:31], v[76:79]\n\t" \
    "ds_read_b128 v[52:55], v2 offset:18944\n\t" \
    "v_mfma_f32_16x16x32_bf16 v[80:83], a[240:243], v[24:27], v[80:83]\n\t" \
    "s_add_u32 m0, s91, 6144\n\t" \
    "v_mfma_f32_16x16x32_bf16 v[84:87], a[240:243], v[28:31], v[84:87]\n\t" \
    "global_load_lds_dwordx4 v209, s[52:53]\n\t" \
    "s_waitcnt lgkmcnt(5)\n\t" \
    "v_mfma_f32_16x16x32_bf16 v[72:75], a[172:175], v[32:35], v[72:75]\n\t" \
    "ds_read_b128 v[56:59], v1 offset:640\n\t" \
    "s_waitcnt lgkmcnt(5)\n\t" \
    "v_mfma_f32_16x16x32_bf16 v[76:79], a[172:175], v[36:39], v[76:79]\n\t" \
    "ds_read_b128 v[60:63], v1 offset:19072\n\t" \
    "v_mfma_f32_16x16x32_bf16 v[80:83], a[244:247], v[32:35], v[80:83]\n\t" \
    "s_add_u32 m0, s91, 7168\n\t" \
    "v_mfma_f32_16x16x32_bf16 v[84:87], a[244:247], v[36:39], v[84:87]\n\t" \
    "global_load_lds_dwordx4 v210, s[52:53]\n\t" \
    "s_waitcnt lgkmcnt(5)\n\t" \
    "v_mfma_f32_16x16x32_bf16 v[72:75], a[176:179], v[40:43], v[72:75]\n\t" \
    "ds_read_b128 v[64:67], v2 offset:640\n\t" \
    "s_waitcnt lgkmcnt(5)\n\t" \
    "v_mfma_f32_16x16x32_bf16 v[76:79], a[176:179], v[44:47], v[76:79]\n\t" \
    "ds_read_b128 v[68:71], v2 offset:19072\n\t" \
    "v_mfma_f32_16x16x32_bf16 v[80:83], a[248:251], v[40:43], v[80:83]\n\t" \
    "s_add_u32 m0, s91, 8192\n\t" \
    "v_mfma_f32_16x16x32_bf16 v[84:87], a[248:251], v[44:47], v[84:87]\n\t" \
    "global_load_lds_dwordx4 v211, s[52:53]\n\t" \
    "s_waitcnt lgkmcnt(5)\n\t" \
    "v_mfma_f32_16x16x32_bf16 v[72:75], a[180:183], v[48:51], v[72:75]\n\t" \
    "ds_read_b128 v[24:27], v1 offset:768\n\t" \
    "s_waitcnt lgkmcnt(5)\n\t" \
    "v_mfma_f32_16x16x32_bf16 v[76:79], a[180:183], v[52:55], v[76:79]\n\t" \
    "ds_read_b128 v[28:31], v1 offset:19200\n\t" \
    "v_mfma_f32_16x16x32_bf16 v[80:83], a[252:255], v[48:51], v[80:83]\n\t" \
    "v_mfma_f32_16x16x32_bf16 v[84:87], a[252:255], v[52:55], v[84:87]\n\t" \
    "s_waitcnt lgkmcnt(5)\n\t" \
    "v_mfma_f32_16x16x32_bf16 v[72:75], a[184:187], v[56:59], v[72:75]\n\t" \
    "ds_read_b128 v[32:35], v2 offset:768\n\t" \
    "s_waitcnt lgkmcnt(5)\n\t" \
    "v_mfma_f32_16x16x32_bf16 v[76:79], a[184:187], v[60:63], v[76:79]\n\t" \
    "ds_read_b128 v[36:39], v2 offset:19200\n\t" \
    "v_mfma_f32_16x16x32_bf16 v[80:83], v[224:227], v[56:59], v[80:83]\n\t" \
    "s_waitcnt vmcnt(9)\n\t" \
    "s_barrier\n\t" \
    "v_mfma_f32_16x16x32_bf16 v[84:87], v[224:227], v[60:63], v[84:87]\n\t" \
    "s_cmp_lg_u32 s66, 0\n\t" \
    "s_cbranch_scc1 gx_SF9_%=\n\t" \
    "s_lshl_b32 s69, s67, 7\n\t" \
    "s_add_u32 s69, s69, 5771264\n\t" \
    "s_add_u32 s70, s65, -1\n\t" \
    "v_mov_b32 v156, s69\n\t" \
    "v_mov_b32 v157, s70\n\t" \
    "s_mov_b64 exec, 1\n\t" \
    "global_store_dword v156, v157, s[36:37] sc1\n\t" \
    "s_mov_b64 exec, -1\n\t" \
    "gx_SF9_%=:\n\t" \
    "s_waitcnt lgkmcnt(5)\n\t" \
    "v_mfma_f32_16x16x32_bf16 v[72:75], a[188:191], v[64:67], v[72:75]\n\t" \
    "ds_read_b128 v[40:43], v1 offset:896\n\t" \
    "s_waitcnt lgkmcnt(5)\n\t" \
    "v_mfma_f32_16x16x32_bf16 v[76:79], a[188:191], v[68:71], v[76:79]\n\t" \
    "ds_read_b128 v[44:47], v1 offset:19328\n\t" \
    "v_mfma_f32_16x16x32_bf16 v[80:83], v[228:231], v[64:67], v[80:83]\n\t" \
    "v_mfma_f32_16x16x32_bf16 v[84:87], v[228:231], v[68:71], v[84:87]\n\t" \
    "s_waitcnt lgkmcnt(5)\n\t" \
    "v_mfma_f32_16x16x32_bf16 v[72:75], a[192:195], v[24:27], v[72:75]\n\t" \
    "ds_read_b128 v[48:51], v2 offset:896\n\t" \
    "s_waitcnt lgkmcnt(5)\n\t" \
    "v_mfma_f32_16x16x32_bf16 v[76:79], a[192:195], v[28:31], v[76:79]\n\t" \
    "ds_read_b128 v[52:55], v2 offset:19328\n\t" \
    "v_mfma_f32_16x16x32_bf16 v[80:83], v[232:235], v[24:27], v[80:83]\n\t" \
    "v_mfma_f32_16x16x32_bf16 v[84:87], v[232:235], v[28:31], v[84:87]\n\t" \
    "s_waitcnt lgkmcnt(5)\n\t" \
    "v_mfma_f32_16x16x32_bf16 v[72:75], a[196:199], v[32:35], v[72:75]\n\t" \
    "ds_read_b128 v[56:59], v1 offset:1024\n\t" \
    "s_waitcnt lgkmcnt(5)\n\t" \
    "v_mfma_f32_16x16x32_bf16 v[76:79], a[196:199], v[36:39], v[76:79]\n\t" \
    "ds_read_b128 v[60:63], v1 offset:19456\n\t" \
    "v_mfma_f32_16x16x32_bf16 v[80:83], v[236:239], v[32:35], v[80:83]\n\t" \
    "v_mfma_f32_16x16x32_bf16 v[84:87], v[236:239], v[36:39], v[84:87]\n\t" \
    "s_waitcnt lgkmcnt(5)\n\t" \
    "v_mfma_f32_16x16x32_bf16 v[72:75], a[200:203], v[40:43], v[72:75]\n\t" \
    "ds_read_b128 v[64:67], v2 offset:1024\n\t" \
    "s_waitcnt lgkmcnt(5)\n\t" \
    "v_mfma_f32_16x16x32_bf16 v[76:79], a[200:203], v[44:47], v[76:79]\n\t" \
    "ds_read_b128 v[68:71], v2 offset:19456\n\t" \
    "v_mfma_f32_16x16x32_bf16 v[80:83], v[240:243], v[40:43], v[80:83]\n\t" \
    "v_mfma_f32_16x16x32_bf16 v[84:87], v[240:243], v[44:47], v[84:87]\n\t" \
    "s_waitcnt lgkmcnt(5)\n\t" \
    "v_mfma_f32_16x16x32_bf16 v[72:75], a[204:207], v[48:51], v[72:75]\n\t" \
    "ds_read_b128 v[24:27], v22 offset:0\n\t" \
    "s_waitcnt lgkmcnt(5)\n\t" \
    "v_mfma_f32_16x16x32_bf16 v[76:79], a[204:207], v[52:55], v[76:79]\n\t" \
    "ds_read_b128 v[28:31], v22 offset:18432\n\t" \
    "v_mfma_f32_16x16x32_bf16 v[80:83], v[244:247], v[48:51], v[80:83]\n\t" \
    "v_mfma_f32_16x16x32_bf16 v[84:87], v[244:247], v[52:55], v[84:87]\n\t" \
    "s_waitcnt lgkmcnt(5)\n\t" \
    "v_mfma_f32_16x16x32_bf16 v[72:75], a[208:211], v[56:59], v[72:75]\n\t" \
    "ds_read_b128 v[32:35], v23 offset:0\n\t" \
    "s_waitcnt lgkmcnt(5)\n\t" \
    "v_mfma_f32_16x16x32_bf16 v[76:79], a[208:211], v[60:63], v[76:79]\n\t" \
    "ds_read_b128 v[36:39], v23 offset:18432\n\t" \
    "v_mfma_f32_16x16x32_bf16 v[80:83], v[248:251], v[56:59], v[80:83]\n\t" \
    "v_mfma_f32_16x16x32_bf16 v[84:87], v[248:251], v[60:63], v[84:87]\n\t" \
    "s_waitcnt lgkmcnt(5)\n\t" \
    "v_mfma_f32_16x16x32_bf16 v[72:75], a[212:215], v[64:67], v[72:75]\n\t" \
    "ds_read_b128 v[40:43], v22 offset:128\n\t" \
    "s_waitcnt lgkmcnt(5)\n\t" \
    "v_mfma_f32_16x16x32_bf16 v[76:79], a[212:215], v[68:71], v[76:79]\n\t" \
    "ds_read_b128 v[44:47], v22 offset:18560\n\t" \
    "v_mfma_f32_16x16x32_bf16 v[80:83], v[252:255], v[64:67], v[80:83]\n\t" \
    "v_mfma_f32_16x16x32_bf16 v[84:87], v[252:255], v[68:71], v[84:87]\n\t" \
    "v_mov_b32 v1, v22\n\t" \
    "v_mov_b32 v2, v23\n\t" \
    "v_cvt_pk_bf16_f32 v104, v72, v73\n\t" \
    "v_cvt_pk_bf16_f32 v105, v74, v75\n\t" \
    "s_nop 2\n\t" \
    "v_cvt_pk_bf16_f32 v106, v80, v81\n\t" \
    "v_cvt_pk_bf16_f32 v107, v82, v83\n\t" \
    "global_store_dwordx4 v8, v[104:107], s[40:41] offset:0 sc1\n\t" \
    "v_cvt_pk_bf16_f32 v108, v76, v77\n\t" \
    "v_cvt_pk_bf16_f32 v109, v78, v79\n\t" \
    "v_cvt_pk_bf16_f32 v110, v84, v85\n\t" \
    "v_cvt_pk_bf16_f32 v111, v86, v87\n\t" \
    "global_store_dwordx4 v8, v[108:111], s[40:41] offset:1024 sc1\n\t" \
    "s_cmp_ge_u32 s65, 1\n\t" \
    "s_cbranch_scc1 gx_nofast7_%=\n\t" \
    "s_waitcnt vmcnt(0)\n\t" \
    "s_barrier\n\t" \
    "s_cmp_lg_u32 s66, 0\n\t" \
    "s_cbranch_scc1 gx_SF10_%=\n\t" \
    "s_lshl_b32 s69, s67, 7\n\t" \
    "s_add_u32 s69, s69, 5771264\n\t" \
    "s_add_u32 s70, s65, 0\n\t" \
    "v_mov_b32 v156, s69\n\t" \
    "v_mov_b32 v157, s70\n\t" \
    "s_mov_b64 exec, 1\n\t" \
    "global_store_dword v156, v157, s[36:37] sc1\n\t" \
    "s_mov_b64 exec, -1\n\t" \
    "gx_SF10_%=:\n\t" \
    "gx_nofast7_%=:\n\t" \
    "s_sub_u32 s69, s65, 4\n\t" \
    "s_cmp_gt_u32 s69, 4\n\t" \
    "s_cbranch_scc1 gx_nt11_%=\n\t" \
    "s_lshl_b32 s69, s69, 7\n\t" \
    "s_lshl_b32 s70, s66, 5\n\t" \
    "s_add_u32 s69, s69, s70\n\t" \
    "v_lshrrev_b32 v156, 3, v6\n\t" \
    "v_add_u32 v156, s69, v156\n\t" \
    "v_min_u32 v156, 575, v156\n\t" \
    "v_and_b32 v157, 4, v6\n\t" \
    "v_lshlrev_b32 v157, 5, v157\n\t" \
    "v_lshl_add_u32 v156, v156, 13, v157\n\t" \
    "s_lshl_b32 s69, s67, 8\n\t" \
    "v_add_u32 v156, s69, v156\n\t" \
    "s_mov_b32 m0, 155648\n\t" \
    "s_nop 0\n\t" \
    "global_load_lds_dword v156, s[44:45]\n\t" \
    "gx_nt11_%=:\n\t" \
    "s_add_u32 s65, s65, 1\n\t" \
    "s_cmp_lt_u32 s65, 9\n\t" \
    "s_cbranch_scc1 gx_l16_%=\n\t" \
    "s_waitcnt vmcnt(0) lgkmcnt(0)\n\t" \
    "s_barrier\n\t" \
    "s_cmp_lg_u32 s66, 0\n\t" \
    "s_cbranch_scc1 gx_SF12_%=\n\t" \
    "s_lshl_b32 s69, s67, 7\n\t" \
    "s_add_u32 s69, s69, 5771264\n\t" \
    "s_add_u32 s70, s65, -1\n\t" \
    "v_mov_b32 v156, s69\n\t" \
    "v_mov_b32 v157, s70\n\t" \
    "s_mov_b64 exec, 1\n\t" \
    "global_store_dword v156, v157, s[36:37] sc1\n\t" \
    "s_mov_b64 exec, -1\n\t" \
    "gx_SF12_%=:\n\t" \
    "s_add_u32 s46, s44, 0\n\t" \
    "s_addc_u32 s47, s45, 0\n\t" \
    "s_add_u32 m0, s82, 0\n\t" \
    "s_nop 0\n\t" \
    "global_load_lds_dwordx4 v200, s[46:47]\n\t" \
    "s_add_u32 s46, s44, 131072\n\t" \
    "s_addc_u32 s47, s45, 0\n\t" \
    "s_add_u32 m0, s82, 4096\n\t" \
    "s_nop 0\n\t" \
    "global_load_lds_dwordx4 v200, s[46:47]\n\t" \
    "s_add_u32 s46, s44, 262144\n\t" \
    "s_addc_u32 s47, s45, 0\n\t" \
    "s_add_u32 m0, s82, 8192\n\t" \
    "s_nop 0\n\t" \
    "global_load_lds_dwordx4 v200, s[46:47]\n\t" \
    "s_add_u32 s46, s44, 393216\n\t" \
    "s_addc_u32 s47, s45, 0\n\t" \
    "s_add_u32 m0, s82, 12288\n\t" \
    "s_nop 0\n\t" \
    "global_load_lds_dwordx4 v200, s[46:47]\n\t" \
    "s_add_u32 s46, s44, 524288\n\t" \
    "s_addc_u32 s47, s45, 0\n\t" \
    "s_add_u32 m0, s82, 16384\n\t" \
    "s_nop 0\n\t" \
    "global_load_lds_dwordx4 v200, s[46:47]\n\t" \
    "s_add_u32 s46, s44, 655360\n\t" \
    "s_addc_u32 s47, s45, 0\n\t" \
    "s_add_u32 m0, s82, 20480\n\t" \
    "s_nop 0\n\t" \
    "global_load_lds_dwordx4 v200, s[46:47]\n\t" \
    "s_add_u32 s46, s44, 786432\n\t" \
    "s_addc_u32 s47, s45, 0\n\t" \
    "s_add_u32 m0, s82, 24576\n\t" \
    "s_nop 0\n\t" \
    "global_load_lds_dwordx4 v200, s[46:47]\n\t" \
    "s_add_u32 s46, s44, 917504\n\t" \
    "s_addc_u32 s47, s45, 0\n\t" \
    "s_add_u32 m0, s82, 28672\n\t" \
    "s_nop 0\n\t" \
    "global_load_lds_dwordx4 v200, s[46:47]\n\t" \
    "s_add_u32 s46, s44, 1048576\n\t" \
    "s_addc_u32 s47, s45, 0\n\t" \
    "s_add_u32 m0, s82, 32768\n\t" \
    "s_nop 0\n\t" \
    "global_load_lds_dwordx4 v200, s[46:47]\n\t" \
    "s_add_u32 s46, s44, 1179648\n\t" \
    "s_addc_u32 s47, s45, 0\n\t" \
    "s_add_u32 m0, s82, 36864\n\t" \
    "s_nop 0\n\t" \
    "global_load_lds_dwordx4 v200, s[46:47]\n\t" \
    "s_add_u32 s46, s44, 1310720\n\t" \
    "s_addc_u32 s47, s45, 0\n\t" \
    "s_add_u32 m0, s82, 40960\n\t" \
    "s_nop 0\n\t" \
    "global_load_lds_dwordx4 v200, s[46:47]\n\t" \
    "s_add_u32 s46, s44, 1441792\n\t" \
    "s_addc_u32 s47, s45, 0\n\t" \
    "s_add_u32 m0, s82, 45056\n\t" \
    "s_nop 0\n\t" \
    "global_load_lds_dwordx4 v200, s[46:47]\n\t" \
    "s_add_u32 s46, s44, 1572864\n\t" \
    "s_addc_u32 s47, s45, 0\n\t" \
    "s_add_u32 m0, s82, 49152\n\t" \
    "s_nop 0\n\t" \
    "global_load_lds_dwordx4 v200, s[46:47]\n\t" \
    "s_add_u32 s46, s44, 1703936\n\t" \
    "s_addc_u32 s47, s45, 0\n\t" \
    "s_add_u32 m0, s82, 53248\n\t" \
    "s_nop 0\n\t" \
    "global_load_lds_dwordx4 v200, s[46:47]\n\t" \
    "s_add_u32 s46, s44, 1835008\n\t" \
    "s_addc_u32 s47, s45, 0\n\t" \
    "s_add_u32 m0, s82, 57344\n\t" \
    "s_nop 0\n\t" \
    "global_load_lds_dwordx4 v200, s[46:47]\n\t" \
    "s_add_u32 s46, s44, 1966080\n\t" \
    "s_addc_u32 s47, s45, 0\n\t" \
    "s_add_u32 m0, s82, 61440\n\t" \
    "s_nop 0\n\t" \
    "global_load_lds_dwordx4 v200, s[46:47]\n\t" \
    "s_add_u32 s46, s44, 2097152\n\t" \
    "s_addc_u32 s47, s45, 0\n\t" \
    "s_add_u32 m0, s82, 65536\n\t" \
    "s_nop 0\n\t" \
    "global_load_lds_dwordx4 v200, s[46:47]\n\t" \
    "s_add_u32 s46, s44, 2228224\n\t" \
    "s_addc_u32 s47, s45, 0\n\t" \
    "s_add_u32 m0, s82, 69632\n\t" \
    "s_nop 0\n\t" \
    "global_load_lds_dwordx4 v200, s[46:47]\n\t" \
    "s_add_u32 s46, s44, 2359296\n\t" \
    "s_addc_u32 s47, s45, 0\n\t" \
    "s_add_u32 m0, s82, 73728\n\t" \
    "s_nop 0\n\t" \
    "global_load_lds_dwordx4 v200, s[46:47]\n\t" \
    "s_add_u32 s46, s44, 2490368\n\t" \
    "s_addc_u32 s47, s45, 0\n\t" \
    "s_add_u32 m0, s82, 77824\n\t" \
    "s_nop 0\n\t" \
    "global_load_lds_dwordx4 v200, s[46:47]\n\t" \
    "s_add_u32 s46, s44, 2621440\n\t" \
    "s_addc_u32 s47, s45, 0\n\t" \
    "s_add_u32 m0, s82, 81920\n\t" \
    "s_nop 0\n\t" \
    "global_load_lds_dwordx4 v200, s[46:47]\n\t" \
    "s_add_u32 s46, s44, 2752512\n\t" \
    "s_addc_u32 s47, s45, 0\n\t" \
    "s_add_u32 m0, s82, 86016\n\t" \
    "s_nop 0\n\t" \
    "global_load_lds_dwordx4 v200, s[46:47]\n\t" \
    "s_add_u32 s46, s44, 2883584\n\t" \
    "s_addc_u32 s47, s45, 0\n\t" \
    "s_add_u32 m0, s82, 90112\n\t" \
    "s_nop 0\n\t" \
    "global_load_lds_dwordx4 v200, s[46:47]\n\t" \
    "s_add_u32 s46, s44, 3014656\n\t" \
    "s_addc_u32 s47, s45, 0\n\t" \
    "s_add_u32 m0, s82, 94208\n\t" \
    "s_nop 0\n\t" \
    "global_load_lds_dwordx4 v200, s[46:47]\n\t" \
    "s_add_u32 s46, s44, 3145728\n\t" \
    "s_addc_u32 s47, s45, 0\n\t" \
    "s_add_u32 m0, s82, 98304\n\t" \
    "s_nop 0\n\t" \
    "global_load_lds_dwordx4 v200, s[46:47]\n\t" \
    "s_add_u32 s46, s44, 3276800\n\t" \
    "s_addc_u32 s47, s45, 0\n\t" \
    "s_add_u32 m0, s82, 102400\n\t" \
    "s_nop 0\n\t" \
    "global_load_lds_dwordx4 v200, s[46:47]\n\t" \
    "s_add_u32 s46, s44, 3407872\n\t" \
    "s_addc_u32 s47, s45, 0\n\t" \
    "s_add_u32 m0, s82, 106496\n\t" \
    "s_nop 0\n\t" \
    "global_load_lds_dwordx4 v200, s[46:47]\n\t" \
    "s_add_u32 s46, s44, 3538944\n\t" \
    "s_addc_u32 s47, s45, 0\n\t" \
    "s_add_u32 m0, s82, 110592\n\t" \
    "s_nop 0\n\t" \
    "global_load_lds_dwordx4 v200, s[46:47]\n\t" \
    "s_add_u32 s46, s44, 3670016\n\t" \
    "s_addc_u32 s47, s45, 0\n\t" \
    "s_add_u32 m0, s82, 114688\n\t" \
    "s_nop 0\n\t" \
    "global_load_lds_dwordx4 v200, s[46:47]\n\t" \
    "s_add_u32 s46, s44, 3801088\n\t" \
    "s_addc_u32 s47, s45, 0\n\t" \
    "s_add_u32 m0, s82, 118784\n\t" \
    "s_nop 0\n\t" \
    "global_load_lds_dwordx4 v200, s[46:47]\n\t" \
    "s_add_u32 s46, s44, 3932160\n\t" \
    "s_addc_u32 s47, s45, 0\n\t" \
    "s_add_u32 m0, s82, 122880\n\t" \
    "s_nop 0\n\t" \
    "global_load_lds_dwordx4 v200, s[46:47]\n\t" \
    "s_add_u32 s46, s44, 4063232\n\t" \
    "s_addc_u32 s47, s45, 0\n\t" \
    "s_add_u32 m0, s82, 126976\n\t" \
    "s_nop 0\n\t" \
    "global_load_lds_dwordx4 v200, s[46:47]\n\t" \
    "s_add_u32 s46, s44, 4194304\n\t" \
    "s_addc_u32 s47, s45, 0\n\t" \
    "s_add_u32 m0, s82, 131072\n\t" \
    "s_nop 0\n\t" \
    "global_load_lds_dwordx4 v200, s[46:47]\n\t" \
    "s_add_u32 s46, s44, 4325376\n\t" \
    "s_addc_u32 s47, s45, 0\n\t" \
    "s_add_u32 m0, s82, 135168\n\t" \
    "s_nop 0\n\t" \
    "global_load_lds_dwordx4 v200, s[46:47]\n\t" \
    "s_add_u32 s46, s44, 4456448\n\t" \
    "s_addc_u32 s47, s45, 0\n\t" \
    "s_add_u32 m0, s82, 139264\n\t" \
    "s_nop 0\n\t" \
    "global_load_lds_dwordx4 v200, s[46:47]\n\t" \
    "s_add_u32 s46, s44, 4587520\n\t" \
    "s_addc_u32 s47, s45, 0\n\t" \
    "s_add_u32 m0, s82, 143360\n\t" \
    "s_nop 0\n\t" \
    "global_load_lds_dwordx4 v200, s[46:47]\n\t" \
    "global_load_dwordx4 v[120:123], v203, s[50:51]\n\t" \
    "global_load_dwordx4 v[124:127], v204, s[50:51]\n\t" \
    "global_load_dwordx4 v[128:131], v205, s[50:51]\n\t" \
    "global_load_dwordx4 v[132:135], v206, s[50:51]\n\t" \
    "global_load_dwordx4 v[136:139], v207, s[50:51]\n\t" \
    "global_load_dwordx4 v[140:143], v208, s[50:51]\n\t" \
    "global_load_dwordx4 v[144:147], v209, s[50:51]\n\t" \
    "global_load_dwordx4 v[148:151], v210, s[50:51]\n\t" \
    "global_load_dwordx4 v[152:155], v211, s[50:51]\n\t" \
    "s_add_u32 s52, s50, 331776\n\t" \
    "s_addc_u32 s53, s51, 0\n\t" \
    "global_load_dwordx4 v[72:75], v203, s[52:53]\n\t" \
    "global_load_dwordx4 v[76:79], v204, s[52:53]\n\t" \
    "global_load_dwordx4 v[80:83], v205, s[52:53]\n\t" \
    "global_load_dwordx4 v[84:87], v206, s[52:53]\n\t" \
    "global_load_dwordx4 v[88:91], v207, s[52:53]\n\t" \
    "global_load_dwordx4 v[92:95], v208, s[52:53]\n\t" \
    "global_load_dwordx4 v[96:99], v209, s[52:53]\n\t" \
    "global_load_dwordx4 v[100:103], v210, s[52:53]\n\t" \
    "global_load_dwordx4 v[104:107], v211, s[52:53]\n\t" \
    "s_waitcnt vmcnt(50) lgkmcnt(0)\n\t" \
    "s_barrier\n\t" \
    "ds_read_b64_tr_b16 a[0:1], v201 offset:0\n\t" \
    "ds_read_b64_tr_b16 a[2:3], v201 offset:1024\n\t" \
    "ds_read_b64_tr_b16 a[4:5], v201 offset:8192\n\t" \
    "ds_read_b64_tr_b16 a[6:7], v201 offset:9216\n\t" \
    "ds_read_b64_tr_b16 a[72:73], v202 offset:0\n\t" \
    "ds_read_b64_tr_b16 a[74:75], v202 offset:1024\n\t" \
    "ds_read_b64_tr_b16 a[76:77], v202 offset:8192\n\t" \
    "ds_read_b64_tr_b16 a[78:79], v202 offset:9216\n\t" \
    "s_waitcnt vmcnt(46) lgkmcnt(0)\n\t" \
    "s_barrier\n\t" \
    "ds_read_b64_tr_b16 a[8:9], v201 offset:16384\n\t" \
    "ds_read_b64_tr_b16 a[10:11], v201 offset:17408\n\t" \
    "ds_read_b64_tr_b16 a[12:13], v201 offset:24576\n\t" \
    "ds_read_b64_tr_b16 a[14:15], v201 offset:25600\n\t" \
    "ds_read_b64_tr_b16 a[80:81], v202 offset:16384\n\t" \
    "ds_read_b64_tr_b16 a[82:83], v202 offset:17408\n\t" \
    "ds_read_b64_tr_b16 a[84:85], v202 offset:24576\n\t" \
    "ds_read_b64_tr_b16 a[86:87], v202 offset:25600\n\t" \
    "s_waitcnt vmcnt(42) lgkmcnt(0)\n\t" \
    "s_barrier\n\t" \
    "ds_read_b64_tr_b16 a[16:17], v201 offset:32768\n\t" \
    "ds_read_b64_tr_b16 a[18:19], v201 offset:33792\n\t" \
    "ds_read_b64_tr_b16 a[20:21], v201 offset:40960\n\t" \
    "ds_read_b64_tr_b16 a[22:23], v201 offset:41984\n\t" \
    "ds_read_b64_tr_b16 a[88:89], v202 offset:32768\n\t" \
    "ds_read_b64_tr_b16 a[90:91], v202 offset:33792\n\t" \
    "ds_read_b64_tr_b16 a[92:93], v202 offset:40960\n\t" \
    "ds_read_b64_tr_b16 a[94:95], v202 offset:41984\n\t" \
    "s_waitcnt vmcnt(38) lgkmcnt(0)\n\t" \
    "s_barrier\n\t" \
    "ds_read_b64_tr_b16 a[24:25], v201 offset:49152\n\t" \
    "ds_read_b64_tr_b16 a[26:27], v201 offset:50176\n\t" \
    "ds_read_b64_tr_b16 a[28:29], v201 offset:57344\n\t" \
    "ds_read_b64_tr_b16 a[30:31], v201 offset:58368\n\t" \
    "ds_read_b64_tr_b16 a[96:97], v202 offset:49152\n\t" \
    "ds_read_b64_tr_b16 a[98:99], v202 offset:50176\n\t" \
    "ds_read_b64_tr_b16 a[100:101], v202 offset:57344\n\t" \
    "ds_read_b64_tr_b16 a[102:103], v202 offset:58368\n\t" \
    "s_waitcnt vmcnt(34) lgkmcnt(0)\n\t" \
    "s_barrier\n\t" \
    "v_add_u32 v201, 0x10000, v201\n\t" \
    "v_add_u32 v202, 0x10000, v202\n\t" \
    "ds_read_b64_tr_b16 a[32:33], v201 offset:0\n\t" \
    "ds_read_b64_tr_b16 a[34:35], v201 offset:1024\n\t" \
    "ds_read_b64_tr_b16 a[36:37], v201 offset:8192\n\t" \
    "ds_read_b64_tr_b16 a[38:39], v201 offset:9216\n\t" \
    "ds_read_b64_tr_b16 a[104:105], v202 offset:0\n\t" \
    "ds_read_b64_tr_b16 a[106:107], v202 offset:1024\n\t" \
    "ds_read_b64_tr_b16 a[108:109], v202 offset:8192\n\t" \
    "ds_read_b64_tr_b16 a[110:111], v202 offset:9216\n\t" \
    "s_waitcnt vmcnt(30) lgkmcnt(0)\n\t" \
    "s_barrier\n\t" \
    "ds_read_b64_tr_b16 a[40:41], v201 offset:16384\n\t" \
    "ds_read_b64_tr_b16 a[42:43], v201 offset:17408\n\t" \
    "ds_read_b64_tr_b16 a[44:45], v201 offset:24576\n\t" \
    "ds_read_b64_tr_b16 a[46:47], v201 offset:25600\n\t" \
    "ds_read_b64_tr_b16 a[112:113], v202 offset:16384\n\t" \
    "ds_read_b64_tr_b16 a[114:115], v202 offset:17408\n\t" \
    "ds_read_b64_tr_b16 a[116:117], v202 offset:24576\n\t" \
    "ds_read_b64_tr_b16 a[118:119], v202 offset:25600\n\t" \
    "s_waitcnt vmcnt(26) lgkmcnt(0)\n\t" \
    "s_barrier\n\t" \
    "ds_read_b64_tr_b16 a[48:49], v201 offset:32768\n\t" \
    "ds_read_b64_tr_b16 a[50:51], v201 offset:33792\n\t" \
    "ds_read_b64_tr_b16 a[52:53], v201 offset:40960\n\t" \
    "ds_read_b64_tr_b16 a[54:55], v201 offset:41984\n\t" \
    "ds_read_b64_tr_b16 a[120:121], v202 offset:32768\n\t" \
    "ds_read_b64_tr_b16 a[122:123], v202 offset:33792\n\t" \
    "ds_read_b64_tr_b16 a[124:125], v202 offset:40960\n\t" \
    "ds_read_b64_tr_b16 a[126:127], v202 offset:41984\n\t" \
    "s_waitcnt vmcnt(22) lgkmcnt(0)\n\t" \
    "s_barrier\n\t" \
    "ds_read_b64_tr_b16 a[56:57], v201 offset:49152\n\t" \
    "ds_read_b64_tr_b16 a[58:59], v201 offset:50176\n\t" \
    "ds_read_b64_tr_b16 a[60:61], v201 offset:57344\n\t" \
    "ds_read_b64_tr_b16 a[62:63], v201 offset:58368\n\t" \
    "ds_read_b64_tr_b16 a[128:129], v202 offset:49152\n\t" \
    "ds_read_b64_tr_b16 a[130:131], v202 offset:50176\n\t" \
    "ds_read_b64_tr_b16 a[132:133], v202 offset:57344\n\t" \
    "ds_read_b64_tr_b16 a[134:135], v202 offset:58368\n\t" \
    "s_waitcnt vmcnt(18) lgkmcnt(0)\n\t" \
    "s_barrier\n\t" \
    "v_add_u32 v201, 0x10000, v201\n\t" \
    "v_add_u32 v202, 0x10000, v202\n\t" \
    "ds_read_b64_tr_b16 a[64:65], v201 offset:0\n\t" \
    "ds_read_b64_tr_b16 a[66:67], v201 offset:1024\n\t" \
    "ds_read_b64_tr_b16 a[68:69], v201 offset:8192\n\t" \
    "ds_read_b64_tr_b16 a[70:71], v201 offset:9216\n\t" \
    "ds_read_b64_tr_b16 a[136:137], v202 offset:0\n\t" \
    "ds_read_b64_tr_b16 a[138:139], v202 offset:1024\n\t" \
    "ds_read_b64_tr_b16 a[140:141], v202 offset:8192\n\t" \
    "ds_read_b64_tr_b16 a[142:143], v202 offset:9216\n\t" \
    "v_add_u32 v201, 0xfffe0000, v201\n\t" \
    "v_add_u32 v202, 0xfffe0000, v202\n\t" \
    "s_waitcnt lgkmcnt(0)\n\t" \
    "s_barrier\n\t" \
    "s_waitcnt vmcnt(0)\n\t" \
    "ds_write_b128 v3, v[120:123] offset:0\n\t" \
    "ds_write_b128 v3, v[124:127] offset:1024\n\t" \
    "ds_write_b128 v3, v[128:131] offset:2048\n\t" \
    "ds_write_b128 v3, v[132:135] offset:3072\n\t" \
    "ds_write_b128 v3, v[136:139] offset:4096\n\t" \
    "ds_write_b128 v3, v[140:143] offset:5120\n\t" \
    "ds_write_b128 v3, v[144:147] offset:6144\n\t" \
    "ds_write_b128 v3, v[148:151] offset:7168\n\t" \
    "ds_write_b128 v3, v[152:155] offset:8192\n\t" \
    "ds_write_b128 v3, v[72:75] offset:36864\n\t" \
    "ds_write_b128 v3, v[76:79] offset:37888\n\t" \
    "ds_write_b128 v3, v[80:83] offset:38912\n\t" \
    "ds_write_b128 v3, v[84:87] offset:39936\n\t" \
    "ds_write_b128 v3, v[88:91] offset:40960\n\t" \
    "ds_write_b128 v3, v[92:95] offset:41984\n\t" \
    "ds_write_b128 v3, v[96:99] offset:43008\n\t" \
    "ds_write_b128 v3, v[100:103] offset:44032\n\t" \
    "ds_write_b128 v3, v[104:107] offset:45056\n\t" \
    "s_mov_b32 s65, 0\n\t" \
    "v_mov_b32 v1, v0\n\t" \
    "v_xor_b32 v2, 64, v0\n\t" \
    "s_mov_b32 s90, 0\n\t" \
    "s_add_u32 s91, s89, 0x12000\n\t" \
    "s_add_u32 s85, s91, s89\n\t" \
    "s_add_u32 s52, s50, 36864\n\t" \
    "s_addc_u32 s53, s51, 0\n\t" \
    "s_mul_i32 s69, s67, 8192\n\t" \
    "s_add_u32 s69, s69, 175112192\n\t" \
    "s_add_u32 s42, s36, s69\n\t" \
    "s_addc_u32 s43, s37, 0\n\t" \
    "s_mov_b64 s[40:41], s[42:43]\n\t" \
    "s_waitcnt lgkmcnt(0)\n\t" \
    "s_barrier\n\t" \
    "ds_read_b128 v[24:27], v1 offset:0\n\t" \
    "ds_read_b128 v[28:31], v1 offset:18432\n\t" \
    "ds_read_b128 v[32:35], v2 offset:0\n\t" \
    "ds_read_b128 v[36:39], v2 offset:18432\n\t" \
    "ds_read_b128 v[40:43], v1 offset:128\n\t" \
    "ds_read_b128 v[44:47], v1 offset:18560\n\t" \
    ".p2align 6\n\t" \
    "s_nop 0\n\t" \
    "s_nop 0\n\t" \
    "gx_l28_%=:\n\t" \
    "s_waitcnt lgkmcnt(5)\n\t" \
    "v_mfma_f32_16x16x32_bf16 v[88:91], a[0:3], v[24:27], 0\n\t" \
    "ds_read_b128 v[48:51], v2 offset:128\n\t" \
    "s_waitcnt lgkmcnt(5)\n\t" \
    "v_mfma_f32_16x16x32_bf16 v[92:95], a[0:3], v[28:31], 0\n\t" \
    "ds_read_b128 v[52:55], v2 offset:18560\n\t" \
    "v_mfma_f32_16x16x32_bf16 v[96:99], a[72:75], v[24:27], 0\n\t" \
    "s_add_u32 m0, s91, 0\n\t" \
    "v_mfma_f32_16x16x32_bf16 v[100:103], a[72:75], v[28:31], 0\n\t" \
    "global_load_lds_dwordx4 v203, s[52:53]\n\t" \
    "s_waitcnt lgkmcnt(5)\n\t" \
    "v_mfma_f32_16x16x32_bf16 v[88:91], a[4:7], v[32:35], v[88:91]\n\t" \
    "ds_read_b128 v[56:59], v1 offset:256\n\t" \
    "s_waitcnt lgkmcnt(5)\n\t" \
    "v_mfma_f32_16x16x32_bf16 v[92:95], a[4:7], v[36:39], v[92:95]\n\t" \
    "ds_read_b128 v[60:63], v1 offset:18688\n\t" \
    "v_mfma_f32_16x16x32_bf16 v[96:99], a[76:79], v[32:35], v[96:99]\n\t" \
    "s_add_u32 m0, s91, 1024\n\t" \
    "v_mfma_f32_16x16x32_bf16 v[100:103], a[76:79], v[36:39], v[100:103]\n\t" \
    "global_load_lds_dwordx4 v204, s[52:53]\n\t" \
    "s_waitcnt lgkmcnt(5)\n\t" \
    "v_mfma_f32_16x16x32_bf16 v[88:91], a[8:11], v[40:43], v[88:91]\n\t" \
    "ds_read_b128 v[64:67], v2 offset:256\n\t" \
    "s_waitcnt lgkmcnt(5)\n\t" \
    "v_mfma_f32_16x16x32_bf16 v[92:95], a[8:11], v[44:47], v[92:95]\n\t" \
    "ds_read_b128 v[68:71], v2 offset:18688\n\t" \
    "v_cvt_pk_bf16_f32 v104, v72, v73\n\t" \
    "v_mfma_f32_16x16x32_bf16 v[96:99], a[80:83], v[40:43], v[96:99]\n\t" \
    "s_add_u32 m0, s91, 2048\n\t" \
    "v_cvt_pk_bf16_f32 v105, v74, v75\n\t" \
    "v_mfma_f32_16x16x32_bf16 v[100:103], a[80:83], v[44:47], v[100:103]\n\t" \
    "global_load_lds_dwordx4 v205, s[52:53]\n\t" \
    "s_waitcnt lgkmcnt(5)\n\t" \
    "v_mfma_f32_16x16x32_bf16 v[88:91], a[12:15], v[48:51], v[88:91]\n\t" \
    "ds_read_b128 v[24:27], v1 offset:384\n\t" \
    "s_waitcnt lgkmcnt(5)\n\t" \
    "v_mfma_f32_16x16x32_bf16 v[92:95], a[12:15], v[52:55], v[92:95]\n\t" \
    "ds_read_b128 v[28:31], v1 offset:18816\n\t" \
    "v_cvt_pk_bf16_f32 v106, v80, v81\n\t" \
    "v_mfma_f32_16x16x32_bf16 v[96:99], a[84:87], v[48:51], v[96:99]\n\t" \
    "s_add_u32 m0, s91, 3072\n\t" \
    "v_cvt_pk_bf16_f32 v107, v82, v83\n\t" \
    "v_mfma_f32_16x16x32_bf16 v[100:103], a[84:87], v[52:55], v[100:103]\n\t" \
    "global_load_lds_dwordx4 v206, s[52:53]\n\t" \
    "s_waitcnt lgkmcnt(5)\n\t" \
    "v_mfma_f32_16x16x32_bf16 v[88:91], a[16:19], v[56:59], v[88:91]\n\t" \
    "ds_read_b128 v[32:35], v2 offset:384\n\t" \
    "s_waitcnt lgkmcnt(5)\n\t" \
    "v_mfma_f32_16x16x32_bf16 v[92:95], a[16:19], v[60:63], v[92:95]\n\t" \
    "ds_read_b128 v[36:39], v2 offset:18816\n\t" \
    "global_store_dwordx4 v8, v[104:107], s[40:41] offset:0 sc1\n\t" \
    "v_mfma_f32_16x16x32_bf16 v[96:99], a[88:91], v[56:59], v[96:99]\n\t" \
    "s_add_u32 m0, s91, 4096\n\t" \
    "v_cvt_pk_bf16_f32 v108, v76, v77\n\t" \
    "v_mfma_f32_16x16x32_bf16 v[100:103], a[88:91], v[60:63], v[100:103]\n\t" \
    "global_load_lds_dwordx4 v207, s[52:53]\n\t" \
    "s_waitcnt lgkmcnt(5)\n\t" \
    "v_mfma_f32_16x16x32_bf16 v[88:91], a[20:23], v[64:67], v[88:91]\n\t" \
    "ds_read_b128 v[40:43], v1 offset:512\n\t" \
    "s_waitcnt lgkmcnt(5)\n\t" \
    "v_mfma_f32_16x16x32_bf16 v[92:95], a[20:23], v[68:71], v[92:95]\n\t" \
    "ds_read_b128 v[44:47], v1 offset:18944\n\t" \
    "v_cvt_pk_bf16_f32 v109, v78, v79\n\t" \
    "v_mfma_f32_16x16x32_bf16 v[96:99], a[92:95], v[64:67], v[96:99]\n\t" \
    "s_add_u32 m0, s91, 5120\n\t" \
    "v_cvt_pk_bf16_f32 v110, v84, v85\n\t" \
    "v_mfma_f32_16x16x32_bf16 v[100:103], a[92:95], v[68:71], v[100:103]\n\t" \
    "global_load_lds_dwordx4 v208, s[52:53]\n\t" \
    "s_waitcnt lgkmcnt(5)\n\t" \
    "v_mfma_f32_16x16x32_bf16 v[88:91], a[24:27], v[24:27], v[88:91]\n\t" \
    "ds_read_b128 v[48:51], v2 offset:512\n\t" \
    "s_waitcnt lgkmcnt(5)\n\t" \
    "v_mfma_f32_16x16x32_bf16 v[92:95], a[24:27], v[28:31], v[92:95]\n\t" \
    "ds_read_b128 v[52:55], v2 offset:18944\n\t" \
    "v_cvt_pk_bf16_f32 v111, v86, v87\n\t" \
    "v_mfma_f32_16x16x32_bf16 v[96:99], a[96:99], v[24:27], v[96:99]\n\t" \
    "s_add_u32 m0, s91, 6144\n\t" \
    "global_store_dwordx4 v8, v[108:111], s[40:41] offset:1024 sc1\n\t" \
    "v_mfma_f32_16x16x32_bf16 v[100:103], a[96:99], v[28:31], v[100:103]\n\t" \
    "global_load_lds_dwordx4 v209, s[52:53]\n\t" \
    "s_waitcnt lgkmcnt(5)\n\t" \
    "v_mfma_f32_16x16x32_bf16 v[88:91], a[28:31], v[32:35], v[88:91]\n\t" \
    "ds_read_b128 v[56:59], v1 offset:640\n\t" \
    "s_waitcnt lgkmcnt(5)\n\t" \
    "v_mfma_f32_16x16x32_bf16 v[92:95], a[28:31], v[36:39], v[92:95]\n\t" \
    "ds_read_b128 v[60:63], v1 offset:19072\n\t" \
    "v_mfma_f32_16x16x32_bf16 v[96:99], a[100:103], v[32:35], v[96:99]\n\t" \
    "s_add_u32 m0, s91, 7168\n\t" \
    "v_mfma_f32_16x16x32_bf16 v[100:103], a[100:103], v[36:39], v[100:103]\n\t" \
    "global_load_lds_dwordx4 v210, s[52:53]\n\t" \
    "s_waitcnt lgkmcnt(5)\n\t" \
    "v_mfma_f32_16x16x32_bf16 v[88:91], a[32:35], v[40:43], v[88:91]\n\t" \
    "ds_read_b128 v[64:67], v2 offset:640\n\t" \
    "s_waitcnt lgkmcnt(5)\n\t" \
    "v_mfma_f32_16x16x32_bf16 v[92:95], a[32:35], v[44:47], v[92:95]\n\t" \
    "ds_read_b128 v[68:71], v2 offset:19072\n\t" \
    "v_mfma_f32_16x16x32_bf16 v[96:99], a[104:107], v[40:43], v[96:99]\n\t" \
    "s_add_u32 m0, s91, 8192\n\t" \
    "v_mfma_f32_16x16x32_bf16 v[100:103], a[104:107], v[44:47], v[100:103]\n\t" \
    "global_load_lds_dwordx4 v211, s[52:53]\n\t" \
    "s_waitcnt lgkmcnt(5)\n\t" \
    "v_mfma_f32_16x16x32_bf16 v[88:91], a[36:39], v[48:51], v[88:91]\n\t" \
    "ds_read_b128 v[24:27], v1 offset:768\n\t" \
    "s_waitcnt lgkmcnt(5)\n\t" \
    "v_mfma_f32_16x16x32_bf16 v[92:95], a[36:39], v[52:55], v[92:95]\n\t" \
    "ds_read_b128 v[28:31], v1 offset:19200\n\t" \
    "v_mfma_f32_16x16x32_bf16 v[96:99], a[108:111], v[48:51], v[96:99]\n\t" \
    "v_mfma_f32_16x16x32_bf16 v[100:103], a[108:111], v[52:55], v[100:103]\n\t" \
    "s_waitcnt lgkmcnt(5)\n\t" \
    "v_mfma_f32_16x16x32_bf16 v[88:91], a[40:43], v[56:59], v[88:91]\n\t" \
    "ds_read_b128 v[32:35], v2 offset:768\n\t" \
    "s_waitcnt lgkmcnt(5)\n\t" \
    "v_mfma_f32_16x16x32_bf16 v[92:95], a[40:43], v[60:63], v[92:95]\n\t" \
    "ds_read_b128 v[36:39], v2 offset:19200\n\t" \
    "v_mfma_f32_16x16x32_bf16 v[96:99], a[112:115], v[56:59], v[96:99]\n\t" \
    "s_waitcnt vmcnt(11)\n\t" \
    "s_barrier\n\t" \
    "v_mfma_f32_16x16x32_bf16 v[100:103], a[112:115], v[60:63], v[100:103]\n\t" \
    "s_cmp_lg_u32 s66, 0\n\t" \
    "s_cbranch_scc1 gx_SF13_%=\n\t" \
    "s_lshl_b32 s69, s67, 7\n\t" \
    "s_add_u32 s69, s69, 5849088\n\t" \
    "s_add_u32 s70, s65, -1\n\t" \
    "v_mov_b32 v156, s69\n\t" \
    "v_mov_b32 v157, s70\n\t" \
    "s_mov_b64 exec, 1\n\t" \
    "global_store_dword v156, v157, s[36:37] sc1\n\t" \
    "s_mov_b64 exec, -1\n\t" \
    "gx_SF13_%=:\n\t" \
    "s_waitcnt lgkmcnt(5)\n\t" \
    "v_mfma_f32_16x16x32_bf16 v[88:91], a[44:47], v[64:67], v[88:91]\n\t" \
    "ds_read_b128 v[40:43], v1 offset:896\n\t" \
    "s_waitcnt lgkmcnt(5)\n\t" \
    "v_mfma_f32_16x16x32_bf16 v[92:95], a[44:47], v[68:71], v[92:95]\n\t" \
    "ds_read_b128 v[44:47], v1 offset:19328\n\t" \
    "v_mfma_f32_16x16x32_bf16 v[96:99], a[116:119], v[64:67], v[96:99]\n\t" \
    "v_add_u32 v22, 0x9000, v1\n\t" \
    "v_add_u32 v23, 0x9000, v2\n\t" \
    "v_mfma_f32_16x16x32_bf16 v[100:103], a[116:119], v[68:71], v[100:103]\n\t" \
    "s_add_u32 s84, s65, 10\n\t" \
    "s_min_u32 s69, s84, 63\n\t" \
    "s_waitcnt lgkmcnt(5)\n\t" \
    "v_mfma_f32_16x16x32_bf16 v[88:91], a[48:51], v[24:27], v[88:91]\n\t" \
    "ds_read_b128 v[48:51], v2 offset:896\n\t" \
    "s_waitcnt lgkmcnt(5)\n\t" \
    "v_mfma_f32_16x16x32_bf16 v[92:95], a[48:51], v[28:31], v[92:95]\n\t" \
    "ds_read_b128 v[52:55], v2 offset:19328\n\t" \
    "v_mfma_f32_16x16x32_bf16 v[96:99], a[120:123], v[24:27], v[96:99]\n\t" \
    "s_mul_i32 s69, s69, 36864\n\t" \
    "s_add_u32 s48, s50, s69\n\t" \
    "v_mfma_f32_16x16x32_bf16 v[100:103], a[120:123], v[28:31], v[100:103]\n\t" \
    "s_addc_u32 s49, s51, 0\n\t" \
    "s_mul_i32 s70, s67, 7\n\t" \
    "s_waitcnt lgkmcnt(5)\n\t" \
    "v_mfma_f32_16x16x32_bf16 v[88:91], a[52:55], v[32:35], v[88:91]\n\t" \
    "ds_read_b128 v[56:59], v1 offset:1024\n\t" \
    "s_waitcnt lgkmcnt(5)\n\t" \
    "v_mfma_f32_16x16x32_bf16 v[92:95], a[52:55], v[36:39], v[92:95]\n\t" \
    "ds_read_b128 v[60:63], v1 offset:19456\n\t" \
    "v_mfma_f32_16x16x32_bf16 v[96:99], a[124:127], v[32:35], v[96:99]\n\t" \
    "s_add_u32 s69, s65, 0\n\t" \
    "s_xor_b32 s69, s69, s70\n\t" \
    "v_mfma_f32_16x16x32_bf16 v[100:103], a[124:127], v[36:39], v[100:103]\n\t" \
    "s_and_b32 s69, s69, 63\n\t" \
    "s_lshl_b32 s69, s69, 13\n\t" \
    "s_waitcnt lgkmcnt(5)\n\t" \
    "v_mfma_f32_16x16x32_bf16 v[88:91], a[56:59], v[40:43], v[88:91]\n\t" \
    "ds_read_b128 v[64:67], v2 offset:1024\n\t" \
    "s_waitcnt lgkmcnt(5)\n\t" \
    "v_mfma_f32_16x16x32_bf16 v[92:95], a[56:59], v[44:47], v[92:95]\n\t" \
    "ds_read_b128 v[68:71], v2 offset:19456\n\t" \
    "v_mfma_f32_16x16x32_bf16 v[96:99], a[128:131], v[40:43], v[96:99]\n\t" \
    "s_mul_i32 s70, s67, 524288\n\t" \
    "s_add_u32 s69, s69, s70\n\t" \
    "v_mfma_f32_16x16x32_bf16 v[100:103], a[128:131], v[44:47], v[100:103]\n\t" \
    "s_add_u32 s69, s69, 6291456\n\t" \
    "s_add_u32 s38, s36, s69\n\t" \
    "s_waitcnt lgkmcnt(5)\n\t" \
    "v_mfma_f32_16x16x32_bf16 v[88:91], a[60:63], v[48:51], v[88:91]\n\t" \
    "ds_read_b128 v[24:27], v22 offset:0\n\t" \
    "s_waitcnt lgkmcnt(5)\n\t" \
    "v_mfma_f32_16x16x32_bf16 v[92:95], a[60:63], v[52:55], v[92:95]\n\t" \
    "ds_read_b128 v[28:31], v22 offset:18432\n\t" \
    "v_mfma_f32_16x16x32_bf16 v[96:99], a[132:135], v[48:51], v[96:99]\n\t" \
    "s_addc_u32 s39, s37, 0\n\t" \
    "v_mfma_f32_16x16x32_bf16 v[100:103], a[132:135], v[52:55], v[100:103]\n\t" \
    "s_waitcnt lgkmcnt(5)\n\t" \
    "v_mfma_f32_16x16x32_bf16 v[88:91], a[64:67], v[56:59], v[88:91]\n\t" \
    "ds_read_b128 v[32:35], v23 offset:0\n\t" \
    "s_waitcnt lgkmcnt(5)\n\t" \
    "v_mfma_f32_16x16x32_bf16 v[92:95], a[64:67], v[60:63], v[92:95]\n\t" \
    "ds_read_b128 v[36:39], v23 offset:18432\n\t" \
    "v_mfma_f32_16x16x32_bf16 v[96:99], a[136:139], v[56:59], v[96:99]\n\t" \
    "v_mfma_f32_16x16x32_bf16 v[100:103], a[136:139], v[60:63], v[100:103]\n\t" \
    "s_waitcnt lgkmcnt(5)\n\t" \
    "v_mfma_f32_16x16x32_bf16 v[88:91], a[68:71], v[64:67], v[88:91]\n\t" \
    "ds_read_b128 v[40:43], v22 offset:128\n\t" \
    "s_waitcnt lgkmcnt(5)\n\t" \
    "v_mfma_f32_16x16x32_bf16 v[92:95], a[68:71], v[68:71], v[92:95]\n\t" \
    "ds_read_b128 v[44:47], v22 offset:18560\n\t" \
    "v_mfma_f32_16x16x32_bf16 v[96:99], a[140:143], v[64:67], v[96:99]\n\t" \
    "v_mfma_f32_16x16x32_bf16 v[100:103], a[140:143], v[68:71], v[100:103]\n\t" \
    "s_waitcnt lgkmcnt(5)\n\t" \
    "v_mfma_f32_16x16x32_bf16 v[72:75], a[144:147], v[24:27], 0\n\t" \
    "ds_read_b128 v[48:51], v23 offset:128\n\t" \
    "s_waitcnt lgkmcnt(5)\n\t" \
    "v_mfma_f32_16x16x32_bf16 v[76:79], a[144:147], v[28:31], 0\n\t" \
    "ds_read_b128 v[52:55], v23 offset:18560\n\t" \
    "v_mfma_f32_16x16x32_bf16 v[80:83], a[216:219], v[24:27], 0\n\t" \
    "s_add_u32 m0, s91, 36864\n\t" \
    "v_mfma_f32_16x16x32_bf16 v[84:87], a[216:219], v[28:31], 0\n\t" \
    "global_load_lds_dwordx4 v203, s[48:49]\n\t" \
    "s_waitcnt lgkmcnt(5)\n\t" \
    "v_mfma_f32_16x16x32_bf16 v[72:75], a[148:151], v[32:35], v[72:75]\n\t" \
    "ds_read_b128 v[56:59], v22 offset:256\n\t" \
    "s_waitcnt lgkmcnt(5)\n\t" \
    "v_mfma_f32_16x16x32_bf16 v[76:79], a[148:151], v[36:39], v[76:79]\n\t" \
    "ds_read_b128 v[60:63], v22 offset:18688\n\t" \
    "v_mfma_f32_16x16x32_bf16 v[80:83], a[220:223], v[32:35], v[80:83]\n\t" \
    "s_add_u32 m0, s91, 37888\n\t" \
    "v_mfma_f32_16x16x32_bf16 v[84:87], a[220:223], v[36:39], v[84:87]\n\t" \
    "global_load_lds_dwordx4 v204, s[48:49]\n\t" \
    "s_waitcnt lgkmcnt(5)\n\t" \
    "v_mfma_f32_16x16x32_bf16 v[72:75], a[152:155], v[40:43], v[72:75]\n\t" \
    "ds_read_b128 v[64:67], v23 offset:256\n\t" \
    "s_waitcnt lgkmcnt(5)\n\t" \
    "v_mfma_f32_16x16x32_bf16 v[76:79], a[152:155], v[44:47], v[76:79]\n\t" \
    "ds_read_b128 v[68:71], v23 offset:18688\n\t" \
    "v_cvt_pk_bf16_f32 v104, v88, v89\n\t" \
    "v_mfma_f32_16x16x32_bf16 v[80:83], a[224:227], v[40:43], v[80:83]\n\t" \
    "s_add_u32 m0, s91, 38912\n\t" \
    "v_cvt_pk_bf16_f32 v105, v90, v91\n\t" \
    "v_mfma_f32_16x16x32_bf16 v[84:87], a[224:227], v[44:47], v[84:87]\n\t" \
    "global_load_lds_dwordx4 v205, s[48:49]\n\t" \
    "s_waitcnt lgkmcnt(5)\n\t" \
    "v_mfma_f32_16x16x32_bf16 v[72:75], a[156:159], v[48:51], v[72:75]\n\t" \
    "ds_read_b128 v[24:27], v22 offset:384\n\t" \
    "s_waitcnt lgkmcnt(5)\n\t" \
    "v_mfma_f32_16x16x32_bf16 v[76:79], a[156:159], v[52:55], v[76:79]\n\t" \
    "ds_read_b128 v[28:31], v22 offset:18816\n\t" \
    "v_cvt_pk_bf16_f32 v106, v96, v97\n\t" \
    "v_mfma_f32_16x16x32_bf16 v[80:83], a[228:231], v[48:51], v[80:83]\n\t" \
    "s_add_u32 m0, s91, 39936\n\t" \
    "v_cvt_pk_bf16_f32 v107, v98, v99\n\t" \
    "v_mfma_f32_16x16x32_bf16 v[84:87], a[228:231], v[52:55], v[84:87]\n\t" \
    "global_load_lds_dwordx4 v206, s[48:49]\n\t" \
    "s_waitcnt lgkmcnt(5)\n\t" \
    "v_mfma_f32_16x16x32_bf16 v[72:75], a[160:163], v[56:59], v[72:75]\n\t" \
    "ds_read_b128 v[32:35], v23 offset:384\n\t" \
    "s_waitcnt lgkmcnt(5)\n\t" \
    "v_mfma_f32_16x16x32_bf16 v[76:79], a[160:163], v[60:63], v[76:79]\n\t" \
    "ds_read_b128 v[36:39], v23 offset:18816\n\t" \
    "global_store_dwordx4 v8, v[104:107], s[38:39] offset:0 sc1\n\t" \
    "v_mfma_f32_16x16x32_bf16 v[80:83], a[232:235], v[56:59], v[80:83]\n\t" \
    "s_add_u32 m0, s91, 40960\n\t" \
    "v_cvt_pk_bf16_f32 v108, v92, v93\n\t" \
    "v_mfma_f32_16x16x32_bf16 v[84:87], a[232:235], v[60:63], v[84:87]\n\t" \
    "global_load_lds_dwordx4 v207, s[48:49]\n\t" \
    "s_waitcnt lgkmcnt(5)\n\t" \
    "v_mfma_f32_16x16x32_bf16 v[72:75], a[164:167], v[64:67], v[72:75]\n\t" \
    "ds_read_b128 v[40:43], v22 offset:512\n\t" \
    "s_waitcnt lgkmcnt(5)\n\t" \
    "v_mfma_f32_16x16x32_bf16 v[76:79], a[164:167], v[68:71], v[76:79]\n\t" \
    "ds_read_b128 v[44:47], v22 offset:18944\n\t" \
    "v_cvt_pk_bf16_f32 v109, v94, v95\n\t" \
    "v_mfma_f32_16x16x32_bf16 v[80:83], a[236:239], v[64:67], v[80:83]\n\t" \
    "s_add_u32 m0, s91, 41984\n\t" \
    "v_cvt_pk_bf16_f32 v110, v100, v101\n\t" \
    "v_mfma_f32_16x16x32_bf16 v[84:87], a[236:239], v[68:71], v[84:87]\n\t" \
    "global_load_lds_dwordx4 v208, s[48:49]\n\t" \
    "s_waitcnt lgkmcnt(5)\n\t" \
    "v_mfma_f32_16x16x32_bf16 v[72:75], a[168:171], v[24:27], v[72:75]\n\t" \
    "ds_read_b128 v[48:51], v23 offset:512\n\t" \
    "s_waitcnt lgkmcnt(5)\n\t" \
    "v_mfma_f32_16x16x32_bf16 v[76:79], a[168:171], v[28:31], v[76:79]\n\t" \
    "ds_read_b128 v[52:55], v23 offset:18944\n\t" \
    "v_cvt_pk_bf16_f32 v111, v102, v103\n\t" \
    "v_mfma_f32_16x16x32_bf16 v[80:83], a[240:243], v[24:27], v[80:83]\n\t" \
    "s_add_u32 m0, s91, 43008\n\t" \
    "global_store_dwordx4 v8, v[108:111], s[38:39] offset:1024 sc1\n\t" \
    "v_mfma_f32_16x16x32_bf16 v[84:87], a[240:243], v[28:31], v[84:87]\n\t" \
    "global_load_lds_dwordx4 v209, s[48:49]\n\t" \
    "s_waitcnt lgkmcnt(5)\n\t" \
    "v_mfma_f32_16x16x32_bf16 v[72:75], a[172:175], v[32:35], v[72:75]\n\t" \
    "ds_read_b128 v[56:59], v22 offset:640\n\t" \
    "s_waitcnt lgkmcnt(5)\n\t" \
    "v_mfma_f32_16x16x32_bf16 v[76:79], a[172:175], v[36:39], v[76:79]\n\t" \
    "ds_read_b128 v[60:63], v22 offset:19072\n\t" \
    "v_mfma_f32_16x16x32_bf16 v[80:83], a[244:247], v[32:35], v[80:83]\n\t" \
    "s_add_u32 m0, s91, 44032\n\t" \
    "v_mfma_f32_16x16x32_bf16 v[84:87], a[244:247], v[36:39], v[84:87]\n\t" \
    "global_load_lds_dwordx4 v210, s[48:49]\n\t" \
    "s_waitcnt lgkmcnt(5)\n\t" \
    "v_mfma_f32_16x16x32_bf16 v[72:75], a[176:179], v[40:43], v[72:75]\n\t" \
    "ds_read_b128 v[64:67], v23 offset:640\n\t" \
    "s_waitcnt lgkmcnt(5)\n\t" \
    "v_mfma_f32_16x16x32_bf16 v[76:79], a[176:179], v[44:47], v[76:79]\n\t" \
    "ds_read_b128 v[68:71], v23 offset:19072\n\t" \
    "v_mfma_f32_16x16x32_bf16 v[80:83], a[248:251], v[40:43], v[80:83]\n\t" \
    "s_add_u32 m0, s91, 45056\n\t" \
    "v_mfma_f32_16x16x32_bf16 v[84:87], a[248:251], v[44:47], v[84:87]\n\t" \
    "global_load_lds_dwordx4 v211, s[48:49]\n\t" \
    "s_waitcnt lgkmcnt(5)\n\t" \
    "v_mfma_f32_16x16x32_bf16 v[72:75], a[180:183], v[48:51], v[72:75]\n\t" \
    "ds_read_b128 v[24:27], v22 offset:768\n\t" \
    "s_waitcnt lgkmcnt(5)\n\t" \
    "v_mfma_f32_16x16x32_bf16 v[76:79], a[180:183], v[52:55], v[76:79]\n\t" \
    "ds_read_b128 v[28:31], v22 offset:19200\n\t" \
    "v_mfma_f32_16x16x32_bf16 v[80:83], a[252:255], v[48:51], v[80:83]\n\t" \
    "v_mfma_f32_16x16x32_bf16 v[84:87], a[252:255], v[52:55], v[84:87]\n\t" \
    "s_waitcnt lgkmcnt(5)\n\t" \
    "v_mfma_f32_16x16x32_bf16 v[72:75], a[184:187], v[56:59], v[72:75]\n\t" \
    "ds_read_b128 v[32:35], v23 offset:768\n\t" \
    "s_waitcnt lgkmcnt(5)\n\t" \
    "v_mfma_f32_16x16x32_bf16 v[76:79], a[184:187], v[60:63], v[76:79]\n\t" \
    "ds_read_b128 v[36:39], v23 offset:19200\n\t" \
    "v_mfma_f32_16x16x32_bf16 v[80:83], v[224:227], v[56:59], v[80:83]\n\t" \
    "s_waitcnt vmcnt(11)\n\t" \
    "s_barrier\n\t" \
    "v_mfma_f32_16x16x32_bf16 v[84:87], v[224:227], v[60:63], v[84:87]\n\t" \
    "s_add_u32 s71, s65, 8\n\t" \
    "s_cmp_lg_u32 s66, 0\n\t" \
    "s_cbranch_scc1 gx_SF14_%=\n\t" \
    "s_lshl_b32 s69, s67, 7\n\t" \
    "s_add_u32 s69, s69, 5771264\n\t" \
    "s_add_u32 s70, s71, 0\n\t" \
    "v_mov_b32 v156, s69\n\t" \
    "v_mov_b32 v157, s70\n\t" \
    "s_mov_b64 exec, 1\n\t" \
    "global_store_dword v156, v157, s[36:37] sc1\n\t" \
    "s_mov_b64 exec, -1\n\t" \
    "gx_SF14_%=:\n\t" \
    "s_waitcnt lgkmcnt(5)\n\t" \
    "v_mfma_f32_16x16x32_bf16 v[72:75], a[188:191], v[64:67], v[72:75]\n\t" \
    "ds_read_b128 v[40:43], v22 offset:896\n\t" \
    "s_waitcnt lgkmcnt(5)\n\t" \
    "v_mfma_f32_16x16x32_bf16 v[76:79], a[188:191], v[68:71], v[76:79]\n\t" \
    "ds_read_b128 v[44:47], v22 offset:19328\n\t" \
    "v_mfma_f32_16x16x32_bf16 v[80:83], v[228:231], v[64:67], v[80:83]\n\t" \
    "s_xor_b32 s90, s90, 0x12000\n\t" \
    "v_add_u32 v1, s90, v0\n\t" \
    "v_mfma_f32_16x16x32_bf16 v[84:87], v[228:231], v[68:71], v[84:87]\n\t" \
    "v_xor_b32 v2, 64, v1\n\t" \
    "s_add_u32 s84, s65, 2\n\t" \
    "s_waitcnt lgkmcnt(5)\n\t" \
    "v_mfma_f32_16x16x32_bf16 v[72:75], a[192:195], v[24:27], v[72:75]\n\t" \
    "ds_read_b128 v[48:51], v23 offset:896\n\t" \
    "s_waitcnt lgkmcnt(5)\n\t" \
    "v_mfma_f32_16x16x32_bf16 v[76:79], a[192:195], v[28:31], v[76:79]\n\t" \
    "ds_read_b128 v[52:55], v23 offset:19328\n\t" \
    "v_mfma_f32_16x16x32_bf16 v[80:83], v[232:235], v[24:27], v[80:83]\n\t" \
    "s_min_u32 s69, s84, 63\n\t" \
    "s_mul_i32 s69, s69, 36864\n\t" \
    "v_mfma_f32_16x16x32_bf16 v[84:87], v[232:235], v[28:31], v[84:87]\n\t" \
    "s_add_u32 s52, s50, s69\n\t" \
    "s_addc_u32 s53, s51, 0\n\t" \
    "s_waitcnt lgkmcnt(5)\n\t" \
    "v_mfma_f32_16x16x32_bf16 v[72:75], a[196:199], v[32:35], v[72:75]\n\t" \
    "ds_read_b128 v[56:59], v22 offset:1024\n\t" \
    "s_waitcnt lgkmcnt(5)\n\t" \
    "v_mfma_f32_16x16x32_bf16 v[76:79], a[196:199], v[36:39], v[76:79]\n\t" \
    "ds_read_b128 v[60:63], v22 offset:19456\n\t" \
    "v_mfma_f32_16x16x32_bf16 v[80:83], v[236:239], v[32:35], v[80:83]\n\t" \
    "s_mul_i32 s70, s67, 7\n\t" \
    "s_add_u32 s69, s65, 9\n\t" \
    "v_mfma_f32_16x16x32_bf16 v[84:87], v[236:239], v[36:39], v[84:87]\n\t" \
    "s_xor_b32 s69, s69, s70\n\t" \
    "s_and_b32 s69, s69, 63\n\t" \
    "s_waitcnt lgkmcnt(5)\n\t" \
    "v_mfma_f32_16x16x32_bf16 v[72:75], a[200:203], v[40:43], v[72:75]\n\t" \
    "ds_read_b128 v[64:67], v23 offset:1024\n\t" \
    "s_waitcnt lgkmcnt(5)\n\t" \
    "v_mfma_f32_16x16x32_bf16 v[76:79], a[200:203], v[44:47], v[76:79]\n\t" \
    "ds_read_b128 v[68:71], v23 offset:19456\n\t" \
    "v_mfma_f32_16x16x32_bf16 v[80:83], v[240:243], v[40:43], v[80:83]\n\t" \
    "s_lshl_b32 s69, s69, 13\n\t" \
    "s_mul_i32 s70, s67, 524288\n\t" \
    "v_mfma_f32_16x16x32_bf16 v[84:87], v[240:243], v[44:47], v[84:87]\n\t" \
    "s_add_u32 s69, s69, s70\n\t" \
    "s_add_u32 s69, s69, 23068672\n\t" \
    "s_waitcnt lgkmcnt(5)\n\t" \
    "v_mfma_f32_16x16x32_bf16 v[72:75], a[204:207], v[48:51], v[72:75]\n\t" \
    "ds_read_b128 v[24:27], v1 offset:0\n\t" \
    "s_waitcnt lgkmcnt(5)\n\t" \
    "v_mfma_f32_16x16x32_bf16 v[76:79], a[204:207], v[52:55], v[76:79]\n\t" \
    "ds_read_b128 v[28:31], v1 offset:18432\n\t" \
    "v_mfma_f32_16x16x32_bf16 v[80:83], v[244:247], v[48:51], v[80:83]\n\t" \
    "s_add_u32 s40, s36, s69\n\t" \
    "s_addc_u32 s41, s37, 0\n\t" \
    "v_mfma_f32_16x16x32_bf16 v[84:87], v[244:247], v[52:55], v[84:87]\n\t" \
    "s_cmp_lt_u32 s65, 55\n\t" \
    "s_cselect_b64 s[40:41], s[40:41], s[42:43]\n\t" \
    "s_waitcnt lgkmcnt(5)\n\t" \
    "v_mfma_f32_16x16x32_bf16 v[72:75], a[208:211], v[56:59], v[72:75]\n\t" \
    "ds_read_b128 v[32:35], v2 offset:0\n\t" \
    "s_waitcnt lgkmcnt(5)\n\t" \
    "v_mfma_f32_16x16x32_bf16 v[76:79], a[208:211], v[60:63], v[76:79]\n\t" \
    "ds_read_b128 v[36:39], v2 offset:18432\n\t" \
    "v_mfma_f32_16x16x32_bf16 v[80:83], v[248:251], v[56:59], v[80:83]\n\t" \
    "s_sub_u32 s91, s85, s91\n\t" \
    "s_add_u32 s65, s65, 1\n\t" \
    "v_mfma_f32_16x16x32_bf16 v[84:87], v[248:251], v[60:63], v[84:87]\n\t" \
    "s_waitcnt lgkmcnt(5)\n\t" \
    "v_mfma_f32_16x16x32_bf16 v[72:75], a[212:215], v[64:67], v[72:75]\n\t" \
    "ds_read_b128 v[40:43], v1 offset:128\n\t" \
    "s_waitcnt lgkmcnt(5)\n\t" \
    "v_mfma_f32_16x16x32_bf16 v[76:79], a[212:215], v[68:71], v[76:79]\n\t" \
    "ds_read_b128 v[44:47], v1 offset:18560\n\t" \
    "v_mfma_f32_16x16x32_bf16 v[80:83], v[252:255], v[64:67], v[80:83]\n\t" \
    "v_mfma_f32_16x16x32_bf16 v[84:87], v[252:255], v[68:71], v[84:87]\n\t" \
    "s_cmp_lt_u32 s65, 64\n\t" \
    "s_cbranch_scc1 gx_l28_%=\n\t" \
    "s_waitcnt vmcnt(0) lgkmcnt(0)\n\t" \
    "s_barrier\n\t" \
    "s_cmp_lg_u32 s66, 0\n\t" \
    "s_cbranch_scc1 gx_SF15_%=\n\t" \
    "s_lshl_b32 s69, s67, 7\n\t" \
    "s_add_u32 s69, s69, 5849088\n\t" \
    "s_add_u32 s70, s65, -1\n\t" \
    "v_mov_b32 v156, s69\n\t" \
    "v_mov_b32 v157, s70\n\t" \
    "s_mov_b64 exec, 1\n\t" \
    "global_store_dword v156, v157, s[36:37] sc1\n\t" \
    "s_mov_b64 exec, -1\n\t" \
    "gx_SF15_%=:\n\t" \
    "gx_last_%=:\n\t" \
    ""
#define ASM_GV \
    "s_load_dwordx2 s[42:43], %3, 56\n\t" \
    "s_load_dwordx2 s[44:45], %3, 64\n\t" \
    "s_load_dwordx2 s[36:37], %3, 0x58\n\t" \
    "s_load_dwordx2 s[50:51], %3, 0x48\n\t" \
    "s_mov_b32 s66, %4\n\t" \
    "s_mov_b32 s67, %5\n\t" \
    "s_mov_b32 s68, 262144\n\t" \
    "s_mov_b32 s72, 0\n\t" \
    "s_mov_b32 s83, -1\n\t" \
    "s_mov_b32 s65, 0\n\t" \
    "s_lshl_b32 s82, s66, 10\n\t" \
    "s_waitcnt lgkmcnt(0)\n\t" \
    "s_lshr_b32 s65, s67, 5\n\t" \
    "s_cselect_b32 s88, 24, 64\n\t" \
    "s_cmp_lt_u32 s65, 24\n\t" \
    "s_addc_u32 s84, s65, 1\n\t" \
    "s_cmp_lt_u32 s84, 24\n\t" \
    "s_addc_u32 s85, s84, 1\n\t" \
    "s_mov_b32 s86, -1\n\t" \
    "s_mov_b32 s87, -1\n\t" \
    "s_add_u32 s38, s36, 48234496\n\t" \
    "s_addc_u32 s39, s37, 0\n\t" \
    "s_and_b32 s69, s67, 31\n\t" \
    "s_mul_i32 s69, s69, 147456\n\t" \
    "s_add_u32 s44, s44, s69\n\t" \
    "s_addc_u32 s45, s45, 0\n\t" \
    "s_mul_i32 s89, s66, 9216\n\t" \
    "s_mov_b32 s90, 0\n\t" \
    "v_mov_b32 v196, -1\n\t" \
    "v_mov_b32 v197, -1\n\t" \
    "v_mov_b32 v198, -1\n\t" \
    "v_mov_b32 v199, -1\n\t" \
    "s_add_u32 s46, s42, 2097152\n\t" \
    "s_addc_u32 s47, s43, 0\n\t" \
    "s_add_u32 m0, s82, 65536\n\t" \
    "s_nop 0\n\t" \
    "global_load_lds_dwordx4 v200, s[46:47]\n\t" \
    "s_add_u32 s46, s42, 2228224\n\t" \
    "s_addc_u32 s47, s43, 0\n\t" \
    "s_add_u32 m0, s82, 69632\n\t" \
    "s_nop 0\n\t" \
    "global_load_lds_dwordx4 v200, s[46:47]\n\t" \
    "s_add_u32 s46, s42, 2359296\n\t" \
    "s_addc_u32 s47, s43, 0\n\t" \
    "s_add_u32 m0, s82, 73728\n\t" \
    "s_nop 0\n\t" \
    "global_load_lds_dwordx4 v200, s[46:47]\n\t" \
    "s_add_u32 s46, s42, 2490368\n\t" \
    "s_addc_u32 s47, s43, 0\n\t" \
    "s_add_u32 m0, s82, 77824\n\t" \
    "s_nop 0\n\t" \
    "global_load_lds_dwordx4 v200, s[46:47]\n\t" \
    "s_add_u32 s46, s42, 2621440\n\t" \
    "s_addc_u32 s47, s43, 0\n\t" \
    "s_add_u32 m0, s82, 81920\n\t" \
    "s_nop 0\n\t" \
    "global_load_lds_dwordx4 v200, s[46:47]\n\t" \
    "s_add_u32 s46, s42, 2752512\n\t" \
    "s_addc_u32 s47, s43, 0\n\t" \
    "s_add_u32 m0, s82, 86016\n\t" \
    "s_nop 0\n\t" \
    "global_load_lds_dwordx4 v200, s[46:47]\n\t" \
    "s_add_u32 s46, s42, 2883584\n\t" \
    "s_addc_u32 s47, s43, 0\n\t" \
    "s_add_u32 m0, s82, 90112\n\t" \
    "s_nop 0\n\t" \
    "global_load_lds_dwordx4 v200, s[46:47]\n\t" \
    "s_add_u32 s46, s42, 3014656\n\t" \
    "s_addc_u32 s47, s43, 0\n\t" \
    "s_add_u32 m0, s82, 94208\n\t" \
    "s_nop 0\n\t" \
    "global_load_lds_dwordx4 v200, s[46:47]\n\t" \
    "s_add_u32 s46, s42, 3145728\n\t" \
    "s_addc_u32 s47, s43, 0\n\t" \
    "s_add_u32 m0, s82, 98304\n\t" \
    "s_nop 0\n\t" \
    "global_load_lds_dwordx4 v200, s[46:47]\n\t" \
    "s_add_u32 s46, s42, 3276800\n\t" \
    "s_addc_u32 s47, s43, 0\n\t" \
    "s_add_u32 m0, s82, 102400\n\t" \
    "s_nop 0\n\t" \
    "global_load_lds_dwordx4 v200, s[46:47]\n\t" \
    "s_add_u32 s46, s42, 3407872\n\t" \
    "s_addc_u32 s47, s43, 0\n\t" \
    "s_add_u32 m0, s82, 106496\n\t" \
    "s_nop 0\n\t" \
    "global_load_lds_dwordx4 v200, s[46:47]\n\t" \
    "s_add_u32 s46, s42, 3538944\n\t" \
    "s_addc_u32 s47, s43, 0\n\t" \
    "s_add_u32 m0, s82, 110592\n\t" \
    "s_nop 0\n\t" \
    "global_load_lds_dwordx4 v200, s[46:47]\n\t" \
    "s_add_u32 s46, s42, 3670016\n\t" \
    "s_addc_u32 s47, s43, 0\n\t" \
    "s_add_u32 m0, s82, 114688\n\t" \
    "s_nop 0\n\t" \
    "global_load_lds_dwordx4 v200, s[46:47]\n\t" \
    "s_add_u32 s46, s42, 3801088\n\t" \
    "s_addc_u32 s47, s43, 0\n\t" \
    "s_add_u32 m0, s82, 118784\n\t" \
    "s_nop 0\n\t" \
    "global_load_lds_dwordx4 v200, s[46:47]\n\t" \
    "s_add_u32 s46, s42, 3932160\n\t" \
    "s_addc_u32 s47, s43, 0\n\t" \
    "s_add_u32 m0, s82, 122880\n\t" \
    "s_nop 0\n\t" \
    "global_load_lds_dwordx4 v200, s[46:47]\n\t" \
    "s_add_u32 s46, s42, 4063232\n\t" \
    "s_addc_u32 s47, s43, 0\n\t" \
    "s_add_u32 m0, s82, 126976\n\t" \
    "s_nop 0\n\t" \
    "global_load_lds_dwordx4 v200, s[46:47]\n\t" \
    "s_add_u32 s46, s42, 4194304\n\t" \
    "s_addc_u32 s47, s43, 0\n\t" \
    "s_add_u32 m0, s82, 131072\n\t" \
    "s_nop 0\n\t" \
    "global_load_lds_dwordx4 v200, s[46:47]\n\t" \
    "s_add_u32 s46, s42, 4325376\n\t" \
    "s_addc_u32 s47, s43, 0\n\t" \
    "s_add_u32 m0, s82, 135168\n\t" \
    "s_nop 0\n\t" \
    "global_load_lds_dwordx4 v200, s[46:47]\n\t" \
    "s_add_u32 s46, s42, 4456448\n\t" \
    "s_addc_u32 s47, s43, 0\n\t" \
    "s_add_u32 m0, s82, 139264\n\t" \
    "s_nop 0\n\t" \
    "global_load_lds_dwordx4 v200, s[46:47]\n\t" \
    "s_add_u32 s46, s42, 4587520\n\t" \
    "s_addc_u32 s47, s43, 0\n\t" \
    "s_add_u32 m0, s82, 143360\n\t" \
    "s_nop 0\n\t" \
    "global_load_lds_dwordx4 v200, s[46:47]\n\t" \
    "s_waitcnt vmcnt(32) lgkmcnt(0)\n\t" \
    "s_barrier\n\t" \
    "ds_read_b64_tr_b16 a[0:1], v201 offset:0\n\t" \
    "ds_read_b64_tr_b16 a[2:3], v201 offset:1024\n\t" \
    "ds_read_b64_tr_b16 a[4:5], v201 offset:8192\n\t" \
    "ds_read_b64_tr_b16 a[6:7], v201 offset:9216\n\t" \
    "ds_read_b64_tr_b16 a[72:73], v202 offset:0\n\t" \
    "ds_read_b64_tr_b16 a[74:75], v202 offset:1024\n\t" \
    "ds_read_b64_tr_b16 a[76:77], v202 offset:8192\n\t" \
    "ds_read_b64_tr_b16 a[78:79], v202 offset:9216\n\t" \
    "s_waitcnt vmcnt(28) lgkmcnt(0)\n\t" \
    "s_barrier\n\t" \
    "ds_read_b64_tr_b16 a[8:9], v201 offset:16384\n\t" \
    "ds_read_b64_tr_b16 a[10:11], v201 offset:17408\n\t" \
    "ds_read_b64_tr_b16 a[12:13], v201 offset:24576\n\t" \
    "ds_read_b64_tr_b16 a[14:15], v201 offset:25600\n\t" \
    "ds_read_b64_tr_b16 a[80:81], v202 offset:16384\n\t" \
    "ds_read_b64_tr_b16 a[82:83], v202 offset:17408\n\t" \
    "ds_read_b64_tr_b16 a[84:85], v202 offset:24576\n\t" \
    "ds_read_b64_tr_b16 a[86:87], v202 offset:25600\n\t" \
    "s_waitcnt vmcnt(24) lgkmcnt(0)\n\t" \
    "s_barrier\n\t" \
    "ds_read_b64_tr_b16 a[16:17], v201 offset:32768\n\t" \
    "ds_read_b64_tr_b16 a[18:19], v201 offset:33792\n\t" \
    "ds_read_b64_tr_b16 a[20:21], v201 offset:40960\n\t" \
    "ds_read_b64_tr_b16 a[22:23], v201 offset:41984\n\t" \
    "ds_read_b64_tr_b16 a[88:89], v202 offset:32768\n\t" \
    "ds_read_b64_tr_b16 a[90:91], v202 offset:33792\n\t" \
    "ds_read_b64_tr_b16 a[92:93], v202 offset:40960\n\t" \
    "ds_read_b64_tr_b16 a[94:95], v202 offset:41984\n\t" \
    "s_waitcnt vmcnt(20) lgkmcnt(0)\n\t" \
    "s_barrier\n\t" \
    "ds_read_b64_tr_b16 a[24:25], v201 offset:49152\n\t" \
    "ds_read_b64_tr_b16 a[26:27], v201 offset:50176\n\t" \
    "ds_read_b64_tr_b16 a[28:29], v201 offset:57344\n\t" \
    "ds_read_b64_tr_b16 a[30:31], v201 offset:58368\n\t" \
    "ds_read_b64_tr_b16 a[96:97], v202 offset:49152\n\t" \
    "ds_read_b64_tr_b16 a[98:99], v202 offset:50176\n\t" \
    "ds_read_b64_tr_b16 a[100:101], v202 offset:57344\n\t" \
    "ds_read_b64_tr_b16 a[102:103], v202 offset:58368\n\t" \
    "s_add_u32 s46, s44, 0\n\t" \
    "s_addc_u32 s47, s45, 0\n\t" \
    "s_mul_i32 s69, s82, 9\n\t" \
    "s_add_u32 m0, s69, 0\n\t" \
    "s_nop 0\n\t" \
    "global_load_lds_dwordx4 v203, s[46:47]\n\t" \
    "s_add_u32 m0, s69, 1024\n\t" \
    "s_nop 0\n\t" \
    "global_load_lds_dwordx4 v204, s[46:47]\n\t" \
    "s_add_u32 m0, s69, 2048\n\t" \
    "s_nop 0\n\t" \
    "global_load_lds_dwordx4 v205, s[46:47]\n\t" \
    "s_add_u32 m0, s69, 3072\n\t" \
    "s_nop 0\n\t" \
    "global_load_lds_dwordx4 v206, s[46:47]\n\t" \
    "s_add_u32 m0, s69, 4096\n\t" \
    "s_nop 0\n\t" \
    "global_load_lds_dwordx4 v207, s[46:47]\n\t" \
    "s_add_u32 m0, s69, 5120\n\t" \
    "s_nop 0\n\t" \
    "global_load_lds_dwordx4 v208, s[46:47]\n\t" \
    "s_add_u32 m0, s69, 6144\n\t" \
    "s_nop 0\n\t" \
    "global_load_lds_dwordx4 v209, s[46:47]\n\t" \
    "s_add_u32 m0, s69, 7168\n\t" \
    "s_nop 0\n\t" \
    "global_load_lds_dwordx4 v210, s[46:47]\n\t" \
    "s_add_u32 m0, s69, 8192\n\t" \
    "s_nop 0\n\t" \
    "global_load_lds_dwordx4 v211, s[46:47]\n\t" \
    "s_min_u32 s69, s65, 63\n\t" \
    "s_mul_i32 s69, s69, 36864\n\t" \
    "s_add_u32 s52, s50, s69\n\t" \
    "s_addc_u32 s53, s51, 0\n\t" \
    "global_load_dwordx4 v[120:123], v203, s[52:53]\n\t" \
    "global_load_dwordx4 v[124:127], v204, s[52:53]\n\t" \
    "global_load_dwordx4 v[128:131], v205, s[52:53]\n\t" \
    "global_load_dwordx4 v[132:135], v206, s[52:53]\n\t" \
    "global_load_dwordx4 v[136:139], v207, s[52:53]\n\t" \
    "global_load_dwordx4 v[140:143], v208, s[52:53]\n\t" \
    "global_load_dwordx4 v[144:147], v209, s[52:53]\n\t" \
    "global_load_dwordx4 v[148:151], v210, s[52:53]\n\t" \
    "global_load_dwordx4 v[152:155], v211, s[52:53]\n\t" \
    "s_waitcnt vmcnt(34) lgkmcnt(0)\n\t" \
    "s_barrier\n\t" \
    "v_add_u32 v201, 0x10000, v201\n\t" \
    "v_add_u32 v202, 0x10000, v202\n\t" \
    "ds_read_b64_tr_b16 a[32:33], v201 offset:0\n\t" \
    "ds_read_b64_tr_b16 a[34:35], v201 offset:1024\n\t" \
    "ds_read_b64_tr_b16 a[36:37], v201 offset:8192\n\t" \
    "ds_read_b64_tr_b16 a[38:39], v201 offset:9216\n\t" \
    "ds_read_b64_tr_b16 a[104:105], v202 offset:0\n\t" \
    "ds_read_b64_tr_b16 a[106:107], v202 offset:1024\n\t" \
    "ds_read_b64_tr_b16 a[108:109], v202 offset:8192\n\t" \
    "ds_read_b64_tr_b16 a[110:111], v202 offset:9216\n\t" \
    "s_waitcnt vmcnt(30) lgkmcnt(0)\n\t" \
    "s_barrier\n\t" \
    "ds_read_b64_tr_b16 a[40:41], v201 offset:16384\n\t" \
    "ds_read_b64_tr_b16 a[42:43], v201 offset:17408\n\t" \
    "ds_read_b64_tr_b16 a[44:45], v201 offset:24576\n\t" \
    "ds_read_b64_tr_b16 a[46:47], v201 offset:25600\n\t" \
    "ds_read_b64_tr_b16 a[112:113], v202 offset:16384\n\t" \
    "ds_read_b64_tr_b16 a[114:115], v202 offset:17408\n\t" \
    "ds_read_b64_tr_b16 a[116:117], v202 offset:24576\n\t" \
    "ds_read_b64_tr_b16 a[118:119], v202 offset:25600\n\t" \
    "s_add_u32 s46, s44, 36864\n\t" \
    "s_addc_u32 s47, s45, 0\n\t" \
    "s_mul_i32 s69, s82, 9\n\t" \
    "s_add_u32 m0, s69, 36864\n\t" \
    "s_nop 0\n\t" \
    "global_load_lds_dwordx4 v203, s[46:47]\n\t" \
    "s_add_u32 m0, s69, 37888\n\t" \
    "s_nop 0\n\t" \
    "global_load_lds_dwordx4 v204, s[46:47]\n\t" \
    "s_add_u32 m0, s69, 38912\n\t" \
    "s_nop 0\n\t" \
    "global_load_lds_dwordx4 v205, s[46:47]\n\t" \
    "s_add_u32 m0, s69, 39936\n\t" \
    "s_nop 0\n\t" \
    "global_load_lds_dwordx4 v206, s[46:47]\n\t" \
    "s_add_u32 m0, s69, 40960\n\t" \
    "s_nop 0\n\t" \
    "global_load_lds_dwordx4 v207, s[46:47]\n\t" \
    "s_add_u32 m0, s69, 41984\n\t" \
    "s_nop 0\n\t" \
    "global_load_lds_dwordx4 v208, s[46:47]\n\t" \
    "s_add_u32 m0, s69, 43008\n\t" \
    "s_nop 0\n\t" \
    "global_load_lds_dwordx4 v209, s[46:47]\n\t" \
    "s_add_u32 m0, s69, 44032\n\t" \
    "s_nop 0\n\t" \
    "global_load_lds_dwordx4 v210, s[46:47]\n\t" \
    "s_add_u32 m0, s69, 45056\n\t" \
    "s_nop 0\n\t" \
    "global_load_lds_dwordx4 v211, s[46:47]\n\t" \
    "s_waitcnt vmcnt(35) lgkmcnt(0)\n\t" \
    "s_barrier\n\t" \
    "ds_read_b64_tr_b16 a[48:49], v201 offset:32768\n\t" \
    "ds_read_b64_tr_b16 a[50:51], v201 offset:33792\n\t" \
    "ds_read_b64_tr_b16 a[52:53], v201 offset:40960\n\t" \
    "ds_read_b64_tr_b16 a[54:55], v201 offset:41984\n\t" \
    "ds_read_b64_tr_b16 a[120:121], v202 offset:32768\n\t" \
    "ds_read_b64_tr_b16 a[122:123], v202 offset:33792\n\t" \
    "ds_read_b64_tr_b16 a[124:125], v202 offset:40960\n\t" \
    "ds_read_b64_tr_b16 a[126:127], v202 offset:41984\n\t" \
    "s_waitcnt vmcnt(31) lgkmcnt(0)\n\t" \
    "s_barrier\n\t" \
    "ds_read_b64_tr_b16 a[56:57], v201 offset:49152\n\t" \
    "ds_read_b64_tr_b16 a[58:59], v201 offset:50176\n\t" \
    "ds_read_b64_tr_b16 a[60:61], v201 offset:57344\n\t" \
    "ds_read_b64_tr_b16 a[62:63], v201 offset:58368\n\t" \
    "ds_read_b64_tr_b16 a[128:129], v202 offset:49152\n\t" \
    "ds_read_b64_tr_b16 a[130:131], v202 offset:50176\n\t" \
    "ds_read_b64_tr_b16 a[132:133], v202 offset:57344\n\t" \
    "ds_read_b64_tr_b16 a[134:135], v202 offset:58368\n\t" \
    "s_add_u32 s46, s44, 73728\n\t" \
    "s_addc_u32 s47, s45, 0\n\t" \
    "s_mul_i32 s69, s82, 9\n\t" \
    "s_add_u32 m0, s69, 73728\n\t" \
    "s_nop 0\n\t" \
    "global_load_lds_dwordx4 v203, s[46:47]\n\t" \
    "s_add_u32 m0, s69, 74752\n\t" \
    "s_nop 0\n\t" \
    "global_load_lds_dwordx4 v204, s[46:47]\n\t" \
    "s_add_u32 m0, s69, 75776\n\t" \
    "s_nop 0\n\t" \
    "global_load_lds_dwordx4 v205, s[46:47]\n\t" \
    "s_add_u32 m0, s69, 76800\n\t" \
    "s_nop 0\n\t" \
    "global_load_lds_dwordx4 v206, s[46:47]\n\t" \
    "s_add_u32 m0, s69, 77824\n\t" \
    "s_nop 0\n\t" \
    "global_load_lds_dwordx4 v207, s[46:47]\n\t" \
    "s_add_u32 m0, s69, 78848\n\t" \
    "s_nop 0\n\t" \
    "global_load_lds_dwordx4 v208, s[46:47]\n\t" \
    "s_add_u32 m0, s69, 79872\n\t" \
    "s_nop 0\n\t" \
    "global_load_lds_dwordx4 v209, s[46:47]\n\t" \
    "s_add_u32 m0, s69, 80896\n\t" \
    "s_nop 0\n\t" \
    "global_load_lds_dwordx4 v210, s[46:47]\n\t" \
    "s_add_u32 m0, s69, 81920\n\t" \
    "s_nop 0\n\t" \
    "global_load_lds_dwordx4 v211, s[46:47]\n\t" \
    "v_mbcnt_lo_u32_b32 v164, -1, 0\n\t" \
    "v_mbcnt_hi_u32_b32 v164, -1, v164\n\t" \
    "v_lshlrev_b32 v164, 6, v164\n\t" \
    "v_add_u32 v164, 6225920, v164\n\t" \
    "v_mov_b32 v165, 6234112\n\t" \
    "global_load_dword v165, v165, s[36:37] sc1\n\t" \
    "global_load_dwordx2 v[166:167], v164, s[36:37] sc1\n\t" \
    "global_load_dword v168, v164, s[36:37] offset:2048 sc1\n\t" \
    "s_waitcnt vmcnt(39) lgkmcnt(0)\n\t" \
    "s_barrier\n\t" \
    "v_add_u32 v201, 0x10000, v201\n\t" \
    "v_add_u32 v202, 0x10000, v202\n\t" \
    "ds_read_b64_tr_b16 a[64:65], v201 offset:0\n\t" \
    "ds_read_b64_tr_b16 a[66:67], v201 offset:1024\n\t" \
    "ds_read_b64_tr_b16 a[68:69], v201 offset:8192\n\t" \
    "ds_read_b64_tr_b16 a[70:71], v201 offset:9216\n\t" \
    "ds_read_b64_tr_b16 a[136:137], v202 offset:0\n\t" \
    "ds_read_b64_tr_b16 a[138:139], v202 offset:1024\n\t" \
    "ds_read_b64_tr_b16 a[140:141], v202 offset:8192\n\t" \
    "ds_read_b64_tr_b16 a[142:143], v202 offset:9216\n\t" \
    "v_add_u32 v201, 0xfffe0000, v201\n\t" \
    "v_add_u32 v202, 0xfffe0000, v202\n\t" \
    "gv_jn2_%=:\n\t" \
    "s_waitcnt lgkmcnt(0)\n\t" \
    "s_barrier\n\t" \
    "s_add_u32 s46, s44, 110592\n\t" \
    "s_addc_u32 s47, s45, 0\n\t" \
    "s_mul_i32 s69, s82, 9\n\t" \
    "s_add_u32 m0, s69, 110592\n\t" \
    "s_nop 0\n\t" \
    "global_load_lds_dwordx4 v203, s[46:47]\n\t" \
    "s_add_u32 m0, s69, 111616\n\t" \
    "s_nop 0\n\t" \
    "global_load_lds_dwordx4 v204, s[46:47]\n\t" \
    "s_add_u32 m0, s69, 112640\n\t" \
    "s_nop 0\n\t" \
    "global_load_lds_dwordx4 v205, s[46:47]\n\t" \
    "s_add_u32 m0, s69, 113664\n\t" \
    "s_nop 0\n\t" \
    "global_load_lds_dwordx4 v206, s[46:47]\n\t" \
    "s_add_u32 m0, s69, 114688\n\t" \
    "s_nop 0\n\t" \
    "global_load_lds_dwordx4 v207, s[46:47]\n\t" \
    "s_add_u32 m0, s69, 115712\n\t" \
    "s_nop 0\n\t" \
    "global_load_lds_dwordx4 v208, s[46:47]\n\t" \
    "s_add_u32 m0, s69, 116736\n\t" \
    "s_nop 0\n\t" \
    "global_load_lds_dwordx4 v209, s[46:47]\n\t" \
    "s_add_u32 m0, s69, 117760\n\t" \
    "s_nop 0\n\t" \
    "global_load_lds_dwordx4 v210, s[46:47]\n\t" \
    "s_add_u32 m0, s69, 118784\n\t" \
    "s_nop 0\n\t" \
    "global_load_lds_dwordx4 v211, s[46:47]\n\t" \
    "s_waitcnt vmcnt(39) lgkmcnt(0)\n\t" \
    "s_barrier\n\t" \
    "ds_read_b64_tr_b16 a[144:145], v212 offset:0\n\t" \
    "ds_read_b64_tr_b16 a[146:147], v212 offset:18432\n\t" \
    "ds_read_b64_tr_b16 a[160:161], v213 offset:0\n\t" \
    "ds_read_b64_tr_b16 a[162:163], v213 offset:18432\n\t" \
    "ds_read_b64_tr_b16 a[176:177], v214 offset:0\n\t" \
    "ds_read_b64_tr_b16 a[178:179], v214 offset:18432\n\t" \
    "s_waitcnt lgkmcnt(6)\n\t" \
    "ds_read_b64_tr_b16 a[192:193], v215 offset:0\n\t" \
    "ds_read_b64_tr_b16 a[194:195], v215 offset:18432\n\t" \
    "ds_read_b64_tr_b16 a[208:209], v212 offset:128\n\t" \
    "ds_read_b64_tr_b16 a[210:211], v212 offset:18560\n\t" \
    "ds_read_b64_tr_b16 a[224:225], v213 offset:128\n\t" \
    "ds_read_b64_tr_b16 a[226:227], v213 offset:18560\n\t" \
    "s_waitcnt lgkmcnt(6)\n\t" \
    "ds_read_b64_tr_b16 a[240:241], v214 offset:128\n\t" \
    "ds_read_b64_tr_b16 a[242:243], v214 offset:18560\n\t" \
    "ds_read_b64_tr_b16 v[224:225], v215 offset:128\n\t" \
    "ds_read_b64_tr_b16 v[226:227], v215 offset:18560\n\t" \
    "ds_read_b64_tr_b16 v[240:241], v212 offset:256\n\t" \
    "ds_read_b64_tr_b16 v[242:243], v212 offset:18688\n\t" \
    "s_waitcnt lgkmcnt(6)\n\t" \
    "v_add_u32 v212, 0x9000, v212\n\t" \
    "v_add_u32 v213, 0x9000, v213\n\t" \
    "v_add_u32 v214, 0x9000, v214\n\t" \
    "v_add_u32 v215, 0x9000, v215\n\t" \
    "s_waitcnt vmcnt(21) lgkmcnt(0)\n\t" \
    "s_barrier\n\t" \
    "ds_read_b64_tr_b16 a[148:149], v212 offset:0\n\t" \
    "ds_read_b64_tr_b16 a[150:151], v212 offset:18432\n\t" \
    "ds_read_b64_tr_b16 a[164:165], v213 offset:0\n\t" \
    "ds_read_b64_tr_b16 a[166:167], v213 offset:18432\n\t" \
    "ds_read_b64_tr_b16 a[180:181], v214 offset:0\n\t" \
    "ds_read_b64_tr_b16 a[182:183], v214 offset:18432\n\t" \
    "s_waitcnt lgkmcnt(6)\n\t" \
    "ds_read_b64_tr_b16 a[196:197], v215 offset:0\n\t" \
    "ds_read_b64_tr_b16 a[198:199], v215 offset:18432\n\t" \
    "ds_read_b64_tr_b16 a[212:213], v212 offset:128\n\t" \
    "ds_read_b64_tr_b16 a[214:215], v212 offset:18560\n\t" \
    "ds_read_b64_tr_b16 a[228:229], v213 offset:128\n\t" \
    "ds_read_b64_tr_b16 a[230:231], v213 offset:18560\n\t" \
    "s_waitcnt lgkmcnt(6)\n\t" \
    "ds_read_b64_tr_b16 a[244:245], v214 offset:128\n\t" \
    "ds_read_b64_tr_b16 a[246:247], v214 offset:18560\n\t" \
    "ds_read_b64_tr_b16 v[228:229], v215 offset:128\n\t" \
    "ds_read_b64_tr_b16 v[230:231], v215 offset:18560\n\t" \
    "ds_read_b64_tr_b16 v[244:245], v212 offset:256\n\t" \
    "ds_read_b64_tr_b16 v[246:247], v212 offset:18688\n\t" \
    "s_waitcnt lgkmcnt(6)\n\t" \
    "v_add_u32 v212, 0x9000, v212\n\t" \
    "v_add_u32 v213, 0x9000, v213\n\t" \
    "v_add_u32 v214, 0x9000, v214\n\t" \
    "v_add_u32 v215, 0x9000, v215\n\t" \
    "ds_write_b128 v3, v[120:123] offset:0\n\t" \
    "ds_write_b128 v3, v[124:127] offset:1024\n\t" \
    "ds_write_b128 v3, v[128:131] offset:2048\n\t" \
    "ds_write_b128 v3, v[132:135] offset:3072\n\t" \
    "ds_write_b128 v3, v[136:139] offset:4096\n\t" \
    "ds_write_b128 v3, v[140:143] offset:5120\n\t" \
    "ds_write_b128 v3, v[144:147] offset:6144\n\t" \
    "ds_write_b128 v3, v[148:151] offset:7168\n\t" \
    "ds_write_b128 v3, v[152:155] offset:8192\n\t" \
    "s_waitcnt vmcnt(12) lgkmcnt(0)\n\t" \
    "s_barrier\n\t" \
    "ds_read_b64_tr_b16 a[152:153], v212 offset:0\n\t" \
    "ds_read_b64_tr_b16 a[154:155], v212 offset:18432\n\t" \
    "ds_read_b64_tr_b16 a[168:169], v213 offset:0\n\t" \
    "ds_read_b64_tr_b16 a[170:171], v213 offset:18432\n\t" \
    "ds_read_b64_tr_b16 a[184:185], v214 offset:0\n\t" \
    "ds_read_b64_tr_b16 a[186:187], v214 offset:18432\n\t" \
    "s_waitcnt lgkmcnt(6)\n\t" \
    "ds_read_b64_tr_b16 a[200:201], v215 offset:0\n\t" \
    "ds_read_b64_tr_b16 a[202:203], v215 offset:18432\n\t" \
    "ds_read_b64_tr_b16 a[216:217], v212 offset:128\n\t" \
    "ds_read_b64_tr_b16 a[218:219], v212 offset:18560\n\t" \
    "ds_read_b64_tr_b16 a[232:233], v213 offset:128\n\t" \
    "ds_read_b64_tr_b16 a[234:235], v213 offset:18560\n\t" \
    "s_waitcnt lgkmcnt(6)\n\t" \
    "ds_read_b64_tr_b16 a[248:249], v214 offset:128\n\t" \
    "ds_read_b64_tr_b16 a[250:251], v214 offset:18560\n\t" \
    "ds_read_b64_tr_b16 v[232:233], v215 offset:128\n\t" \
    "ds_read_b64_tr_b16 v[234:235], v215 offset:18560\n\t" \
    "ds_read_b64_tr_b16 v[248:249], v212 offset:256\n\t" \
    "ds_read_b64_tr_b16 v[250:251], v212 offset:18688\n\t" \
    "s_waitcnt lgkmcnt(6)\n\t" \
    "v_add_u32 v212, 0x9000, v212\n\t" \
    "v_add_u32 v213, 0x9000, v213\n\t" \
    "v_add_u32 v214, 0x9000, v214\n\t" \
    "v_add_u32 v215, 0x9000, v215\n\t" \
    "s_min_u32 s69, s84, 63\n\t" \
    "s_mul_i32 s69, s69, 36864\n\t" \
    "s_add_u32 s52, s50, s69\n\t" \
    "s_addc_u32 s53, s51, 0\n\t" \
    "s_add_u32 m0, s89, 36864\n\t" \
    "s_nop 0\n\t" \
    "global_load_lds_dwordx4 v203, s[52:53]\n\t" \
    "s_add_u32 m0, s89, 37888\n\t" \
    "s_nop 0\n\t" \
    "global_load_lds_dwordx4 v204, s[52:53]\n\t" \
    "s_add_u32 m0, s89, 38912\n\t" \
    "s_nop 0\n\t" \
    "global_load_lds_dwordx4 v205, s[52:53]\n\t" \
    "s_add_u32 m0, s89, 39936\n\t" \
    "s_nop 0\n\t" \
    "global_load_lds_dwordx4 v206, s[52:53]\n\t" \
    "s_add_u32 m0, s89, 40960\n\t" \
    "s_nop 0\n\t" \
    "global_load_lds_dwordx4 v207, s[52:53]\n\t" \
    "s_add_u32 m0, s89, 41984\n\t" \
    "s_nop 0\n\t" \
    "global_load_lds_dwordx4 v208, s[52:53]\n\t" \
    "s_add_u32 m0, s89, 43008\n\t" \
    "s_nop 0\n\t" \
    "global_load_lds_dwordx4 v209, s[52:53]\n\t" \
    "s_add_u32 m0, s89, 44032\n\t" \
    "s_nop 0\n\t" \
    "global_load_lds_dwordx4 v210, s[52:53]\n\t" \
    "s_add_u32 m0, s89, 45056\n\t" \
    "s_nop 0\n\t" \
    "global_load_lds_dwordx4 v211, s[52:53]\n\t" \
    "s_waitcnt vmcnt(18)\n\t" \
    "gv_C3_%=:\n\t" \
    "v_cmp_ne_u32 s[54:55], v166, v168\n\t" \
    "v_sub_u32 v166, v166, v165\n\t" \
    "v_cmp_ne_u32 vcc, 1, v166\n\t" \
    "s_nop 1\n\t" \
    "s_or_b64 vcc, vcc, s[54:55]\n\t" \
    "s_cbranch_scc0 gv_Cok4_%=\n\t" \
    "s_cmp_eq_u32 s68, 0\n\t" \
    "s_cbranch_scc1 gv_F6_%=\n\t" \
    "s_sub_u32 s68, s68, 1\n\t" \
    "s_sleep 6\n\t" \
    "s_branch gv_Cag5_%=\n\t" \
    "gv_F6_%=:\n\t" \
    "v_mov_b32 v168, 5767424\n\t" \
    "v_mov_b32 v169, 4\n\t" \
    "global_store_dword v168, v169, s[36:37] sc1\n\t" \
    "s_waitcnt vmcnt(0)\n\t" \
    "s_branch gv_Cok4_%=\n\t" \
    "gv_Cag5_%=:\n\t" \
    "global_load_dwordx2 v[166:167], v164, s[36:37] sc1\n\t" \
    "global_load_dword v168, v164, s[36:37] offset:2048 sc1\n\t" \
    "s_waitcnt vmcnt(0)\n\t" \
    "s_branch gv_C3_%=\n\t" \
    "gv_Cok4_%=:\n\t" \
    "s_cmp_lg_u32 s66, 0\n\t" \
    "s_cbranch_scc1 gv_Ca7_%=\n\t" \
    "v_mov_b32 v164, 5836800\n\t" \
    "s_mov_b64 exec, 1\n\t" \
    "global_atomic_add v164, v166, s[36:37] sc1\n\t" \
    "s_mov_b64 exec, -1\n\t" \
    "gv_Ca7_%=:\n\t" \
    "v_mov_b32_dpp v164, v167 quad_perm:[1,0,3,2] row_mask:0xf bank_mask:0xf\n\t" \
    "v_cmp_lt_u32 s[44:45], v164, v167\n\t" \
    "s_nop 1\n\t" \
    "s_waitcnt lgkmcnt(0)\n\t" \
    "s_barrier\n\t" \
    "ds_read_b128 v[24:27], v1 offset:0\n\t" \
    "ds_read_b128 v[28:31], v1 offset:18432\n\t" \
    "ds_read_b128 v[32:35], v2 offset:0\n\t" \
    "ds_read_b128 v[36:39], v2 offset:18432\n\t" \
    "ds_read_b128 v[40:43], v1 offset:128\n\t" \
    "ds_read_b128 v[44:47], v1 offset:18560\n\t" \
    ".p2align 6\n\t" \
    "s_nop 0\n\t" \
    "s_nop 0\n\t" \
    "gv_loop8_%=:\n\t" \
    "s_min_u32 s69, s85, 63\n\t" \
    "s_mul_i32 s69, s69, 36864\n\t" \
    "s_add_u32 s52, s50, s69\n\t" \
    "s_addc_u32 s53, s51, 0\n\t" \
    "s_add_u32 s90, s90, 36864\n\t" \
    "s_cmp_eq_u32 s90, 110592\n\t" \
    "s_cselect_b32 s90, 0, s90\n\t" \
    "s_add_u32 s91, s90, 36864\n\t" \
    "s_cmp_eq_u32 s91, 110592\n\t" \
    "s_cselect_b32 s91, 0, s91\n\t" \
    "s_add_u32 s91, s91, s89\n\t" \
    "v_add_u32 v22, s90, v0\n\t" \
    "v_xor_b32 v23, 64, v22\n\t" \
    "s_lshl_b32 s40, s65, 15\n\t" \
    "s_cselect_b32 s70, s65, 64\n\t" \
    "s_mul_i32 s69, s70, 171\n\t" \
    "s_lshr_b32 s69, s69, 9\n\t" \
    "s_mul_i32 s70, s70, 589824\n\t" \
    "s_mul_i32 s71, s69, 327680\n\t" \
    "s_add_u32 s70, s70, s71\n\t" \
    "s_lshl1_add_u32 s69, s69, 4\n\t" \
    "s_bitcmp1_b64 s[44:45], s69\n\t" \
    "s_cselect_b32 s69, 8192, 0\n\t" \
    "s_add_u32 s69, s69, s70\n\t" \
    "s_add_u32 s69, s69, 123731968\n\t" \
    "s_add_u32 s48, s36, s69\n\t" \
    "s_addc_u32 s49, s37, 0\n\t" \
    "s_waitcnt lgkmcnt(5)\n\t" \
    "v_mfma_f32_16x16x32_bf16 v[72:75], a[0:3], v[24:27], 0\n\t" \
    "ds_read_b128 v[48:51], v2 offset:128\n\t" \
    "s_waitcnt lgkmcnt(5)\n\t" \
    "v_mfma_f32_16x16x32_bf16 v[76:79], a[0:3], v[28:31], 0\n\t" \
    "ds_read_b128 v[52:55], v2 offset:18560\n\t" \
    "v_mfma_f32_16x16x32_bf16 v[80:83], a[72:75], v[24:27], 0\n\t" \
    "s_add_u32 m0, s91, 0\n\t" \
    "v_mfma_f32_16x16x32_bf16 v[84:87], a[72:75], v[28:31], 0\n\t" \
    "global_load_lds_dwordx4 v203, s[52:53]\n\t" \
    "s_waitcnt lgkmcnt(5)\n\t" \
    "v_mfma_f32_16x16x32_bf16 v[72:75], a[4:7], v[32:35], v[72:75]\n\t" \
    "ds_read_b128 v[56:59], v1 offset:256\n\t" \
    "s_waitcnt lgkmcnt(5)\n\t" \
    "v_mfma_f32_16x16x32_bf16 v[76:79], a[4:7], v[36:39], v[76:79]\n\t" \
    "ds_read_b128 v[60:63], v1 offset:18688\n\t" \
    "v_mfma_f32_16x16x32_bf16 v[80:83], a[76:79], v[32:35], v[80:83]\n\t" \
    "s_add_u32 m0, s91, 1024\n\t" \
    "v_mfma_f32_16x16x32_bf16 v[84:87], a[76:79], v[36:39], v[84:87]\n\t" \
    "global_load_lds_dwordx4 v204, s[52:53]\n\t" \
    "s_waitcnt lgkmcnt(5)\n\t" \
    "v_mfma_f32_16x16x32_bf16 v[72:75], a[8:11], v[40:43], v[72:75]\n\t" \
    "ds_read_b128 v[64:67], v2 offset:256\n\t" \
    "s_waitcnt lgkmcnt(5)\n\t" \
    "v_mfma_f32_16x16x32_bf16 v[76:79], a[8:11], v[44:47], v[76:79]\n\t" \
    "ds_read_b128 v[68:71], v2 offset:18688\n\t" \
    "v_mfma_f32_16x16x32_bf16 v[80:83], a[80:83], v[40:43], v[80:83]\n\t" \
    "s_add_u32 m0, s91, 2048\n\t" \
    "v_mfma_f32_16x16x32_bf16 v[84:87], a[80:83], v[44:47], v[84:87]\n\t" \
    "global_load_lds_dwordx4 v205, s[52:53]\n\t" \
    "s_waitcnt lgkmcnt(5)\n\t" \
    "v_mfma_f32_16x16x32_bf16 v[72:75], a[12:15], v[48:51], v[72:75]\n\t" \
    "ds_read_b128 v[24:27], v1 offset:384\n\t" \
    "s_waitcnt lgkmcnt(5)\n\t" \
    "v_mfma_f32_16x16x32_bf16 v[76:79], a[12:15], v[52:55], v[76:79]\n\t" \
    "ds_read_b128 v[28:31], v1 offset:18816\n\t" \
    "v_mfma_f32_16x16x32_bf16 v[80:83], a[84:87], v[48:51], v[80:83]\n\t" \
    "s_add_u32 m0, s91, 3072\n\t" \
    "v_mfma_f32_16x16x32_bf16 v[84:87], a[84:87], v[52:55], v[84:87]\n\t" \
    "global_load_lds_dwordx4 v206, s[52:53]\n\t" \
    "s_waitcnt lgkmcnt(5)\n\t" \
    "v_mfma_f32_16x16x32_bf16 v[72:75], a[16:19], v[56:59], v[72:75]\n\t" \
    "ds_read_b128 v[32:35], v2 offset:384\n\t" \
    "s_waitcnt lgkmcnt(5)\n\t" \
    "v_mfma_f32_16x16x32_bf16 v[76:79], a[16:19], v[60:63], v[76:79]\n\t" \
    "ds_read_b128 v[36:39], v2 offset:18816\n\t" \
    "v_mfma_f32_16x16x32_bf16 v[80:83], a[88:91], v[56:59], v[80:83]\n\t" \
    "s_add_u32 m0, s91, 4096\n\t" \
    "v_mfma_f32_16x16x32_bf16 v[84:87], a[88:91], v[60:63], v[84:87]\n\t" \
    "global_load_lds_dwordx4 v207, s[52:53]\n\t" \
    "s_waitcnt lgkmcnt(5)\n\t" \
    "v_mfma_f32_16x16x32_bf16 v[72:75], a[20:23], v[64:67], v[72:75]\n\t" \
    "ds_read_b128 v[40:43], v1 offset:512\n\t" \
    "s_waitcnt lgkmcnt(5)\n\t" \
    "v_mfma_f32_16x16x32_bf16 v[76:79], a[20:23], v[68:71], v[76:79]\n\t" \
    "ds_read_b128 v[44:47], v1 offset:18944\n\t" \
    "v_mfma_f32_16x16x32_bf16 v[80:83], a[92:95], v[64:67], v[80:83]\n\t" \
    "s_add_u32 m0, s91, 5120\n\t" \
    "v_mfma_f32_16x16x32_bf16 v[84:87], a[92:95], v[68:71], v[84:87]\n\t" \
    "global_load_lds_dwordx4 v208, s[52:53]\n\t" \
    "s_waitcnt lgkmcnt(5)\n\t" \
    "v_mfma_f32_16x16x32_bf16 v[72:75], a[24:27], v[24:27], v[72:75]\n\t" \
    "ds_read_b128 v[48:51], v2 offset:512\n\t" \
    "s_waitcnt lgkmcnt(5)\n\t" \
    "v_mfma_f32_16x16x32_bf16 v[76:79], a[24:27], v[28:31], v[76:79]\n\t" \
    "ds_read_b128 v[52:55], v2 offset:18944\n\t" \
    "v_mfma_f32_16x16x32_bf16 v[80:83], a[96:99], v[24:27], v[80:83]\n\t" \
    "s_add_u32 m0, s91, 6144\n\t" \
    "v_mfma_f32_16x16x32_bf16 v[84:87], a[96:99], v[28:31], v[84:87]\n\t" \
    "global_load_lds_dwordx4 v209, s[52:53]\n\t" \
    "s_waitcnt lgkmcnt(5)\n\t" \
    "v_mfma_f32_16x16x32_bf16 v[72:75], a[28:31], v[32:35], v[72:75]\n\t" \
    "ds_read_b128 v[56:59], v1 offset:640\n\t" \
    "s_waitcnt lgkmcnt(5)\n\t" \
    "v_mfma_f32_16x16x32_bf16 v[76:79], a[28:31], v[36:39], v[76:79]\n\t" \
    "ds_read_b128 v[60:63], v1 offset:19072\n\t" \
    "v_mfma_f32_16x16x32_bf16 v[80:83], a[100:103], v[32:35], v[80:83]\n\t" \
    "s_add_u32 m0, s91, 7168\n\t" \
    "v_mfma_f32_16x16x32_bf16 v[84:87], a[100:103], v[36:39], v[84:87]\n\t" \
    "global_load_lds_dwordx4 v210, s[52:53]\n\t" \
    "s_waitcnt lgkmcnt(5)\n\t" \
    "v_mfma_f32_16x16x32_bf16 v[72:75], a[32:35], v[40:43], v[72:75]\n\t" \
    "ds_read_b128 v[64:67], v2 offset:640\n\t" \
    "s_waitcnt lgkmcnt(5)\n\t" \
    "v_mfma_f32_16x16x32_bf16 v[76:79], a[32:35], v[44:47], v[76:79]\n\t" \
    "ds_read_b128 v[68:71], v2 offset:19072\n\t" \
    "v_mfma_f32_16x16x32_bf16 v[80:83], a[104:107], v[40:43], v[80:83]\n\t" \
    "s_add_u32 m0, s91, 8192\n\t" \
    "v_mfma_f32_16x16x32_bf16 v[84:87], a[104:107], v[44:47], v[84:87]\n\t" \
    "global_load_lds_dwordx4 v211, s[52:53]\n\t" \
    "s_waitcnt lgkmcnt(5)\n\t" \
    "v_mfma_f32_16x16x32_bf16 v[72:75], a[36:39], v[48:51], v[72:75]\n\t" \
    "ds_read_b128 v[24:27], v1 offset:768\n\t" \
    "s_waitcnt lgkmcnt(5)\n\t" \
    "v_mfma_f32_16x16x32_bf16 v[76:79], a[36:39], v[52:55], v[76:79]\n\t" \
    "ds_read_b128 v[28:31], v1 offset:19200\n\t" \
    "v_mfma_f32_16x16x32_bf16 v[80:83], a[108:111], v[48:51], v[80:83]\n\t" \
    "v_mfma_f32_16x16x32_bf16 v[84:87], a[108:111], v[52:55], v[84:87]\n\t" \
    "s_waitcnt lgkmcnt(5)\n\t" \
    "v_mfma_f32_16x16x32_bf16 v[72:75], a[40:43], v[56:59], v[72:75]\n\t" \
    "ds_read_b128 v[32:35], v2 offset:768\n\t" \
    "s_waitcnt lgkmcnt(5)\n\t" \
    "v_mfma_f32_16x16x32_bf16 v[76:79], a[40:43], v[60:63], v[76:79]\n\t" \
    "ds_read_b128 v[36:39], v2 offset:19200\n\t" \
    "v_mfma_f32_16x16x32_bf16 v[80:83], a[112:115], v[56:59], v[80:83]\n\t" \
    "v_mfma_f32_16x16x32_bf16 v[84:87], a[112:115], v[60:63], v[84:87]\n\t" \
    "global_store_dwordx4 v19, v[196:199], s[48:49] sc1\n\t" \
    "s_waitcnt lgkmcnt(5)\n\t" \
    "v_mfma_f32_16x16x32_bf16 v[72:75], a[44:47], v[64:67], v[72:75]\n\t" \
    "ds_read_b128 v[40:43], v1 offset:896\n\t" \
    "s_waitcnt lgkmcnt(5)\n\t" \
    "v_mfma_f32_16x16x32_bf16 v[76:79], a[44:47], v[68:71], v[76:79]\n\t" \
    "ds_read_b128 v[44:47], v1 offset:19328\n\t" \
    "v_mfma_f32_16x16x32_bf16 v[80:83], a[116:119], v[64:67], v[80:83]\n\t" \
    "v_mfma_f32_16x16x32_bf16 v[84:87], a[116:119], v[68:71], v[84:87]\n\t" \
    "s_waitcnt lgkmcnt(5)\n\t" \
    "v_mfma_f32_16x16x32_bf16 v[72:75], a[48:51], v[24:27], v[72:75]\n\t" \
    "ds_read_b128 v[48:51], v2 offset:896\n\t" \
    "s_waitcnt lgkmcnt(5)\n\t" \
    "v_mfma_f32_16x16x32_bf16 v[76:79], a[48:51], v[28:31], v[76:79]\n\t" \
    "ds_read_b128 v[52:55], v2 offset:19328\n\t" \
    "v_mfma_f32_16x16x32_bf16 v[80:83], a[120:123], v[24:27], v[80:83]\n\t" \
    "v_mfma_f32_16x16x32_bf16 v[84:87], a[120:123], v[28:31], v[84:87]\n\t" \
    "s_waitcnt lgkmcnt(5)\n\t" \
    "v_mfma_f32_16x16x32_bf16 v[72:75], a[52:55], v[32:35], v[72:75]\n\t" \
    "ds_read_b128 v[56:59], v1 offset:1024\n\t" \
    "s_waitcnt lgkmcnt(5)\n\t" \
    "v_mfma_f32_16x16x32_bf16 v[76:79], a[52:55], v[36:39], v[76:79]\n\t" \
    "ds_read_b128 v[60:63], v1 offset:19456\n\t" \
    "v_mfma_f32_16x16x32_bf16 v[80:83], a[124:127], v[32:35], v[80:83]\n\t" \
    "v_mfma_f32_16x16x32_bf16 v[84:87], a[124:127], v[36:39], v[84:87]\n\t" \
    "global_store_dwordx4 v20, v[196:199], s[48:49] sc1\n\t" \
    "s_waitcnt lgkmcnt(5)\n\t" \
    "v_mfma_f32_16x16x32_bf16 v[72:75], a[56:59], v[40:43], v[72:75]\n\t" \
    "ds_read_b128 v[64:67], v2 offset:1024\n\t" \
    "s_waitcnt lgkmcnt(5)\n\t" \
    "v_mfma_f32_16x16x32_bf16 v[76:79], a[56:59], v[44:47], v[76:79]\n\t" \
    "ds_read_b128 v[68:71], v2 offset:19456\n\t" \
    "v_mfma_f32_16x16x32_bf16 v[80:83], a[128:131], v[40:43], v[80:83]\n\t" \
    "v_mfma_f32_16x16x32_bf16 v[84:87], a[128:131], v[44:47], v[84:87]\n\t" \
    "s_waitcnt lgkmcnt(5)\n\t" \
    "v_mfma_f32_16x16x32_bf16 v[72:75], a[60:63], v[48:51], v[72:75]\n\t" \
    "s_waitcnt lgkmcnt(4)\n\t" \
    "v_mfma_f32_16x16x32_bf16 v[76:79], a[60:63], v[52:55], v[76:79]\n\t" \
    "v_mfma_f32_16x16x32_bf16 v[80:83], a[132:135], v[48:51], v[80:83]\n\t" \
    "v_mfma_f32_16x16x32_bf16 v[84:87], a[132:135], v[52:55], v[84:87]\n\t" \
    "s_waitcnt lgkmcnt(3)\n\t" \
    "v_mfma_f32_16x16x32_bf16 v[72:75], a[64:67], v[56:59], v[72:75]\n\t" \
    "s_waitcnt lgkmcnt(2)\n\t" \
    "v_mfma_f32_16x16x32_bf16 v[76:79], a[64:67], v[60:63], v[76:79]\n\t" \
    "v_mfma_f32_16x16x32_bf16 v[80:83], a[136:139], v[56:59], v[80:83]\n\t" \
    "v_mfma_f32_16x16x32_bf16 v[84:87], a[136:139], v[60:63], v[84:87]\n\t" \
    "global_store_dword v21, v196, s[48:49] sc1\n\t" \
    "s_waitcnt lgkmcnt(1)\n\t" \
    "v_mfma_f32_16x16x32_bf16 v[72:75], a[68:71], v[64:67], v[72:75]\n\t" \
    "s_waitcnt lgkmcnt(0)\n\t" \
    "v_mfma_f32_16x16x32_bf16 v[76:79], a[68:71], v[68:71], v[76:79]\n\t" \
    "v_mfma_f32_16x16x32_bf16 v[80:83], a[140:143], v[64:67], v[80:83]\n\t" \
    "v_mfma_f32_16x16x32_bf16 v[84:87], a[140:143], v[68:71], v[84:87]\n\t" \
    "v_mov_b32 v1, v22\n\t" \
    "v_mov_b32 v2, v23\n\t" \
    "s_nop 1\n\t" \
    "v_cvt_pk_bf16_f32 v104, v72, v73\n\t" \
    "v_cvt_pk_bf16_f32 v105, v74, v75\n\t" \
    "s_nop 0\n\t" \
    "v_cvt_pk_bf16_f32 v106, v80, v81\n\t" \
    "v_cvt_pk_bf16_f32 v107, v82, v83\n\t" \
    "ds_write_b128 v4, v[104:107] offset:0\n\t" \
    "v_cvt_pk_bf16_f32 v108, v76, v77\n\t" \
    "v_cvt_pk_bf16_f32 v109, v78, v79\n\t" \
    "v_cvt_pk_bf16_f32 v110, v84, v85\n\t" \
    "v_cvt_pk_bf16_f32 v111, v86, v87\n\t" \
    "ds_write_b128 v4, v[108:111] offset:1024\n\t" \
    "s_cmp_lt_i32 s86, 0\n\t" \
    "s_cbranch_scc1 gv_rest10_%=\n\t" \
    "gv_back11_%=:\n\t" \
    "s_waitcnt vmcnt(21) lgkmcnt(0)\n\t" \
    "s_barrier\n\t" \
    "ds_read_b128 v[164:167], v5 offset:0\n\t" \
    "ds_read_b128 v[168:171], v5 offset:1024\n\t" \
    "ds_read_b128 v[172:175], v5 offset:2048\n\t" \
    "ds_read_b128 v[176:179], v5 offset:3072\n\t" \
    "ds_read_b128 v[180:183], v5 offset:4096\n\t" \
    "ds_read_b128 v[184:187], v5 offset:5120\n\t" \
    "ds_read_b128 v[188:191], v5 offset:6144\n\t" \
    "ds_read_b128 v[192:195], v5 offset:7168\n\t" \
    "s_cmp_lt_i32 s87, 16\n\t" \
    "s_cbranch_scc1 gv_fl12_%=\n\t" \
    "s_cmp_lg_u32 s66, 0\n\t" \
    "s_cbranch_scc1 gv_SF13_%=\n\t" \
    "s_lshl_b32 s69, s67, 7\n\t" \
    "s_add_u32 s69, s69, 5783552\n\t" \
    "s_add_u32 s70, s87, 0\n\t" \
    "v_mov_b32 v156, s69\n\t" \
    "v_mov_b32 v157, s70\n\t" \
    "s_mov_b64 exec, 1\n\t" \
    "global_store_dword v156, v157, s[36:37] sc1\n\t" \
    "s_mov_b64 exec, -1\n\t" \
    "gv_SF13_%=:\n\t" \
    "gv_fl12_%=:\n\t" \
    "v_xor_b32 v4, 0x2000, v4\n\t" \
    "v_xor_b32 v5, 0x2000, v5\n\t" \
    "s_waitcnt lgkmcnt(7)\n\t" \
    "v_mfma_f32_16x16x32_bf16 v[72:75], v[164:167], a[144:147], 0\n\t" \
    "s_waitcnt lgkmcnt(6)\n\t" \
    "v_mfma_f32_16x16x32_bf16 v[76:79], v[168:171], a[144:147], 0\n\t" \
    "s_waitcnt lgkmcnt(5)\n\t" \
    "v_mfma_f32_16x16x32_bf16 v[72:75], v[172:175], a[148:151], v[72:75]\n\t" \
    "s_waitcnt lgkmcnt(4)\n\t" \
    "v_mfma_f32_16x16x32_bf16 v[76:79], v[176:179], a[148:151], v[76:79]\n\t" \
    "s_waitcnt lgkmcnt(3)\n\t" \
    "v_mfma_f32_16x16x32_bf16 v[72:75], v[180:183], a[152:155], v[72:75]\n\t" \
    "s_waitcnt lgkmcnt(2)\n\t" \
    "v_mfma_f32_16x16x32_bf16 v[76:79], v[184:187], a[152:155], v[76:79]\n\t" \
    "s_waitcnt lgkmcnt(1)\n\t" \
    "v_mfma_f32_16x16x32_bf16 v[72:75], v[188:191], a[156:159], v[72:75]\n\t" \
    "s_waitcnt lgkmcnt(0)\n\t" \
    "v_mfma_f32_16x16x32_bf16 v[76:79], v[192:195], a[156:159], v[76:79]\n\t" \
    "v_mfma_f32_16x16x32_bf16 v[80:83], v[164:167], a[160:163], 0\n\t" \
    "v_mfma_f32_16x16x32_bf16 v[84:87], v[168:171], a[160:163], 0\n\t" \
    "v_mfma_f32_16x16x32_bf16 v[80:83], v[172:175], a[164:167], v[80:83]\n\t" \
    "v_mfma_f32_16x16x32_bf16 v[84:87], v[176:179], a[164:167], v[84:87]\n\t" \
    "v_mfma_f32_16x16x32_bf16 v[80:83], v[180:183], a[168:171], v[80:83]\n\t" \
    "v_mfma_f32_16x16x32_bf16 v[84:87], v[184:187], a[168:171], v[84:87]\n\t" \
    "v_cvt_pk_bf16_f32 v104, v72, v73\n\t" \
    "v_mfma_f32_16x16x32_bf16 v[80:83], v[188:191], a[172:175], v[80:83]\n\t" \
    "v_cvt_pk_bf16_f32 v105, v74, v75\n\t" \
    "v_mfma_f32_16x16x32_bf16 v[84:87], v[192:195], a[172:175], v[84:87]\n\t" \
    "v_cvt_pk_bf16_f32 v106, v76, v77\n\t" \
    "v_mfma_f32_16x16x32_bf16 v[88:91], v[164:167], a[176:179], 0\n\t" \
    "v_cvt_pk_bf16_f32 v107, v78, v79\n\t" \
    "v_mfma_f32_16x16x32_bf16 v[92:95], v[168:171], a[176:179], 0\n\t" \
    "v_xor_b32 v18, s40, v8\n\t" \
    "v_mfma_f32_16x16x32_bf16 v[88:91], v[172:175], a[180:183], v[88:91]\n\t" \
    "global_store_dwordx4 v18, v[104:107], s[38:39] sc1\n\t" \
    "v_mfma_f32_16x16x32_bf16 v[92:95], v[176:179], a[180:183], v[92:95]\n\t" \
    "v_mfma_f32_16x16x32_bf16 v[88:91], v[180:183], a[184:187], v[88:91]\n\t" \
    "v_mfma_f32_16x16x32_bf16 v[92:95], v[184:187], a[184:187], v[92:95]\n\t" \
    "v_cvt_pk_bf16_f32 v108, v80, v81\n\t" \
    "v_mfma_f32_16x16x32_bf16 v[88:91], v[188:191], a[188:191], v[88:91]\n\t" \
    "v_cvt_pk_bf16_f32 v109, v82, v83\n\t" \
    "v_mfma_f32_16x16x32_bf16 v[92:95], v[192:195], a[188:191], v[92:95]\n\t" \
    "v_cvt_pk_bf16_f32 v110, v84, v85\n\t" \
    "v_mfma_f32_16x16x32_bf16 v[72:75], v[164:167], a[192:195], 0\n\t" \
    "v_cvt_pk_bf16_f32 v111, v86, v87\n\t" \
    "v_mfma_f32_16x16x32_bf16 v[76:79], v[168:171], a[192:195], 0\n\t" \
    "v_xor_b32 v18, s40, v9\n\t" \
    "v_mfma_f32_16x16x32_bf16 v[72:75], v[172:175], a[196:199], v[72:75]\n\t" \
    "global_store_dwordx4 v18, v[108:111], s[38:39] sc1\n\t" \
    "v_mfma_f32_16x16x32_bf16 v[76:79], v[176:179], a[196:199], v[76:79]\n\t" \
    "v_mfma_f32_16x16x32_bf16 v[72:75], v[180:183], a[200:203], v[72:75]\n\t" \
    "v_mfma_f32_16x16x32_bf16 v[76:79], v[184:187], a[200:203], v[76:79]\n\t" \
    "v_cvt_pk_bf16_f32 v104, v88, v89\n\t" \
    "v_mfma_f32_16x16x32_bf16 v[72:75], v[188:191], a[204:207], v[72:75]\n\t" \
    "v_cvt_pk_bf16_f32 v105, v90, v91\n\t" \
    "v_mfma_f32_16x16x32_bf16 v[76:79], v[192:195], a[204:207], v[76:79]\n\t" \
    "v_cvt_pk_bf16_f32 v106, v92, v93\n\t" \
    "v_mfma_f32_16x16x32_bf16 v[80:83], v[164:167], a[208:211], 0\n\t" \
    "v_cvt_pk_bf16_f32 v107, v94, v95\n\t" \
    "v_mfma_f32_16x16x32_bf16 v[84:87], v[168:171], a[208:211], 0\n\t" \
    "v_xor_b32 v18, s40, v10\n\t" \
    "v_mfma_f32_16x16x32_bf16 v[80:83], v[172:175], a[212:215], v[80:83]\n\t" \
    "global_store_dwordx4 v18, v[104:107], s[38:39] sc1\n\t" \
    "v_mfma_f32_16x16x32_bf16 v[84:87], v[176:179], a[212:215], v[84:87]\n\t" \
    "v_mfma_f32_16x16x32_bf16 v[80:83], v[180:183], a[216:219], v[80:83]\n\t" \
    "v_mfma_f32_16x16x32_bf16 v[84:87], v[184:187], a[216:219], v[84:87]\n\t" \
    "v_cvt_pk_bf16_f32 v108, v72, v73\n\t" \
    "v_mfma_f32_16x16x32_bf16 v[80:83], v[188:191], a[220:223], v[80:83]\n\t" \
    "v_cvt_pk_bf16_f32 v109, v74, v75\n\t" \
    "v_mfma_f32_16x16x32_bf16 v[84:87], v[192:195], a[220:223], v[84:87]\n\t" \
    "v_cvt_pk_bf16_f32 v110, v76, v77\n\t" \
    "v_mfma_f32_16x16x32_bf16 v[88:91], v[164:167], a[224:227], 0\n\t" \
    "v_cvt_pk_bf16_f32 v111, v78, v79\n\t" \
    "v_mfma_f32_16x16x32_bf16 v[92:95], v[168:171], a[224:227], 0\n\t" \
    "v_xor_b32 v18, s40, v11\n\t" \
    "v_mfma_f32_16x16x32_bf16 v[88:91], v[172:175], a[228:231], v[88:91]\n\t" \
    "global_store_dwordx4 v18, v[108:111], s[38:39] sc1\n\t" \
    "v_mfma_f32_16x16x32_bf16 v[92:95], v[176:179], a[228:231], v[92:95]\n\t" \
    "v_mfma_f32_16x16x32_bf16 v[88:91], v[180:183], a[232:235], v[88:91]\n\t" \
    "v_mfma_f32_16x16x32_bf16 v[92:95], v[184:187], a[232:235], v[92:95]\n\t" \
    "v_cvt_pk_bf16_f32 v104, v80, v81\n\t" \
    "v_mfma_f32_16x16x32_bf16 v[88:91], v[188:191], a[236:239], v[88:91]\n\t" \
    "v_cvt_pk_bf16_f32 v105, v82, v83\n\t" \
    "v_mfma_f32_16x16x32_bf16 v[92:95], v[192:195], a[236:239], v[92:95]\n\t" \
    "v_cvt_pk_bf16_f32 v106, v84, v85\n\t" \
    "v_mfma_f32_16x16x32_bf16 v[72:75], v[164:167], a[240:243], 0\n\t" \
    "v_cvt_pk_bf16_f32 v107, v86, v87\n\t" \
    "v_mfma_f32_16x16x32_bf16 v[76:79], v[168:171], a[240:243], 0\n\t" \
    "v_xor_b32 v18, s40, v12\n\t" \
    "v_mfma_f32_16x16x32_bf16 v[72:75], v[172:175], a[244:247], v[72:75]\n\t" \
    "global_store_dwordx4 v18, v[104:107], s[38:39] sc1\n\t" \
    "v_mfma_f32_16x16x32_bf16 v[76:79], v[176:179], a[244:247], v[76:79]\n\t" \
    "v_mfma_f32_16x16x32_bf16 v[72:75], v[180:183], a[248:251], v[72:75]\n\t" \
    "v_mfma_f32_16x16x32_bf16 v[76:79], v[184:187], a[248:251], v[76:79]\n\t" \
    "v_cvt_pk_bf16_f32 v108, v88, v89\n\t" \
    "v_mfma_f32_16x16x32_bf16 v[72:75], v[188:191], a[252:255], v[72:75]\n\t" \
    "v_cvt_pk_bf16_f32 v109, v90, v91\n\t" \
    "v_mfma_f32_16x16x32_bf16 v[76:79], v[192:195], a[252:255], v[76:79]\n\t" \
    "v_cvt_pk_bf16_f32 v110, v92, v93\n\t" \
    "v_mfma_f32_16x16x32_bf16 v[80:83], v[164:167], v[224:227], 0\n\t" \
    "v_cvt_pk_bf16_f32 v111, v94, v95\n\t" \
    "v_mfma_f32_16x16x32_bf16 v[84:87], v[168:171], v[224:227], 0\n\t" \
    "v_xor_b32 v18, s40, v13\n\t" \
    "v_mfma_f32_16x16x32_bf16 v[80:83], v[172:175], v[228:231], v[80:83]\n\t" \
    "global_store_dwordx4 v18, v[108:111], s[38:39] sc1\n\t" \
    "v_mfma_f32_16x16x32_bf16 v[84:87], v[176:179], v[228:231], v[84:87]\n\t" \
    "v_mfma_f32_16x16x32_bf16 v[80:83], v[180:183], v[232:235], v[80:83]\n\t" \
    "v_mfma_f32_16x16x32_bf16 v[84:87], v[184:187], v[232:235], v[84:87]\n\t" \
    "v_cvt_pk_bf16_f32 v104, v72, v73\n\t" \
    "v_mfma_f32_16x16x32_bf16 v[80:83], v[188:191], v[236:239], v[80:83]\n\t" \
    "v_cvt_pk_bf16_f32 v105, v74, v75\n\t" \
    "v_mfma_f32_16x16x32_bf16 v[84:87], v[192:195], v[236:239], v[84:87]\n\t" \
    "v_cvt_pk_bf16_f32 v106, v76, v77\n\t" \
    "v_mfma_f32_16x16x32_bf16 v[88:91], v[164:167], v[240:243], 0\n\t" \
    "v_cvt_pk_bf16_f32 v107, v78, v79\n\t" \
    "ds_read_b128 v[24:27], v22 offset:0\n\t" \
    "v_mfma_f32_16x16x32_bf16 v[92:95], v[168:171], v[240:243], 0\n\t" \
    "v_xor_b32 v18, s40, v14\n\t" \
    "ds_read_b128 v[28:31], v22 offset:18432\n\t" \
    "v_mfma_f32_16x16x32_bf16 v[88:91], v[172:175], v[244:247], v[88:91]\n\t" \
    "global_store_dwordx4 v18, v[104:107], s[38:39] sc1\n\t" \
    "ds_read_b128 v[32:35], v23 offset:0\n\t" \
    "v_mfma_f32_16x16x32_bf16 v[92:95], v[176:179], v[244:247], v[92:95]\n\t" \
    "ds_read_b128 v[36:39], v23 offset:18432\n\t" \
    "v_mfma_f32_16x16x32_bf16 v[88:91], v[180:183], v[248:251], v[88:91]\n\t" \
    "ds_read_b128 v[40:43], v22 offset:128\n\t" \
    "v_mfma_f32_16x16x32_bf16 v[92:95], v[184:187], v[248:251], v[92:95]\n\t" \
    "v_cvt_pk_bf16_f32 v108, v80, v81\n\t" \
    "ds_read_b128 v[44:47], v22 offset:18560\n\t" \
    "v_mfma_f32_16x16x32_bf16 v[88:91], v[188:191], v[252:255], v[88:91]\n\t" \
    "v_cvt_pk_bf16_f32 v109, v82, v83\n\t" \
    "v_mfma_f32_16x16x32_bf16 v[92:95], v[192:195], v[252:255], v[92:95]\n\t" \
    "v_cvt_pk_bf16_f32 v110, v84, v85\n\t" \
    "v_cvt_pk_bf16_f32 v111, v86, v87\n\t" \
    "v_xor_b32 v18, s40, v15\n\t" \
    "global_store_dwordx4 v18, v[108:111], s[38:39] sc1\n\t" \
    "s_nop 1\n\t" \
    "v_cvt_pk_bf16_f32 v104, v88, v89\n\t" \
    "v_cvt_pk_bf16_f32 v105, v90, v91\n\t" \
    "v_cvt_pk_bf16_f32 v106, v92, v93\n\t" \
    "v_cvt_pk_bf16_f32 v107, v94, v95\n\t" \
    "v_xor_b32 v18, s40, v16\n\t" \
    "global_store_dwordx4 v18, v[104:107], s[38:39] sc1\n\t" \
    "s_cmp_ge_u32 s65, 16\n\t" \
    "s_cbranch_scc1 gv_nofast9_%=\n\t" \
    "s_waitcnt vmcnt(0)\n\t" \
    "s_barrier\n\t" \
    "s_cmp_lg_u32 s66, 0\n\t" \
    "s_cbranch_scc1 gv_SF14_%=\n\t" \
    "s_lshl_b32 s69, s67, 7\n\t" \
    "s_add_u32 s69, s69, 5783552\n\t" \
    "s_add_u32 s70, s65, 0\n\t" \
    "v_mov_b32 v156, s69\n\t" \
    "v_mov_b32 v157, s70\n\t" \
    "s_mov_b64 exec, 1\n\t" \
    "global_store_dword v156, v157, s[36:37] sc1\n\t" \
    "s_mov_b64 exec, -1\n\t" \
    "gv_SF14_%=:\n\t" \
    "s_xor_b32 s69, s66, 1\n\t" \
    "s_lshr_b32 s70, s65, 1\n\t" \
    "s_or_b32 s69, s69, s70\n\t" \
    "s_cbranch_scc1 gv_nofast9_%=\n\t" \
    "v_mov_b32 v156, 5838848\n\t" \
    "v_mov_b32 v157, 1\n\t" \
    "s_mov_b64 exec, 1\n\t" \
    "global_atomic_add v158, v156, v157, s[36:37] sc0 sc1\n\t" \
    "s_mov_b64 exec, -1\n\t" \
    "v_mbcnt_lo_u32_b32 v156, -1, 0\n\t" \
    "v_and_b32 v157, 3, v156\n\t" \
    "v_lshlrev_b32 v157, 16, v157\n\t" \
    "v_bfe_u32 v159, v156, 2, 2\n\t" \
    "v_lshl_add_u32 v157, v159, 11, v157\n\t" \
    "v_bfe_u32 v159, v156, 4, 1\n\t" \
    "v_lshl_add_u32 v157, v159, 14, v157\n\t" \
    "s_bitcmp1_b32 s44, 2\n\t" \
    "s_cselect_b32 s63, 8192, 0\n\t" \
    "s_or_b32 s63, s63, 0x200000\n\t" \
    "v_add_u32 v157, s63, v157\n\t" \
    "s_waitcnt vmcnt(0)\n\t" \
    "v_readfirstlane_b32 s69, v158\n\t" \
    "s_nop 1\n\t" \
    "s_cmp_lg_u32 s69, 126\n\t" \
    "s_cbranch_scc1 gv_go15_%=\n\t" \
    "s_memrealtime s[56:57]\n\t" \
    "s_waitcnt lgkmcnt(0)\n\t" \
    "s_lshl_b32 s69, s56, 4\n\t" \
    "s_add_u32 s69, s69, 2800\n\t" \
    "s_add_u32 s70, s56, 24\n\t" \
    "s_lshr_b32 s70, s70, 4\n\t" \
    "v_lshrrev_b32 v159, 2, v156\n\t" \
    "v_add_u32 v159, s70, v159\n\t" \
    "v_and_b32 v159, 63, v159\n\t" \
    "v_lshlrev_b32 v159, 6, v159\n\t" \
    "v_and_b32 v158, 3, v156\n\t" \
    "v_lshl_add_u32 v159, v158, 12, v159\n\t" \
    "v_add_u32 v159, 5865472, v159\n\t" \
    "v_mov_b32 v156, s69\n\t" \
    "s_mov_b32 exec_hi, 0\n\t" \
    "global_store_dword v159, v156, s[36:37] sc1\n\t" \
    "global_store_dword v157, v156, s[36:37] sc1\n\t" \
    "s_mov_b32 exec_hi, -1\n\t" \
    "gv_go15_%=:\n\t" \
    "gv_nofast9_%=:\n\t" \
    "s_mov_b32 s87, s86\n\t" \
    "s_mov_b32 s86, s65\n\t" \
    "s_mov_b32 s65, s84\n\t" \
    "s_mov_b32 s84, s85\n\t" \
    "s_cmp_lt_u32 s84, 24\n\t" \
    "s_addc_u32 s85, s84, 1\n\t" \
    "s_cmp_lt_u32 s65, s88\n\t" \
    "s_cbranch_scc1 gv_loop8_%=\n\t" \
    "s_waitcnt vmcnt(0) lgkmcnt(0)\n\t" \
    "s_barrier\n\t" \
    "s_mov_b32 s65, 99\n\t" \
    "s_cmp_lg_u32 s66, 0\n\t" \
    "s_cbranch_scc1 gv_SF16_%=\n\t" \
    "s_lshl_b32 s69, s67, 7\n\t" \
    "s_add_u32 s69, s69, 5783552\n\t" \
    "s_add_u32 s70, s65, 0\n\t" \
    "v_mov_b32 v156, s69\n\t" \
    "v_mov_b32 v157, s70\n\t" \
    "s_mov_b64 exec, 1\n\t" \
    "global_store_dword v156, v157, s[36:37] sc1\n\t" \
    "s_mov_b64 exec, -1\n\t" \
    "gv_SF16_%=:\n\t" \
    "s_branch gv_end17_%=\n\t" \
    "gv_rest10_%=:\n\t" \
    "s_waitcnt vmcnt(12) lgkmcnt(0)\n\t" \
    "s_barrier\n\t" \
    "ds_read_b64_tr_b16 a[156:157], v212 offset:0\n\t" \
    "ds_read_b64_tr_b16 a[158:159], v212 offset:18432\n\t" \
    "ds_read_b64_tr_b16 a[172:173], v213 offset:0\n\t" \
    "ds_read_b64_tr_b16 a[174:175], v213 offset:18432\n\t" \
    "ds_read_b64_tr_b16 a[188:189], v214 offset:0\n\t" \
    "ds_read_b64_tr_b16 a[190:191], v214 offset:18432\n\t" \
    "s_waitcnt lgkmcnt(6)\n\t" \
    "ds_read_b64_tr_b16 a[204:205], v215 offset:0\n\t" \
    "ds_read_b64_tr_b16 a[206:207], v215 offset:18432\n\t" \
    "ds_read_b64_tr_b16 a[220:221], v212 offset:128\n\t" \
    "ds_read_b64_tr_b16 a[222:223], v212 offset:18560\n\t" \
    "ds_read_b64_tr_b16 a[236:237], v213 offset:128\n\t" \
    "ds_read_b64_tr_b16 a[238:239], v213 offset:18560\n\t" \
    "s_waitcnt lgkmcnt(6)\n\t" \
    "ds_read_b64_tr_b16 a[252:253], v214 offset:128\n\t" \
    "ds_read_b64_tr_b16 a[254:255], v214 offset:18560\n\t" \
    "ds_read_b64_tr_b16 v[236:237], v215 offset:128\n\t" \
    "ds_read_b64_tr_b16 v[238:239], v215 offset:18560\n\t" \
    "ds_read_b64_tr_b16 v[252:253], v212 offset:256\n\t" \
    "ds_read_b64_tr_b16 v[254:255], v212 offset:18688\n\t" \
    "s_waitcnt lgkmcnt(6)\n\t" \
    "v_add_u32 v212, 0x9000, v212\n\t" \
    "v_add_u32 v213, 0x9000, v213\n\t" \
    "v_add_u32 v214, 0x9000, v214\n\t" \
    "v_add_u32 v215, 0x9000, v215\n\t" \
    "s_branch gv_back11_%=\n\t" \
    "gv_end17_%=:\n\t" \
    "gv_last_%=:\n\t" \
    ""
#define ASM_CV \
    "v_mov_b32 v0, %0\n\t" \
    "v_mov_b32 v2, %2\n\t" \
    "v_subrev_u32 v1, 768, %1\n\t" \
    "v_mov_b32 v4, 0\n\t" \
    "v_mov_b32 v5, 0\n\t" \
    "v_mov_b32 v6, 0\n\t" \
    "v_mov_b32 v7, 0\n\t" \
    "v_mov_b32 v8, 0\n\t" \
    "v_mov_b32 v9, 0\n\t" \
    "v_mov_b32 v10, 0\n\t" \
    "v_mov_b32 v11, 0\n\t" \
    "v_mov_b32 v12, 0\n\t" \
    "v_mov_b32 v13, 0\n\t" \
    "v_mov_b32 v14, 0\n\t" \
    "v_mov_b32 v15, 0\n\t" \
    "v_mov_b32 v16, 0\n\t" \
    "v_mov_b32 v17, 0\n\t" \
    "v_mov_b32 v18, 0\n\t" \
    "v_mov_b32 v19, 0\n\t" \
    "v_mov_b32 v20, 0\n\t" \
    "v_mov_b32 v21, 0\n\t" \
    "v_mov_b32 v22, 0\n\t" \
    "v_mov_b32 v23, 0\n\t" \
    "v_mov_b32 v24, 0\n\t" \
    "v_mov_b32 v25, 0\n\t" \
    "v_mov_b32 v26, 0\n\t" \
    "v_mov_b32 v27, 0\n\t" \
    "v_mov_b32 v28, 0\n\t" \
    "v_mov_b32 v29, 0\n\t" \
    "v_mov_b32 v30, 0\n\t" \
    "v_mov_b32 v31, 0\n\t" \
    "v_mov_b32 v32, 0\n\t" \
    "v_mov_b32 v33, 0\n\t" \
    "v_mov_b32 v34, 0\n\t" \
    "v_mov_b32 v35, 0\n\t" \
    "s_waitcnt lgkmcnt(0)\n\t" \
    "ds_read_b64 v[36:37], v0 offset:0\n\t" \
    "ds_read_b64 v[38:39], v0 offset:8\n\t" \
    "ds_read_b64 v[40:41], v0 offset:32\n\t" \
    "ds_read_b64 v[42:43], v0 offset:40\n\t" \
    "ds_read_b128 v[44:47], v1 offset:768\n\t" \
    "ds_read_b128 v[48:51], v1 offset:2304\n\t" \
    "ds_read_b128 v[52:55], v1 offset:3840\n\t" \
    "ds_read_b128 v[56:59], v1 offset:5376\n\t" \
    "ds_read_b128 v[60:63], v1 offset:8448\n\t" \
    "ds_read_b128 v[64:67], v1 offset:9984\n\t" \
    "ds_read_b128 v[68:71], v1 offset:11520\n\t" \
    "ds_read_b128 v[72:75], v1 offset:13056\n\t" \
    "s_mov_b32 s69, 8\n\t" \
    "cv_c1_%=:\n\t" \
    "s_waitcnt lgkmcnt(7)\n\t" \
    "v_mfma_f32_16x16x32_bf16 v[4:7], v[36:39], v[44:47], v[4:7]\n\t" \
    "ds_read_b64 v[76:77], v0 offset:64\n\t" \
    "ds_read_b64 v[78:79], v0 offset:72\n\t" \
    "v_mfma_f32_16x16x32_bf16 v[20:23], v[40:43], v[44:47], v[20:23]\n\t" \
    "ds_read_b64 v[80:81], v0 offset:96\n\t" \
    "ds_read_b64 v[82:83], v0 offset:104\n\t" \
    "s_waitcnt lgkmcnt(10)\n\t" \
    "v_mfma_f32_16x16x32_bf16 v[8:11], v[36:39], v[48:51], v[8:11]\n\t" \
    "ds_read_b128 v[84:87], v1 offset:672\n\t" \
    "ds_read_b128 v[88:91], v1 offset:2208\n\t" \
    "v_mfma_f32_16x16x32_bf16 v[24:27], v[40:43], v[48:51], v[24:27]\n\t" \
    "ds_read_b128 v[92:95], v1 offset:3744\n\t" \
    "ds_read_b128 v[96:99], v1 offset:5280\n\t" \
    "s_waitcnt lgkmcnt(13)\n\t" \
    "v_mfma_f32_16x16x32_bf16 v[12:15], v[36:39], v[52:55], v[12:15]\n\t" \
    "ds_read_b128 v[100:103], v1 offset:8352\n\t" \
    "ds_read_b128 v[104:107], v1 offset:9888\n\t" \
    "v_mfma_f32_16x16x32_bf16 v[28:31], v[40:43], v[52:55], v[28:31]\n\t" \
    "s_waitcnt lgkmcnt(14)\n\t" \
    "v_mfma_f32_16x16x32_bf16 v[16:19], v[36:39], v[56:59], v[16:19]\n\t" \
    "ds_read_b128 v[108:111], v1 offset:11424\n\t" \
    "v_mfma_f32_16x16x32_bf16 v[32:35], v[40:43], v[56:59], v[32:35]\n\t" \
    "s_waitcnt lgkmcnt(14)\n\t" \
    "v_mfma_f32_16x16x32_bf16 v[4:7], v[36:39], v[60:63], v[4:7]\n\t" \
    "ds_read_b128 v[112:115], v1 offset:12960\n\t" \
    "v_mfma_f32_16x16x32_bf16 v[20:23], v[40:43], v[60:63], v[20:23]\n\t" \
    "s_waitcnt lgkmcnt(14)\n\t" \
    "v_mfma_f32_16x16x32_bf16 v[8:11], v[36:39], v[64:67], v[8:11]\n\t" \
    "v_mfma_f32_16x16x32_bf16 v[24:27], v[40:43], v[64:67], v[24:27]\n\t" \
    "s_waitcnt lgkmcnt(13)\n\t" \
    "v_mfma_f32_16x16x32_bf16 v[12:15], v[36:39], v[68:71], v[12:15]\n\t" \
    "v_mfma_f32_16x16x32_bf16 v[28:31], v[40:43], v[68:71], v[28:31]\n\t" \
    "s_waitcnt lgkmcnt(12)\n\t" \
    "v_mfma_f32_16x16x32_bf16 v[16:19], v[36:39], v[72:75], v[16:19]\n\t" \
    "v_mfma_f32_16x16x32_bf16 v[32:35], v[40:43], v[72:75], v[32:35]\n\t" \
    "s_waitcnt lgkmcnt(7)\n\t" \
    "v_mfma_f32_16x16x32_bf16 v[4:7], v[76:79], v[84:87], v[4:7]\n\t" \
    "ds_read_b64 v[36:37], v0 offset:128\n\t" \
    "ds_read_b64 v[38:39], v0 offset:136\n\t" \
    "v_mfma_f32_16x16x32_bf16 v[20:23], v[80:83], v[84:87], v[20:23]\n\t" \
    "ds_read_b64 v[40:41], v0 offset:160\n\t" \
    "ds_read_b64 v[42:43], v0 offset:168\n\t" \
    "s_waitcnt lgkmcnt(10)\n\t" \
    "v_mfma_f32_16x16x32_bf16 v[8:11], v[76:79], v[88:91], v[8:11]\n\t" \
    "ds_read_b128 v[44:47], v1 offset:576\n\t" \
    "ds_read_b128 v[48:51], v1 offset:2112\n\t" \
    "v_mfma_f32_16x16x32_bf16 v[24:27], v[80:83], v[88:91], v[24:27]\n\t" \
    "ds_read_b128 v[52:55], v1 offset:3648\n\t" \
    "ds_read_b128 v[56:59], v1 offset:5184\n\t" \
    "s_waitcnt lgkmcnt(13)\n\t" \
    "v_mfma_f32_16x16x32_bf16 v[12:15], v[76:79], v[92:95], v[12:15]\n\t" \
    "ds_read_b128 v[60:63], v1 offset:8256\n\t" \
    "ds_read_b128 v[64:67], v1 offset:9792\n\t" \
    "v_mfma_f32_16x16x32_bf16 v[28:31], v[80:83], v[92:95], v[28:31]\n\t" \
    "s_waitcnt lgkmcnt(14)\n\t" \
    "v_mfma_f32_16x16x32_bf16 v[16:19], v[76:79], v[96:99], v[16:19]\n\t" \
    "ds_read_b128 v[68:71], v1 offset:11328\n\t" \
    "v_mfma_f32_16x16x32_bf16 v[32:35], v[80:83], v[96:99], v[32:35]\n\t" \
    "s_waitcnt lgkmcnt(14)\n\t" \
    "v_mfma_f32_16x16x32_bf16 v[4:7], v[76:79], v[100:103], v[4:7]\n\t" \
    "ds_read_b128 v[72:75], v1 offset:12864\n\t" \
    "v_mfma_f32_16x16x32_bf16 v[20:23], v[80:83], v[100:103], v[20:23]\n\t" \
    "s_waitcnt lgkmcnt(14)\n\t" \
    "v_mfma_f32_16x16x32_bf16 v[8:11], v[76:79], v[104:107], v[8:11]\n\t" \
    "v_mfma_f32_16x16x32_bf16 v[24:27], v[80:83], v[104:107], v[24:27]\n\t" \
    "s_waitcnt lgkmcnt(13)\n\t" \
    "v_mfma_f32_16x16x32_bf16 v[12:15], v[76:79], v[108:111], v[12:15]\n\t" \
    "v_mfma_f32_16x16x32_bf16 v[28:31], v[80:83], v[108:111], v[28:31]\n\t" \
    "s_waitcnt lgkmcnt(12)\n\t" \
    "v_mfma_f32_16x16x32_bf16 v[16:19], v[76:79], v[112:115], v[16:19]\n\t" \
    "v_mfma_f32_16x16x32_bf16 v[32:35], v[80:83], v[112:115], v[32:35]\n\t" \
    "v_add_u32 v0, 128, v0\n\t" \
    "v_subrev_u32 v1, 192, v1\n\t" \
    "s_sub_u32 s69, s69, 1\n\t" \
    "s_cmp_lg_u32 s69, 0\n\t" \
    "s_cbranch_scc1 cv_c1_%=\n\t" \
    "s_waitcnt lgkmcnt(0)\n\t" \
    "s_mov_b32 s69, 8\n\t" \
    "cv_c2_%=:\n\t" \
    "s_waitcnt lgkmcnt(5)\n\t" \
    "v_mfma_f32_16x16x32_bf16 v[8:11], v[36:39], v[48:51], v[8:11]\n\t" \
    "ds_read_b64 v[76:77], v0 offset:64\n\t" \
    "ds_read_b64 v[78:79], v0 offset:72\n\t" \
    "v_mfma_f32_16x16x32_bf16 v[24:27], v[40:43], v[48:51], v[24:27]\n\t" \
    "ds_read_b64 v[80:81], v0 offset:96\n\t" \
    "ds_read_b64 v[82:83], v0 offset:104\n\t" \
    "s_waitcnt lgkmcnt(8)\n\t" \
    "v_mfma_f32_16x16x32_bf16 v[12:15], v[36:39], v[52:55], v[12:15]\n\t" \
    "ds_read_b128 v[88:91], v1 offset:2208\n\t" \
    "ds_read_b128 v[92:95], v1 offset:3744\n\t" \
    "v_mfma_f32_16x16x32_bf16 v[28:31], v[40:43], v[52:55], v[28:31]\n\t" \
    "ds_read_b128 v[96:99], v1 offset:5280\n\t" \
    "ds_read_b128 v[104:107], v1 offset:9888\n\t" \
    "s_waitcnt lgkmcnt(11)\n\t" \
    "v_mfma_f32_16x16x32_bf16 v[16:19], v[36:39], v[56:59], v[16:19]\n\t" \
    "ds_read_b128 v[108:111], v1 offset:11424\n\t" \
    "ds_read_b128 v[112:115], v1 offset:12960\n\t" \
    "v_mfma_f32_16x16x32_bf16 v[32:35], v[40:43], v[56:59], v[32:35]\n\t" \
    "s_waitcnt lgkmcnt(12)\n\t" \
    "v_mfma_f32_16x16x32_bf16 v[8:11], v[36:39], v[64:67], v[8:11]\n\t" \
    "v_mfma_f32_16x16x32_bf16 v[24:27], v[40:43], v[64:67], v[24:27]\n\t" \
    "s_waitcnt lgkmcnt(11)\n\t" \
    "v_mfma_f32_16x16x32_bf16 v[12:15], v[36:39], v[68:71], v[12:15]\n\t" \
    "v_mfma_f32_16x16x32_bf16 v[28:31], v[40:43], v[68:71], v[28:31]\n\t" \
    "s_waitcnt lgkmcnt(10)\n\t" \
    "v_mfma_f32_16x16x32_bf16 v[16:19], v[36:39], v[72:75], v[16:19]\n\t" \
    "v_mfma_f32_16x16x32_bf16 v[32:35], v[40:43], v[72:75], v[32:35]\n\t" \
    "s_waitcnt lgkmcnt(5)\n\t" \
    "v_mfma_f32_16x16x32_bf16 v[8:11], v[76:79], v[88:91], v[8:11]\n\t" \
    "ds_read_b64 v[36:37], v0 offset:128\n\t" \
    "ds_read_b64 v[38:39], v0 offset:136\n\t" \
    "v_mfma_f32_16x16x32_bf16 v[24:27], v[80:83], v[88:91], v[24:27]\n\t" \
    "ds_read_b64 v[40:41], v0 offset:160\n\t" \
    "ds_read_b64 v[42:43], v0 offset:168\n\t" \
    "s_waitcnt lgkmcnt(8)\n\t" \
    "v_mfma_f32_16x16x32_bf16 v[12:15], v[76:79], v[92:95], v[12:15]\n\t" \
    "ds_read_b128 v[48:51], v1 offset:2112\n\t" \
    "ds_read_b128 v[52:55], v1 offset:3648\n\t" \
    "v_mfma_f32_16x16x32_bf16 v[28:31], v[80:83], v[92:95], v[28:31]\n\t" \
    "ds_read_b128 v[56:59], v1 offset:5184\n\t" \
    "ds_read_b128 v[64:67], v1 offset:9792\n\t" \
    "s_waitcnt lgkmcnt(11)\n\t" \
    "v_mfma_f32_16x16x32_bf16 v[16:19], v[76:79], v[96:99], v[16:19]\n\t" \
    "ds_read_b128 v[68:71], v1 offset:11328\n\t" \
    "ds_read_b128 v[72:75], v1 offset:12864\n\t" \
    "v_mfma_f32_16x16x32_bf16 v[32:35], v[80:83], v[96:99], v[32:35]\n\t" \
    "s_waitcnt lgkmcnt(12)\n\t" \
    "v_mfma_f32_16x16x32_bf16 v[8:11], v[76:79], v[104:107], v[8:11]\n\t" \
    "v_mfma_f32_16x16x32_bf16 v[24:27], v[80:83], v[104:107], v[24:27]\n\t" \
    "s_waitcnt lgkmcnt(11)\n\t" \
    "v_mfma_f32_16x16x32_bf16 v[12:15], v[76:79], v[108:111], v[12:15]\n\t" \
    "v_mfma_f32_16x16x32_bf16 v[28:31], v[80:83], v[108:111], v[28:31]\n\t" \
    "s_waitcnt lgkmcnt(10)\n\t" \
    "v_mfma_f32_16x16x32_bf16 v[16:19], v[76:79], v[112:115], v[16:19]\n\t" \
    "v_mfma_f32_16x16x32_bf16 v[32:35], v[80:83], v[112:115], v[32:35]\n\t" \
    "v_add_u32 v0, 128, v0\n\t" \
    "v_subrev_u32 v1, 192, v1\n\t" \
    "s_sub_u32 s69, s69, 1\n\t" \
    "s_cmp_lg_u32 s69, 0\n\t" \
    "s_cbranch_scc1 cv_c2_%=\n\t" \
    "s_waitcnt lgkmcnt(0)\n\t" \
    "s_mov_b32 s69, 8\n\t" \
    "cv_c3_%=:\n\t" \
    "s_waitcnt lgkmcnt(3)\n\t" \
    "v_mfma_f32_16x16x32_bf16 v[12:15], v[36:39], v[52:55], v[12:15]\n\t" \
    "ds_read_b64 v[76:77], v0 offset:64\n\t" \
    "ds_read_b64 v[78:79], v0 offset:72\n\t" \
    "v_mfma_f32_16x16x32_bf16 v[28:31], v[40:43], v[52:55], v[28:31]\n\t" \
    "ds_read_b64 v[80:81], v0 offset:96\n\t" \
    "ds_read_b64 v[82:83], v0 offset:104\n\t" \
    "s_waitcnt lgkmcnt(6)\n\t" \
    "v_mfma_f32_16x16x32_bf16 v[16:19], v[36:39], v[56:59], v[16:19]\n\t" \
    "ds_read_b128 v[92:95], v1 offset:3744\n\t" \
    "ds_read_b128 v[96:99], v1 offset:5280\n\t" \
    "v_mfma_f32_16x16x32_bf16 v[32:35], v[40:43], v[56:59], v[32:35]\n\t" \
    "ds_read_b128 v[108:111], v1 offset:11424\n\t" \
    "ds_read_b128 v[112:115], v1 offset:12960\n\t" \
    "s_waitcnt lgkmcnt(9)\n\t" \
    "v_mfma_f32_16x16x32_bf16 v[12:15], v[36:39], v[68:71], v[12:15]\n\t" \
    "v_mfma_f32_16x16x32_bf16 v[28:31], v[40:43], v[68:71], v[28:31]\n\t" \
    "s_waitcnt lgkmcnt(8)\n\t" \
    "v_mfma_f32_16x16x32_bf16 v[16:19], v[36:39], v[72:75], v[16:19]\n\t" \
    "v_mfma_f32_16x16x32_bf16 v[32:35], v[40:43], v[72:75], v[32:35]\n\t" \
    "s_waitcnt lgkmcnt(3)\n\t" \
    "v_mfma_f32_16x16x32_bf16 v[12:15], v[76:79], v[92:95], v[12:15]\n\t" \
    "ds_read_b64 v[36:37], v0 offset:128\n\t" \
    "ds_read_b64 v[38:39], v0 offset:136\n\t" \
    "v_mfma_f32_16x16x32_bf16 v[28:31], v[80:83], v[92:95], v[28:31]\n\t" \
    "ds_read_b64 v[40:41], v0 offset:160\n\t" \
    "ds_read_b64 v[42:43], v0 offset:168\n\t" \
    "s_waitcnt lgkmcnt(6)\n\t" \
    "v_mfma_f32_16x16x32_bf16 v[16:19], v[76:79], v[96:99], v[16:19]\n\t" \
    "ds_read_b128 v[52:55], v1 offset:3648\n\t" \
    "ds_read_b128 v[56:59], v1 offset:5184\n\t" \
    "v_mfma_f32_16x16x32_bf16 v[32:35], v[80:83], v[96:99], v[32:35]\n\t" \
    "ds_read_b128 v[68:71], v1 offset:11328\n\t" \
    "ds_read_b128 v[72:75], v1 offset:12864\n\t" \
    "s_waitcnt lgkmcnt(9)\n\t" \
    "v_mfma_f32_16x16x32_bf16 v[12:15], v[76:79], v[108:111], v[12:15]\n\t" \
    "v_mfma_f32_16x16x32_bf16 v[28:31], v[80:83], v[108:111], v[28:31]\n\t" \
    "s_waitcnt lgkmcnt(8)\n\t" \
    "v_mfma_f32_16x16x32_bf16 v[16:19], v[76:79], v[112:115], v[16:19]\n\t" \
    "v_mfma_f32_16x16x32_bf16 v[32:35], v[80:83], v[112:115], v[32:35]\n\t" \
    "v_add_u32 v0, 128, v0\n\t" \
    "v_subrev_u32 v1, 192, v1\n\t" \
    "s_sub_u32 s69, s69, 1\n\t" \
    "s_cmp_lg_u32 s69, 0\n\t" \
    "s_cbranch_scc1 cv_c3_%=\n\t" \
    "s_waitcnt lgkmcnt(0)\n\t" \
    "ds_read_b64 v[36:37], v0 offset:0\n\t" \
    "ds_read_b64 v[38:39], v0 offset:8\n\t" \
    "ds_read_b64 v[40:41], v0 offset:32\n\t" \
    "ds_read_b64 v[42:43], v0 offset:40\n\t" \
    "ds_read_b128 v[56:59], v1 offset:5376\n\t" \
    "ds_read_b128 v[72:75], v1 offset:13056\n\t" \
    "ds_read_b64 v[76:77], v0 offset:64\n\t" \
    "ds_read_b64 v[78:79], v0 offset:72\n\t" \
    "ds_read_b64 v[80:81], v0 offset:96\n\t" \
    "ds_read_b64 v[82:83], v0 offset:104\n\t" \
    "ds_read_b128 v[96:99], v1 offset:5280\n\t" \
    "ds_read_b128 v[112:115], v1 offset:12960\n\t" \
    "s_mov_b32 s69, 4\n\t" \
    "cv_c4_%=:\n\t" \
    "s_waitcnt lgkmcnt(7)\n\t" \
    "v_mfma_f32_16x16x32_bf16 v[16:19], v[36:39], v[56:59], v[16:19]\n\t" \
    "ds_read_b64 v[116:117], v0 offset:128\n\t" \
    "ds_read_b64 v[118:119], v0 offset:136\n\t" \
    "v_mfma_f32_16x16x32_bf16 v[32:35], v[40:43], v[56:59], v[32:35]\n\t" \
    "ds_read_b64 v[120:121], v0 offset:160\n\t" \
    "ds_read_b64 v[122:123], v0 offset:168\n\t" \
    "s_waitcnt lgkmcnt(10)\n\t" \
    "v_mfma_f32_16x16x32_bf16 v[16:19], v[36:39], v[72:75], v[16:19]\n\t" \
    "ds_read_b128 v[136:139], v1 offset:5184\n\t" \
    "ds_read_b128 v[152:155], v1 offset:12864\n\t" \
    "v_mfma_f32_16x16x32_bf16 v[32:35], v[40:43], v[72:75], v[32:35]\n\t" \
    "ds_read_b64 v[156:157], v0 offset:192\n\t" \
    "ds_read_b64 v[158:159], v0 offset:200\n\t" \
    "s_waitcnt lgkmcnt(9)\n\t" \
    "v_mfma_f32_16x16x32_bf16 v[16:19], v[76:79], v[96:99], v[16:19]\n\t" \
    "ds_read_b64 v[160:161], v0 offset:224\n\t" \
    "ds_read_b64 v[162:163], v0 offset:232\n\t" \
    "v_mfma_f32_16x16x32_bf16 v[32:35], v[80:83], v[96:99], v[32:35]\n\t" \
    "ds_read_b128 v[176:179], v1 offset:5088\n\t" \
    "ds_read_b128 v[192:195], v1 offset:12768\n\t" \
    "s_waitcnt lgkmcnt(12)\n\t" \
    "v_mfma_f32_16x16x32_bf16 v[16:19], v[76:79], v[112:115], v[16:19]\n\t" \
    "v_mfma_f32_16x16x32_bf16 v[32:35], v[80:83], v[112:115], v[32:35]\n\t" \
    "s_waitcnt lgkmcnt(7)\n\t" \
    "v_mfma_f32_16x16x32_bf16 v[16:19], v[116:119], v[136:139], v[16:19]\n\t" \
    "ds_read_b64 v[36:37], v0 offset:256\n\t" \
    "ds_read_b64 v[38:39], v0 offset:264\n\t" \
    "v_mfma_f32_16x16x32_bf16 v[32:35], v[120:123], v[136:139], v[32:35]\n\t" \
    "ds_read_b64 v[40:41], v0 offset:288\n\t" \
    "ds_read_b64 v[42:43], v0 offset:296\n\t" \
    "s_waitcnt lgkmcnt(10)\n\t" \
    "v_mfma_f32_16x16x32_bf16 v[16:19], v[116:119], v[152:155], v[16:19]\n\t" \
    "ds_read_b128 v[56:59], v1 offset:4992\n\t" \
    "ds_read_b128 v[72:75], v1 offset:12672\n\t" \
    "v_mfma_f32_16x16x32_bf16 v[32:35], v[120:123], v[152:155], v[32:35]\n\t" \
    "ds_read_b64 v[76:77], v0 offset:320\n\t" \
    "ds_read_b64 v[78:79], v0 offset:328\n\t" \
    "s_waitcnt lgkmcnt(9)\n\t" \
    "v_mfma_f32_16x16x32_bf16 v[16:19], v[156:159], v[176:179], v[16:19]\n\t" \
    "ds_read_b64 v[80:81], v0 offset:352\n\t" \
    "ds_read_b64 v[82:83], v0 offset:360\n\t" \
    "v_mfma_f32_16x16x32_bf16 v[32:35], v[160:163], v[176:179], v[32:35]\n\t" \
    "ds_read_b128 v[96:99], v1 offset:4896\n\t" \
    "ds_read_b128 v[112:115], v1 offset:12576\n\t" \
    "s_waitcnt lgkmcnt(12)\n\t" \
    "v_mfma_f32_16x16x32_bf16 v[16:19], v[156:159], v[192:195], v[16:19]\n\t" \
    "v_mfma_f32_16x16x32_bf16 v[32:35], v[160:163], v[192:195], v[32:35]\n\t" \
    "v_add_u32 v0, 256, v0\n\t" \
    "v_subrev_u32 v1, 384, v1\n\t" \
    "s_sub_u32 s69, s69, 1\n\t" \
    "s_cmp_lg_u32 s69, 0\n\t" \
    "s_cbranch_scc1 cv_c4_%=\n\t" \
    "s_waitcnt lgkmcnt(0)\n\t" \
    "s_nop 7\n\t" \
    "ds_write_b128 v2, v[4:7] offset:0\n\t" \
    "ds_write_b128 v2, v[8:11] offset:1024\n\t" \
    "ds_write_b128 v2, v[12:15] offset:2048\n\t" \
    "ds_write_b128 v2, v[16:19] offset:3072\n\t" \
    "ds_write_b128 v2, v[20:23] offset:4096\n\t" \
    "ds_write_b128 v2, v[24:27] offset:5120\n\t" \
    "ds_write_b128 v2, v[28:31] offset:6144\n\t" \
    "ds_write_b128 v2, v[32:35] offset:7168\n\t" \
    "s_waitcnt lgkmcnt(0)\n\t" \
    "cv_last_%=:\n\t" \
    ""
#define ASM_EA \
    "s_bitcmp1_b32 %1, 2\n\t" \
    "s_cselect_b32 s40, 0x30, 0x28\n\t" \
    "s_cmp_lt_u32 %1, 64\n\t" \
    "s_cselect_b32 s40, s40, 0x38\n\t" \
    "s_load_dwordx2 s[42:43], %0, s40\n\t" \
    "s_load_dword s48, %0, 0x40\n\t" \
    "s_getpc_b64 s[44:45]\n\t" \
    "ea_pc_%=:\n\t" \
    "s_bfe_u32 s40, %1, 0x20003\n\t" \
    "s_lshl_b32 s40, s40, 15\n\t" \
    "v_lshl_add_u32 v1, %2, 7, s40\n\t" \
    "v_min_u32 v1, code_end_marker-ea_pc_%=, v1\n\t" \
    "s_mov_b32 m0, 155648\n\t" \
    "v_readfirstlane_b32 s41, %2\n\t" \
    "global_load_lds_dword v1, s[44:45]\n\t" \
    "s_bitcmp1_b32 %1, 2\n\t" \
    "s_cselect_b32 s40, 24, 16\n\t" \
    "s_lshr_b32 s47, %1, 3\n\t" \
    "s_cmp_ge_u32 s47, s40\n\t" \
    "s_cbranch_scc1 ea_done1_%=\n\t" \
    "s_mov_b32 s46, pad_qk-ea_pc_%=\n\t" \
    "s_mov_b32 s40, pad_vv-ea_pc_%=\n\t" \
    "s_bitcmp1_b32 %1, 2\n\t" \
    "s_cselect_b32 s46, pad_gx-ea_pc_%=, s46\n\t" \
    "s_cselect_b32 s40, pad_gv-ea_pc_%=, s40\n\t" \
    "s_cmp_lt_u32 %1, 64\n\t" \
    "s_cselect_b32 s40, s46, s40\n\t" \
    "s_add_u32 s44, s44, s40\n\t" \
    "s_addc_u32 s45, s45, 0\n\t" \
    "s_swappc_b64 s[46:47], s[44:45]\n\t" \
    "s_lshl_b32 m0, s41, 4\n\t" \
    "s_and_b32 s40, %1, 3\n\t" \
    "s_lshl_b32 s40, s40, 11\n\t" \
    "s_bfe_u32 s46, %1, 0x30003\n\t" \
    "s_lshl_b32 s46, s46, 8\n\t" \
    "s_add_u32 s40, s40, s46\n\t" \
    "s_and_b32 s41, s41, 0x80\n\t" \
    "s_xor_b32 s40, s40, s41\n\t" \
    "v_lshrrev_b32 v1, 3, %2\n\t" \
    "v_and_b32 v1, 6, v1\n\t" \
    "v_xor_b32 v1, v1, %2\n\t" \
    "v_and_b32 v1, 15, v1\n\t" \
    "v_lshlrev_b32 v1, 4, v1\n\t" \
    "v_xor_b32 v1, s40, v1\n\t" \
    "v_lshrrev_b32 v2, 4, %2\n\t" \
    "v_lshl_add_u32 v1, v2, 13, v1\n\t" \
    "s_mov_b32 s40, 16\n\t" \
    "s_waitcnt lgkmcnt(0)\n\t" \
    "ea_lp2_%=:\n\t" \
    "global_load_lds_dwordx4 v1, s[42:43]\n\t" \
    "v_add_u32 v1, 0x20000, v1\n\t" \
    "s_add_u32 m0, m0, 4096\n\t" \
    "s_sub_u32 s40, s40, 1\n\t" \
    "s_cmp_lg_u32 s40, 0\n\t" \
    "s_cbranch_scc1 ea_lp2_%=\n\t" \
    "ea_done1_%=:\n\t" \
    "s_waitcnt lgkmcnt(0)\n\t" \
    "ea_last_%=:\n\t" \
    ""
#define ASM_CLOB "v0", "v1", "v2", "v3", "v4", "v5", "v6", "v7", "v8", "v9", "v10", "v11", "v12", "v13", "v14", "v15", "v16", "v17", "v18", "v19", "v20", "v21", "v22", "v23", "v24", "v25", "v26", "v27", "v28", "v29", "v30", "v31", "v32", "v33", "v34", "v35", "v36", "v37", "v38", "v39", "v40", "v41", "v42", "v43", "v44", "v45", "v46", "v47", "v48", "v49", "v50", "v51", "v52", "v53", "v54", "v55", "v56", "v57", "v58", "v59", "v60", "v61", "v62", "v63", "v64", "v65", "v66", "v67", "v68", "v69", "v70", "v71", "v72", "v73", "v74", "v75", "v76", "v77", "v78", "v79", "v80", "v81", "v82", "v83", "v84", "v85", "v86", "v87", "v88", "v89", "v90", "v91", "v92", "v93", "v94", "v95", "v96", "v97", "v98", "v99", "v100", "v101", "v102", "v103", "v104", "v105", "v106", "v107", "v108", "v109", "v110", "v111", "v112", "v113", "v114", "v115", "v116", "v117", "v118", "v119", "v120", "v121", "v122", "v123", "v124", "v125", "v126", "v127", "v128", "v129", "v130", "v131", "v132", "v133", "v134", "v135", "v136", "v137", "v138", "v139", "v140", "v141", "v142", "v143", "v144", "v145", "v146", "v147", "v148", "v149", "v150", "v151", "v152", "v153", "v154", "v155", "v156", "v157", "v158", "v159", "v160", "v161", "v162", "v163", "v164", "v165", "v166", "v167", "v168", "v169", "v170", "v171", "v172", "v173", "v174", "v175", "v176", "v177", "v178", "v179", "v180", "v181", "v182", "v183", "v184", "v185", "v186", "v187", "v188", "v189", "v190", "v191", "v192", "v193", "v194", "v195", "v196", "v197", "v198", "v199", "v200", "v201", "v202", "v203", "v204", "v205", "v206", "v207", "v208", "v209", "v210", "v211", "v212", "v213", "v214", "v215", "v222", "v223", "v224", "v225", "v226", "v227", "v228", "v229", "v230", "v231", "v232", "v233", "v234", "v235", "v236", "v237", "v238", "v239", "v240", "v241", "v242", "v243", "v244", "v245", "v246", "v247", "v248", "v249", "v250", "v251", "v252", "v253", "v254", "v255", "a0", "a1", "a2", "a3", "a4", "a5", "a6", "a7", "a8", "a9", "a10", "a11", "a12", "a13", "a14", "a15", "a16", "a17", "a18", "a19", "a20", "a21", "a22", "a23", "a24", "a25", "a26", "a27", "a28", "a29", "a30", "a31", "a32", "a33", "a34", "a35", "a36", "a37", "a38", "a39", "a40", "a41", "a42", "a43", "a44", "a45", "a46", "a47", "a48", "a49", "a50", "a51", "a52", "a53", "a54", "a55", "a56", "a57", "a58", "a59", "a60", "a61", "a62", "a63", "a64", "a65", "a66", "a67", "a68", "a69", "a70", "a71", "a72", "a73", "a74", "a75", "a76", "a77", "a78", "a79", "a80", "a81", "a82", "a83", "a84", "a85", "a86", "a87", "a88", "a89", "a90", "a91", "a92", "a93", "a94", "a95", "a96", "a97", "a98", "a99", "a100", "a101", "a102", "a103", "a104", "a105", "a106", "a107", "a108", "a109", "a110", "a111", "a112", "a113", "a114", "a115", "a116", "a117", "a118", "a119", "a120", "a121", "a122", "a123", "a124", "a125", "a126", "a127", "a128", "a129", "a130", "a131", "a132", "a133", "a134", "a135", "a136", "a137", "a138", "a139", "a140", "a141", "a142", "a143", "a144", "a145", "a146", "a147", "a148", "a149", "a150", "a151", "a152", "a153", "a154", "a155", "a156", "a157", "a158", "a159", "a160", "a161", "a162", "a163", "a164", "a165", "a166", "a167", "a168", "a169", "a170", "a171", "a172", "a173", "a174", "a175", "a176", "a177", "a178", "a179", "a180", "a181", "a182", "a183", "a184", "a185", "a186", "a187", "a188", "a189", "a190", "a191", "a192", "a193", "a194", "a195", "a196", "a197", "a198", "a199", "a200", "a201", "a202", "a203", "a204", "a205", "a206", "a207", "a208", "a209", "a210", "a211", "a212", "a213", "a214", "a215", "a216", "a217", "a218", "a219", "a220", "a221", "a222", "a223", "a224", "a225", "a226", "a227", "a228", "a229", "a230", "a231", "a232", "a233", "a234", "a235", "a236", "a237", "a238", "a239", "a240", "a241", "a242", "a243", "a244", "a245", "a246", "a247", "a248", "a249", "a250", "a251", "a252", "a253", "a254", "a255", "s33", "s34", "s35", "s36", "s37", "s38", "s39", "s40", "s41", "s42", "s43", "s44", "s45", "s46", "s47", "s48", "s49", "s50", "s51", "s52", "s53", "s54", "s55", "s56", "s57", "s58", "s59", "s60", "s61", "s62", "s63", "s64", "s65", "s66", "s67", "s68", "s69", "s70", "s71", "s72", "s73", "s74", "s75", "s76", "s77", "s78", "s79", "s80", "s81", "s82", "s83", "s84", "s85", "s86", "s87", "s88", "s89", "s90", "s91", "s92", "s93", "s94", "s95", "s96", "s97", "s98", "s99", "vcc", "scc", "memory"
#define ASM_CLOBW "v32", "v33", "v34", "v35", "v36", "v37", "v38", "v39", "v40", "v41", "v42", "v43", "v44", "v45", "v46", "v47", "v48", "v49", "v50", "v51", "v52", "v53", "v54", "v55", "v56", "v57", "v58", "v59", "v60", "v61", "v62", "v63", "v64", "v65", "v66", "v67", "v68", "v69", "v70", "v71", "v72", "v73", "v74", "v75", "v76", "v77", "v78", "v79", "v80", "v81", "v82", "v83", "v84", "v85", "v86", "v87", "v88", "v89", "v90", "v91", "v92", "v93", "v94", "v95", "v96", "v97", "v98", "v99", "v100", "v101", "v102", "v103", "v104", "v105", "v106", "v107", "v108", "v109", "v110", "v111", "v112", "v113", "v114", "v115", "v116", "v117", "v118", "v119", "v120", "v121", "v122", "v123", "v124", "v125", "v126", "v127", "v128", "v129", "v130", "v131", "v132", "v133", "v134", "v135", "v136", "v137", "v138", "v139", "v140", "v141", "v142", "v143", "v144", "v145", "v146", "v147", "v148", "v149", "v150", "v151", "v152", "v153", "v154", "v155", "v156", "v157", "v158", "v159", "v160", "v161", "v162", "v163", "v164", "v165", "v166", "v167", "v168", "v169", "v170", "v171", "v172", "v173", "v174", "v175", "v176", "v177", "v178", "v179", "v180", "v181", "v182", "v183", "v184", "v185", "v186", "v187", "v188", "v189", "v190", "v191", "v192", "v193", "v194", "v195", "v196", "v197", "v198", "v199", "v222", "v223", "v224", "v225", "v226", "v227", "v228", "v229", "v230", "v231", "v232", "v233", "v234", "v235", "v236", "v237", "v238", "v239", "v240", "v241", "v242", "v243", "v244", "v245", "v246", "v247", "v248", "v249", "v250", "v251", "v252", "v253", "v254", "v255", "a0", "a1", "a2", "a3", "a4", "a5", "a6", "a7", "a8", "a9", "a10", "a11", "a12", "a13", "a14", "a15", "a16", "a17", "a18", "a19", "a20", "a21", "a22", "a23", "a24", "a25", "a26", "a27", "a28", "a29", "a30", "a31", "a32", "a33", "a34", "a35", "a36", "a37", "a38", "a39", "a40", "a41", "a42", "a43", "a44", "a45", "a46", "a47", "a48", "a49", "a50", "a51", "a52", "a53", "a54", "a55", "a56", "a57", "a58", "a59", "a60", "a61", "a62", "a63", "a64", "a65", "a66", "a67", "a68", "a69", "a70", "a71", "a72", "a73", "a74", "a75", "a76", "a77", "a78", "a79", "a80", "a81", "a82", "a83", "a84", "a85", "a86", "a87", "a88", "a89", "a90", "a91", "a92", "a93", "a94", "a95", "a96", "a97", "a98", "a99", "a100", "a101", "a102", "a103", "a104", "a105", "a106", "a107", "a108", "a109", "a110", "a111", "a112", "a113", "a114", "a115", "a116", "a117", "a118", "a119", "a120", "a121", "a122", "a123", "a124", "a125", "a126", "a127", "a128", "a129", "a130", "a131", "a132", "a133", "a134", "a135", "a136", "a137", "a138", "a139", "a140", "a141", "a142", "a143", "a144", "a145", "a146", "a147", "a148", "a149", "a150", "a151", "a152", "a153", "a154", "a155", "a156", "a157", "a158", "a159", "a160", "a161", "a162", "a163", "a164", "a165", "a166", "a167", "a168", "a169", "a170", "a171", "a172", "a173", "a174", "a175", "a176", "a177", "a178", "a179", "a180", "a181", "a182", "a183", "a184", "a185", "a186", "a187", "a188", "a189", "a190", "a191", "a192", "a193", "a194", "a195", "a196", "a197", "a198", "a199", "a200", "a201", "a202", "a203", "a204", "a205", "a206", "a207", "a208", "a209", "a210", "a211", "a212", "a213", "a214", "a215", "a216", "a217", "a218", "a219", "a220", "a221", "a222", "a223", "a224", "a225", "a226", "a227", "a228", "a229", "a230", "a231", "a232", "a233", "a234", "a235", "a236", "a237", "a238", "a239", "a240", "a241", "a242", "a243", "a244", "a245", "a246", "a247", "a248", "a249", "a250", "a251", "a252", "a253", "a254", "a255", "s33", "s34", "s35", "s36", "s37", "s38", "s39", "s40", "s41", "s42", "s43", "s44", "s45", "s46", "s47", "s48", "s49", "s50", "s51", "s52", "s53", "s54", "s55", "s56", "s57", "s58", "s59", "s60", "s61", "s62", "s63", "s64", "s65", "s66", "s67", "s68", "s69", "s70", "s71", "s72", "s73", "s74", "s75", "s76", "s77", "s78", "s79", "s80", "s81", "s82", "s83", "s84", "s85", "s86", "s87", "s88", "s89", "s90", "s91", "s92", "s93", "s94", "s95", "s96", "s97", "s98", "s99", "vcc", "scc", "memory"
#define CV_CLOB "v0", "v1", "v2", "v3", "v4", "v5", "v6", "v7", "v8", "v9", "v10", "v11", "v12", "v13", "v14", "v15", "v16", "v17", "v18", "v19", "v20", "v21", "v22", "v23", "v24", "v25", "v26", "v27", "v28", "v29", "v30", "v31", "v32", "v33", "v34", "v35", "v36", "v37", "v38", "v39", "v40", "v41", "v42", "v43", "v44", "v45", "v46", "v47", "v48", "v49", "v50", "v51", "v52", "v53", "v54", "v55", "v56", "v57", "v58", "v59", "v60", "v61", "v62", "v63", "v64", "v65", "v66", "v67", "v68", "v69", "v70", "v71", "v72", "v73", "v74", "v75", "v76", "v77", "v78", "v79", "v80", "v81", "v82", "v83", "v84", "v85", "v86", "v87", "v88", "v89", "v90", "v91", "v92", "v93", "v94", "v95", "v96", "v97", "v98", "v99", "v100", "v101", "v102", "v103", "v104", "v105", "v106", "v107", "v108", "v109", "v110", "v111", "v112", "v113", "v114", "v115", "v116", "v117", "v118", "v119", "v120", "v121", "v122", "v123", "v124", "v125", "v126", "v127", "v128", "v129", "v130", "v131", "v132", "v133", "v134", "v135", "v136", "v137", "v138", "v139", "v140", "v141", "v142", "v143", "v144", "v145", "v146", "v147", "v148", "v149", "v150", "v151", "v152", "v153", "v154", "v155", "v156", "v157", "v158", "v159", "v160", "v161", "v162", "v163", "v164", "v165", "v166", "v167", "v168", "v169", "v170", "v171", "v172", "v173", "v174", "v175", "v176", "v177", "v178", "v179", "v180", "v181", "v182", "v183", "v184", "v185", "v186", "v187", "v188", "v189", "v190", "v191", "v192", "v193", "v194", "v195", "s69", "scc", "memory"
static constexpr unsigned G_STEP = 0x10000;
static constexpr unsigned G_WS_CTL = 0x580000;
static constexpr unsigned G_WS_DONE = 0x5f0000;
static constexpr unsigned G_WS_EP = 0x5f2000;
static constexpr unsigned G_WS_STAMPS = 0xc000000;
static constexpr unsigned G_C_CAL = 0x0;
static constexpr unsigned G_C_ERR = 0x100;
static constexpr unsigned G_C_TASK = 0x200;
static constexpr unsigned G_C_GX = 0x1000;
static constexpr unsigned G_C_GV = 0x4000;
static constexpr unsigned G_C_KM = 0x8000;
static constexpr unsigned G_C_UM = 0x9000;
static constexpr unsigned G_C_PC = 0xc000;
static constexpr unsigned G_C_UT = 0xd000;
static constexpr unsigned G_C_YC = 0x10000;
static constexpr unsigned G_C_ARR = 0x11000;
static constexpr unsigned G_C_KG = 0x12000;
static constexpr unsigned G_C_GQ = 0x14000;
static constexpr unsigned G_C_RDY = 0x15000;
static constexpr unsigned G_C_GT = 0x18000;
static constexpr unsigned G_C_GO = 0x11800;
static constexpr unsigned G_C_RW = 0xf000;
static constexpr unsigned G_WS_H = 0x0;
static constexpr unsigned G_HS8 = 0x80000;
static constexpr unsigned G_HS4 = 0x40000;
static constexpr unsigned G_UXS = 0x200000;
static constexpr unsigned G_UMS = 0x90000;
static constexpr unsigned G_PROBE = 0x1bd400;
static constexpr unsigned G_D_QX = 0x600000;
static constexpr unsigned G_D_KX = 0x1600000;
static constexpr unsigned G_D_KM = 0x2600000;
static constexpr unsigned G_D_UX = 0x2e00000;
static constexpr unsigned G_D_UM = 0x7600000;
static constexpr unsigned G_D_PX = 0xa200000;
static constexpr unsigned G_D_PM = 0xa600000;
static constexpr unsigned G_D_KT = 0xa700000;
static constexpr unsigned G_D_UT = 0xac00000;
static constexpr unsigned G_D_YB = 0xaf00000;
static constexpr unsigned G_D_END = 0xb200000;
static constexpr unsigned G_SKM = 0x7;
static constexpr unsigned G_S1 = 0x18;
static constexpr unsigned G_S0 = 0x6;
static constexpr unsigned G_GVN = 0x2;
static constexpr unsigned G_STG = 0x9000;
static constexpr unsigned G_KROW = 0x30;
#define FLG(x_) (kDefault[5])
#define FLH(x_) (kDefault[7])
static constexpr int kDefault[8] = {PRM_DEFAULT};
#define MFMA32(a_, b_, c_) __builtin_amdgcn_mfma_f32_16x16x32_bf16(__builtin_bit_cast(bf16x8, a_), __builtin_bit_cast(bf16x8, b_), c_, 0, 0, 0)
#define MFMA16(a_, b_, c_) __builtin_amdgcn_mfma_f32_16x16x16bf16_1k(__builtin_bit_cast(s16x4, a_), __builtin_bit_cast(s16x4, b_), c_, 0, 0, 0)
#define WLD8(d_, voff_, base_, off_) asm volatile("s_nop 4\n\tglobal_load_dwordx2 %0, %1, %2 offset:%3 sc1" : "=v"(d_) : "v"(voff_), "s"(base_), "i"(off_) : "memory")
#define WLD16(d_, voff_, base_, off_) asm volatile("s_nop 4\n\tglobal_load_dwordx4 %0, %1, %2 offset:%3 sc1" : "=v"(d_) : "v"(voff_), "s"(base_), "i"(off_) : "memory")
#define WST16(voff_, d_, base_, off_) asm volatile("s_nop 4\n\tglobal_store_dwordx4 %0, %1, %2 offset:%3 sc1\n\ts_nop 1" :: "v"(voff_), "v"(d_), "s"(base_), "i"(off_) : "memory")
#define WLD16X4(d0, d1, d2, d3, voff_, base_) asm volatile("s_nop 4\n\tglobal_load_dwordx4 %0, %4, %5 sc1\n\tglobal_load_dwordx4 %1, %4, %5 offset:1024 sc1\n\t" \
    "global_load_dwordx4 %2, %4, %5 offset:2048 sc1\n\tglobal_load_dwordx4 %3, %4, %5 offset:3072 sc1" \
    : "=&v"(d0), "=&v"(d1), "=&v"(d2), "=&v"(d3) : "v"(voff_), "s"(base_) : "memory")

__device__ __forceinline__ u32x4 pack8(const f32x4& a, const f32x4& b) {
    return u32x4{pk2bf(a[0], a[1]), pk2bf(a[2], a[3]), pk2bf(b[0], b[1]), pk2bf(b[2], b[3])};
}
template <class T> __device__ __forceinline__ T* uni(T* q) {
    const u64 v = (u64)q;
    unsigned lo = __builtin_amdgcn_readfirstlane((unsigned)v), hi = __builtin_amdgcn_readfirstlane((unsigned)(v >> 32));
    asm volatile("s_nop 4" : "+s"(lo), "+s"(hi));
    return (T*)(((u64)hi << 32) | lo);
}
template <bool FAST = false>
__device__ __forceinline__ void wait_word(const MainP& p, const char* f, int want, unsigned code) {
    unsigned tries = 0;
    while (__any((int)__hip_atomic_load((const gu32*)f, RLX_AGENT) < want)) {
        if (++tries > kWaitCap) { __hip_atomic_store((gu32*)(p.ws + G_WS_CTL + G_C_ERR), code, RLX_AGENT); break; }
        if ((tries & 1023u) == 0u && __hip_atomic_load((const gu32*)(p.ws + G_WS_CTL + G_C_ERR), RLX_AGENT) != 0xffffffffu) break;
        if (FAST) __builtin_amdgcn_s_sleep(1); else __builtin_amdgcn_s_sleep(10);
    }
}

__device__ __forceinline__ int skew(int s, int id) { return (s ^ (G_SKM * id)) & 63; }
__device__ __forceinline__ unsigned um_base(int s, unsigned nm) { return G_D_UM + (s / 3) * 0x200000 + (s % 3) * G_UMS + ((nm >> (2 + s / 3)) & 1) * 8192; }
typedef GAS unsigned long long gu64;
#define IST(k_, slot_)
__device__ __forceinline__ void init_done(char* ws, int slot, unsigned val) {
    const unsigned e = __hip_atomic_load((const gu32*)(ws + G_WS_EP), RLX_AGENT);
    __hip_atomic_store((gu64*)(ws + G_WS_DONE + slot * 64), ((u64)val << 32) | (e + 1u), RLX_AGENT);
}
__device__ __forceinline__ void init_wait(char* ws, int w, int lane) {
    if (w == 0) {
        const unsigned e = __hip_atomic_load((const gu32*)(ws + G_WS_EP), RLX_AGENT);
        for (unsigned tries = 0; tries < kWaitCap; ++tries) {
            const unsigned d0 = __hip_atomic_load((const gu32*)(ws + G_WS_DONE + lane * 64), RLX_AGENT), d1 = __hip_atomic_load((const gu32*)(ws + G_WS_DONE + 2048 + lane * 64), RLX_AGENT);
            if (!__any((d0 != e + 1u) | (d1 != e + 1u))) break;
            __builtin_amdgcn_s_sleep(8);
        }
    }
    __syncthreads();
    if (w == 0 && lane == 0) __hip_atomic_fetch_add((gu32*)(ws + G_WS_CTL + G_C_ARR), 1u, RLX_AGENT);
}
__device__ __forceinline__ void fill_block(char* ws, int b, int tid, const u32x4& v0, const u32x4& v1) {
    const unsigned o = b * 8192 + tid * 16, o2 = o + 4096;
    asm volatile("global_store_dwordx4 %0, %1, %2 sc1\n\ts_nop 1" :: "v"(o), "v"(v0), "s"(ws) : "memory");
    if (b >= 512 || (b & 6) != 6 || tid < 64)
        asm volatile("global_store_dwordx4 %0, %1, %2 sc1\n\ts_nop 1" :: "v"(o2), "v"(v1), "s"(ws) : "memory");
}
__device__ __forceinline__ void init_fill(char* ws, const u16* mem0, int f, int tid) {
    const u32x4 sent = {0xffffffffu, 0xffffffffu, 0xffffffffu, 0xffffffffu};
    u32x4 v0 = sent, v1 = sent;
    if (f < 32 && (f & 4)) {
#pragma unroll
        for (int k = 0; k < 2; ++k) {
            const int q = tid + 256 * k + ((f & 2) ? 256 : 0), m = q & 15;
            if (!(f & 2) || (q >= 512 && q < 576)) {
                const unsigned* src = (const unsigned*)(mem0 + (2 * (m >> 2) + (m & 1)) * DM + 16 * (q >> 4) + (m & 2));
                const u32x4 o = {min(src[0], 0xfffffffeu), min(src[2], 0xfffffffeu), min(src[4], 0xfffffffeu), min(src[6], 0xfffffffeu)};
                if (k) v1 = o; else v0 = o;
            }
        }
    }
#pragma unroll 1
    for (int q = f; q < 182; q += 48) {
        if (q >= 136 && q < 152 && (tid & 127)) { v0 = u32x4{0u, 0u, 0u, 0u}; v1 = v0; }
        fill_block(ws, q < 64 ? q : q < 136 ? q - 64 + G_D_UM / 8192 : q < 168 ? 256 + ((q - 136) >> 2) * 8 + (q & 3) : q - 168 + G_WS_CTL / 8192, tid, v0, v1);
        v0 = sent; v1 = sent;
    }
    const unsigned e = __hip_atomic_load((const gu32*)(ws + G_WS_EP), RLX_AGENT);
    asm volatile("s_waitcnt vmcnt(0)" :: "v"(e) : "memory");
    __syncthreads();
    if (tid == 0) __hip_atomic_store((gu64*)(ws + G_WS_DONE + (48 + f) * 64), (u64)(e + 1u), RLX_AGENT);
    if (tid == 0) IST(0, 48 + f);
}
__device__ __forceinline__ void init_cal(char* ws, int id, int tid, int n) {
    if (tid < 64) {
        const int pg = id >> 1;
        char* a = ws + (pg < 2 ? pg * 0x200000u : G_D_UM + (pg - 2) * 0x200000u) + G_PROBE + (id & 1) * 8192;
        const u32x4 z = {0u, 0u, 0u, 0u};
        asm volatile("global_store_dwordx4 %0, %1, off sc1\n\tglobal_store_dwordx4 %0, %1, off offset:1024 sc1\n\ts_waitcnt vmcnt(0)" :: "v"(a + tid * 16), "v"(z) : "memory");
        if (tid == 0) {
            unsigned best = 0xffffu;
            const unsigned ts1 = (unsigned)__builtin_amdgcn_s_memrealtime() << 4;
#pragma unroll 1
            for (int it = 0; it < n; ++it) {
                const unsigned c0 = (unsigned)__builtin_amdgcn_s_memtime();
                const unsigned v = __hip_atomic_load((const gu32*)(a + it * 128), RLX_AGENT);
                asm volatile("s_waitcnt vmcnt(0)" :: "v"(v) : "memory");
                best = min(best, (unsigned)__builtin_amdgcn_s_memtime() - c0);
            }
            init_done(ws, id, best);
            IST(0, id);
        }
    }
    __syncthreads();
}
__device__ __forceinline__ unsigned near_mask(char* ws, int lane) {
    const gu32* q = (const gu32*)(ws + G_WS_DONE + (lane & 31) * 128 + 4);
    return (unsigned)__ballot(__hip_atomic_load(q + 16, RLX_AGENT) < __hip_atomic_load(q, RLX_AGENT));
}

#define TST(slot_, i_)
#define PIN8(a) asm volatile("" : "+v"(a[0]), "+v"(a[1]), "+v"(a[2]), "+v"(a[3]), "+v"(a[4]), "+v"(a[5]), "+v"(a[6]), "+v"(a[7]))
struct AOps { u32x4 q[4], k[8]; int fl; };
struct APtr { const char* q; const char* k; const char* m; unsigned voff, vkm, voffm, vfl; };
__device__ __forceinline__ void attn_batch(const MainP& p, AOps& o, const APtr& a) {
    const char* q2 = a.q + 4096; const char* k2 = a.k + 4096;
    WLD16(o.q[0], a.voff, a.q, 0); WLD16(o.q[1], a.voff, a.q, 2048); WLD16(o.q[2], a.voff, q2, 0); WLD16(o.q[3], a.voff, q2, 2048);
    WLD16X4(o.k[0], o.k[1], o.k[2], o.k[3], a.voff, a.k); WLD16X4(o.k[4], o.k[5], o.k[6], o.k[7], a.voff, k2);
    asm volatile("global_load_dword %0, %1, %2 sc1" : "=v"(o.fl) : "v"(a.vfl), "s"(p.ws) : "memory");
}
__device__ __forceinline__ void attn_pin(AOps& o) {
    asm volatile("" : "+v"(o.fl), "+v"(o.q[0]), "+v"(o.q[1]), "+v"(o.q[2]), "+v"(o.q[3])); PIN8(o.k);
}
static constexpr int NFINE = 6;
static constexpr int NG1 = 64 - NFINE;
__device__ __forceinline__ void score_round4(float* r, const f32x4& x) {
    asm volatile("v_cvt_pk_bf16_f32 %0, 0, %4\n\tv_cvt_pk_bf16_f32 %1, 0, %5\n\tv_cvt_pk_bf16_f32 %2, 0, %6\n\tv_cvt_pk_bf16_f32 %3, 0, %7\n\t"
                 "v_mul_f32 %0, 0x3db504f3, %0\n\tv_mul_f32 %1, 0x3db504f3, %1\n\tv_mul_f32 %2, 0x3db504f3, %2\n\tv_mul_f32 %3, 0x3db504f3, %3\n\t"
                 "v_cvt_pk_bf16_f32 %0, 0, %0\n\tv_cvt_pk_bf16_f32 %1, 0, %1\n\tv_cvt_pk_bf16_f32 %2, 0, %2\n\tv_cvt_pk_bf16_f32 %3, 0, %3"
                 : "=&v"(r[0]), "=&v"(r[1]), "=&v"(r[2]), "=&v"(r[3]) : "v"(x[0]), "v"(x[1]), "v"(x[2]), "v"(x[3]));
}
__device__ __forceinline__ float vmax3(float a, float b, float c) { float r; asm("v_max3_f32 %0, %1, %2, %3" : "=v"(r) : "v"(a), "v"(b), "v"(c)); return r; }
template <int N>
__device__ __forceinline__ void attn_soft(float (&q)[12], u32x2& Pm, u32x4& Px) {
    float m = vmax3(q[0], q[1], q[2]);
#pragma unroll
    for (int i = 3; i < N; i += 2) m = vmax3(m, q[i], q[i + 1 < N ? i + 1 : i]);
    { auto a = __builtin_amdgcn_permlane16_swap(__float_as_uint(m), __float_as_uint(m), false, false); m = vmax3(m, __uint_as_float(a[0]), __uint_as_float(a[1])); }
    { auto a = __builtin_amdgcn_permlane32_swap(__float_as_uint(m), __float_as_uint(m), false, false); m = vmax3(m, __uint_as_float(a[0]), __uint_as_float(a[1])); }
    const float ml = -m * 1.4426950408889634f;
#pragma unroll
    for (int i = 0; i < N; ++i) q[i] = __builtin_amdgcn_exp2f(fmaf(q[i], 1.4426950408889634f, ml));
    float sum = 0.5f * ((q[0] + q[1]) + (q[2] + q[3])) + ((q[4] + q[5]) + (q[6] + q[7]));
    if (N > 8) sum += (q[8] + q[9]) + (q[10] + q[11]);
    sum = rowsum4(sum);
    const float inv = __builtin_amdgcn_rcpf(sum);
    Pm = u32x2{pk2bf(q[0] * inv, q[1] * inv), pk2bf(q[2] * inv, q[3] * inv)};
    Px = u32x4{pk2bf(q[4] * inv, q[5] * inv), pk2bf(q[6] * inv, q[7] * inv), N > 8 ? pk2bf(q[8] * inv, q[9] * inv) : 0u, N > 8 ? pk2bf(q[10] * inv, q[11] * inv) : 0u};
}
#define NASPLIT 12
#define HSL 1
#define NAG 2
static constexpr int NA = NASPLIT;
__device__ __forceinline__ int attn_next(int s) { return s < NA ? s + 2 : s + NAG; }
__device__ __forceinline__ unsigned attn_flag(int s, int hq) { return G_WS_CTL + G_C_PC + (s < NA ? s & 1 : (s - NA) % NAG) * 1024 + hq * 128; }
struct BOps { u32x4 q[8], k[8]; };
#define WLD16X4A(d0, d1, d2, d3, voff_, base_) asm volatile("s_nop 4\n\tglobal_load_dwordx4 %0, %4, %5 sc1\n\tglobal_load_dwordx4 %1, %4, %5 offset:1024 sc1\n\t" \
    "global_load_dwordx4 %2, %4, %5 offset:2048 sc1\n\tglobal_load_dwordx4 %3, %4, %5 offset:3072 sc1" \
    : "=&a"(d0), "=&a"(d1), "=&a"(d2), "=&a"(d3) : "v"(voff_), "s"(base_) : "memory")
#define PIN8A(a) asm volatile("" : "+a"(a[0]), "+a"(a[1]), "+a"(a[2]), "+a"(a[3]), "+a"(a[4]), "+a"(a[5]), "+a"(a[6]), "+a"(a[7]))
#define MFMA_AV0(acc, a_, b_) asm volatile("v_mfma_f32_16x16x32_bf16 %0, %1, %2, 0" : "=&v"(acc) : "a"(a_), "v"(b_))
#define MFMA_AV(acc, a_, b_) asm volatile("v_mfma_f32_16x16x32_bf16 %0, %1, %2, %0" : "+v"(acc) : "a"(a_), "v"(b_))
__device__ __forceinline__ void attn_batch8(const MainP& p, BOps& o, const APtr& a) {
    const char* q2 = a.q + 4096; const char* k2 = a.k + 4096;
    WLD16X4(o.q[0], o.q[1], o.q[2], o.q[3], a.voff, a.q); WLD16X4(o.q[4], o.q[5], o.q[6], o.q[7], a.voff, q2);
    WLD16X4A(o.k[0], o.k[1], o.k[2], o.k[3], a.voff, a.k); WLD16X4A(o.k[4], o.k[5], o.k[6], o.k[7], a.voff, k2);
}
__device__ __forceinline__ void attn_step(const MainP& p, BOps& o, BOps& n1, BOps& n2, APtr& a, int& kg, int& kk, int s, int h, int w, int lane) {
    const int g = lane >> 4, c = lane & 15;
    const float scale = 0.08838834764831845f;
    TST(s * 64 + h * 2, 0);
    const int s2 = min(attn_next(attn_next(s)), NSEG - 1);
    if (kg < s2 || kk < s) {
        VMC0();
        if (s < 50) wait_word(p, p.ws + a.vfl, (lane & 1) ? s : s2, 13u); else wait_word<true>(p, p.ws + a.vfl, (lane & 1) ? s : s2, 13u);
    }
    TST(s * 64 + h * 2, 1);
    a.q = p.ws + G_D_QX + (size_t)h * G_HS8 + skew(s2, h) * 8192; a.k = a.q + (G_D_KX - G_D_QX); a.m = p.ws + G_D_KM + (size_t)h * G_HS4 + skew(s, h) * 4096;
    __builtin_amdgcn_sched_barrier(0);
    u32x4 km[4];
    int fl;
    WLD16X4A(km[0], km[1], km[2], km[3], a.vkm, a.m);
    asm volatile("global_load_dword %0, %1, %2 sc1" : "=v"(fl) : "v"(a.vfl), "s"(p.ws) : "memory");
    attn_batch8(p, n2, a);
    f32x4 y[5];
#pragma unroll
    for (int wc = 0; wc < 4; ++wc)
#pragma unroll
        for (int i = 0; i < 3; ++i) { if (wc == 0) MFMA_AV0(y[i], o.k[i >> 1], o.q[(i + 1) >> 1]); else MFMA_AV(y[i], o.k[wc * 2 + (i >> 1)], o.q[wc * 2 + ((i + 1) >> 1)]); }
    asm volatile("s_nop 7" : "+v"(y[0]), "+v"(y[1]), "+v"(y[2]));
    float q0[12], q1[12];
    score_round4(q0 + 4, y[0]); score_round4(q1 + 4, y[1]); score_round4(q1 + 8, y[2]);
#pragma unroll
    for (int i = 0; i < 4; ++i) {
        const bool vis = 4 * g + i <= c;
        q0[4 + i] = vis ? q0[4 + i] : -INFINITY; q1[8 + i] = vis ? q1[8 + i] : -INFINITY;
    }
    __builtin_amdgcn_sched_barrier(0);
    asm volatile("s_waitcnt vmcnt(16)\n\ts_barrier" : "+a"(km[0]), "+a"(km[1]), "+a"(km[2]), "+a"(km[3]), "+v"(fl) :: "memory");
    PIN8(n1.q); PIN8A(n1.k);
    { const unsigned fo = attn_flag(s, h >> 2); const int fv = s - 1;
      asm volatile("s_cmp_lg_u32 %3, 0\n\ts_cbranch_scc1 1f\n\tglobal_store_dword %0, %1, %2 sc1\n1:" :: "v"(fo), "v"(fv), "s"(p.ws), "s"(w) : "scc", "memory"); }
#pragma unroll
    for (int wc = 0; wc < 4; ++wc)
#pragma unroll
        for (int i = 0; i < 2; ++i) { if (wc == 0) MFMA_AV0(y[3 + i], km[0], o.q[i]); else MFMA_AV(y[3 + i], km[wc], o.q[wc * 2 + i]); }
    asm volatile("s_nop 7" : "+v"(y[3]), "+v"(y[4]));
    kg = __builtin_amdgcn_readlane(fl, 0); kk = __builtin_amdgcn_readlane(fl, 1);
    score_round4(q0, y[3]); score_round4(q1, y[4]);
    u32x2 Pm0, Pm1; u32x4 Px0, Px1;
    attn_soft<8>(q0, Pm0, Px0); attn_soft<12>(q1, Pm1, Px1);
    char* pxb = p.ws + G_D_PX + (size_t)(s * 32 + h) * 2048;
    char* pmb = p.ws + G_D_PM + (size_t)s * 16384 + h * 256; char* pm2 = pmb + 8192;
    WST16(a.voff, Px0, pxb, 0); WST16(a.voff, Px1, pxb, 1024);
    asm volatile("global_store_dwordx2 %0, %1, %3 sc1\n\tglobal_store_dwordx2 %0, %2, %4 sc1" :: "v"(a.voffm), "v"(Pm0), "v"(Pm1), "s"(pmb), "s"(pm2) : "memory");
    { const unsigned fo = attn_flag(s, h >> 2);
      asm volatile("s_cmp_lt_u32 %4, 46\n\ts_cbranch_scc1 1f\n\ts_waitcnt vmcnt(0)\n\ts_barrier\n\ts_cmp_lg_u32 %3, 0\n\ts_cbranch_scc1 1f\n\tglobal_store_dword %0, %1, %2 sc1\n1:"
                   :: "v"(fo), "v"(s), "s"(p.ws), "s"(w), "s"(s) : "scc", "memory"); }
    TST(s * 64 + h * 2, 2);
}
__device__ __forceinline__ void attn_role(const MainP& p, int s, int h, int w, int lane) {
    APtr a;
    a.q = p.ws + G_D_QX + (size_t)h * G_HS8 + skew(s, h) * 8192; a.k = a.q + (G_D_KX - G_D_QX);
    a.voff = lane * 16; a.vkm = (lane & 55) * 16; a.voffm = (lane & 15) * 16 + ((lane >> 4) & 1) * 8;
    a.vfl = G_WS_CTL + ((lane & 1) ? G_C_KM : G_C_GQ) + h * 128;
    BOps o0, o1, o2;
    wait_word(p, p.ws + G_WS_CTL + G_C_GQ + h * 128, attn_next(s), 10u);
    attn_batch8(p, o0, a);
    a.q = p.ws + G_D_QX + (size_t)h * G_HS8 + skew(attn_next(s), h) * 8192; a.k = a.q + (G_D_KX - G_D_QX);
    attn_batch8(p, o1, a);
    VMC0();
    PIN8(o0.q); PIN8A(o0.k); PIN8(o1.q); PIN8A(o1.k);
    int kg = -1, kk = -1;
    static_assert(NA % 2 == 0, "ranges");
#pragma unroll 1
    for (;;) {
        attn_step(p, o0, o1, o2, a, kg, kk, s, h, w, lane);
        if ((s = attn_next(s)) >= NG1) break;
        attn_step(p, o1, o2, o0, a, kg, kk, s, h, w, lane);
        if ((s = attn_next(s)) >= NG1) break;
        attn_step(p, o2, o0, o1, a, kg, kk, s, h, w, lane);
        if ((s = attn_next(s)) >= NG1) break;
    }
    VMC0();
}
__device__ __forceinline__ unsigned umax4(const u32x4& v) { return max(max(v[0], v[1]), max(v[2], v[3])); }
__device__ __forceinline__ bool give_up(const MainP& p, unsigned& tries, unsigned code) {
    if (++tries <= kWaitCap / 4) return false;
    __hip_atomic_store((gu32*)(p.ws + G_WS_CTL + G_C_ERR), code, RLX_AGENT);
    return true;
}
__device__ __forceinline__ void task_attn_tail(const MainP& p, int s, int h, int w, int lane) {
    const int g = lane >> 4, c = lane & 15, qt = w & 1;
    const float scale = 0.08838834764831845f;
    APtr a;
    a.q = p.ws + G_D_QX + (size_t)h * G_HS8 + skew(s, h) * 8192 + qt * 1024; a.k = p.ws + G_D_KX + (size_t)h * G_HS8 + skew(s, h) * 8192; a.m = p.ws + G_D_KM + (size_t)h * G_HS4 + skew(s, h) * 4096;
    a.voff = lane * 16; a.vkm = (lane & 55) * 16; a.voffm = c * 16 + (g & 1) * 8; a.vfl = G_WS_CTL;
    AOps o;
    wait_word(p, p.ws + G_WS_CTL + G_C_GQ + h * 128, s, 14u);
    attn_batch(p, o, a);
    VMC0();
    attn_pin(o);
    float q[12];
    u32x4 km[4];
#pragma unroll
    for (int kt = 1; kt >= -1; --kt) {
        if (kt < 0) {
            TST(s * 64 + h * 2 + qt, 0);
            wait_word(p, p.ws + G_WS_CTL + G_C_KM + h * 128, s - 2, 15u);
            if (FLG(p.prm[5]) & 2) wait_word<true>(p, p.ws + G_WS_CTL + G_C_KM + h * 128, s - 1, 15u);
            for (unsigned tries = 0;;) {
                WLD16X4(km[0], km[1], km[2], km[3], a.vkm, a.m);
                asm volatile("s_waitcnt vmcnt(0)" : "+v"(km[0]), "+v"(km[1]), "+v"(km[2]), "+v"(km[3]) :: "memory");
                if (!__any(max(max(umax4(km[0]), umax4(km[1])), max(umax4(km[2]), umax4(km[3]))) == 0xffffffffu) || give_up(p, tries, 16u)) break;
            }
            TST(s * 64 + h * 2 + qt, 1);
        }
        f32x4 y = {0.f, 0.f, 0.f, 0.f};
#pragma unroll
        for (int wc = 0; wc < 4; ++wc) y = MFMA32(kt < 0 ? km[wc] : o.k[wc * 2 + (kt < 0 ? 0 : kt)], o.q[wc], y);
#pragma unroll
        for (int i = 0; i < 4; ++i) q[4 + 4 * kt + i] = (16 * kt + 4 * g + i <= 16 * qt + c) ? bfr(bfr(y[i]) * scale) : -INFINITY;
    }
    u32x2 Pm; u32x4 Px;
    attn_soft<12>(q, Pm, Px);
    char* pxb = p.ws + G_D_PX + (size_t)(s * 32 + h) * 2048 + qt * 1024;
    char* pmb = p.ws + G_D_PM + (size_t)s * 16384 + h * 256 + qt * 8192;
    WST16(a.voff, Px, pxb, 0);
    asm volatile("global_store_dwordx2 %0, %1, %2 sc1" :: "v"(a.voffm), "v"(Pm), "s"(pmb) : "memory");
    TST(s * 64 + h * 2 + qt, 2);
}

#define RLD(d_, voff_, base_, off_) asm volatile("global_load_dwordx4 %0, %1, %2 offset:%3 sc1" : "=v"(d_) : "v"(voff_), "s"(base_), "i"(off_) : "memory")
#define RLDA(d_, voff_, base_, off_) asm volatile("global_load_dwordx4 %0, %1, %2 offset:%3 sc1" : "=a"(d_) : "v"(voff_), "s"(base_), "i"(off_) : "memory")
#define MFMA_ACC_A(acc, a_, b_) asm volatile("v_mfma_f32_16x16x32_bf16 %0, %1, %2, %0" : "+v"(acc) : "a"(a_), "v"(b_))
#define VMW(n_) asm volatile("s_waitcnt vmcnt(%0)" :: "i"(n_) : "memory")
template <int NT, int DEPTH, bool TAIL>
__device__ __forceinline__ void task_out(const MainP& p, char* smem, int s, int ct0, int w, int lane) {
    const int g = lane >> 4, c = lane & 15;
    const unsigned voff = lane * 16;
    f32x4 acc[NT][2];
#pragma unroll
    for (int t = 0; t < NT; ++t) { acc[t][0] = f32x4{0.f, 0.f, 0.f, 0.f}; acc[t][1] = acc[t][0]; asm volatile("" : "+v"(acc[t][0]), "+v"(acc[t][1])); }
    const char* ub[NT];
#pragma unroll
    for (int t = 0; t < NT; ++t) ub[t] = uni(p.ws + G_D_UX + (size_t)(ct0 + t) * G_UXS + skew(s, ct0 + t) * 32768 + w * 8192);
    const char* pb = uni(p.ws + G_D_PX + (size_t)(s * 32 + w * 8) * 2048);
    const char* um = uni(p.ws + um_base(s, p.prm[0]) + ct0 * 16384);
    unsigned vu[NT];
#pragma unroll
    for (int t = 0; t < NT; ++t) { vu[t] = t * 16384 + w * 2048 + voff; asm volatile("" : "+v"(vu[t])); }
    const char* pm = uni(p.ws + G_D_PM + (size_t)s * 16384 + w * 2048);
    constexpr int NR = DEPTH + 1 > 10 ? 10 : DEPTH + 1;
    u32x4 a[NR][NT], b[10][2];
    unsigned vb = voff, va = voff;
    TST(4096 + (s * 36 + ct0) * 4 + w, 0);
    const int f0 = (int)__hip_atomic_load((const gu32*)(p.ws + G_WS_CTL + (lane < 8 ? G_C_UM + (w * 8 + lane) * 128 : lane < 16 ? G_C_GV + ((s < (int)G_S1 ? s & 1 : 0) * 32 + w * 8 + lane - 8) * 128
                                                                                                                                : attn_flag(s, w * 2 + (lane & 1)) - G_WS_CTL)), RLX_AGENT);
    const bool early = !TAIL && !__any(f0 < s);
    if (!early) wait_word(p, p.ws + G_WS_CTL + (lane < 8 ? G_C_GV + ((s < (int)G_S1 ? s & 1 : 0) * 32 + w * 8 + lane) * 128 : G_C_UM + w * 8 * 128), lane < 8 ? s : s - 3, 21u);
#define ALOAD(q) do { if ((q) < 8) { _Pragma("unroll") for (int t = 0; t < NT; ++t) RLDA(a[(q) % NR][t], va, ub[t], 0);  \
                                     asm volatile("v_add_u32 %0, 0x400, %0" : "+v"(va)); }                                            \
                      else { _Pragma("unroll") for (int t = 0; t < NT; ++t) RLDA(a[(q) % NR][t], vu[t], um, ((q) & 1) * 1024); } } while (0)
    constexpr int D0 = DEPTH < 8 ? DEPTH : 8;
    asm volatile("s_nop 4");
#pragma unroll
    for (int q = 0; q < D0; ++q) ALOAD(q);
    asm volatile("s_cmp_lg_u32 %0, 0\n\ts_cbranch_scc1 1f\n\ts_waitcnt vmcnt(0)\n1:\n\ts_waitcnt vmcnt(30)" :: "s"(__builtin_amdgcn_readfirstlane((int)early)) : "scc", "memory");
    unsigned tries = 0;
    if (TAIL) {
        const char* sp = pm + (lane & 7) * 256 + ((lane >> 3) & 1) * 8192;
        const int dl = (FLG(p.prm[5]) >> 26) & 31;
        if (dl) {
            wait_word<true>(p, p.ws + G_WS_CTL + G_C_KM + ((ct0 >> 1) + w) * 128, s - 1, 25u);
            for (int i = 0; i < dl + (s < 63 ? 5 : 0); ++i) __builtin_amdgcn_s_sleep(2);
        } else
        while (__any(__hip_atomic_load((const gu32*)sp, RLX_AGENT) == 0xffffffffu) && !give_up(p, tries, 23u)) __builtin_amdgcn_s_sleep(1);
    } else if (!early) wait_word(p, p.ws + G_WS_CTL + (lane < 8 ? G_C_UM + (w * 8 + lane) * 128 : attn_flag(s, w * 2 + (lane & 1)) - G_WS_CTL), s, 22u);
    if (!early) TST(4096 + (s * 36 + ct0) * 4 + w, 1);
    for (;;) {
    asm volatile("s_nop 4");
#pragma unroll
    for (int q = 0; q < 8; ++q) { RLD(b[q][0], vb, pb, 0); RLD(b[q][1], vb, pb, 1024); asm volatile("v_add_u32 %0, 0x800, %0" : "+v"(vb)); }
#pragma unroll
    for (int q = 8; q < 10; ++q) { RLD(b[q][0], voff, pm, (q & 1) * 1024); RLD(b[q][1], voff, pm + 8192, (q & 1) * 1024); }
#pragma unroll
    for (int q = D0; q < NR; ++q) ALOAD(q);
#pragma unroll
    for (int q = 0; q < 10; ++q) {
        const int issuedA = (NR + q < 10 ? NR + q : 10) - D0;
        if (q < D0) VMW(2 * (9 - q) + issuedA * NT); else VMW((issuedA - (q - D0 + 1)) * NT);
#pragma unroll
        for (int t = 0; t < NT; ++t) { MFMA_ACC_A(acc[t][0], a[q % NR][t], b[q][0]); MFMA_ACC_A(acc[t][1], a[q % NR][t], b[q][1]); }
        if (NR + q < 10) ALOAD(NR + q);
    }
    asm volatile("s_nop 7" ::: "memory");
#pragma unroll
    for (int t = 0; t < NT; ++t) asm volatile("" : "+v"(acc[t][0]), "+v"(acc[t][1]));
    if (!TAIL) break;
    f32x4 chk = acc[0][0];
#pragma unroll
    for (int t = 0; t < NT; ++t) chk += acc[t][1] + acc[t][0];
    const float ck = (chk[0] + chk[1]) + (chk[2] + chk[3]);
    if (!__any(ck != ck)) break;
    unsigned mx = 0;
#pragma unroll
    for (int q = 0; q < 10; ++q) { asm volatile("" : "+v"(b[q][0]), "+v"(b[q][1])); mx = max(mx, max(umax4(b[q][0]), umax4(b[q][1]))); }
#pragma unroll
    for (int t = 0; t < NT; ++t) { asm volatile("" : "+a"(a[8 % NR][t]), "+a"(a[9 % NR][t])); mx = max(mx, max(umax4(a[8 % NR][t]), umax4(a[9 % NR][t]))); }
    if (!__any(mx == 0xffffffffu) || give_up(p, tries, 24u)) break;
    vb = voff;
#pragma unroll
    for (int t = 0; t < NT; ++t) { acc[t][0] = f32x4{0.f, 0.f, 0.f, 0.f}; acc[t][1] = acc[t][0]; asm volatile("" : "+v"(acc[t][0]), "+v"(acc[t][1])); }
    }
    TST(4096 + (s * 36 + ct0) * 4 + w, 2);
    f32x4* red = (f32x4*)smem;
#pragma unroll
    for (int t = 0; t < NT; ++t) { red[((w * NT + t) * 2 + 0) * 64 + lane] = acc[t][0]; red[((w * NT + t) * 2 + 1) * 64 + lane] = acc[t][1]; }
    __syncthreads();
    u16* orow = p.out + (size_t)(32 * s + 16 * (w & 1) + c) * OUTW + 16 * ct0 + 4 * g;
#pragma unroll
    for (int t = 0; t < NT; ++t)
        if ((t & 1) == (w >> 1)) {
            const int m = w & 1;
            const f32x4 v = (red[((0 * NT + t) * 2 + m) * 64 + lane] + red[((1 * NT + t) * 2 + m) * 64 + lane]) +
                            (red[((2 * NT + t) * 2 + m) * 64 + lane] + red[((3 * NT + t) * 2 + m) * 64 + lane]);
            *(u32x2*)(orow + 16 * t) = u32x2{pk2bf(v[0], v[1]), pk2bf(v[2], v[3])};
        }
}

__device__ __forceinline__ int index_of_l(int l) {
    const int pv = (int)bfr((float)l);
    if (pv <= 256) return pv;
    if (pv <= 512) return 256 + ((pv - 256) >> 1);
    if (pv <= 1024) return 384 + ((pv - 512) >> 2);
    return 512 + ((pv - 1024) >> 3);
}
static constexpr int KROW = G_KROW;
static constexpr int KARR = 80 * KROW;
static constexpr int UCS = 2208;
struct BCh {
    u16 uc[4][UCS];
    f32x4 cst[NST];
    unsigned Kd[648];
    u16 Kh[KARR];
    u16 Kl[KARR];
};
static_assert(4 * sizeof(BCh) <= LDS_TOTAL - 32, "LDS");
__device__ __forceinline__ unsigned prm(unsigned hi, unsigned lo, unsigned sel) { return __builtin_amdgcn_perm(hi, lo, sel); }
#define BST(i) asm volatile("" ::: "memory")
#define UT_LOAD(p_) __builtin_nontemporal_load(p_)
template <class F>
__device__ __forceinline__ void task_ut(const MainP& p, char* smem, int rb, int w, int lane, F more) {
    const int tid = w * 64 + lane, g = lane >> 4, c = lane & 15;
    { char* ws = p.ws; if (tid == 0) IST(4, rb); }
    const u32x4* src = (const u32x4*)(p.U + (size_t)rb * 64 * DM);
    u32x4 v[18];
#pragma unroll
    for (int i = 0; i < 18; ++i) v[i] = UT_LOAD(src + tid + 256 * i);
#pragma unroll
    for (int i = 0; i < 18; ++i) { const int ch = tid + 256 * i, row = ch / 72; *(u32x4*)(smem + row * 1184 + (ch - 72 * row) * 16) = v[i]; }
    __syncthreads();
    { char* ws = p.ws; if (tid == 0) IST(3, rb); }
    const unsigned ad = (unsigned)(uintptr_t)smem + (16 * g + (c >> 2)) * 1184 + (w * 144 + 4 * (c & 3)) * 2;
    char* tw = smem + 77824 + w * 20736;
#pragma unroll
    for (int i = 0; i < 9; ++i) {
        u32x2 x0, x1, x2, x3;
        asm volatile("ds_read_b64_tr_b16 %0, %4 offset:%5\n\tds_read_b64_tr_b16 %1, %4 offset:%6\n\tds_read_b64_tr_b16 %2, %4 offset:%7\n\tds_read_b64_tr_b16 %3, %4 offset:%8\n\ts_waitcnt lgkmcnt(0)"
                     : "=&v"(x0), "=&v"(x1), "=&v"(x2), "=&v"(x3) : "v"(ad), "i"(i * 32), "i"(4 * 1184 + i * 32), "i"(8 * 1184 + i * 32), "i"(12 * 1184 + i * 32) : "memory");
        u32x4* o = (u32x4*)(tw + (16 * i + c) * 144 + g * 32);
        o[0] = u32x4{x0[0], x0[1], x1[0], x1[1]}; o[1] = u32x4{x2[0], x2[1], x3[0], x3[1]};
    }
    asm volatile("" ::: "memory");
    const unsigned vo = (w * 144 + (lane >> 3)) * (SL * 2) + rb * 128 + (lane & 7) * 16;
    const char* dst = p.ws + G_D_UT;
#pragma unroll
    for (int i = 0; i < 18; ++i) {
        const u32x4 a = *(const u32x4*)(tw + (8 * i + (lane >> 3)) * 144 + (lane & 7) * 16);
        const unsigned vi = vo + i * 8 * (SL * 2);
        WST16(vi, a, dst, 0);
    }
    VMC0();
    init_wait(p.ws, w, lane);
    if (tid == 0) __hip_atomic_store((gu32*)(p.ws + G_WS_CTL + G_C_UT + rb * 128), 1u, RLX_AGENT);
    { char* ws = p.ws; if (tid == 0) IST(2, rb); }
    more();
    VMC0();
    __syncthreads();
    if (tid == 0) __hip_atomic_store((gu32*)(p.ws + G_WS_CTL + G_C_UT + rb * 128), 2u, RLX_AGENT);
}
struct KPar { u16 ldt, pa[2][4]; };
__device__ __forceinline__ KPar kgen_par(const MainP& p, int d, int lane) {
    KPar k;
    k.ldt = p.logdt[d];
#pragma unroll
    for (int q = 0; q < 2; ++q) { const int i = d * NST + lane + 64 * q; k.pa[q][0] = p.logA[i]; k.pa[q][1] = p.Aim[i]; k.pa[q][2] = p.Cre[i]; k.pa[q][3] = p.Cim[i]; }
    return k;
}
__device__ __forceinline__ void task_s4d(const MainP& p, BCh& L, int d, int lane, bool first, int& nb) {
    const int g = lane >> 4, c = lane & 15;
    BST(0);
    const KPar kp = kgen_par(p, d, lane);
    const float dsk = bf2f(p.Dskip[d]);
    const auto& pa = kp.pa;
    const float dt = bfr(expf(bf2f(kp.ldt)));
#pragma unroll
    for (int q = 0; q < 2; ++q) {
        const float ar = -bfr(expf(bf2f(pa[q][0])));
        const float cr = bf2f(pa[q][2]) * dt, ci = bf2f(pa[q][3]) * dt;
        L.cst[lane + 64 * q] = f32x4{ar * dt * 1.4426950408889634f, bf2f(pa[q][1]) * dt * 0.15915494309189535f,
                                     __log2f(hypotf(cr, ci)), atan2f(ci, cr) * 0.15915494309189535f};
    }
    BST(1);
    {
        f32x4 acc[3];
#pragma unroll
        for (int v = 0; v < 3; ++v) acc[v] = f32x4{0.f, 0.f, 0.f, 0.f};
        auto base_of = [](int v, int r) { return v == 0 ? (r & 7) + 128 * (r >> 3) : v == 1 ? (r < 8 ? 256 + 2 * (r & 3) + 128 * (r >> 2) : 512 + 4 * (r & 1) + 128 * ((r - 8) >> 1))
                                                                                           : 1024 + 128 * min(r, 8); };
        const float b0 = (float)base_of(0, c), b1 = (float)base_of(1, c), b2 = (float)base_of(2, c), off = (float)(8 * c);
#define KG_POLAR(h_, l_, lg_, f_, a_, b_) do { const float m_ = __builtin_amdgcn_exp2f(fmaf(cc[0], b_, lg_)), p_ = __builtin_amdgcn_fractf(fmaf(a_, b_, f_)); \
            const float re_ = m_ * __builtin_amdgcn_cosf(p_), im_ = m_ * __builtin_amdgcn_sinf(p_);                                                            \
            h_ = pk2bf(re_, im_); l_ = pk2bf(re_ - lo2f(h_), im_ - hi2f(h_)); } while (0)
#pragma unroll 1
        for (int t = 0; t < NST / 16; ++t) {
            u32x4 fh, fl, ah[3], al[3];
#pragma unroll
            for (int j = 0; j < 4; ++j) {
                const f32x4 cc = L.cst[16 * t + 4 * g + j];
                KG_POLAR(fh[j], fl[j], 0.f, 0.f, -cc[1], off);
                KG_POLAR(ah[0][j], al[0][j], cc[2], cc[3], cc[1], b0); KG_POLAR(ah[1][j], al[1][j], cc[2], cc[3], cc[1], b1); KG_POLAR(ah[2][j], al[2][j], cc[2], cc[3], cc[1], b2);
            }
#pragma unroll
            for (int v = 0; v < 3; ++v) { acc[v] = MFMA32(al[v], fh, acc[v]); acc[v] = MFMA32(ah[v], fl, acc[v]); acc[v] = MFMA32(ah[v], fh, acc[v]); }
        }
        BST(2);
        auto split = [](float k) { const unsigned hi = pk2bf(k, 0.f) & 0xffffu; return hi | (pk2bf(0.f, k - __uint_as_float(hi << 16)) & 0xffff0000u); };
#pragma unroll
        for (int v = 0; v < 3; ++v)
#pragma unroll
            for (int i = 0; i < 4; ++i) {
                const int pos = base_of(v, 4 * g + i) + 8 * c;
                if (pos <= 2048) L.Kd[index_of_l(pos)] = split(2.0f * acc[v][i]);
            }
    }
    if (first) wait_word(p, p.ws + G_WS_CTL + G_C_UT + (lane & 31) * 128, 1, 30u);
    const char* ut = uni(p.ws + G_D_UT + (size_t)d * 4096);
    const unsigned voff = lane * 16;
    u32x4 uu[4];
    WLD16X4(uu[0], uu[1], uu[2], uu[3], voff, ut);
    const u32x4 z4 = {0u, 0u, 0u, 0u};
    *(u32x4*)&L.Kh[8 * lane] = z4; *(u32x4*)&L.Kl[8 * lane] = z4;
    *(u32x4*)&L.Kh[512 + 8 * (lane & 31)] = z4; *(u32x4*)&L.Kl[512 + 8 * (lane & 31)] = z4;
    *(u32x4*)&L.uc[0][64 + SL] = z4;
    *(u32x4*)&L.uc[(lane >> 3) & 3][8 * (lane & 7)] = z4;
    asm volatile("" ::: "memory");
#pragma unroll
    for (int q = 0; q < 4; ++q) {
        const int e = lane + 64 * q, at = (16 + (e >> 2)) * KROW + 24 - 8 * (e & 3);
        unsigned k[8];
#pragma unroll
        for (int j = 0; j < 8; ++j) k[j] = L.Kd[index_of_l(8 * e + 7 - j)];
        u32x4 kh, kl;
#pragma unroll
        for (int j = 0; j < 4; ++j) { kh[j] = prm(k[2 * j + 1], k[2 * j], 0x05040100u); kl[j] = prm(k[2 * j + 1], k[2 * j], 0x07060302u); }
        *(u32x4*)&L.Kh[at] = kh; *(u32x4*)&L.Kl[at] = kl;
    }
    __builtin_amdgcn_sched_barrier(0);
    asm volatile("s_waitcnt vmcnt(0)" : "+v"(uu[0]), "+v"(uu[1]), "+v"(uu[2]), "+v"(uu[3]) :: "memory");
    BST(3);
#pragma unroll
    for (int q = 0; q < 4; ++q) *(u32x4*)&L.uc[0][64 + 8 * (lane + 64 * q)] = uu[q];
    asm volatile("" ::: "memory");
#pragma unroll
    for (int q = 0; q < 4; ++q) {
        const int e = lane + 64 * q;
        const int ue = 56 + 8 * e;
        const u32x4 a = *(const u32x4*)&L.uc[0][ue];
        const u32x2 b = *(const u32x2*)&L.uc[0][ue + 8];
        const unsigned s0 = __builtin_amdgcn_alignbit(a[1], a[0], 16), s1 = __builtin_amdgcn_alignbit(a[2], a[1], 16), s2 = __builtin_amdgcn_alignbit(a[3], a[2], 16),
                       s3 = __builtin_amdgcn_alignbit(b[0], a[3], 16), s4 = __builtin_amdgcn_alignbit(b[1], b[0], 16);
        *(u32x4*)&L.uc[1][ue] = u32x4{s0, s1, s2, s3};
        *(u32x4*)&L.uc[2][ue] = u32x4{a[1], a[2], a[3], b[0]};
        *(u32x4*)&L.uc[3][ue] = u32x4{s1, s2, s3, s4};
    }
    if (lane == 0) {
        const u32x4 a = *(const u32x4*)&L.uc[0][56 + SL];
        const unsigned s0 = __builtin_amdgcn_alignbit(a[1], a[0], 16), s1 = __builtin_amdgcn_alignbit(a[2], a[1], 16), s2 = __builtin_amdgcn_alignbit(a[3], a[2], 16), s3 = a[3] >> 16;
        *(u32x4*)&L.uc[1][56 + SL] = u32x4{s0, s1, s2, s3};
        *(u32x4*)&L.uc[2][56 + SL] = u32x4{a[1], a[2], a[3], 0u};
        *(u32x4*)&L.uc[3][56 + SL] = u32x4{s1, s2, s3, 0u};
    }
    BST(4);
    {
        const int o = 33 + c + 8 * g;
        const unsigned ua = (unsigned)(uintptr_t)&L.uc[o & 3][o & ~3], ka = (unsigned)(uintptr_t)&L.Kh[(16 + c) * KROW + 8 * g];
        const f32x4* res = (const f32x4*)L.Kh + lane;
        asm volatile(ASM_CV :: "v"(ua), "v"(ka), "v"((unsigned)(uintptr_t)res) : CV_CLOB);
        BST(5);
#pragma unroll
        for (int par = 0; par < 2; ++par)
#pragma unroll
            for (int t = 0; t < 4; ++t) {
                const int n = 16 * (par + 2 * (16 * t + c)) + 4 * g;
                const u32x2 uu = *(const u32x2*)&L.uc[0][64 + n];
                const f32x4 y4 = res[(par * 4 + t) * 64];
                const float r0 = bfr(y4[0]) + bfr(lo2f(uu[0]) * dsk), r1 = bfr(y4[1]) + bfr(hi2f(uu[0]) * dsk);
                const float r2 = bfr(y4[2]) + bfr(lo2f(uu[1]) * dsk), r3 = bfr(y4[3]) + bfr(hi2f(uu[1]) * dsk);
                *(u32x2*)&L.uc[1][n] = u32x2{pk2bf(r0, r1), pk2bf(r2, r3)};
            }
    }
    BST(6);
    char* yb = uni(p.ws + G_D_YB + (size_t)d * 4096);
#pragma unroll
    for (int q = 0; q < 4; ++q) { const u32x4 o = *(const u32x4*)&L.uc[1][8 * (lane + 64 * q)]; WST16(voff, o, yb, q * 1024); }
    if (lane == 0) nb = (int)(__hip_atomic_fetch_add((gu32*)(p.ws + G_WS_CTL + G_C_TASK + 128), nb >= 0 ? 1u : 0u, RLX_AGENT) + 1u);
    VMC0();
    if (lane == 0) __hip_atomic_fetch_add((gu32*)(p.ws + G_WS_CTL + G_C_YC + (d >> 6) * 128), 1u, RLX_AGENT);
    BST(7);
}
__device__ __forceinline__ void s4d_wave(const MainP& p, char* smem, int w, int lane, int quota) {
    int nb = 0;
    if (lane == 0) nb = (int)(__hip_atomic_fetch_add((gu32*)(p.ws + G_WS_CTL + G_C_TASK + 128), 1u, RLX_AGENT) + 1u);
#pragma unroll 1
    for (int n = 0; n < quota; ++n) {
        const int d = __builtin_amdgcn_readfirstlane(nb);
        if (d >= DM) break;
        nb = n + 1 < quota ? 0 : -1;
        task_s4d(p, reinterpret_cast<BCh*>(smem)[w], d, lane, n == 0, nb);
    }
}
__device__ __forceinline__ void watch_role(const MainP& p, int lane) {
    const char* c = p.ws + G_WS_CTL;
#pragma unroll 1
    for (int s = G_S0; s < NSEG; ++s) {
        const int n = min(s + 3, NSEG - 1);
        wait_word(p, c + G_C_GV + lane * 128, n - (n < (int)G_S1 ? (n ^ (lane >> 5)) & 1 : 0), 60u);
        wait_word(p, c + (lane < 32 ? G_C_GX : G_C_UT - 4096) + lane * 128, lane < 32 ? min(s + 2, NSEG - 1) : s < 40 ? -1 : 2, 61u);
        if (lane == 0) __hip_atomic_store((gu32*)(p.ws + G_WS_CTL + G_C_RDY + s * 128), 1u, RLX_AGENT);
    }
}
__device__ __forceinline__ void task_merge(const MainP& p, char* smem, int gr, int i, int w, int lane) {
    const int g = lane >> 4, c = lane & 15;
    wait_word(p, p.ws + G_WS_CTL + G_C_YC + gr * 128, 63, 31u);
    const char* sb = uni(p.ws + G_D_YB + (size_t)gr * 262144 + i * 1024 + w * 256);
    const unsigned vt = g * 4096 + c * 16;
    u32x4 z[16];
#pragma unroll
    for (int j = 0; j < 16; ++j) { const char* sj = sb + j * 16384; WLD16(z[j], vt, sj, 0); }
    asm volatile("s_waitcnt vmcnt(0)" : "+v"(z[0]), "+v"(z[1]), "+v"(z[2]), "+v"(z[3]), "+v"(z[4]), "+v"(z[5]), "+v"(z[6]), "+v"(z[7]),
                 "+v"(z[8]), "+v"(z[9]), "+v"(z[10]), "+v"(z[11]), "+v"(z[12]), "+v"(z[13]), "+v"(z[14]), "+v"(z[15]) :: "memory");
    char* tw = smem + w * 17408;
#pragma unroll
    for (int j = 0; j < 16; ++j) *(u32x4*)(tw + (4 * j + g) * 272 + c * 16) = z[j];
    const unsigned ad = (unsigned)(uintptr_t)tw + (8 * g + (c >> 2)) * 272 + (c & 3) * 8;
    u16* orow = p.out + (size_t)(512 * i + 128 * w + c) * OUTW + DM + 64 * gr + 8 * g;
#pragma unroll
    for (int k = 0; k < 8; ++k) {
        u32x2 x0, x1, x2, x3;
        asm volatile("ds_read_b64_tr_b16 %0, %4 offset:%5\n\tds_read_b64_tr_b16 %1, %4 offset:%6\n\tds_read_b64_tr_b16 %2, %4 offset:%7\n\tds_read_b64_tr_b16 %3, %4 offset:%8\n\ts_waitcnt lgkmcnt(0)"
                     : "=&v"(x0), "=&v"(x1), "=&v"(x2), "=&v"(x3) : "v"(ad), "i"(k * 32), "i"(4 * 272 + k * 32), "i"(32 * 272 + k * 32), "i"(36 * 272 + k * 32) : "memory");
        *(u32x4*)(orow + (size_t)16 * k * OUTW) = u32x4{x0[0], x0[1], x1[0], x1[1]};
        *(u32x4*)(orow + (size_t)16 * k * OUTW + 32) = u32x4{x2[0], x2[1], x3[0], x3[1]};
    }
}

static constexpr int Q1 = NG1 * 6 + 36, Q2 = Q1 + (NFINE - 2) * 25;
static constexpr int NTASK = Q2 + 68;
__host__ __device__ __forceinline__ void task_of(int u, int m0, int& sg, int& r) {
    if (u < 6 * m0) { sg = u / 6; r = u % 6; }
    else if (u < 6 * m0 + 108) { sg = m0 + (u - 6 * m0) / 9; r = (u - 6 * m0) % 9; }
    else if (u < Q1) { sg = (u - 36) / 6; r = (u - 36) % 6; }
    else if (u < Q2) { sg = NG1 + (u - Q1) / 25; r = (u - Q1) % 25; }
    else { sg = 62 + (u - Q2) / 34; r = (u - Q2) % 34; }
}
__device__ __forceinline__ void worker_body(const MainP& p, char* smem, const int w, const int lane) {
    const int tid = w * 64 + lane;
    int* slot = (int*)(smem + LDS_TOTAL - 16);
    int nst = 0, nxt = -1;
    gu32* qa = (gu32*)(p.ws + G_WS_CTL + G_C_TASK);
    const int m0 = (FLH(p.prm[7]) >> 4) & 63;
    for (;;) {
        if (tid == 0) {
            *slot = nxt >= 0 ? nxt : (int)(__hip_atomic_fetch_add(qa, 1u, RLX_AGENT) + 1u);
            nxt = -1;
        }
        __syncthreads();
        const int u = __builtin_amdgcn_readfirstlane(*slot);
        if (u >= NTASK) break;
        if (tid == 0 && u < Q1 - 140) nxt = (int)(__hip_atomic_fetch_add(qa, 1u, RLX_AGENT) + 1u);
        const bool last2 = u >= Q2;
        int sg, r;
        task_of(u, m0, sg, r);
        if (u < Q1) {
            if (r < 6) task_out<6, 6, false>(p, smem, sg, 6 * r, w, lane);
            else { const int m = 3 * (sg - m0) + r - 6; task_merge(p, smem, m >> 2, m & 3, w, lane); }
        }
        else if (r < 16) task_attn_tail(p, sg, 2 * r + (w >> 1), w, lane);
        else if (last2) task_out<2, 9, true>(p, smem, sg, 2 * (r - 16), w, lane);
        else task_out<4, 9, true>(p, smem, sg, 4 * (r - 16), w, lane);
        if (last2) break;
        __syncthreads();
    }
}

__host__ __device__ constexpr unsigned phys(unsigned x) { return (x >> 13) * 16384 + (x & 8191); }
__host__ __device__ constexpr unsigned aMB(int f, int b) { return phys(f < 8 ? 16384 + f * 1024 + b : 28672 + b); }
__host__ __device__ constexpr unsigned aPX(int pr, int b) { return phys(pr * 1024 + b); }
__host__ __device__ constexpr unsigned aPM(int u, int b) { return phys(24576 + u * 1024 + b); }
static constexpr int L_KI = 8192, L_QI = 16384, L_VI = 147456;

__device__ __forceinline__ int lane_id() { int l; asm volatile("v_mbcnt_lo_u32_b32 %0, -1, 0\n\tv_mbcnt_hi_u32_b32 %0, -1, %0" : "=v"(l)); return l; }
typedef const __attribute__((address_space(4))) MainP* KArg;

__global__ __launch_bounds__(256, 1) void main_kernel(MainP p_) {
    __shared__ __attribute__((aligned(16))) char smem[LDS_TOTAL];
    KArg ka = (KArg)__builtin_amdgcn_kernarg_segment_ptr();
    static_assert(offsetof(MainP, W) == 0x28, "argument layout");
    asm volatile(ASM_EA :: "s"(ka), "s"(blockIdx.x), "v"(threadIdx.x) : "v1", "v2", "s40", "s41", "s42", "s43", "s44", "s45", "s46", "s47", "s48", "s49", "scc", "memory");
    asm volatile("" : "+s"(ka));
    const int b = blockIdx.x, x = b & 7, ix = b >> 3;
    const int w = __builtin_amdgcn_readfirstlane(threadIdx.x >> 6);
    const bool isr = x < 4 && ix >= 16;
    constexpr int NRF = 8 + 8 * G_GVN;
    if (__builtin_expect(ix < (x < 4 ? 16 : NRF), 1)) {
        const int h = (x & 3) * 8 + (ix & 7), id = h + (ix >> 4) * 32;
        const unsigned stb = 0;
#define UNR _Pragma("unroll")
#define CW(k, val) do { if ((k) < 16) ca[(k) & 15] = (unsigned)(val); else if ((k) < 32) cb[(k) & 15] = (unsigned)(val); else cl[((k) - 40) & 15] = (unsigned)(val); } while (0)
#define ROLEW(A_) asm volatile(A_ : "+{v[0:15]}"(ca), "+{v[16:31]}"(cb), "+{v[200:215]}"(cl) : "s"(ka), "s"(w), "s"(id), "s"(stb) : ASM_CLOBW)
#define PAD(n_) asm volatile("s_branch skip_" n_ "\npad_" n_ ":\n\ts_setpc_b64 s[46:47]\nskip_" n_ ":")
#define CW_ALL const int lane = lane_id(), g = lane >> 4, c = lane & 15, q = c >> 2; u32x16 ca = {}, cb = {}, cl = {};                                      \
        CW(0, lane * 16); CW(40, (4 * w + g) * 8192 + h * 256 + (c ^ (2 * g + 8 * (w >> 1))) * 16);                                                          \
        UNR for (int k = 0; k < 2; ++k) CW(41 + k, (8 * g + q) * 256 + ((4 * w + 2 * k + ((c >> 1) & 1)) ^ (2 * q + 8 * (g & 1))) * 16 + (c & 1) * 8)
#define CW_SRC(b_, key_) UNR for (int k = 0; k < 9; ++k) { const int pp = 64 * k + lane, r = pp / 72, pc = pp - 72 * r; CW(43 + k, (b_) + (8 * w + r) * 1152 + (pc ^ (key_)) * 16); }
#define UNIT(u_, key_) (((u_) >> 2) * 128 + (((u_) & 3) ^ (key_)) * 32 + (c & 3) * 8)
#define CW_NEAR CW(1, x * 65536 + aMB(2 * w, lane * 16)); CW(2, x * 65536 + aMB(8, w * 256 + lane * 4)); CW(3, 2 * w * 1024 + lane * 16);                            \
        UNR for (int i = 0; i < 6; ++i) CW(4 + i, ((2 * w + 2 + i) & 7) * 1024 + lane * 16);                                                                 \
        CW(10, (c & 2) ? 0xEEEE : 0x4444);                                                                                                                   \
        CW(20, x * 65536 + 22528 + (lane & 31) * 64); CW(21, lane < 32 ? (lane & 3) * 65536 + phys((lane >> 2) * 2048) + h * 64 : 32768 + h * 128);          \
        CW(22, lane * 4); CW(23, x * 65536 + aMB(2 * w + 1, lane * 16))
#define CW_FAR CW(0, c * 1152 + (g ^ (c >> 1)) * 16); CW(1, ca[0]); CW(2, ca[0] ^ 64); CW(3, w * 9216 + lane * 16); CW_SRC(0, (r >> 1) + 4 * (w & 1))                                   \
        CW(4, L_VI + w * 2048 + lane * 16); CW(5, L_VI + lane * 16); CW(6, lane * 4)
        if (x >= 4 && ix >= 8) {
            PAD("gv"); CW_ALL; CW_FAR;
            UNR for (int t = 0; t < 9; ++t) CW(8 + t, (9 * w + t) * G_UXS + skew(0, 9 * w + t) * 32768 + h * 1024 + lane * 16);
            UNR for (int k = 0; k < 2; ++k) CW(19 + k, (9 * w + 4 * k + g) * 16384 + h * 256 + c * 16);
            CW(21, (9 * w + 8) * 16384 + h * 256 + lane * 4);
            UNR for (int k = 0; k < 4; ++k) CW(52 + k, (4 * g + q) * 1152 + ((9 * w + k) >> 2) * 128 + ((2 * ((9 * w + k) & 3) + ((c >> 1) & 1)) ^ (2 * g + (q >> 1))) * 16 + (c & 1) * 8);
            ROLEW(ASM_GV);
        } else if (x >= 4) {
            PAD("gx"); CW_ALL; CW_FAR;
            CW(8, w * 2048 + lane * 16);
            ROLEW(ASM_GX);
        } else if (ix < 8) {
            PAD("qk"); CW_ALL; CW_NEAR;
            CW(12, L_QI + (16 * w + (c & 13)) * 32 + 8 * g + 4 * ((c >> 1) & 1));
            CW(13, L_QI + (8 * g + q) * 32 + (c & 3) * 8);
            CW(14, G_WS_CTL + G_C_KM + h * 128); CW(15, lane * 16 + 4096);
            CW(16, L_KI + (32 * w + c) * 16 + 4 * g);
            CW(17, L_KI + (4 * g + q) * 16 + (c & 1) * 8);
            const int mm = 8 * (w & 1) + 4 * (g >> 1) + (g & 1);
            CW(18, (x ^ 3) * 65536 + aPX(h >> 1, (h & 1) * 512 + (w & 1) * 256 + g * 64 + (c >> 3) * 32 + ((c >> 1) & 1) * 16 + (2 * (q & 1) + (c & 1)) * 4));
            CW(19, ((c & 1) + 2 * (c >> 3)) * 65536 + aPM(h >> 3, ((h >> 1) & 3) * 256 + (mm + 2 * ((c >> 1) & 1)) * 16 + (h & 1) * 8 + (q & 1) * 4));
            CW(24, G_WS_CTL + G_C_GX + h * 128);
            ROLEW(ASM_QK);
        } else {
            PAD("vv"); CW_ALL; CW_NEAR; CW_SRC(h * 147456, r & 4);
            const int tq = 9 * w + 2 * (g & 1) + (g >> 1);
            CW(14, tq * 16384 + h * 256 + c * 16);
            CW(15, (tq + 4) * 16384 + h * 256 + c * 16);
            CW(16, (9 * w + 8) * 16384 + h * 256 + c * 16 + (g & 1) * 8);
            CW(19, (c & 8) ? 0xEEEE : 0x4444);
            CW(12, L_QI + w * 576 + c * 16 + 4 * g);
            CW(13, L_QI + (8 * g + q) * 16 + (c & 1) * 8 + ((c >> 1) & 1) * 576);
            CW(25, L_QI + (8 * g + q) * 16 + (c & 1) * 8 + (1 - ((c >> 1) & 1)) * 576);
            UNR for (int k = 0; k < 4; ++k) CW(52 + k, ((k >> 1) ^ (q >> 1)) * 36864 + (4 * g + (q & 1)) * 1152 + UNIT(9 * w + 2 * (k & 1) + (q >> 1), 2 * (g & 1)));
            CW(24, (q >> 1) * 36864 + (4 * g + (q & 1)) * 1152 + UNIT(9 * w + 8, 2 * (g & 1)));
            ROLEW(ASM_V);
        }
    } else {
    if (isr && (ix < 25 || ix >= 29)) {
        const int id = x * 12 + (ix < 25 ? ix - 13 : ix - 29);
        init_cal(ka->ws, id, threadIdx.x, (FLG(ka->prm[5]) >> 16) & 15);
        if (FLG(ka->prm[5]) & 4) {
            const unsigned o = (id * 384 + threadIdx.x) * 128, o2 = o + (threadIdx.x & 128 ? 16384 : 32768);
            const char* a = (const char*)ka->X + o; const char* b2 = (const char*)ka->U + o;
            const char* c2 = (const char*)(w < 2 ? ka->X : ka->U) + o2;
            const char* ps = (const char*)(w == 0 ? ka->logA : w == 1 ? ka->Aim : w == 2 ? ka->Cre : ka->Cim) + id * 3072 + min((int)(threadIdx.x & 63), 23) * 128;
            asm volatile("s_mov_b32 m0, %4\n\ts_nop 0\n\tglobal_load_lds_dword %0, off\n\tglobal_load_lds_dword %1, off\n\tglobal_load_lds_dword %2, off\n\tglobal_load_lds_dword %3, off"
                         :: "v"(a), "v"(c2), "v"(b2), "v"(ps), "s"(163072) : "memory");
        }
    }
    if (x >= 4 ? ix >= 24 : (ix >= 25 && ix < 29)) init_fill(ka->ws, ka->mem0, x >= 4 ? (ix - 24) * 4 + (x & 3) : 32 + (ix - 25) * 4 + x, threadIdx.x);
    if (x >= 4 && !(FLG(ka->prm[5]) & 1)) {
        const char* src = (const char*)ka->W[3] + (size_t)((x & 3) * 8 + (ix & 7)) * 147456 + threadIdx.x * 128;
#pragma unroll
        for (int i = 0; i < 5; ++i)
            if (i < 4 || w < 2) asm volatile("s_mov_b32 m0, %1\n\ts_nop 0\n\tglobal_load_lds_dword %0, off" :: "v"(src + i * 32768), "s"(155648) : "memory");
    }
    if (isr && ix >= 25) {
        for (int i = 0; i < (ix < 29 ? 3 : 0) + HSL; ++i) __builtin_amdgcn_s_sleep(40);
        init_wait(ka->ws, w, threadIdx.x & 63);
    }
    asm volatile("" : "+s"(ka));
    if (x < 4 && (ix < 25 || w == 0)) {
        const int lane = threadIdx.x & 63, g = lane >> 4, c = lane & 15;
        const unsigned lb = w * G_STG;
        unsigned* ct = (unsigned*)(smem + lb) + lane;
        asm volatile("" :: "v"(ct) : "memory");
#define CTW(k, val) ct[(k) * 64] = (unsigned)(val)
        const u64 ws = (u64)ka->ws, prm = (u64)ka;
        const u64 stb = 0;
        CTW(0, lane * 16);
        const int j = x * 9 + ix - 16;
        if (ix >= 25) {
            CTW(1, x * 65536 + 22528 + (lane & 31) * 64);
            for (int k = 0; k < 7; ++k) {
                const int q = min(ix - 25 + 7 * k, 43), id = q < 36 ? 9 * x + (q >> 2) : 8 * x + q - 36;
                CTW(4 + k, (q < 36 ? G_D_UX + id * G_UXS + skew(0, id) * 32768 + (q & 3) * 8192 : G_D_KX + id * G_HS8 + skew(0, id) * 8192) + lane * 128);
                CTW(11 + k, q < 36 ? 32768 : 8192);
            }
        } else {
            const int rot = (FLG(ka->prm[5]) & 16) ? 0 : (x ^ 3) * 4;
            if (w >= 2) {
                return;
            }
            CTW(1, lane * 16 + 4096);
            CTW(2, x * 65536 + aMB(j >> 2, (j & 3) * 256 + (4 * g + 2 * ((c >> 1) & 1) + (c & 1)) * 16 + (c >> 2) * 4));
            for (int k = 0; k < 5; ++k) { const int q = 9 * k + ix - 16; CTW(46 + k, x * 65536 + lane * 16 + (q < 29 ? phys(q * 1024) : 0x200000 + phys((q - 29) * 1024))); }
            CTW(4, (c & 2) ? 0xEEEE : 0x4444); CTW(5, (c & 1) ? 0x03020706u : 0x05040100u);
            CTW(6, G_WS_CTL + G_C_GV + ((w & (G_GVN - 1)) * 32 + ((lane & 31) ^ (2 * rot))) * 128); CTW(7, x * 65536 + (lane & 31) * 64); CTW(8, lane * 4); CTW(9, 131072);
            for (int k = 0; k < 16; ++k) { const int i = k ^ rot; CTW(10 + k, x * 65536 + aPX(i, (g >> 1) * 512 + (2 * (c >> 2) + (c & 1)) * 64 + (g & 1) * 32 + ((c >> 1) & 1) * 16)); CTW(26 + k, i * 2048 + lane * 16); }
            for (int k = 0; k < 4; ++k) CTW(42 + k, x * 65536 + aPM(k, lane * 16));
        }
        const int id = ix >= 25 ? ix - 25 : j;
#define ROLE(A_) asm volatile(A_ :: "s"(ws), "s"(ws), "s"(ws), "s"(w), "s"(id), "s"(ws), "s"(prm), "s"(lb), "s"(stb) : ASM_CLOB)
        if (ix >= 25) ROLE(ASM_H);
        else ROLE(ASM_R);
    }
    else if (x >= 4 && ix >= 24) {
        if (ix >= 25) for (int i = 0; i < ((FLG(ka->prm[5]) >> 5) & 7); ++i) __builtin_amdgcn_s_sleep(36);
        const char* xs = (const char*)ka->X + (size_t)((ix - 24) * 4 + (x & 3)) * 73728 + threadIdx.x * 128;
        const int f = (ix - 24) * 4 + (x & 3);
        const u16* ps = (w == 0 ? ka->logA : w == 1 ? ka->Aim : w == 2 ? ka->Cre : ka->Cim) + f * 2304 + min((int)(threadIdx.x & 63), 35) * 64;
        if (ix < 25 || !(FLG(ka->prm[5]) & 4))
        asm volatile("s_mov_b32 m0, %4\n\ts_nop 0\n\tglobal_load_lds_dword %0, off\n\tglobal_load_lds_dword %1, off\n\tglobal_load_lds_dword %2, off\n\tglobal_load_lds_dword %3, off"
                     :: "v"(xs), "v"(xs + 32768), "v"(xs + 65536 - w * 8192), "v"(ps), "s"(163072) : "memory");
        if (ix < 25) for (int i = 0; i < ((FLG(ka->prm[5]) >> 5) & 7); ++i) __builtin_amdgcn_s_sleep(36);
    }
    }
    asm volatile("" : "+s"(ka));
    if (ix >= 24) {
        unsigned co = (w * 64 + lane_id()) * 128 + (x < 4 ? -8192 : 0);
        asm volatile("s_getpc_b64 s[44:45]\ncp2_%=:\n\ts_mov_b32 m0, 163072\n\tv_min_u32 %0, code_end_marker-cp2_%=, %0\n\tglobal_load_lds_dword %0, s[44:45]\n\t"
                     "v_add_u32 %0, 0x8000, %0\n\tv_min_u32 %0, code_end_marker-cp2_%=, %0\n\tglobal_load_lds_dword %0, s[44:45]" : "+v"(co) :: "s44", "s45", "memory");
    }
    MainP p;
    p.prm[5] = ka->prm[5]; p.prm[7] = ka->prm[7];
    p.U = ka->U; p.ws = ka->ws; p.logA = ka->logA; p.Aim = ka->Aim; p.Cre = ka->Cre; p.Cim = ka->Cim; p.logdt = ka->logdt; p.Dskip = ka->Dskip; p.out = ka->out;
    if (x >= 4 && ix >= 24) {
        const u32x4 sent = {0xffffffffu, 0xffffffffu, 0xffffffffu, 0xffffffffu};
        const int tid = w * 64 + lane_id(), h = (ix - 24) * 4 + (x & 3);
        task_ut(p, smem, h, w, lane_id(), [&] {
#pragma unroll 1
            for (int sg = NG1; sg < 64; ++sg) {
                const unsigned ok = G_D_KM + h * G_HS4 + skew(sg, h) * 4096 + tid * 16;
                const unsigned op = tid < 128 ? G_D_PX + (sg * 32 + h) * 2048 + tid * 16 : G_D_PM + sg * 16384 + ((tid >> 4) & 1) * 8192 + h * 256 + (tid & 15) * 16;
                asm volatile("global_store_dwordx4 %0, %2, %3 sc1\n\tglobal_store_dwordx4 %1, %2, %3 sc1\n\ts_nop 1" :: "v"(ok), "v"(op), "v"(sent), "s"(p.ws) : "memory");
            }
        });
    }
    {
        const int nq = x < 4 ? ((FLH(p.prm[7]) & 2) ? 0 : 1000) : ix < 8 ? (FLG(p.prm[5]) >> 8) & 3 : ix < 16 ? (FLG(p.prm[5]) >> 10) & 3 : (ix & 2) && ((31 - ix) >> 2) < NAG ? 0 : ix < 24 ? (ix < 16 + ((FLH(p.prm[7]) >> 10) & 15) ? (FLG(p.prm[5]) >> 12) & 3 : 0) : (FLG(p.prm[5]) >> 14) & 3;
        if (b == 239 && w == 0) watch_role(p, lane_id());
        else if (nq || b == 239) {
            for (int i = 0; i < (int)(((ix & 7) * 4 + w) * ((FLG(p.prm[5]) >> 23) & 7)); ++i) __builtin_amdgcn_s_sleep(3);
            s4d_wave(p, smem, w, lane_id(), nq == 3 || b == 239 ? 1000 : nq);
        }
        if (x < 4) return;
        __syncthreads();
    }
    if ((ix & 18) == 18 && ((31 - ix) >> 2) < NAG) { attn_role(p, ((31 - ix) >> 2) + (ix < 24 ? NA : 0), (x & 3) * 8 + (ix & 1) * 4 + w, w, lane_id());
        if (b == 255) {
            wait_word(p, p.ws + G_WS_CTL + G_C_ARR, 255, 50u);
            if (w == 0 && lane_id() == 0) __hip_atomic_fetch_add((gu32*)(p.ws + G_WS_EP), 1u, RLX_AGENT);
        }
        __syncthreads();
    }
    p.prm[0] = near_mask(p.ws, lane_id());
    worker_body(p, smem, w, lane_id());
}

extern "C" __global__ void code_end_marker() {}

static constexpr int g_prm[8] = {PRM_DEFAULT};

extern "C" void kernel_launch(void* const* d_in, const int* in_sizes, int n_in, void* d_out, int out_size,
                              void* d_ws, size_t ws_size, hipStream_t stream) {
    char* ws = (char*)(((uintptr_t)d_ws + 0x1FFFFF) & ~(uintptr_t)0x1FFFFF);
    MainP p;
    for (int i = 0; i < 8; ++i) p.prm[i] = g_prm[i];
    p.mem0 = (const u16*)d_in[1];
    for (int i = 0; i < 4; ++i) p.W[i] = (const u16*)d_in[2 + i];
    p.X = (const u16*)d_in[0]; p.U = (const u16*)d_in[7]; p.ws = ws;
    p.logA = (const u16*)d_in[8]; p.Aim = (const u16*)d_in[9]; p.Cre = (const u16*)d_in[10]; p.Cim = (const u16*)d_in[11];
    p.logdt = (const u16*)d_in[12]; p.Dskip = (const u16*)d_in[13]; p.out = (u16*)d_out;
    main_kernel<<<256, 256, 0, stream>>>(p);
}
```

```cpp
#include <hip/hip_runtime.h>
#include <hip/hip_bf16.h>
#include <stdint.h>
#include <stddef.h>

typedef __attribute__((ext_vector_type(8))) __bf16 bf16x8;
typedef __attribute__((ext_vector_type(4))) short s16x4;
typedef __attribute__((ext_vector_type(4))) float f32x4;
typedef __attribute__((ext_vector_type(4))) unsigned u32x4;
typedef __attribute__((ext_vector_type(2))) unsigned u32x2;
typedef __attribute__((ext_vector_type(2))) float f32x2;
typedef __attribute__((ext_vector_type(16))) unsigned u32x16;
typedef unsigned short u16;
typedef unsigned long long u64;

#define GAS __attribute__((address_space(1)))
typedef GAS unsigned gu32;
#define RLX_AGENT __ATOMIC_RELAXED, __HIP_MEMORY_SCOPE_AGENT

static constexpr int DM = 576, NH = 32, DH = 128, MM = 8, SEG = 32, NSEG = 64, SL = 2048;
static constexpr int HD = NH * DH;
static constexpr int OUTW = 2 * DM;
static constexpr int NST = 128;

__device__ __forceinline__ float bf2f(u16 h) { return __uint_as_float(((unsigned)h) << 16); }
typedef __attribute__((ext_vector_type(2))) __bf16 bf16x2_t;
__device__ __forceinline__ unsigned pk2bf(float a, float b) {
    bf16x2_t v; v[0] = (__bf16)a; v[1] = (__bf16)b;
    return __builtin_bit_cast(unsigned, v);
}
__device__ __forceinline__ float bfr(float f) { return __uint_as_float(pk2bf(f, f) << 16); }
__device__ __forceinline__ float lo2f(unsigned u) { return __uint_as_float(u << 16); }
__device__ __forceinline__ float hi2f(unsigned u) { return __uint_as_float(u & 0xffff0000u); }
__device__ __forceinline__ float rowmax4(float x) {
    auto a = __builtin_amdgcn_permlane16_swap(__float_as_uint(x), __float_as_uint(x), false, false);
    x = fmaxf(__uint_as_float(a[0]), __uint_as_float(a[1]));
    auto b = __builtin_amdgcn_permlane32_swap(__float_as_uint(x), __float_as_uint(x), false, false);
    return fmaxf(__uint_as_float(b[0]), __uint_as_float(b[1]));
}
__device__ __forceinline__ float rowsum4(float x) {
    auto a = __builtin_amdgcn_permlane16_swap(__float_as_uint(x), __float_as_uint(x), false, false);
    x = __uint_as_float(a[0]) + __uint_as_float(a[1]);
    auto b = __builtin_amdgcn_permlane32_swap(__float_as_uint(x), __float_as_uint(x), false, false);
    return __uint_as_float(b[0]) + __uint_as_float(b[1]);
}

struct MainP {
    int prm[8];
    const u16* mem0; const u16* W[4]; const u16* X; const u16* U;
    char* ws;
    const u16* logA; const u16* Aim; const u16* Cre; const u16* Cim; const u16* logdt; const u16* Dskip;
    u16* out;
};
static constexpr int LDS_TOTAL = 163840;
static constexpr unsigned kWaitCap = 1u << 18;
#define VMC0() asm volatile("s_waitcnt vmcnt(0)" ::: "memory")
#define PRM_DEFAULT 1050, 600, 120, 675, 760, 1300656331, 3145992, 3779
#define ASM_QK \
    "s_load_dwordx2 s[42:43], %3, 40\n\t" \
    "s_load_dwordx2 s[44:45], %3, 48\n\t" \
    "s_load_dwordx2 s[36:37], %3, 0x58\n\t" \
    "s_load_dwordx2 s[50:51], %3, 0x48\n\t" \
    "s_load_dwordx8 s[84:91], %3, 0\n\t" \
    "s_mov_b32 s66, %4\n\t" \
    "s_mov_b32 s67, %5\n\t" \
    "s_mov_b32 s68, 262144\n\t" \
    "s_mov_b32 s72, 0\n\t" \
    "s_mov_b32 s83, -1\n\t" \
    "s_mov_b32 s65, 0\n\t" \
    "s_lshl_b32 s82, s66, 10\n\t" \
    "s_waitcnt lgkmcnt(0)\n\t" \
    "s_add_u32 s46, s42, 2097152\n\t" \
    "s_addc_u32 s47, s43, 0\n\t" \
    "s_add_u32 m0, s82, 65536\n\t" \
    "s_nop 0\n\t" \
    "global_load_lds_dwordx4 v200, s[46:47]\n\t" \
    "s_add_u32 s46, s42, 2228224\n\t" \
    "s_addc_u32 s47, s43, 0\n\t" \
    "s_add_u32 m0, s82, 69632\n\t" \
    "s_nop 0\n\t" \
    "global_load_lds_dwordx4 v200, s[46:47]\n\t" \
    "s_add_u32 s46, s42, 2359296\n\t" \
    "s_addc_u32 s47, s43, 0\n\t" \
    "s_add_u32 m0, s82, 73728\n\t" \
    "s_nop 0\n\t" \
    "global_load_lds_dwordx4 v200, s[46:47]\n\t" \
    "s_add_u32 s46, s42, 2490368\n\t" \
    "s_addc_u32 s47, s43, 0\n\t" \
    "s_add_u32 m0, s82, 77824\n\t" \
    "s_nop 0\n\t" \
    "global_load_lds_dwordx4 v200, s[46:47]\n\t" \
    "s_add_u32 s46, s42, 2621440\n\t" \
    "s_addc_u32 s47, s43, 0\n\t" \
    "s_add_u32 m0, s82, 81920\n\t" \
    "s_nop 0\n\t" \
    "global_load_lds_dwordx4 v200, s[46:47]\n\t" \
    "s_add_u32 s46, s42, 2752512\n\t" \
    "s_addc_u32 s47, s43, 0\n\t" \
    "s_add_u32 m0, s82, 86016\n\t" \
    "s_nop 0\n\t" \
    "global_load_lds_dwordx4 v200, s[46:47]\n\t" \
    "s_add_u32 s46, s42, 2883584\n\t" \
    "s_addc_u32 s47, s43, 0\n\t" \
    "s_add_u32 m0, s82, 90112\n\t" \
    "s_nop 0\n\t" \
    "global_load_lds_dwordx4 v200, s[46:47]\n\t" \
    "s_add_u32 s46, s42, 3014656\n\t" \
    "s_addc_u32 s47, s43, 0\n\t" \
    "s_add_u32 m0, s82, 94208\n\t" \
    "s_nop 0\n\t" \
    "global_load_lds_dwordx4 v200, s[46:47]\n\t" \
    "s_add_u32 s46, s42, 3145728\n\t" \
    "s_addc_u32 s47, s43, 0\n\t" \
    "s_add_u32 m0, s82, 98304\n\t" \
    "s_nop 0\n\t" \
    "global_load_lds_dwordx4 v200, s[46:47]\n\t" \
    "s_add_u32 s46, s42, 3276800\n\t" \
    "s_addc_u32 s47, s43, 0\n\t" \
    "s_add_u32 m0, s82, 102400\n\t" \
    "s_nop 0\n\t" \
    "global_load_lds_dwordx4 v200, s[46:47]\n\t" \
    "s_add_u32 s46, s42, 3407872\n\t" \
    "s_addc_u32 s47, s43, 0\n\t" \
    "s_add_u32 m0, s82, 106496\n\t" \
    "s_nop 0\n\t" \
    "global_load_lds_dwordx4 v200, s[46:47]\n\t" \
    "s_add_u32 s46, s42, 3538944\n\t" \
    "s_addc_u32 s47, s43, 0\n\t" \
    "s_add_u32 m0, s82, 110592\n\t" \
    "s_nop 0\n\t" \
    "global_load_lds_dwordx4 v200, s[46:47]\n\t" \
    "s_add_u32 s46, s42, 3670016\n\t" \
    "s_addc_u32 s47, s43, 0\n\t" \
    "s_add_u32 m0, s82, 114688\n\t" \
    "s_nop 0\n\t" \
    "global_load_lds_dwordx4 v200, s[46:47]\n\t" \
    "s_add_u32 s46, s42, 3801088\n\t" \
    "s_addc_u32 s47, s43, 0\n\t" \
    "s_add_u32 m0, s82, 118784\n\t" \
    "s_nop 0\n\t" \
    "global_load_lds_dwordx4 v200, s[46:47]\n\t" \
    "s_add_u32 s46, s42, 3932160\n\t" \
    "s_addc_u32 s47, s43, 0\n\t" \
    "s_add_u32 m0, s82, 122880\n\t" \
    "s_nop 0\n\t" \
    "global_load_lds_dwordx4 v200, s[46:47]\n\t" \
    "s_add_u32 s46, s42, 4063232\n\t" \
    "s_addc_u32 s47, s43, 0\n\t" \
    "s_add_u32 m0, s82, 126976\n\t" \
    "s_nop 0\n\t" \
    "global_load_lds_dwordx4 v200, s[46:47]\n\t" \
    "s_add_u32 s46, s42, 4194304\n\t" \
    "s_addc_u32 s47, s43, 0\n\t" \
    "s_add_u32 m0, s82, 131072\n\t" \
    "s_nop 0\n\t" \
    "global_load_lds_dwordx4 v200, s[46:47]\n\t" \
    "s_add_u32 s46, s42, 4325376\n\t" \
    "s_addc_u32 s47, s43, 0\n\t" \
    "s_add_u32 m0, s82, 135168\n\t" \
    "s_nop 0\n\t" \
    "global_load_lds_dwordx4 v200, s[46:47]\n\t" \
    "s_add_u32 s46, s42, 4456448\n\t" \
    "s_addc_u32 s47, s43, 0\n\t" \
    "s_add_u32 m0, s82, 139264\n\t" \
    "s_nop 0\n\t" \
    "global_load_lds_dwordx4 v200, s[46:47]\n\t" \
    "s_add_u32 s46, s42, 4587520\n\t" \
    "s_addc_u32 s47, s43, 0\n\t" \
    "s_add_u32 m0, s82, 143360\n\t" \
    "s_nop 0\n\t" \
    "global_load_lds_dwordx4 v200, s[46:47]\n\t" \
    "s_mov_b32 s77, 3019898880\n\t" \
    "s_mov_b32 s78, 3054198966\n\t" \
    "s_waitcnt lgkmcnt(0)\n\t" \
    "s_mov_b32 s70, 116\n\t" \
    "qk_E1_%=:\n\t" \
    "s_memtime s[56:57]\n\t" \
    "s_memrealtime s[2:3]\n\t" \
    "s_nop 0\n\t" \
    "s_memrealtime s[4:5]\n\t" \
    "s_nop 0\n\t" \
    "s_memrealtime s[6:7]\n\t" \
    "s_nop 0\n\t" \
    "s_memrealtime s[8:9]\n\t" \
    "s_nop 0\n\t" \
    "s_memrealtime s[10:11]\n\t" \
    "s_nop 0\n\t" \
    "s_memrealtime s[12:13]\n\t" \
    "s_nop 0\n\t" \
    "s_memrealtime s[14:15]\n\t" \
    "s_nop 0\n\t" \
    "s_memrealtime s[16:17]\n\t" \
    "s_nop 0\n\t" \
    "s_memrealtime s[18:19]\n\t" \
    "s_nop 0\n\t" \
    "s_memrealtime s[20:21]\n\t" \
    "s_nop 0\n\t" \
    "s_memrealtime s[22:23]\n\t" \
    "s_nop 0\n\t" \
    "s_memrealtime s[24:25]\n\t" \
    "s_nop 0\n\t" \
    "s_memrealtime s[26:27]\n\t" \
    "s_nop 0\n\t" \
    "s_memrealtime s[28:29]\n\t" \
    "s_memtime s[54:55]\n\t" \
    "s_add_u32 s70, s70, 8\n\t" \
    "s_waitcnt lgkmcnt(0)\n\t" \
    "s_sub_u32 s69, s54, s56\n\t" \
    "s_cmp_gt_u32 s69, s70\n\t" \
    "s_cbranch_scc1 qk_E1_%=\n\t" \
    "s_add_u32 s2, s2, s4\n\t" \
    "s_add_u32 s6, s6, s8\n\t" \
    "s_add_u32 s10, s10, s12\n\t" \
    "s_add_u32 s14, s14, s16\n\t" \
    "s_add_u32 s18, s18, s20\n\t" \
    "s_add_u32 s22, s22, s24\n\t" \
    "s_add_u32 s2, s2, s6\n\t" \
    "s_add_u32 s10, s10, s14\n\t" \
    "s_add_u32 s18, s18, s22\n\t" \
    "s_add_u32 s2, s2, s26\n\t" \
    "s_add_u32 s10, s10, s18\n\t" \
    "s_add_u32 s2, s2, s10\n\t" \
    "s_mul_i32 s70, s28, 13\n\t" \
    "s_sub_u32 s70, s70, s2\n\t" \
    "s_min_u32 s70, s70, 52\n\t" \
    "s_mul_i32 s69, s69, s70\n\t" \
    "s_mul_hi_u32 s69, s69, 76695844\n\t" \
    "s_mov_b32 s75, s73\n\t" \
    "s_mov_b32 s76, s74\n\t" \
    "s_add_u32 s73, s56, s69\n\t" \
    "s_lshl_b32 s74, s28, 4\n\t" \
    "s_waitcnt vmcnt(32) lgkmcnt(0)\n\t" \
    "s_barrier\n\t" \
    "ds_read_b64_tr_b16 a[0:1], v201 offset:0\n\t" \
    "ds_read_b64_tr_b16 a[2:3], v201 offset:1024\n\t" \
    "ds_read_b64_tr_b16 a[4:5], v201 offset:8192\n\t" \
    "ds_read_b64_tr_b16 a[6:7], v201 offset:9216\n\t" \
    "ds_read_b64_tr_b16 a[72:73], v202 offset:0\n\t" \
    "ds_read_b64_tr_b16 a[74:75], v202 offset:1024\n\t" \
    "ds_read_b64_tr_b16 a[76:77], v202 offset:8192\n\t" \
    "ds_read_b64_tr_b16 a[78:79], v202 offset:9216\n\t" \
    "s_waitcnt vmcnt(28) lgkmcnt(0)\n\t" \
    "s_barrier\n\t" \
    "ds_read_b64_tr_b16 a[8:9], v201 offset:16384\n\t" \
    "ds_read_b64_tr_b16 a[10:11], v201 offset:17408\n\t" \
    "ds_read_b64_tr_b16 a[12:13], v201 offset:24576\n\t" \
    "ds_read_b64_tr_b16 a[14:15], v201 offset:25600\n\t" \
    "ds_read_b64_tr_b16 a[80:81], v202 offset:16384\n\t" \
    "ds_read_b64_tr_b16 a[82:83], v202 offset:17408\n\t" \
    "ds_read_b64_tr_b16 a[84:85], v202 offset:24576\n\t" \
    "ds_read_b64_tr_b16 a[86:87], v202 offset:25600\n\t" \
    "s_add_u32 s46, s44, 0\n\t" \
    "s_addc_u32 s47, s45, 0\n\t" \
    "s_add_u32 m0, s82, 0\n\t" \
    "s_nop 0\n\t" \
    "global_load_lds_dwordx4 v200, s[46:47]\n\t" \
    "s_add_u32 s46, s44, 131072\n\t" \
    "s_addc_u32 s47, s45, 0\n\t" \
    "s_add_u32 m0, s82, 4096\n\t" \
    "s_nop 0\n\t" \
    "global_load_lds_dwordx4 v200, s[46:47]\n\t" \
    "s_add_u32 s46, s44, 262144\n\t" \
    "s_addc_u32 s47, s45, 0\n\t" \
    "s_add_u32 m0, s82, 8192\n\t" \
    "s_nop 0\n\t" \
    "global_load_lds_dwordx4 v200, s[46:47]\n\t" \
    "s_add_u32 s46, s44, 393216\n\t" \
    "s_addc_u32 s47, s45, 0\n\t" \
    "s_add_u32 m0, s82, 12288\n\t" \
    "s_nop 0\n\t" \
    "global_load_lds_dwordx4 v200, s[46:47]\n\t" \
    "s_waitcnt vmcnt(28) lgkmcnt(0)\n\t" \
    "s_barrier\n\t" \
    "ds_read_b64_tr_b16 a[16:17], v201 offset:32768\n\t" \
    "ds_read_b64_tr_b16 a[18:19], v201 offset:33792\n\t" \
    "ds_read_b64_tr_b16 a[20:21], v201 offset:40960\n\t" \
    "ds_read_b64_tr_b16 a[22:23], v201 offset:41984\n\t" \
    "ds_read_b64_tr_b16 a[88:89], v202 offset:32768\n\t" \
    "ds_read_b64_tr_b16 a[90:91], v202 offset:33792\n\t" \
    "ds_read_b64_tr_b16 a[92:93], v202 offset:40960\n\t" \
    "ds_read_b64_tr_b16 a[94:95], v202 offset:41984\n\t" \
    "s_add_u32 s46, s44, 524288\n\t" \
    "s_addc_u32 s47, s45, 0\n\t" \
    "s_add_u32 m0, s82, 16384\n\t" \
    "s_nop 0\n\t" \
    "global_load_lds_dwordx4 v200, s[46:47]\n\t" \
    "s_add_u32 s46, s44, 655360\n\t" \
    "s_addc_u32 s47, s45, 0\n\t" \
    "s_add_u32 m0, s82, 20480\n\t" \
    "s_nop 0\n\t" \
    "global_load_lds_dwordx4 v200, s[46:47]\n\t" \
    "s_add_u32 s46, s44, 786432\n\t" \
    "s_addc_u32 s47, s45, 0\n\t" \
    "s_add_u32 m0, s82, 24576\n\t" \
    "s_nop 0\n\t" \
    "global_load_lds_dwordx4 v200, s[46:47]\n\t" \
    "s_add_u32 s46, s44, 917504\n\t" \
    "s_addc_u32 s47, s45, 0\n\t" \
    "s_add_u32 m0, s82, 28672\n\t" \
    "s_nop 0\n\t" \
    "global_load_lds_dwordx4 v200, s[46:47]\n\t" \
    "s_waitcnt vmcnt(28) lgkmcnt(0)\n\t" \
    "s_barrier\n\t" \
    "ds_read_b64_tr_b16 a[24:25], v201 offset:49152\n\t" \
    "ds_read_b64_tr_b16 a[26:27], v201 offset:50176\n\t" \
    "ds_read_b64_tr_b16 a[28:29], v201 offset:57344\n\t" \
    "ds_read_b64_tr_b16 a[30:31], v201 offset:58368\n\t" \
    "ds_read_b64_tr_b16 a[96:97], v202 offset:49152\n\t" \
    "ds_read_b64_tr_b16 a[98:99], v202 offset:50176\n\t" \
    "ds_read_b64_tr_b16 a[100:101], v202 offset:57344\n\t" \
    "ds_read_b64_tr_b16 a[102:103], v202 offset:58368\n\t" \
    "s_add_u32 s46, s44, 1048576\n\t" \
    "s_addc_u32 s47, s45, 0\n\t" \
    "s_add_u32 m0, s82, 32768\n\t" \
    "s_nop 0\n\t" \
    "global_load_lds_dwordx4 v200, s[46:47]\n\t" \
    "s_add_u32 s46, s44, 1179648\n\t" \
    "s_addc_u32 s47, s45, 0\n\t" \
    "s_add_u32 m0, s82, 36864\n\t" \
    "s_nop 0\n\t" \
    "global_load_lds_dwordx4 v200, s[46:47]\n\t" \
    "s_add_u32 s46, s44, 1310720\n\t" \
    "s_addc_u32 s47, s45, 0\n\t" \
    "s_add_u32 m0, s82, 40960\n\t" \
    "s_nop 0\n\t" \
    "global_load_lds_dwordx4 v200, s[46:47]\n\t" \
    "s_add_u32 s46, s44, 1441792\n\t" \
    "s_addc_u32 s47, s45, 0\n\t" \
    "s_add_u32 m0, s82, 45056\n\t" \
    "s_nop 0\n\t" \
    "global_load_lds_dwordx4 v200, s[46:47]\n\t" \
    "s_waitcnt vmcnt(28) lgkmcnt(0)\n\t" \
    "s_barrier\n\t" \
    "v_add_u32 v201, 0x10000, v201\n\t" \
    "v_add_u32 v202, 0x10000, v202\n\t" \
    "ds_read_b64_tr_b16 a[32:33], v201 offset:0\n\t" \
    "ds_read_b64_tr_b16 a[34:35], v201 offset:1024\n\t" \
    "ds_read_b64_tr_b16 a[36:37], v201 offset:8192\n\t" \
    "ds_read_b64_tr_b16 a[38:39], v201 offset:9216\n\t" \
    "ds_read_b64_tr_b16 a[104:105], v202 offset:0\n\t" \
    "ds_read_b64_tr_b16 a[106:107], v202 offset:1024\n\t" \
    "ds_read_b64_tr_b16 a[108:109], v202 offset:8192\n\t" \
    "ds_read_b64_tr_b16 a[110:111], v202 offset:9216\n\t" \
    "s_add_u32 s46, s44, 1572864\n\t" \
    "s_addc_u32 s47, s45, 0\n\t" \
    "s_add_u32 m0, s82, 49152\n\t" \
    "s_nop 0\n\t" \
    "global_load_lds_dwordx4 v200, s[46:47]\n\t" \
    "s_add_u32 s46, s44, 1703936\n\t" \
    "s_addc_u32 s47, s45, 0\n\t" \
    "s_add_u32 m0, s82, 53248\n\t" \
    "s_nop 0\n\t" \
    "global_load_lds_dwordx4 v200, s[46:47]\n\t" \
    "s_add_u32 s46, s44, 1835008\n\t" \
    "s_addc_u32 s47, s45, 0\n\t" \
    "s_add_u32 m0, s82, 57344\n\t" \
    "s_nop 0\n\t" \
    "global_load_lds_dwordx4 v200, s[46:47]\n\t" \
    "s_add_u32 s46, s44, 1966080\n\t" \
    "s_addc_u32 s47, s45, 0\n\t" \
    "s_add_u32 m0, s82, 61440\n\t" \
    "s_nop 0\n\t" \
    "global_load_lds_dwordx4 v200, s[46:47]\n\t" \
    "s_waitcnt vmcnt(28) lgkmcnt(0)\n\t" \
    "s_barrier\n\t" \
    "ds_read_b64_tr_b16 a[40:41], v201 offset:16384\n\t" \
    "ds_read_b64_tr_b16 a[42:43], v201 offset:17408\n\t" \
    "ds_read_b64_tr_b16 a[44:45], v201 offset:24576\n\t" \
    "ds_read_b64_tr_b16 a[46:47], v201 offset:25600\n\t" \
    "ds_read_b64_tr_b16 a[112:113], v202 offset:16384\n\t" \
    "ds_read_b64_tr_b16 a[114:115], v202 offset:17408\n\t" \
    "ds_read_b64_tr_b16 a[116:117], v202 offset:24576\n\t" \
    "ds_read_b64_tr_b16 a[118:119], v202 offset:25600\n\t" \
    "s_add_u32 s46, s44, 2097152\n\t" \
    "s_addc_u32 s47, s45, 0\n\t" \
    "s_add_u32 m0, s82, 65536\n\t" \
    "s_nop 0\n\t" \
    "global_load_lds_dwordx4 v200, s[46:47]\n\t" \
    "s_add_u32 s46, s44, 2228224\n\t" \
    "s_addc_u32 s47, s45, 0\n\t" \
    "s_add_u32 m0, s82, 69632\n\t" \
    "s_nop 0\n\t" \
    "global_load_lds_dwordx4 v200, s[46:47]\n\t" \
    "s_add_u32 s46, s44, 2359296\n\t" \
    "s_addc_u32 s47, s45, 0\n\t" \
    "s_add_u32 m0, s82, 73728\n\t" \
    "s_nop 0\n\t" \
    "global_load_lds_dwordx4 v200, s[46:47]\n\t" \
    "s_add_u32 s46, s44, 2490368\n\t" \
    "s_addc_u32 s47, s45, 0\n\t" \
    "s_add_u32 m0, s82, 77824\n\t" \
    "s_nop 0\n\t" \
    "global_load_lds_dwordx4 v200, s[46:47]\n\t" \
    "s_waitcnt vmcnt(28) lgkmcnt(0)\n\t" \
    "s_barrier\n\t" \
    "ds_read_b64_tr_b16 a[48:49], v201 offset:32768\n\t" \
    "ds_read_b64_tr_b16 a[50:51], v201 offset:33792\n\t" \
    "ds_read_b64_tr_b16 a[52:53], v201 offset:40960\n\t" \
    "ds_read_b64_tr_b16 a[54:55], v201 offset:41984\n\t" \
    "ds_read_b64_tr_b16 a[120:121], v202 offset:32768\n\t" \
    "ds_read_b64_tr_b16 a[122:123], v202 offset:33792\n\t" \
    "ds_read_b64_tr_b16 a[124:125], v202 offset:40960\n\t" \
    "ds_read_b64_tr_b16 a[126:127], v202 offset:41984\n\t" \
    "s_add_u32 s46, s44, 2621440\n\t" \
    "s_addc_u32 s47, s45, 0\n\t" \
    "s_add_u32 m0, s82, 81920\n\t" \
    "s_nop 0\n\t" \
    "global_load_lds_dwordx4 v200, s[46:47]\n\t" \
    "s_add_u32 s46, s44, 2752512\n\t" \
    "s_addc_u32 s47, s45, 0\n\t" \
    "s_add_u32 m0, s82, 86016\n\t" \
    "s_nop 0\n\t" \
    "global_load_lds_dwordx4 v200, s[46:47]\n\t" \
    "s_add_u32 s46, s44, 2883584\n\t" \
    "s_addc_u32 s47, s45, 0\n\t" \
    "s_add_u32 m0, s82, 90112\n\t" \
    "s_nop 0\n\t" \
    "global_load_lds_dwordx4 v200, s[46:47]\n\t" \
    "s_add_u32 s46, s44, 3014656\n\t" \
    "s_addc_u32 s47, s45, 0\n\t" \
    "s_add_u32 m0, s82, 94208\n\t" \
    "s_nop 0\n\t" \
    "global_load_lds_dwordx4 v200, s[46:47]\n\t" \
    "s_waitcnt vmcnt(28) lgkmcnt(0)\n\t" \
    "s_barrier\n\t" \
    "ds_read_b64_tr_b16 a[56:57], v201 offset:49152\n\t" \
    "ds_read_b64_tr_b16 a[58:59], v201 offset:50176\n\t" \
    "ds_read_b64_tr_b16 a[60:61], v201 offset:57344\n\t" \
    "ds_read_b64_tr_b16 a[62:63], v201 offset:58368\n\t" \
    "ds_read_b64_tr_b16 a[128:129], v202 offset:49152\n\t" \
    "ds_read_b64_tr_b16 a[130:131], v202 offset:50176\n\t" \
    "ds_read_b64_tr_b16 a[132:133], v202 offset:57344\n\t" \
    "ds_read_b64_tr_b16 a[134:135], v202 offset:58368\n\t" \
    "s_add_u32 s46, s44, 3145728\n\t" \
    "s_addc_u32 s47, s45, 0\n\t" \
    "s_add_u32 m0, s82, 98304\n\t" \
    "s_nop 0\n\t" \
    "global_load_lds_dwordx4 v200, s[46:47]\n\t" \
    "s_add_u32 s46, s44, 3276800\n\t" \
    "s_addc_u32 s47, s45, 0\n\t" \
    "s_add_u32 m0, s82, 102400\n\t" \
    "s_nop 0\n\t" \
    "global_load_lds_dwordx4 v200, s[46:47]\n\t" \
    "s_add_u32 s46, s44, 3407872\n\t" \
    "s_addc_u32 s47, s45, 0\n\t" \
    "s_add_u32 m0, s82, 106496\n\t" \
    "s_nop 0\n\t" \
    "global_load_lds_dwordx4 v200, s[46:47]\n\t" \
    "s_add_u32 s46, s44, 3538944\n\t" \
    "s_addc_u32 s47, s45, 0\n\t" \
    "s_add_u32 m0, s82, 110592\n\t" \
    "s_nop 0\n\t" \
    "global_load_lds_dwordx4 v200, s[46:47]\n\t" \
    "v_mbcnt_lo_u32_b32 v160, -1, 0\n\t" \
    "v_mbcnt_hi_u32_b32 v160, -1, v160\n\t" \
    "v_lshlrev_b32 v160, 6, v160\n\t" \
    "v_add_u32 v160, 6225920, v160\n\t" \
    "v_mov_b32 v161, 6234112\n\t" \
    "global_load_dword v161, v161, s[36:37] sc1\n\t" \
    "global_load_dwordx2 v[162:163], v160, s[36:37] sc1\n\t" \
    "global_load_dword v164, v160, s[36:37] offset:2048 sc1\n\t" \
    "s_waitcnt vmcnt(31) lgkmcnt(0)\n\t" \
    "s_barrier\n\t" \
    "v_add_u32 v201, 0x10000, v201\n\t" \
    "v_add_u32 v202, 0x10000, v202\n\t" \
    "ds_read_b64_tr_b16 a[64:65], v201 offset:0\n\t" \
    "ds_read_b64_tr_b16 a[66:67], v201 offset:1024\n\t" \
    "ds_read_b64_tr_b16 a[68:69], v201 offset:8192\n\t" \
    "ds_read_b64_tr_b16 a[70:71], v201 offset:9216\n\t" \
    "ds_read_b64_tr_b16 a[136:137], v202 offset:0\n\t" \
    "ds_read_b64_tr_b16 a[138:139], v202 offset:1024\n\t" \
    "ds_read_b64_tr_b16 a[140:141], v202 offset:8192\n\t" \
    "ds_read_b64_tr_b16 a[142:143], v202 offset:9216\n\t" \
    "v_add_u32 v201, 0xfffe0000, v201\n\t" \
    "v_add_u32 v202, 0xfffe0000, v202\n\t" \
    "s_add_u32 s46, s44, 3670016\n\t" \
    "s_addc_u32 s47, s45, 0\n\t" \
    "s_add_u32 m0, s82, 114688\n\t" \
    "s_nop 0\n\t" \
    "global_load_lds_dwordx4 v200, s[46:47]\n\t" \
    "s_add_u32 s46, s44, 3801088\n\t" \
    "s_addc_u32 s47, s45, 0\n\t" \
    "s_add_u32 m0, s82, 118784\n\t" \
    "s_nop 0\n\t" \
    "global_load_lds_dwordx4 v200, s[46:47]\n\t" \
    "s_add_u32 s46, s44, 3932160\n\t" \
    "s_addc_u32 s47, s45, 0\n\t" \
    "s_add_u32 m0, s82, 122880\n\t" \
    "s_nop 0\n\t" \
    "global_load_lds_dwordx4 v200, s[46:47]\n\t" \
    "s_add_u32 s46, s44, 4063232\n\t" \
    "s_addc_u32 s47, s45, 0\n\t" \
    "s_add_u32 m0, s82, 126976\n\t" \
    "s_nop 0\n\t" \
    "global_load_lds_dwordx4 v200, s[46:47]\n\t" \
    "s_waitcnt lgkmcnt(0)\n\t" \
    "s_barrier\n\t" \
    "s_add_u32 s46, s44, 4194304\n\t" \
    "s_addc_u32 s47, s45, 0\n\t" \
    "s_add_u32 m0, s82, 131072\n\t" \
    "s_nop 0\n\t" \
    "global_load_lds_dwordx4 v200, s[46:47]\n\t" \
    "s_add_u32 s46, s44, 4325376\n\t" \
    "s_addc_u32 s47, s45, 0\n\t" \
    "s_add_u32 m0, s82, 135168\n\t" \
    "s_nop 0\n\t" \
    "global_load_lds_dwordx4 v200, s[46:47]\n\t" \
    "s_add_u32 s46, s44, 4456448\n\t" \
    "s_addc_u32 s47, s45, 0\n\t" \
    "s_add_u32 m0, s82, 139264\n\t" \
    "s_nop 0\n\t" \
    "global_load_lds_dwordx4 v200, s[46:47]\n\t" \
    "s_add_u32 s46, s44, 4587520\n\t" \
    "s_addc_u32 s47, s45, 0\n\t" \
    "s_add_u32 m0, s82, 143360\n\t" \
    "s_nop 0\n\t" \
    "global_load_lds_dwordx4 v200, s[46:47]\n\t" \
    "s_waitcnt vmcnt(35) lgkmcnt(0)\n\t" \
    "s_barrier\n\t" \
    "ds_read_b64_tr_b16 a[144:145], v201 offset:0\n\t" \
    "ds_read_b64_tr_b16 a[146:147], v201 offset:1024\n\t" \
    "ds_read_b64_tr_b16 a[148:149], v201 offset:8192\n\t" \
    "ds_read_b64_tr_b16 a[150:151], v201 offset:9216\n\t" \
    "ds_read_b64_tr_b16 a[216:217], v202 offset:0\n\t" \
    "ds_read_b64_tr_b16 a[218:219], v202 offset:1024\n\t" \
    "ds_read_b64_tr_b16 a[220:221], v202 offset:8192\n\t" \
    "ds_read_b64_tr_b16 a[222:223], v202 offset:9216\n\t" \
    "s_waitcnt vmcnt(31) lgkmcnt(0)\n\t" \
    "s_barrier\n\t" \
    "ds_read_b64_tr_b16 a[152:153], v201 offset:16384\n\t" \
    "ds_read_b64_tr_b16 a[154:155], v201 offset:17408\n\t" \
    "ds_read_b64_tr_b16 a[156:157], v201 offset:24576\n\t" \
    "ds_read_b64_tr_b16 a[158:159], v201 offset:25600\n\t" \
    "ds_read_b64_tr_b16 a[224:225], v202 offset:16384\n\t" \
    "ds_read_b64_tr_b16 a[226:227], v202 offset:17408\n\t" \
    "ds_read_b64_tr_b16 a[228:229], v202 offset:24576\n\t" \
    "ds_read_b64_tr_b16 a[230:231], v202 offset:25600\n\t" \
    "s_waitcnt vmcnt(27) lgkmcnt(0)\n\t" \
    "s_barrier\n\t" \
    "ds_read_b64_tr_b16 a[160:161], v201 offset:32768\n\t" \
    "ds_read_b64_tr_b16 a[162:163], v201 offset:33792\n\t" \
    "ds_read_b64_tr_b16 a[164:165], v201 offset:40960\n\t" \
    "ds_read_b64_tr_b16 a[166:167], v201 offset:41984\n\t" \
    "ds_read_b64_tr_b16 a[232:233], v202 offset:32768\n\t" \
    "ds_read_b64_tr_b16 a[234:235], v202 offset:33792\n\t" \
    "ds_read_b64_tr_b16 a[236:237], v202 offset:40960\n\t" \
    "ds_read_b64_tr_b16 a[238:239], v202 offset:41984\n\t" \
    "s_waitcnt vmcnt(23) lgkmcnt(0)\n\t" \
    "s_barrier\n\t" \
    "ds_read_b64_tr_b16 a[168:169], v201 offset:49152\n\t" \
    "ds_read_b64_tr_b16 a[170:171], v201 offset:50176\n\t" \
    "ds_read_b64_tr_b16 a[172:173], v201 offset:57344\n\t" \
    "ds_read_b64_tr_b16 a[174:175], v201 offset:58368\n\t" \
    "ds_read_b64_tr_b16 a[240:241], v202 offset:49152\n\t" \
    "ds_read_b64_tr_b16 a[242:243], v202 offset:50176\n\t" \
    "ds_read_b64_tr_b16 a[244:245], v202 offset:57344\n\t" \
    "ds_read_b64_tr_b16 a[246:247], v202 offset:58368\n\t" \
    "s_waitcnt vmcnt(19) lgkmcnt(0)\n\t" \
    "s_barrier\n\t" \
    "v_add_u32 v201, 0x10000, v201\n\t" \
    "v_add_u32 v202, 0x10000, v202\n\t" \
    "ds_read_b64_tr_b16 a[176:177], v201 offset:0\n\t" \
    "ds_read_b64_tr_b16 a[178:179], v201 offset:1024\n\t" \
    "ds_read_b64_tr_b16 a[180:181], v201 offset:8192\n\t" \
    "ds_read_b64_tr_b16 a[182:183], v201 offset:9216\n\t" \
    "ds_read_b64_tr_b16 a[248:249], v202 offset:0\n\t" \
    "ds_read_b64_tr_b16 a[250:251], v202 offset:1024\n\t" \
    "ds_read_b64_tr_b16 a[252:253], v202 offset:8192\n\t" \
    "ds_read_b64_tr_b16 a[254:255], v202 offset:9216\n\t" \
    "s_waitcnt vmcnt(15) lgkmcnt(0)\n\t" \
    "s_barrier\n\t" \
    "ds_read_b64_tr_b16 a[184:185], v201 offset:16384\n\t" \
    "ds_read_b64_tr_b16 a[186:187], v201 offset:17408\n\t" \
    "ds_read_b64_tr_b16 a[188:189], v201 offset:24576\n\t" \
    "ds_read_b64_tr_b16 a[190:191], v201 offset:25600\n\t" \
    "ds_read_b64_tr_b16 v[224:225], v202 offset:16384\n\t" \
    "ds_read_b64_tr_b16 v[226:227], v202 offset:17408\n\t" \
    "ds_read_b64_tr_b16 v[228:229], v202 offset:24576\n\t" \
    "ds_read_b64_tr_b16 v[230:231], v202 offset:25600\n\t" \
    "s_waitcnt vmcnt(11) lgkmcnt(0)\n\t" \
    "s_barrier\n\t" \
    "ds_read_b64_tr_b16 a[192:193], v201 offset:32768\n\t" \
    "ds_read_b64_tr_b16 a[194:195], v201 offset:33792\n\t" \
    "ds_read_b64_tr_b16 a[196:197], v201 offset:40960\n\t" \
    "ds_read_b64_tr_b16 a[198:199], v201 offset:41984\n\t" \
    "ds_read_b64_tr_b16 v[232:233], v202 offset:32768\n\t" \
    "ds_read_b64_tr_b16 v[234:235], v202 offset:33792\n\t" \
    "ds_read_b64_tr_b16 v[236:237], v202 offset:40960\n\t" \
    "ds_read_b64_tr_b16 v[238:239], v202 offset:41984\n\t" \
    "s_waitcnt vmcnt(8)\n\t" \
    "qk_C2_%=:\n\t" \
    "v_cmp_ne_u32 s[54:55], v162, v164\n\t" \
    "v_sub_u32 v162, v162, v161\n\t" \
    "v_cmp_ne_u32 vcc, 1, v162\n\t" \
    "s_nop 1\n\t" \
    "s_or_b64 vcc, vcc, s[54:55]\n\t" \
    "s_cbranch_scc0 qk_Cok3_%=\n\t" \
    "s_cmp_eq_u32 s68, 0\n\t" \
    "s_cbranch_scc1 qk_F5_%=\n\t" \
    "s_sub_u32 s68, s68, 1\n\t" \
    "s_sleep 6\n\t" \
    "s_branch qk_Cag4_%=\n\t" \
    "qk_F5_%=:\n\t" \
    "v_mov_b32 v164, 5767424\n\t" \
    "v_mov_b32 v165, 4\n\t" \
    "global_store_dword v164, v165, s[36:37] sc1\n\t" \
    "s_waitcnt vmcnt(0)\n\t" \
    "s_branch qk_Cok3_%=\n\t" \
    "qk_Cag4_%=:\n\t" \
    "global_load_dwordx2 v[162:163], v160, s[36:37] sc1\n\t" \
    "global_load_dword v164, v160, s[36:37] offset:2048 sc1\n\t" \
    "s_waitcnt vmcnt(0)\n\t" \
    "s_branch qk_C2_%=\n\t" \
    "qk_Cok3_%=:\n\t" \
    "s_cmp_lg_u32 s66, 0\n\t" \
    "s_cbranch_scc1 qk_Ca6_%=\n\t" \
    "v_mov_b32 v160, 5836800\n\t" \
    "s_mov_b64 exec, 1\n\t" \
    "global_atomic_add v160, v162, s[36:37] sc1\n\t" \
    "s_mov_b64 exec, -1\n\t" \
    "qk_Ca6_%=:\n\t" \
    "v_mov_b32_dpp v160, v163 quad_perm:[1,0,3,2] row_mask:0xf bank_mask:0xf\n\t" \
    "v_cmp_lt_u32 s[44:45], v160, v163\n\t" \
    "s_nop 1\n\t" \
    "s_bitcmp1_b32 s44, 0\n\t" \
    "s_cselect_b32 s62, 8192, 0\n\t" \
    "s_bitcmp1_b32 s44, 2\n\t" \
    "s_cselect_b32 s63, 8192, 0\n\t" \
    "s_or_b32 s63, s63, 0x200000\n\t" \
    "v_min_u32 v160, v160, v163\n\t" \
    "v_cmp_lt_u32 vcc, 660, v160\n\t" \
    "s_nop 1\n\t" \
    "s_cmp_lg_u64 vcc, 0\n\t" \
    "s_cselect_b32 s69, 1, 0\n\t" \
    "s_mul_i32 s70, s69, 160\n\t" \
    "s_add_u32 s86, s86, s70\n\t" \
    "s_mul_i32 s70, s69, 536\n\t" \
    "s_add_u32 s87, s87, s70\n\t" \
    "s_mul_i32 s70, s69, 256\n\t" \
    "s_add_u32 s88, s88, s70\n\t" \
    "global_load_dword v25, v24, s[36:37] sc1\n\t" \
    "s_waitcnt vmcnt(5) lgkmcnt(0)\n\t" \
    "s_barrier\n\t" \
    "ds_read_b64_tr_b16 a[200:201], v201 offset:49152\n\t" \
    "ds_read_b64_tr_b16 a[202:203], v201 offset:50176\n\t" \
    "ds_read_b64_tr_b16 a[204:205], v201 offset:57344\n\t" \
    "ds_read_b64_tr_b16 a[206:207], v201 offset:58368\n\t" \
    "ds_read_b64_tr_b16 v[240:241], v202 offset:49152\n\t" \
    "ds_read_b64_tr_b16 v[242:243], v202 offset:50176\n\t" \
    "ds_read_b64_tr_b16 v[244:245], v202 offset:57344\n\t" \
    "ds_read_b64_tr_b16 v[246:247], v202 offset:58368\n\t" \
    "s_waitcnt vmcnt(1) lgkmcnt(0)\n\t" \
    "s_barrier\n\t" \
    "v_add_u32 v201, 0x10000, v201\n\t" \
    "v_add_u32 v202, 0x10000, v202\n\t" \
    "ds_read_b64_tr_b16 a[208:209], v201 offset:0\n\t" \
    "ds_read_b64_tr_b16 a[210:211], v201 offset:1024\n\t" \
    "ds_read_b64_tr_b16 a[212:213], v201 offset:8192\n\t" \
    "ds_read_b64_tr_b16 a[214:215], v201 offset:9216\n\t" \
    "ds_read_b64_tr_b16 v[248:249], v202 offset:0\n\t" \
    "ds_read_b64_tr_b16 v[250:251], v202 offset:1024\n\t" \
    "ds_read_b64_tr_b16 v[252:253], v202 offset:8192\n\t" \
    "ds_read_b64_tr_b16 v[254:255], v202 offset:9216\n\t" \
    "v_add_u32 v201, 0xfffe0000, v201\n\t" \
    "v_add_u32 v202, 0xfffe0000, v202\n\t" \
    "s_waitcnt lgkmcnt(0)\n\t" \
    "s_barrier\n\t" \
    "s_mul_i32 s70, s67, 7\n\t" \
    "s_add_u32 s69, s65, 0\n\t" \
    "s_xor_b32 s69, s69, s70\n\t" \
    "s_and_b32 s69, s69, 63\n\t" \
    "s_lshl_b32 s69, s69, 13\n\t" \
    "s_mul_i32 s70, s67, 524288\n\t" \
    "s_add_u32 s69, s69, s70\n\t" \
    "s_add_u32 s69, s69, 23068672\n\t" \
    "s_add_u32 s38, s36, s69\n\t" \
    "s_addc_u32 s39, s37, 0\n\t" \
    "v_mov_b32 v26, 0x3db504f3\n\t" \
    "v_mov_b32 v28, 0x3fb8aa3b\n\t" \
    "v_mov_b32 v27, 0x3db504f3\n\t" \
    "v_mov_b32 v29, 0x3fb8aa3b\n\t" \
    "v_xor_b32 v180, 65536, v18\n\t" \
    "v_xor_b32 v181, 131072, v18\n\t" \
    "v_xor_b32 v182, 196608, v18\n\t" \
    "s_mov_b32 s48, 0xaaaaaaaa\n\t" \
    "s_mov_b32 s49, 0xaaaaaaaa\n\t" \
    "s_mov_b32 s58, 0x00ff00ff\n\t" \
    "s_mov_b32 s59, 0x00ff00ff\n\t" \
    "v_mov_b32 v31, -1\n\t" \
    "s_mov_b32 s91, -1\n\t" \
    "ds_write_b32 v12, v31 offset:0\n\t" \
    "ds_write_b32 v12, v31 offset:64\n\t" \
    "s_waitcnt vmcnt(0)\n\t" \
    "s_cmp_gt_u32 s66, 1\n\t" \
    "s_cbranch_scc1 qk_nk7_%=\n\t" \
    "v_readfirstlane_b32 s83, v25\n\t" \
    "s_nop 1\n\t" \
    "qk_WF8_%=:\n\t" \
    "s_cmp_ge_i32 s83, s65\n\t" \
    "s_cbranch_scc1 qk_WFok9_%=\n\t" \
    "global_load_dword v160, v24, s[36:37] sc1\n\t" \
    "s_waitcnt vmcnt(0)\n\t" \
    "v_readfirstlane_b32 s83, v160\n\t" \
    "s_nop 1\n\t" \
    "s_cmp_ge_i32 s83, s65\n\t" \
    "s_cbranch_scc1 qk_WFok9_%=\n\t" \
    "s_cmp_eq_u32 s68, 0\n\t" \
    "s_cbranch_scc1 qk_F10_%=\n\t" \
    "s_sub_u32 s68, s68, 1\n\t" \
    "s_sleep 2\n\t" \
    "s_branch qk_WF8_%=\n\t" \
    "qk_F10_%=:\n\t" \
    "v_mov_b32 v160, 5767424\n\t" \
    "v_mov_b32 v161, 8\n\t" \
    "global_store_dword v160, v161, s[36:37] sc1\n\t" \
    "s_waitcnt vmcnt(0)\n\t" \
    "s_branch qk_WFok9_%=\n\t" \
    "qk_WFok9_%=:\n\t" \
    "global_load_dwordx4 v[100:103], v0, s[38:39] offset:0 sc1\n\t" \
    "global_load_dwordx4 v[104:107], v0, s[38:39] offset:2048 sc1\n\t" \
    "global_load_dwordx4 v[108:111], v15, s[38:39] offset:0 sc1\n\t" \
    "global_load_dwordx4 v[112:115], v15, s[38:39] offset:2048 sc1\n\t" \
    "global_load_dwordx4 v[116:119], v0, s[38:39] offset:1024 sc1\n\t" \
    "global_load_dwordx4 v[120:123], v0, s[38:39] offset:3072 sc1\n\t" \
    "global_load_dwordx4 v[124:127], v15, s[38:39] offset:1024 sc1\n\t" \
    "global_load_dwordx4 v[128:131], v15, s[38:39] offset:3072 sc1\n\t" \
    "qk_nk7_%=:\n\t" \
    "v_mov_b32 v33, v21\n\t" \
    "v_readlane_b32 s69, v21, 63\n\t" \
    "s_nop 1\n\t" \
    "v_mov_b32 v21, s69\n\t" \
    "s_mov_b32 s79, s74\n\t" \
    "s_mov_b32 s80, s74\n\t" \
    "s_cmp_eq_u32 s66, 1\n\t" \
    "s_cbranch_scc1 qk_loop12_%=\n\t" \
    "s_cmp_eq_u32 s66, 2\n\t" \
    "s_cbranch_scc1 qk_loop13_%=\n\t" \
    "s_cmp_eq_u32 s66, 3\n\t" \
    "s_cbranch_scc1 qk_loop14_%=\n\t" \
    "qk_loop11_%=:\n\t" \
    "v_mov_b32 v72, 0\n\t" \
    "v_mov_b32 v73, 0\n\t" \
    "v_mov_b32 v74, 0\n\t" \
    "v_mov_b32 v75, 0\n\t" \
    "v_mov_b32 v76, 0\n\t" \
    "v_mov_b32 v77, 0\n\t" \
    "v_mov_b32 v78, 0\n\t" \
    "v_mov_b32 v79, 0\n\t" \
    "v_mov_b32 v80, 0\n\t" \
    "v_mov_b32 v81, 0\n\t" \
    "v_mov_b32 v82, 0\n\t" \
    "v_mov_b32 v83, 0\n\t" \
    "v_mov_b32 v84, 0\n\t" \
    "v_mov_b32 v85, 0\n\t" \
    "v_mov_b32 v86, 0\n\t" \
    "v_mov_b32 v87, 0\n\t" \
    "v_mov_b32 v88, 0\n\t" \
    "v_mov_b32 v89, 0\n\t" \
    "v_mov_b32 v90, 0\n\t" \
    "v_mov_b32 v91, 0\n\t" \
    "v_mov_b32 v92, 0\n\t" \
    "v_mov_b32 v93, 0\n\t" \
    "v_mov_b32 v94, 0\n\t" \
    "v_mov_b32 v95, 0\n\t" \
    "v_mov_b32 v96, 0\n\t" \
    "v_mov_b32 v97, 0\n\t" \
    "v_mov_b32 v98, 0\n\t" \
    "v_mov_b32 v99, 0\n\t" \
    "s_add_u32 s69, s65, 0\n\t" \
    "s_and_b32 s69, s69, 7\n\t" \
    "s_lshl_b32 s69, s69, 18\n\t" \
    "s_add_u32 s69, s69, s62\n\t" \
    "s_add_u32 s46, s36, s69\n\t" \
    "s_addc_u32 s47, s37, 0\n\t" \
    "s_sub_u32 s70, s79, s74\n\t" \
    "s_max_i32 s70, s70, 0\n\t" \
    "s_min_i32 s70, s70, 16000\n\t" \
    "s_lshl_b32 s70, s70, 1\n\t" \
    "s_mul_hi_u32 s70, s70, s77\n\t" \
    "s_add_u32 s70, s70, s73\n\t" \
    ".p2align 6\n\t" \
    "s_nop 0\n\t" \
    "s_nop 0\n\t" \
    "s_nop 0\n\t" \
    "s_nop 0\n\t" \
    "s_nop 0\n\t" \
    "s_nop 0\n\t" \
    "qk_go16_%=:\n\t" \
    "s_getpc_b64 s[30:31]\n\t" \
    "qk_Wp18_%=:\n\t" \
    "s_add_u32 s30, s30, qk_We19_%=-qk_Wp18_%=\n\t" \
    "s_addc_u32 s31, s31, 0\n\t" \
    "s_sub_u32 s70, s70, 96\n\t" \
    "qk_W17_%=:\n\t" \
    "s_memtime s[56:57]\n\t" \
    "s_waitcnt lgkmcnt(0)\n\t" \
    "s_sub_u32 s69, s70, s56\n\t" \
    "s_cmp_gt_i32 s69, 80\n\t" \
    "s_cbranch_scc1 qk_W17_%=\n\t" \
    "s_max_i32 s69, s69, 0\n\t" \
    "s_and_b32 s69, s69, -4\n\t" \
    "s_sub_u32 s30, s30, s69\n\t" \
    "s_subb_u32 s31, s31, 0\n\t" \
    "s_setpc_b64 s[30:31]\n\t" \
    "s_nop 0\n\t" \
    "s_nop 0\n\t" \
    "s_nop 0\n\t" \
    "s_nop 0\n\t" \
    "s_nop 0\n\t" \
    "s_nop 0\n\t" \
    "s_nop 0\n\t" \
    "s_nop 0\n\t" \
    "s_nop 0\n\t" \
    "s_nop 0\n\t" \
    "s_nop 0\n\t" \
    "s_nop 0\n\t" \
    "s_nop 0\n\t" \
    "s_nop 0\n\t" \
    "s_nop 0\n\t" \
    "s_nop 0\n\t" \
    "s_nop 0\n\t" \
    "s_nop 0\n\t" \
    "s_nop 0\n\t" \
    "s_nop 0\n\t" \
    "qk_We19_%=:\n\t" \
    "qk_G20_%=:\n\t" \
    "global_load_dwordx4 v[36:39], v1, s[46:47] sc1\n\t" \
    "global_load_dword v161, v2, s[46:47] sc1\n\t" \
    "global_load_dwordx4 v[40:43], v23, s[46:47] sc1\n\t" \
    "s_waitcnt vmcnt(2)\n\t" \
    "ds_write_b128 v3, v[36:39]\n\t" \
    "v_smfmac_f32_16x16x64_bf16 v[72:75], v[36:39], a[0:7], v10\n\t" \
    "s_waitcnt vmcnt(1)\n\t" \
    "ds_write_b32 v22, v161 offset:19456\n\t" \
    "v_smfmac_f32_16x16x64_bf16 v[76:79], v[36:39], a[72:79], v10\n\t" \
    "s_waitcnt vmcnt(0)\n\t" \
    "ds_write_b128 v3, v[40:43] offset:1024\n\t" \
    "v_max3_u32 v160, v36, v40, v161\n\t" \
    "v_cmp_eq_u32 vcc, -1, v160\n\t" \
    "v_smfmac_f32_16x16x64_bf16 v[80:83], v[36:39], a[144:151], v10\n\t" \
    "v_smfmac_f32_16x16x64_bf16 v[84:87], v[36:39], a[216:223], v10\n\t" \
    "v_smfmac_f32_16x16x64_bf16 v[72:75], v[40:43], a[8:15], v10\n\t" \
    "s_cbranch_vccnz qk_Gbad21_%=\n\t" \
    "qk_Gc22_%=:\n\t" \
    "s_waitcnt lgkmcnt(0)\n\t" \
    "s_barrier\n\t" \
    "ds_read_b128 v[48:51], v4\n\t" \
    "ds_read_b128 v[52:55], v5\n\t" \
    "v_smfmac_f32_16x16x64_bf16 v[76:79], v[40:43], a[80:87], v10\n\t" \
    "ds_read_b128 v[56:59], v6\n\t" \
    "ds_read_b128 v[60:63], v7\n\t" \
    "v_smfmac_f32_16x16x64_bf16 v[80:83], v[40:43], a[152:159], v10\n\t" \
    "ds_read_b128 v[64:67], v8\n\t" \
    "v_smfmac_f32_16x16x64_bf16 v[84:87], v[40:43], a[224:231], v10\n\t" \
    "ds_read_b128 v[68:71], v9\n\t" \
    "s_waitcnt lgkmcnt(5)\n\t" \
    "v_smfmac_f32_16x16x64_bf16 v[72:75], v[48:51], a[16:23], v10\n\t" \
    "ds_read_b128 v[44:47], v0 offset:19456\n\t" \
    "s_waitcnt lgkmcnt(5)\n\t" \
    "v_smfmac_f32_16x16x64_bf16 v[72:75], v[52:55], a[24:31], v10\n\t" \
    "s_waitcnt lgkmcnt(4)\n\t" \
    "v_smfmac_f32_16x16x64_bf16 v[72:75], v[56:59], a[32:39], v10\n\t" \
    "s_waitcnt lgkmcnt(3)\n\t" \
    "v_smfmac_f32_16x16x64_bf16 v[72:75], v[60:63], a[40:47], v10\n\t" \
    "s_waitcnt lgkmcnt(2)\n\t" \
    "v_smfmac_f32_16x16x64_bf16 v[72:75], v[64:67], a[48:55], v10\n\t" \
    "s_waitcnt lgkmcnt(1)\n\t" \
    "v_smfmac_f32_16x16x64_bf16 v[72:75], v[68:71], a[56:63], v10\n\t" \
    "s_waitcnt lgkmcnt(0)\n\t" \
    "v_smfmac_f32_16x16x64_bf16 v[72:75], v[44:47], a[64:71], v10\n\t" \
    "s_nop 2\n\t" \
    "v_smfmac_f32_16x16x64_bf16 v[76:79], v[48:51], a[88:95], v10\n\t" \
    "s_nop 2\n\t" \
    "v_smfmac_f32_16x16x64_bf16 v[76:79], v[52:55], a[96:103], v10\n\t" \
    "v_add_f32 v72, v72, v74\n\t" \
    "v_smfmac_f32_16x16x64_bf16 v[76:79], v[56:59], a[104:111], v10\n\t" \
    "v_add_f32 v73, v73, v75\n\t" \
    "v_smfmac_f32_16x16x64_bf16 v[76:79], v[60:63], a[112:119], v10\n\t" \
    "v_cvt_pk_bf16_f32 v156, v72, v73\n\t" \
    "v_smfmac_f32_16x16x64_bf16 v[76:79], v[64:67], a[120:127], v10\n\t" \
    "ds_write_b32 v12, v156 offset:0\n\t" \
    "v_smfmac_f32_16x16x64_bf16 v[76:79], v[68:71], a[128:135], v10\n\t" \
    "v_smfmac_f32_16x16x64_bf16 v[76:79], v[44:47], a[136:143], v10\n\t" \
    "s_nop 2\n\t" \
    "v_smfmac_f32_16x16x64_bf16 v[80:83], v[48:51], a[160:167], v10\n\t" \
    "s_nop 2\n\t" \
    "v_smfmac_f32_16x16x64_bf16 v[80:83], v[52:55], a[168:175], v10\n\t" \
    "v_add_f32 v76, v76, v78\n\t" \
    "v_smfmac_f32_16x16x64_bf16 v[80:83], v[56:59], a[176:183], v10\n\t" \
    "v_add_f32 v77, v77, v79\n\t" \
    "v_smfmac_f32_16x16x64_bf16 v[80:83], v[60:63], a[184:191], v10\n\t" \
    "v_cvt_pk_bf16_f32 v157, v76, v77\n\t" \
    "v_smfmac_f32_16x16x64_bf16 v[80:83], v[64:67], a[192:199], v10\n\t" \
    "ds_write_b32 v12, v157 offset:64\n\t" \
    "v_smfmac_f32_16x16x64_bf16 v[80:83], v[68:71], a[200:207], v10\n\t" \
    "v_smfmac_f32_16x16x64_bf16 v[80:83], v[44:47], a[208:215], v10\n\t" \
    "s_nop 2\n\t" \
    "v_smfmac_f32_16x16x64_bf16 v[84:87], v[48:51], a[232:239], v10\n\t" \
    "s_nop 2\n\t" \
    "v_smfmac_f32_16x16x64_bf16 v[84:87], v[52:55], a[240:247], v10\n\t" \
    "v_add_f32 v80, v80, v82\n\t" \
    "v_smfmac_f32_16x16x64_bf16 v[84:87], v[56:59], a[248:255], v10\n\t" \
    "v_add_f32 v81, v81, v83\n\t" \
    "v_smfmac_f32_16x16x64_bf16 v[84:87], v[60:63], v[224:231], v10\n\t" \
    "v_cvt_pk_bf16_f32 v158, v80, v81\n\t" \
    "v_smfmac_f32_16x16x64_bf16 v[84:87], v[64:67], v[232:239], v10\n\t" \
    "ds_write_b32 v16, v158 offset:0\n\t" \
    "v_smfmac_f32_16x16x64_bf16 v[84:87], v[68:71], v[240:247], v10\n\t" \
    "ds_read_b64_tr_b16 v[132:133], v13 offset:0\n\t" \
    "ds_read_b64_tr_b16 v[134:135], v13 offset:128\n\t" \
    "v_smfmac_f32_16x16x64_bf16 v[84:87], v[44:47], v[248:255], v10\n\t" \
    "ds_read_b64_tr_b16 v[136:137], v13 offset:1024\n\t" \
    "ds_read_b64_tr_b16 v[138:139], v13 offset:1152\n\t" \
    "s_nop 4\n\t" \
    "v_cmp_u_f32 vcc, v72, v73\n\t" \
    "v_pk_add_f32 v[84:85], v[84:85], v[86:87]\n\t" \
    "v_cvt_pk_bf16_f32 v159, v84, v85\n\t" \
    "ds_write_b32 v16, v159 offset:256\n\t" \
    "s_cbranch_vccnz qk_Gslow23_%=\n\t" \
    "qk_Gback24_%=:\n\t" \
    "s_waitcnt lgkmcnt(3)\n\t" \
    "v_smfmac_f32_16x16x64_bf16 v[88:91], v[132:135], v[100:107], v10\n\t" \
    "s_waitcnt lgkmcnt(1)\n\t" \
    "v_smfmac_f32_16x16x64_bf16 v[88:91], v[136:139], v[108:115], v10\n\t" \
    "s_waitcnt lgkmcnt(0)\n\t" \
    "s_barrier\n\t" \
    "ds_read_b64_tr_b16 v[140:141], v17 offset:0\n\t" \
    "v_smfmac_f32_16x16x64_bf16 v[92:95], v[132:135], v[116:123], v10\n\t" \
    "ds_read_b64_tr_b16 v[142:143], v17 offset:256\n\t" \
    "ds_read_b64_tr_b16 v[144:145], v17 offset:512\n\t" \
    "ds_read_b64_tr_b16 v[146:147], v17 offset:768\n\t" \
    "ds_read_b64_tr_b16 v[148:149], v17 offset:1024\n\t" \
    "v_smfmac_f32_16x16x64_bf16 v[92:95], v[136:139], v[124:131], v10\n\t" \
    "ds_read_b64_tr_b16 v[150:151], v17 offset:1280\n\t" \
    "ds_read_b64_tr_b16 v[152:153], v17 offset:1536\n\t" \
    "ds_read_b64_tr_b16 v[154:155], v17 offset:1792\n\t" \
    "v_smfmac_f32_16x16x64_bf16 v[184:187], v[132:135], v[100:107], v10\n\t" \
    "qk_qb26_%=:\n\t" \
    "s_waitcnt lgkmcnt(4)\n\t" \
    "v_smfmac_f32_16x16x64_bf16 v[96:99], v[132:135], v[140:147], v10\n\t" \
    "s_waitcnt lgkmcnt(0)\n\t" \
    "v_smfmac_f32_16x16x64_bf16 v[96:99], v[136:139], v[148:155], v10\n\t" \
    "v_add_f32 v168, v88, v90\n\t" \
    "v_add_f32 v169, v92, v94\n\t" \
    "v_cmp_u_f32 vcc, v168, v169\n\t" \
    "v_cvt_pk_bf16_f32 v168, 0, v168\n\t" \
    "v_cvt_pk_bf16_f32 v169, 0, v169\n\t" \
    "v_mul_f32 v168, v168, v26\n\t" \
    "v_mul_f32 v169, v169, v26\n\t" \
    "v_cvt_pk_bf16_f32 v168, 0, v168\n\t" \
    "v_add_f32 v172, v96, v98\n\t" \
    "v_cvt_pk_bf16_f32 v169, 0, v169\n\t" \
    "v_max_f32 v178, v168, v169\n\t" \
    "v_cvt_pk_bf16_f32 v172, 0, v172\n\t" \
    "v_mul_f32 v172, v172, v26\n\t" \
    "v_max_f32_dpp v178, v178, v178 row_mirror row_mask:0xf bank_mask:0xf\n\t" \
    "v_cvt_pk_bf16_f32 v172, 0, v172\n\t" \
    "v_max_f32 v173, v178, v172\n\t" \
    "v_mul_f32 v168, v168, v28\n\t" \
    "v_mul_f32 v169, v169, v28\n\t" \
    "v_max_f32_dpp v173, v173, v173 quad_perm:[1,0,3,2] row_mask:0xf bank_mask:0xf\n\t" \
    "v_mul_f32 v172, v172, v28\n\t" \
    "s_cbranch_vccnz qk_qs25_%=\n\t" \
    "qk_qb227_%=:\n\t" \
    "v_max_f32_dpp v173, v173, v173 quad_perm:[2,3,0,1] row_mask:0xf bank_mask:0xf\n\t" \
    "s_nop 1\n\t" \
    "v_max_f32_dpp v173, v173, v173 row_half_mirror row_mask:0xf bank_mask:0xf\n\t" \
    "v_fma_f32 v172, -v173, v28, v172\n\t" \
    "v_fma_f32 v168, -v173, v28, v168\n\t" \
    "v_fma_f32 v169, -v173, v28, v169\n\t" \
    "v_exp_f32 v172, v172\n\t" \
    "v_exp_f32 v168, v168\n\t" \
    "v_exp_f32 v169, v169\n\t" \
    "v_fma_f32 v173, v172, 0.5, v168\n\t" \
    "v_add_f32 v173, v173, v169\n\t" \
    "v_mov_b32_dpp v174, v172 quad_perm:[0,0,2,2] row_mask:0xf bank_mask:0xf\n\t" \
    "v_mov_b32_dpp v175, v172 quad_perm:[1,1,3,3] row_mask:0xf bank_mask:0xf\n\t" \
    "v_add_f32_dpp v173, v173, v173 quad_perm:[1,0,3,2] row_mask:0xf bank_mask:0xf\n\t" \
    "v_mov_b32_dpp v176, v168 quad_perm:[1,0,3,2] row_mask:0xf bank_mask:0xf\n\t" \
    "v_mov_b32_dpp v177, v169 quad_perm:[1,0,3,2] row_mask:0xf bank_mask:0xf\n\t" \
    "v_add_f32_dpp v173, v173, v173 quad_perm:[2,3,0,1] row_mask:0xf bank_mask:0xf\n\t" \
    "v_cndmask_b32 v168, v168, v177, s[48:49]\n\t" \
    "v_cndmask_b32 v169, v176, v169, s[48:49]\n\t" \
    "v_add_f32_dpp v173, v173, v173 row_half_mirror row_mask:0xf bank_mask:0xf\n\t" \
    "s_nop 1\n\t" \
    "v_add_f32_dpp v173, v173, v173 row_mirror row_mask:0xf bank_mask:0xf\n\t" \
    "v_rcp_f32 v173, v173\n\t" \
    "s_nop 0\n\t" \
    "v_pk_mul_f32 v[168:169], v[168:169], v[172:173] op_sel:[0,1] op_sel_hi:[1,1]\n\t" \
    "v_cvt_pk_bf16_f32 v176, v168, v169\n\t" \
    "global_store_dword v18, v176, s[46:47] sc1\n\t" \
    "global_store_dword v180, v176, s[46:47] sc1\n\t" \
    "global_store_dword v181, v176, s[46:47] sc1\n\t" \
    "global_store_dword v182, v176, s[46:47] sc1\n\t" \
    "v_pk_mul_f32 v[174:175], v[174:175], v[172:173] op_sel:[0,1] op_sel_hi:[1,1]\n\t" \
    "v_cvt_pk_bf16_f32 v177, v174, v175\n\t" \
    "global_store_dword v19, v177, s[46:47] sc1\n\t" \
    "s_cmp_eq_u32 s65, 63\n\t" \
    "s_cbranch_scc1 qk_last15_%=\n\t" \
    "v_readfirstlane_b32 s69, v25\n\t" \
    "s_add_u32 s65, s65, 1\n\t" \
    "s_max_i32 s83, s83, s69\n\t" \
    "s_cmp_gt_u32 s65, 6\n\t" \
    "s_cselect_b32 s83, 99, s83\n\t" \
    "v_mov_b32 v72, 0\n\t" \
    "v_mov_b32 v73, 0\n\t" \
    "v_mov_b32 v74, 0\n\t" \
    "v_mov_b32 v75, 0\n\t" \
    "v_mov_b32 v76, 0\n\t" \
    "v_mov_b32 v77, 0\n\t" \
    "v_mov_b32 v78, 0\n\t" \
    "v_mov_b32 v79, 0\n\t" \
    "v_mov_b32 v80, 0\n\t" \
    "v_mov_b32 v81, 0\n\t" \
    "v_mov_b32 v82, 0\n\t" \
    "v_mov_b32 v83, 0\n\t" \
    "v_mov_b32 v84, 0\n\t" \
    "v_mov_b32 v85, 0\n\t" \
    "v_mov_b32 v86, 0\n\t" \
    "v_mov_b32 v87, 0\n\t" \
    "v_mov_b32 v88, 0\n\t" \
    "v_mov_b32 v89, 0\n\t" \
    "v_mov_b32 v90, 0\n\t" \
    "v_mov_b32 v91, 0\n\t" \
    "v_mov_b32 v92, 0\n\t" \
    "v_mov_b32 v93, 0\n\t" \
    "v_mov_b32 v94, 0\n\t" \
    "v_mov_b32 v95, 0\n\t" \
    "v_mov_b32 v96, 0\n\t" \
    "v_mov_b32 v97, 0\n\t" \
    "v_mov_b32 v98, 0\n\t" \
    "v_mov_b32 v99, 0\n\t" \
    "s_add_u32 s69, s65, 0\n\t" \
    "s_and_b32 s69, s69, 7\n\t" \
    "s_lshl_b32 s69, s69, 18\n\t" \
    "s_add_u32 s69, s69, s62\n\t" \
    "s_add_u32 s46, s36, s69\n\t" \
    "s_addc_u32 s47, s37, 0\n\t" \
    "s_mul_i32 s70, s67, 7\n\t" \
    "s_add_u32 s69, s65, 0\n\t" \
    "s_xor_b32 s69, s69, s70\n\t" \
    "s_and_b32 s69, s69, 63\n\t" \
    "s_lshl_b32 s69, s69, 13\n\t" \
    "s_mul_i32 s70, s67, 524288\n\t" \
    "s_add_u32 s69, s69, s70\n\t" \
    "s_add_u32 s69, s69, 23068672\n\t" \
    "s_add_u32 s38, s36, s69\n\t" \
    "s_addc_u32 s39, s37, 0\n\t" \
    "qk_WF28_%=:\n\t" \
    "s_cmp_ge_i32 s83, s65\n\t" \
    "s_cbranch_scc1 qk_WFok29_%=\n\t" \
    "global_load_dword v160, v24, s[36:37] sc1\n\t" \
    "s_waitcnt vmcnt(0)\n\t" \
    "v_readfirstlane_b32 s83, v160\n\t" \
    "s_nop 1\n\t" \
    "s_cmp_ge_i32 s83, s65\n\t" \
    "s_cbranch_scc1 qk_WFok29_%=\n\t" \
    "s_cmp_eq_u32 s68, 0\n\t" \
    "s_cbranch_scc1 qk_F30_%=\n\t" \
    "s_sub_u32 s68, s68, 1\n\t" \
    "s_sleep 2\n\t" \
    "s_branch qk_WF28_%=\n\t" \
    "qk_F30_%=:\n\t" \
    "v_mov_b32 v160, 5767424\n\t" \
    "v_mov_b32 v161, 9\n\t" \
    "global_store_dword v160, v161, s[36:37] sc1\n\t" \
    "s_waitcnt vmcnt(0)\n\t" \
    "s_branch qk_WFok29_%=\n\t" \
    "qk_WFok29_%=:\n\t" \
    "global_load_dwordx4 v[100:103], v0, s[38:39] offset:0 sc1\n\t" \
    "global_load_dwordx4 v[104:107], v0, s[38:39] offset:2048 sc1\n\t" \
    "global_load_dwordx4 v[108:111], v15, s[38:39] offset:0 sc1\n\t" \
    "global_load_dwordx4 v[112:115], v15, s[38:39] offset:2048 sc1\n\t" \
    "global_load_dwordx4 v[116:119], v0, s[38:39] offset:1024 sc1\n\t" \
    "global_load_dwordx4 v[120:123], v0, s[38:39] offset:3072 sc1\n\t" \
    "global_load_dwordx4 v[124:127], v15, s[38:39] offset:1024 sc1\n\t" \
    "global_load_dwordx4 v[128:131], v15, s[38:39] offset:3072 sc1\n\t" \
    "s_cmp_gt_i32 s83, s65\n\t" \
    "s_cbranch_scc1 qk_nf31_%=\n\t" \
    "global_load_dword v25, v24, s[36:37] sc1\n\t" \
    "qk_nf31_%=:\n\t" \
    "s_barrier\n\t" \
    "ds_read_b32 v168, v11 offset:20480\n\t" \
    "s_waitcnt lgkmcnt(0)\n\t" \
    "v_readfirstlane_b32 s70, v168\n\t" \
    "ds_write_b32 v12, v31 offset:0\n\t" \
    "ds_write_b32 v12, v31 offset:64\n\t" \
    "s_branch qk_go16_%=\n\t" \
    "qk_Gbad21_%=:\n\t" \
    "s_waitcnt lgkmcnt(0)\n\t" \
    "s_nop 7\n\t" \
    "v_mov_b32 v72, 0\n\t" \
    "v_mov_b32 v73, 0\n\t" \
    "v_mov_b32 v74, 0\n\t" \
    "v_mov_b32 v75, 0\n\t" \
    "v_mov_b32 v76, 0\n\t" \
    "v_mov_b32 v77, 0\n\t" \
    "v_mov_b32 v78, 0\n\t" \
    "v_mov_b32 v79, 0\n\t" \
    "v_mov_b32 v80, 0\n\t" \
    "v_mov_b32 v81, 0\n\t" \
    "v_mov_b32 v82, 0\n\t" \
    "v_mov_b32 v83, 0\n\t" \
    "v_mov_b32 v84, 0\n\t" \
    "v_mov_b32 v85, 0\n\t" \
    "v_mov_b32 v86, 0\n\t" \
    "v_mov_b32 v87, 0\n\t" \
    "s_cmp_eq_u32 s68, 0\n\t" \
    "s_cbranch_scc1 qk_F32_%=\n\t" \
    "s_sub_u32 s68, s68, 1\n\t" \
    "s_branch qk_G20_%=\n\t" \
    "qk_F32_%=:\n\t" \
    "v_mov_b32 v160, 5767424\n\t" \
    "v_mov_b32 v161, 1\n\t" \
    "global_store_dword v160, v161, s[36:37] sc1\n\t" \
    "s_waitcnt vmcnt(0)\n\t" \
    "s_branch qk_Gc22_%=\n\t" \
    "qk_Gslow23_%=:\n\t" \
    "v_max_u32 v160, v36, v37\n\t" \
    "v_max3_u32 v160, v160, v38, v39\n\t" \
    "v_max3_u32 v160, v160, v40, v41\n\t" \
    "v_max3_u32 v160, v160, v42, v43\n\t" \
    "v_max3_u32 v160, v160, v44, v45\n\t" \
    "v_max3_u32 v160, v160, v46, v47\n\t" \
    "v_max3_u32 v160, v160, v48, v49\n\t" \
    "v_max3_u32 v160, v160, v50, v51\n\t" \
    "v_max3_u32 v160, v160, v52, v53\n\t" \
    "v_max3_u32 v160, v160, v54, v55\n\t" \
    "v_max3_u32 v160, v160, v56, v57\n\t" \
    "v_max3_u32 v160, v160, v58, v59\n\t" \
    "v_max3_u32 v160, v160, v60, v61\n\t" \
    "v_max3_u32 v160, v160, v62, v63\n\t" \
    "v_max3_u32 v160, v160, v64, v65\n\t" \
    "v_max3_u32 v160, v160, v66, v67\n\t" \
    "v_max3_u32 v160, v160, v68, v69\n\t" \
    "v_max3_u32 v160, v160, v70, v71\n\t" \
    "v_cmp_eq_u32 vcc, -1, v160\n\t" \
    "s_cbranch_vccz qk_Gback24_%=\n\t" \
    "s_waitcnt lgkmcnt(0)\n\t" \
    "v_sub_u32 v160, v1, v3\n\t" \
    "s_nop 0\n\t" \
    "v_readfirstlane_b32 s69, v160\n\t" \
    "v_add_u32 v163, 20480, v0\n\t" \
    "s_nop 7\n\t" \
    "s_add_u32 s30, s46, s69\n\t" \
    "s_addc_u32 s31, s47, 0\n\t" \
    "qk_Gag33_%=:\n\t" \
    "global_load_dwordx4 v[36:39], v1, s[46:47] sc1\n\t" \
    "global_load_dwordx4 v[40:43], v23, s[46:47] sc1\n\t" \
    "global_load_dwordx4 v[44:47], v163, s[30:31] sc1\n\t" \
    "global_load_dwordx4 v[48:51], v4, s[30:31] sc1\n\t" \
    "global_load_dwordx4 v[52:55], v5, s[30:31] sc1\n\t" \
    "global_load_dwordx4 v[56:59], v6, s[30:31] sc1\n\t" \
    "global_load_dwordx4 v[60:63], v7, s[30:31] sc1\n\t" \
    "global_load_dwordx4 v[64:67], v8, s[30:31] sc1\n\t" \
    "global_load_dwordx4 v[68:71], v9, s[30:31] sc1\n\t" \
    "s_waitcnt vmcnt(0)\n\t" \
    "v_max_u32 v160, v36, v37\n\t" \
    "v_max3_u32 v160, v160, v38, v39\n\t" \
    "v_max3_u32 v160, v160, v40, v41\n\t" \
    "v_max3_u32 v160, v160, v42, v43\n\t" \
    "v_max3_u32 v160, v160, v44, v45\n\t" \
    "v_max3_u32 v160, v160, v46, v47\n\t" \
    "v_max3_u32 v160, v160, v48, v49\n\t" \
    "v_max3_u32 v160, v160, v50, v51\n\t" \
    "v_max3_u32 v160, v160, v52, v53\n\t" \
    "v_max3_u32 v160, v160, v54, v55\n\t" \
    "v_max3_u32 v160, v160, v56, v57\n\t" \
    "v_max3_u32 v160, v160, v58, v59\n\t" \
    "v_max3_u32 v160, v160, v60, v61\n\t" \
    "v_max3_u32 v160, v160, v62, v63\n\t" \
    "v_max3_u32 v160, v160, v64, v65\n\t" \
    "v_max3_u32 v160, v160, v66, v67\n\t" \
    "v_max3_u32 v160, v160, v68, v69\n\t" \
    "v_max3_u32 v160, v160, v70, v71\n\t" \
    "v_cmp_eq_u32 vcc, -1, v160\n\t" \
    "s_cbranch_vccz qk_Gredo34_%=\n\t" \
    "s_cmp_eq_u32 s68, 0\n\t" \
    "s_cbranch_scc1 qk_F35_%=\n\t" \
    "s_sub_u32 s68, s68, 1\n\t" \
    "s_sleep 1\n\t" \
    "s_branch qk_Gag33_%=\n\t" \
    "qk_F35_%=:\n\t" \
    "v_mov_b32 v160, 5767424\n\t" \
    "v_mov_b32 v161, 2\n\t" \
    "global_store_dword v160, v161, s[36:37] sc1\n\t" \
    "s_waitcnt vmcnt(0)\n\t" \
    "s_branch qk_Gredo34_%=\n\t" \
    "qk_Gredo34_%=:\n\t" \
    "v_mov_b32 v72, 0\n\t" \
    "v_mov_b32 v73, 0\n\t" \
    "v_mov_b32 v74, 0\n\t" \
    "v_mov_b32 v75, 0\n\t" \
    "v_mov_b32 v76, 0\n\t" \
    "v_mov_b32 v77, 0\n\t" \
    "v_mov_b32 v78, 0\n\t" \
    "v_mov_b32 v79, 0\n\t" \
    "v_mov_b32 v80, 0\n\t" \
    "v_mov_b32 v81, 0\n\t" \
    "v_mov_b32 v82, 0\n\t" \
    "v_mov_b32 v83, 0\n\t" \
    "v_mov_b32 v84, 0\n\t" \
    "v_mov_b32 v85, 0\n\t" \
    "v_mov_b32 v86, 0\n\t" \
    "v_mov_b32 v87, 0\n\t" \
    "v_smfmac_f32_16x16x64_bf16 v[72:75], v[36:39], a[0:7], v10\n\t" \
    "v_smfmac_f32_16x16x64_bf16 v[72:75], v[40:43], a[8:15], v10\n\t" \
    "v_smfmac_f32_16x16x64_bf16 v[72:75], v[48:51], a[16:23], v10\n\t" \
    "v_smfmac_f32_16x16x64_bf16 v[72:75], v[52:55], a[24:31], v10\n\t" \
    "v_smfmac_f32_16x16x64_bf16 v[72:75], v[56:59], a[32:39], v10\n\t" \
    "v_smfmac_f32_16x16x64_bf16 v[72:75], v[60:63], a[40:47], v10\n\t" \
    "v_smfmac_f32_16x16x64_bf16 v[72:75], v[64:67], a[48:55], v10\n\t" \
    "v_smfmac_f32_16x16x64_bf16 v[72:75], v[68:71], a[56:63], v10\n\t" \
    "v_smfmac_f32_16x16x64_bf16 v[72:75], v[44:47], a[64:71], v10\n\t" \
    "v_smfmac_f32_16x16x64_bf16 v[76:79], v[36:39], a[72:79], v10\n\t" \
    "v_smfmac_f32_16x16x64_bf16 v[76:79], v[40:43], a[80:87], v10\n\t" \
    "v_smfmac_f32_16x16x64_bf16 v[76:79], v[48:51], a[88:95], v10\n\t" \
    "v_smfmac_f32_16x16x64_bf16 v[76:79], v[52:55], a[96:103], v10\n\t" \
    "v_smfmac_f32_16x16x64_bf16 v[76:79], v[56:59], a[104:111], v10\n\t" \
    "v_smfmac_f32_16x16x64_bf16 v[76:79], v[60:63], a[112:119], v10\n\t" \
    "v_smfmac_f32_16x16x64_bf16 v[76:79], v[64:67], a[120:127], v10\n\t" \
    "v_smfmac_f32_16x16x64_bf16 v[76:79], v[68:71], a[128:135], v10\n\t" \
    "v_smfmac_f32_16x16x64_bf16 v[76:79], v[44:47], a[136:143], v10\n\t" \
    "v_smfmac_f32_16x16x64_bf16 v[80:83], v[36:39], a[144:151], v10\n\t" \
    "v_smfmac_f32_16x16x64_bf16 v[80:83], v[40:43], a[152:159], v10\n\t" \
    "v_smfmac_f32_16x16x64_bf16 v[80:83], v[48:51], a[160:167], v10\n\t" \
    "v_smfmac_f32_16x16x64_bf16 v[80:83], v[52:55], a[168:175], v10\n\t" \
    "v_smfmac_f32_16x16x64_bf16 v[80:83], v[56:59], a[176:183], v10\n\t" \
    "v_smfmac_f32_16x16x64_bf16 v[80:83], v[60:63], a[184:191], v10\n\t" \
    "v_smfmac_f32_16x16x64_bf16 v[80:83], v[64:67], a[192:199], v10\n\t" \
    "v_smfmac_f32_16x16x64_bf16 v[80:83], v[68:71], a[200:207], v10\n\t" \
    "v_smfmac_f32_16x16x64_bf16 v[80:83], v[44:47], a[208:215], v10\n\t" \
    "v_smfmac_f32_16x16x64_bf16 v[84:87], v[36:39], a[216:223], v10\n\t" \
    "v_smfmac_f32_16x16x64_bf16 v[84:87], v[40:43], a[224:231], v10\n\t" \
    "v_smfmac_f32_16x16x64_bf16 v[84:87], v[48:51], a[232:239], v10\n\t" \
    "v_smfmac_f32_16x16x64_bf16 v[84:87], v[52:55], a[240:247], v10\n\t" \
    "v_smfmac_f32_16x16x64_bf16 v[84:87], v[56:59], a[248:255], v10\n\t" \
    "v_smfmac_f32_16x16x64_bf16 v[84:87], v[60:63], v[224:231], v10\n\t" \
    "v_smfmac_f32_16x16x64_bf16 v[84:87], v[64:67], v[232:239], v10\n\t" \
    "v_smfmac_f32_16x16x64_bf16 v[84:87], v[68:71], v[240:247], v10\n\t" \
    "v_smfmac_f32_16x16x64_bf16 v[84:87], v[44:47], v[248:255], v10\n\t" \
    "v_add_f32 v72, v72, v74\n\t" \
    "v_add_f32 v73, v73, v75\n\t" \
    "v_cvt_pk_bf16_f32 v156, v72, v73\n\t" \
    "ds_write_b32 v12, v156 offset:0\n\t" \
    "v_add_f32 v76, v76, v78\n\t" \
    "v_add_f32 v77, v77, v79\n\t" \
    "v_cvt_pk_bf16_f32 v157, v76, v77\n\t" \
    "ds_write_b32 v12, v157 offset:64\n\t" \
    "v_add_f32 v80, v80, v82\n\t" \
    "v_add_f32 v81, v81, v83\n\t" \
    "v_cvt_pk_bf16_f32 v158, v80, v81\n\t" \
    "ds_write_b32 v16, v158 offset:0\n\t" \
    "v_pk_add_f32 v[84:85], v[84:85], v[86:87]\n\t" \
    "v_cvt_pk_bf16_f32 v159, v84, v85\n\t" \
    "ds_write_b32 v16, v159 offset:256\n\t" \
    "s_branch qk_Gback24_%=\n\t" \
    "qk_qs25_%=:\n\t" \
    "s_cmp_eq_u32 s91, s65\n\t" \
    "s_cbranch_scc1 qk_qb227_%=\n\t" \
    "s_mov_b32 s91, s65\n\t" \
    "s_nop 7\n\t" \
    "ds_read_b64_tr_b16 v[132:133], v13 offset:0\n\t" \
    "ds_read_b64_tr_b16 v[134:135], v13 offset:128\n\t" \
    "ds_read_b64_tr_b16 v[136:137], v13 offset:1024\n\t" \
    "ds_read_b64_tr_b16 v[138:139], v13 offset:1152\n\t" \
    "v_mov_b32 v88, 0\n\t" \
    "v_mov_b32 v89, 0\n\t" \
    "v_mov_b32 v90, 0\n\t" \
    "v_mov_b32 v91, 0\n\t" \
    "v_mov_b32 v92, 0\n\t" \
    "v_mov_b32 v93, 0\n\t" \
    "v_mov_b32 v94, 0\n\t" \
    "v_mov_b32 v95, 0\n\t" \
    "v_mov_b32 v96, 0\n\t" \
    "v_mov_b32 v97, 0\n\t" \
    "v_mov_b32 v98, 0\n\t" \
    "v_mov_b32 v99, 0\n\t" \
    "s_waitcnt lgkmcnt(0)\n\t" \
    "v_smfmac_f32_16x16x64_bf16 v[88:91], v[132:135], v[100:107], v10\n\t" \
    "v_smfmac_f32_16x16x64_bf16 v[92:95], v[132:135], v[116:123], v10\n\t" \
    "v_smfmac_f32_16x16x64_bf16 v[88:91], v[136:139], v[108:115], v10\n\t" \
    "v_smfmac_f32_16x16x64_bf16 v[92:95], v[136:139], v[124:131], v10\n\t" \
    "s_nop 7\n\t" \
    "s_branch qk_qb26_%=\n\t" \
    "qk_last15_%=:\n\t" \
    "s_endpgm\n\t" \
    "qk_loop12_%=:\n\t" \
    "v_mov_b32 v72, 0\n\t" \
    "v_mov_b32 v73, 0\n\t" \
    "v_mov_b32 v74, 0\n\t" \
    "v_mov_b32 v75, 0\n\t" \
    "v_mov_b32 v76, 0\n\t" \
    "v_mov_b32 v77, 0\n\t" \
    "v_mov_b32 v78, 0\n\t" \
    "v_mov_b32 v79, 0\n\t" \
    "v_mov_b32 v80, 0\n\t" \
    "v_mov_b32 v81, 0\n\t" \
    "v_mov_b32 v82, 0\n\t" \
    "v_mov_b32 v83, 0\n\t" \
    "v_mov_b32 v84, 0\n\t" \
    "v_mov_b32 v85, 0\n\t" \
    "v_mov_b32 v86, 0\n\t" \
    "v_mov_b32 v87, 0\n\t" \
    "v_mov_b32 v88, 0\n\t" \
    "v_mov_b32 v89, 0\n\t" \
    "v_mov_b32 v90, 0\n\t" \
    "v_mov_b32 v91, 0\n\t" \
    "v_mov_b32 v92, 0\n\t" \
    "v_mov_b32 v93, 0\n\t" \
    "v_mov_b32 v94, 0\n\t" \
    "v_mov_b32 v95, 0\n\t" \
    "v_mov_b32 v96, 0\n\t" \
    "v_mov_b32 v97, 0\n\t" \
    "v_mov_b32 v98, 0\n\t" \
    "v_mov_b32 v99, 0\n\t" \
    "s_add_u32 s69, s65, 0\n\t" \
    "s_and_b32 s69, s69, 7\n\t" \
    "s_lshl_b32 s69, s69, 18\n\t" \
    "s_add_u32 s69, s69, s62\n\t" \
    "s_add_u32 s46, s36, s69\n\t" \
    "s_addc_u32 s47, s37, 0\n\t" \
    "s_sub_u32 s70, s79, s74\n\t" \
    "s_max_i32 s70, s70, 0\n\t" \
    "s_min_i32 s70, s70, 16000\n\t" \
    "s_lshl_b32 s70, s70, 1\n\t" \
    "s_mul_hi_u32 s70, s70, s77\n\t" \
    "s_add_u32 s70, s70, s73\n\t" \
    ".p2align 6\n\t" \
    "s_nop 0\n\t" \
    "s_nop 0\n\t" \
    "s_nop 0\n\t" \
    "s_nop 0\n\t" \
    "s_nop 0\n\t" \
    "s_nop 0\n\t" \
    "qk_go37_%=:\n\t" \
    "s_getpc_b64 s[30:31]\n\t" \
    "qk_Wp39_%=:\n\t" \
    "s_add_u32 s30, s30, qk_We40_%=-qk_Wp39_%=\n\t" \
    "s_addc_u32 s31, s31, 0\n\t" \
    "s_sub_u32 s70, s70, 96\n\t" \
    "qk_W38_%=:\n\t" \
    "s_memtime s[56:57]\n\t" \
    "s_waitcnt lgkmcnt(0)\n\t" \
    "s_sub_u32 s69, s70, s56\n\t" \
    "s_cmp_gt_i32 s69, 80\n\t" \
    "s_cbranch_scc1 qk_W38_%=\n\t" \
    "s_max_i32 s69, s69, 0\n\t" \
    "s_and_b32 s69, s69, -4\n\t" \
    "s_sub_u32 s30, s30, s69\n\t" \
    "s_subb_u32 s31, s31, 0\n\t" \
    "s_setpc_b64 s[30:31]\n\t" \
    "s_nop 0\n\t" \
    "s_nop 0\n\t" \
    "s_nop 0\n\t" \
    "s_nop 0\n\t" \
    "s_nop 0\n\t" \
    "s_nop 0\n\t" \
    "s_nop 0\n\t" \
    "s_nop 0\n\t" \
    "s_nop 0\n\t" \
    "s_nop 0\n\t" \
    "s_nop 0\n\t" \
    "s_nop 0\n\t" \
    "s_nop 0\n\t" \
    "s_nop 0\n\t" \
    "s_nop 0\n\t" \
    "s_nop 0\n\t" \
    "s_nop 0\n\t" \
    "s_nop 0\n\t" \
    "s_nop 0\n\t" \
    "s_nop 0\n\t" \
    "qk_We40_%=:\n\t" \
    "qk_G41_%=:\n\t" \
    "global_load_dwordx4 v[36:39], v1, s[46:47] sc1\n\t" \
    "global_load_dword v161, v2, s[46:47] sc1\n\t" \
    "global_load_dwordx4 v[40:43], v23, s[46:47] sc1\n\t" \
    "s_waitcnt vmcnt(2)\n\t" \
    "ds_write_b128 v3, v[36:39]\n\t" \
    "v_smfmac_f32_16x16x64_bf16 v[72:75], v[36:39], a[16:23], v10\n\t" \
    "s_waitcnt vmcnt(1)\n\t" \
    "ds_write_b32 v22, v161 offset:19712\n\t" \
    "v_smfmac_f32_16x16x64_bf16 v[76:79], v[36:39], a[88:95], v10\n\t" \
    "s_waitcnt vmcnt(0)\n\t" \
    "ds_write_b128 v3, v[40:43] offset:1024\n\t" \
    "v_max3_u32 v160, v36, v40, v161\n\t" \
    "v_cmp_eq_u32 vcc, -1, v160\n\t" \
    "v_smfmac_f32_16x16x64_bf16 v[80:83], v[36:39], a[160:167], v10\n\t" \
    "v_smfmac_f32_16x16x64_bf16 v[84:87], v[36:39], a[232:239], v10\n\t" \
    "v_smfmac_f32_16x16x64_bf16 v[72:75], v[40:43], a[24:31], v10\n\t" \
    "s_cbranch_vccnz qk_Gbad42_%=\n\t" \
    "qk_Gc43_%=:\n\t" \
    "s_waitcnt lgkmcnt(0)\n\t" \
    "s_barrier\n\t" \
    "ds_read_b128 v[48:51], v4\n\t" \
    "ds_read_b128 v[52:55], v5\n\t" \
    "v_smfmac_f32_16x16x64_bf16 v[76:79], v[40:43], a[96:103], v10\n\t" \
    "ds_read_b128 v[56:59], v6\n\t" \
    "ds_read_b128 v[60:63], v7\n\t" \
    "v_smfmac_f32_16x16x64_bf16 v[80:83], v[40:43], a[168:175], v10\n\t" \
    "ds_read_b128 v[64:67], v8\n\t" \
    "v_smfmac_f32_16x16x64_bf16 v[84:87], v[40:43], a[240:247], v10\n\t" \
    "ds_read_b128 v[68:71], v9\n\t" \
    "s_waitcnt lgkmcnt(5)\n\t" \
    "v_smfmac_f32_16x16x64_bf16 v[72:75], v[48:51], a[32:39], v10\n\t" \
    "ds_read_b128 v[44:47], v0 offset:19456\n\t" \
    "s_waitcnt lgkmcnt(5)\n\t" \
    "v_smfmac_f32_16x16x64_bf16 v[72:75], v[52:55], a[40:47], v10\n\t" \
    "s_waitcnt lgkmcnt(4)\n\t" \
    "v_smfmac_f32_16x16x64_bf16 v[72:75], v[56:59], a[48:55], v10\n\t" \
    "s_waitcnt lgkmcnt(3)\n\t" \
    "v_smfmac_f32_16x16x64_bf16 v[72:75], v[60:63], a[56:63], v10\n\t" \
    "s_waitcnt lgkmcnt(2)\n\t" \
    "v_smfmac_f32_16x16x64_bf16 v[72:75], v[64:67], a[0:7], v10\n\t" \
    "s_waitcnt lgkmcnt(1)\n\t" \
    "v_smfmac_f32_16x16x64_bf16 v[72:75], v[68:71], a[8:15], v10\n\t" \
    "s_waitcnt lgkmcnt(0)\n\t" \
    "v_smfmac_f32_16x16x64_bf16 v[72:75], v[44:47], a[64:71], v10\n\t" \
    "s_nop 2\n\t" \
    "v_smfmac_f32_16x16x64_bf16 v[76:79], v[48:51], a[104:111], v10\n\t" \
    "s_nop 2\n\t" \
    "v_smfmac_f32_16x16x64_bf16 v[76:79], v[52:55], a[112:119], v10\n\t" \
    "v_add_f32 v72, v72, v74\n\t" \
    "v_smfmac_f32_16x16x64_bf16 v[76:79], v[56:59], a[120:127], v10\n\t" \
    "v_add_f32 v73, v73, v75\n\t" \
    "v_smfmac_f32_16x16x64_bf16 v[76:79], v[60:63], a[128:135], v10\n\t" \
    "v_cvt_pk_bf16_f32 v156, v72, v73\n\t" \
    "v_smfmac_f32_16x16x64_bf16 v[76:79], v[64:67], a[72:79], v10\n\t" \
    "ds_write_b32 v12, v156 offset:0\n\t" \
    "v_smfmac_f32_16x16x64_bf16 v[76:79], v[68:71], a[80:87], v10\n\t" \
    "v_smfmac_f32_16x16x64_bf16 v[76:79], v[44:47], a[136:143], v10\n\t" \
    "s_nop 2\n\t" \
    "v_smfmac_f32_16x16x64_bf16 v[80:83], v[48:51], a[176:183], v10\n\t" \
    "s_nop 2\n\t" \
    "v_smfmac_f32_16x16x64_bf16 v[80:83], v[52:55], a[184:191], v10\n\t" \
    "v_add_f32 v76, v76, v78\n\t" \
    "v_smfmac_f32_16x16x64_bf16 v[80:83], v[56:59], a[192:199], v10\n\t" \
    "v_add_f32 v77, v77, v79\n\t" \
    "v_smfmac_f32_16x16x64_bf16 v[80:83], v[60:63], a[200:207], v10\n\t" \
    "v_cvt_pk_bf16_f32 v157, v76, v77\n\t" \
    "v_smfmac_f32_16x16x64_bf16 v[80:83], v[64:67], a[144:151], v10\n\t" \
    "ds_write_b32 v12, v157 offset:64\n\t" \
    "v_smfmac_f32_16x16x64_bf16 v[80:83], v[68:71], a[152:159], v10\n\t" \
    "v_smfmac_f32_16x16x64_bf16 v[80:83], v[44:47], a[208:215], v10\n\t" \
    "s_nop 2\n\t" \
    "v_smfmac_f32_16x16x64_bf16 v[84:87], v[48:51], a[248:255], v10\n\t" \
    "s_nop 2\n\t" \
    "v_smfmac_f32_16x16x64_bf16 v[84:87], v[52:55], v[224:231], v10\n\t" \
    "v_add_f32 v80, v80, v82\n\t" \
    "v_smfmac_f32_16x16x64_bf16 v[84:87], v[56:59], v[232:239], v10\n\t" \
    "v_add_f32 v81, v81, v83\n\t" \
    "v_smfmac_f32_16x16x64_bf16 v[84:87], v[60:63], v[240:247], v10\n\t" \
    "v_cvt_pk_bf16_f32 v158, v80, v81\n\t" \
    "v_smfmac_f32_16x16x64_bf16 v[84:87], v[64:67], a[216:223], v10\n\t" \
    "ds_write_b32 v16, v158 offset:0\n\t" \
    "v_smfmac_f32_16x16x64_bf16 v[84:87], v[68:71], a[224:231], v10\n\t" \
    "ds_read_b64_tr_b16 v[132:133], v13 offset:0\n\t" \
    "ds_read_b64_tr_b16 v[134:135], v13 offset:128\n\t" \
    "v_smfmac_f32_16x16x64_bf16 v[84:87], v[44:47], v[248:255], v10\n\t" \
    "ds_read_b64_tr_b16 v[136:137], v13 offset:1024\n\t" \
    "ds_read_b64_tr_b16 v[138:139], v13 offset:1152\n\t" \
    "s_nop 4\n\t" \
    "v_cmp_u_f32 vcc, v72, v73\n\t" \
    "v_pk_add_f32 v[84:85], v[84:85], v[86:87]\n\t" \
    "v_cvt_pk_bf16_f32 v159, v84, v85\n\t" \
    "ds_write_b32 v16, v159 offset:256\n\t" \
    "s_cbranch_vccnz qk_Gslow44_%=\n\t" \
    "qk_Gback45_%=:\n\t" \
    "s_waitcnt lgkmcnt(3)\n\t" \
    "v_smfmac_f32_16x16x64_bf16 v[88:91], v[132:135], v[100:107], v10\n\t" \
    "s_waitcnt lgkmcnt(1)\n\t" \
    "v_smfmac_f32_16x16x64_bf16 v[88:91], v[136:139], v[108:115], v10\n\t" \
    "s_waitcnt lgkmcnt(0)\n\t" \
    "s_barrier\n\t" \
    "ds_read_b64_tr_b16 v[140:141], v17 offset:0\n\t" \
    "v_smfmac_f32_16x16x64_bf16 v[92:95], v[132:135], v[116:123], v10\n\t" \
    "ds_read_b64_tr_b16 v[142:143], v17 offset:256\n\t" \
    "ds_read_b64_tr_b16 v[144:145], v17 offset:512\n\t" \
    "ds_read_b64_tr_b16 v[146:147], v17 offset:768\n\t" \
    "ds_read_b64_tr_b16 v[148:149], v17 offset:1024\n\t" \
    "v_smfmac_f32_16x16x64_bf16 v[92:95], v[136:139], v[124:131], v10\n\t" \
    "ds_read_b64_tr_b16 v[150:151], v17 offset:1280\n\t" \
    "ds_read_b64_tr_b16 v[152:153], v17 offset:1536\n\t" \
    "ds_read_b64_tr_b16 v[154:155], v17 offset:1792\n\t" \
    "v_smfmac_f32_16x16x64_bf16 v[184:187], v[132:135], v[100:107], v10\n\t" \
    "qk_qb47_%=:\n\t" \
    "s_waitcnt lgkmcnt(4)\n\t" \
    "v_smfmac_f32_16x16x64_bf16 v[96:99], v[132:135], v[140:147], v10\n\t" \
    "s_waitcnt lgkmcnt(0)\n\t" \
    "v_smfmac_f32_16x16x64_bf16 v[96:99], v[136:139], v[148:155], v10\n\t" \
    "v_add_f32 v168, v89, v91\n\t" \
    "v_add_f32 v169, v93, v95\n\t" \
    "v_cmp_u_f32 vcc, v168, v169\n\t" \
    "v_cvt_pk_bf16_f32 v168, 0, v168\n\t" \
    "v_cvt_pk_bf16_f32 v169, 0, v169\n\t" \
    "v_mul_f32 v168, v168, v26\n\t" \
    "v_mul_f32 v169, v169, v26\n\t" \
    "v_cvt_pk_bf16_f32 v168, 0, v168\n\t" \
    "v_add_f32 v172, v97, v99\n\t" \
    "v_cvt_pk_bf16_f32 v169, 0, v169\n\t" \
    "v_max_f32 v178, v168, v169\n\t" \
    "v_cvt_pk_bf16_f32 v172, 0, v172\n\t" \
    "v_mul_f32 v172, v172, v26\n\t" \
    "v_max_f32_dpp v178, v178, v178 row_mirror row_mask:0xf bank_mask:0xf\n\t" \
    "v_cvt_pk_bf16_f32 v172, 0, v172\n\t" \
    "v_max_f32 v173, v178, v172\n\t" \
    "v_mul_f32 v168, v168, v28\n\t" \
    "v_mul_f32 v169, v169, v28\n\t" \
    "v_max_f32_dpp v173, v173, v173 quad_perm:[1,0,3,2] row_mask:0xf bank_mask:0xf\n\t" \
    "v_mul_f32 v172, v172, v28\n\t" \
    "s_cbranch_vccnz qk_qs46_%=\n\t" \
    "qk_qb248_%=:\n\t" \
    "v_max_f32_dpp v173, v173, v173 quad_perm:[2,3,0,1] row_mask:0xf bank_mask:0xf\n\t" \
    "s_nop 1\n\t" \
    "v_max_f32_dpp v173, v173, v173 row_half_mirror row_mask:0xf bank_mask:0xf\n\t" \
    "v_fma_f32 v172, -v173, v28, v172\n\t" \
    "v_fma_f32 v168, -v173, v28, v168\n\t" \
    "v_fma_f32 v169, -v173, v28, v169\n\t" \
    "v_exp_f32 v172, v172\n\t" \
    "v_exp_f32 v168, v168\n\t" \
    "v_exp_f32 v169, v169\n\t" \
    "v_fma_f32 v173, v172, 0.5, v168\n\t" \
    "v_add_f32 v173, v173, v169\n\t" \
    "v_mov_b32_dpp v174, v172 quad_perm:[0,0,2,2] row_mask:0xf bank_mask:0xf\n\t" \
    "v_mov_b32_dpp v175, v172 quad_perm:[1,1,3,3] row_mask:0xf bank_mask:0xf\n\t" \
    "v_add_f32_dpp v173, v173, v173 quad_perm:[1,0,3,2] row_mask:0xf bank_mask:0xf\n\t" \
    "v_mov_b32_dpp v176, v168 quad_perm:[1,0,3,2] row_mask:0xf bank_mask:0xf\n\t" \
    "v_mov_b32_dpp v177, v169 quad_perm:[1,0,3,2] row_mask:0xf bank_mask:0xf\n\t" \
    "v_add_f32_dpp v173, v173, v173 quad_perm:[2,3,0,1] row_mask:0xf bank_mask:0xf\n\t" \
    "v_cndmask_b32 v168, v168, v177, s[48:49]\n\t" \
    "v_cndmask_b32 v169, v176, v169, s[48:49]\n\t" \
    "v_add_f32_dpp v173, v173, v173 row_half_mirror row_mask:0xf bank_mask:0xf\n\t" \
    "s_nop 1\n\t" \
    "v_add_f32_dpp v173, v173, v173 row_mirror row_mask:0xf bank_mask:0xf\n\t" \
    "v_rcp_f32 v173, v173\n\t" \
    "s_nop 0\n\t" \
    "v_pk_mul_f32 v[168:169], v[168:169], v[172:173] op_sel:[0,1] op_sel_hi:[1,1]\n\t" \
    "v_cvt_pk_bf16_f32 v176, v168, v169\n\t" \
    "global_store_dword v18, v176, s[46:47] sc1\n\t" \
    "global_store_dword v180, v176, s[46:47] sc1\n\t" \
    "global_store_dword v181, v176, s[46:47] sc1\n\t" \
    "global_store_dword v182, v176, s[46:47] sc1\n\t" \
    "v_pk_mul_f32 v[174:175], v[174:175], v[172:173] op_sel:[0,1] op_sel_hi:[1,1]\n\t" \
    "v_cvt_pk_bf16_f32 v177, v174, v175\n\t" \
    "global_store_dword v19, v177, s[46:47] sc1\n\t" \
    "s_cmp_eq_u32 s65, 63\n\t" \
    "s_cbranch_scc1 qk_last36_%=\n\t" \
    "v_readfirstlane_b32 s69, v25\n\t" \
    "s_add_u32 s65, s65, 1\n\t" \
    "s_max_i32 s83, s83, s69\n\t" \
    "s_cmp_gt_u32 s65, 6\n\t" \
    "s_cselect_b32 s83, 99, s83\n\t" \
    "v_mov_b32 v72, 0\n\t" \
    "v_mov_b32 v73, 0\n\t" \
    "v_mov_b32 v74, 0\n\t" \
    "v_mov_b32 v75, 0\n\t" \
    "v_mov_b32 v76, 0\n\t" \
    "v_mov_b32 v77, 0\n\t" \
    "v_mov_b32 v78, 0\n\t" \
    "v_mov_b32 v79, 0\n\t" \
    "v_mov_b32 v80, 0\n\t" \
    "v_mov_b32 v81, 0\n\t" \
    "v_mov_b32 v82, 0\n\t" \
    "v_mov_b32 v83, 0\n\t" \
    "v_mov_b32 v84, 0\n\t" \
    "v_mov_b32 v85, 0\n\t" \
    "v_mov_b32 v86, 0\n\t" \
    "v_mov_b32 v87, 0\n\t" \
    "v_mov_b32 v88, 0\n\t" \
    "v_mov_b32 v89, 0\n\t" \
    "v_mov_b32 v90, 0\n\t" \
    "v_mov_b32 v91, 0\n\t" \
    "v_mov_b32 v92, 0\n\t" \
    "v_mov_b32 v93, 0\n\t" \
    "v_mov_b32 v94, 0\n\t" \
    "v_mov_b32 v95, 0\n\t" \
    "v_mov_b32 v96, 0\n\t" \
    "v_mov_b32 v97, 0\n\t" \
    "v_mov_b32 v98, 0\n\t" \
    "v_mov_b32 v99, 0\n\t" \
    "s_add_u32 s69, s65, 0\n\t" \
    "s_and_b32 s69, s69, 7\n\t" \
    "s_lshl_b32 s69, s69, 18\n\t" \
    "s_add_u32 s69, s69, s62\n\t" \
    "s_add_u32 s46, s36, s69\n\t" \
    "s_addc_u32 s47, s37, 0\n\t" \
    "s_mul_i32 s70, s67, 7\n\t" \
    "s_add_u32 s69, s65, 0\n\t" \
    "s_xor_b32 s69, s69, s70\n\t" \
    "s_and_b32 s69, s69, 63\n\t" \
    "s_lshl_b32 s69, s69, 13\n\t" \
    "s_mul_i32 s70, s67, 524288\n\t" \
    "s_add_u32 s69, s69, s70\n\t" \
    "s_add_u32 s69, s69, 23068672\n\t" \
    "s_add_u32 s38, s36, s69\n\t" \
    "s_addc_u32 s39, s37, 0\n\t" \
    "qk_WF49_%=:\n\t" \
    "s_cmp_ge_i32 s83, s65\n\t" \
    "s_cbranch_scc1 qk_WFok50_%=\n\t" \
    "global_load_dword v160, v24, s[36:37] sc1\n\t" \
    "s_waitcnt vmcnt(0)\n\t" \
    "v_readfirstlane_b32 s83, v160\n\t" \
    "s_nop 1\n\t" \
    "s_cmp_ge_i32 s83, s65\n\t" \
    "s_cbranch_scc1 qk_WFok50_%=\n\t" \
    "s_cmp_eq_u32 s68, 0\n\t" \
    "s_cbranch_scc1 qk_F51_%=\n\t" \
    "s_sub_u32 s68, s68, 1\n\t" \
    "s_sleep 2\n\t" \
    "s_branch qk_WF49_%=\n\t" \
    "qk_F51_%=:\n\t" \
    "v_mov_b32 v160, 5767424\n\t" \
    "v_mov_b32 v161, 9\n\t" \
    "global_store_dword v160, v161, s[36:37] sc1\n\t" \
    "s_waitcnt vmcnt(0)\n\t" \
    "s_branch qk_WFok50_%=\n\t" \
    "qk_WFok50_%=:\n\t" \
    "global_load_dwordx4 v[100:103], v0, s[38:39] offset:0 sc1\n\t" \
    "global_load_dwordx4 v[104:107], v0, s[38:39] offset:2048 sc1\n\t" \
    "global_load_dwordx4 v[108:111], v15, s[38:39] offset:0 sc1\n\t" \
    "global_load_dwordx4 v[112:115], v15, s[38:39] offset:2048 sc1\n\t" \
    "global_load_dwordx4 v[116:119], v0, s[38:39] offset:1024 sc1\n\t" \
    "global_load_dwordx4 v[120:123], v0, s[38:39] offset:3072 sc1\n\t" \
    "global_load_dwordx4 v[124:127], v15, s[38:39] offset:1024 sc1\n\t" \
    "global_load_dwordx4 v[128:131], v15, s[38:39] offset:3072 sc1\n\t" \
    "s_cmp_gt_i32 s83, s65\n\t" \
    "s_cbranch_scc1 qk_nf52_%=\n\t" \
    "global_load_dword v25, v24, s[36:37] sc1\n\t" \
    "qk_nf52_%=:\n\t" \
    "s_barrier\n\t" \
    "ds_read_b32 v168, v11 offset:20480\n\t" \
    "s_waitcnt lgkmcnt(0)\n\t" \
    "v_readfirstlane_b32 s70, v168\n\t" \
    "ds_write_b32 v12, v31 offset:0\n\t" \
    "ds_write_b32 v12, v31 offset:64\n\t" \
    "s_branch qk_go37_%=\n\t" \
    "qk_Gbad42_%=:\n\t" \
    "s_waitcnt lgkmcnt(0)\n\t" \
    "s_nop 7\n\t" \
    "v_mov_b32 v72, 0\n\t" \
    "v_mov_b32 v73, 0\n\t" \
    "v_mov_b32 v74, 0\n\t" \
    "v_mov_b32 v75, 0\n\t" \
    "v_mov_b32 v76, 0\n\t" \
    "v_mov_b32 v77, 0\n\t" \
    "v_mov_b32 v78, 0\n\t" \
    "v_mov_b32 v79, 0\n\t" \
    "v_mov_b32 v80, 0\n\t" \
    "v_mov_b32 v81, 0\n\t" \
    "v_mov_b32 v82, 0\n\t" \
    "v_mov_b32 v83, 0\n\t" \
    "v_mov_b32 v84, 0\n\t" \
    "v_mov_b32 v85, 0\n\t" \
    "v_mov_b32 v86, 0\n\t" \
    "v_mov_b32 v87, 0\n\t" \
    "s_cmp_eq_u32 s68, 0\n\t" \
    "s_cbranch_scc1 qk_F53_%=\n\t" \
    "s_sub_u32 s68, s68, 1\n\t" \
    "s_branch qk_G41_%=\n\t" \
    "qk_F53_%=:\n\t" \
    "v_mov_b32 v160, 5767424\n\t" \
    "v_mov_b32 v161, 1\n\t" \
    "global_store_dword v160, v161, s[36:37] sc1\n\t" \
    "s_waitcnt vmcnt(0)\n\t" \
    "s_branch qk_Gc43_%=\n\t" \
    "qk_Gslow44_%=:\n\t" \
    "v_max_u32 v160, v36, v37\n\t" \
    "v_max3_u32 v160, v160, v38, v39\n\t" \
    "v_max3_u32 v160, v160, v40, v41\n\t" \
    "v_max3_u32 v160, v160, v42, v43\n\t" \
    "v_max3_u32 v160, v160, v44, v45\n\t" \
    "v_max3_u32 v160, v160, v46, v47\n\t" \
    "v_max3_u32 v160, v160, v48, v49\n\t" \
    "v_max3_u32 v160, v160, v50, v51\n\t" \
    "v_max3_u32 v160, v160, v52, v53\n\t" \
    "v_max3_u32 v160, v160, v54, v55\n\t" \
    "v_max3_u32 v160, v160, v56, v57\n\t" \
    "v_max3_u32 v160, v160, v58, v59\n\t" \
    "v_max3_u32 v160, v160, v60, v61\n\t" \
    "v_max3_u32 v160, v160, v62, v63\n\t" \
    "v_max3_u32 v160, v160, v64, v65\n\t" \
    "v_max3_u32 v160, v160, v66, v67\n\t" \
    "v_max3_u32 v160, v160, v68, v69\n\t" \
    "v_max3_u32 v160, v160, v70, v71\n\t" \
    "v_cmp_eq_u32 vcc, -1, v160\n\t" \
    "s_cbranch_vccz qk_Gback45_%=\n\t" \
    "s_waitcnt lgkmcnt(0)\n\t" \
    "v_sub_u32 v160, v1, v3\n\t" \
    "s_nop 0\n\t" \
    "v_readfirstlane_b32 s69, v160\n\t" \
    "v_add_u32 v163, 20480, v0\n\t" \
    "s_nop 7\n\t" \
    "s_add_u32 s30, s46, s69\n\t" \
    "s_addc_u32 s31, s47, 0\n\t" \
    "qk_Gag54_%=:\n\t" \
    "global_load_dwordx4 v[36:39], v1, s[46:47] sc1\n\t" \
    "global_load_dwordx4 v[40:43], v23, s[46:47] sc1\n\t" \
    "global_load_dwordx4 v[44:47], v163, s[30:31] sc1\n\t" \
    "global_load_dwordx4 v[48:51], v4, s[30:31] sc1\n\t" \
    "global_load_dwordx4 v[52:55], v5, s[30:31] sc1\n\t" \
    "global_load_dwordx4 v[56:59], v6, s[30:31] sc1\n\t" \
    "global_load_dwordx4 v[60:63], v7, s[30:31] sc1\n\t" \
    "global_load_dwordx4 v[64:67], v8, s[30:31] sc1\n\t" \
    "global_load_dwordx4 v[68:71], v9, s[30:31] sc1\n\t" \
    "s_waitcnt vmcnt(0)\n\t" \
    "v_max_u32 v160, v36, v37\n\t" \
    "v_max3_u32 v160, v160, v38, v39\n\t" \
    "v_max3_u32 v160, v160, v40, v41\n\t" \
    "v_max3_u32 v160, v160, v42, v43\n\t" \
    "v_max3_u32 v160, v160, v44, v45\n\t" \
    "v_max3_u32 v160, v160, v46, v47\n\t" \
    "v_max3_u32 v160, v160, v48, v49\n\t" \
    "v_max3_u32 v160, v160, v50, v51\n\t" \
    "v_max3_u32 v160, v160, v52, v53\n\t" \
    "v_max3_u32 v160, v160, v54, v55\n\t" \
    "v_max3_u32 v160, v160, v56, v57\n\t" \
    "v_max3_u32 v160, v160, v58, v59\n\t" \
    "v_max3_u32 v160, v160, v60, v61\n\t" \
    "v_max3_u32 v160, v160, v62, v63\n\t" \
    "v_max3_u32 v160, v160, v64, v65\n\t" \
    "v_max3_u32 v160, v160, v66, v67\n\t" \
    "v_max3_u32 v160, v160, v68, v69\n\t" \
    "v_max3_u32 v160, v160, v70, v71\n\t" \
    "v_cmp_eq_u32 vcc, -1, v160\n\t" \
    "s_cbranch_vccz qk_Gredo55_%=\n\t" \
    "s_cmp_eq_u32 s68, 0\n\t" \
    "s_cbranch_scc1 qk_F56_%=\n\t" \
    "s_sub_u32 s68, s68, 1\n\t" \
    "s_sleep 1\n\t" \
    "s_branch qk_Gag54_%=\n\t" \
    "qk_F56_%=:\n\t" \
    "v_mov_b32 v160, 5767424\n\t" \
    "v_mov_b32 v161, 2\n\t" \
    "global_store_dword v160, v161, s[36:37] sc1\n\t" \
    "s_waitcnt vmcnt(0)\n\t" \
    "s_branch qk_Gredo55_%=\n\t" \
    "qk_Gredo55_%=:\n\t" \
    "v_mov_b32 v72, 0\n\t" \
    "v_mov_b32 v73, 0\n\t" \
    "v_mov_b32 v74, 0\n\t" \
    "v_mov_b32 v75, 0\n\t" \
    "v_mov_b32 v76, 0\n\t" \
    "v_mov_b32 v77, 0\n\t" \
    "v_mov_b32 v78, 0\n\t" \
    "v_mov_b32 v79, 0\n\t" \
    "v_mov_b32 v80, 0\n\t" \
    "v_mov_b32 v81, 0\n\t" \
    "v_mov_b32 v82, 0\n\t" \
    "v_mov_b32 v83, 0\n\t" \
    "v_mov_b32 v84, 0\n\t" \
    "v_mov_b32 v85, 0\n\t" \
    "v_mov_b32 v86, 0\n\t" \
    "v_mov_b32 v87, 0\n\t" \
    "v_smfmac_f32_16x16x64_bf16 v[72:75], v[36:39], a[16:23], v10\n\t" \
    "v_smfmac_f32_16x16x64_bf16 v[72:75], v[40:43], a[24:31], v10\n\t" \
    "v_smfmac_f32_16x16x64_bf16 v[72:75], v[48:51], a[32:39], v10\n\t" \
    "v_smfmac_f32_16x16x64_bf16 v[72:75], v[52:55], a[40:47], v10\n\t" \
    "v_smfmac_f32_16x16x64_bf16 v[72:75], v[56:59], a[48:55], v10\n\t" \
    "v_smfmac_f32_16x16x64_bf16 v[72:75], v[60:63], a[56:63], v10\n\t" \
    "v_smfmac_f32_16x16x64_bf16 v[72:75], v[64:67], a[0:7], v10\n\t" \
    "v_smfmac_f32_16x16x64_bf16 v[72:75], v[68:71], a[8:15], v10\n\t" \
    "v_smfmac_f32_16x16x64_bf16 v[72:75], v[44:47], a[64:71], v10\n\t" \
    "v_smfmac_f32_16x16x64_bf16 v[76:79], v[36:39], a[88:95], v10\n\t" \
    "v_smfmac_f32_16x16x64_bf16 v[76:79], v[40:43], a[96:103], v10\n\t" \
    "v_smfmac_f32_16x16x64_bf16 v[76:79], v[48:51], a[104:111], v10\n\t" \
    "v_smfmac_f32_16x16x64_bf16 v[76:79], v[52:55], a[112:119], v10\n\t" \
    "v_smfmac_f32_16x16x64_bf16 v[76:79], v[56:59], a[120:127], v10\n\t" \
    "v_smfmac_f32_16x16x64_bf16 v[76:79], v[60:63], a[128:135], v10\n\t" \
    "v_smfmac_f32_16x16x64_bf16 v[76:79], v[64:67], a[72:79], v10\n\t" \
    "v_smfmac_f32_16x16x64_bf16 v[76:79], v[68:71], a[80:87], v10\n\t" \
    "v_smfmac_f32_16x16x64_bf16 v[76:79], v[44:47], a[136:143], v10\n\t" \
    "v_smfmac_f32_16x16x64_bf16 v[80:83], v[36:39], a[160:167], v10\n\t" \
    "v_smfmac_f32_16x16x64_bf16 v[80:83], v[40:43], a[168:175], v10\n\t" \
    "v_smfmac_f32_16x16x64_bf16 v[80:83], v[48:51], a[176:183], v10\n\t" \
    "v_smfmac_f32_16x16x64_bf16 v[80:83], v[52:55], a[184:191], v10\n\t" \
    "v_smfmac_f32_16x16x64_bf16 v[80:83], v[56:59], a[192:199], v10\n\t" \
    "v_smfmac_f32_16x16x64_bf16 v[80:83], v[60:63], a[200:207], v10\n\t" \
    "v_smfmac_f32_16x16x64_bf16 v[80:83], v[64:67], a[144:151], v10\n\t" \
    "v_smfmac_f32_16x16x64_bf16 v[80:83], v[68:71], a[152:159], v10\n\t" \
    "v_smfmac_f32_16x16x64_bf16 v[80:83], v[44:47], a[208:215], v10\n\t" \
    "v_smfmac_f32_16x16x64_bf16 v[84:87], v[36:39], a[232:239], v10\n\t" \
    "v_smfmac_f32_16x16x64_bf16 v[84:87], v[40:43], a[240:247], v10\n\t" \
    "v_smfmac_f32_16x16x64_bf16 v[84:87], v[48:51], a[248:255], v10\n\t" \
    "v_smfmac_f32_16x16x64_bf16 v[84:87], v[52:55], v[224:231], v10\n\t" \
    "v_smfmac_f32_16x16x64_bf16 v[84:87], v[56:59], v[232:239], v10\n\t" \
    "v_smfmac_f32_16x16x64_bf16 v[84:87], v[60:63], v[240:247], v10\n\t" \
    "v_smfmac_f32_16x16x64_bf16 v[84:87], v[64:67], a[216:223], v10\n\t" \
    "v_smfmac_f32_16x16x64_bf16 v[84:87], v[68:71], a[224:231], v10\n\t" \
    "v_smfmac_f32_16x16x64_bf16 v[84:87], v[44:47], v[248:255], v10\n\t" \
    "v_add_f32 v72, v72, v74\n\t" \
    "v_add_f32 v73, v73, v75\n\t" \
    "v_cvt_pk_bf16_f32 v156, v72, v73\n\t" \
    "ds_write_b32 v12, v156 offset:0\n\t" \
    "v_add_f32 v76, v76, v78\n\t" \
    "v_add_f32 v77, v77, v79\n\t" \
    "v_cvt_pk_bf16_f32 v157, v76, v77\n\t" \
    "ds_write_b32 v12, v157 offset:64\n\t" \
    "v_add_f32 v80, v80, v82\n\t" \
    "v_add_f32 v81, v81, v83\n\t" \
    "v_cvt_pk_bf16_f32 v158, v80, v81\n\t" \
    "ds_write_b32 v16, v158 offset:0\n\t" \
    "v_pk_add_f32 v[84:85], v[84:85], v[86:87]\n\t" \
    "v_cvt_pk_bf16_f32 v159, v84, v85\n\t" \
    "ds_write_b32 v16, v159 offset:256\n\t" \
    "s_branch qk_Gback45_%=\n\t" \
    "qk_qs46_%=:\n\t" \
    "s_cmp_eq_u32 s91, s65\n\t" \
    "s_cbranch_scc1 qk_qb248_%=\n\t" \
    "s_mov_b32 s91, s65\n\t" \
    "s_nop 7\n\t" \
    "ds_read_b64_tr_b16 v[132:133], v13 offset:0\n\t" \
    "ds_read_b64_tr_b16 v[134:135], v13 offset:128\n\t" \
    "ds_read_b64_tr_b16 v[136:137], v13 offset:1024\n\t" \
    "ds_read_b64_tr_b16 v[138:139], v13 offset:1152\n\t" \
    "v_mov_b32 v88, 0\n\t" \
    "v_mov_b32 v89, 0\n\t" \
    "v_mov_b32 v90, 0\n\t" \
    "v_mov_b32 v91, 0\n\t" \
    "v_mov_b32 v92, 0\n\t" \
    "v_mov_b32 v93, 0\n\t" \
    "v_mov_b32 v94, 0\n\t" \
    "v_mov_b32 v95, 0\n\t" \
    "v_mov_b32 v96, 0\n\t" \
    "v_mov_b32 v97, 0\n\t" \
    "v_mov_b32 v98, 0\n\t" \
    "v_mov_b32 v99, 0\n\t" \
    "s_waitcnt lgkmcnt(0)\n\t" \
    "v_smfmac_f32_16x16x64_bf16 v[88:91], v[132:135], v[100:107], v10\n\t" \
    "v_smfmac_f32_16x16x64_bf16 v[92:95], v[132:135], v[116:123], v10\n\t" \
    "v_smfmac_f32_16x16x64_bf16 v[88:91], v[136:139], v[108:115], v10\n\t" \
    "v_smfmac_f32_16x16x64_bf16 v[92:95], v[136:139], v[124:131], v10\n\t" \
    "s_nop 7\n\t" \
    "s_branch qk_qb47_%=\n\t" \
    "qk_last36_%=:\n\t" \
    "s_endpgm\n\t" \
    "qk_loop13_%=:\n\t" \
    "v_mov_b32 v72, 0\n\t" \
    "v_mov_b32 v73, 0\n\t" \
    "v_mov_b32 v74, 0\n\t" \
    "v_mov_b32 v75, 0\n\t" \
    "v_mov_b32 v76, 0\n\t" \
    "v_mov_b32 v77, 0\n\t" \
    "v_mov_b32 v78, 0\n\t" \
    "v_mov_b32 v79, 0\n\t" \
    "v_mov_b32 v80, 0\n\t" \
    "v_mov_b32 v81, 0\n\t" \
    "v_mov_b32 v82, 0\n\t" \
    "v_mov_b32 v83, 0\n\t" \
    "v_mov_b32 v84, 0\n\t" \
    "v_mov_b32 v85, 0\n\t" \
    "v_mov_b32 v86, 0\n\t" \
    "v_mov_b32 v87, 0\n\t" \
    "s_add_u32 s69, s65, 0\n\t" \
    "s_and_b32 s69, s69, 7\n\t" \
    "s_lshl_b32 s69, s69, 18\n\t" \
    "s_add_u32 s69, s69, s62\n\t" \
    "s_add_u32 s46, s36, s69\n\t" \
    "s_addc_u32 s47, s37, 0\n\t" \
    "s_mul_i32 s70, s67, 7\n\t" \
    "s_add_u32 s69, s65, 0\n\t" \
    "s_xor_b32 s69, s69, s70\n\t" \
    "s_and_b32 s69, s69, 63\n\t" \
    "s_lshl_b32 s69, s69, 12\n\t" \
    "s_mul_i32 s70, s67, 262144\n\t" \
    "s_add_u32 s69, s69, s70\n\t" \
    "s_add_u32 s69, s69, 39845888\n\t" \
    "s_add_u32 s40, s36, s69\n\t" \
    "s_addc_u32 s41, s37, 0\n\t" \
    "s_sub_u32 s91, s65, 1\n\t" \
    "v_mov_b32 v25, s91\n\t" \
    "s_sub_u32 s70, s79, s74\n\t" \
    "s_max_i32 s70, s70, 0\n\t" \
    "s_min_i32 s70, s70, 16000\n\t" \
    "s_lshl_b32 s70, s70, 1\n\t" \
    "s_mul_hi_u32 s70, s70, s77\n\t" \
    "s_add_u32 s70, s70, s73\n\t" \
    ".p2align 6\n\t" \
    "s_nop 0\n\t" \
    "s_nop 0\n\t" \
    "s_nop 0\n\t" \
    "s_nop 0\n\t" \
    "s_nop 0\n\t" \
    "s_nop 0\n\t" \
    "qk_go58_%=:\n\t" \
    "s_getpc_b64 s[30:31]\n\t" \
    "qk_Wp62_%=:\n\t" \
    "s_add_u32 s30, s30, qk_We63_%=-qk_Wp62_%=\n\t" \
    "s_addc_u32 s31, s31, 0\n\t" \
    "s_sub_u32 s70, s70, 96\n\t" \
    "qk_W61_%=:\n\t" \
    "s_memtime s[56:57]\n\t" \
    "s_waitcnt lgkmcnt(0)\n\t" \
    "s_sub_u32 s69, s70, s56\n\t" \
    "s_cmp_gt_i32 s69, 80\n\t" \
    "s_cbranch_scc1 qk_W61_%=\n\t" \
    "s_max_i32 s69, s69, 0\n\t" \
    "s_and_b32 s69, s69, -4\n\t" \
    "s_sub_u32 s30, s30, s69\n\t" \
    "s_subb_u32 s31, s31, 0\n\t" \
    "s_setpc_b64 s[30:31]\n\t" \
    "s_nop 0\n\t" \
    "s_nop 0\n\t" \
    "s_nop 0\n\t" \
    "s_nop 0\n\t" \
    "s_nop 0\n\t" \
    "s_nop 0\n\t" \
    "s_nop 0\n\t" \
    "s_nop 0\n\t" \
    "s_nop 0\n\t" \
    "s_nop 0\n\t" \
    "s_nop 0\n\t" \
    "s_nop 0\n\t" \
    "s_nop 0\n\t" \
    "s_nop 0\n\t" \
    "s_nop 0\n\t" \
    "s_nop 0\n\t" \
    "s_nop 0\n\t" \
    "s_nop 0\n\t" \
    "s_nop 0\n\t" \
    "s_nop 0\n\t" \
    "qk_We63_%=:\n\t" \
    "qk_G64_%=:\n\t" \
    "global_load_dwordx4 v[36:39], v1, s[46:47] sc1\n\t" \
    "global_load_dword v161, v2, s[46:47] sc1\n\t" \
    "global_load_dwordx4 v[40:43], v23, s[46:47] sc1\n\t" \
    "s_waitcnt vmcnt(2)\n\t" \
    "ds_write_b128 v3, v[36:39]\n\t" \
    "v_smfmac_f32_16x16x64_bf16 v[72:75], v[36:39], a[32:39], v10\n\t" \
    "s_waitcnt vmcnt(1)\n\t" \
    "ds_write_b32 v22, v161 offset:19968\n\t" \
    "v_smfmac_f32_16x16x64_bf16 v[76:79], v[36:39], a[104:111], v10\n\t" \
    "s_waitcnt vmcnt(0)\n\t" \
    "ds_write_b128 v3, v[40:43] offset:1024\n\t" \
    "v_max3_u32 v160, v36, v40, v161\n\t" \
    "v_cmp_eq_u32 vcc, -1, v160\n\t" \
    "v_smfmac_f32_16x16x64_bf16 v[80:83], v[36:39], a[176:183], v10\n\t" \
    "v_smfmac_f32_16x16x64_bf16 v[84:87], v[36:39], a[248:255], v10\n\t" \
    "v_smfmac_f32_16x16x64_bf16 v[72:75], v[40:43], a[40:47], v10\n\t" \
    "s_cbranch_vccnz qk_Gbad65_%=\n\t" \
    "qk_Gc66_%=:\n\t" \
    "s_cmp_ge_u32 s91, 57\n\t" \
    "s_cbranch_scc1 qk_kf59_%=\n\t" \
    "qk_kb60_%=:\n\t" \
    "s_waitcnt lgkmcnt(0)\n\t" \
    "s_barrier\n\t" \
    "ds_read_b128 v[48:51], v4\n\t" \
    "ds_read_b128 v[52:55], v5\n\t" \
    "v_smfmac_f32_16x16x64_bf16 v[76:79], v[40:43], a[112:119], v10\n\t" \
    "ds_read_b128 v[56:59], v6\n\t" \
    "ds_read_b128 v[60:63], v7\n\t" \
    "v_smfmac_f32_16x16x64_bf16 v[80:83], v[40:43], a[184:191], v10\n\t" \
    "ds_read_b128 v[64:67], v8\n\t" \
    "v_smfmac_f32_16x16x64_bf16 v[84:87], v[40:43], v[224:231], v10\n\t" \
    "ds_read_b128 v[68:71], v9\n\t" \
    "s_waitcnt lgkmcnt(5)\n\t" \
    "v_smfmac_f32_16x16x64_bf16 v[72:75], v[48:51], a[48:55], v10\n\t" \
    "ds_read_b128 v[44:47], v0 offset:19456\n\t" \
    "s_waitcnt lgkmcnt(5)\n\t" \
    "v_smfmac_f32_16x16x64_bf16 v[72:75], v[52:55], a[56:63], v10\n\t" \
    "s_waitcnt lgkmcnt(4)\n\t" \
    "v_smfmac_f32_16x16x64_bf16 v[72:75], v[56:59], a[0:7], v10\n\t" \
    "s_waitcnt lgkmcnt(3)\n\t" \
    "v_smfmac_f32_16x16x64_bf16 v[72:75], v[60:63], a[8:15], v10\n\t" \
    "s_waitcnt lgkmcnt(2)\n\t" \
    "v_smfmac_f32_16x16x64_bf16 v[72:75], v[64:67], a[16:23], v10\n\t" \
    "s_waitcnt lgkmcnt(1)\n\t" \
    "v_smfmac_f32_16x16x64_bf16 v[72:75], v[68:71], a[24:31], v10\n\t" \
    "s_waitcnt lgkmcnt(0)\n\t" \
    "v_smfmac_f32_16x16x64_bf16 v[72:75], v[44:47], a[64:71], v10\n\t" \
    "s_nop 2\n\t" \
    "v_smfmac_f32_16x16x64_bf16 v[76:79], v[48:51], a[120:127], v10\n\t" \
    "s_nop 2\n\t" \
    "v_smfmac_f32_16x16x64_bf16 v[76:79], v[52:55], a[128:135], v10\n\t" \
    "v_add_f32 v72, v72, v74\n\t" \
    "v_smfmac_f32_16x16x64_bf16 v[76:79], v[56:59], a[72:79], v10\n\t" \
    "v_add_f32 v73, v73, v75\n\t" \
    "v_smfmac_f32_16x16x64_bf16 v[76:79], v[60:63], a[80:87], v10\n\t" \
    "v_cvt_pk_bf16_f32 v156, v72, v73\n\t" \
    "v_smfmac_f32_16x16x64_bf16 v[76:79], v[64:67], a[88:95], v10\n\t" \
    "ds_write_b32 v12, v156 offset:0\n\t" \
    "v_smfmac_f32_16x16x64_bf16 v[76:79], v[68:71], a[96:103], v10\n\t" \
    "v_smfmac_f32_16x16x64_bf16 v[76:79], v[44:47], a[136:143], v10\n\t" \
    "s_nop 2\n\t" \
    "v_smfmac_f32_16x16x64_bf16 v[80:83], v[48:51], a[192:199], v10\n\t" \
    "s_nop 2\n\t" \
    "v_smfmac_f32_16x16x64_bf16 v[80:83], v[52:55], a[200:207], v10\n\t" \
    "v_add_f32 v76, v76, v78\n\t" \
    "v_smfmac_f32_16x16x64_bf16 v[80:83], v[56:59], a[144:151], v10\n\t" \
    "v_add_f32 v77, v77, v79\n\t" \
    "v_smfmac_f32_16x16x64_bf16 v[80:83], v[60:63], a[152:159], v10\n\t" \
    "v_cvt_pk_bf16_f32 v157, v76, v77\n\t" \
    "v_smfmac_f32_16x16x64_bf16 v[80:83], v[64:67], a[160:167], v10\n\t" \
    "ds_write_b32 v12, v157 offset:64\n\t" \
    "v_smfmac_f32_16x16x64_bf16 v[80:83], v[68:71], a[168:175], v10\n\t" \
    "v_smfmac_f32_16x16x64_bf16 v[80:83], v[44:47], a[208:215], v10\n\t" \
    "s_nop 2\n\t" \
    "v_smfmac_f32_16x16x64_bf16 v[84:87], v[48:51], v[232:239], v10\n\t" \
    "s_nop 2\n\t" \
    "v_smfmac_f32_16x16x64_bf16 v[84:87], v[52:55], v[240:247], v10\n\t" \
    "v_add_f32 v80, v80, v82\n\t" \
    "v_smfmac_f32_16x16x64_bf16 v[84:87], v[56:59], a[216:223], v10\n\t" \
    "v_add_f32 v81, v81, v83\n\t" \
    "v_smfmac_f32_16x16x64_bf16 v[84:87], v[60:63], a[224:231], v10\n\t" \
    "v_cvt_pk_bf16_f32 v158, v80, v81\n\t" \
    "v_smfmac_f32_16x16x64_bf16 v[84:87], v[64:67], a[232:239], v10\n\t" \
    "ds_write_b32 v16, v158 offset:0\n\t" \
    "v_smfmac_f32_16x16x64_bf16 v[84:87], v[68:71], a[240:247], v10\n\t" \
    "v_smfmac_f32_16x16x64_bf16 v[84:87], v[44:47], v[248:255], v10\n\t" \
    "s_nop 6\n\t" \
    "v_cmp_u_f32 vcc, v72, v73\n\t" \
    "v_pk_add_f32 v[84:85], v[84:85], v[86:87]\n\t" \
    "v_cvt_pk_bf16_f32 v159, v84, v85\n\t" \
    "ds_write_b32 v16, v159 offset:256\n\t" \
    "s_cbranch_vccnz qk_Gslow67_%=\n\t" \
    "qk_Gback68_%=:\n\t" \
    "s_waitcnt lgkmcnt(0)\n\t" \
    "s_barrier\n\t" \
    "s_sleep 1\n\t" \
    "ds_read_b64_tr_b16 v[140:141], v17 offset:0\n\t" \
    "ds_read_b64_tr_b16 v[142:143], v17 offset:256\n\t" \
    "ds_read_b64_tr_b16 v[144:145], v17 offset:512\n\t" \
    "ds_read_b64_tr_b16 v[146:147], v17 offset:768\n\t" \
    "ds_read_b64_tr_b16 v[148:149], v17 offset:1024\n\t" \
    "ds_read_b64_tr_b16 v[150:151], v17 offset:1280\n\t" \
    "ds_read_b64_tr_b16 v[152:153], v17 offset:1536\n\t" \
    "ds_read_b64_tr_b16 v[154:155], v17 offset:1792\n\t" \
    "s_waitcnt lgkmcnt(0)\n\t" \
    "s_cmp_eq_u32 s65, 63\n\t" \
    "s_cbranch_scc1 qk_last57_%=\n\t" \
    "s_cmp_ge_u32 s65, 58\n\t" \
    "s_cbranch_scc1 qk_nsl69_%=\n\t" \
    "s_sleep 6\n\t" \
    "global_store_dword v14, v25, s[36:37] sc1\n\t" \
    "qk_nsl69_%=:\n\t" \
    "s_mov_b64 exec, s[58:59]\n\t" \
    "global_store_dwordx4 v0, v[140:143], s[40:41] offset:0 sc1\n\t" \
    "global_store_dwordx4 v0, v[144:147], s[40:41] offset:1024 sc1\n\t" \
    "global_store_dwordx4 v0, v[148:151], s[40:41] offset:2048 sc1\n\t" \
    "global_store_dwordx4 v0, v[152:155], s[40:41] offset:3072 sc1\n\t" \
    "s_mov_b64 exec, -1\n\t" \
    "s_add_u32 s65, s65, 1\n\t" \
    "v_mov_b32 v72, 0\n\t" \
    "v_mov_b32 v73, 0\n\t" \
    "v_mov_b32 v74, 0\n\t" \
    "v_mov_b32 v75, 0\n\t" \
    "v_mov_b32 v76, 0\n\t" \
    "v_mov_b32 v77, 0\n\t" \
    "v_mov_b32 v78, 0\n\t" \
    "v_mov_b32 v79, 0\n\t" \
    "v_mov_b32 v80, 0\n\t" \
    "v_mov_b32 v81, 0\n\t" \
    "v_mov_b32 v82, 0\n\t" \
    "v_mov_b32 v83, 0\n\t" \
    "v_mov_b32 v84, 0\n\t" \
    "v_mov_b32 v85, 0\n\t" \
    "v_mov_b32 v86, 0\n\t" \
    "v_mov_b32 v87, 0\n\t" \
    "s_add_u32 s69, s65, 0\n\t" \
    "s_and_b32 s69, s69, 7\n\t" \
    "s_lshl_b32 s69, s69, 18\n\t" \
    "s_add_u32 s69, s69, s62\n\t" \
    "s_add_u32 s46, s36, s69\n\t" \
    "s_addc_u32 s47, s37, 0\n\t" \
    "s_mul_i32 s70, s67, 7\n\t" \
    "s_add_u32 s69, s65, 0\n\t" \
    "s_xor_b32 s69, s69, s70\n\t" \
    "s_and_b32 s69, s69, 63\n\t" \
    "s_lshl_b32 s69, s69, 12\n\t" \
    "s_mul_i32 s70, s67, 262144\n\t" \
    "s_add_u32 s69, s69, s70\n\t" \
    "s_add_u32 s69, s69, 39845888\n\t" \
    "s_add_u32 s40, s36, s69\n\t" \
    "s_addc_u32 s41, s37, 0\n\t" \
    "s_sub_u32 s91, s65, 1\n\t" \
    "v_mov_b32 v25, s91\n\t" \
    "s_barrier\n\t" \
    "ds_read_b32 v168, v11 offset:20480\n\t" \
    "s_waitcnt lgkmcnt(0)\n\t" \
    "v_readfirstlane_b32 s70, v168\n\t" \
    "ds_write_b32 v12, v31 offset:0\n\t" \
    "ds_write_b32 v12, v31 offset:64\n\t" \
    "s_branch qk_go58_%=\n\t" \
    "qk_Gbad65_%=:\n\t" \
    "s_waitcnt lgkmcnt(0)\n\t" \
    "s_nop 7\n\t" \
    "v_mov_b32 v72, 0\n\t" \
    "v_mov_b32 v73, 0\n\t" \
    "v_mov_b32 v74, 0\n\t" \
    "v_mov_b32 v75, 0\n\t" \
    "v_mov_b32 v76, 0\n\t" \
    "v_mov_b32 v77, 0\n\t" \
    "v_mov_b32 v78, 0\n\t" \
    "v_mov_b32 v79, 0\n\t" \
    "v_mov_b32 v80, 0\n\t" \
    "v_mov_b32 v81, 0\n\t" \
    "v_mov_b32 v82, 0\n\t" \
    "v_mov_b32 v83, 0\n\t" \
    "v_mov_b32 v84, 0\n\t" \
    "v_mov_b32 v85, 0\n\t" \
    "v_mov_b32 v86, 0\n\t" \
    "v_mov_b32 v87, 0\n\t" \
    "s_cmp_eq_u32 s68, 0\n\t" \
    "s_cbranch_scc1 qk_F70_%=\n\t" \
    "s_sub_u32 s68, s68, 1\n\t" \
    "s_branch qk_G64_%=\n\t" \
    "qk_F70_%=:\n\t" \
    "v_mov_b32 v160, 5767424\n\t" \
    "v_mov_b32 v161, 1\n\t" \
    "global_store_dword v160, v161, s[36:37] sc1\n\t" \
    "s_waitcnt vmcnt(0)\n\t" \
    "s_branch qk_Gc66_%=\n\t" \
    "qk_Gslow67_%=:\n\t" \
    "v_max_u32 v160, v36, v37\n\t" \
    "v_max3_u32 v160, v160, v38, v39\n\t" \
    "v_max3_u32 v160, v160, v40, v41\n\t" \
    "v_max3_u32 v160, v160, v42, v43\n\t" \
    "v_max3_u32 v160, v160, v44, v45\n\t" \
    "v_max3_u32 v160, v160, v46, v47\n\t" \
    "v_max3_u32 v160, v160, v48, v49\n\t" \
    "v_max3_u32 v160, v160, v50, v51\n\t" \
    "v_max3_u32 v160, v160, v52, v53\n\t" \
    "v_max3_u32 v160, v160, v54, v55\n\t" \
    "v_max3_u32 v160, v160, v56, v57\n\t" \
    "v_max3_u32 v160, v160, v58, v59\n\t" \
    "v_max3_u32 v160, v160, v60, v61\n\t" \
    "v_max3_u32 v160, v160, v62, v63\n\t" \
    "v_max3_u32 v160, v160, v64, v65\n\t" \
    "v_max3_u32 v160, v160, v66, v67\n\t" \
    "v_max3_u32 v160, v160, v68, v69\n\t" \
    "v_max3_u32 v160, v160, v70, v71\n\t" \
    "v_cmp_eq_u32 vcc, -1, v160\n\t" \
    "s_cbranch_vccz qk_Gback68_%=\n\t" \
    "s_waitcnt lgkmcnt(0)\n\t" \
    "v_sub_u32 v160, v1, v3\n\t" \
    "s_nop 0\n\t" \
    "v_readfirstlane_b32 s69, v160\n\t" \
    "v_add_u32 v163, 20480, v0\n\t" \
    "s_nop 7\n\t" \
    "s_add_u32 s30, s46, s69\n\t" \
    "s_addc_u32 s31, s47, 0\n\t" \
    "qk_Gag71_%=:\n\t" \
    "global_load_dwordx4 v[36:39], v1, s[46:47] sc1\n\t" \
    "global_load_dwordx4 v[40:43], v23, s[46:47] sc1\n\t" \
    "global_load_dwordx4 v[44:47], v163, s[30:31] sc1\n\t" \
    "global_load_dwordx4 v[48:51], v4, s[30:31] sc1\n\t" \
    "global_load_dwordx4 v[52:55], v5, s[30:31] sc1\n\t" \
    "global_load_dwordx4 v[56:59], v6, s[30:31] sc1\n\t" \
    "global_load_dwordx4 v[60:63], v7, s[30:31] sc1\n\t" \
    "global_load_dwordx4 v[64:67], v8, s[30:31] sc1\n\t" \
    "global_load_dwordx4 v[68:71], v9, s[30:31] sc1\n\t" \
    "s_waitcnt vmcnt(0)\n\t" \
    "v_max_u32 v160, v36, v37\n\t" \
    "v_max3_u32 v160, v160, v38, v39\n\t" \
    "v_max3_u32 v160, v160, v40, v41\n\t" \
    "v_max3_u32 v160, v160, v42, v43\n\t" \
    "v_max3_u32 v160, v160, v44, v45\n\t" \
    "v_max3_u32 v160, v160, v46, v47\n\t" \
    "v_max3_u32 v160, v160, v48, v49\n\t" \
    "v_max3_u32 v160, v160, v50, v51\n\t" \
    "v_max3_u32 v160, v160, v52, v53\n\t" \
    "v_max3_u32 v160, v160, v54, v55\n\t" \
    "v_max3_u32 v160, v160, v56, v57\n\t" \
    "v_max3_u32 v160, v160, v58, v59\n\t" \
    "v_max3_u32 v160, v160, v60, v61\n\t" \
    "v_max3_u32 v160, v160, v62, v63\n\t" \
    "v_max3_u32 v160, v160, v64, v65\n\t" \
    "v_max3_u32 v160, v160, v66, v67\n\t" \
    "v_max3_u32 v160, v160, v68, v69\n\t" \
    "v_max3_u32 v160, v160, v70, v71\n\t" \
    "v_cmp_eq_u32 vcc, -1, v160\n\t" \
    "s_cbranch_vccz qk_Gredo72_%=\n\t" \
    "s_cmp_eq_u32 s68, 0\n\t" \
    "s_cbranch_scc1 qk_F73_%=\n\t" \
    "s_sub_u32 s68, s68, 1\n\t" \
    "s_sleep 1\n\t" \
    "s_branch qk_Gag71_%=\n\t" \
    "qk_F73_%=:\n\t" \
    "v_mov_b32 v160, 5767424\n\t" \
    "v_mov_b32 v161, 2\n\t" \
    "global_store_dword v160, v161, s[36:37] sc1\n\t" \
    "s_waitcnt vmcnt(0)\n\t" \
    "s_branch qk_Gredo72_%=\n\t" \
    "qk_Gredo72_%=:\n\t" \
    "v_mov_b32 v72, 0\n\t" \
    "v_mov_b32 v73, 0\n\t" \
    "v_mov_b32 v74, 0\n\t" \
    "v_mov_b32 v75, 0\n\t" \
    "v_mov_b32 v76, 0\n\t" \
    "v_mov_b32 v77, 0\n\t" \
    "v_mov_b32 v78, 0\n\t" \
    "v_mov_b32 v79, 0\n\t" \
    "v_mov_b32 v80, 0\n\t" \
    "v_mov_b32 v81, 0\n\t" \
    "v_mov_b32 v82, 0\n\t" \
    "v_mov_b32 v83, 0\n\t" \
    "v_mov_b32 v84, 0\n\t" \
    "v_mov_b32 v85, 0\n\t" \
    "v_mov_b32 v86, 0\n\t" \
    "v_mov_b32 v87, 0\n\t" \
    "v_smfmac_f32_16x16x64_bf16 v[72:75], v[36:39], a[32:39], v10\n\t" \
    "v_smfmac_f32_16x16x64_bf16 v[72:75], v[40:43], a[40:47], v10\n\t" \
    "v_smfmac_f32_16x16x64_bf16 v[72:75], v[48:51], a[48:55], v10\n\t" \
    "v_smfmac_f32_16x16x64_bf16 v[72:75], v[52:55], a[56:63], v10\n\t" \
    "v_smfmac_f32_16x16x64_bf16 v[72:75], v[56:59], a[0:7], v10\n\t" \
    "v_smfmac_f32_16x16x64_bf16 v[72:75], v[60:63], a[8:15], v10\n\t" \
    "v_smfmac_f32_16x16x64_bf16 v[72:75], v[64:67], a[16:23], v10\n\t" \
    "v_smfmac_f32_16x16x64_bf16 v[72:75], v[68:71], a[24:31], v10\n\t" \
    "v_smfmac_f32_16x16x64_bf16 v[72:75], v[44:47], a[64:71], v10\n\t" \
    "v_smfmac_f32_16x16x64_bf16 v[76:79], v[36:39], a[104:111], v10\n\t" \
    "v_smfmac_f32_16x16x64_bf16 v[76:79], v[40:43], a[112:119], v10\n\t" \
    "v_smfmac_f32_16x16x64_bf16 v[76:79], v[48:51], a[120:127], v10\n\t" \
    "v_smfmac_f32_16x16x64_bf16 v[76:79], v[52:55], a[128:135], v10\n\t" \
    "v_smfmac_f32_16x16x64_bf16 v[76:79], v[56:59], a[72:79], v10\n\t" \
    "v_smfmac_f32_16x16x64_bf16 v[76:79], v[60:63], a[80:87], v10\n\t" \
    "v_smfmac_f32_16x16x64_bf16 v[76:79], v[64:67], a[88:95], v10\n\t" \
    "v_smfmac_f32_16x16x64_bf16 v[76:79], v[68:71], a[96:103], v10\n\t" \
    "v_smfmac_f32_16x16x64_bf16 v[76:79], v[44:47], a[136:143], v10\n\t" \
    "v_smfmac_f32_16x16x64_bf16 v[80:83], v[36:39], a[176:183], v10\n\t" \
    "v_smfmac_f32_16x16x64_bf16 v[80:83], v[40:43], a[184:191], v10\n\t" \
    "v_smfmac_f32_16x16x64_bf16 v[80:83], v[48:51], a[192:199], v10\n\t" \
    "v_smfmac_f32_16x16x64_bf16 v[80:83], v[52:55], a[200:207], v10\n\t" \
    "v_smfmac_f32_16x16x64_bf16 v[80:83], v[56:59], a[144:151], v10\n\t" \
    "v_smfmac_f32_16x16x64_bf16 v[80:83], v[60:63], a[152:159], v10\n\t" \
    "v_smfmac_f32_16x16x64_bf16 v[80:83], v[64:67], a[160:167], v10\n\t" \
    "v_smfmac_f32_16x16x64_bf16 v[80:83], v[68:71], a[168:175], v10\n\t" \
    "v_smfmac_f32_16x16x64_bf16 v[80:83], v[44:47], a[208:215], v10\n\t" \
    "v_smfmac_f32_16x16x64_bf16 v[84:87], v[36:39], a[248:255], v10\n\t" \
    "v_smfmac_f32_16x16x64_bf16 v[84:87], v[40:43], v[224:231], v10\n\t" \
    "v_smfmac_f32_16x16x64_bf16 v[84:87], v[48:51], v[232:239], v10\n\t" \
    "v_smfmac_f32_16x16x64_bf16 v[84:87], v[52:55], v[240:247], v10\n\t" \
    "v_smfmac_f32_16x16x64_bf16 v[84:87], v[56:59], a[216:223], v10\n\t" \
    "v_smfmac_f32_16x16x64_bf16 v[84:87], v[60:63], a[224:231], v10\n\t" \
    "v_smfmac_f32_16x16x64_bf16 v[84:87], v[64:67], a[232:239], v10\n\t" \
    "v_smfmac_f32_16x16x64_bf16 v[84:87], v[68:71], a[240:247], v10\n\t" \
    "v_smfmac_f32_16x16x64_bf16 v[84:87], v[44:47], v[248:255], v10\n\t" \
    "v_add_f32 v72, v72, v74\n\t" \
    "v_add_f32 v73, v73, v75\n\t" \
    "v_cvt_pk_bf16_f32 v156, v72, v73\n\t" \
    "ds_write_b32 v12, v156 offset:0\n\t" \
    "v_add_f32 v76, v76, v78\n\t" \
    "v_add_f32 v77, v77, v79\n\t" \
    "v_cvt_pk_bf16_f32 v157, v76, v77\n\t" \
    "ds_write_b32 v12, v157 offset:64\n\t" \
    "v_add_f32 v80, v80, v82\n\t" \
    "v_add_f32 v81, v81, v83\n\t" \
    "v_cvt_pk_bf16_f32 v158, v80, v81\n\t" \
    "ds_write_b32 v16, v158 offset:0\n\t" \
    "v_pk_add_f32 v[84:85], v[84:85], v[86:87]\n\t" \
    "v_cvt_pk_bf16_f32 v159, v84, v85\n\t" \
    "ds_write_b32 v16, v159 offset:256\n\t" \
    "s_branch qk_Gback68_%=\n\t" \
    "qk_kf59_%=:\n\t" \
    "global_store_dword v14, v25, s[36:37] sc1\n\t" \
    "s_branch qk_kb60_%=\n\t" \
    "qk_last57_%=:\n\t" \
    "s_mov_b64 exec, s[58:59]\n\t" \
    "global_store_dwordx4 v0, v[140:143], s[40:41] offset:0 sc1\n\t" \
    "global_store_dwordx4 v0, v[144:147], s[40:41] offset:1024 sc1\n\t" \
    "global_store_dwordx4 v0, v[148:151], s[40:41] offset:2048 sc1\n\t" \
    "global_store_dwordx4 v0, v[152:155], s[40:41] offset:3072 sc1\n\t" \
    "s_mov_b64 exec, -1\n\t" \
    "s_endpgm\n\t" \
    "qk_loop14_%=:\n\t" \
    "s_mov_b32 s38, 0x22349500\n\t" \
    "s_mov_b32 s39, 0x1a\n\t" \
    "v_mov_b32 v72, 0\n\t" \
    "v_mov_b32 v73, 0\n\t" \
    "v_mov_b32 v74, 0\n\t" \
    "v_mov_b32 v75, 0\n\t" \
    "v_mov_b32 v76, 0\n\t" \
    "v_mov_b32 v77, 0\n\t" \
    "v_mov_b32 v78, 0\n\t" \
    "v_mov_b32 v79, 0\n\t" \
    "v_mov_b32 v80, 0\n\t" \
    "v_mov_b32 v81, 0\n\t" \
    "v_mov_b32 v82, 0\n\t" \
    "v_mov_b32 v83, 0\n\t" \
    "v_mov_b32 v84, 0\n\t" \
    "v_mov_b32 v85, 0\n\t" \
    "v_mov_b32 v86, 0\n\t" \
    "v_mov_b32 v87, 0\n\t" \
    "s_add_u32 s69, s65, 0\n\t" \
    "s_and_b32 s69, s69, 7\n\t" \
    "s_lshl_b32 s69, s69, 18\n\t" \
    "s_add_u32 s69, s69, s62\n\t" \
    "s_add_u32 s46, s36, s69\n\t" \
    "s_addc_u32 s47, s37, 0\n\t" \
    "s_lshr_b64 s[38:39], s[38:39], 4\n\t" \
    "s_and_b32 s97, s38, 15\n\t" \
    "s_lshl_b32 s97, s97, 6\n\t" \
    "s_and_b32 s69, s65, 7\n\t" \
    "s_lshl_b32 s69, s69, 18\n\t" \
    "s_add_u32 s69, s69, s63\n\t" \
    "s_add_u32 s52, s36, s69\n\t" \
    "s_addc_u32 s53, s37, 0\n\t" \
    "s_sub_u32 s70, s79, s74\n\t" \
    "s_max_i32 s70, s70, 0\n\t" \
    "s_min_i32 s70, s70, 16000\n\t" \
    "s_lshl_b32 s70, s70, 1\n\t" \
    "s_mul_hi_u32 s70, s70, s77\n\t" \
    "s_add_u32 s70, s70, s73\n\t" \
    ".p2align 6\n\t" \
    "s_nop 0\n\t" \
    "s_nop 0\n\t" \
    "s_nop 0\n\t" \
    "s_nop 0\n\t" \
    "s_nop 0\n\t" \
    "s_nop 0\n\t" \
    "qk_go75_%=:\n\t" \
    "s_getpc_b64 s[30:31]\n\t" \
    "qk_Wp77_%=:\n\t" \
    "s_add_u32 s30, s30, qk_We78_%=-qk_Wp77_%=\n\t" \
    "s_addc_u32 s31, s31, 0\n\t" \
    "s_sub_u32 s70, s70, 96\n\t" \
    "qk_W76_%=:\n\t" \
    "s_memtime s[56:57]\n\t" \
    "s_waitcnt lgkmcnt(0)\n\t" \
    "s_sub_u32 s69, s70, s56\n\t" \
    "s_cmp_gt_i32 s69, 80\n\t" \
    "s_cbranch_scc1 qk_W76_%=\n\t" \
    "s_max_i32 s69, s69, 0\n\t" \
    "s_and_b32 s69, s69, -4\n\t" \
    "s_sub_u32 s30, s30, s69\n\t" \
    "s_subb_u32 s31, s31, 0\n\t" \
    "s_setpc_b64 s[30:31]\n\t" \
    "s_nop 0\n\t" \
    "s_nop 0\n\t" \
    "s_nop 0\n\t" \
    "s_nop 0\n\t" \
    "s_nop 0\n\t" \
    "s_nop 0\n\t" \
    "s_nop 0\n\t" \
    "s_nop 0\n\t" \
    "s_nop 0\n\t" \
    "s_nop 0\n\t" \
    "s_nop 0\n\t" \
    "s_nop 0\n\t" \
    "s_nop 0\n\t" \
    "s_nop 0\n\t" \
    "s_nop 0\n\t" \
    "s_nop 0\n\t" \
    "s_nop 0\n\t" \
    "s_nop 0\n\t" \
    "s_nop 0\n\t" \
    "s_nop 0\n\t" \
    "qk_We78_%=:\n\t" \
    "qk_G79_%=:\n\t" \
    "global_load_dwordx4 v[36:39], v1, s[46:47] sc1\n\t" \
    "global_load_dword v161, v2, s[46:47] sc1\n\t" \
    "global_load_dwordx4 v[40:43], v23, s[46:47] sc1\n\t" \
    "s_mul_hi_u32 s69, s84, s78\n\t" \
    "s_add_u32 s33, s74, s69\n\t" \
    "s_add_u32 s33, s33, s97\n\t" \
    "s_lshr_b32 s69, s90, 16\n\t" \
    "s_add_u32 s34, s33, s69\n\t" \
    "s_and_b32 s69, s90, 0xffff\n\t" \
    "s_sub_u32 s35, s80, s74\n\t" \
    "s_sub_u32 s35, s35, s69\n\t" \
    "s_add_u32 s99, s35, 240\n\t" \
    "s_add_u32 s98, s33, 448\n\t" \
    "s_sub_u32 s96, s79, s74\n\t" \
    "s_cmp_le_u32 s65, 0\n\t" \
    "s_cselect_b32 s69, 112, 0\n\t" \
    "s_add_u32 s69, s69, 448\n\t" \
    "s_add_u32 s96, s96, s69\n\t" \
    "s_max_i32 s35, s35, s96\n\t" \
    "s_add_u32 s99, s35, 716\n\t" \
    "s_mul_i32 s69, s35, 10\n\t" \
    "s_ashr_i32 s69, s69, 4\n\t" \
    "s_sub_u32 s69, s33, s69\n\t" \
    "v_lshrrev_b32_e64 v163, 1, s69\n\t" \
    "s_waitcnt vmcnt(2)\n\t" \
    "ds_write_b128 v3, v[36:39]\n\t" \
    "v_smfmac_f32_16x16x64_bf16 v[72:75], v[36:39], a[48:55], v10\n\t" \
    "s_waitcnt vmcnt(1)\n\t" \
    "ds_write_b32 v22, v161 offset:20224\n\t" \
    "v_smfmac_f32_16x16x64_bf16 v[76:79], v[36:39], a[120:127], v10\n\t" \
    "s_waitcnt vmcnt(0)\n\t" \
    "ds_write_b128 v3, v[40:43] offset:1024\n\t" \
    "v_max3_u32 v160, v36, v40, v161\n\t" \
    "v_cmp_eq_u32 vcc, -1, v160\n\t" \
    "v_smfmac_f32_16x16x64_bf16 v[80:83], v[36:39], a[192:199], v10\n\t" \
    "v_smfmac_f32_16x16x64_bf16 v[84:87], v[36:39], v[232:239], v10\n\t" \
    "v_smfmac_f32_16x16x64_bf16 v[72:75], v[40:43], a[56:63], v10\n\t" \
    "s_cbranch_vccnz qk_Gbad80_%=\n\t" \
    "qk_Gc81_%=:\n\t" \
    "s_waitcnt lgkmcnt(0)\n\t" \
    "s_barrier\n\t" \
    "s_memtime s[56:57]\n\t" \
    "ds_read_b128 v[48:51], v4\n\t" \
    "ds_read_b128 v[52:55], v5\n\t" \
    "v_smfmac_f32_16x16x64_bf16 v[76:79], v[40:43], a[128:135], v10\n\t" \
    "ds_read_b128 v[56:59], v6\n\t" \
    "ds_read_b128 v[60:63], v7\n\t" \
    "v_smfmac_f32_16x16x64_bf16 v[80:83], v[40:43], a[200:207], v10\n\t" \
    "ds_read_b128 v[64:67], v8\n\t" \
    "ds_read_b128 v[68:71], v9\n\t" \
    "v_smfmac_f32_16x16x64_bf16 v[84:87], v[40:43], v[240:247], v10\n\t" \
    "ds_read_b128 v[44:47], v0 offset:19456\n\t" \
    "s_waitcnt lgkmcnt(6)\n\t" \
    "v_smfmac_f32_16x16x64_bf16 v[72:75], v[48:51], a[0:7], v10\n\t" \
    "s_sub_u32 s69, s56, s73\n\t" \
    "s_mul_hi_u32 s69, s69, s78\n\t" \
    "s_waitcnt lgkmcnt(5)\n\t" \
    "v_smfmac_f32_16x16x64_bf16 v[72:75], v[52:55], a[8:15], v10\n\t" \
    "s_max_i32 s69, s69, s96\n\t" \
    "s_max_i32 s70, s69, s35\n\t" \
    "s_waitcnt lgkmcnt(4)\n\t" \
    "v_smfmac_f32_16x16x64_bf16 v[72:75], v[56:59], a[16:23], v10\n\t" \
    "s_min_i32 s70, s70, s99\n\t" \
    "s_mul_i32 s70, s70, 10\n\t" \
    "s_waitcnt lgkmcnt(3)\n\t" \
    "v_smfmac_f32_16x16x64_bf16 v[72:75], v[60:63], a[24:31], v10\n\t" \
    "s_lshl4_add_u32 s70, s69, s70\n\t" \
    "s_ashr_i32 s70, s70, 5\n\t" \
    "v_add_lshl_u32 v162, s70, v163, 1\n\t" \
    "s_waitcnt lgkmcnt(2)\n\t" \
    "v_smfmac_f32_16x16x64_bf16 v[72:75], v[64:67], a[32:39], v10\n\t" \
    "global_store_dword v21, v162, s[52:53] sc1\n\t" \
    "s_waitcnt lgkmcnt(1)\n\t" \
    "v_smfmac_f32_16x16x64_bf16 v[72:75], v[68:71], a[40:47], v10\n\t" \
    "s_waitcnt lgkmcnt(0)\n\t" \
    "v_smfmac_f32_16x16x64_bf16 v[72:75], v[44:47], a[64:71], v10\n\t" \
    "s_nop 2\n\t" \
    "v_smfmac_f32_16x16x64_bf16 v[76:79], v[48:51], a[72:79], v10\n\t" \
    "s_nop 2\n\t" \
    "v_smfmac_f32_16x16x64_bf16 v[76:79], v[52:55], a[80:87], v10\n\t" \
    "v_add_f32 v72, v72, v74\n\t" \
    "v_smfmac_f32_16x16x64_bf16 v[76:79], v[56:59], a[88:95], v10\n\t" \
    "v_add_f32 v73, v73, v75\n\t" \
    "v_smfmac_f32_16x16x64_bf16 v[76:79], v[60:63], a[96:103], v10\n\t" \
    "v_cvt_pk_bf16_f32 v156, v72, v73\n\t" \
    "v_smfmac_f32_16x16x64_bf16 v[76:79], v[64:67], a[104:111], v10\n\t" \
    "ds_write_b32 v12, v156 offset:0\n\t" \
    "v_smfmac_f32_16x16x64_bf16 v[76:79], v[68:71], a[112:119], v10\n\t" \
    "v_smfmac_f32_16x16x64_bf16 v[76:79], v[44:47], a[136:143], v10\n\t" \
    "s_nop 2\n\t" \
    "v_smfmac_f32_16x16x64_bf16 v[80:83], v[48:51], a[144:151], v10\n\t" \
    "s_nop 2\n\t" \
    "v_smfmac_f32_16x16x64_bf16 v[80:83], v[52:55], a[152:159], v10\n\t" \
    "v_add_f32 v76, v76, v78\n\t" \
    "v_smfmac_f32_16x16x64_bf16 v[80:83], v[56:59], a[160:167], v10\n\t" \
    "v_add_f32 v77, v77, v79\n\t" \
    "v_smfmac_f32_16x16x64_bf16 v[80:83], v[60:63], a[168:175], v10\n\t" \
    "v_cvt_pk_bf16_f32 v157, v76, v77\n\t" \
    "v_smfmac_f32_16x16x64_bf16 v[80:83], v[64:67], a[176:183], v10\n\t" \
    "ds_write_b32 v12, v157 offset:64\n\t" \
    "v_smfmac_f32_16x16x64_bf16 v[80:83], v[68:71], a[184:191], v10\n\t" \
    "v_smfmac_f32_16x16x64_bf16 v[80:83], v[44:47], a[208:215], v10\n\t" \
    "s_nop 2\n\t" \
    "v_smfmac_f32_16x16x64_bf16 v[84:87], v[48:51], a[216:223], v10\n\t" \
    "s_nop 2\n\t" \
    "v_smfmac_f32_16x16x64_bf16 v[84:87], v[52:55], a[224:231], v10\n\t" \
    "v_add_f32 v80, v80, v82\n\t" \
    "v_smfmac_f32_16x16x64_bf16 v[84:87], v[56:59], a[232:239], v10\n\t" \
    "v_add_f32 v81, v81, v83\n\t" \
    "v_smfmac_f32_16x16x64_bf16 v[84:87], v[60:63], a[240:247], v10\n\t" \
    "v_cvt_pk_bf16_f32 v158, v80, v81\n\t" \
    "v_smfmac_f32_16x16x64_bf16 v[84:87], v[64:67], a[248:255], v10\n\t" \
    "ds_write_b32 v16, v158 offset:0\n\t" \
    "v_smfmac_f32_16x16x64_bf16 v[84:87], v[68:71], v[224:231], v10\n\t" \
    "v_smfmac_f32_16x16x64_bf16 v[84:87], v[44:47], v[248:255], v10\n\t" \
    "s_nop 6\n\t" \
    "v_cmp_u_f32 vcc, v72, v73\n\t" \
    "v_pk_add_f32 v[84:85], v[84:85], v[86:87]\n\t" \
    "v_cvt_pk_bf16_f32 v159, v84, v85\n\t" \
    "ds_write_b32 v16, v159 offset:256\n\t" \
    "s_cbranch_vccnz qk_Gslow82_%=\n\t" \
    "qk_Gback83_%=:\n\t" \
    "s_waitcnt lgkmcnt(0)\n\t" \
    "s_barrier\n\t" \
    "s_cmp_eq_u32 s65, 63\n\t" \
    "s_cbranch_scc1 qk_last74_%=\n\t" \
    "s_cmp_lg_u32 s65, 0\n\t" \
    "s_cbranch_scc1 qk_ng84_%=\n\t" \
    "v_mov_b32 v168, 5838848\n\t" \
    "v_mov_b32 v169, 1\n\t" \
    "s_mov_b64 exec, 1\n\t" \
    "global_atomic_add v170, v168, v169, s[36:37] sc0 sc1\n\t" \
    "s_mov_b64 exec, -1\n\t" \
    "v_mbcnt_lo_u32_b32 v168, -1, 0\n\t" \
    "v_and_b32 v169, 3, v168\n\t" \
    "v_lshlrev_b32 v169, 16, v169\n\t" \
    "v_bfe_u32 v171, v168, 2, 2\n\t" \
    "v_lshl_add_u32 v169, v171, 11, v169\n\t" \
    "v_bfe_u32 v171, v168, 4, 1\n\t" \
    "v_lshl_add_u32 v169, v171, 14, v169\n\t" \
    "v_add_u32 v169, s63, v169\n\t" \
    "s_waitcnt vmcnt(0)\n\t" \
    "v_readfirstlane_b32 s69, v170\n\t" \
    "s_nop 1\n\t" \
    "s_cmp_lg_u32 s69, 126\n\t" \
    "s_cbranch_scc1 qk_go85_%=\n\t" \
    "s_memrealtime s[56:57]\n\t" \
    "s_waitcnt lgkmcnt(0)\n\t" \
    "s_lshl_b32 s69, s56, 4\n\t" \
    "s_add_u32 s69, s69, 2960\n\t" \
    "s_add_u32 s70, s56, 24\n\t" \
    "s_lshr_b32 s70, s70, 4\n\t" \
    "v_lshrrev_b32 v171, 2, v168\n\t" \
    "v_add_u32 v171, s70, v171\n\t" \
    "v_and_b32 v171, 63, v171\n\t" \
    "v_lshlrev_b32 v171, 6, v171\n\t" \
    "v_and_b32 v170, 3, v168\n\t" \
    "v_lshl_add_u32 v171, v170, 12, v171\n\t" \
    "v_add_u32 v171, 5865472, v171\n\t" \
    "v_mov_b32 v168, s69\n\t" \
    "s_mov_b32 exec_hi, 0\n\t" \
    "global_store_dword v171, v168, s[36:37] sc1\n\t" \
    "global_store_dword v169, v168, s[36:37] sc1\n\t" \
    "s_mov_b32 exec_hi, -1\n\t" \
    "qk_go85_%=:\n\t" \
    "v_mov_b32 v21, v33\n\t" \
    "qk_ng84_%=:\n\t" \
    "s_sub_u32 s70, s80, s74\n\t" \
    "s_max_i32 s70, s70, 0\n\t" \
    "s_min_i32 s70, s70, 16000\n\t" \
    "s_lshl_b32 s70, s70, 1\n\t" \
    "s_mul_hi_u32 s70, s70, s77\n\t" \
    "s_add_u32 s70, s70, s73\n\t" \
    "qk_W86_%=:\n\t" \
    "s_memtime s[56:57]\n\t" \
    "s_waitcnt lgkmcnt(0)\n\t" \
    "s_sub_u32 s69, s70, s56\n\t" \
    "s_cmp_gt_i32 s69, 24\n\t" \
    "s_cbranch_scc1 qk_W86_%=\n\t" \
    "global_load_dword v160, v20, s[52:53] sc1\n\t" \
    "s_waitcnt lgkmcnt(0)\n\t" \
    "s_mov_b32 s70, 116\n\t" \
    "qk_E90_%=:\n\t" \
    "s_memtime s[56:57]\n\t" \
    "s_memrealtime s[2:3]\n\t" \
    "s_nop 0\n\t" \
    "s_memrealtime s[4:5]\n\t" \
    "s_nop 0\n\t" \
    "s_memrealtime s[6:7]\n\t" \
    "s_nop 0\n\t" \
    "s_memrealtime s[8:9]\n\t" \
    "s_nop 0\n\t" \
    "s_memrealtime s[10:11]\n\t" \
    "s_nop 0\n\t" \
    "s_memrealtime s[12:13]\n\t" \
    "s_nop 0\n\t" \
    "s_memrealtime s[14:15]\n\t" \
    "s_nop 0\n\t" \
    "s_memrealtime s[16:17]\n\t" \
    "s_nop 0\n\t" \
    "s_memrealtime s[18:19]\n\t" \
    "s_nop 0\n\t" \
    "s_memrealtime s[20:21]\n\t" \
    "s_nop 0\n\t" \
    "s_memrealtime s[22:23]\n\t" \
    "s_nop 0\n\t" \
    "s_memrealtime s[24:25]\n\t" \
    "s_nop 0\n\t" \
    "s_memrealtime s[26:27]\n\t" \
    "s_nop 0\n\t" \
    "s_memrealtime s[28:29]\n\t" \
    "s_memtime s[54:55]\n\t" \
    "s_add_u32 s70, s70, 8\n\t" \
    "s_waitcnt lgkmcnt(0)\n\t" \
    "s_sub_u32 s69, s54, s56\n\t" \
    "s_cmp_gt_u32 s69, s70\n\t" \
    "s_cbranch_scc1 qk_E90_%=\n\t" \
    "s_add_u32 s2, s2, s4\n\t" \
    "s_add_u32 s6, s6, s8\n\t" \
    "s_add_u32 s10, s10, s12\n\t" \
    "s_add_u32 s14, s14, s16\n\t" \
    "s_add_u32 s18, s18, s20\n\t" \
    "s_add_u32 s22, s22, s24\n\t" \
    "s_add_u32 s2, s2, s6\n\t" \
    "s_add_u32 s10, s10, s14\n\t" \
    "s_add_u32 s18, s18, s22\n\t" \
    "s_add_u32 s2, s2, s26\n\t" \
    "s_add_u32 s10, s10, s18\n\t" \
    "s_add_u32 s2, s2, s10\n\t" \
    "s_mul_i32 s70, s28, 13\n\t" \
    "s_sub_u32 s70, s70, s2\n\t" \
    "s_min_u32 s70, s70, 52\n\t" \
    "s_mul_i32 s69, s69, s70\n\t" \
    "s_mul_hi_u32 s69, s69, 76695844\n\t" \
    "s_mov_b32 s75, s73\n\t" \
    "s_mov_b32 s76, s74\n\t" \
    "s_add_u32 s73, s56, s69\n\t" \
    "s_lshl_b32 s74, s28, 4\n\t" \
    "s_sub_u32 s69, s73, s75\n\t" \
    "s_sub_u32 s70, s74, s76\n\t" \
    "s_sub_u32 s71, s70, 1600\n\t" \
    "s_cmp_lt_u32 s71, 400000\n\t" \
    "s_cbranch_scc0 qk_K91_%=\n\t" \
    "v_cvt_f32_u32 v168, s69\n\t" \
    "v_cvt_f32_u32 v169, s70\n\t" \
    "v_rcp_f32 v170, v168\n\t" \
    "v_rcp_f32 v171, v169\n\t" \
    "v_mul_f32 v170, v170, v169\n\t" \
    "v_mul_f32 v171, v171, v168\n\t" \
    "v_mul_f32 v170, 0x4f800000, v170\n\t" \
    "v_mul_f32 v171, 0x4f000000, v171\n\t" \
    "v_cvt_u32_f32 v170, v170\n\t" \
    "v_cvt_u32_f32 v171, v171\n\t" \
    "v_readfirstlane_b32 s69, v170\n\t" \
    "v_readfirstlane_b32 s70, v171\n\t" \
    "s_nop 1\n\t" \
    "s_min_u32 s71, s65, 1\n\t" \
    "s_sub_u32 s69, s69, s78\n\t" \
    "s_ashr_i32 s69, s69, s71\n\t" \
    "s_add_u32 s78, s78, s69\n\t" \
    "s_sub_u32 s70, s70, s77\n\t" \
    "s_ashr_i32 s70, s70, s71\n\t" \
    "s_add_u32 s77, s77, s70\n\t" \
    "qk_K91_%=:\n\t" \
    "s_add_u32 s65, s65, 1\n\t" \
    "v_mov_b32 v72, 0\n\t" \
    "v_mov_b32 v73, 0\n\t" \
    "v_mov_b32 v74, 0\n\t" \
    "v_mov_b32 v75, 0\n\t" \
    "v_mov_b32 v76, 0\n\t" \
    "v_mov_b32 v77, 0\n\t" \
    "v_mov_b32 v78, 0\n\t" \
    "v_mov_b32 v79, 0\n\t" \
    "v_mov_b32 v80, 0\n\t" \
    "v_mov_b32 v81, 0\n\t" \
    "v_mov_b32 v82, 0\n\t" \
    "v_mov_b32 v83, 0\n\t" \
    "v_mov_b32 v84, 0\n\t" \
    "v_mov_b32 v85, 0\n\t" \
    "v_mov_b32 v86, 0\n\t" \
    "v_mov_b32 v87, 0\n\t" \
    "s_add_u32 s69, s65, 0\n\t" \
    "s_and_b32 s69, s69, 7\n\t" \
    "s_lshl_b32 s69, s69, 18\n\t" \
    "s_add_u32 s69, s69, s62\n\t" \
    "s_add_u32 s46, s36, s69\n\t" \
    "s_addc_u32 s47, s37, 0\n\t" \
    "s_lshr_b64 s[38:39], s[38:39], 4\n\t" \
    "s_and_b32 s97, s38, 15\n\t" \
    "s_lshl_b32 s97, s97, 6\n\t" \
    "s_waitcnt vmcnt(0)\n\t" \
    "v_cmp_eq_u32 vcc, -1, v160\n\t" \
    "s_cbranch_vccz qk_Hok88_%=\n\t" \
    "qk_H87_%=:\n\t" \
    "global_load_dword v160, v20, s[52:53] sc1\n\t" \
    "s_waitcnt vmcnt(0)\n\t" \
    "v_cmp_eq_u32 vcc, -1, v160\n\t" \
    "s_cbranch_vccz qk_Hc89_%=\n\t" \
    "s_cmp_eq_u32 s68, 0\n\t" \
    "s_cbranch_scc1 qk_F92_%=\n\t" \
    "s_sub_u32 s68, s68, 1\n\t" \
    "s_sleep 2\n\t" \
    "s_branch qk_H87_%=\n\t" \
    "qk_F92_%=:\n\t" \
    "v_mov_b32 v161, 5767424\n\t" \
    "v_mov_b32 v162, 5\n\t" \
    "global_store_dword v161, v162, s[36:37] sc1\n\t" \
    "s_waitcnt vmcnt(0)\n\t" \
    "s_branch qk_Hc89_%=\n\t" \
    "qk_Hc89_%=:\n\t" \
    "s_memtime s[56:57]\n\t" \
    "s_waitcnt lgkmcnt(0)\n\t" \
    "s_sub_u32 s69, s56, s73\n\t" \
    "s_cmp_lt_u32 s69, 3000\n\t" \
    "s_cbranch_scc1 qk_St93_%=\n\t" \
    "s_waitcnt lgkmcnt(0)\n\t" \
    "s_mov_b32 s70, 116\n\t" \
    "qk_E94_%=:\n\t" \
    "s_memtime s[56:57]\n\t" \
    "s_memrealtime s[2:3]\n\t" \
    "s_nop 0\n\t" \
    "s_memrealtime s[4:5]\n\t" \
    "s_nop 0\n\t" \
    "s_memrealtime s[6:7]\n\t" \
    "s_nop 0\n\t" \
    "s_memrealtime s[8:9]\n\t" \
    "s_nop 0\n\t" \
    "s_memrealtime s[10:11]\n\t" \
    "s_nop 0\n\t" \
    "s_memrealtime s[12:13]\n\t" \
    "s_nop 0\n\t" \
    "s_memrealtime s[14:15]\n\t" \
    "s_nop 0\n\t" \
    "s_memrealtime s[16:17]\n\t" \
    "s_nop 0\n\t" \
    "s_memrealtime s[18:19]\n\t" \
    "s_nop 0\n\t" \
    "s_memrealtime s[20:21]\n\t" \
    "s_nop 0\n\t" \
    "s_memrealtime s[22:23]\n\t" \
    "s_nop 0\n\t" \
    "s_memrealtime s[24:25]\n\t" \
    "s_nop 0\n\t" \
    "s_memrealtime s[26:27]\n\t" \
    "s_nop 0\n\t" \
    "s_memrealtime s[28:29]\n\t" \
    "s_memtime s[54:55]\n\t" \
    "s_add_u32 s70, s70, 8\n\t" \
    "s_waitcnt lgkmcnt(0)\n\t" \
    "s_sub_u32 s69, s54, s56\n\t" \
    "s_cmp_gt_u32 s69, s70\n\t" \
    "s_cbranch_scc1 qk_E94_%=\n\t" \
    "s_add_u32 s2, s2, s4\n\t" \
    "s_add_u32 s6, s6, s8\n\t" \
    "s_add_u32 s10, s10, s12\n\t" \
    "s_add_u32 s14, s14, s16\n\t" \
    "s_add_u32 s18, s18, s20\n\t" \
    "s_add_u32 s22, s22, s24\n\t" \
    "s_add_u32 s2, s2, s6\n\t" \
    "s_add_u32 s10, s10, s14\n\t" \
    "s_add_u32 s18, s18, s22\n\t" \
    "s_add_u32 s2, s2, s26\n\t" \
    "s_add_u32 s10, s10, s18\n\t" \
    "s_add_u32 s2, s2, s10\n\t" \
    "s_mul_i32 s70, s28, 13\n\t" \
    "s_sub_u32 s70, s70, s2\n\t" \
    "s_min_u32 s70, s70, 52\n\t" \
    "s_mul_i32 s69, s69, s70\n\t" \
    "s_mul_hi_u32 s69, s69, 76695844\n\t" \
    "s_mov_b32 s75, s73\n\t" \
    "s_mov_b32 s76, s74\n\t" \
    "s_add_u32 s73, s56, s69\n\t" \
    "s_lshl_b32 s74, s28, 4\n\t" \
    "qk_St93_%=:\n\t" \
    "qk_Hok88_%=:\n\t" \
    "v_max_u32_dpp v160, v160, v160 row_ror:1 row_mask:0xf bank_mask:0xf\n\t" \
    "s_nop 1\n\t" \
    "v_max_u32_dpp v160, v160, v160 row_ror:2 row_mask:0xf bank_mask:0xf\n\t" \
    "s_nop 1\n\t" \
    "v_max_u32_dpp v160, v160, v160 row_ror:4 row_mask:0xf bank_mask:0xf\n\t" \
    "s_nop 1\n\t" \
    "v_max_u32_dpp v160, v160, v160 row_ror:8 row_mask:0xf bank_mask:0xf\n\t" \
    "s_nop 0\n\t" \
    "v_readlane_b32 s69, v160, 0\n\t" \
    "v_readlane_b32 s71, v160, 16\n\t" \
    "s_nop 1\n\t" \
    "s_max_u32 s81, s69, s71\n\t" \
    "s_mul_hi_u32 s69, s85, s78\n\t" \
    "s_add_u32 s79, s81, s69\n\t" \
    "s_add_u32 s79, s79, s87\n\t" \
    "s_cmp_ge_u32 s65, 53\n\t" \
    "s_cselect_b32 s69, 32, 0\n\t" \
    "s_sub_u32 s79, s79, s69\n\t" \
    "s_cmp_lt_u32 s65, 10\n\t" \
    "s_cselect_b32 s69, 48, 0\n\t" \
    "s_add_u32 s79, s79, s69\n\t" \
    "s_add_u32 s80, s79, s88\n\t" \
    "s_sub_u32 s70, s79, s74\n\t" \
    "s_max_i32 s70, s70, 0\n\t" \
    "s_min_i32 s70, s70, 16000\n\t" \
    "s_lshl_b32 s70, s70, 1\n\t" \
    "s_mul_hi_u32 s70, s70, s77\n\t" \
    "s_add_u32 s70, s70, s73\n\t" \
    "v_mov_b32 v168, s70\n\t" \
    "ds_write_b32 v11, v168 offset:20480\n\t" \
    "s_and_b32 s69, s65, 7\n\t" \
    "s_lshl_b32 s69, s69, 18\n\t" \
    "s_add_u32 s69, s69, s63\n\t" \
    "s_add_u32 s52, s36, s69\n\t" \
    "s_addc_u32 s53, s37, 0\n\t" \
    "s_waitcnt lgkmcnt(0)\n\t" \
    "s_barrier\n\t" \
    "ds_write_b32 v12, v31 offset:0\n\t" \
    "ds_write_b32 v12, v31 offset:64\n\t" \
    "s_branch qk_go75_%=\n\t" \
    "qk_Gbad80_%=:\n\t" \
    "s_waitcnt lgkmcnt(0)\n\t" \
    "s_nop 7\n\t" \
    "v_mov_b32 v72, 0\n\t" \
    "v_mov_b32 v73, 0\n\t" \
    "v_mov_b32 v74, 0\n\t" \
    "v_mov_b32 v75, 0\n\t" \
    "v_mov_b32 v76, 0\n\t" \
    "v_mov_b32 v77, 0\n\t" \
    "v_mov_b32 v78, 0\n\t" \
    "v_mov_b32 v79, 0\n\t" \
    "v_mov_b32 v80, 0\n\t" \
    "v_mov_b32 v81, 0\n\t" \
    "v_mov_b32 v82, 0\n\t" \
    "v_mov_b32 v83, 0\n\t" \
    "v_mov_b32 v84, 0\n\t" \
    "v_mov_b32 v85, 0\n\t" \
    "v_mov_b32 v86, 0\n\t" \
    "v_mov_b32 v87, 0\n\t" \
    "s_cmp_eq_u32 s68, 0\n\t" \
    "s_cbranch_scc1 qk_F95_%=\n\t" \
    "s_sub_u32 s68, s68, 1\n\t" \
    "s_branch qk_G79_%=\n\t" \
    "qk_F95_%=:\n\t" \
    "v_mov_b32 v160, 5767424\n\t" \
    "v_mov_b32 v161, 1\n\t" \
    "global_store_dword v160, v161, s[36:37] sc1\n\t" \
    "s_waitcnt vmcnt(0)\n\t" \
    "s_branch qk_Gc81_%=\n\t" \
    "qk_Gslow82_%=:\n\t" \
    "v_max_u32 v160, v36, v37\n\t" \
    "v_max3_u32 v160, v160, v38, v39\n\t" \
    "v_max3_u32 v160, v160, v40, v41\n\t" \
    "v_max3_u32 v160, v160, v42, v43\n\t" \
    "v_max3_u32 v160, v160, v44, v45\n\t" \
    "v_max3_u32 v160, v160, v46, v47\n\t" \
    "v_max3_u32 v160, v160, v48, v49\n\t" \
    "v_max3_u32 v160, v160, v50, v51\n\t" \
    "v_max3_u32 v160, v160, v52, v53\n\t" \
    "v_max3_u32 v160, v160, v54, v55\n\t" \
    "v_max3_u32 v160, v160, v56, v57\n\t" \
    "v_max3_u32 v160, v160, v58, v59\n\t" \
    "v_max3_u32 v160, v160, v60, v61\n\t" \
    "v_max3_u32 v160, v160, v62, v63\n\t" \
    "v_max3_u32 v160, v160, v64, v65\n\t" \
    "v_max3_u32 v160, v160, v66, v67\n\t" \
    "v_max3_u32 v160, v160, v68, v69\n\t" \
    "v_max3_u32 v160, v160, v70, v71\n\t" \
    "v_cmp_eq_u32 vcc, -1, v160\n\t" \
    "s_cbranch_vccz qk_Gback83_%=\n\t" \
    "s_waitcnt lgkmcnt(0)\n\t" \
    "v_sub_u32 v160, v1, v3\n\t" \
    "s_nop 0\n\t" \
    "v_readfirstlane_b32 s69, v160\n\t" \
    "v_add_u32 v163, 20480, v0\n\t" \
    "s_nop 7\n\t" \
    "s_add_u32 s30, s46, s69\n\t" \
    "s_addc_u32 s31, s47, 0\n\t" \
    "qk_Gag96_%=:\n\t" \
    "global_load_dwordx4 v[36:39], v1, s[46:47] sc1\n\t" \
    "global_load_dwordx4 v[40:43], v23, s[46:47] sc1\n\t" \
    "global_load_dwordx4 v[44:47], v163, s[30:31] sc1\n\t" \
    "global_load_dwordx4 v[48:51], v4, s[30:31] sc1\n\t" \
    "global_load_dwordx4 v[52:55], v5, s[30:31] sc1\n\t" \
    "global_load_dwordx4 v[56:59], v6, s[30:31] sc1\n\t" \
    "global_load_dwordx4 v[60:63], v7, s[30:31] sc1\n\t" \
    "global_load_dwordx4 v[64:67], v8, s[30:31] sc1\n\t" \
    "global_load_dwordx4 v[68:71], v9, s[30:31] sc1\n\t" \
    "s_waitcnt vmcnt(0)\n\t" \
    "v_max_u32 v160, v36, v37\n\t" \
    "v_max3_u32 v160, v160, v38, v39\n\t" \
    "v_max3_u32 v160, v160, v40, v41\n\t" \
    "v_max3_u32 v160, v160, v42, v43\n\t" \
    "v_max3_u32 v160, v160, v44, v45\n\t" \
    "v_max3_u32 v160, v160, v46, v47\n\t" \
    "v_max3_u32 v160, v160, v48, v49\n\t" \
    "v_max3_u32 v160, v160, v50, v51\n\t" \
    "v_max3_u32 v160, v160, v52, v53\n\t" \
    "v_max3_u32 v160, v160, v54, v55\n\t" \
    "v_max3_u32 v160, v160, v56, v57\n\t" \
    "v_max3_u32 v160, v160, v58, v59\n\t" \
    "v_max3_u32 v160, v160, v60, v61\n\t" \
    "v_max3_u32 v160, v160, v62, v63\n\t" \
    "v_max3_u32 v160, v160, v64, v65\n\t" \
    "v_max3_u32 v160, v160, v66, v67\n\t" \
    "v_max3_u32 v160, v160, v68, v69\n\t" \
    "v_max3_u32 v160, v160, v70, v71\n\t" \
    "v_cmp_eq_u32 vcc, -1, v160\n\t" \
    "s_cbranch_vccz qk_Gredo97_%=\n\t" \
    "s_cmp_eq_u32 s68, 0\n\t" \
    "s_cbranch_scc1 qk_F98_%=\n\t" \
    "s_sub_u32 s68, s68, 1\n\t" \
    "s_sleep 1\n\t" \
    "s_branch qk_Gag96_%=\n\t" \
    "qk_F98_%=:\n\t" \
    "v_mov_b32 v160, 5767424\n\t" \
    "v_mov_b32 v161, 2\n\t" \
    "global_store_dword v160, v161, s[36:37] sc1\n\t" \
    "s_waitcnt vmcnt(0)\n\t" \
    "s_branch qk_Gredo97_%=\n\t" \
    "qk_Gredo97_%=:\n\t" \
    "v_mov_b32 v72, 0\n\t" \
    "v_mov_b32 v73, 0\n\t" \
    "v_mov_b32 v74, 0\n\t" \
    "v_mov_b32 v75, 0\n\t" \
    "v_mov_b32 v76, 0\n\t" \
    "v_mov_b32 v77, 0\n\t" \
    "v_mov_b32 v78, 0\n\t" \
    "v_mov_b32 v79, 0\n\t" \
    "v_mov_b32 v80, 0\n\t" \
    "v_mov_b32 v81, 0\n\t" \
    "v_mov_b32 v82, 0\n\t" \
    "v_mov_b32 v83, 0\n\t" \
    "v_mov_b32 v84, 0\n\t" \
    "v_mov_b32 v85, 0\n\t" \
    "v_mov_b32 v86, 0\n\t" \
    "v_mov_b32 v87, 0\n\t" \
    "v_smfmac_f32_16x16x64_bf16 v[72:75], v[36:39], a[48:55], v10\n\t" \
    "v_smfmac_f32_16x16x64_bf16 v[72:75], v[40:43], a[56:63], v10\n\t" \
    "v_smfmac_f32_16x16x64_bf16 v[72:75], v[48:51], a[0:7], v10\n\t" \
    "v_smfmac_f32_16x16x64_bf16 v[72:75], v[52:55], a[8:15], v10\n\t" \
    "v_smfmac_f32_16x16x64_bf16 v[72:75], v[56:59], a[16:23], v10\n\t" \
    "v_smfmac_f32_16x16x64_bf16 v[72:75], v[60:63], a[24:31], v10\n\t" \
    "v_smfmac_f32_16x16x64_bf16 v[72:75], v[64:67], a[32:39], v10\n\t" \
    "v_smfmac_f32_16x16x64_bf16 v[72:75], v[68:71], a[40:47], v10\n\t" \
    "v_smfmac_f32_16x16x64_bf16 v[72:75], v[44:47], a[64:71], v10\n\t" \
    "v_smfmac_f32_16x16x64_bf16 v[76:79], v[36:39], a[120:127], v10\n\t" \
    "v_smfmac_f32_16x16x64_bf16 v[76:79], v[40:43], a[128:135], v10\n\t" \
    "v_smfmac_f32_16x16x64_bf16 v[76:79], v[48:51], a[72:79], v10\n\t" \
    "v_smfmac_f32_16x16x64_bf16 v[76:79], v[52:55], a[80:87], v10\n\t" \
    "v_smfmac_f32_16x16x64_bf16 v[76:79], v[56:59], a[88:95], v10\n\t" \
    "v_smfmac_f32_16x16x64_bf16 v[76:79], v[60:63], a[96:103], v10\n\t" \
    "v_smfmac_f32_16x16x64_bf16 v[76:79], v[64:67], a[104:111], v10\n\t" \
    "v_smfmac_f32_16x16x64_bf16 v[76:79], v[68:71], a[112:119], v10\n\t" \
    "v_smfmac_f32_16x16x64_bf16 v[76:79], v[44:47], a[136:143], v10\n\t" \
    "v_smfmac_f32_16x16x64_bf16 v[80:83], v[36:39], a[192:199], v10\n\t" \
    "v_smfmac_f32_16x16x64_bf16 v[80:83], v[40:43], a[200:207], v10\n\t" \
    "v_smfmac_f32_16x16x64_bf16 v[80:83], v[48:51], a[144:151], v10\n\t" \
    "v_smfmac_f32_16x16x64_bf16 v[80:83], v[52:55], a[152:159], v10\n\t" \
    "v_smfmac_f32_16x16x64_bf16 v[80:83], v[56:59], a[160:167], v10\n\t" \
    "v_smfmac_f32_16x16x64_bf16 v[80:83], v[60:63], a[168:175], v10\n\t" \
    "v_smfmac_f32_16x16x64_bf16 v[80:83], v[64:67], a[176:183], v10\n\t" \
    "v_smfmac_f32_16x16x64_bf16 v[80:83], v[68:71], a[184:191], v10\n\t" \
    "v_smfmac_f32_16x16x64_bf16 v[80:83], v[44:47], a[208:215], v10\n\t" \
    "v_smfmac_f32_16x16x64_bf16 v[84:87], v[36:39], v[232:239], v10\n\t" \
    "v_smfmac_f32_16x16x64_bf16 v[84:87], v[40:43], v[240:247], v10\n\t" \
    "v_smfmac_f32_16x16x64_bf16 v[84:87], v[48:51], a[216:223], v10\n\t" \
    "v_smfmac_f32_16x16x64_bf16 v[84:87], v[52:55], a[224:231], v10\n\t" \
    "v_smfmac_f32_16x16x64_bf16 v[84:87], v[56:59], a[232:239], v10\n\t" \
    "v_smfmac_f32_16x16x64_bf16 v[84:87], v[60:63], a[240:247], v10\n\t" \
    "v_smfmac_f32_16x16x64_bf16 v[84:87], v[64:67], a[248:255], v10\n\t" \
    "v_smfmac_f32_16x16x64_bf16 v[84:87], v[68:71], v[224:231], v10\n\t" \
    "v_smfmac_f32_16x16x64_bf16 v[84:87], v[44:47], v[248:255], v10\n\t" \
    "v_add_f32 v72, v72, v74\n\t" \
    "v_add_f32 v73, v73, v75\n\t" \
    "v_cvt_pk_bf16_f32 v156, v72, v73\n\t" \
    "ds_write_b32 v12, v156 offset:0\n\t" \
    "v_add_f32 v76, v76, v78\n\t" \
    "v_add_f32 v77, v77, v79\n\t" \
    "v_cvt_pk_bf16_f32 v157, v76, v77\n\t" \
    "ds_write_b32 v12, v157 offset:64\n\t" \
    "v_add_f32 v80, v80, v82\n\t" \
    "v_add_f32 v81, v81, v83\n\t" \
    "v_cvt_pk_bf16_f32 v158, v80, v81\n\t" \
    "ds_write_b32 v16, v158 offset:0\n\t" \
    "v_pk_add_f32 v[84:85], v[84:85], v[86:87]\n\t" \
    "v_cvt_pk_bf16_f32 v159, v84, v85\n\t" \
    "ds_write_b32 v16, v159 offset:256\n\t" \
    "s_branch qk_Gback83_%=\n\t" \
    "qk_last74_%=:\n\t" \
    "s_endpgm\n\t" \
    "qk_last_%=:\n\t" \
    ""
#define ASM_V \
    "s_load_dwordx2 s[42:43], %3, 56\n\t" \
    "s_load_dwordx2 s[44:45], %3, 64\n\t" \
    "s_load_dwordx2 s[36:37], %3, 0x58\n\t" \
    "s_load_dwordx2 s[50:51], %3, 0x48\n\t" \
    "s_load_dwordx8 s[84:91], %3, 0\n\t" \
    "s_mov_b32 s66, %4\n\t" \
    "s_mov_b32 s67, %5\n\t" \
    "s_mov_b32 s68, 262144\n\t" \
    "s_mov_b32 s72, 0\n\t" \
    "s_mov_b32 s83, -1\n\t" \
    "s_mov_b32 s65, 0\n\t" \
    "s_lshl_b32 s82, s66, 10\n\t" \
    "s_waitcnt lgkmcnt(0)\n\t" \
    "s_add_u32 s46, s42, 2097152\n\t" \
    "s_addc_u32 s47, s43, 0\n\t" \
    "s_add_u32 m0, s82, 65536\n\t" \
    "s_nop 0\n\t" \
    "global_load_lds_dwordx4 v200, s[46:47]\n\t" \
    "s_add_u32 s46, s42, 2228224\n\t" \
    "s_addc_u32 s47, s43, 0\n\t" \
    "s_add_u32 m0, s82, 69632\n\t" \
    "s_nop 0\n\t" \
    "global_load_lds_dwordx4 v200, s[46:47]\n\t" \
    "s_add_u32 s46, s42, 2359296\n\t" \
    "s_addc_u32 s47, s43, 0\n\t" \
    "s_add_u32 m0, s82, 73728\n\t" \
    "s_nop 0\n\t" \
    "global_load_lds_dwordx4 v200, s[46:47]\n\t" \
    "s_add_u32 s46, s42, 2490368\n\t" \
    "s_addc_u32 s47, s43, 0\n\t" \
    "s_add_u32 m0, s82, 77824\n\t" \
    "s_nop 0\n\t" \
    "global_load_lds_dwordx4 v200, s[46:47]\n\t" \
    "s_add_u32 s46, s42, 2621440\n\t" \
    "s_addc_u32 s47, s43, 0\n\t" \
    "s_add_u32 m0, s82, 81920\n\t" \
    "s_nop 0\n\t" \
    "global_load_lds_dwordx4 v200, s[46:47]\n\t" \
    "s_add_u32 s46, s42, 2752512\n\t" \
    "s_addc_u32 s47, s43, 0\n\t" \
    "s_add_u32 m0, s82, 86016\n\t" \
    "s_nop 0\n\t" \
    "global_load_lds_dwordx4 v200, s[46:47]\n\t" \
    "s_add_u32 s46, s42, 2883584\n\t" \
    "s_addc_u32 s47, s43, 0\n\t" \
    "s_add_u32 m0, s82, 90112\n\t" \
    "s_nop 0\n\t" \
    "global_load_lds_dwordx4 v200, s[46:47]\n\t" \
    "s_add_u32 s46, s42, 3014656\n\t" \
    "s_addc_u32 s47, s43, 0\n\t" \
    "s_add_u32 m0, s82, 94208\n\t" \
    "s_nop 0\n\t" \
    "global_load_lds_dwordx4 v200, s[46:47]\n\t" \
    "s_add_u32 s46, s42, 3145728\n\t" \
    "s_addc_u32 s47, s43, 0\n\t" \
    "s_add_u32 m0, s82, 98304\n\t" \
    "s_nop 0\n\t" \
    "global_load_lds_dwordx4 v200, s[46:47]\n\t" \
    "s_add_u32 s46, s42, 3276800\n\t" \
    "s_addc_u32 s47, s43, 0\n\t" \
    "s_add_u32 m0, s82, 102400\n\t" \
    "s_nop 0\n\t" \
    "global_load_lds_dwordx4 v200, s[46:47]\n\t" \
    "s_add_u32 s46, s42, 3407872\n\t" \
    "s_addc_u32 s47, s43, 0\n\t" \
    "s_add_u32 m0, s82, 106496\n\t" \
    "s_nop 0\n\t" \
    "global_load_lds_dwordx4 v200, s[46:47]\n\t" \
    "s_add_u32 s46, s42, 3538944\n\t" \
    "s_addc_u32 s47, s43, 0\n\t" \
    "s_add_u32 m0, s82, 110592\n\t" \
    "s_nop 0\n\t" \
    "global_load_lds_dwordx4 v200, s[46:47]\n\t" \
    "s_add_u32 s46, s42, 3670016\n\t" \
    "s_addc_u32 s47, s43, 0\n\t" \
    "s_add_u32 m0, s82, 114688\n\t" \
    "s_nop 0\n\t" \
    "global_load_lds_dwordx4 v200, s[46:47]\n\t" \
    "s_add_u32 s46, s42, 3801088\n\t" \
    "s_addc_u32 s47, s43, 0\n\t" \
    "s_add_u32 m0, s82, 118784\n\t" \
    "s_nop 0\n\t" \
    "global_load_lds_dwordx4 v200, s[46:47]\n\t" \
    "s_add_u32 s46, s42, 3932160\n\t" \
    "s_addc_u32 s47, s43, 0\n\t" \
    "s_add_u32 m0, s82, 122880\n\t" \
    "s_nop 0\n\t" \
    "global_load_lds_dwordx4 v200, s[46:47]\n\t" \
    "s_add_u32 s46, s42, 4063232\n\t" \
    "s_addc_u32 s47, s43, 0\n\t" \
    "s_add_u32 m0, s82, 126976\n\t" \
    "s_nop 0\n\t" \
    "global_load_lds_dwordx4 v200, s[46:47]\n\t" \
    "s_add_u32 s46, s42, 4194304\n\t" \
    "s_addc_u32 s47, s43, 0\n\t" \
    "s_add_u32 m0, s82, 131072\n\t" \
    "s_nop 0\n\t" \
    "global_load_lds_dwordx4 v200, s[46:47]\n\t" \
    "s_add_u32 s46, s42, 4325376\n\t" \
    "s_addc_u32 s47, s43, 0\n\t" \
    "s_add_u32 m0, s82, 135168\n\t" \
    "s_nop 0\n\t" \
    "global_load_lds_dwordx4 v200, s[46:47]\n\t" \
    "s_add_u32 s46, s42, 4456448\n\t" \
    "s_addc_u32 s47, s43, 0\n\t" \
    "s_add_u32 m0, s82, 139264\n\t" \
    "s_nop 0\n\t" \
    "global_load_lds_dwordx4 v200, s[46:47]\n\t" \
    "s_add_u32 s46, s42, 4587520\n\t" \
    "s_addc_u32 s47, s43, 0\n\t" \
    "s_add_u32 m0, s82, 143360\n\t" \
    "s_nop 0\n\t" \
    "global_load_lds_dwordx4 v200, s[46:47]\n\t" \
    "s_mov_b32 s77, 3019898880\n\t" \
    "s_mov_b32 s78, 3054198966\n\t" \
    "s_waitcnt lgkmcnt(0)\n\t" \
    "s_mov_b32 s70, 116\n\t" \
    "vv_E1_%=:\n\t" \
    "s_memtime s[56:57]\n\t" \
    "s_memrealtime s[2:3]\n\t" \
    "s_nop 0\n\t" \
    "s_memrealtime s[4:5]\n\t" \
    "s_nop 0\n\t" \
    "s_memrealtime s[6:7]\n\t" \
    "s_nop 0\n\t" \
    "s_memrealtime s[8:9]\n\t" \
    "s_nop 0\n\t" \
    "s_memrealtime s[10:11]\n\t" \
    "s_nop 0\n\t" \
    "s_memrealtime s[12:13]\n\t" \
    "s_nop 0\n\t" \
    "s_memrealtime s[14:15]\n\t" \
    "s_nop 0\n\t" \
    "s_memrealtime s[16:17]\n\t" \
    "s_nop 0\n\t" \
    "s_memrealtime s[18:19]\n\t" \
    "s_nop 0\n\t" \
    "s_memrealtime s[20:21]\n\t" \
    "s_nop 0\n\t" \
    "s_memrealtime s[22:23]\n\t" \
    "s_nop 0\n\t" \
    "s_memrealtime s[24:25]\n\t" \
    "s_nop 0\n\t" \
    "s_memrealtime s[26:27]\n\t" \
    "s_nop 0\n\t" \
    "s_memrealtime s[28:29]\n\t" \
    "s_memtime s[54:55]\n\t" \
    "s_add_u32 s70, s70, 8\n\t" \
    "s_waitcnt lgkmcnt(0)\n\t" \
    "s_sub_u32 s69, s54, s56\n\t" \
    "s_cmp_gt_u32 s69, s70\n\t" \
    "s_cbranch_scc1 vv_E1_%=\n\t" \
    "s_add_u32 s2, s2, s4\n\t" \
    "s_add_u32 s6, s6, s8\n\t" \
    "s_add_u32 s10, s10, s12\n\t" \
    "s_add_u32 s14, s14, s16\n\t" \
    "s_add_u32 s18, s18, s20\n\t" \
    "s_add_u32 s22, s22, s24\n\t" \
    "s_add_u32 s2, s2, s6\n\t" \
    "s_add_u32 s10, s10, s14\n\t" \
    "s_add_u32 s18, s18, s22\n\t" \
    "s_add_u32 s2, s2, s26\n\t" \
    "s_add_u32 s10, s10, s18\n\t" \
    "s_add_u32 s2, s2, s10\n\t" \
    "s_mul_i32 s70, s28, 13\n\t" \
    "s_sub_u32 s70, s70, s2\n\t" \
    "s_min_u32 s70, s70, 52\n\t" \
    "s_mul_i32 s69, s69, s70\n\t" \
    "s_mul_hi_u32 s69, s69, 76695844\n\t" \
    "s_mov_b32 s75, s73\n\t" \
    "s_mov_b32 s76, s74\n\t" \
    "s_add_u32 s73, s56, s69\n\t" \
    "s_lshl_b32 s74, s28, 4\n\t" \
    "s_waitcnt vmcnt(32) lgkmcnt(0)\n\t" \
    "s_barrier\n\t" \
    "ds_read_b64_tr_b16 a[0:1], v201 offset:0\n\t" \
    "ds_read_b64_tr_b16 a[2:3], v201 offset:1024\n\t" \
    "ds_read_b64_tr_b16 a[4:5], v201 offset:8192\n\t" \
    "ds_read_b64_tr_b16 a[6:7], v201 offset:9216\n\t" \
    "ds_read_b64_tr_b16 a[72:73], v202 offset:0\n\t" \
    "ds_read_b64_tr_b16 a[74:75], v202 offset:1024\n\t" \
    "ds_read_b64_tr_b16 a[76:77], v202 offset:8192\n\t" \
    "ds_read_b64_tr_b16 a[78:79], v202 offset:9216\n\t" \
    "s_waitcnt vmcnt(28) lgkmcnt(0)\n\t" \
    "s_barrier\n\t" \
    "ds_read_b64_tr_b16 a[8:9], v201 offset:16384\n\t" \
    "ds_read_b64_tr_b16 a[10:11], v201 offset:17408\n\t" \
    "ds_read_b64_tr_b16 a[12:13], v201 offset:24576\n\t" \
    "ds_read_b64_tr_b16 a[14:15], v201 offset:25600\n\t" \
    "ds_read_b64_tr_b16 a[80:81], v202 offset:16384\n\t" \
    "ds_read_b64_tr_b16 a[82:83], v202 offset:17408\n\t" \
    "ds_read_b64_tr_b16 a[84:85], v202 offset:24576\n\t" \
    "ds_read_b64_tr_b16 a[86:87], v202 offset:25600\n\t" \
    "s_waitcnt vmcnt(24) lgkmcnt(0)\n\t" \
    "s_barrier\n\t" \
    "ds_read_b64_tr_b16 a[16:17], v201 offset:32768\n\t" \
    "ds_read_b64_tr_b16 a[18:19], v201 offset:33792\n\t" \
    "ds_read_b64_tr_b16 a[20:21], v201 offset:40960\n\t" \
    "ds_read_b64_tr_b16 a[22:23], v201 offset:41984\n\t" \
    "ds_read_b64_tr_b16 a[88:89], v202 offset:32768\n\t" \
    "ds_read_b64_tr_b16 a[90:91], v202 offset:33792\n\t" \
    "ds_read_b64_tr_b16 a[92:93], v202 offset:40960\n\t" \
    "ds_read_b64_tr_b16 a[94:95], v202 offset:41984\n\t" \
    "s_waitcnt vmcnt(20) lgkmcnt(0)\n\t" \
    "s_barrier\n\t" \
    "ds_read_b64_tr_b16 a[24:25], v201 offset:49152\n\t" \
    "ds_read_b64_tr_b16 a[26:27], v201 offset:50176\n\t" \
    "ds_read_b64_tr_b16 a[28:29], v201 offset:57344\n\t" \
    "ds_read_b64_tr_b16 a[30:31], v201 offset:58368\n\t" \
    "ds_read_b64_tr_b16 a[96:97], v202 offset:49152\n\t" \
    "ds_read_b64_tr_b16 a[98:99], v202 offset:50176\n\t" \
    "ds_read_b64_tr_b16 a[100:101], v202 offset:57344\n\t" \
    "ds_read_b64_tr_b16 a[102:103], v202 offset:58368\n\t" \
    "s_add_u32 s46, s44, 0\n\t" \
    "s_addc_u32 s47, s45, 0\n\t" \
    "s_mul_i32 s69, s82, 9\n\t" \
    "s_add_u32 m0, s69, 0\n\t" \
    "s_nop 0\n\t" \
    "global_load_lds_dwordx4 v203, s[46:47]\n\t" \
    "s_add_u32 m0, s69, 1024\n\t" \
    "s_nop 0\n\t" \
    "global_load_lds_dwordx4 v204, s[46:47]\n\t" \
    "s_add_u32 m0, s69, 2048\n\t" \
    "s_nop 0\n\t" \
    "global_load_lds_dwordx4 v205, s[46:47]\n\t" \
    "s_add_u32 m0, s69, 3072\n\t" \
    "s_nop 0\n\t" \
    "global_load_lds_dwordx4 v206, s[46:47]\n\t" \
    "s_add_u32 m0, s69, 4096\n\t" \
    "s_nop 0\n\t" \
    "global_load_lds_dwordx4 v207, s[46:47]\n\t" \
    "s_add_u32 m0, s69, 5120\n\t" \
    "s_nop 0\n\t" \
    "global_load_lds_dwordx4 v208, s[46:47]\n\t" \
    "s_add_u32 m0, s69, 6144\n\t" \
    "s_nop 0\n\t" \
    "global_load_lds_dwordx4 v209, s[46:47]\n\t" \
    "s_add_u32 m0, s69, 7168\n\t" \
    "s_nop 0\n\t" \
    "global_load_lds_dwordx4 v210, s[46:47]\n\t" \
    "s_add_u32 m0, s69, 8192\n\t" \
    "s_nop 0\n\t" \
    "global_load_lds_dwordx4 v211, s[46:47]\n\t" \
    "s_waitcnt vmcnt(25) lgkmcnt(0)\n\t" \
    "s_barrier\n\t" \
    "v_add_u32 v201, 0x10000, v201\n\t" \
    "v_add_u32 v202, 0x10000, v202\n\t" \
    "ds_read_b64_tr_b16 a[32:33], v201 offset:0\n\t" \
    "ds_read_b64_tr_b16 a[34:35], v201 offset:1024\n\t" \
    "ds_read_b64_tr_b16 a[36:37], v201 offset:8192\n\t" \
    "ds_read_b64_tr_b16 a[38:39], v201 offset:9216\n\t" \
    "ds_read_b64_tr_b16 a[104:105], v202 offset:0\n\t" \
    "ds_read_b64_tr_b16 a[106:107], v202 offset:1024\n\t" \
    "ds_read_b64_tr_b16 a[108:109], v202 offset:8192\n\t" \
    "ds_read_b64_tr_b16 a[110:111], v202 offset:9216\n\t" \
    "s_waitcnt vmcnt(21) lgkmcnt(0)\n\t" \
    "s_barrier\n\t" \
    "ds_read_b64_tr_b16 a[40:41], v201 offset:16384\n\t" \
    "ds_read_b64_tr_b16 a[42:43], v201 offset:17408\n\t" \
    "ds_read_b64_tr_b16 a[44:45], v201 offset:24576\n\t" \
    "ds_read_b64_tr_b16 a[46:47], v201 offset:25600\n\t" \
    "ds_read_b64_tr_b16 a[112:113], v202 offset:16384\n\t" \
    "ds_read_b64_tr_b16 a[114:115], v202 offset:17408\n\t" \
    "ds_read_b64_tr_b16 a[116:117], v202 offset:24576\n\t" \
    "ds_read_b64_tr_b16 a[118:119], v202 offset:25600\n\t" \
    "s_add_u32 s46, s44, 36864\n\t" \
    "s_addc_u32 s47, s45, 0\n\t" \
    "s_mul_i32 s69, s82, 9\n\t" \
    "s_add_u32 m0, s69, 36864\n\t" \
    "s_nop 0\n\t" \
    "global_load_lds_dwordx4 v203, s[46:47]\n\t" \
    "s_add_u32 m0, s69, 37888\n\t" \
    "s_nop 0\n\t" \
    "global_load_lds_dwordx4 v204, s[46:47]\n\t" \
    "s_add_u32 m0, s69, 38912\n\t" \
    "s_nop 0\n\t" \
    "global_load_lds_dwordx4 v205, s[46:47]\n\t" \
    "s_add_u32 m0, s69, 39936\n\t" \
    "s_nop 0\n\t" \
    "global_load_lds_dwordx4 v206, s[46:47]\n\t" \
    "s_add_u32 m0, s69, 40960\n\t" \
    "s_nop 0\n\t" \
    "global_load_lds_dwordx4 v207, s[46:47]\n\t" \
    "s_add_u32 m0, s69, 41984\n\t" \
    "s_nop 0\n\t" \
    "global_load_lds_dwordx4 v208, s[46:47]\n\t" \
    "s_add_u32 m0, s69, 43008\n\t" \
    "s_nop 0\n\t" \
    "global_load_lds_dwordx4 v209, s[46:47]\n\t" \
    "s_add_u32 m0, s69, 44032\n\t" \
    "s_nop 0\n\t" \
    "global_load_lds_dwordx4 v210, s[46:47]\n\t" \
    "s_add_u32 m0, s69, 45056\n\t" \
    "s_nop 0\n\t" \
    "global_load_lds_dwordx4 v211, s[46:47]\n\t" \
    "s_waitcnt vmcnt(26) lgkmcnt(0)\n\t" \
    "s_barrier\n\t" \
    "ds_read_b64_tr_b16 a[48:49], v201 offset:32768\n\t" \
    "ds_read_b64_tr_b16 a[50:51], v201 offset:33792\n\t" \
    "ds_read_b64_tr_b16 a[52:53], v201 offset:40960\n\t" \
    "ds_read_b64_tr_b16 a[54:55], v201 offset:41984\n\t" \
    "ds_read_b64_tr_b16 a[120:121], v202 offset:32768\n\t" \
    "ds_read_b64_tr_b16 a[122:123], v202 offset:33792\n\t" \
    "ds_read_b64_tr_b16 a[124:125], v202 offset:40960\n\t" \
    "ds_read_b64_tr_b16 a[126:127], v202 offset:41984\n\t" \
    "s_waitcnt vmcnt(22) lgkmcnt(0)\n\t" \
    "s_barrier\n\t" \
    "ds_read_b64_tr_b16 a[56:57], v201 offset:49152\n\t" \
    "ds_read_b64_tr_b16 a[58:59], v201 offset:50176\n\t" \
    "ds_read_b64_tr_b16 a[60:61], v201 offset:57344\n\t" \
    "ds_read_b64_tr_b16 a[62:63], v201 offset:58368\n\t" \
    "ds_read_b64_tr_b16 a[128:129], v202 offset:49152\n\t" \
    "ds_read_b64_tr_b16 a[130:131], v202 offset:50176\n\t" \
    "ds_read_b64_tr_b16 a[132:133], v202 offset:57344\n\t" \
    "ds_read_b64_tr_b16 a[134:135], v202 offset:58368\n\t" \
    "s_add_u32 s46, s44, 73728\n\t" \
    "s_addc_u32 s47, s45, 0\n\t" \
    "s_mul_i32 s69, s82, 9\n\t" \
    "s_add_u32 m0, s69, 73728\n\t" \
    "s_nop 0\n\t" \
    "global_load_lds_dwordx4 v203, s[46:47]\n\t" \
    "s_add_u32 m0, s69, 74752\n\t" \
    "s_nop 0\n\t" \
    "global_load_lds_dwordx4 v204, s[46:47]\n\t" \
    "s_add_u32 m0, s69, 75776\n\t" \
    "s_nop 0\n\t" \
    "global_load_lds_dwordx4 v205, s[46:47]\n\t" \
    "s_add_u32 m0, s69, 76800\n\t" \
    "s_nop 0\n\t" \
    "global_load_lds_dwordx4 v206, s[46:47]\n\t" \
    "s_add_u32 m0, s69, 77824\n\t" \
    "s_nop 0\n\t" \
    "global_load_lds_dwordx4 v207, s[46:47]\n\t" \
    "s_add_u32 m0, s69, 78848\n\t" \
    "s_nop 0\n\t" \
    "global_load_lds_dwordx4 v208, s[46:47]\n\t" \
    "s_add_u32 m0, s69, 79872\n\t" \
    "s_nop 0\n\t" \
    "global_load_lds_dwordx4 v209, s[46:47]\n\t" \
    "s_add_u32 m0, s69, 80896\n\t" \
    "s_nop 0\n\t" \
    "global_load_lds_dwordx4 v210, s[46:47]\n\t" \
    "s_add_u32 m0, s69, 81920\n\t" \
    "s_nop 0\n\t" \
    "global_load_lds_dwordx4 v211, s[46:47]\n\t" \
    "v_mbcnt_lo_u32_b32 v140, -1, 0\n\t" \
    "v_mbcnt_hi_u32_b32 v140, -1, v140\n\t" \
    "v_lshlrev_b32 v140, 6, v140\n\t" \
    "v_add_u32 v140, 6225920, v140\n\t" \
    "v_mov_b32 v141, 6234112\n\t" \
    "global_load_dword v141, v141, s[36:37] sc1\n\t" \
    "global_load_dwordx2 v[142:143], v140, s[36:37] sc1\n\t" \
    "global_load_dword v144, v140, s[36:37] offset:2048 sc1\n\t" \
    "s_waitcnt vmcnt(30) lgkmcnt(0)\n\t" \
    "s_barrier\n\t" \
    "v_add_u32 v201, 0x10000, v201\n\t" \
    "v_add_u32 v202, 0x10000, v202\n\t" \
    "ds_read_b64_tr_b16 a[64:65], v201 offset:0\n\t" \
    "ds_read_b64_tr_b16 a[66:67], v201 offset:1024\n\t" \
    "ds_read_b64_tr_b16 a[68:69], v201 offset:8192\n\t" \
    "ds_read_b64_tr_b16 a[70:71], v201 offset:9216\n\t" \
    "ds_read_b64_tr_b16 a[136:137], v202 offset:0\n\t" \
    "ds_read_b64_tr_b16 a[138:139], v202 offset:1024\n\t" \
    "ds_read_b64_tr_b16 a[140:141], v202 offset:8192\n\t" \
    "ds_read_b64_tr_b16 a[142:143], v202 offset:9216\n\t" \
    "v_add_u32 v201, 0xfffe0000, v201\n\t" \
    "v_add_u32 v202, 0xfffe0000, v202\n\t" \
    "s_waitcnt lgkmcnt(0)\n\t" \
    "s_barrier\n\t" \
    "s_add_u32 s46, s44, 110592\n\t" \
    "s_addc_u32 s47, s45, 0\n\t" \
    "s_mul_i32 s69, s82, 9\n\t" \
    "s_add_u32 m0, s69, 110592\n\t" \
    "s_nop 0\n\t" \
    "global_load_lds_dwordx4 v203, s[46:47]\n\t" \
    "s_add_u32 m0, s69, 111616\n\t" \
    "s_nop 0\n\t" \
    "global_load_lds_dwordx4 v204, s[46:47]\n\t" \
    "s_add_u32 m0, s69, 112640\n\t" \
    "s_nop 0\n\t" \
    "global_load_lds_dwordx4 v205, s[46:47]\n\t" \
    "s_add_u32 m0, s69, 113664\n\t" \
    "s_nop 0\n\t" \
    "global_load_lds_dwordx4 v206, s[46:47]\n\t" \
    "s_add_u32 m0, s69, 114688\n\t" \
    "s_nop 0\n\t" \
    "global_load_lds_dwordx4 v207, s[46:47]\n\t" \
    "s_add_u32 m0, s69, 115712\n\t" \
    "s_nop 0\n\t" \
    "global_load_lds_dwordx4 v208, s[46:47]\n\t" \
    "s_add_u32 m0, s69, 116736\n\t" \
    "s_nop 0\n\t" \
    "global_load_lds_dwordx4 v209, s[46:47]\n\t" \
    "s_add_u32 m0, s69, 117760\n\t" \
    "s_nop 0\n\t" \
    "global_load_lds_dwordx4 v210, s[46:47]\n\t" \
    "s_add_u32 m0, s69, 118784\n\t" \
    "s_nop 0\n\t" \
    "global_load_lds_dwordx4 v211, s[46:47]\n\t" \
    "s_waitcnt vmcnt(21) lgkmcnt(0)\n\t" \
    "s_barrier\n\t" \
    "ds_read_b64_tr_b16 a[144:145], v212 offset:0\n\t" \
    "ds_read_b64_tr_b16 a[146:147], v212 offset:2304\n\t" \
    "ds_read_b64_tr_b16 a[148:149], v212 offset:18432\n\t" \
    "ds_read_b64_tr_b16 a[150:151], v212 offset:20736\n\t" \
    "s_waitcnt lgkmcnt(6)\n\t" \
    "ds_read_b64_tr_b16 a[176:177], v213 offset:0\n\t" \
    "ds_read_b64_tr_b16 a[178:179], v213 offset:2304\n\t" \
    "ds_read_b64_tr_b16 a[180:181], v213 offset:18432\n\t" \
    "ds_read_b64_tr_b16 a[182:183], v213 offset:20736\n\t" \
    "s_waitcnt lgkmcnt(6)\n\t" \
    "ds_read_b64_tr_b16 a[208:209], v212 offset:128\n\t" \
    "ds_read_b64_tr_b16 a[210:211], v212 offset:2432\n\t" \
    "ds_read_b64_tr_b16 a[212:213], v212 offset:18560\n\t" \
    "ds_read_b64_tr_b16 a[214:215], v212 offset:20864\n\t" \
    "s_waitcnt lgkmcnt(6)\n\t" \
    "ds_read_b64_tr_b16 a[240:241], v213 offset:128\n\t" \
    "ds_read_b64_tr_b16 a[242:243], v213 offset:2432\n\t" \
    "ds_read_b64_tr_b16 a[244:245], v213 offset:18560\n\t" \
    "ds_read_b64_tr_b16 a[246:247], v213 offset:20864\n\t" \
    "s_waitcnt lgkmcnt(6)\n\t" \
    "ds_read_b64_tr_b16 a[152:153], v214 offset:0\n\t" \
    "ds_read_b64_tr_b16 a[154:155], v214 offset:2304\n\t" \
    "ds_read_b64_tr_b16 a[156:157], v214 offset:18432\n\t" \
    "ds_read_b64_tr_b16 a[158:159], v214 offset:20736\n\t" \
    "s_waitcnt lgkmcnt(6)\n\t" \
    "ds_read_b64_tr_b16 a[184:185], v215 offset:0\n\t" \
    "ds_read_b64_tr_b16 a[186:187], v215 offset:2304\n\t" \
    "ds_read_b64_tr_b16 a[188:189], v215 offset:18432\n\t" \
    "ds_read_b64_tr_b16 a[190:191], v215 offset:20736\n\t" \
    "s_waitcnt lgkmcnt(6)\n\t" \
    "ds_read_b64_tr_b16 a[216:217], v214 offset:128\n\t" \
    "ds_read_b64_tr_b16 a[218:219], v214 offset:2432\n\t" \
    "ds_read_b64_tr_b16 a[220:221], v214 offset:18560\n\t" \
    "ds_read_b64_tr_b16 a[222:223], v214 offset:20864\n\t" \
    "s_waitcnt lgkmcnt(6)\n\t" \
    "ds_read_b64_tr_b16 a[248:249], v215 offset:128\n\t" \
    "ds_read_b64_tr_b16 a[250:251], v215 offset:2432\n\t" \
    "ds_read_b64_tr_b16 a[252:253], v215 offset:18560\n\t" \
    "ds_read_b64_tr_b16 a[254:255], v215 offset:20864\n\t" \
    "s_waitcnt lgkmcnt(6)\n\t" \
    "ds_read_b64_tr_b16 v[240:241], v24 offset:0\n\t" \
    "ds_read_b64_tr_b16 v[242:243], v24 offset:2304\n\t" \
    "ds_read_b64_tr_b16 v[244:245], v24 offset:18432\n\t" \
    "ds_read_b64_tr_b16 v[246:247], v24 offset:20736\n\t" \
    "v_add_u32 v212, 0x12000, v212\n\t" \
    "v_add_u32 v213, 0x12000, v213\n\t" \
    "v_add_u32 v214, 0x12000, v214\n\t" \
    "v_add_u32 v215, 0x12000, v215\n\t" \
    "v_add_u32 v24, 0x12000, v24\n\t" \
    "s_waitcnt vmcnt(9)\n\t" \
    "vv_C2_%=:\n\t" \
    "v_cmp_ne_u32 s[54:55], v142, v144\n\t" \
    "v_sub_u32 v142, v142, v141\n\t" \
    "v_cmp_ne_u32 vcc, 1, v142\n\t" \
    "s_nop 1\n\t" \
    "s_or_b64 vcc, vcc, s[54:55]\n\t" \
    "s_cbranch_scc0 vv_Cok3_%=\n\t" \
    "s_cmp_eq_u32 s68, 0\n\t" \
    "s_cbranch_scc1 vv_F5_%=\n\t" \
    "s_sub_u32 s68, s68, 1\n\t" \
    "s_sleep 6\n\t" \
    "s_branch vv_Cag4_%=\n\t" \
    "vv_F5_%=:\n\t" \
    "v_mov_b32 v144, 5767424\n\t" \
    "v_mov_b32 v145, 4\n\t" \
    "global_store_dword v144, v145, s[36:37] sc1\n\t" \
    "s_waitcnt vmcnt(0)\n\t" \
    "s_branch vv_Cok3_%=\n\t" \
    "vv_Cag4_%=:\n\t" \
    "global_load_dwordx2 v[142:143], v140, s[36:37] sc1\n\t" \
    "global_load_dword v144, v140, s[36:37] offset:2048 sc1\n\t" \
    "s_waitcnt vmcnt(0)\n\t" \
    "s_branch vv_C2_%=\n\t" \
    "vv_Cok3_%=:\n\t" \
    "s_cmp_lg_u32 s66, 0\n\t" \
    "s_cbranch_scc1 vv_Ca6_%=\n\t" \
    "v_mov_b32 v140, 5836800\n\t" \
    "s_mov_b64 exec, 1\n\t" \
    "global_atomic_add v140, v142, s[36:37] sc1\n\t" \
    "s_mov_b64 exec, -1\n\t" \
    "vv_Ca6_%=:\n\t" \
    "v_mov_b32_dpp v140, v143 quad_perm:[1,0,3,2] row_mask:0xf bank_mask:0xf\n\t" \
    "v_cmp_lt_u32 s[44:45], v140, v143\n\t" \
    "s_nop 1\n\t" \
    "s_bitcmp1_b32 s44, 0\n\t" \
    "s_cselect_b32 s62, 8192, 0\n\t" \
    "s_bitcmp1_b32 s44, 2\n\t" \
    "s_cselect_b32 s63, 8192, 0\n\t" \
    "s_or_b32 s63, s63, 0x200000\n\t" \
    "v_min_u32 v140, v140, v143\n\t" \
    "v_cmp_lt_u32 vcc, 660, v140\n\t" \
    "s_nop 1\n\t" \
    "s_cmp_lg_u64 vcc, 0\n\t" \
    "s_cselect_b32 s69, 1, 0\n\t" \
    "s_mul_i32 s70, s69, 160\n\t" \
    "s_add_u32 s86, s86, s70\n\t" \
    "s_mul_i32 s70, s69, 536\n\t" \
    "s_add_u32 s87, s87, s70\n\t" \
    "s_mul_i32 s70, s69, 256\n\t" \
    "s_add_u32 s88, s88, s70\n\t" \
    "s_waitcnt vmcnt(0) lgkmcnt(0)\n\t" \
    "s_barrier\n\t" \
    "ds_read_b64_tr_b16 a[160:161], v212 offset:0\n\t" \
    "ds_read_b64_tr_b16 a[162:163], v212 offset:2304\n\t" \
    "ds_read_b64_tr_b16 a[164:165], v212 offset:18432\n\t" \
    "ds_read_b64_tr_b16 a[166:167], v212 offset:20736\n\t" \
    "s_waitcnt lgkmcnt(6)\n\t" \
    "ds_read_b64_tr_b16 a[192:193], v213 offset:0\n\t" \
    "ds_read_b64_tr_b16 a[194:195], v213 offset:2304\n\t" \
    "ds_read_b64_tr_b16 a[196:197], v213 offset:18432\n\t" \
    "ds_read_b64_tr_b16 a[198:199], v213 offset:20736\n\t" \
    "s_waitcnt lgkmcnt(6)\n\t" \
    "ds_read_b64_tr_b16 a[224:225], v212 offset:128\n\t" \
    "ds_read_b64_tr_b16 a[226:227], v212 offset:2432\n\t" \
    "ds_read_b64_tr_b16 a[228:229], v212 offset:18560\n\t" \
    "ds_read_b64_tr_b16 a[230:231], v212 offset:20864\n\t" \
    "s_waitcnt lgkmcnt(6)\n\t" \
    "ds_read_b64_tr_b16 v[224:225], v213 offset:128\n\t" \
    "ds_read_b64_tr_b16 v[226:227], v213 offset:2432\n\t" \
    "ds_read_b64_tr_b16 v[228:229], v213 offset:18560\n\t" \
    "ds_read_b64_tr_b16 v[230:231], v213 offset:20864\n\t" \
    "s_waitcnt lgkmcnt(6)\n\t" \
    "ds_read_b64_tr_b16 a[168:169], v214 offset:0\n\t" \
    "ds_read_b64_tr_b16 a[170:171], v214 offset:2304\n\t" \
    "ds_read_b64_tr_b16 a[172:173], v214 offset:18432\n\t" \
    "ds_read_b64_tr_b16 a[174:175], v214 offset:20736\n\t" \
    "s_waitcnt lgkmcnt(6)\n\t" \
    "ds_read_b64_tr_b16 a[200:201], v215 offset:0\n\t" \
    "ds_read_b64_tr_b16 a[202:203], v215 offset:2304\n\t" \
    "ds_read_b64_tr_b16 a[204:205], v215 offset:18432\n\t" \
    "ds_read_b64_tr_b16 a[206:207], v215 offset:20736\n\t" \
    "s_waitcnt lgkmcnt(6)\n\t" \
    "ds_read_b64_tr_b16 a[232:233], v214 offset:128\n\t" \
    "ds_read_b64_tr_b16 a[234:235], v214 offset:2432\n\t" \
    "ds_read_b64_tr_b16 a[236:237], v214 offset:18560\n\t" \
    "ds_read_b64_tr_b16 a[238:239], v214 offset:20864\n\t" \
    "s_waitcnt lgkmcnt(6)\n\t" \
    "ds_read_b64_tr_b16 v[232:233], v215 offset:128\n\t" \
    "ds_read_b64_tr_b16 v[234:235], v215 offset:2432\n\t" \
    "ds_read_b64_tr_b16 v[236:237], v215 offset:18560\n\t" \
    "ds_read_b64_tr_b16 v[238:239], v215 offset:20864\n\t" \
    "s_waitcnt lgkmcnt(6)\n\t" \
    "ds_read_b64_tr_b16 v[248:249], v24 offset:0\n\t" \
    "ds_read_b64_tr_b16 v[250:251], v24 offset:2304\n\t" \
    "ds_read_b64_tr_b16 v[252:253], v24 offset:18432\n\t" \
    "ds_read_b64_tr_b16 v[254:255], v24 offset:20736\n\t" \
    "v_add_u32 v212, 0x12000, v212\n\t" \
    "v_add_u32 v213, 0x12000, v213\n\t" \
    "v_add_u32 v214, 0x12000, v214\n\t" \
    "v_add_u32 v215, 0x12000, v215\n\t" \
    "v_add_u32 v24, 0x12000, v24\n\t" \
    "s_waitcnt lgkmcnt(0)\n\t" \
    "s_barrier\n\t" \
    "s_mov_b32 s79, s74\n\t" \
    "s_sub_u32 s80, s74, 32000\n\t" \
    "v_mov_b32 v72, 0\n\t" \
    "v_mov_b32 v73, 0\n\t" \
    "v_mov_b32 v74, 0\n\t" \
    "v_mov_b32 v75, 0\n\t" \
    "v_mov_b32 v76, 0\n\t" \
    "v_mov_b32 v77, 0\n\t" \
    "v_mov_b32 v78, 0\n\t" \
    "v_mov_b32 v79, 0\n\t" \
    "v_mov_b32 v108, 0\n\t" \
    "v_mov_b32 v109, 0\n\t" \
    "v_mov_b32 v110, 0\n\t" \
    "v_mov_b32 v111, 0\n\t" \
    "v_mov_b32 v112, 0\n\t" \
    "v_mov_b32 v113, 0\n\t" \
    "v_mov_b32 v114, 0\n\t" \
    "v_mov_b32 v115, 0\n\t" \
    "v_mov_b32 v116, 0\n\t" \
    "v_mov_b32 v117, 0\n\t" \
    "v_mov_b32 v118, 0\n\t" \
    "v_mov_b32 v119, 0\n\t" \
    "v_mov_b32 v120, 0\n\t" \
    "v_mov_b32 v121, 0\n\t" \
    "v_mov_b32 v122, 0\n\t" \
    "v_mov_b32 v123, 0\n\t" \
    "v_mov_b32 v124, 0\n\t" \
    "v_mov_b32 v125, 0\n\t" \
    "v_mov_b32 v126, 0\n\t" \
    "v_mov_b32 v127, 0\n\t" \
    "s_add_u32 s69, s65, 0\n\t" \
    "s_and_b32 s69, s69, 7\n\t" \
    "s_lshl_b32 s69, s69, 18\n\t" \
    "s_add_u32 s69, s69, s62\n\t" \
    "s_add_u32 s46, s36, s69\n\t" \
    "s_addc_u32 s47, s37, 0\n\t" \
    "s_add_u32 s70, s65, 0\n\t" \
    "s_mul_i32 s69, s70, 171\n\t" \
    "s_lshr_b32 s69, s69, 9\n\t" \
    "s_mul_i32 s70, s70, 589824\n\t" \
    "s_mul_i32 s71, s69, 327680\n\t" \
    "s_add_u32 s70, s70, s71\n\t" \
    "s_lshl1_add_u32 s69, s69, 4\n\t" \
    "s_bitcmp1_b64 s[44:45], s69\n\t" \
    "s_cselect_b32 s69, 8192, 0\n\t" \
    "s_add_u32 s69, s69, s70\n\t" \
    "s_add_u32 s69, s69, 123731968\n\t" \
    "s_add_u32 s38, s36, s69\n\t" \
    "s_addc_u32 s39, s37, 0\n\t" \
    "s_and_b32 s69, s69, 0xffe00000\n\t" \
    "s_add_u32 s69, s69, 1823744\n\t" \
    "v_mov_b32 v17, s69\n\t" \
    "global_load_dword v17, v17, s[36:37]\n\t" \
    "s_and_b32 s69, s65, 7\n\t" \
    "s_lshl_b32 s69, s69, 18\n\t" \
    "s_add_u32 s69, s69, s63\n\t" \
    "s_add_u32 s52, s36, s69\n\t" \
    "s_addc_u32 s53, s37, 0\n\t" \
    "v_mov_b32 v100, 0\n\t" \
    "v_mov_b32 v101, 0\n\t" \
    "v_mov_b32 v102, 0\n\t" \
    "v_mov_b32 v103, 0\n\t" \
    "s_sub_u32 s70, s79, s74\n\t" \
    "s_max_i32 s70, s70, 0\n\t" \
    "s_min_i32 s70, s70, 16000\n\t" \
    "s_lshl_b32 s70, s70, 1\n\t" \
    "s_mul_hi_u32 s70, s70, s77\n\t" \
    "s_add_u32 s70, s70, s73\n\t" \
    "s_cmp_eq_u32 s66, 1\n\t" \
    "s_cbranch_scc1 vv_loop10_%=\n\t" \
    "s_cmp_eq_u32 s66, 2\n\t" \
    "s_cbranch_scc1 vv_loop11_%=\n\t" \
    "s_cmp_eq_u32 s66, 3\n\t" \
    "s_cbranch_scc1 vv_loop12_%=\n\t" \
    ".p2align 6\n\t" \
    "s_nop 0\n\t" \
    "s_nop 0\n\t" \
    "s_nop 0\n\t" \
    "s_nop 0\n\t" \
    "s_nop 0\n\t" \
    "s_nop 0\n\t" \
    "vv_loop9_%=:\n\t" \
    "s_getpc_b64 s[30:31]\n\t" \
    "vv_Wp15_%=:\n\t" \
    "s_add_u32 s30, s30, vv_We16_%=-vv_Wp15_%=\n\t" \
    "s_addc_u32 s31, s31, 0\n\t" \
    "s_sub_u32 s70, s70, 96\n\t" \
    "vv_W14_%=:\n\t" \
    "s_memtime s[56:57]\n\t" \
    "s_waitcnt lgkmcnt(0)\n\t" \
    "s_sub_u32 s69, s70, s56\n\t" \
    "s_cmp_gt_i32 s69, 80\n\t" \
    "s_cbranch_scc1 vv_W14_%=\n\t" \
    "s_max_i32 s69, s69, 0\n\t" \
    "s_and_b32 s69, s69, -4\n\t" \
    "s_sub_u32 s30, s30, s69\n\t" \
    "s_subb_u32 s31, s31, 0\n\t" \
    "s_setpc_b64 s[30:31]\n\t" \
    "s_nop 0\n\t" \
    "s_nop 0\n\t" \
    "s_nop 0\n\t" \
    "s_nop 0\n\t" \
    "s_nop 0\n\t" \
    "s_nop 0\n\t" \
    "s_nop 0\n\t" \
    "s_nop 0\n\t" \
    "s_nop 0\n\t" \
    "s_nop 0\n\t" \
    "s_nop 0\n\t" \
    "s_nop 0\n\t" \
    "s_nop 0\n\t" \
    "s_nop 0\n\t" \
    "s_nop 0\n\t" \
    "s_nop 0\n\t" \
    "s_nop 0\n\t" \
    "s_nop 0\n\t" \
    "s_nop 0\n\t" \
    "s_nop 0\n\t" \
    "vv_We16_%=:\n\t" \
    "vv_G17_%=:\n\t" \
    "global_load_dwordx4 v[36:39], v1, s[46:47] sc1\n\t" \
    "global_load_dword v141, v2, s[46:47] sc1\n\t" \
    "global_load_dwordx4 v[40:43], v23, s[46:47] sc1\n\t" \
    "s_waitcnt vmcnt(2)\n\t" \
    "ds_write_b128 v3, v[36:39]\n\t" \
    "v_smfmac_f32_16x16x64_bf16 v[72:75], v[36:39], a[0:7], v10\n\t" \
    "s_waitcnt vmcnt(1)\n\t" \
    "ds_write_b32 v22, v141 offset:19456\n\t" \
    "s_waitcnt vmcnt(0)\n\t" \
    "ds_write_b128 v3, v[40:43] offset:1024\n\t" \
    "v_max3_u32 v140, v36, v40, v141\n\t" \
    "v_cmp_eq_u32 vcc, -1, v140\n\t" \
    "s_cbranch_vccnz vv_Gbad18_%=\n\t" \
    "vv_Gc19_%=:\n\t" \
    "s_waitcnt lgkmcnt(0)\n\t" \
    "s_barrier\n\t" \
    "ds_read_b128 v[48:51], v4\n\t" \
    "ds_read_b128 v[52:55], v5\n\t" \
    "v_smfmac_f32_16x16x64_bf16 v[76:79], v[36:39], a[72:79], v10\n\t" \
    "ds_read_b128 v[56:59], v6\n\t" \
    "ds_read_b128 v[60:63], v7\n\t" \
    "v_smfmac_f32_16x16x64_bf16 v[72:75], v[40:43], a[8:15], v10\n\t" \
    "ds_read_b128 v[64:67], v8\n\t" \
    "v_smfmac_f32_16x16x64_bf16 v[76:79], v[40:43], a[80:87], v10\n\t" \
    "ds_read_b128 v[68:71], v9\n\t" \
    "s_waitcnt lgkmcnt(5)\n\t" \
    "v_smfmac_f32_16x16x64_bf16 v[72:75], v[48:51], a[16:23], v10\n\t" \
    "ds_read_b128 v[44:47], v0 offset:19456\n\t" \
    "s_waitcnt lgkmcnt(5)\n\t" \
    "v_smfmac_f32_16x16x64_bf16 v[72:75], v[52:55], a[24:31], v10\n\t" \
    "s_waitcnt lgkmcnt(4)\n\t" \
    "v_smfmac_f32_16x16x64_bf16 v[72:75], v[56:59], a[32:39], v10\n\t" \
    "s_waitcnt lgkmcnt(3)\n\t" \
    "v_smfmac_f32_16x16x64_bf16 v[72:75], v[60:63], a[40:47], v10\n\t" \
    "s_waitcnt lgkmcnt(2)\n\t" \
    "v_smfmac_f32_16x16x64_bf16 v[72:75], v[64:67], a[48:55], v10\n\t" \
    "s_waitcnt lgkmcnt(1)\n\t" \
    "v_smfmac_f32_16x16x64_bf16 v[72:75], v[68:71], a[56:63], v10\n\t" \
    "s_waitcnt lgkmcnt(0)\n\t" \
    "v_smfmac_f32_16x16x64_bf16 v[72:75], v[44:47], a[64:71], v10\n\t" \
    "s_nop 2\n\t" \
    "v_smfmac_f32_16x16x64_bf16 v[76:79], v[48:51], a[88:95], v10\n\t" \
    "s_nop 2\n\t" \
    "v_smfmac_f32_16x16x64_bf16 v[76:79], v[52:55], a[96:103], v10\n\t" \
    "v_add_f32 v72, v72, v74\n\t" \
    "v_smfmac_f32_16x16x64_bf16 v[76:79], v[56:59], a[104:111], v10\n\t" \
    "v_add_f32 v73, v73, v75\n\t" \
    "v_smfmac_f32_16x16x64_bf16 v[76:79], v[60:63], a[112:119], v10\n\t" \
    "v_cvt_pk_bf16_f32 v80, v72, v73\n\t" \
    "v_smfmac_f32_16x16x64_bf16 v[76:79], v[64:67], a[120:127], v10\n\t" \
    "ds_write_b32 v12, v80 offset:0\n\t" \
    "v_smfmac_f32_16x16x64_bf16 v[76:79], v[68:71], a[128:135], v10\n\t" \
    "v_smfmac_f32_16x16x64_bf16 v[76:79], v[44:47], a[136:143], v10\n\t" \
    "s_nop 6\n\t" \
    "v_cmp_u_f32 vcc, v72, v73\n\t" \
    "v_pk_add_f32 v[76:77], v[76:77], v[78:79]\n\t" \
    "v_cvt_pk_bf16_f32 v81, v76, v77\n\t" \
    "ds_write_b32 v12, v81 offset:256\n\t" \
    "s_cbranch_vccnz vv_Gslow20_%=\n\t" \
    "vv_Gback21_%=:\n\t" \
    "s_waitcnt lgkmcnt(0)\n\t" \
    "s_barrier\n\t" \
    "ds_read_b64_tr_b16 v[84:85], v13 offset:0\n\t" \
    "v_smfmac_f32_16x16x64_bf16 v[100:103], v[36:39], a[0:7], v10\n\t" \
    "ds_read_b64_tr_b16 v[86:87], v13 offset:64\n\t" \
    "ds_read_b64_tr_b16 v[92:93], v13 offset:1152\n\t" \
    "v_smfmac_f32_16x16x64_bf16 v[100:103], v[36:39], a[0:7], v10\n\t" \
    "ds_read_b64_tr_b16 v[94:95], v13 offset:1216\n\t" \
    "ds_read_b64_tr_b16 v[88:89], v25 offset:0\n\t" \
    "v_smfmac_f32_16x16x64_bf16 v[100:103], v[36:39], a[0:7], v10\n\t" \
    "ds_read_b64_tr_b16 v[90:91], v25 offset:64\n\t" \
    "s_waitcnt lgkmcnt(4)\n\t" \
    "v_smfmac_f32_16x16x64_bf16 v[124:127], v[84:87], v[240:247], v19\n\t" \
    "ds_read_b64_tr_b16 v[96:97], v25 offset:1152\n\t" \
    "ds_read_b64_tr_b16 v[98:99], v25 offset:1216\n\t" \
    "s_waitcnt lgkmcnt(4)\n\t" \
    "v_smfmac_f32_16x16x64_bf16 v[124:127], v[92:95], v[248:255], v19\n\t" \
    "s_nop 2\n\t" \
    "v_smfmac_f32_16x16x64_bf16 v[108:111], v[84:87], a[144:151], v19\n\t" \
    "s_nop 2\n\t" \
    "v_smfmac_f32_16x16x64_bf16 v[108:111], v[92:95], a[160:167], v19\n\t" \
    "s_nop 1\n\t" \
    "v_permlane32_swap_b32 v124, v126\n\t" \
    "v_smfmac_f32_16x16x64_bf16 v[112:115], v[84:87], a[176:183], v19\n\t" \
    "s_nop 1\n\t" \
    "v_permlane32_swap_b32 v125, v127\n\t" \
    "v_smfmac_f32_16x16x64_bf16 v[112:115], v[92:95], a[192:199], v19\n\t" \
    "s_nop 0\n\t" \
    "v_add_f32 v124, v124, v126\n\t" \
    "s_waitcnt lgkmcnt(2)\n\t" \
    "v_smfmac_f32_16x16x64_bf16 v[108:111], v[88:91], a[152:159], v19\n\t" \
    "s_nop 1\n\t" \
    "v_add_f32 v125, v125, v127\n\t" \
    "v_smfmac_f32_16x16x64_bf16 v[112:115], v[88:91], a[184:191], v19\n\t" \
    "s_nop 0\n\t" \
    "v_cvt_pk_bf16_f32 v136, v124, v125\n\t" \
    "s_waitcnt lgkmcnt(0)\n\t" \
    "v_smfmac_f32_16x16x64_bf16 v[108:111], v[96:99], a[168:175], v19\n\t" \
    "s_nop 1\n\t" \
    "v_cvt_pk_bf16_f32 v137, v124, v125\n\t" \
    "v_smfmac_f32_16x16x64_bf16 v[112:115], v[96:99], a[200:207], v19\n\t" \
    "s_nop 1\n\t" \
    "v_permlane32_swap_b32 v136, v137\n\t" \
    "v_smfmac_f32_16x16x64_bf16 v[116:119], v[84:87], a[208:215], v19\n\t" \
    "s_mov_b32 exec_hi, 0\n\t" \
    "global_store_dwordx2 v16, v[136:137], s[38:39] sc1\n\t" \
    "s_mov_b32 exec_hi, -1\n\t" \
    "v_cvt_pk_bf16_f32 v128, v108, v109\n\t" \
    "v_smfmac_f32_16x16x64_bf16 v[116:119], v[88:91], a[216:223], v19\n\t" \
    "s_nop 1\n\t" \
    "v_cvt_pk_bf16_f32 v129, v110, v111\n\t" \
    "v_smfmac_f32_16x16x64_bf16 v[116:119], v[92:95], a[224:231], v19\n\t" \
    "s_nop 1\n\t" \
    "v_cvt_pk_bf16_f32 v130, v112, v113\n\t" \
    "v_smfmac_f32_16x16x64_bf16 v[116:119], v[96:99], a[232:239], v19\n\t" \
    "s_nop 1\n\t" \
    "v_cvt_pk_bf16_f32 v131, v114, v115\n\t" \
    "v_smfmac_f32_16x16x64_bf16 v[120:123], v[84:87], a[240:247], v19\n\t" \
    "s_nop 1\n\t" \
    "v_permlane16_swap_b32 v128, v130\n\t" \
    "v_smfmac_f32_16x16x64_bf16 v[120:123], v[88:91], a[248:255], v19\n\t" \
    "s_nop 1\n\t" \
    "v_permlane16_swap_b32 v129, v131\n\t" \
    "v_smfmac_f32_16x16x64_bf16 v[120:123], v[92:95], v[224:231], v19\n\t" \
    "s_nop 0\n\t" \
    "global_store_dwordx4 v14, v[128:131], s[38:39] sc1\n\t" \
    "v_cvt_pk_bf16_f32 v132, v116, v117\n\t" \
    "v_smfmac_f32_16x16x64_bf16 v[120:123], v[96:99], v[232:239], v19\n\t" \
    "s_nop 1\n\t" \
    "v_cvt_pk_bf16_f32 v133, v118, v119\n\t" \
    "s_nop 4\n\t" \
    "v_cvt_pk_bf16_f32 v134, v120, v121\n\t" \
    "v_cvt_pk_bf16_f32 v135, v122, v123\n\t" \
    "s_nop 0\n\t" \
    "v_permlane16_swap_b32 v132, v134\n\t" \
    "v_permlane16_swap_b32 v133, v135\n\t" \
    "global_store_dwordx4 v15, v[132:135], s[38:39] sc1\n\t" \
    "s_cmp_eq_u32 s65, 63\n\t" \
    "s_cbranch_scc1 vv_end13_%=\n\t" \
    "s_cmp_lg_u32 s66, 0\n\t" \
    "s_cbranch_scc1 vv_SF22_%=\n\t" \
    "s_lshl_b32 s69, s67, 7\n\t" \
    "s_add_u32 s69, s69, 5804032\n\t" \
    "s_add_u32 s70, s65, -1\n\t" \
    "v_mov_b32 v142, s69\n\t" \
    "v_mov_b32 v143, s70\n\t" \
    "s_mov_b64 exec, 1\n\t" \
    "global_store_dword v142, v143, s[36:37] sc1\n\t" \
    "s_mov_b64 exec, -1\n\t" \
    "vv_SF22_%=:\n\t" \
    "s_add_u32 s65, s65, 1\n\t" \
    "v_mov_b32 v72, 0\n\t" \
    "v_mov_b32 v73, 0\n\t" \
    "v_mov_b32 v74, 0\n\t" \
    "v_mov_b32 v75, 0\n\t" \
    "v_mov_b32 v76, 0\n\t" \
    "v_mov_b32 v77, 0\n\t" \
    "v_mov_b32 v78, 0\n\t" \
    "v_mov_b32 v79, 0\n\t" \
    "v_mov_b32 v108, 0\n\t" \
    "v_mov_b32 v109, 0\n\t" \
    "v_mov_b32 v110, 0\n\t" \
    "v_mov_b32 v111, 0\n\t" \
    "v_mov_b32 v112, 0\n\t" \
    "v_mov_b32 v113, 0\n\t" \
    "v_mov_b32 v114, 0\n\t" \
    "v_mov_b32 v115, 0\n\t" \
    "v_mov_b32 v116, 0\n\t" \
    "v_mov_b32 v117, 0\n\t" \
    "v_mov_b32 v118, 0\n\t" \
    "v_mov_b32 v119, 0\n\t" \
    "v_mov_b32 v120, 0\n\t" \
    "v_mov_b32 v121, 0\n\t" \
    "v_mov_b32 v122, 0\n\t" \
    "v_mov_b32 v123, 0\n\t" \
    "v_mov_b32 v124, 0\n\t" \
    "v_mov_b32 v125, 0\n\t" \
    "v_mov_b32 v126, 0\n\t" \
    "v_mov_b32 v127, 0\n\t" \
    "s_add_u32 s69, s65, 0\n\t" \
    "s_and_b32 s69, s69, 7\n\t" \
    "s_lshl_b32 s69, s69, 18\n\t" \
    "s_add_u32 s69, s69, s62\n\t" \
    "s_add_u32 s46, s36, s69\n\t" \
    "s_addc_u32 s47, s37, 0\n\t" \
    "s_add_u32 s70, s65, 0\n\t" \
    "s_mul_i32 s69, s70, 171\n\t" \
    "s_lshr_b32 s69, s69, 9\n\t" \
    "s_mul_i32 s70, s70, 589824\n\t" \
    "s_mul_i32 s71, s69, 327680\n\t" \
    "s_add_u32 s70, s70, s71\n\t" \
    "s_lshl1_add_u32 s69, s69, 4\n\t" \
    "s_bitcmp1_b64 s[44:45], s69\n\t" \
    "s_cselect_b32 s69, 8192, 0\n\t" \
    "s_add_u32 s69, s69, s70\n\t" \
    "s_add_u32 s69, s69, 123731968\n\t" \
    "s_add_u32 s38, s36, s69\n\t" \
    "s_addc_u32 s39, s37, 0\n\t" \
    "s_and_b32 s69, s69, 0xffe00000\n\t" \
    "s_add_u32 s69, s69, 1823744\n\t" \
    "v_mov_b32 v17, s69\n\t" \
    "global_load_dword v17, v17, s[36:37]\n\t" \
    "s_barrier\n\t" \
    "ds_read_b32 v160, v11 offset:20480\n\t" \
    "s_waitcnt lgkmcnt(0)\n\t" \
    "v_readfirstlane_b32 s70, v160\n\t" \
    "s_branch vv_loop9_%=\n\t" \
    "vv_Gbad18_%=:\n\t" \
    "s_waitcnt lgkmcnt(0)\n\t" \
    "s_nop 7\n\t" \
    "v_mov_b32 v72, 0\n\t" \
    "v_mov_b32 v73, 0\n\t" \
    "v_mov_b32 v74, 0\n\t" \
    "v_mov_b32 v75, 0\n\t" \
    "v_mov_b32 v76, 0\n\t" \
    "v_mov_b32 v77, 0\n\t" \
    "v_mov_b32 v78, 0\n\t" \
    "v_mov_b32 v79, 0\n\t" \
    "s_cmp_eq_u32 s68, 0\n\t" \
    "s_cbranch_scc1 vv_F23_%=\n\t" \
    "s_sub_u32 s68, s68, 1\n\t" \
    "s_branch vv_G17_%=\n\t" \
    "vv_F23_%=:\n\t" \
    "v_mov_b32 v140, 5767424\n\t" \
    "v_mov_b32 v141, 1\n\t" \
    "global_store_dword v140, v141, s[36:37] sc1\n\t" \
    "s_waitcnt vmcnt(0)\n\t" \
    "s_branch vv_Gc19_%=\n\t" \
    "vv_Gslow20_%=:\n\t" \
    "v_max_u32 v140, v36, v37\n\t" \
    "v_max3_u32 v140, v140, v38, v39\n\t" \
    "v_max3_u32 v140, v140, v40, v41\n\t" \
    "v_max3_u32 v140, v140, v42, v43\n\t" \
    "v_max3_u32 v140, v140, v44, v45\n\t" \
    "v_max3_u32 v140, v140, v46, v47\n\t" \
    "v_max3_u32 v140, v140, v48, v49\n\t" \
    "v_max3_u32 v140, v140, v50, v51\n\t" \
    "v_max3_u32 v140, v140, v52, v53\n\t" \
    "v_max3_u32 v140, v140, v54, v55\n\t" \
    "v_max3_u32 v140, v140, v56, v57\n\t" \
    "v_max3_u32 v140, v140, v58, v59\n\t" \
    "v_max3_u32 v140, v140, v60, v61\n\t" \
    "v_max3_u32 v140, v140, v62, v63\n\t" \
    "v_max3_u32 v140, v140, v64, v65\n\t" \
    "v_max3_u32 v140, v140, v66, v67\n\t" \
    "v_max3_u32 v140, v140, v68, v69\n\t" \
    "v_max3_u32 v140, v140, v70, v71\n\t" \
    "v_cmp_eq_u32 vcc, -1, v140\n\t" \
    "s_cbranch_vccz vv_Gback21_%=\n\t" \
    "s_waitcnt lgkmcnt(0)\n\t" \
    "v_sub_u32 v140, v1, v3\n\t" \
    "s_nop 0\n\t" \
    "v_readfirstlane_b32 s69, v140\n\t" \
    "v_add_u32 v143, 20480, v0\n\t" \
    "s_nop 7\n\t" \
    "s_add_u32 s30, s46, s69\n\t" \
    "s_addc_u32 s31, s47, 0\n\t" \
    "vv_Gag24_%=:\n\t" \
    "global_load_dwordx4 v[36:39], v1, s[46:47] sc1\n\t" \
    "global_load_dwordx4 v[40:43], v23, s[46:47] sc1\n\t" \
    "global_load_dwordx4 v[44:47], v143, s[30:31] sc1\n\t" \
    "global_load_dwordx4 v[48:51], v4, s[30:31] sc1\n\t" \
    "global_load_dwordx4 v[52:55], v5, s[30:31] sc1\n\t" \
    "global_load_dwordx4 v[56:59], v6, s[30:31] sc1\n\t" \
    "global_load_dwordx4 v[60:63], v7, s[30:31] sc1\n\t" \
    "global_load_dwordx4 v[64:67], v8, s[30:31] sc1\n\t" \
    "global_load_dwordx4 v[68:71], v9, s[30:31] sc1\n\t" \
    "s_waitcnt vmcnt(0)\n\t" \
    "v_max_u32 v140, v36, v37\n\t" \
    "v_max3_u32 v140, v140, v38, v39\n\t" \
    "v_max3_u32 v140, v140, v40, v41\n\t" \
    "v_max3_u32 v140, v140, v42, v43\n\t" \
    "v_max3_u32 v140, v140, v44, v45\n\t" \
    "v_max3_u32 v140, v140, v46, v47\n\t" \
    "v_max3_u32 v140, v140, v48, v49\n\t" \
    "v_max3_u32 v140, v140, v50, v51\n\t" \
    "v_max3_u32 v140, v140, v52, v53\n\t" \
    "v_max3_u32 v140, v140, v54, v55\n\t" \
    "v_max3_u32 v140, v140, v56, v57\n\t" \
    "v_max3_u32 v140, v140, v58, v59\n\t" \
    "v_max3_u32 v140, v140, v60, v61\n\t" \
    "v_max3_u32 v140, v140, v62, v63\n\t" \
    "v_max3_u32 v140, v140, v64, v65\n\t" \
    "v_max3_u32 v140, v140, v66, v67\n\t" \
    "v_max3_u32 v140, v140, v68, v69\n\t" \
    "v_max3_u32 v140, v140, v70, v71\n\t" \
    "v_cmp_eq_u32 vcc, -1, v140\n\t" \
    "s_cbranch_vccz vv_Gredo25_%=\n\t" \
    "s_cmp_eq_u32 s68, 0\n\t" \
    "s_cbranch_scc1 vv_F26_%=\n\t" \
    "s_sub_u32 s68, s68, 1\n\t" \
    "s_sleep 1\n\t" \
    "s_branch vv_Gag24_%=\n\t" \
    "vv_F26_%=:\n\t" \
    "v_mov_b32 v140, 5767424\n\t" \
    "v_mov_b32 v141, 2\n\t" \
    "global_store_dword v140, v141, s[36:37] sc1\n\t" \
    "s_waitcnt vmcnt(0)\n\t" \
    "s_branch vv_Gredo25_%=\n\t" \
    "vv_Gredo25_%=:\n\t" \
    "v_mov_b32 v72, 0\n\t" \
    "v_mov_b32 v73, 0\n\t" \
    "v_mov_b32 v74, 0\n\t" \
    "v_mov_b32 v75, 0\n\t" \
    "v_mov_b32 v76, 0\n\t" \
    "v_mov_b32 v77, 0\n\t" \
    "v_mov_b32 v78, 0\n\t" \
    "v_mov_b32 v79, 0\n\t" \
    "v_smfmac_f32_16x16x64_bf16 v[72:75], v[36:39], a[0:7], v10\n\t" \
    "v_smfmac_f32_16x16x64_bf16 v[72:75], v[40:43], a[8:15], v10\n\t" \
    "v_smfmac_f32_16x16x64_bf16 v[72:75], v[48:51], a[16:23], v10\n\t" \
    "v_smfmac_f32_16x16x64_bf16 v[72:75], v[52:55], a[24:31], v10\n\t" \
    "v_smfmac_f32_16x16x64_bf16 v[72:75], v[56:59], a[32:39], v10\n\t" \
    "v_smfmac_f32_16x16x64_bf16 v[72:75], v[60:63], a[40:47], v10\n\t" \
    "v_smfmac_f32_16x16x64_bf16 v[72:75], v[64:67], a[48:55], v10\n\t" \
    "v_smfmac_f32_16x16x64_bf16 v[72:75], v[68:71], a[56:63], v10\n\t" \
    "v_smfmac_f32_16x16x64_bf16 v[72:75], v[44:47], a[64:71], v10\n\t" \
    "v_smfmac_f32_16x16x64_bf16 v[76:79], v[36:39], a[72:79], v10\n\t" \
    "v_smfmac_f32_16x16x64_bf16 v[76:79], v[40:43], a[80:87], v10\n\t" \
    "v_smfmac_f32_16x16x64_bf16 v[76:79], v[48:51], a[88:95], v10\n\t" \
    "v_smfmac_f32_16x16x64_bf16 v[76:79], v[52:55], a[96:103], v10\n\t" \
    "v_smfmac_f32_16x16x64_bf16 v[76:79], v[56:59], a[104:111], v10\n\t" \
    "v_smfmac_f32_16x16x64_bf16 v[76:79], v[60:63], a[112:119], v10\n\t" \
    "v_smfmac_f32_16x16x64_bf16 v[76:79], v[64:67], a[120:127], v10\n\t" \
    "v_smfmac_f32_16x16x64_bf16 v[76:79], v[68:71], a[128:135], v10\n\t" \
    "v_smfmac_f32_16x16x64_bf16 v[76:79], v[44:47], a[136:143], v10\n\t" \
    "v_add_f32 v72, v72, v74\n\t" \
    "v_add_f32 v73, v73, v75\n\t" \
    "v_cvt_pk_bf16_f32 v80, v72, v73\n\t" \
    "ds_write_b32 v12, v80 offset:0\n\t" \
    "s_nop 3\n\t" \
    "v_pk_add_f32 v[76:77], v[76:77], v[78:79]\n\t" \
    "v_cvt_pk_bf16_f32 v81, v76, v77\n\t" \
    "ds_write_b32 v12, v81 offset:256\n\t" \
    "s_branch vv_Gback21_%=\n\t" \
    "vv_end13_%=:\n\t" \
    "s_endpgm\n\t" \
    ".p2align 6\n\t" \
    "s_nop 0\n\t" \
    "s_nop 0\n\t" \
    "s_nop 0\n\t" \
    "s_nop 0\n\t" \
    "s_nop 0\n\t" \
    "s_nop 0\n\t" \
    "vv_loop10_%=:\n\t" \
    "s_getpc_b64 s[30:31]\n\t" \
    "vv_Wp29_%=:\n\t" \
    "s_add_u32 s30, s30, vv_We30_%=-vv_Wp29_%=\n\t" \
    "s_addc_u32 s31, s31, 0\n\t" \
    "s_sub_u32 s70, s70, 96\n\t" \
    "vv_W28_%=:\n\t" \
    "s_memtime s[56:57]\n\t" \
    "s_waitcnt lgkmcnt(0)\n\t" \
    "s_sub_u32 s69, s70, s56\n\t" \
    "s_cmp_gt_i32 s69, 80\n\t" \
    "s_cbranch_scc1 vv_W28_%=\n\t" \
    "s_max_i32 s69, s69, 0\n\t" \
    "s_and_b32 s69, s69, -4\n\t" \
    "s_sub_u32 s30, s30, s69\n\t" \
    "s_subb_u32 s31, s31, 0\n\t" \
    "s_setpc_b64 s[30:31]\n\t" \
    "s_nop 0\n\t" \
    "s_nop 0\n\t" \
    "s_nop 0\n\t" \
    "s_nop 0\n\t" \
    "s_nop 0\n\t" \
    "s_nop 0\n\t" \
    "s_nop 0\n\t" \
    "s_nop 0\n\t" \
    "s_nop 0\n\t" \
    "s_nop 0\n\t" \
    "s_nop 0\n\t" \
    "s_nop 0\n\t" \
    "s_nop 0\n\t" \
    "s_nop 0\n\t" \
    "s_nop 0\n\t" \
    "s_nop 0\n\t" \
    "s_nop 0\n\t" \
    "s_nop 0\n\t" \
    "s_nop 0\n\t" \
    "s_nop 0\n\t" \
    "vv_We30_%=:\n\t" \
    "vv_G31_%=:\n\t" \
    "global_load_dwordx4 v[36:39], v1, s[46:47] sc1\n\t" \
    "global_load_dword v141, v2, s[46:47] sc1\n\t" \
    "global_load_dwordx4 v[40:43], v23, s[46:47] sc1\n\t" \
    "s_waitcnt vmcnt(2)\n\t" \
    "ds_write_b128 v3, v[36:39]\n\t" \
    "v_smfmac_f32_16x16x64_bf16 v[72:75], v[36:39], a[16:23], v10\n\t" \
    "s_waitcnt vmcnt(1)\n\t" \
    "ds_write_b32 v22, v141 offset:19712\n\t" \
    "s_waitcnt vmcnt(0)\n\t" \
    "ds_write_b128 v3, v[40:43] offset:1024\n\t" \
    "v_max3_u32 v140, v36, v40, v141\n\t" \
    "v_cmp_eq_u32 vcc, -1, v140\n\t" \
    "s_cbranch_vccnz vv_Gbad32_%=\n\t" \
    "vv_Gc33_%=:\n\t" \
    "s_waitcnt lgkmcnt(0)\n\t" \
    "s_barrier\n\t" \
    "ds_read_b128 v[48:51], v4\n\t" \
    "ds_read_b128 v[52:55], v5\n\t" \
    "v_smfmac_f32_16x16x64_bf16 v[76:79], v[36:39], a[88:95], v10\n\t" \
    "ds_read_b128 v[56:59], v6\n\t" \
    "ds_read_b128 v[60:63], v7\n\t" \
    "v_smfmac_f32_16x16x64_bf16 v[72:75], v[40:43], a[24:31], v10\n\t" \
    "ds_read_b128 v[64:67], v8\n\t" \
    "v_smfmac_f32_16x16x64_bf16 v[76:79], v[40:43], a[96:103], v10\n\t" \
    "ds_read_b128 v[68:71], v9\n\t" \
    "s_waitcnt lgkmcnt(5)\n\t" \
    "v_smfmac_f32_16x16x64_bf16 v[72:75], v[48:51], a[32:39], v10\n\t" \
    "ds_read_b128 v[44:47], v0 offset:19456\n\t" \
    "s_waitcnt lgkmcnt(5)\n\t" \
    "v_smfmac_f32_16x16x64_bf16 v[72:75], v[52:55], a[40:47], v10\n\t" \
    "s_waitcnt lgkmcnt(4)\n\t" \
    "v_smfmac_f32_16x16x64_bf16 v[72:75], v[56:59], a[48:55], v10\n\t" \
    "s_waitcnt lgkmcnt(3)\n\t" \
    "v_smfmac_f32_16x16x64_bf16 v[72:75], v[60:63], a[56:63], v10\n\t" \
    "s_waitcnt lgkmcnt(2)\n\t" \
    "v_smfmac_f32_16x16x64_bf16 v[72:75], v[64:67], a[0:7], v10\n\t" \
    "s_waitcnt lgkmcnt(1)\n\t" \
    "v_smfmac_f32_16x16x64_bf16 v[72:75], v[68:71], a[8:15], v10\n\t" \
    "s_waitcnt lgkmcnt(0)\n\t" \
    "v_smfmac_f32_16x16x64_bf16 v[72:75], v[44:47], a[64:71], v10\n\t" \
    "s_nop 2\n\t" \
    "v_smfmac_f32_16x16x64_bf16 v[76:79], v[48:51], a[104:111], v10\n\t" \
    "s_nop 2\n\t" \
    "v_smfmac_f32_16x16x64_bf16 v[76:79], v[52:55], a[112:119], v10\n\t" \
    "v_add_f32 v72, v72, v74\n\t" \
    "v_smfmac_f32_16x16x64_bf16 v[76:79], v[56:59], a[120:127], v10\n\t" \
    "v_add_f32 v73, v73, v75\n\t" \
    "v_smfmac_f32_16x16x64_bf16 v[76:79], v[60:63], a[128:135], v10\n\t" \
    "v_cvt_pk_bf16_f32 v80, v72, v73\n\t" \
    "v_smfmac_f32_16x16x64_bf16 v[76:79], v[64:67], a[72:79], v10\n\t" \
    "ds_write_b32 v12, v80 offset:0\n\t" \
    "v_smfmac_f32_16x16x64_bf16 v[76:79], v[68:71], a[80:87], v10\n\t" \
    "v_smfmac_f32_16x16x64_bf16 v[76:79], v[44:47], a[136:143], v10\n\t" \
    "s_nop 6\n\t" \
    "v_cmp_u_f32 vcc, v72, v73\n\t" \
    "v_pk_add_f32 v[76:77], v[76:77], v[78:79]\n\t" \
    "v_cvt_pk_bf16_f32 v81, v76, v77\n\t" \
    "ds_write_b32 v12, v81 offset:256\n\t" \
    "s_cbranch_vccnz vv_Gslow34_%=\n\t" \
    "vv_Gback35_%=:\n\t" \
    "s_waitcnt lgkmcnt(0)\n\t" \
    "s_barrier\n\t" \
    "ds_read_b64_tr_b16 v[84:85], v13 offset:0\n\t" \
    "v_smfmac_f32_16x16x64_bf16 v[100:103], v[36:39], a[0:7], v10\n\t" \
    "ds_read_b64_tr_b16 v[86:87], v13 offset:64\n\t" \
    "ds_read_b64_tr_b16 v[92:93], v13 offset:1152\n\t" \
    "v_smfmac_f32_16x16x64_bf16 v[100:103], v[36:39], a[0:7], v10\n\t" \
    "ds_read_b64_tr_b16 v[94:95], v13 offset:1216\n\t" \
    "ds_read_b64_tr_b16 v[88:89], v25 offset:0\n\t" \
    "v_smfmac_f32_16x16x64_bf16 v[100:103], v[36:39], a[0:7], v10\n\t" \
    "ds_read_b64_tr_b16 v[90:91], v25 offset:64\n\t" \
    "s_waitcnt lgkmcnt(4)\n\t" \
    "v_smfmac_f32_16x16x64_bf16 v[124:127], v[84:87], v[240:247], v19\n\t" \
    "ds_read_b64_tr_b16 v[96:97], v25 offset:1152\n\t" \
    "ds_read_b64_tr_b16 v[98:99], v25 offset:1216\n\t" \
    "s_waitcnt lgkmcnt(4)\n\t" \
    "v_smfmac_f32_16x16x64_bf16 v[124:127], v[92:95], v[248:255], v19\n\t" \
    "s_nop 2\n\t" \
    "v_smfmac_f32_16x16x64_bf16 v[108:111], v[84:87], a[144:151], v19\n\t" \
    "s_nop 2\n\t" \
    "v_smfmac_f32_16x16x64_bf16 v[108:111], v[92:95], a[160:167], v19\n\t" \
    "s_nop 1\n\t" \
    "v_permlane32_swap_b32 v124, v126\n\t" \
    "v_smfmac_f32_16x16x64_bf16 v[112:115], v[84:87], a[176:183], v19\n\t" \
    "s_nop 1\n\t" \
    "v_permlane32_swap_b32 v125, v127\n\t" \
    "v_smfmac_f32_16x16x64_bf16 v[112:115], v[92:95], a[192:199], v19\n\t" \
    "s_nop 0\n\t" \
    "v_add_f32 v124, v124, v126\n\t" \
    "s_waitcnt lgkmcnt(2)\n\t" \
    "v_smfmac_f32_16x16x64_bf16 v[108:111], v[88:91], a[152:159], v19\n\t" \
    "s_nop 1\n\t" \
    "v_add_f32 v125, v125, v127\n\t" \
    "v_smfmac_f32_16x16x64_bf16 v[112:115], v[88:91], a[184:191], v19\n\t" \
    "s_nop 0\n\t" \
    "v_cvt_pk_bf16_f32 v136, v124, v125\n\t" \
    "s_waitcnt lgkmcnt(0)\n\t" \
    "v_smfmac_f32_16x16x64_bf16 v[108:111], v[96:99], a[168:175], v19\n\t" \
    "s_nop 1\n\t" \
    "v_cvt_pk_bf16_f32 v137, v124, v125\n\t" \
    "v_smfmac_f32_16x16x64_bf16 v[112:115], v[96:99], a[200:207], v19\n\t" \
    "s_nop 1\n\t" \
    "v_permlane32_swap_b32 v136, v137\n\t" \
    "v_smfmac_f32_16x16x64_bf16 v[116:119], v[84:87], a[208:215], v19\n\t" \
    "s_mov_b32 exec_hi, 0\n\t" \
    "global_store_dwordx2 v16, v[136:137], s[38:39] sc1\n\t" \
    "s_mov_b32 exec_hi, -1\n\t" \
    "v_cvt_pk_bf16_f32 v128, v108, v109\n\t" \
    "v_smfmac_f32_16x16x64_bf16 v[116:119], v[88:91], a[216:223], v19\n\t" \
    "s_nop 1\n\t" \
    "v_cvt_pk_bf16_f32 v129, v110, v111\n\t" \
    "v_smfmac_f32_16x16x64_bf16 v[116:119], v[92:95], a[224:231], v19\n\t" \
    "s_nop 1\n\t" \
    "v_cvt_pk_bf16_f32 v130, v112, v113\n\t" \
    "v_smfmac_f32_16x16x64_bf16 v[116:119], v[96:99], a[232:239], v19\n\t" \
    "s_nop 1\n\t" \
    "v_cvt_pk_bf16_f32 v131, v114, v115\n\t" \
    "v_smfmac_f32_16x16x64_bf16 v[120:123], v[84:87], a[240:247], v19\n\t" \
    "s_nop 1\n\t" \
    "v_permlane16_swap_b32 v128, v130\n\t" \
    "v_smfmac_f32_16x16x64_bf16 v[120:123], v[88:91], a[248:255], v19\n\t" \
    "s_nop 1\n\t" \
    "v_permlane16_swap_b32 v129, v131\n\t" \
    "v_smfmac_f32_16x16x64_bf16 v[120:123], v[92:95], v[224:231], v19\n\t" \
    "s_nop 0\n\t" \
    "global_store_dwordx4 v14, v[128:131], s[38:39] sc1\n\t" \
    "v_cvt_pk_bf16_f32 v132, v116, v117\n\t" \
    "v_smfmac_f32_16x16x64_bf16 v[120:123], v[96:99], v[232:239], v19\n\t" \
    "s_nop 1\n\t" \
    "v_cvt_pk_bf16_f32 v133, v118, v119\n\t" \
    "s_nop 4\n\t" \
    "v_cvt_pk_bf16_f32 v134, v120, v121\n\t" \
    "v_cvt_pk_bf16_f32 v135, v122, v123\n\t" \
    "s_nop 0\n\t" \
    "v_permlane16_swap_b32 v132, v134\n\t" \
    "v_permlane16_swap_b32 v133, v135\n\t" \
    "global_store_dwordx4 v15, v[132:135], s[38:39] sc1\n\t" \
    "s_cmp_eq_u32 s65, 63\n\t" \
    "s_cbranch_scc1 vv_end27_%=\n\t" \
    "s_add_u32 s65, s65, 1\n\t" \
    "v_mov_b32 v72, 0\n\t" \
    "v_mov_b32 v73, 0\n\t" \
    "v_mov_b32 v74, 0\n\t" \
    "v_mov_b32 v75, 0\n\t" \
    "v_mov_b32 v76, 0\n\t" \
    "v_mov_b32 v77, 0\n\t" \
    "v_mov_b32 v78, 0\n\t" \
    "v_mov_b32 v79, 0\n\t" \
    "v_mov_b32 v108, 0\n\t" \
    "v_mov_b32 v109, 0\n\t" \
    "v_mov_b32 v110, 0\n\t" \
    "v_mov_b32 v111, 0\n\t" \
    "v_mov_b32 v112, 0\n\t" \
    "v_mov_b32 v113, 0\n\t" \
    "v_mov_b32 v114, 0\n\t" \
    "v_mov_b32 v115, 0\n\t" \
    "v_mov_b32 v116, 0\n\t" \
    "v_mov_b32 v117, 0\n\t" \
    "v_mov_b32 v118, 0\n\t" \
    "v_mov_b32 v119, 0\n\t" \
    "v_mov_b32 v120, 0\n\t" \
    "v_mov_b32 v121, 0\n\t" \
    "v_mov_b32 v122, 0\n\t" \
    "v_mov_b32 v123, 0\n\t" \
    "v_mov_b32 v124, 0\n\t" \
    "v_mov_b32 v125, 0\n\t" \
    "v_mov_b32 v126, 0\n\t" \
    "v_mov_b32 v127, 0\n\t" \
    "s_add_u32 s69, s65, 0\n\t" \
    "s_and_b32 s69, s69, 7\n\t" \
    "s_lshl_b32 s69, s69, 18\n\t" \
    "s_add_u32 s69, s69, s62\n\t" \
    "s_add_u32 s46, s36, s69\n\t" \
    "s_addc_u32 s47, s37, 0\n\t" \
    "s_add_u32 s70, s65, 0\n\t" \
    "s_mul_i32 s69, s70, 171\n\t" \
    "s_lshr_b32 s69, s69, 9\n\t" \
    "s_mul_i32 s70, s70, 589824\n\t" \
    "s_mul_i32 s71, s69, 327680\n\t" \
    "s_add_u32 s70, s70, s71\n\t" \
    "s_lshl1_add_u32 s69, s69, 4\n\t" \
    "s_bitcmp1_b64 s[44:45], s69\n\t" \
    "s_cselect_b32 s69, 8192, 0\n\t" \
    "s_add_u32 s69, s69, s70\n\t" \
    "s_add_u32 s69, s69, 123731968\n\t" \
    "s_add_u32 s38, s36, s69\n\t" \
    "s_addc_u32 s39, s37, 0\n\t" \
    "s_and_b32 s69, s69, 0xffe00000\n\t" \
    "s_add_u32 s69, s69, 1823744\n\t" \
    "v_mov_b32 v17, s69\n\t" \
    "global_load_dword v17, v17, s[36:37]\n\t" \
    "s_barrier\n\t" \
    "ds_read_b32 v160, v11 offset:20480\n\t" \
    "s_waitcnt lgkmcnt(0)\n\t" \
    "v_readfirstlane_b32 s70, v160\n\t" \
    "s_branch vv_loop10_%=\n\t" \
    "vv_Gbad32_%=:\n\t" \
    "s_waitcnt lgkmcnt(0)\n\t" \
    "s_nop 7\n\t" \
    "v_mov_b32 v72, 0\n\t" \
    "v_mov_b32 v73, 0\n\t" \
    "v_mov_b32 v74, 0\n\t" \
    "v_mov_b32 v75, 0\n\t" \
    "v_mov_b32 v76, 0\n\t" \
    "v_mov_b32 v77, 0\n\t" \
    "v_mov_b32 v78, 0\n\t" \
    "v_mov_b32 v79, 0\n\t" \
    "s_cmp_eq_u32 s68, 0\n\t" \
    "s_cbranch_scc1 vv_F36_%=\n\t" \
    "s_sub_u32 s68, s68, 1\n\t" \
    "s_branch vv_G31_%=\n\t" \
    "vv_F36_%=:\n\t" \
    "v_mov_b32 v140, 5767424\n\t" \
    "v_mov_b32 v141, 1\n\t" \
    "global_store_dword v140, v141, s[36:37] sc1\n\t" \
    "s_waitcnt vmcnt(0)\n\t" \
    "s_branch vv_Gc33_%=\n\t" \
    "vv_Gslow34_%=:\n\t" \
    "v_max_u32 v140, v36, v37\n\t" \
    "v_max3_u32 v140, v140, v38, v39\n\t" \
    "v_max3_u32 v140, v140, v40, v41\n\t" \
    "v_max3_u32 v140, v140, v42, v43\n\t" \
    "v_max3_u32 v140, v140, v44, v45\n\t" \
    "v_max3_u32 v140, v140, v46, v47\n\t" \
    "v_max3_u32 v140, v140, v48, v49\n\t" \
    "v_max3_u32 v140, v140, v50, v51\n\t" \
    "v_max3_u32 v140, v140, v52, v53\n\t" \
    "v_max3_u32 v140, v140, v54, v55\n\t" \
    "v_max3_u32 v140, v140, v56, v57\n\t" \
    "v_max3_u32 v140, v140, v58, v59\n\t" \
    "v_max3_u32 v140, v140, v60, v61\n\t" \
    "v_max3_u32 v140, v140, v62, v63\n\t" \
    "v_max3_u32 v140, v140, v64, v65\n\t" \
    "v_max3_u32 v140, v140, v66, v67\n\t" \
    "v_max3_u32 v140, v140, v68, v69\n\t" \
    "v_max3_u32 v140, v140, v70, v71\n\t" \
    "v_cmp_eq_u32 vcc, -1, v140\n\t" \
    "s_cbranch_vccz vv_Gback35_%=\n\t" \
    "s_waitcnt lgkmcnt(0)\n\t" \
    "v_sub_u32 v140, v1, v3\n\t" \
    "s_nop 0\n\t" \
    "v_readfirstlane_b32 s69, v140\n\t" \
    "v_add_u32 v143, 20480, v0\n\t" \
    "s_nop 7\n\t" \
    "s_add_u32 s30, s46, s69\n\t" \
    "s_addc_u32 s31, s47, 0\n\t" \
    "vv_Gag37_%=:\n\t" \
    "global_load_dwordx4 v[36:39], v1, s[46:47] sc1\n\t" \
    "global_load_dwordx4 v[40:43], v23, s[46:47] sc1\n\t" \
    "global_load_dwordx4 v[44:47], v143, s[30:31] sc1\n\t" \
    "global_load_dwordx4 v[48:51], v4, s[30:31] sc1\n\t" \
    "global_load_dwordx4 v[52:55], v5, s[30:31] sc1\n\t" \
    "global_load_dwordx4 v[56:59], v6, s[30:31] sc1\n\t" \
    "global_load_dwordx4 v[60:63], v7, s[30:31] sc1\n\t" \
    "global_load_dwordx4 v[64:67], v8, s[30:31] sc1\n\t" \
    "global_load_dwordx4 v[68:71], v9, s[30:31] sc1\n\t" \
    "s_waitcnt vmcnt(0)\n\t" \
    "v_max_u32 v140, v36, v37\n\t" \
    "v_max3_u32 v140, v140, v38, v39\n\t" \
    "v_max3_u32 v140, v140, v40, v41\n\t" \
    "v_max3_u32 v140, v140, v42, v43\n\t" \
    "v_max3_u32 v140, v140, v44, v45\n\t" \
    "v_max3_u32 v140, v140, v46, v47\n\t" \
    "v_max3_u32 v140, v140, v48, v49\n\t" \
    "v_max3_u32 v140, v140, v50, v51\n\t" \
    "v_max3_u32 v140, v140, v52, v53\n\t" \
    "v_max3_u32 v140, v140, v54, v55\n\t" \
    "v_max3_u32 v140, v140, v56, v57\n\t" \
    "v_max3_u32 v140, v140, v58, v59\n\t" \
    "v_max3_u32 v140, v140, v60, v61\n\t" \
    "v_max3_u32 v140, v140, v62, v63\n\t" \
    "v_max3_u32 v140, v140, v64, v65\n\t" \
    "v_max3_u32 v140, v140, v66, v67\n\t" \
    "v_max3_u32 v140, v140, v68, v69\n\t" \
    "v_max3_u32 v140, v140, v70, v71\n\t" \
    "v_cmp_eq_u32 vcc, -1, v140\n\t" \
    "s_cbranch_vccz vv_Gredo38_%=\n\t" \
    "s_cmp_eq_u32 s68, 0\n\t" \
    "s_cbranch_scc1 vv_F39_%=\n\t" \
    "s_sub_u32 s68, s68, 1\n\t" \
    "s_sleep 1\n\t" \
    "s_branch vv_Gag37_%=\n\t" \
    "vv_F39_%=:\n\t" \
    "v_mov_b32 v140, 5767424\n\t" \
    "v_mov_b32 v141, 2\n\t" \
    "global_store_dword v140, v141, s[36:37] sc1\n\t" \
    "s_waitcnt vmcnt(0)\n\t" \
    "s_branch vv_Gredo38_%=\n\t" \
    "vv_Gredo38_%=:\n\t" \
    "v_mov_b32 v72, 0\n\t" \
    "v_mov_b32 v73, 0\n\t" \
    "v_mov_b32 v74, 0\n\t" \
    "v_mov_b32 v75, 0\n\t" \
    "v_mov_b32 v76, 0\n\t" \
    "v_mov_b32 v77, 0\n\t" \
    "v_mov_b32 v78, 0\n\t" \
    "v_mov_b32 v79, 0\n\t" \
    "v_smfmac_f32_16x16x64_bf16 v[72:75], v[36:39], a[16:23], v10\n\t" \
    "v_smfmac_f32_16x16x64_bf16 v[72:75], v[40:43], a[24:31], v10\n\t" \
    "v_smfmac_f32_16x16x64_bf16 v[72:75], v[48:51], a[32:39], v10\n\t" \
    "v_smfmac_f32_16x16x64_bf16 v[72:75], v[52:55], a[40:47], v10\n\t" \
    "v_smfmac_f32_16x16x64_bf16 v[72:75], v[56:59], a[48:55], v10\n\t" \
    "v_smfmac_f32_16x16x64_bf16 v[72:75], v[60:63], a[56:63], v10\n\t" \
    "v_smfmac_f32_16x16x64_bf16 v[72:75], v[64:67], a[0:7], v10\n\t" \
    "v_smfmac_f32_16x16x64_bf16 v[72:75], v[68:71], a[8:15], v10\n\t" \
    "v_smfmac_f32_16x16x64_bf16 v[72:75], v[44:47], a[64:71], v10\n\t" \
    "v_smfmac_f32_16x16x64_bf16 v[76:79], v[36:39], a[88:95], v10\n\t" \
    "v_smfmac_f32_16x16x64_bf16 v[76:79], v[40:43], a[96:103], v10\n\t" \
    "v_smfmac_f32_16x16x64_bf16 v[76:79], v[48:51], a[104:111], v10\n\t" \
    "v_smfmac_f32_16x16x64_bf16 v[76:79], v[52:55], a[112:119], v10\n\t" \
    "v_smfmac_f32_16x16x64_bf16 v[76:79], v[56:59], a[120:127], v10\n\t" \
    "v_smfmac_f32_16x16x64_bf16 v[76:79], v[60:63], a[128:135], v10\n\t" \
    "v_smfmac_f32_16x16x64_bf16 v[76:79], v[64:67], a[72:79], v10\n\t" \
    "v_smfmac_f32_16x16x64_bf16 v[76:79], v[68:71], a[80:87], v10\n\t" \
    "v_smfmac_f32_16x16x64_bf16 v[76:79], v[44:47], a[136:143], v10\n\t" \
    "v_add_f32 v72, v72, v74\n\t" \
    "v_add_f32 v73, v73, v75\n\t" \
    "v_cvt_pk_bf16_f32 v80, v72, v73\n\t" \
    "ds_write_b32 v12, v80 offset:0\n\t" \
    "s_nop 3\n\t" \
    "v_pk_add_f32 v[76:77], v[76:77], v[78:79]\n\t" \
    "v_cvt_pk_bf16_f32 v81, v76, v77\n\t" \
    "ds_write_b32 v12, v81 offset:256\n\t" \
    "s_branch vv_Gback35_%=\n\t" \
    "vv_end27_%=:\n\t" \
    "s_endpgm\n\t" \
    ".p2align 6\n\t" \
    "s_nop 0\n\t" \
    "s_nop 0\n\t" \
    "s_nop 0\n\t" \
    "s_nop 0\n\t" \
    "s_nop 0\n\t" \
    "s_nop 0\n\t" \
    "vv_loop11_%=:\n\t" \
    "s_getpc_b64 s[30:31]\n\t" \
    "vv_Wp42_%=:\n\t" \
    "s_add_u32 s30, s30, vv_We43_%=-vv_Wp42_%=\n\t" \
    "s_addc_u32 s31, s31, 0\n\t" \
    "s_sub_u32 s70, s70, 96\n\t" \
    "vv_W41_%=:\n\t" \
    "s_memtime s[56:57]\n\t" \
    "s_waitcnt lgkmcnt(0)\n\t" \
    "s_sub_u32 s69, s70, s56\n\t" \
    "s_cmp_gt_i32 s69, 80\n\t" \
    "s_cbranch_scc1 vv_W41_%=\n\t" \
    "s_max_i32 s69, s69, 0\n\t" \
    "s_and_b32 s69, s69, -4\n\t" \
    "s_sub_u32 s30, s30, s69\n\t" \
    "s_subb_u32 s31, s31, 0\n\t" \
    "s_setpc_b64 s[30:31]\n\t" \
    "s_nop 0\n\t" \
    "s_nop 0\n\t" \
    "s_nop 0\n\t" \
    "s_nop 0\n\t" \
    "s_nop 0\n\t" \
    "s_nop 0\n\t" \
    "s_nop 0\n\t" \
    "s_nop 0\n\t" \
    "s_nop 0\n\t" \
    "s_nop 0\n\t" \
    "s_nop 0\n\t" \
    "s_nop 0\n\t" \
    "s_nop 0\n\t" \
    "s_nop 0\n\t" \
    "s_nop 0\n\t" \
    "s_nop 0\n\t" \
    "s_nop 0\n\t" \
    "s_nop 0\n\t" \
    "s_nop 0\n\t" \
    "s_nop 0\n\t" \
    "vv_We43_%=:\n\t" \
    "vv_G44_%=:\n\t" \
    "global_load_dwordx4 v[36:39], v1, s[46:47] sc1\n\t" \
    "global_load_dword v141, v2, s[46:47] sc1\n\t" \
    "global_load_dwordx4 v[40:43], v23, s[46:47] sc1\n\t" \
    "s_waitcnt vmcnt(2)\n\t" \
    "ds_write_b128 v3, v[36:39]\n\t" \
    "v_smfmac_f32_16x16x64_bf16 v[72:75], v[36:39], a[32:39], v10\n\t" \
    "s_waitcnt vmcnt(1)\n\t" \
    "ds_write_b32 v22, v141 offset:19968\n\t" \
    "s_waitcnt vmcnt(0)\n\t" \
    "ds_write_b128 v3, v[40:43] offset:1024\n\t" \
    "v_max3_u32 v140, v36, v40, v141\n\t" \
    "v_cmp_eq_u32 vcc, -1, v140\n\t" \
    "s_cbranch_vccnz vv_Gbad45_%=\n\t" \
    "vv_Gc46_%=:\n\t" \
    "s_waitcnt lgkmcnt(0)\n\t" \
    "s_barrier\n\t" \
    "ds_read_b128 v[48:51], v4\n\t" \
    "ds_read_b128 v[52:55], v5\n\t" \
    "v_smfmac_f32_16x16x64_bf16 v[76:79], v[36:39], a[104:111], v10\n\t" \
    "ds_read_b128 v[56:59], v6\n\t" \
    "ds_read_b128 v[60:63], v7\n\t" \
    "v_smfmac_f32_16x16x64_bf16 v[72:75], v[40:43], a[40:47], v10\n\t" \
    "ds_read_b128 v[64:67], v8\n\t" \
    "v_smfmac_f32_16x16x64_bf16 v[76:79], v[40:43], a[112:119], v10\n\t" \
    "ds_read_b128 v[68:71], v9\n\t" \
    "s_waitcnt lgkmcnt(5)\n\t" \
    "v_smfmac_f32_16x16x64_bf16 v[72:75], v[48:51], a[48:55], v10\n\t" \
    "ds_read_b128 v[44:47], v0 offset:19456\n\t" \
    "s_waitcnt lgkmcnt(5)\n\t" \
    "v_smfmac_f32_16x16x64_bf16 v[72:75], v[52:55], a[56:63], v10\n\t" \
    "s_waitcnt lgkmcnt(4)\n\t" \
    "v_smfmac_f32_16x16x64_bf16 v[72:75], v[56:59], a[0:7], v10\n\t" \
    "s_waitcnt lgkmcnt(3)\n\t" \
    "v_smfmac_f32_16x16x64_bf16 v[72:75], v[60:63], a[8:15], v10\n\t" \
    "s_waitcnt lgkmcnt(2)\n\t" \
    "v_smfmac_f32_16x16x64_bf16 v[72:75], v[64:67], a[16:23], v10\n\t" \
    "s_waitcnt lgkmcnt(1)\n\t" \
    "v_smfmac_f32_16x16x64_bf16 v[72:75], v[68:71], a[24:31], v10\n\t" \
    "s_waitcnt lgkmcnt(0)\n\t" \
    "v_smfmac_f32_16x16x64_bf16 v[72:75], v[44:47], a[64:71], v10\n\t" \
    "s_nop 2\n\t" \
    "v_smfmac_f32_16x16x64_bf16 v[76:79], v[48:51], a[120:127], v10\n\t" \
    "s_nop 2\n\t" \
    "v_smfmac_f32_16x16x64_bf16 v[76:79], v[52:55], a[128:135], v10\n\t" \
    "v_add_f32 v72, v72, v74\n\t" \
    "v_smfmac_f32_16x16x64_bf16 v[76:79], v[56:59], a[72:79], v10\n\t" \
    "v_add_f32 v73, v73, v75\n\t" \
    "v_smfmac_f32_16x16x64_bf16 v[76:79], v[60:63], a[80:87], v10\n\t" \
    "v_cvt_pk_bf16_f32 v80, v72, v73\n\t" \
    "v_smfmac_f32_16x16x64_bf16 v[76:79], v[64:67], a[88:95], v10\n\t" \
    "ds_write_b32 v12, v80 offset:0\n\t" \
    "v_smfmac_f32_16x16x64_bf16 v[76:79], v[68:71], a[96:103], v10\n\t" \
    "v_smfmac_f32_16x16x64_bf16 v[76:79], v[44:47], a[136:143], v10\n\t" \
    "s_nop 6\n\t" \
    "v_cmp_u_f32 vcc, v72, v73\n\t" \
    "v_pk_add_f32 v[76:77], v[76:77], v[78:79]\n\t" \
    "v_cvt_pk_bf16_f32 v81, v76, v77\n\t" \
    "ds_write_b32 v12, v81 offset:256\n\t" \
    "s_cbranch_vccnz vv_Gslow47_%=\n\t" \
    "vv_Gback48_%=:\n\t" \
    "s_waitcnt lgkmcnt(0)\n\t" \
    "s_barrier\n\t" \
    "ds_read_b64_tr_b16 v[84:85], v13 offset:0\n\t" \
    "v_smfmac_f32_16x16x64_bf16 v[100:103], v[36:39], a[0:7], v10\n\t" \
    "ds_read_b64_tr_b16 v[86:87], v13 offset:64\n\t" \
    "ds_read_b64_tr_b16 v[92:93], v13 offset:1152\n\t" \
    "v_smfmac_f32_16x16x64_bf16 v[100:103], v[36:39], a[0:7], v10\n\t" \
    "ds_read_b64_tr_b16 v[94:95], v13 offset:1216\n\t" \
    "ds_read_b64_tr_b16 v[88:89], v25 offset:0\n\t" \
    "v_smfmac_f32_16x16x64_bf16 v[100:103], v[36:39], a[0:7], v10\n\t" \
    "ds_read_b64_tr_b16 v[90:91], v25 offset:64\n\t" \
    "s_waitcnt lgkmcnt(4)\n\t" \
    "v_smfmac_f32_16x16x64_bf16 v[124:127], v[84:87], v[240:247], v19\n\t" \
    "ds_read_b64_tr_b16 v[96:97], v25 offset:1152\n\t" \
    "ds_read_b64_tr_b16 v[98:99], v25 offset:1216\n\t" \
    "s_waitcnt lgkmcnt(4)\n\t" \
    "v_smfmac_f32_16x16x64_bf16 v[124:127], v[92:95], v[248:255], v19\n\t" \
    "s_nop 2\n\t" \
    "v_smfmac_f32_16x16x64_bf16 v[108:111], v[84:87], a[144:151], v19\n\t" \
    "s_nop 2\n\t" \
    "v_smfmac_f32_16x16x64_bf16 v[108:111], v[92:95], a[160:167], v19\n\t" \
    "s_nop 1\n\t" \
    "v_permlane32_swap_b32 v124, v126\n\t" \
    "v_smfmac_f32_16x16x64_bf16 v[112:115], v[84:87], a[176:183], v19\n\t" \
    "s_nop 1\n\t" \
    "v_permlane32_swap_b32 v125, v127\n\t" \
    "v_smfmac_f32_16x16x64_bf16 v[112:115], v[92:95], a[192:199], v19\n\t" \
    "s_nop 0\n\t" \
    "v_add_f32 v124, v124, v126\n\t" \
    "s_waitcnt lgkmcnt(2)\n\t" \
    "v_smfmac_f32_16x16x64_bf16 v[108:111], v[88:91], a[152:159], v19\n\t" \
    "s_nop 1\n\t" \
    "v_add_f32 v125, v125, v127\n\t" \
    "v_smfmac_f32_16x16x64_bf16 v[112:115], v[88:91], a[184:191], v19\n\t" \
    "s_nop 0\n\t" \
    "v_cvt_pk_bf16_f32 v136, v124, v125\n\t" \
    "s_waitcnt lgkmcnt(0)\n\t" \
    "v_smfmac_f32_16x16x64_bf16 v[108:111], v[96:99], a[168:175], v19\n\t" \
    "s_nop 1\n\t" \
    "v_cvt_pk_bf16_f32 v137, v124, v125\n\t" \
    "v_smfmac_f32_16x16x64_bf16 v[112:115], v[96:99], a[200:207], v19\n\t" \
    "s_nop 1\n\t" \
    "v_permlane32_swap_b32 v136, v137\n\t" \
    "v_smfmac_f32_16x16x64_bf16 v[116:119], v[84:87], a[208:215], v19\n\t" \
    "s_mov_b32 exec_hi, 0\n\t" \
    "global_store_dwordx2 v16, v[136:137], s[38:39] sc1\n\t" \
    "s_mov_b32 exec_hi, -1\n\t" \
    "v_cvt_pk_bf16_f32 v128, v108, v109\n\t" \
    "v_smfmac_f32_16x16x64_bf16 v[116:119], v[88:91], a[216:223], v19\n\t" \
    "s_nop 1\n\t" \
    "v_cvt_pk_bf16_f32 v129, v110, v111\n\t" \
    "v_smfmac_f32_16x16x64_bf16 v[116:119], v[92:95], a[224:231], v19\n\t" \
    "s_nop 1\n\t" \
    "v_cvt_pk_bf16_f32 v130, v112, v113\n\t" \
    "v_smfmac_f32_16x16x64_bf16 v[116:119], v[96:99], a[232:239], v19\n\t" \
    "s_nop 1\n\t" \
    "v_cvt_pk_bf16_f32 v131, v114, v115\n\t" \
    "v_smfmac_f32_16x16x64_bf16 v[120:123], v[84:87], a[240:247], v19\n\t" \
    "s_nop 1\n\t" \
    "v_permlane16_swap_b32 v128, v130\n\t" \
    "v_smfmac_f32_16x16x64_bf16 v[120:123], v[88:91], a[248:255], v19\n\t" \
    "s_nop 1\n\t" \
    "v_permlane16_swap_b32 v129, v131\n\t" \
    "v_smfmac_f32_16x16x64_bf16 v[120:123], v[92:95], v[224:231], v19\n\t" \
    "s_nop 0\n\t" \
    "global_store_dwordx4 v14, v[128:131], s[38:39] sc1\n\t" \
    "v_cvt_pk_bf16_f32 v132, v116, v117\n\t" \
    "v_smfmac_f32_16x16x64_bf16 v[120:123], v[96:99], v[232:239], v19\n\t" \
    "s_nop 1\n\t" \
    "v_cvt_pk_bf16_f32 v133, v118, v119\n\t" \
    "s_nop 4\n\t" \
    "v_cvt_pk_bf16_f32 v134, v120, v121\n\t" \
    "v_cvt_pk_bf16_f32 v135, v122, v123\n\t" \
    "s_nop 0\n\t" \
    "v_permlane16_swap_b32 v132, v134\n\t" \
    "v_permlane16_swap_b32 v133, v135\n\t" \
    "global_store_dwordx4 v15, v[132:135], s[38:39] sc1\n\t" \
    "s_cmp_eq_u32 s65, 63\n\t" \
    "s_cbranch_scc1 vv_end40_%=\n\t" \
    "s_add_u32 s65, s65, 1\n\t" \
    "v_mov_b32 v72, 0\n\t" \
    "v_mov_b32 v73, 0\n\t" \
    "v_mov_b32 v74, 0\n\t" \
    "v_mov_b32 v75, 0\n\t" \
    "v_mov_b32 v76, 0\n\t" \
    "v_mov_b32 v77, 0\n\t" \
    "v_mov_b32 v78, 0\n\t" \
    "v_mov_b32 v79, 0\n\t" \
    "v_mov_b32 v108, 0\n\t" \
    "v_mov_b32 v109, 0\n\t" \
    "v_mov_b32 v110, 0\n\t" \
    "v_mov_b32 v111, 0\n\t" \
    "v_mov_b32 v112, 0\n\t" \
    "v_mov_b32 v113, 0\n\t" \
    "v_mov_b32 v114, 0\n\t" \
    "v_mov_b32 v115, 0\n\t" \
    "v_mov_b32 v116, 0\n\t" \
    "v_mov_b32 v117, 0\n\t" \
    "v_mov_b32 v118, 0\n\t" \
    "v_mov_b32 v119, 0\n\t" \
    "v_mov_b32 v120, 0\n\t" \
    "v_mov_b32 v121, 0\n\t" \
    "v_mov_b32 v122, 0\n\t" \
    "v_mov_b32 v123, 0\n\t" \
    "v_mov_b32 v124, 0\n\t" \
    "v_mov_b32 v125, 0\n\t" \
    "v_mov_b32 v126, 0\n\t" \
    "v_mov_b32 v127, 0\n\t" \
    "s_add_u32 s69, s65, 0\n\t" \
    "s_and_b32 s69, s69, 7\n\t" \
    "s_lshl_b32 s69, s69, 18\n\t" \
    "s_add_u32 s69, s69, s62\n\t" \
    "s_add_u32 s46, s36, s69\n\t" \
    "s_addc_u32 s47, s37, 0\n\t" \
    "s_add_u32 s70, s65, 0\n\t" \
    "s_mul_i32 s69, s70, 171\n\t" \
    "s_lshr_b32 s69, s69, 9\n\t" \
    "s_mul_i32 s70, s70, 589824\n\t" \
    "s_mul_i32 s71, s69, 327680\n\t" \
    "s_add_u32 s70, s70, s71\n\t" \
    "s_lshl1_add_u32 s69, s69, 4\n\t" \
    "s_bitcmp1_b64 s[44:45], s69\n\t" \
    "s_cselect_b32 s69, 8192, 0\n\t" \
    "s_add_u32 s69, s69, s70\n\t" \
    "s_add_u32 s69, s69, 123731968\n\t" \
    "s_add_u32 s38, s36, s69\n\t" \
    "s_addc_u32 s39, s37, 0\n\t" \
    "s_and_b32 s69, s69, 0xffe00000\n\t" \
    "s_add_u32 s69, s69, 1823744\n\t" \
    "v_mov_b32 v17, s69\n\t" \
    "global_load_dword v17, v17, s[36:37]\n\t" \
    "s_barrier\n\t" \
    "ds_read_b32 v160, v11 offset:20480\n\t" \
    "s_waitcnt lgkmcnt(0)\n\t" \
    "v_readfirstlane_b32 s70, v160\n\t" \
    "s_branch vv_loop11_%=\n\t" \
    "vv_Gbad45_%=:\n\t" \
    "s_waitcnt lgkmcnt(0)\n\t" \
    "s_nop 7\n\t" \
    "v_mov_b32 v72, 0\n\t" \
    "v_mov_b32 v73, 0\n\t" \
    "v_mov_b32 v74, 0\n\t" \
    "v_mov_b32 v75, 0\n\t" \
    "v_mov_b32 v76, 0\n\t" \
    "v_mov_b32 v77, 0\n\t" \
    "v_mov_b32 v78, 0\n\t" \
    "v_mov_b32 v79, 0\n\t" \
    "s_cmp_eq_u32 s68, 0\n\t" \
    "s_cbranch_scc1 vv_F49_%=\n\t" \
    "s_sub_u32 s68, s68, 1\n\t" \
    "s_branch vv_G44_%=\n\t" \
    "vv_F49_%=:\n\t" \
    "v_mov_b32 v140, 5767424\n\t" \
    "v_mov_b32 v141, 1\n\t" \
    "global_store_dword v140, v141, s[36:37] sc1\n\t" \
    "s_waitcnt vmcnt(0)\n\t" \
    "s_branch vv_Gc46_%=\n\t" \
    "vv_Gslow47_%=:\n\t" \
    "v_max_u32 v140, v36, v37\n\t" \
    "v_max3_u32 v140, v140, v38, v39\n\t" \
    "v_max3_u32 v140, v140, v40, v41\n\t" \
    "v_max3_u32 v140, v140, v42, v43\n\t" \
    "v_max3_u32 v140, v140, v44, v45\n\t" \
    "v_max3_u32 v140, v140, v46, v47\n\t" \
    "v_max3_u32 v140, v140, v48, v49\n\t" \
    "v_max3_u32 v140, v140, v50, v51\n\t" \
    "v_max3_u32 v140, v140, v52, v53\n\t" \
    "v_max3_u32 v140, v140, v54, v55\n\t" \
    "v_max3_u32 v140, v140, v56, v57\n\t" \
    "v_max3_u32 v140, v140, v58, v59\n\t" \
    "v_max3_u32 v140, v140, v60, v61\n\t" \
    "v_max3_u32 v140, v140, v62, v63\n\t" \
    "v_max3_u32 v140, v140, v64, v65\n\t" \
    "v_max3_u32 v140, v140, v66, v67\n\t" \
    "v_max3_u32 v140, v140, v68, v69\n\t" \
    "v_max3_u32 v140, v140, v70, v71\n\t" \
    "v_cmp_eq_u32 vcc, -1, v140\n\t" \
    "s_cbranch_vccz vv_Gback48_%=\n\t" \
    "s_waitcnt lgkmcnt(0)\n\t" \
    "v_sub_u32 v140, v1, v3\n\t" \
    "s_nop 0\n\t" \
    "v_readfirstlane_b32 s69, v140\n\t" \
    "v_add_u32 v143, 20480, v0\n\t" \
    "s_nop 7\n\t" \
    "s_add_u32 s30, s46, s69\n\t" \
    "s_addc_u32 s31, s47, 0\n\t" \
    "vv_Gag50_%=:\n\t" \
    "global_load_dwordx4 v[36:39], v1, s[46:47] sc1\n\t" \
    "global_load_dwordx4 v[40:43], v23, s[46:47] sc1\n\t" \
    "global_load_dwordx4 v[44:47], v143, s[30:31] sc1\n\t" \
    "global_load_dwordx4 v[48:51], v4, s[30:31] sc1\n\t" \
    "global_load_dwordx4 v[52:55], v5, s[30:31] sc1\n\t" \
    "global_load_dwordx4 v[56:59], v6, s[30:31] sc1\n\t" \
    "global_load_dwordx4 v[60:63], v7, s[30:31] sc1\n\t" \
    "global_load_dwordx4 v[64:67], v8, s[30:31] sc1\n\t" \
    "global_load_dwordx4 v[68:71], v9, s[30:31] sc1\n\t" \
    "s_waitcnt vmcnt(0)\n\t" \
    "v_max_u32 v140, v36, v37\n\t" \
    "v_max3_u32 v140, v140, v38, v39\n\t" \
    "v_max3_u32 v140, v140, v40, v41\n\t" \
    "v_max3_u32 v140, v140, v42, v43\n\t" \
    "v_max3_u32 v140, v140, v44, v45\n\t" \
    "v_max3_u32 v140, v140, v46, v47\n\t" \
    "v_max3_u32 v140, v140, v48, v49\n\t" \
    "v_max3_u32 v140, v140, v50, v51\n\t" \
    "v_max3_u32 v140, v140, v52, v53\n\t" \
    "v_max3_u32 v140, v140, v54, v55\n\t" \
    "v_max3_u32 v140, v140, v56, v57\n\t" \
    "v_max3_u32 v140, v140, v58, v59\n\t" \
    "v_max3_u32 v140, v140, v60, v61\n\t" \
    "v_max3_u32 v140, v140, v62, v63\n\t" \
    "v_max3_u32 v140, v140, v64, v65\n\t" \
    "v_max3_u32 v140, v140, v66, v67\n\t" \
    "v_max3_u32 v140, v140, v68, v69\n\t" \
    "v_max3_u32 v140, v140, v70, v71\n\t" \
    "v_cmp_eq_u32 vcc, -1, v140\n\t" \
    "s_cbranch_vccz vv_Gredo51_%=\n\t" \
    "s_cmp_eq_u32 s68, 0\n\t" \
    "s_cbranch_scc1 vv_F52_%=\n\t" \
    "s_sub_u32 s68, s68, 1\n\t" \
    "s_sleep 1\n\t" \
    "s_branch vv_Gag50_%=\n\t" \
    "vv_F52_%=:\n\t" \
    "v_mov_b32 v140, 5767424\n\t" \
    "v_mov_b32 v141, 2\n\t" \
    "global_store_dword v140, v141, s[36:37] sc1\n\t" \
    "s_waitcnt vmcnt(0)\n\t" \
    "s_branch vv_Gredo51_%=\n\t" \
    "vv_Gredo51_%=:\n\t" \
    "v_mov_b32 v72, 0\n\t" \
    "v_mov_b32 v73, 0\n\t" \
    "v_mov_b32 v74, 0\n\t" \
    "v_mov_b32 v75, 0\n\t" \
    "v_mov_b32 v76, 0\n\t" \
    "v_mov_b32 v77, 0\n\t" \
    "v_mov_b32 v78, 0\n\t" \
    "v_mov_b32 v79, 0\n\t" \
    "v_smfmac_f32_16x16x64_bf16 v[72:75], v[36:39], a[32:39], v10\n\t" \
    "v_smfmac_f32_16x16x64_bf16 v[72:75], v[40:43], a[40:47], v10\n\t" \
    "v_smfmac_f32_16x16x64_bf16 v[72:75], v[48:51], a[48:55], v10\n\t" \
    "v_smfmac_f32_16x16x64_bf16 v[72:75], v[52:55], a[56:63], v10\n\t" \
    "v_smfmac_f32_16x16x64_bf16 v[72:75], v[56:59], a[0:7], v10\n\t" \
    "v_smfmac_f32_16x16x64_bf16 v[72:75], v[60:63], a[8:15], v10\n\t" \
    "v_smfmac_f32_16x16x64_bf16 v[72:75], v[64:67], a[16:23], v10\n\t" \
    "v_smfmac_f32_16x16x64_bf16 v[72:75], v[68:71], a[24:31], v10\n\t" \
    "v_smfmac_f32_16x16x64_bf16 v[72:75], v[44:47], a[64:71], v10\n\t" \
    "v_smfmac_f32_16x16x64_bf16 v[76:79], v[36:39], a[104:111], v10\n\t" \
    "v_smfmac_f32_16x16x64_bf16 v[76:79], v[40:43], a[112:119], v10\n\t" \
    "v_smfmac_f32_16x16x64_bf16 v[76:79], v[48:51], a[120:127], v10\n\t" \
    "v_smfmac_f32_16x16x64_bf16 v[76:79], v[52:55], a[128:135], v10\n\t" \
    "v_smfmac_f32_16x16x64_bf16 v[76:79], v[56:59], a[72:79], v10\n\t" \
    "v_smfmac_f32_16x16x64_bf16 v[76:79], v[60:63], a[80:87], v10\n\t" \
    "v_smfmac_f32_16x16x64_bf16 v[76:79], v[64:67], a[88:95], v10\n\t" \
    "v_smfmac_f32_16x16x64_bf16 v[76:79], v[68:71], a[96:103], v10\n\t" \
    "v_smfmac_f32_16x16x64_bf16 v[76:79], v[44:47], a[136:143], v10\n\t" \
    "v_add_f32 v72, v72, v74\n\t" \
    "v_add_f32 v73, v73, v75\n\t" \
    "v_cvt_pk_bf16_f32 v80, v72, v73\n\t" \
    "ds_write_b32 v12, v80 offset:0\n\t" \
    "s_nop 3\n\t" \
    "v_pk_add_f32 v[76:77], v[76:77], v[78:79]\n\t" \
    "v_cvt_pk_bf16_f32 v81, v76, v77\n\t" \
    "ds_write_b32 v12, v81 offset:256\n\t" \
    "s_branch vv_Gback48_%=\n\t" \
    "vv_end40_%=:\n\t" \
    "s_endpgm\n\t" \
    ".p2align 6\n\t" \
    "s_nop 0\n\t" \
    "s_nop 0\n\t" \
    "s_nop 0\n\t" \
    "s_nop 0\n\t" \
    "s_nop 0\n\t" \
    "s_nop 0\n\t" \
    "vv_loop12_%=:\n\t" \
    "s_getpc_b64 s[30:31]\n\t" \
    "vv_Wp55_%=:\n\t" \
    "s_add_u32 s30, s30, vv_We56_%=-vv_Wp55_%=\n\t" \
    "s_addc_u32 s31, s31, 0\n\t" \
    "s_sub_u32 s70, s70, 96\n\t" \
    "vv_W54_%=:\n\t" \
    "s_memtime s[56:57]\n\t" \
    "s_waitcnt lgkmcnt(0)\n\t" \
    "s_sub_u32 s69, s70, s56\n\t" \
    "s_cmp_gt_i32 s69, 80\n\t" \
    "s_cbranch_scc1 vv_W54_%=\n\t" \
    "s_max_i32 s69, s69, 0\n\t" \
    "s_and_b32 s69, s69, -4\n\t" \
    "s_sub_u32 s30, s30, s69\n\t" \
    "s_subb_u32 s31, s31, 0\n\t" \
    "s_setpc_b64 s[30:31]\n\t" \
    "s_nop 0\n\t" \
    "s_nop 0\n\t" \
    "s_nop 0\n\t" \
    "s_nop 0\n\t" \
    "s_nop 0\n\t" \
    "s_nop 0\n\t" \
    "s_nop 0\n\t" \
    "s_nop 0\n\t" \
    "s_nop 0\n\t" \
    "s_nop 0\n\t" \
    "s_nop 0\n\t" \
    "s_nop 0\n\t" \
    "s_nop 0\n\t" \
    "s_nop 0\n\t" \
    "s_nop 0\n\t" \
    "s_nop 0\n\t" \
    "s_nop 0\n\t" \
    "s_nop 0\n\t" \
    "s_nop 0\n\t" \
    "s_nop 0\n\t" \
    "vv_We56_%=:\n\t" \
    "vv_G57_%=:\n\t" \
    "global_load_dwordx4 v[36:39], v1, s[46:47] sc1\n\t" \
    "global_load_dword v141, v2, s[46:47] sc1\n\t" \
    "global_load_dwordx4 v[40:43], v23, s[46:47] sc1\n\t" \
    "s_waitcnt vmcnt(2)\n\t" \
    "ds_write_b128 v3, v[36:39]\n\t" \
    "v_smfmac_f32_16x16x64_bf16 v[72:75], v[36:39], a[48:55], v10\n\t" \
    "s_waitcnt vmcnt(1)\n\t" \
    "ds_write_b32 v22, v141 offset:20224\n\t" \
    "s_waitcnt vmcnt(0)\n\t" \
    "ds_write_b128 v3, v[40:43] offset:1024\n\t" \
    "v_max3_u32 v140, v36, v40, v141\n\t" \
    "v_cmp_eq_u32 vcc, -1, v140\n\t" \
    "s_cbranch_vccnz vv_Gbad58_%=\n\t" \
    "vv_Gc59_%=:\n\t" \
    "s_waitcnt lgkmcnt(0)\n\t" \
    "s_barrier\n\t" \
    "ds_read_b128 v[48:51], v4\n\t" \
    "ds_read_b128 v[52:55], v5\n\t" \
    "v_smfmac_f32_16x16x64_bf16 v[76:79], v[36:39], a[120:127], v10\n\t" \
    "ds_read_b128 v[56:59], v6\n\t" \
    "ds_read_b128 v[60:63], v7\n\t" \
    "v_smfmac_f32_16x16x64_bf16 v[72:75], v[40:43], a[56:63], v10\n\t" \
    "ds_read_b128 v[64:67], v8\n\t" \
    "v_smfmac_f32_16x16x64_bf16 v[76:79], v[40:43], a[128:135], v10\n\t" \
    "ds_read_b128 v[68:71], v9\n\t" \
    "s_waitcnt lgkmcnt(5)\n\t" \
    "v_smfmac_f32_16x16x64_bf16 v[72:75], v[48:51], a[0:7], v10\n\t" \
    "ds_read_b128 v[44:47], v0 offset:19456\n\t" \
    "s_waitcnt lgkmcnt(5)\n\t" \
    "v_smfmac_f32_16x16x64_bf16 v[72:75], v[52:55], a[8:15], v10\n\t" \
    "s_waitcnt lgkmcnt(4)\n\t" \
    "v_smfmac_f32_16x16x64_bf16 v[72:75], v[56:59], a[16:23], v10\n\t" \
    "s_waitcnt lgkmcnt(3)\n\t" \
    "v_smfmac_f32_16x16x64_bf16 v[72:75], v[60:63], a[24:31], v10\n\t" \
    "s_waitcnt lgkmcnt(2)\n\t" \
    "v_smfmac_f32_16x16x64_bf16 v[72:75], v[64:67], a[32:39], v10\n\t" \
    "s_waitcnt lgkmcnt(1)\n\t" \
    "v_smfmac_f32_16x16x64_bf16 v[72:75], v[68:71], a[40:47], v10\n\t" \
    "s_waitcnt lgkmcnt(0)\n\t" \
    "v_smfmac_f32_16x16x64_bf16 v[72:75], v[44:47], a[64:71], v10\n\t" \
    "s_nop 2\n\t" \
    "v_smfmac_f32_16x16x64_bf16 v[76:79], v[48:51], a[72:79], v10\n\t" \
    "s_nop 2\n\t" \
    "v_smfmac_f32_16x16x64_bf16 v[76:79], v[52:55], a[80:87], v10\n\t" \
    "v_add_f32 v72, v72, v74\n\t" \
    "v_smfmac_f32_16x16x64_bf16 v[76:79], v[56:59], a[88:95], v10\n\t" \
    "v_add_f32 v73, v73, v75\n\t" \
    "v_smfmac_f32_16x16x64_bf16 v[76:79], v[60:63], a[96:103], v10\n\t" \
    "v_cvt_pk_bf16_f32 v80, v72, v73\n\t" \
    "v_smfmac_f32_16x16x64_bf16 v[76:79], v[64:67], a[104:111], v10\n\t" \
    "ds_write_b32 v12, v80 offset:0\n\t" \
    "v_smfmac_f32_16x16x64_bf16 v[76:79], v[68:71], a[112:119], v10\n\t" \
    "v_smfmac_f32_16x16x64_bf16 v[76:79], v[44:47], a[136:143], v10\n\t" \
    "s_nop 6\n\t" \
    "v_cmp_u_f32 vcc, v72, v73\n\t" \
    "v_pk_add_f32 v[76:77], v[76:77], v[78:79]\n\t" \
    "v_cvt_pk_bf16_f32 v81, v76, v77\n\t" \
    "ds_write_b32 v12, v81 offset:256\n\t" \
    "s_cbranch_vccnz vv_Gslow60_%=\n\t" \
    "vv_Gback61_%=:\n\t" \
    "s_waitcnt lgkmcnt(0)\n\t" \
    "s_barrier\n\t" \
    "ds_read_b64_tr_b16 v[84:85], v13 offset:0\n\t" \
    "v_smfmac_f32_16x16x64_bf16 v[100:103], v[36:39], a[0:7], v10\n\t" \
    "ds_read_b64_tr_b16 v[86:87], v13 offset:64\n\t" \
    "ds_read_b64_tr_b16 v[92:93], v13 offset:1152\n\t" \
    "v_smfmac_f32_16x16x64_bf16 v[100:103], v[36:39], a[0:7], v10\n\t" \
    "ds_read_b64_tr_b16 v[94:95], v13 offset:1216\n\t" \
    "ds_read_b64_tr_b16 v[88:89], v25 offset:0\n\t" \
    "v_smfmac_f32_16x16x64_bf16 v[100:103], v[36:39], a[0:7], v10\n\t" \
    "ds_read_b64_tr_b16 v[90:91], v25 offset:64\n\t" \
    "s_waitcnt lgkmcnt(4)\n\t" \
    "v_smfmac_f32_16x16x64_bf16 v[124:127], v[84:87], v[240:247], v19\n\t" \
    "ds_read_b64_tr_b16 v[96:97], v25 offset:1152\n\t" \
    "ds_read_b64_tr_b16 v[98:99], v25 offset:1216\n\t" \
    "s_waitcnt lgkmcnt(4)\n\t" \
    "v_smfmac_f32_16x16x64_bf16 v[124:127], v[92:95], v[248:255], v19\n\t" \
    "s_nop 2\n\t" \
    "v_smfmac_f32_16x16x64_bf16 v[108:111], v[84:87], a[144:151], v19\n\t" \
    "s_nop 2\n\t" \
    "v_smfmac_f32_16x16x64_bf16 v[108:111], v[92:95], a[160:167], v19\n\t" \
    "s_nop 1\n\t" \
    "v_permlane32_swap_b32 v124, v126\n\t" \
    "v_smfmac_f32_16x16x64_bf16 v[112:115], v[84:87], a[176:183], v19\n\t" \
    "s_nop 1\n\t" \
    "v_permlane32_swap_b32 v125, v127\n\t" \
    "v_smfmac_f32_16x16x64_bf16 v[112:115], v[92:95], a[192:199], v19\n\t" \
    "s_nop 0\n\t" \
    "v_add_f32 v124, v124, v126\n\t" \
    "s_waitcnt lgkmcnt(2)\n\t" \
    "v_smfmac_f32_16x16x64_bf16 v[108:111], v[88:91], a[152:159], v19\n\t" \
    "s_nop 1\n\t" \
    "v_add_f32 v125, v125, v127\n\t" \
    "v_smfmac_f32_16x16x64_bf16 v[112:115], v[88:91], a[184:191], v19\n\t" \
    "s_nop 0\n\t" \
    "v_cvt_pk_bf16_f32 v136, v124, v125\n\t" \
    "s_waitcnt lgkmcnt(0)\n\t" \
    "v_smfmac_f32_16x16x64_bf16 v[108:111], v[96:99], a[168:175], v19\n\t" \
    "s_nop 1\n\t" \
    "v_cvt_pk_bf16_f32 v137, v124, v125\n\t" \
    "v_smfmac_f32_16x16x64_bf16 v[112:115], v[96:99], a[200:207], v19\n\t" \
    "s_nop 1\n\t" \
    "v_permlane32_swap_b32 v136, v137\n\t" \
    "v_smfmac_f32_16x16x64_bf16 v[116:119], v[84:87], a[208:215], v19\n\t" \
    "s_mov_b32 exec_hi, 0\n\t" \
    "global_store_dwordx2 v16, v[136:137], s[38:39] sc1\n\t" \
    "s_mov_b32 exec_hi, -1\n\t" \
    "v_cvt_pk_bf16_f32 v128, v108, v109\n\t" \
    "v_smfmac_f32_16x16x64_bf16 v[116:119], v[88:91], a[216:223], v19\n\t" \
    "s_nop 1\n\t" \
    "v_cvt_pk_bf16_f32 v129, v110, v111\n\t" \
    "v_smfmac_f32_16x16x64_bf16 v[116:119], v[92:95], a[224:231], v19\n\t" \
    "s_nop 1\n\t" \
    "v_cvt_pk_bf16_f32 v130, v112, v113\n\t" \
    "v_smfmac_f32_16x16x64_bf16 v[116:119], v[96:99], a[232:239], v19\n\t" \
    "s_nop 1\n\t" \
    "v_cvt_pk_bf16_f32 v131, v114, v115\n\t" \
    "v_smfmac_f32_16x16x64_bf16 v[120:123], v[84:87], a[240:247], v19\n\t" \
    "s_nop 1\n\t" \
    "v_permlane16_swap_b32 v128, v130\n\t" \
    "v_smfmac_f32_16x16x64_bf16 v[120:123], v[88:91], a[248:255], v19\n\t" \
    "s_nop 1\n\t" \
    "v_permlane16_swap_b32 v129, v131\n\t" \
    "v_smfmac_f32_16x16x64_bf16 v[120:123], v[92:95], v[224:231], v19\n\t" \
    "s_nop 0\n\t" \
    "global_store_dwordx4 v14, v[128:131], s[38:39] sc1\n\t" \
    "v_cvt_pk_bf16_f32 v132, v116, v117\n\t" \
    "v_smfmac_f32_16x16x64_bf16 v[120:123], v[96:99], v[232:239], v19\n\t" \
    "s_nop 1\n\t" \
    "v_cvt_pk_bf16_f32 v133, v118, v119\n\t" \
    "s_nop 4\n\t" \
    "v_cvt_pk_bf16_f32 v134, v120, v121\n\t" \
    "v_cvt_pk_bf16_f32 v135, v122, v123\n\t" \
    "s_nop 0\n\t" \
    "v_permlane16_swap_b32 v132, v134\n\t" \
    "v_permlane16_swap_b32 v133, v135\n\t" \
    "global_store_dwordx4 v15, v[132:135], s[38:39] sc1\n\t" \
    "global_load_dword v140, v20, s[52:53] sc1\n\t" \
    "s_cmp_eq_u32 s65, 63\n\t" \
    "s_cbranch_scc1 vv_end53_%=\n\t" \
    "s_cmp_lg_u32 s65, 0\n\t" \
    "s_cbranch_scc1 vv_ng62_%=\n\t" \
    "v_mov_b32 v160, 5838848\n\t" \
    "v_mov_b32 v161, 1\n\t" \
    "s_mov_b64 exec, 1\n\t" \
    "global_atomic_add v162, v160, v161, s[36:37] sc0 sc1\n\t" \
    "s_mov_b64 exec, -1\n\t" \
    "v_mbcnt_lo_u32_b32 v160, -1, 0\n\t" \
    "v_and_b32 v161, 3, v160\n\t" \
    "v_lshlrev_b32 v161, 16, v161\n\t" \
    "v_bfe_u32 v163, v160, 2, 2\n\t" \
    "v_lshl_add_u32 v161, v163, 11, v161\n\t" \
    "v_bfe_u32 v163, v160, 4, 1\n\t" \
    "v_lshl_add_u32 v161, v163, 14, v161\n\t" \
    "v_add_u32 v161, s63, v161\n\t" \
    "s_waitcnt vmcnt(0)\n\t" \
    "v_readfirstlane_b32 s69, v162\n\t" \
    "s_nop 1\n\t" \
    "s_cmp_lg_u32 s69, 126\n\t" \
    "s_cbranch_scc1 vv_go63_%=\n\t" \
    "s_memrealtime s[56:57]\n\t" \
    "s_waitcnt lgkmcnt(0)\n\t" \
    "s_lshl_b32 s69, s56, 4\n\t" \
    "s_add_u32 s69, s69, 2960\n\t" \
    "s_add_u32 s70, s56, 24\n\t" \
    "s_lshr_b32 s70, s70, 4\n\t" \
    "v_lshrrev_b32 v163, 2, v160\n\t" \
    "v_add_u32 v163, s70, v163\n\t" \
    "v_and_b32 v163, 63, v163\n\t" \
    "v_lshlrev_b32 v163, 6, v163\n\t" \
    "v_and_b32 v162, 3, v160\n\t" \
    "v_lshl_add_u32 v163, v162, 12, v163\n\t" \
    "v_add_u32 v163, 5865472, v163\n\t" \
    "v_mov_b32 v160, s69\n\t" \
    "s_mov_b32 exec_hi, 0\n\t" \
    "global_store_dword v163, v160, s[36:37] sc1\n\t" \
    "global_store_dword v161, v160, s[36:37] sc1\n\t" \
    "s_mov_b32 exec_hi, -1\n\t" \
    "vv_go63_%=:\n\t" \
    "vv_ng62_%=:\n\t" \
    "s_waitcnt lgkmcnt(0)\n\t" \
    "s_mov_b32 s70, 116\n\t" \
    "vv_E67_%=:\n\t" \
    "s_memtime s[56:57]\n\t" \
    "s_memrealtime s[2:3]\n\t" \
    "s_nop 0\n\t" \
    "s_memrealtime s[4:5]\n\t" \
    "s_nop 0\n\t" \
    "s_memrealtime s[6:7]\n\t" \
    "s_nop 0\n\t" \
    "s_memrealtime s[8:9]\n\t" \
    "s_nop 0\n\t" \
    "s_memrealtime s[10:11]\n\t" \
    "s_nop 0\n\t" \
    "s_memrealtime s[12:13]\n\t" \
    "s_nop 0\n\t" \
    "s_memrealtime s[14:15]\n\t" \
    "s_nop 0\n\t" \
    "s_memrealtime s[16:17]\n\t" \
    "s_nop 0\n\t" \
    "s_memrealtime s[18:19]\n\t" \
    "s_nop 0\n\t" \
    "s_memrealtime s[20:21]\n\t" \
    "s_nop 0\n\t" \
    "s_memrealtime s[22:23]\n\t" \
    "s_nop 0\n\t" \
    "s_memrealtime s[24:25]\n\t" \
    "s_nop 0\n\t" \
    "s_memrealtime s[26:27]\n\t" \
    "s_nop 0\n\t" \
    "s_memrealtime s[28:29]\n\t" \
    "s_memtime s[54:55]\n\t" \
    "s_add_u32 s70, s70, 8\n\t" \
    "s_waitcnt lgkmcnt(0)\n\t" \
    "s_sub_u32 s69, s54, s56\n\t" \
    "s_cmp_gt_u32 s69, s70\n\t" \
    "s_cbranch_scc1 vv_E67_%=\n\t" \
    "s_add_u32 s2, s2, s4\n\t" \
    "s_add_u32 s6, s6, s8\n\t" \
    "s_add_u32 s10, s10, s12\n\t" \
    "s_add_u32 s14, s14, s16\n\t" \
    "s_add_u32 s18, s18, s20\n\t" \
    "s_add_u32 s22, s22, s24\n\t" \
    "s_add_u32 s2, s2, s6\n\t" \
    "s_add_u32 s10, s10, s14\n\t" \
    "s_add_u32 s18, s18, s22\n\t" \
    "s_add_u32 s2, s2, s26\n\t" \
    "s_add_u32 s10, s10, s18\n\t" \
    "s_add_u32 s2, s2, s10\n\t" \
    "s_mul_i32 s70, s28, 13\n\t" \
    "s_sub_u32 s70, s70, s2\n\t" \
    "s_min_u32 s70, s70, 52\n\t" \
    "s_mul_i32 s69, s69, s70\n\t" \
    "s_mul_hi_u32 s69, s69, 76695844\n\t" \
    "s_mov_b32 s75, s73\n\t" \
    "s_mov_b32 s76, s74\n\t" \
    "s_add_u32 s73, s56, s69\n\t" \
    "s_lshl_b32 s74, s28, 4\n\t" \
    "s_sub_u32 s69, s73, s75\n\t" \
    "s_sub_u32 s70, s74, s76\n\t" \
    "s_sub_u32 s71, s70, 1600\n\t" \
    "s_cmp_lt_u32 s71, 400000\n\t" \
    "s_cbranch_scc0 vv_K68_%=\n\t" \
    "v_cvt_f32_u32 v160, s69\n\t" \
    "v_cvt_f32_u32 v161, s70\n\t" \
    "v_rcp_f32 v162, v160\n\t" \
    "v_rcp_f32 v163, v161\n\t" \
    "v_mul_f32 v162, v162, v161\n\t" \
    "v_mul_f32 v163, v163, v160\n\t" \
    "v_mul_f32 v162, 0x4f800000, v162\n\t" \
    "v_mul_f32 v163, 0x4f000000, v163\n\t" \
    "v_cvt_u32_f32 v162, v162\n\t" \
    "v_cvt_u32_f32 v163, v163\n\t" \
    "v_readfirstlane_b32 s69, v162\n\t" \
    "v_readfirstlane_b32 s70, v163\n\t" \
    "s_nop 1\n\t" \
    "s_min_u32 s71, s65, 1\n\t" \
    "s_sub_u32 s69, s69, s78\n\t" \
    "s_ashr_i32 s69, s69, s71\n\t" \
    "s_add_u32 s78, s78, s69\n\t" \
    "s_sub_u32 s70, s70, s77\n\t" \
    "s_ashr_i32 s70, s70, s71\n\t" \
    "s_add_u32 s77, s77, s70\n\t" \
    "vv_K68_%=:\n\t" \
    "s_add_u32 s65, s65, 1\n\t" \
    "v_mov_b32 v72, 0\n\t" \
    "v_mov_b32 v73, 0\n\t" \
    "v_mov_b32 v74, 0\n\t" \
    "v_mov_b32 v75, 0\n\t" \
    "v_mov_b32 v76, 0\n\t" \
    "v_mov_b32 v77, 0\n\t" \
    "v_mov_b32 v78, 0\n\t" \
    "v_mov_b32 v79, 0\n\t" \
    "v_mov_b32 v108, 0\n\t" \
    "v_mov_b32 v109, 0\n\t" \
    "v_mov_b32 v110, 0\n\t" \
    "v_mov_b32 v111, 0\n\t" \
    "v_mov_b32 v112, 0\n\t" \
    "v_mov_b32 v113, 0\n\t" \
    "v_mov_b32 v114, 0\n\t" \
    "v_mov_b32 v115, 0\n\t" \
    "v_mov_b32 v116, 0\n\t" \
    "v_mov_b32 v117, 0\n\t" \
    "v_mov_b32 v118, 0\n\t" \
    "v_mov_b32 v119, 0\n\t" \
    "v_mov_b32 v120, 0\n\t" \
    "v_mov_b32 v121, 0\n\t" \
    "v_mov_b32 v122, 0\n\t" \
    "v_mov_b32 v123, 0\n\t" \
    "v_mov_b32 v124, 0\n\t" \
    "v_mov_b32 v125, 0\n\t" \
    "v_mov_b32 v126, 0\n\t" \
    "v_mov_b32 v127, 0\n\t" \
    "s_add_u32 s69, s65, 0\n\t" \
    "s_and_b32 s69, s69, 7\n\t" \
    "s_lshl_b32 s69, s69, 18\n\t" \
    "s_add_u32 s69, s69, s62\n\t" \
    "s_add_u32 s46, s36, s69\n\t" \
    "s_addc_u32 s47, s37, 0\n\t" \
    "s_add_u32 s70, s65, 0\n\t" \
    "s_mul_i32 s69, s70, 171\n\t" \
    "s_lshr_b32 s69, s69, 9\n\t" \
    "s_mul_i32 s70, s70, 589824\n\t" \
    "s_mul_i32 s71, s69, 327680\n\t" \
    "s_add_u32 s70, s70, s71\n\t" \
    "s_lshl1_add_u32 s69, s69, 4\n\t" \
    "s_bitcmp1_b64 s[44:45], s69\n\t" \
    "s_cselect_b32 s69, 8192, 0\n\t" \
    "s_add_u32 s69, s69, s70\n\t" \
    "s_add_u32 s69, s69, 123731968\n\t" \
    "s_add_u32 s38, s36, s69\n\t" \
    "s_addc_u32 s39, s37, 0\n\t" \
    "s_and_b32 s69, s69, 0xffe00000\n\t" \
    "s_add_u32 s69, s69, 1823744\n\t" \
    "v_mov_b32 v17, s69\n\t" \
    "global_load_dword v17, v17, s[36:37]\n\t" \
    "s_waitcnt vmcnt(0)\n\t" \
    "v_cmp_eq_u32 vcc, -1, v140\n\t" \
    "s_cbranch_vccz vv_Hok65_%=\n\t" \
    "vv_H64_%=:\n\t" \
    "global_load_dword v140, v20, s[52:53] sc1\n\t" \
    "s_waitcnt vmcnt(0)\n\t" \
    "v_cmp_eq_u32 vcc, -1, v140\n\t" \
    "s_cbranch_vccz vv_Hc66_%=\n\t" \
    "s_cmp_eq_u32 s68, 0\n\t" \
    "s_cbranch_scc1 vv_F69_%=\n\t" \
    "s_sub_u32 s68, s68, 1\n\t" \
    "s_sleep 2\n\t" \
    "s_branch vv_H64_%=\n\t" \
    "vv_F69_%=:\n\t" \
    "v_mov_b32 v141, 5767424\n\t" \
    "v_mov_b32 v142, 6\n\t" \
    "global_store_dword v141, v142, s[36:37] sc1\n\t" \
    "s_waitcnt vmcnt(0)\n\t" \
    "s_branch vv_Hc66_%=\n\t" \
    "vv_Hc66_%=:\n\t" \
    "s_memtime s[56:57]\n\t" \
    "s_waitcnt lgkmcnt(0)\n\t" \
    "s_sub_u32 s69, s56, s73\n\t" \
    "s_cmp_lt_u32 s69, 3000\n\t" \
    "s_cbranch_scc1 vv_St70_%=\n\t" \
    "s_waitcnt lgkmcnt(0)\n\t" \
    "s_mov_b32 s70, 116\n\t" \
    "vv_E71_%=:\n\t" \
    "s_memtime s[56:57]\n\t" \
    "s_memrealtime s[2:3]\n\t" \
    "s_nop 0\n\t" \
    "s_memrealtime s[4:5]\n\t" \
    "s_nop 0\n\t" \
    "s_memrealtime s[6:7]\n\t" \
    "s_nop 0\n\t" \
    "s_memrealtime s[8:9]\n\t" \
    "s_nop 0\n\t" \
    "s_memrealtime s[10:11]\n\t" \
    "s_nop 0\n\t" \
    "s_memrealtime s[12:13]\n\t" \
    "s_nop 0\n\t" \
    "s_memrealtime s[14:15]\n\t" \
    "s_nop 0\n\t" \
    "s_memrealtime s[16:17]\n\t" \
    "s_nop 0\n\t" \
    "s_memrealtime s[18:19]\n\t" \
    "s_nop 0\n\t" \
    "s_memrealtime s[20:21]\n\t" \
    "s_nop 0\n\t" \
    "s_memrealtime s[22:23]\n\t" \
    "s_nop 0\n\t" \
    "s_memrealtime s[24:25]\n\t" \
    "s_nop 0\n\t" \
    "s_memrealtime s[26:27]\n\t" \
    "s_nop 0\n\t" \
    "s_memrealtime s[28:29]\n\t" \
    "s_memtime s[54:55]\n\t" \
    "s_add_u32 s70, s70, 8\n\t" \
    "s_waitcnt lgkmcnt(0)\n\t" \
    "s_sub_u32 s69, s54, s56\n\t" \
    "s_cmp_gt_u32 s69, s70\n\t" \
    "s_cbranch_scc1 vv_E71_%=\n\t" \
    "s_add_u32 s2, s2, s4\n\t" \
    "s_add_u32 s6, s6, s8\n\t" \
    "s_add_u32 s10, s10, s12\n\t" \
    "s_add_u32 s14, s14, s16\n\t" \
    "s_add_u32 s18, s18, s20\n\t" \
    "s_add_u32 s22, s22, s24\n\t" \
    "s_add_u32 s2, s2, s6\n\t" \
    "s_add_u32 s10, s10, s14\n\t" \
    "s_add_u32 s18, s18, s22\n\t" \
    "s_add_u32 s2, s2, s26\n\t" \
    "s_add_u32 s10, s10, s18\n\t" \
    "s_add_u32 s2, s2, s10\n\t" \
    "s_mul_i32 s70, s28, 13\n\t" \
    "s_sub_u32 s70, s70, s2\n\t" \
    "s_min_u32 s70, s70, 52\n\t" \
    "s_mul_i32 s69, s69, s70\n\t" \
    "s_mul_hi_u32 s69, s69, 76695844\n\t" \
    "s_mov_b32 s75, s73\n\t" \
    "s_mov_b32 s76, s74\n\t" \
    "s_add_u32 s73, s56, s69\n\t" \
    "s_lshl_b32 s74, s28, 4\n\t" \
    "vv_St70_%=:\n\t" \
    "vv_Hok65_%=:\n\t" \
    "v_max_u32_dpp v140, v140, v140 row_ror:1 row_mask:0xf bank_mask:0xf\n\t" \
    "s_nop 1\n\t" \
    "v_max_u32_dpp v140, v140, v140 row_ror:2 row_mask:0xf bank_mask:0xf\n\t" \
    "s_nop 1\n\t" \
    "v_max_u32_dpp v140, v140, v140 row_ror:4 row_mask:0xf bank_mask:0xf\n\t" \
    "s_nop 1\n\t" \
    "v_max_u32_dpp v140, v140, v140 row_ror:8 row_mask:0xf bank_mask:0xf\n\t" \
    "s_nop 0\n\t" \
    "v_readlane_b32 s69, v140, 0\n\t" \
    "v_readlane_b32 s71, v140, 16\n\t" \
    "s_nop 1\n\t" \
    "s_max_u32 s81, s69, s71\n\t" \
    "s_mul_hi_u32 s69, s85, s78\n\t" \
    "s_add_u32 s79, s81, s69\n\t" \
    "s_add_u32 s79, s79, s87\n\t" \
    "s_cmp_ge_u32 s65, 53\n\t" \
    "s_cselect_b32 s69, 32, 0\n\t" \
    "s_sub_u32 s79, s79, s69\n\t" \
    "s_cmp_lt_u32 s65, 10\n\t" \
    "s_cselect_b32 s69, 48, 0\n\t" \
    "s_add_u32 s79, s79, s69\n\t" \
    "s_add_u32 s80, s79, s88\n\t" \
    "s_sub_u32 s70, s79, s74\n\t" \
    "s_max_i32 s70, s70, 0\n\t" \
    "s_min_i32 s70, s70, 16000\n\t" \
    "s_lshl_b32 s70, s70, 1\n\t" \
    "s_mul_hi_u32 s70, s70, s77\n\t" \
    "s_add_u32 s70, s70, s73\n\t" \
    "v_mov_b32 v160, s70\n\t" \
    "ds_write_b32 v11, v160 offset:20480\n\t" \
    "s_and_b32 s69, s65, 7\n\t" \
    "s_lshl_b32 s69, s69, 18\n\t" \
    "s_add_u32 s69, s69, s63\n\t" \
    "s_add_u32 s52, s36, s69\n\t" \
    "s_addc_u32 s53, s37, 0\n\t" \
    "s_waitcnt lgkmcnt(0)\n\t" \
    "s_barrier\n\t" \
    "s_branch vv_loop12_%=\n\t" \
    "vv_Gbad58_%=:\n\t" \
    "s_waitcnt lgkmcnt(0)\n\t" \
    "s_nop 7\n\t" \
    "v_mov_b32 v72, 0\n\t" \
    "v_mov_b32 v73, 0\n\t" \
    "v_mov_b32 v74, 0\n\t" \
    "v_mov_b32 v75, 0\n\t" \
    "v_mov_b32 v76, 0\n\t" \
    "v_mov_b32 v77, 0\n\t" \
    "v_mov_b32 v78, 0\n\t" \
    "v_mov_b32 v79, 0\n\t" \
    "s_cmp_eq_u32 s68, 0\n\t" \
    "s_cbranch_scc1 vv_F72_%=\n\t" \
    "s_sub_u32 s68, s68, 1\n\t" \
    "s_branch vv_G57_%=\n\t" \
    "vv_F72_%=:\n\t" \
    "v_mov_b32 v140, 5767424\n\t" \
    "v_mov_b32 v141, 1\n\t" \
    "global_store_dword v140, v141, s[36:37] sc1\n\t" \
    "s_waitcnt vmcnt(0)\n\t" \
    "s_branch vv_Gc59_%=\n\t" \
    "vv_Gslow60_%=:\n\t" \
    "v_max_u32 v140, v36, v37\n\t" \
    "v_max3_u32 v140, v140, v38, v39\n\t" \
    "v_max3_u32 v140, v140, v40, v41\n\t" \
    "v_max3_u32 v140, v140, v42, v43\n\t" \
    "v_max3_u32 v140, v140, v44, v45\n\t" \
    "v_max3_u32 v140, v140, v46, v47\n\t" \
    "v_max3_u32 v140, v140, v48, v49\n\t" \
    "v_max3_u32 v140, v140, v50, v51\n\t" \
    "v_max3_u32 v140, v140, v52, v53\n\t" \
    "v_max3_u32 v140, v140, v54, v55\n\t" \
    "v_max3_u32 v140, v140, v56, v57\n\t" \
    "v_max3_u32 v140, v140, v58, v59\n\t" \
    "v_max3_u32 v140, v140, v60, v61\n\t" \
    "v_max3_u32 v140, v140, v62, v63\n\t" \
    "v_max3_u32 v140, v140, v64, v65\n\t" \
    "v_max3_u32 v140, v140, v66, v67\n\t" \
    "v_max3_u32 v140, v140, v68, v69\n\t" \
    "v_max3_u32 v140, v140, v70, v71\n\t" \
    "v_cmp_eq_u32 vcc, -1, v140\n\t" \
    "s_cbranch_vccz vv_Gback61_%=\n\t" \
    "s_waitcnt lgkmcnt(0)\n\t" \
    "v_sub_u32 v140, v1, v3\n\t" \
    "s_nop 0\n\t" \
    "v_readfirstlane_b32 s69, v140\n\t" \
    "v_add_u32 v143, 20480, v0\n\t" \
    "s_nop 7\n\t" \
    "s_add_u32 s30, s46, s69\n\t" \
    "s_addc_u32 s31, s47, 0\n\t" \
    "vv_Gag73_%=:\n\t" \
    "global_load_dwordx4 v[36:39], v1, s[46:47] sc1\n\t" \
    "global_load_dwordx4 v[40:43], v23, s[46:47] sc1\n\t" \
    "global_load_dwordx4 v[44:47], v143, s[30:31] sc1\n\t" \
    "global_load_dwordx4 v[48:51], v4, s[30:31] sc1\n\t" \
    "global_load_dwordx4 v[52:55], v5, s[30:31] sc1\n\t" \
    "global_load_dwordx4 v[56:59], v6, s[30:31] sc1\n\t" \
    "global_load_dwordx4 v[60:63], v7, s[30:31] sc1\n\t" \
    "global_load_dwordx4 v[64:67], v8, s[30:31] sc1\n\t" \
    "global_load_dwordx4 v[68:71], v9, s[30:31] sc1\n\t" \
    "s_waitcnt vmcnt(0)\n\t" \
    "v_max_u32 v140, v36, v37\n\t" \
    "v_max3_u32 v140, v140, v38, v39\n\t" \
    "v_max3_u32 v140, v140, v40, v41\n\t" \
    "v_max3_u32 v140, v140, v42, v43\n\t" \
    "v_max3_u32 v140, v140, v44, v45\n\t" \
    "v_max3_u32 v140, v140, v46, v47\n\t" \
    "v_max3_u32 v140, v140, v48, v49\n\t" \
    "v_max3_u32 v140, v140, v50, v51\n\t" \
    "v_max3_u32 v140, v140, v52, v53\n\t" \
    "v_max3_u32 v140, v140, v54, v55\n\t" \
    "v_max3_u32 v140, v140, v56, v57\n\t" \
    "v_max3_u32 v140, v140, v58, v59\n\t" \
    "v_max3_u32 v140, v140, v60, v61\n\t" \
    "v_max3_u32 v140, v140, v62, v63\n\t" \
    "v_max3_u32 v140, v140, v64, v65\n\t" \
    "v_max3_u32 v140, v140, v66, v67\n\t" \
    "v_max3_u32 v140, v140, v68, v69\n\t" \
    "v_max3_u32 v140, v140, v70, v71\n\t" \
    "v_cmp_eq_u32 vcc, -1, v140\n\t" \
    "s_cbranch_vccz vv_Gredo74_%=\n\t" \
    "s_cmp_eq_u32 s68, 0\n\t" \
    "s_cbranch_scc1 vv_F75_%=\n\t" \
    "s_sub_u32 s68, s68, 1\n\t" \
    "s_sleep 1\n\t" \
    "s_branch vv_Gag73_%=\n\t" \
    "vv_F75_%=:\n\t" \
    "v_mov_b32 v140, 5767424\n\t" \
    "v_mov_b32 v141, 2\n\t" \
    "global_store_dword v140, v141, s[36:37] sc1\n\t" \
    "s_waitcnt vmcnt(0)\n\t" \
    "s_branch vv_Gredo74_%=\n\t" \
    "vv_Gredo74_%=:\n\t" \
    "v_mov_b32 v72, 0\n\t" \
    "v_mov_b32 v73, 0\n\t" \
    "v_mov_b32 v74, 0\n\t" \
    "v_mov_b32 v75, 0\n\t" \
    "v_mov_b32 v76, 0\n\t" \
    "v_mov_b32 v77, 0\n\t" \
    "v_mov_b32 v78, 0\n\t" \
    "v_mov_b32 v79, 0\n\t" \
    "v_smfmac_f32_16x16x64_bf16 v[72:75], v[36:39], a[48:55], v10\n\t" \
    "v_smfmac_f32_16x16x64_bf16 v[72:75], v[40:43], a[56:63], v10\n\t" \
    "v_smfmac_f32_16x16x64_bf16 v[72:75], v[48:51], a[0:7], v10\n\t" \
    "v_smfmac_f32_16x16x64_bf16 v[72:75], v[52:55], a[8:15], v10\n\t" \
    "v_smfmac_f32_16x16x64_bf16 v[72:75], v[56:59], a[16:23], v10\n\t" \
    "v_smfmac_f32_16x16x64_bf16 v[72:75], v[60:63], a[24:31], v10\n\t" \
    "v_smfmac_f32_16x16x64_bf16 v[72:75], v[64:67], a[32:39], v10\n\t" \
    "v_smfmac_f32_16x16x64_bf16 v[72:75], v[68:71], a[40:47], v10\n\t" \
    "v_smfmac_f32_16x16x64_bf16 v[72:75], v[44:47], a[64:71], v10\n\t" \
    "v_smfmac_f32_16x16x64_bf16 v[76:79], v[36:39], a[120:127], v10\n\t" \
    "v_smfmac_f32_16x16x64_bf16 v[76:79], v[40:43], a[128:135], v10\n\t" \
    "v_smfmac_f32_16x16x64_bf16 v[76:79], v[48:51], a[72:79], v10\n\t" \
    "v_smfmac_f32_16x16x64_bf16 v[76:79], v[52:55], a[80:87], v10\n\t" \
    "v_smfmac_f32_16x16x64_bf16 v[76:79], v[56:59], a[88:95], v10\n\t" \
    "v_smfmac_f32_16x16x64_bf16 v[76:79], v[60:63], a[96:103], v10\n\t" \
    "v_smfmac_f32_16x16x64_bf16 v[76:79], v[64:67], a[104:111], v10\n\t" \
    "v_smfmac_f32_16x16x64_bf16 v[76:79], v[68:71], a[112:119], v10\n\t" \
    "v_smfmac_f32_16x16x64_bf16 v[76:79], v[44:47], a[136:143], v10\n\t" \
    "v_add_f32 v72, v72, v74\n\t" \
    "v_add_f32 v73, v73, v75\n\t" \
    "v_cvt_pk_bf16_f32 v80, v72, v73\n\t" \
    "ds_write_b32 v12, v80 offset:0\n\t" \
    "s_nop 3\n\t" \
    "v_pk_add_f32 v[76:77], v[76:77], v[78:79]\n\t" \
    "v_cvt_pk_bf16_f32 v81, v76, v77\n\t" \
    "ds_write_b32 v12, v81 offset:256\n\t" \
    "s_branch vv_Gback61_%=\n\t" \
    "vv_end53_%=:\n\t" \
    "s_endpgm\n\t" \
    "vv_last_%=:\n\t" \
    ""
#define ASM_R \
    "s_mov_b64 s[36:37], %0\n\t" \
    "s_mov_b64 s[42:43], %1\n\t" \
    "s_mov_b64 s[44:45], %2\n\t" \
    "s_mov_b32 s66, %3\n\t" \
    "s_mov_b32 s67, %4\n\t" \
    "s_mov_b64 s[50:51], %5\n\t" \
    "s_mov_b32 s82, %7\n\t" \
    "s_load_dwordx8 s[84:91], %6, 0\n\t" \
    "v_mbcnt_lo_u32_b32 v223, -1, 0\n\t" \
    "v_mbcnt_hi_u32_b32 v223, -1, v223\n\t" \
    "s_getpc_b64 s[46:47]\n\t" \
    "rr_pc_%=:\n\t" \
    "s_lshl_b32 s69, s66, 13\n\t" \
    "v_lshl_add_u32 v222, v223, 7, s69\n\t" \
    "v_min_u32 v222, rr_last_%=-rr_pc_%=-4, v222\n\t" \
    "global_load_dword v222, v222, s[46:47]\n\t" \
    "v_lshlrev_b32 v223, 2, v223\n\t" \
    "v_add_u32 v223, s82, v223\n\t" \
    "ds_read_b32 v0, v223 offset:0\n\t" \
    "ds_read_b32 v1, v223 offset:256\n\t" \
    "ds_read_b32 v2, v223 offset:512\n\t" \
    "ds_read_b32 v3, v223 offset:768\n\t" \
    "ds_read_b32 v4, v223 offset:1024\n\t" \
    "ds_read_b32 v5, v223 offset:1280\n\t" \
    "ds_read_b32 v6, v223 offset:1536\n\t" \
    "ds_read_b32 v7, v223 offset:1792\n\t" \
    "ds_read_b32 v8, v223 offset:2048\n\t" \
    "ds_read_b32 v9, v223 offset:2304\n\t" \
    "ds_read_b32 v10, v223 offset:2560\n\t" \
    "ds_read_b32 v11, v223 offset:2816\n\t" \
    "s_waitcnt lgkmcnt(0)\n\t" \
    "ds_read_b32 v12, v223 offset:3072\n\t" \
    "ds_read_b32 v13, v223 offset:3328\n\t" \
    "ds_read_b32 v14, v223 offset:3584\n\t" \
    "ds_read_b32 v15, v223 offset:3840\n\t" \
    "ds_read_b32 v16, v223 offset:4096\n\t" \
    "ds_read_b32 v17, v223 offset:4352\n\t" \
    "ds_read_b32 v18, v223 offset:4608\n\t" \
    "ds_read_b32 v19, v223 offset:4864\n\t" \
    "ds_read_b32 v20, v223 offset:5120\n\t" \
    "ds_read_b32 v21, v223 offset:5376\n\t" \
    "ds_read_b32 v22, v223 offset:5632\n\t" \
    "ds_read_b32 v23, v223 offset:5888\n\t" \
    "s_waitcnt lgkmcnt(0)\n\t" \
    "ds_read_b32 v24, v223 offset:6144\n\t" \
    "ds_read_b32 v25, v223 offset:6400\n\t" \
    "ds_read_b32 v26, v223 offset:6656\n\t" \
    "ds_read_b32 v27, v223 offset:6912\n\t" \
    "ds_read_b32 v28, v223 offset:7168\n\t" \
    "ds_read_b32 v29, v223 offset:7424\n\t" \
    "ds_read_b32 v30, v223 offset:7680\n\t" \
    "ds_read_b32 v31, v223 offset:7936\n\t" \
    "ds_read_b32 v32, v223 offset:8192\n\t" \
    "ds_read_b32 v33, v223 offset:8448\n\t" \
    "ds_read_b32 v34, v223 offset:8704\n\t" \
    "ds_read_b32 v35, v223 offset:8960\n\t" \
    "s_waitcnt lgkmcnt(0)\n\t" \
    "ds_read_b32 v36, v223 offset:9216\n\t" \
    "ds_read_b32 v37, v223 offset:9472\n\t" \
    "ds_read_b32 v38, v223 offset:9728\n\t" \
    "ds_read_b32 v39, v223 offset:9984\n\t" \
    "ds_read_b32 v40, v223 offset:10240\n\t" \
    "ds_read_b32 v41, v223 offset:10496\n\t" \
    "ds_read_b32 v42, v223 offset:10752\n\t" \
    "ds_read_b32 v43, v223 offset:11008\n\t" \
    "ds_read_b32 v44, v223 offset:11264\n\t" \
    "ds_read_b32 v45, v223 offset:11520\n\t" \
    "ds_read_b32 v184, v223 offset:11776\n\t" \
    "ds_read_b32 v185, v223 offset:12032\n\t" \
    "s_waitcnt lgkmcnt(0)\n\t" \
    "ds_read_b32 v186, v223 offset:12288\n\t" \
    "ds_read_b32 v187, v223 offset:12544\n\t" \
    "ds_read_b32 v188, v223 offset:12800\n\t" \
    "s_waitcnt lgkmcnt(0)\n\t" \
    "s_mov_b32 s68, 262144\n\t" \
    "s_mov_b32 s72, 0\n\t" \
    "s_mov_b32 s83, -1\n\t" \
    "s_mov_b32 s65, 0\n\t" \
    "v_xor_b32 v180, 65536, v2\n\t" \
    "v_xor_b32 v181, 131072, v2\n\t" \
    "v_xor_b32 v182, 196608, v2\n\t" \
    "v_add_u32 v189, 4096, v7\n\t" \
    "v_add_u32 v190, 16384, v7\n\t" \
    "v_add_u32 v191, 20480, v7\n\t" \
    "s_mul_i32 s69, s67, 57\n\t" \
    "s_lshr_b32 s69, s69, 9\n\t" \
    "s_mul_i32 s69, s69, 9\n\t" \
    "s_sub_u32 s69, s67, s69\n\t" \
    "s_cmp_lt_u32 s69, 4\n\t" \
    "s_cselect_b32 s70, 21, 16\n\t" \
    "s_cmp_eq_u32 s69, 8\n\t" \
    "s_cselect_b32 s70, 26, s70\n\t" \
    "s_lshr_b32 s97, s91, s70\n\t" \
    "s_and_b32 s97, s97, 31\n\t" \
    "s_min_u32 s97, s97, 0\n\t" \
    "s_lshl_b32 s97, s97, 4\n\t" \
    "s_mov_b32 s77, 3019898880\n\t" \
    "s_mov_b32 s78, 3054198966\n\t" \
    "s_waitcnt lgkmcnt(0)\n\t" \
    "s_mov_b32 s70, 116\n\t" \
    "rr_E1_%=:\n\t" \
    "s_memtime s[56:57]\n\t" \
    "s_memrealtime s[2:3]\n\t" \
    "s_nop 0\n\t" \
    "s_memrealtime s[4:5]\n\t" \
    "s_nop 0\n\t" \
    "s_memrealtime s[6:7]\n\t" \
    "s_nop 0\n\t" \
    "s_memrealtime s[8:9]\n\t" \
    "s_nop 0\n\t" \
    "s_memrealtime s[10:11]\n\t" \
    "s_nop 0\n\t" \
    "s_memrealtime s[12:13]\n\t" \
    "s_nop 0\n\t" \
    "s_memrealtime s[14:15]\n\t" \
    "s_nop 0\n\t" \
    "s_memrealtime s[16:17]\n\t" \
    "s_nop 0\n\t" \
    "s_memrealtime s[18:19]\n\t" \
    "s_nop 0\n\t" \
    "s_memrealtime s[20:21]\n\t" \
    "s_nop 0\n\t" \
    "s_memrealtime s[22:23]\n\t" \
    "s_nop 0\n\t" \
    "s_memrealtime s[24:25]\n\t" \
    "s_nop 0\n\t" \
    "s_memrealtime s[26:27]\n\t" \
    "s_nop 0\n\t" \
    "s_memrealtime s[28:29]\n\t" \
    "s_memtime s[54:55]\n\t" \
    "s_add_u32 s70, s70, 8\n\t" \
    "s_waitcnt lgkmcnt(0)\n\t" \
    "s_sub_u32 s69, s54, s56\n\t" \
    "s_cmp_gt_u32 s69, s70\n\t" \
    "s_cbranch_scc1 rr_E1_%=\n\t" \
    "s_add_u32 s2, s2, s4\n\t" \
    "s_add_u32 s6, s6, s8\n\t" \
    "s_add_u32 s10, s10, s12\n\t" \
    "s_add_u32 s14, s14, s16\n\t" \
    "s_add_u32 s18, s18, s20\n\t" \
    "s_add_u32 s22, s22, s24\n\t" \
    "s_add_u32 s2, s2, s6\n\t" \
    "s_add_u32 s10, s10, s14\n\t" \
    "s_add_u32 s18, s18, s22\n\t" \
    "s_add_u32 s2, s2, s26\n\t" \
    "s_add_u32 s10, s10, s18\n\t" \
    "s_add_u32 s2, s2, s10\n\t" \
    "s_mul_i32 s70, s28, 13\n\t" \
    "s_sub_u32 s70, s70, s2\n\t" \
    "s_min_u32 s70, s70, 52\n\t" \
    "s_mul_i32 s69, s69, s70\n\t" \
    "s_mul_hi_u32 s69, s69, 76695844\n\t" \
    "s_mov_b32 s75, s73\n\t" \
    "s_mov_b32 s76, s74\n\t" \
    "s_add_u32 s73, s56, s69\n\t" \
    "s_lshl_b32 s74, s28, 4\n\t" \
    "v_mov_b32 v172, 0xd800\n\t" \
    "v_mov_b32 v173, 0x20d800\n\t" \
    "global_load_dword v172, v172, s[36:37]\n\t" \
    "global_load_dword v173, v173, s[36:37]\n\t" \
    "s_mov_b32 s62, 0\n\t" \
    "s_mov_b32 s63, 0x200000\n\t" \
    "s_mov_b64 s[44:45], 0\n\t" \
    "v_mov_b32 v170, 0\n\t" \
    "s_mov_b32 s65, s66\n\t" \
    "s_lshl_b32 s58, s67, 14\n\t" \
    "s_sub_u32 s80, s74, 32000\n\t" \
    "s_mov_b32 s64, 1\n\t" \
    "s_mov_b32 s65, 5\n\t" \
    "s_mov_b32 s83, 99\n\t" \
    "s_add_u32 s70, s65, 59\n\t" \
    "s_mul_i32 s69, s70, 171\n\t" \
    "s_lshr_b32 s69, s69, 9\n\t" \
    "s_mul_i32 s70, s70, 589824\n\t" \
    "s_mul_i32 s71, s69, 327680\n\t" \
    "s_add_u32 s70, s70, s71\n\t" \
    "s_lshl1_add_u32 s69, s69, 4\n\t" \
    "s_bitcmp1_b64 s[44:45], s69\n\t" \
    "s_cselect_b32 s69, 8192, 0\n\t" \
    "s_add_u32 s69, s69, s70\n\t" \
    "s_add_u32 s69, s69, 123731968\n\t" \
    "s_add_u32 s69, s69, s58\n\t" \
    "s_add_u32 s40, s36, s69\n\t" \
    "s_addc_u32 s41, s37, 0\n\t" \
    "v_mov_b32 v192, 0\n\t" \
    "s_mov_b64 exec, 1\n\t" \
    "s_branch rr_act3_%=\n\t" \
    "rr_g214_%=:\n\t" \
    "s_mov_b64 exec, -1\n\t" \
    "s_waitcnt vmcnt(0)\n\t" \
    "v_mbcnt_lo_u32_b32 v164, -1, 0\n\t" \
    "v_mbcnt_hi_u32_b32 v164, -1, v164\n\t" \
    "v_lshlrev_b32 v164, 6, v164\n\t" \
    "v_add_u32 v164, 6225920, v164\n\t" \
    "v_mov_b32 v165, 6234112\n\t" \
    "global_load_dword v165, v165, s[36:37] sc1\n\t" \
    "global_load_dwordx2 v[166:167], v164, s[36:37] sc1\n\t" \
    "global_load_dword v168, v164, s[36:37] offset:2048 sc1\n\t" \
    "s_waitcnt vmcnt(0)\n\t" \
    "rr_C15_%=:\n\t" \
    "v_cmp_ne_u32 s[54:55], v166, v168\n\t" \
    "v_sub_u32 v166, v166, v165\n\t" \
    "v_cmp_ne_u32 vcc, 1, v166\n\t" \
    "s_nop 1\n\t" \
    "s_or_b64 vcc, vcc, s[54:55]\n\t" \
    "s_cbranch_scc0 rr_Cok16_%=\n\t" \
    "s_cmp_eq_u32 s68, 0\n\t" \
    "s_cbranch_scc1 rr_F18_%=\n\t" \
    "s_sub_u32 s68, s68, 1\n\t" \
    "s_sleep 6\n\t" \
    "s_branch rr_Cag17_%=\n\t" \
    "rr_F18_%=:\n\t" \
    "v_mov_b32 v168, 5767424\n\t" \
    "v_mov_b32 v169, 4\n\t" \
    "global_store_dword v168, v169, s[36:37] sc1\n\t" \
    "s_waitcnt vmcnt(0)\n\t" \
    "s_branch rr_Cok16_%=\n\t" \
    "rr_Cag17_%=:\n\t" \
    "global_load_dwordx2 v[166:167], v164, s[36:37] sc1\n\t" \
    "global_load_dword v168, v164, s[36:37] offset:2048 sc1\n\t" \
    "s_waitcnt vmcnt(0)\n\t" \
    "s_branch rr_C15_%=\n\t" \
    "rr_Cok16_%=:\n\t" \
    "s_cmp_lg_u32 s66, 0\n\t" \
    "s_cbranch_scc1 rr_Ca19_%=\n\t" \
    "v_mov_b32 v164, 5836800\n\t" \
    "s_mov_b64 exec, 1\n\t" \
    "global_atomic_add v164, v166, s[36:37] sc1\n\t" \
    "s_mov_b64 exec, -1\n\t" \
    "rr_Ca19_%=:\n\t" \
    "v_mov_b32_dpp v164, v167 quad_perm:[1,0,3,2] row_mask:0xf bank_mask:0xf\n\t" \
    "v_cmp_lt_u32 s[44:45], v164, v167\n\t" \
    "s_nop 1\n\t" \
    "s_bitcmp1_b32 s44, 0\n\t" \
    "s_cselect_b32 s62, 8192, 0\n\t" \
    "s_bitcmp1_b32 s44, 2\n\t" \
    "s_cselect_b32 s63, 8192, 0\n\t" \
    "s_or_b32 s63, s63, 0x200000\n\t" \
    "v_min_u32 v164, v164, v167\n\t" \
    "v_cmp_lt_u32 vcc, 660, v164\n\t" \
    "s_nop 1\n\t" \
    "s_cmp_lg_u64 vcc, 0\n\t" \
    "s_cselect_b32 s69, 1, 0\n\t" \
    "s_mul_i32 s70, s69, 160\n\t" \
    "s_add_u32 s86, s86, s70\n\t" \
    "s_mul_i32 s70, s69, 536\n\t" \
    "s_add_u32 s87, s87, s70\n\t" \
    "s_mul_i32 s70, s69, 256\n\t" \
    "s_add_u32 s88, s88, s70\n\t" \
    "v_mov_b32 v172, -1\n\t" \
    "v_mov_b32 v173, -1\n\t" \
    "v_mov_b32 v174, -1\n\t" \
    "v_mov_b32 v175, -1\n\t" \
    "v_mov_b32 v176, 32000\n\t" \
    "s_sub_u32 s69, s63, s62\n\t" \
    "s_sub_u32 s69, s69, 0x200000\n\t" \
    "v_bfe_u32 v164, v187, 21, 1\n\t" \
    "v_mad_i32_i24 v187, v164, s69, v187\n\t" \
    "v_bfe_u32 v164, v188, 21, 1\n\t" \
    "v_mad_i32_i24 v188, v164, s69, v188\n\t" \
    "s_mov_b32 s64, 0\n\t" \
    "s_mov_b32 s65, s66\n\t" \
    "s_mov_b32 s83, 1\n\t" \
    "s_mov_b32 s77, 3154116608\n\t" \
    "rr_pf9_%=:\n\t" \
    "s_cmp_gt_u32 s65, 7\n\t" \
    "s_cselect_b32 s83, 99, s83\n\t" \
    "rr_WF20_%=:\n\t" \
    "s_cmp_ge_i32 s83, s65\n\t" \
    "s_cbranch_scc1 rr_WFok21_%=\n\t" \
    "global_load_dword v164, v6, s[36:37] sc1\n\t" \
    "s_waitcnt vmcnt(0)\n\t" \
    "v_min_i32_dpp v164, v164, v164 row_ror:1 row_mask:0xf bank_mask:0xf\n\t" \
    "s_nop 1\n\t" \
    "v_min_i32_dpp v164, v164, v164 row_ror:2 row_mask:0xf bank_mask:0xf\n\t" \
    "s_nop 1\n\t" \
    "v_min_i32_dpp v164, v164, v164 row_ror:4 row_mask:0xf bank_mask:0xf\n\t" \
    "s_nop 1\n\t" \
    "v_min_i32_dpp v164, v164, v164 row_ror:8 row_mask:0xf bank_mask:0xf\n\t" \
    "s_nop 0\n\t" \
    "v_readlane_b32 s69, v164, 0\n\t" \
    "v_readlane_b32 s70, v164, 16\n\t" \
    "s_nop 1\n\t" \
    "s_min_i32 s83, s69, s70\n\t" \
    "s_cmp_ge_i32 s83, s65\n\t" \
    "s_cbranch_scc1 rr_WFok21_%=\n\t" \
    "s_cmp_eq_u32 s68, 0\n\t" \
    "s_cbranch_scc1 rr_F22_%=\n\t" \
    "s_sub_u32 s68, s68, 1\n\t" \
    "s_sleep 2\n\t" \
    "s_branch rr_WF20_%=\n\t" \
    "rr_F22_%=:\n\t" \
    "v_mov_b32 v164, 5767424\n\t" \
    "v_mov_b32 v165, 10\n\t" \
    "global_store_dword v164, v165, s[36:37] sc1\n\t" \
    "s_waitcnt vmcnt(0)\n\t" \
    "s_branch rr_WFok21_%=\n\t" \
    "rr_WFok21_%=:\n\t" \
    "s_mul_i32 s70, s67, 7\n\t" \
    "s_add_u32 s69, s65, 0\n\t" \
    "s_xor_b32 s69, s69, s70\n\t" \
    "s_and_b32 s69, s69, 63\n\t" \
    "s_lshl_b32 s69, s69, 15\n\t" \
    "s_mul_i32 s70, s67, 2097152\n\t" \
    "s_add_u32 s69, s69, s70\n\t" \
    "s_add_u32 s69, s69, 48234496\n\t" \
    "s_add_u32 s38, s36, s69\n\t" \
    "s_addc_u32 s39, s37, 0\n\t" \
    "s_add_u32 s70, s65, 0\n\t" \
    "s_mul_i32 s69, s70, 171\n\t" \
    "s_lshr_b32 s69, s69, 9\n\t" \
    "s_mul_i32 s70, s70, 589824\n\t" \
    "s_mul_i32 s71, s69, 327680\n\t" \
    "s_add_u32 s70, s70, s71\n\t" \
    "s_lshl1_add_u32 s69, s69, 4\n\t" \
    "s_bitcmp1_b64 s[44:45], s69\n\t" \
    "s_cselect_b32 s69, 8192, 0\n\t" \
    "s_add_u32 s69, s69, s70\n\t" \
    "s_add_u32 s69, s69, 123731968\n\t" \
    "s_add_u32 s69, s69, s58\n\t" \
    "s_add_u32 s40, s36, s69\n\t" \
    "s_addc_u32 s41, s37, 0\n\t" \
    "s_and_b32 s69, s69, 0xffe00000\n\t" \
    "s_add_u32 s69, s69, 1823744\n\t" \
    "v_mov_b32 v168, s69\n\t" \
    "global_load_dword v168, v168, s[36:37]\n\t" \
    "v_mov_b32 v164, s83\n\t" \
    "s_add_u32 s69, s65, 2\n\t" \
    "s_cmp_ge_i32 s83, s69\n\t" \
    "s_cbranch_scc1 rr_nq23_%=\n\t" \
    "global_load_dword v164, v6, s[36:37] sc1\n\t" \
    "rr_nq23_%=:\n\t" \
    "s_cmp_gt_u32 s65, 61\n\t" \
    "s_cbranch_scc1 rr_idle2_%=\n\t" \
    "s_add_u32 s69, s65, 2\n\t" \
    "s_and_b32 s69, s69, 7\n\t" \
    "s_lshl_b32 s69, s69, 18\n\t" \
    "s_add_u32 s69, s69, s62\n\t" \
    "s_add_u32 s46, s36, s69\n\t" \
    "s_addc_u32 s47, s37, 0\n\t" \
    "global_store_dwordx4 v184, v[172:175], s[46:47] sc1\n\t" \
    "global_store_dwordx4 v185, v[172:175], s[46:47] sc1\n\t" \
    "global_store_dwordx4 v186, v[172:175], s[46:47] sc1\n\t" \
    "global_store_dwordx4 v187, v[172:175], s[46:47] sc1\n\t" \
    "global_store_dwordx4 v188, v[172:175], s[46:47] sc1\n\t" \
    "rr_idle2_%=:\n\t" \
    "v_mov_b32 v200, 0\n\t" \
    "s_cmp_lg_u32 s67, 35\n\t" \
    "s_cbranch_scc1 rr_cke24_%=\n\t" \
    "s_cmp_lt_u32 s65, 6\n\t" \
    "s_cbranch_scc1 rr_cke24_%=\n\t" \
    "s_lshl_b32 s69, s65, 7\n\t" \
    "s_add_u32 s69, s69, 5853184\n\t" \
    "v_mov_b32 v201, s69\n\t" \
    "global_load_dword v200, v201, s[36:37] sc1\n\t" \
    "rr_cke24_%=:\n\t" \
    "s_cmp_gt_u32 s65, 62\n\t" \
    "s_cbranch_scc1 rr_n225_%=\n\t" \
    "s_cmp_lt_u32 s65, 2\n\t" \
    "s_cbranch_scc1 rr_nw226_%=\n\t" \
    "s_mul_hi_u32 s69, s85, s78\n\t" \
    "s_add_u32 s79, s81, s69\n\t" \
    "s_add_u32 s79, s79, s87\n\t" \
    "s_cmp_ge_u32 s65, 54\n\t" \
    "s_cselect_b32 s69, 32, 0\n\t" \
    "s_sub_u32 s79, s79, s69\n\t" \
    "s_cmp_lt_u32 s65, 11\n\t" \
    "s_cselect_b32 s69, 48, 0\n\t" \
    "s_add_u32 s79, s79, s69\n\t" \
    "s_add_u32 s80, s79, s88\n\t" \
    "s_cmp_gt_u32 s65, 8\n\t" \
    "s_cselect_b32 s71, 240, 64\n\t" \
    "s_add_u32 s71, s71, s79\n\t" \
    "s_sub_u32 s70, s71, s74\n\t" \
    "s_max_i32 s70, s70, 0\n\t" \
    "s_min_i32 s70, s70, 16000\n\t" \
    "s_lshl_b32 s70, s70, 1\n\t" \
    "s_mul_hi_u32 s70, s70, s77\n\t" \
    "s_add_u32 s70, s70, s73\n\t" \
    "rr_W27_%=:\n\t" \
    "s_memtime s[56:57]\n\t" \
    "s_waitcnt lgkmcnt(0)\n\t" \
    "s_sub_u32 s69, s70, s56\n\t" \
    "s_cmp_gt_i32 s69, 24\n\t" \
    "s_cbranch_scc1 rr_W27_%=\n\t" \
    "s_branch rr_g113_%=\n\t" \
    "rr_nw226_%=:\n\t" \
    "s_mov_b32 s96, 0xaaaaaaaa\n\t" \
    "v_mov_b32 v166, -1\n\t" \
    "v_readfirstlane_b32 s71, v7\n\t" \
    "s_nop 1\n\t" \
    "s_lshr_b32 s71, s71, 4\n\t" \
    "s_add_u32 s71, s71, 5865472\n\t" \
    "rr_g012_%=:\n\t" \
    "s_memrealtime s[54:55]\n\t" \
    "s_waitcnt lgkmcnt(0)\n\t" \
    "s_bfe_u32 s69, s54, 0x60004\n\t" \
    "s_lshl_b32 s69, s69, 6\n\t" \
    "s_add_u32 s69, s69, s71\n\t" \
    "v_mov_b32 v165, s69\n\t" \
    "global_load_dword v169, v165, s[36:37] sc1\n\t" \
    "s_and_b32 s69, s68, 63\n\t" \
    "s_cbranch_scc1 rr_gf28_%=\n\t" \
    "global_load_dword v166, v6, s[36:37] sc1\n\t" \
    "rr_gf28_%=:\n\t" \
    "s_waitcnt vmcnt(0)\n\t" \
    "v_cmp_ne_u32 s[30:31], -1, v169\n\t" \
    "v_cmp_le_i32 vcc, s65, v166\n\t" \
    "s_nop 1\n\t" \
    "s_lshr_b32 s69, vcc_lo, 1\n\t" \
    "s_and_b32 s69, s69, vcc_lo\n\t" \
    "s_or_b32 s69, s69, s30\n\t" \
    "s_andn2_b32 s69, s69, s96\n\t" \
    "s_cbranch_scc0 rr_gs29_%=\n\t" \
    "s_or_b32 s96, s96, s69\n\t" \
    "s_cmp_eq_u32 s66, 0\n\t" \
    "s_cbranch_scc1 rr_w030_%=\n\t" \
    "s_sleep 8\n\t" \
    "rr_w030_%=:\n\t" \
    "s_bitcmp0_b32 s69, 0\n\t" \
    "s_cbranch_scc1 rr_gk31_%=\n\t" \
    "global_load_dwordx4 a[0:3], v26, s[38:39] offset:0 sc1\n\t" \
    "global_load_dwordx4 a[4:7], v26, s[38:39] offset:1024 sc1\n\t" \
    "rr_gk31_%=:\n\t" \
    "s_bitcmp0_b32 s69, 2\n\t" \
    "s_cbranch_scc1 rr_gk32_%=\n\t" \
    "global_load_dwordx4 a[8:11], v27, s[38:39] offset:0 sc1\n\t" \
    "global_load_dwordx4 a[12:15], v27, s[38:39] offset:1024 sc1\n\t" \
    "rr_gk32_%=:\n\t" \
    "s_bitcmp0_b32 s69, 4\n\t" \
    "s_cbranch_scc1 rr_gk33_%=\n\t" \
    "global_load_dwordx4 a[16:19], v28, s[38:39] offset:0 sc1\n\t" \
    "global_load_dwordx4 a[20:23], v28, s[38:39] offset:1024 sc1\n\t" \
    "rr_gk33_%=:\n\t" \
    "s_bitcmp0_b32 s69, 6\n\t" \
    "s_cbranch_scc1 rr_gk34_%=\n\t" \
    "global_load_dwordx4 a[24:27], v29, s[38:39] offset:0 sc1\n\t" \
    "global_load_dwordx4 a[28:31], v29, s[38:39] offset:1024 sc1\n\t" \
    "rr_gk34_%=:\n\t" \
    "s_bitcmp0_b32 s69, 8\n\t" \
    "s_cbranch_scc1 rr_gk35_%=\n\t" \
    "global_load_dwordx4 a[32:35], v30, s[38:39] offset:0 sc1\n\t" \
    "global_load_dwordx4 a[36:39], v30, s[38:39] offset:1024 sc1\n\t" \
    "rr_gk35_%=:\n\t" \
    "s_bitcmp0_b32 s69, 10\n\t" \
    "s_cbranch_scc1 rr_gk36_%=\n\t" \
    "global_load_dwordx4 a[40:43], v31, s[38:39] offset:0 sc1\n\t" \
    "global_load_dwordx4 a[44:47], v31, s[38:39] offset:1024 sc1\n\t" \
    "rr_gk36_%=:\n\t" \
    "s_bitcmp0_b32 s69, 12\n\t" \
    "s_cbranch_scc1 rr_gk37_%=\n\t" \
    "global_load_dwordx4 a[48:51], v32, s[38:39] offset:0 sc1\n\t" \
    "global_load_dwordx4 a[52:55], v32, s[38:39] offset:1024 sc1\n\t" \
    "rr_gk37_%=:\n\t" \
    "s_bitcmp0_b32 s69, 14\n\t" \
    "s_cbranch_scc1 rr_gk38_%=\n\t" \
    "global_load_dwordx4 a[56:59], v33, s[38:39] offset:0 sc1\n\t" \
    "global_load_dwordx4 a[60:63], v33, s[38:39] offset:1024 sc1\n\t" \
    "rr_gk38_%=:\n\t" \
    "s_bitcmp0_b32 s69, 16\n\t" \
    "s_cbranch_scc1 rr_gk39_%=\n\t" \
    "global_load_dwordx4 a[64:67], v34, s[38:39] offset:0 sc1\n\t" \
    "global_load_dwordx4 a[68:71], v34, s[38:39] offset:1024 sc1\n\t" \
    "rr_gk39_%=:\n\t" \
    "s_bitcmp0_b32 s69, 18\n\t" \
    "s_cbranch_scc1 rr_gk40_%=\n\t" \
    "global_load_dwordx4 a[72:75], v35, s[38:39] offset:0 sc1\n\t" \
    "global_load_dwordx4 a[76:79], v35, s[38:39] offset:1024 sc1\n\t" \
    "rr_gk40_%=:\n\t" \
    "s_bitcmp0_b32 s69, 20\n\t" \
    "s_cbranch_scc1 rr_gk41_%=\n\t" \
    "global_load_dwordx4 a[80:83], v36, s[38:39] offset:0 sc1\n\t" \
    "global_load_dwordx4 a[84:87], v36, s[38:39] offset:1024 sc1\n\t" \
    "rr_gk41_%=:\n\t" \
    "s_bitcmp0_b32 s69, 22\n\t" \
    "s_cbranch_scc1 rr_gk42_%=\n\t" \
    "global_load_dwordx4 a[88:91], v37, s[38:39] offset:0 sc1\n\t" \
    "global_load_dwordx4 a[92:95], v37, s[38:39] offset:1024 sc1\n\t" \
    "rr_gk42_%=:\n\t" \
    "s_bitcmp0_b32 s69, 24\n\t" \
    "s_cbranch_scc1 rr_gk43_%=\n\t" \
    "global_load_dwordx4 a[96:99], v38, s[38:39] offset:0 sc1\n\t" \
    "global_load_dwordx4 a[100:103], v38, s[38:39] offset:1024 sc1\n\t" \
    "rr_gk43_%=:\n\t" \
    "s_bitcmp0_b32 s69, 26\n\t" \
    "s_cbranch_scc1 rr_gk44_%=\n\t" \
    "global_load_dwordx4 a[104:107], v39, s[38:39] offset:0 sc1\n\t" \
    "global_load_dwordx4 a[108:111], v39, s[38:39] offset:1024 sc1\n\t" \
    "rr_gk44_%=:\n\t" \
    "s_bitcmp0_b32 s69, 28\n\t" \
    "s_cbranch_scc1 rr_gk45_%=\n\t" \
    "global_load_dwordx4 a[112:115], v40, s[38:39] offset:0 sc1\n\t" \
    "global_load_dwordx4 a[116:119], v40, s[38:39] offset:1024 sc1\n\t" \
    "rr_gk45_%=:\n\t" \
    "s_bitcmp0_b32 s69, 30\n\t" \
    "s_cbranch_scc1 rr_gk46_%=\n\t" \
    "global_load_dwordx4 a[120:123], v41, s[38:39] offset:0 sc1\n\t" \
    "global_load_dwordx4 a[124:127], v41, s[38:39] offset:1024 sc1\n\t" \
    "rr_gk46_%=:\n\t" \
    "s_cmp_eq_u32 s96, -1\n\t" \
    "s_cbranch_scc1 rr_n225_%=\n\t" \
    "rr_gs29_%=:\n\t" \
    "s_cmp_eq_u32 s68, 0\n\t" \
    "s_cbranch_scc1 rr_F47_%=\n\t" \
    "s_sub_u32 s68, s68, 1\n\t" \
    "s_sleep 1\n\t" \
    "s_branch rr_g012_%=\n\t" \
    "rr_F47_%=:\n\t" \
    "v_mov_b32 v167, 5767424\n\t" \
    "v_mov_b32 v168, 16\n\t" \
    "global_store_dword v167, v168, s[36:37] sc1\n\t" \
    "s_waitcnt vmcnt(0)\n\t" \
    "s_branch rr_n225_%=\n\t" \
    "rr_g113_%=:\n\t" \
    "global_load_dwordx4 a[0:3], v26, s[38:39] offset:0 sc1\n\t" \
    "global_load_dwordx4 a[4:7], v26, s[38:39] offset:1024 sc1\n\t" \
    "global_load_dwordx4 a[8:11], v27, s[38:39] offset:0 sc1\n\t" \
    "global_load_dwordx4 a[12:15], v27, s[38:39] offset:1024 sc1\n\t" \
    "global_load_dwordx4 a[16:19], v28, s[38:39] offset:0 sc1\n\t" \
    "global_load_dwordx4 a[20:23], v28, s[38:39] offset:1024 sc1\n\t" \
    "global_load_dwordx4 a[24:27], v29, s[38:39] offset:0 sc1\n\t" \
    "global_load_dwordx4 a[28:31], v29, s[38:39] offset:1024 sc1\n\t" \
    "global_load_dwordx4 a[32:35], v30, s[38:39] offset:0 sc1\n\t" \
    "global_load_dwordx4 a[36:39], v30, s[38:39] offset:1024 sc1\n\t" \
    "global_load_dwordx4 a[40:43], v31, s[38:39] offset:0 sc1\n\t" \
    "global_load_dwordx4 a[44:47], v31, s[38:39] offset:1024 sc1\n\t" \
    "global_load_dwordx4 a[48:51], v32, s[38:39] offset:0 sc1\n\t" \
    "global_load_dwordx4 a[52:55], v32, s[38:39] offset:1024 sc1\n\t" \
    "global_load_dwordx4 a[56:59], v33, s[38:39] offset:0 sc1\n\t" \
    "global_load_dwordx4 a[60:63], v33, s[38:39] offset:1024 sc1\n\t" \
    "global_load_dwordx4 a[64:67], v34, s[38:39] offset:0 sc1\n\t" \
    "global_load_dwordx4 a[68:71], v34, s[38:39] offset:1024 sc1\n\t" \
    "global_load_dwordx4 a[72:75], v35, s[38:39] offset:0 sc1\n\t" \
    "global_load_dwordx4 a[76:79], v35, s[38:39] offset:1024 sc1\n\t" \
    "global_load_dwordx4 a[80:83], v36, s[38:39] offset:0 sc1\n\t" \
    "global_load_dwordx4 a[84:87], v36, s[38:39] offset:1024 sc1\n\t" \
    "global_load_dwordx4 a[88:91], v37, s[38:39] offset:0 sc1\n\t" \
    "global_load_dwordx4 a[92:95], v37, s[38:39] offset:1024 sc1\n\t" \
    "global_load_dwordx4 a[96:99], v38, s[38:39] offset:0 sc1\n\t" \
    "global_load_dwordx4 a[100:103], v38, s[38:39] offset:1024 sc1\n\t" \
    "global_load_dwordx4 a[104:107], v39, s[38:39] offset:0 sc1\n\t" \
    "global_load_dwordx4 a[108:111], v39, s[38:39] offset:1024 sc1\n\t" \
    "global_load_dwordx4 a[112:115], v40, s[38:39] offset:0 sc1\n\t" \
    "global_load_dwordx4 a[116:119], v40, s[38:39] offset:1024 sc1\n\t" \
    "global_load_dwordx4 a[120:123], v41, s[38:39] offset:0 sc1\n\t" \
    "global_load_dwordx4 a[124:127], v41, s[38:39] offset:1024 sc1\n\t" \
    "rr_n225_%=:\n\t" \
    "s_cmp_eq_u32 s65, 0\n\t" \
    "s_cbranch_scc1 rr_act3_%=\n\t" \
    "s_waitcnt vmcnt(0)\n\t" \
    "rr_ckl49_%=:\n\t" \
    "v_cmp_eq_u32 vcc, -1, v200\n\t" \
    "s_cbranch_vccz rr_ck48_%=\n\t" \
    "global_load_dword v200, v201, s[36:37] sc1\n\t" \
    "s_waitcnt vmcnt(0)\n\t" \
    "s_cmp_eq_u32 s68, 0\n\t" \
    "s_cbranch_scc1 rr_F50_%=\n\t" \
    "s_sub_u32 s68, s68, 1\n\t" \
    "s_sleep 1\n\t" \
    "s_branch rr_ckl49_%=\n\t" \
    "rr_F50_%=:\n\t" \
    "v_mov_b32 v166, 5767424\n\t" \
    "v_mov_b32 v167, 15\n\t" \
    "global_store_dword v166, v167, s[36:37] sc1\n\t" \
    "s_waitcnt vmcnt(0)\n\t" \
    "s_branch rr_ck48_%=\n\t" \
    "rr_ck48_%=:\n\t" \
    "v_min_i32_dpp v164, v164, v164 row_ror:1 row_mask:0xf bank_mask:0xf\n\t" \
    "s_nop 1\n\t" \
    "v_min_i32_dpp v164, v164, v164 row_ror:2 row_mask:0xf bank_mask:0xf\n\t" \
    "s_nop 1\n\t" \
    "v_min_i32_dpp v164, v164, v164 row_ror:4 row_mask:0xf bank_mask:0xf\n\t" \
    "s_nop 1\n\t" \
    "v_min_i32_dpp v164, v164, v164 row_ror:8 row_mask:0xf bank_mask:0xf\n\t" \
    "s_nop 0\n\t" \
    "v_readlane_b32 s69, v164, 0\n\t" \
    "v_readlane_b32 s70, v164, 16\n\t" \
    "s_nop 1\n\t" \
    "s_min_i32 s69, s69, s70\n\t" \
    "s_max_i32 s83, s83, s69\n\t" \
    "s_cmp_eq_u32 s65, 0\n\t" \
    "s_cbranch_scc1 rr_act3_%=\n\t" \
    "s_barrier\n\t" \
    "s_cmp_gt_u32 s65, 62\n\t" \
    "s_cbranch_scc1 rr_end4_%=\n\t" \
    "ds_read_b32 v164, v9\n\t" \
    "s_waitcnt lgkmcnt(0)\n\t" \
    "v_readfirstlane_b32 s81, v164\n\t" \
    "s_nop 1\n\t" \
    "s_cmp_lg_u32 s65, 1\n\t" \
    "s_cbranch_scc1 rr_n151_%=\n\t" \
    "s_waitcnt lgkmcnt(0)\n\t" \
    "s_mov_b32 s70, 116\n\t" \
    "rr_E52_%=:\n\t" \
    "s_memtime s[56:57]\n\t" \
    "s_memrealtime s[2:3]\n\t" \
    "s_nop 0\n\t" \
    "s_memrealtime s[4:5]\n\t" \
    "s_nop 0\n\t" \
    "s_memrealtime s[6:7]\n\t" \
    "s_nop 0\n\t" \
    "s_memrealtime s[8:9]\n\t" \
    "s_nop 0\n\t" \
    "s_memrealtime s[10:11]\n\t" \
    "s_nop 0\n\t" \
    "s_memrealtime s[12:13]\n\t" \
    "s_nop 0\n\t" \
    "s_memrealtime s[14:15]\n\t" \
    "s_nop 0\n\t" \
    "s_memrealtime s[16:17]\n\t" \
    "s_nop 0\n\t" \
    "s_memrealtime s[18:19]\n\t" \
    "s_nop 0\n\t" \
    "s_memrealtime s[20:21]\n\t" \
    "s_nop 0\n\t" \
    "s_memrealtime s[22:23]\n\t" \
    "s_nop 0\n\t" \
    "s_memrealtime s[24:25]\n\t" \
    "s_nop 0\n\t" \
    "s_memrealtime s[26:27]\n\t" \
    "s_nop 0\n\t" \
    "s_memrealtime s[28:29]\n\t" \
    "s_memtime s[54:55]\n\t" \
    "s_add_u32 s70, s70, 8\n\t" \
    "s_waitcnt lgkmcnt(0)\n\t" \
    "s_sub_u32 s69, s54, s56\n\t" \
    "s_cmp_gt_u32 s69, s70\n\t" \
    "s_cbranch_scc1 rr_E52_%=\n\t" \
    "s_add_u32 s2, s2, s4\n\t" \
    "s_add_u32 s6, s6, s8\n\t" \
    "s_add_u32 s10, s10, s12\n\t" \
    "s_add_u32 s14, s14, s16\n\t" \
    "s_add_u32 s18, s18, s20\n\t" \
    "s_add_u32 s22, s22, s24\n\t" \
    "s_add_u32 s2, s2, s6\n\t" \
    "s_add_u32 s10, s10, s14\n\t" \
    "s_add_u32 s18, s18, s22\n\t" \
    "s_add_u32 s2, s2, s26\n\t" \
    "s_add_u32 s10, s10, s18\n\t" \
    "s_add_u32 s2, s2, s10\n\t" \
    "s_mul_i32 s70, s28, 13\n\t" \
    "s_sub_u32 s70, s70, s2\n\t" \
    "s_min_u32 s70, s70, 52\n\t" \
    "s_mul_i32 s69, s69, s70\n\t" \
    "s_mul_hi_u32 s69, s69, 76695844\n\t" \
    "s_mov_b32 s75, s73\n\t" \
    "s_mov_b32 s76, s74\n\t" \
    "s_add_u32 s73, s56, s69\n\t" \
    "s_lshl_b32 s74, s28, 4\n\t" \
    "rr_n151_%=:\n\t" \
    "s_mul_hi_u32 s69, s85, s78\n\t" \
    "s_add_u32 s79, s81, s69\n\t" \
    "s_add_u32 s79, s79, s87\n\t" \
    "s_cmp_ge_u32 s65, 53\n\t" \
    "s_cselect_b32 s69, 32, 0\n\t" \
    "s_sub_u32 s79, s79, s69\n\t" \
    "s_cmp_lt_u32 s65, 10\n\t" \
    "s_cselect_b32 s69, 48, 0\n\t" \
    "s_add_u32 s79, s79, s69\n\t" \
    "s_add_u32 s80, s79, s88\n\t" \
    "s_sub_u32 s70, s79, s74\n\t" \
    "s_max_i32 s70, s70, 0\n\t" \
    "s_min_i32 s70, s70, 16000\n\t" \
    "s_lshl_b32 s70, s70, 1\n\t" \
    "s_mul_hi_u32 s70, s70, s77\n\t" \
    "s_add_u32 s70, s70, s73\n\t" \
    "rr_W53_%=:\n\t" \
    "s_memtime s[56:57]\n\t" \
    "s_waitcnt lgkmcnt(0)\n\t" \
    "s_sub_u32 s69, s70, s56\n\t" \
    "s_cmp_gt_i32 s69, 24\n\t" \
    "s_cbranch_scc1 rr_W53_%=\n\t" \
    "rr_act3_%=:\n\t" \
    "s_add_u32 s69, s65, 0\n\t" \
    "s_and_b32 s69, s69, 7\n\t" \
    "s_lshl_b32 s69, s69, 18\n\t" \
    "s_add_u32 s69, s69, s62\n\t" \
    "s_add_u32 s46, s36, s69\n\t" \
    "s_addc_u32 s47, s37, 0\n\t" \
    "s_add_u32 s69, s65, 1\n\t" \
    "s_and_b32 s69, s69, 7\n\t" \
    "s_lshl_b32 s69, s69, 18\n\t" \
    "s_add_u32 s69, s69, s62\n\t" \
    "s_add_u32 s48, s36, s69\n\t" \
    "s_addc_u32 s49, s37, 0\n\t" \
    "s_and_b32 s69, s65, 7\n\t" \
    "s_lshl_b32 s69, s69, 18\n\t" \
    "s_add_u32 s69, s69, s63\n\t" \
    "s_add_u32 s52, s36, s69\n\t" \
    "s_addc_u32 s53, s37, 0\n\t" \
    "s_cmp_eq_u32 s65, 0\n\t" \
    "s_cbranch_scc1 rr_nf54_%=\n\t" \
    "s_add_u32 s69, s65, 2\n\t" \
    "s_cmp_ge_i32 s83, s69\n\t" \
    "s_cbranch_scc1 rr_nf54_%=\n\t" \
    "global_load_dword v164, v6, s[36:37] sc1\n\t" \
    "s_waitcnt vmcnt(0)\n\t" \
    "v_min_i32_dpp v164, v164, v164 row_ror:1 row_mask:0xf bank_mask:0xf\n\t" \
    "s_nop 1\n\t" \
    "v_min_i32_dpp v164, v164, v164 row_ror:2 row_mask:0xf bank_mask:0xf\n\t" \
    "s_nop 1\n\t" \
    "v_min_i32_dpp v164, v164, v164 row_ror:4 row_mask:0xf bank_mask:0xf\n\t" \
    "s_nop 1\n\t" \
    "v_min_i32_dpp v164, v164, v164 row_ror:8 row_mask:0xf bank_mask:0xf\n\t" \
    "s_nop 0\n\t" \
    "v_readlane_b32 s69, v164, 0\n\t" \
    "v_readlane_b32 s70, v164, 16\n\t" \
    "s_nop 1\n\t" \
    "s_min_i32 s83, s69, s70\n\t" \
    "rr_nf54_%=:\n\t" \
    "s_waitcnt lgkmcnt(0)\n\t" \
    "s_mov_b32 s70, 116\n\t" \
    "rr_E55_%=:\n\t" \
    "s_memtime s[56:57]\n\t" \
    "s_memrealtime s[2:3]\n\t" \
    "s_nop 0\n\t" \
    "s_memrealtime s[4:5]\n\t" \
    "s_nop 0\n\t" \
    "s_memrealtime s[6:7]\n\t" \
    "s_nop 0\n\t" \
    "s_memrealtime s[8:9]\n\t" \
    "s_nop 0\n\t" \
    "s_memrealtime s[10:11]\n\t" \
    "s_nop 0\n\t" \
    "s_memrealtime s[12:13]\n\t" \
    "s_nop 0\n\t" \
    "s_memrealtime s[14:15]\n\t" \
    "s_nop 0\n\t" \
    "s_memrealtime s[16:17]\n\t" \
    "s_nop 0\n\t" \
    "s_memrealtime s[18:19]\n\t" \
    "s_nop 0\n\t" \
    "s_memrealtime s[20:21]\n\t" \
    "s_nop 0\n\t" \
    "s_memrealtime s[22:23]\n\t" \
    "s_nop 0\n\t" \
    "s_memrealtime s[24:25]\n\t" \
    "s_nop 0\n\t" \
    "s_memrealtime s[26:27]\n\t" \
    "s_nop 0\n\t" \
    "s_memrealtime s[28:29]\n\t" \
    "s_memtime s[54:55]\n\t" \
    "s_add_u32 s70, s70, 8\n\t" \
    "s_waitcnt lgkmcnt(0)\n\t" \
    "s_sub_u32 s69, s54, s56\n\t" \
    "s_cmp_gt_u32 s69, s70\n\t" \
    "s_cbranch_scc1 rr_E55_%=\n\t" \
    "s_add_u32 s2, s2, s4\n\t" \
    "s_add_u32 s6, s6, s8\n\t" \
    "s_add_u32 s10, s10, s12\n\t" \
    "s_add_u32 s14, s14, s16\n\t" \
    "s_add_u32 s18, s18, s20\n\t" \
    "s_add_u32 s22, s22, s24\n\t" \
    "s_add_u32 s2, s2, s6\n\t" \
    "s_add_u32 s10, s10, s14\n\t" \
    "s_add_u32 s18, s18, s22\n\t" \
    "s_add_u32 s2, s2, s26\n\t" \
    "s_add_u32 s10, s10, s18\n\t" \
    "s_add_u32 s2, s2, s10\n\t" \
    "s_mul_i32 s70, s28, 13\n\t" \
    "s_sub_u32 s70, s70, s2\n\t" \
    "s_min_u32 s70, s70, 52\n\t" \
    "s_mul_i32 s69, s69, s70\n\t" \
    "s_mul_hi_u32 s69, s69, 76695844\n\t" \
    "s_mov_b32 s75, s73\n\t" \
    "s_mov_b32 s76, s74\n\t" \
    "s_add_u32 s73, s56, s69\n\t" \
    "s_lshl_b32 s74, s28, 4\n\t" \
    "s_sub_u32 s69, s73, s75\n\t" \
    "s_sub_u32 s70, s74, s76\n\t" \
    "s_sub_u32 s71, s70, 1600\n\t" \
    "s_cmp_lt_u32 s71, 400000\n\t" \
    "s_cbranch_scc0 rr_K56_%=\n\t" \
    "v_cvt_f32_u32 v164, s69\n\t" \
    "v_cvt_f32_u32 v165, s70\n\t" \
    "v_rcp_f32 v166, v164\n\t" \
    "v_rcp_f32 v167, v165\n\t" \
    "v_mul_f32 v166, v166, v165\n\t" \
    "v_mul_f32 v167, v167, v164\n\t" \
    "v_mul_f32 v166, 0x4f800000, v166\n\t" \
    "v_mul_f32 v167, 0x4f000000, v167\n\t" \
    "v_cvt_u32_f32 v166, v166\n\t" \
    "v_cvt_u32_f32 v167, v167\n\t" \
    "v_readfirstlane_b32 s69, v166\n\t" \
    "v_readfirstlane_b32 s70, v167\n\t" \
    "s_nop 1\n\t" \
    "s_lshr_b32 s71, s65, 1\n\t" \
    "s_min_u32 s71, s71, 1\n\t" \
    "s_sub_u32 s69, s69, s78\n\t" \
    "s_ashr_i32 s69, s69, s71\n\t" \
    "s_add_u32 s78, s78, s69\n\t" \
    "s_sub_u32 s70, s70, s77\n\t" \
    "s_ashr_i32 s70, s70, s71\n\t" \
    "s_add_u32 s77, s77, s70\n\t" \
    "rr_K56_%=:\n\t" \
    "s_mov_b32 s91, s64\n\t" \
    "s_cmp_lg_u32 s64, 0\n\t" \
    "s_cselect_b32 s77, 0, s77\n\t" \
    "v_mov_b32 v160, 0\n\t" \
    "v_mov_b32 v161, 0\n\t" \
    "v_mov_b32 v162, 0\n\t" \
    "v_mov_b32 v163, 0\n\t" \
    ".p2align 6\n\t" \
    "s_nop 0\n\t" \
    "s_nop 0\n\t" \
    "s_sub_u32 s70, s80, s74\n\t" \
    "s_max_i32 s70, s70, 0\n\t" \
    "s_min_i32 s70, s70, 16000\n\t" \
    "s_lshl_b32 s70, s70, 1\n\t" \
    "s_mul_hi_u32 s70, s70, s77\n\t" \
    "s_add_u32 s70, s70, s73\n\t" \
    "s_getpc_b64 s[30:31]\n\t" \
    "rr_Wp59_%=:\n\t" \
    "s_add_u32 s30, s30, rr_We60_%=-rr_Wp59_%=\n\t" \
    "s_addc_u32 s31, s31, 0\n\t" \
    "s_sub_u32 s70, s70, 96\n\t" \
    "rr_W58_%=:\n\t" \
    "s_memtime s[56:57]\n\t" \
    "s_waitcnt lgkmcnt(0)\n\t" \
    "s_sub_u32 s69, s70, s56\n\t" \
    "s_cmp_gt_i32 s69, 80\n\t" \
    "s_cbranch_scc1 rr_W58_%=\n\t" \
    "s_max_i32 s69, s69, 0\n\t" \
    "s_and_b32 s69, s69, -4\n\t" \
    "s_sub_u32 s30, s30, s69\n\t" \
    "s_subb_u32 s31, s31, 0\n\t" \
    "s_setpc_b64 s[30:31]\n\t" \
    "s_nop 0\n\t" \
    "s_nop 0\n\t" \
    "s_nop 0\n\t" \
    "s_nop 0\n\t" \
    "s_nop 0\n\t" \
    "s_nop 0\n\t" \
    "s_nop 0\n\t" \
    "s_nop 0\n\t" \
    "s_nop 0\n\t" \
    "s_nop 0\n\t" \
    "s_nop 0\n\t" \
    "s_nop 0\n\t" \
    "s_nop 0\n\t" \
    "s_nop 0\n\t" \
    "s_nop 0\n\t" \
    "s_nop 0\n\t" \
    "s_nop 0\n\t" \
    "s_nop 0\n\t" \
    "s_nop 0\n\t" \
    "s_nop 0\n\t" \
    "rr_We60_%=:\n\t" \
    "global_load_dword v192, v7, s[52:53] offset:0 sc1\n\t" \
    "s_sub_u32 s71, s86, s74\n\t" \
    "s_lshl_b32 s71, s71, 2\n\t" \
    "s_lshr_b32 s33, s77, 1\n\t" \
    "s_sub_u32 s34, s73, 176\n\t" \
    "s_mov_b32 s96, 0\n\t" \
    "s_getpc_b64 s[30:31]\n\t" \
    "rr_rpc64_%=:\n\t" \
    "s_add_u32 s50, s30, rr_pgo67_%=-rr_rpc64_%=\n\t" \
    "s_addc_u32 s51, s31, 0\n\t" \
    "s_add_u32 s30, s30, rr_again5_%=-rr_rpc64_%=\n\t" \
    "s_addc_u32 s31, s31, 0\n\t" \
    "s_nop 7\n\t" \
    "s_nop 7\n\t" \
    "s_nop 7\n\t" \
    "s_nop 7\n\t" \
    "s_nop 3\n\t" \
    "global_load_dword v193, v7, s[52:53] offset:2048 sc1\n\t" \
    "s_nop 7\n\t" \
    "s_nop 7\n\t" \
    "s_nop 7\n\t" \
    "s_nop 7\n\t" \
    "s_nop 7\n\t" \
    "s_nop 7\n\t" \
    "global_load_dword v194, v189, s[52:53] offset:0 sc1\n\t" \
    "s_waitcnt vmcnt(2)\n\t" \
    "rr_hok61_%=:\n\t" \
    "s_memtime s[54:55]\n\t" \
    "v_max_u32_dpp v192, v192, v192 row_ror:1 row_mask:0xf bank_mask:0xf\n\t" \
    "s_nop 1\n\t" \
    "v_max_u32_dpp v192, v192, v192 row_ror:2 row_mask:0xf bank_mask:0xf\n\t" \
    "s_nop 1\n\t" \
    "v_max_u32_dpp v192, v192, v192 row_ror:4 row_mask:0xf bank_mask:0xf\n\t" \
    "s_nop 1\n\t" \
    "v_max_u32_dpp v192, v192, v192 row_ror:8 row_mask:0xf bank_mask:0xf\n\t" \
    "s_nop 0\n\t" \
    "v_readlane_b32 s69, v192, 0\n\t" \
    "v_readlane_b32 s70, v192, 16\n\t" \
    "s_waitcnt lgkmcnt(0)\n\t" \
    "rr_hmax70_%=:\n\t" \
    "s_sub_u32 s56, s34, s54\n\t" \
    "s_max_u32 s81, s69, s70\n\t" \
    "s_cmp_eq_u32 s81, -1\n\t" \
    "s_cbranch_scc1 rr_hlate62_%=\n\t" \
    "s_lshl2_add_u32 s70, s81, s71\n\t" \
    "s_mul_hi_i32 s70, s70, s33\n\t" \
    "s_add_u32 s69, s70, s56\n\t" \
    "s_max_i32 s69, s69, 0\n\t" \
    "s_cmp_gt_i32 s69, 80\n\t" \
    "s_cbranch_scc1 rr_rpoll65_%=\n\t" \
    "rr_rback66_%=:\n\t" \
    "s_and_b32 s69, s69, -4\n\t" \
    "s_sub_u32 s30, s30, s69\n\t" \
    "s_subb_u32 s31, s31, 0\n\t" \
    "s_setpc_b64 s[30:31]\n\t" \
    "rr_rpoll65_%=:\n\t" \
    "s_min_i32 s70, s70, 24000\n\t" \
    "s_add_u32 s70, s70, s73\n\t" \
    "s_sub_u32 s70, s70, 124\n\t" \
    "s_sub_u32 s70, s70, s96\n\t" \
    "rr_wl63_%=:\n\t" \
    "s_memtime s[56:57]\n\t" \
    "s_waitcnt lgkmcnt(0)\n\t" \
    "s_sub_u32 s69, s70, s56\n\t" \
    "s_cmp_gt_i32 s69, 80\n\t" \
    "s_cbranch_scc1 rr_wl63_%=\n\t" \
    "s_max_i32 s69, s69, 0\n\t" \
    "s_branch rr_rback66_%=\n\t" \
    "s_nop 0\n\t" \
    "s_nop 0\n\t" \
    "s_nop 0\n\t" \
    "s_nop 0\n\t" \
    "s_nop 0\n\t" \
    "s_nop 0\n\t" \
    "s_nop 0\n\t" \
    "s_nop 0\n\t" \
    "s_nop 0\n\t" \
    "s_nop 0\n\t" \
    "s_nop 0\n\t" \
    "s_nop 0\n\t" \
    "s_nop 0\n\t" \
    "s_nop 0\n\t" \
    "s_nop 0\n\t" \
    "s_nop 0\n\t" \
    "s_nop 0\n\t" \
    "s_nop 0\n\t" \
    "s_nop 0\n\t" \
    "s_nop 0\n\t" \
    "rr_again5_%=:\n\t" \
    "rr_pgo67_%=:\n\t" \
    "global_load_dwordx4 v[48:51], v10, s[46:47] sc1\n\t" \
    "global_load_dwordx4 v[52:55], v11, s[46:47] sc1\n\t" \
    "global_load_dwordx4 v[56:59], v12, s[46:47] sc1\n\t" \
    "global_load_dwordx4 v[60:63], v13, s[46:47] sc1\n\t" \
    "global_load_dwordx4 v[64:67], v14, s[46:47] sc1\n\t" \
    "global_load_dwordx4 v[68:71], v15, s[46:47] sc1\n\t" \
    "global_load_dwordx4 v[72:75], v16, s[46:47] sc1\n\t" \
    "global_load_dwordx4 v[76:79], v17, s[46:47] sc1\n\t" \
    "global_load_dwordx4 v[80:83], v18, s[46:47] sc1\n\t" \
    "global_load_dwordx4 v[84:87], v19, s[46:47] sc1\n\t" \
    "global_load_dwordx4 v[88:91], v20, s[46:47] sc1\n\t" \
    "global_load_dwordx4 v[92:95], v21, s[46:47] sc1\n\t" \
    "global_load_dwordx4 v[96:99], v22, s[46:47] sc1\n\t" \
    "global_load_dwordx4 v[100:103], v23, s[46:47] sc1\n\t" \
    "global_load_dwordx4 v[104:107], v24, s[46:47] sc1\n\t" \
    "global_load_dwordx4 v[108:111], v25, s[46:47] sc1\n\t" \
    "global_load_dwordx4 v[112:115], v42, s[46:47] sc1\n\t" \
    "global_load_dwordx4 v[116:119], v43, s[46:47] sc1\n\t" \
    "global_load_dwordx4 v[120:123], v44, s[46:47] sc1\n\t" \
    "global_load_dwordx4 v[124:127], v45, s[46:47] sc1\n\t" \
    "global_load_dwordx4 v[128:131], v0, s[40:41] offset:0 sc1\n\t" \
    "global_load_dwordx4 v[132:135], v0, s[40:41] offset:1024 sc1\n\t" \
    "global_load_dwordx4 v[136:139], v0, s[40:41] offset:2048 sc1\n\t" \
    "global_load_dwordx4 v[140:143], v0, s[40:41] offset:3072 sc1\n\t" \
    "global_load_dwordx4 v[144:147], v1, s[40:41] offset:0 sc1\n\t" \
    "global_load_dwordx4 v[148:151], v1, s[40:41] offset:1024 sc1\n\t" \
    "global_load_dwordx4 v[152:155], v1, s[40:41] offset:2048 sc1\n\t" \
    "global_load_dwordx4 v[156:159], v1, s[40:41] offset:3072 sc1\n\t" \
    "s_cmp_lg_u32 s91, 0\n\t" \
    "s_cbranch_scc1 rr_nob8_%=\n\t" \
    "rr_hbk71_%=:\n\t" \
    "v_mov_b32 v164, s81\n\t" \
    "ds_write_b32 v9, v164\n\t" \
    "s_waitcnt lgkmcnt(0)\n\t" \
    "s_barrier\n\t" \
    "rr_nob8_%=:\n\t" \
    "s_waitcnt vmcnt(27)\n\t" \
    "v_smfmac_f32_16x16x64_bf16 v[160:163], v[48:51], a[0:7], v4\n\t" \
    "s_waitcnt vmcnt(26)\n\t" \
    "v_smfmac_f32_16x16x64_bf16 v[160:163], v[52:55], a[8:15], v4\n\t" \
    "s_waitcnt vmcnt(25)\n\t" \
    "v_smfmac_f32_16x16x64_bf16 v[160:163], v[56:59], a[16:23], v4\n\t" \
    "s_waitcnt vmcnt(24)\n\t" \
    "v_smfmac_f32_16x16x64_bf16 v[160:163], v[60:63], a[24:31], v4\n\t" \
    "s_waitcnt vmcnt(23)\n\t" \
    "v_smfmac_f32_16x16x64_bf16 v[160:163], v[64:67], a[32:39], v4\n\t" \
    "s_waitcnt vmcnt(22)\n\t" \
    "v_smfmac_f32_16x16x64_bf16 v[160:163], v[68:71], a[40:47], v4\n\t" \
    "s_waitcnt vmcnt(21)\n\t" \
    "v_smfmac_f32_16x16x64_bf16 v[160:163], v[72:75], a[48:55], v4\n\t" \
    "s_waitcnt vmcnt(20)\n\t" \
    "v_smfmac_f32_16x16x64_bf16 v[160:163], v[76:79], a[56:63], v4\n\t" \
    "s_waitcnt vmcnt(19)\n\t" \
    "v_smfmac_f32_16x16x64_bf16 v[160:163], v[80:83], a[64:71], v4\n\t" \
    "s_waitcnt vmcnt(18)\n\t" \
    "v_smfmac_f32_16x16x64_bf16 v[160:163], v[84:87], a[72:79], v4\n\t" \
    "s_waitcnt vmcnt(17)\n\t" \
    "v_smfmac_f32_16x16x64_bf16 v[160:163], v[88:91], a[80:87], v4\n\t" \
    "s_waitcnt vmcnt(16)\n\t" \
    "v_smfmac_f32_16x16x64_bf16 v[160:163], v[92:95], a[88:95], v4\n\t" \
    "s_waitcnt vmcnt(15)\n\t" \
    "v_smfmac_f32_16x16x64_bf16 v[160:163], v[96:99], a[96:103], v4\n\t" \
    "s_waitcnt vmcnt(14)\n\t" \
    "v_smfmac_f32_16x16x64_bf16 v[160:163], v[100:103], a[104:111], v4\n\t" \
    "s_waitcnt vmcnt(13)\n\t" \
    "v_smfmac_f32_16x16x64_bf16 v[160:163], v[104:107], a[112:119], v4\n\t" \
    "s_waitcnt vmcnt(12)\n\t" \
    "v_smfmac_f32_16x16x64_bf16 v[160:163], v[108:111], a[120:127], v4\n\t" \
    "s_waitcnt vmcnt(6)\n\t" \
    "v_smfmac_f32_16x16x64_bf16 v[160:163], v[112:115], v[128:135], v4\n\t" \
    "s_waitcnt vmcnt(4)\n\t" \
    "v_smfmac_f32_16x16x64_bf16 v[160:163], v[116:119], v[136:143], v4\n\t" \
    "s_waitcnt vmcnt(2)\n\t" \
    "v_smfmac_f32_16x16x64_bf16 v[160:163], v[120:123], v[144:151], v4\n\t" \
    "s_waitcnt vmcnt(0)\n\t" \
    "v_smfmac_f32_16x16x64_bf16 v[160:163], v[124:127], v[152:159], v4\n\t" \
    "s_nop 7\n\t" \
    "v_pk_add_f32 v[160:161], v[160:161], v[162:163]\n\t" \
    "v_add_f32 v166, v160, v161\n\t" \
    "v_cvt_pk_bf16_f32 v164, v160, v161\n\t" \
    "s_nop 0\n\t" \
    "v_add_f32_dpp v166, v166, v166 quad_perm:[1,0,3,2] row_mask:0xf bank_mask:0xf\n\t" \
    "v_mov_b32_dpp v165, v164 quad_perm:[1,0,3,2] row_mask:0xf bank_mask:0xf\n\t" \
    "v_cmp_u_f32 vcc, v166, v166\n\t" \
    "v_perm_b32 v167, v165, v164, v5\n\t" \
    "s_nop 0\n\t" \
    "v_cndmask_b32 v164, v167, v172, vcc\n\t" \
    "rr_cont7_%=:\n\t" \
    "global_store_dword v180, v164, s[48:49] sc1\n\t" \
    "global_store_dword v181, v164, s[48:49] sc1\n\t" \
    "global_store_dword v182, v164, s[48:49] sc1\n\t" \
    "global_store_dword v2, v164, s[48:49] sc1\n\t" \
    "s_cmp_lg_u32 s64, 0\n\t" \
    "s_cbranch_scc1 rr_g214_%=\n\t" \
    "s_cbranch_vccnz rr_bad6_%=\n\t" \
    "s_cmp_eq_u32 s65, 62\n\t" \
    "s_cbranch_scc1 rr_end4_%=\n\t" \
    "s_add_u32 s65, s65, 2\n\t" \
    "s_branch rr_pf9_%=\n\t" \
    "rr_bad6_%=:\n\t" \
    "s_cmp_lg_u32 s64, 0\n\t" \
    "s_cbranch_scc1 rr_cont7_%=\n\t" \
    "s_waitcnt lgkmcnt(0)\n\t" \
    "v_max_u32 v165, v48, v49\n\t" \
    "v_max3_u32 v165, v165, v50, v51\n\t" \
    "v_max3_u32 v165, v165, v52, v53\n\t" \
    "v_max3_u32 v165, v165, v54, v55\n\t" \
    "v_max3_u32 v165, v165, v56, v57\n\t" \
    "v_max3_u32 v165, v165, v58, v59\n\t" \
    "v_max3_u32 v165, v165, v60, v61\n\t" \
    "v_max3_u32 v165, v165, v62, v63\n\t" \
    "v_max3_u32 v165, v165, v64, v65\n\t" \
    "v_max3_u32 v165, v165, v66, v67\n\t" \
    "v_max3_u32 v165, v165, v68, v69\n\t" \
    "v_max3_u32 v165, v165, v70, v71\n\t" \
    "v_max3_u32 v165, v165, v72, v73\n\t" \
    "v_max3_u32 v165, v165, v74, v75\n\t" \
    "v_max3_u32 v165, v165, v76, v77\n\t" \
    "v_max3_u32 v165, v165, v78, v79\n\t" \
    "v_max3_u32 v165, v165, v80, v81\n\t" \
    "v_max3_u32 v165, v165, v82, v83\n\t" \
    "v_max3_u32 v165, v165, v84, v85\n\t" \
    "v_max3_u32 v165, v165, v86, v87\n\t" \
    "v_max3_u32 v165, v165, v88, v89\n\t" \
    "v_max3_u32 v165, v165, v90, v91\n\t" \
    "v_max3_u32 v165, v165, v92, v93\n\t" \
    "v_max3_u32 v165, v165, v94, v95\n\t" \
    "v_max3_u32 v165, v165, v96, v97\n\t" \
    "v_max3_u32 v165, v165, v98, v99\n\t" \
    "v_max3_u32 v165, v165, v100, v101\n\t" \
    "v_max3_u32 v165, v165, v102, v103\n\t" \
    "v_max3_u32 v165, v165, v104, v105\n\t" \
    "v_max3_u32 v165, v165, v106, v107\n\t" \
    "v_max3_u32 v165, v165, v108, v109\n\t" \
    "v_max3_u32 v165, v165, v110, v111\n\t" \
    "v_max3_u32 v165, v165, v112, v113\n\t" \
    "v_max3_u32 v165, v165, v114, v115\n\t" \
    "v_max3_u32 v165, v165, v116, v117\n\t" \
    "v_max3_u32 v165, v165, v118, v119\n\t" \
    "v_max3_u32 v165, v165, v120, v121\n\t" \
    "v_max3_u32 v165, v165, v122, v123\n\t" \
    "v_max3_u32 v165, v165, v124, v125\n\t" \
    "v_max3_u32 v165, v165, v126, v127\n\t" \
    "v_max_u32 v166, v128, v129\n\t" \
    "v_max3_u32 v166, v166, v130, v131\n\t" \
    "v_max3_u32 v166, v166, v132, v133\n\t" \
    "v_max3_u32 v166, v166, v134, v135\n\t" \
    "v_max3_u32 v166, v166, v136, v137\n\t" \
    "v_max3_u32 v166, v166, v138, v139\n\t" \
    "v_max3_u32 v166, v166, v140, v141\n\t" \
    "v_max3_u32 v166, v166, v142, v143\n\t" \
    "v_max3_u32 v166, v166, v144, v145\n\t" \
    "v_max3_u32 v166, v166, v146, v147\n\t" \
    "v_max3_u32 v166, v166, v148, v149\n\t" \
    "v_max3_u32 v166, v166, v150, v151\n\t" \
    "v_max3_u32 v166, v166, v152, v153\n\t" \
    "v_max3_u32 v166, v166, v154, v155\n\t" \
    "v_max3_u32 v166, v166, v156, v157\n\t" \
    "v_max3_u32 v166, v166, v158, v159\n\t" \
    "v_max_u32 v165, v165, v166\n\t" \
    "v_mov_b32 v164, v167\n\t" \
    "v_cmp_eq_u32 vcc, -1, v165\n\t" \
    "s_cbranch_vccz rr_cont7_%=\n\t" \
    "s_mov_b64 vcc, 0\n\t" \
    "s_mov_b32 s91, 1\n\t" \
    "s_mov_b32 s96, 0\n\t" \
    "v_mov_b32 v160, 0\n\t" \
    "v_mov_b32 v161, 0\n\t" \
    "v_mov_b32 v162, 0\n\t" \
    "v_mov_b32 v163, 0\n\t" \
    "s_cmp_eq_u32 s68, 0\n\t" \
    "s_cbranch_scc1 rr_F74_%=\n\t" \
    "s_sub_u32 s68, s68, 1\n\t" \
    "s_branch rr_again5_%=\n\t" \
    "rr_F74_%=:\n\t" \
    "v_mov_b32 v166, 5767424\n\t" \
    "v_mov_b32 v167, 3\n\t" \
    "global_store_dword v166, v167, s[36:37] sc1\n\t" \
    "s_waitcnt vmcnt(0)\n\t" \
    "s_branch rr_cont7_%=\n\t" \
    "rr_hlate62_%=:\n\t" \
    "s_cmp_lg_u32 s64, 0\n\t" \
    "s_cbranch_scc1 rr_hpd73_%=\n\t" \
    "global_load_dword v195, v189, s[52:53] offset:2048 sc1\n\t" \
    "s_waitcnt vmcnt(2)\n\t" \
    "v_mov_b32 v192, v193\n\t" \
    "v_cmp_eq_u32 vcc, -1, v193\n\t" \
    "s_cbranch_vccz rr_hok61_%=\n\t" \
    "global_load_dword v196, v190, s[52:53] offset:0 sc1\n\t" \
    "s_waitcnt vmcnt(2)\n\t" \
    "v_mov_b32 v192, v194\n\t" \
    "v_cmp_eq_u32 vcc, -1, v194\n\t" \
    "s_cbranch_vccz rr_hok61_%=\n\t" \
    "global_load_dword v197, v190, s[52:53] offset:2048 sc1\n\t" \
    "s_waitcnt vmcnt(2)\n\t" \
    "v_mov_b32 v192, v195\n\t" \
    "v_cmp_eq_u32 vcc, -1, v195\n\t" \
    "s_cbranch_vccz rr_hok61_%=\n\t" \
    "s_waitcnt vmcnt(1)\n\t" \
    "v_mov_b32 v192, v196\n\t" \
    "v_cmp_eq_u32 vcc, -1, v196\n\t" \
    "s_cbranch_vccz rr_hok61_%=\n\t" \
    "s_waitcnt vmcnt(0)\n\t" \
    "v_mov_b32 v192, v197\n\t" \
    "v_cmp_eq_u32 vcc, -1, v197\n\t" \
    "s_cbranch_vccz rr_hok61_%=\n\t" \
    "rr_hp72_%=:\n\t" \
    "global_load_dword v192, v191, s[52:53] offset:0 sc1\n\t" \
    "s_waitcnt vmcnt(0)\n\t" \
    "v_cmp_eq_u32 vcc, -1, v192\n\t" \
    "s_cbranch_vccz rr_hpd73_%=\n\t" \
    "s_cmp_eq_u32 s68, 0\n\t" \
    "s_cbranch_scc1 rr_F75_%=\n\t" \
    "s_sub_u32 s68, s68, 1\n\t" \
    "s_sleep 1\n\t" \
    "s_branch rr_hp72_%=\n\t" \
    "rr_F75_%=:\n\t" \
    "v_mov_b32 v166, 5767424\n\t" \
    "v_mov_b32 v167, 7\n\t" \
    "global_store_dword v166, v167, s[36:37] sc1\n\t" \
    "s_waitcnt vmcnt(0)\n\t" \
    "s_branch rr_hpd73_%=\n\t" \
    "rr_hpd73_%=:\n\t" \
    "v_and_b32 v192, -2, v192\n\t" \
    "s_waitcnt lgkmcnt(0)\n\t" \
    "s_mov_b32 s70, 116\n\t" \
    "rr_E76_%=:\n\t" \
    "s_memtime s[56:57]\n\t" \
    "s_memrealtime s[2:3]\n\t" \
    "s_nop 0\n\t" \
    "s_memrealtime s[4:5]\n\t" \
    "s_nop 0\n\t" \
    "s_memrealtime s[6:7]\n\t" \
    "s_nop 0\n\t" \
    "s_memrealtime s[8:9]\n\t" \
    "s_nop 0\n\t" \
    "s_memrealtime s[10:11]\n\t" \
    "s_nop 0\n\t" \
    "s_memrealtime s[12:13]\n\t" \
    "s_nop 0\n\t" \
    "s_memrealtime s[14:15]\n\t" \
    "s_nop 0\n\t" \
    "s_memrealtime s[16:17]\n\t" \
    "s_nop 0\n\t" \
    "s_memrealtime s[18:19]\n\t" \
    "s_nop 0\n\t" \
    "s_memrealtime s[20:21]\n\t" \
    "s_nop 0\n\t" \
    "s_memrealtime s[22:23]\n\t" \
    "s_nop 0\n\t" \
    "s_memrealtime s[24:25]\n\t" \
    "s_nop 0\n\t" \
    "s_memrealtime s[26:27]\n\t" \
    "s_nop 0\n\t" \
    "s_memrealtime s[28:29]\n\t" \
    "s_memtime s[54:55]\n\t" \
    "s_add_u32 s70, s70, 8\n\t" \
    "s_waitcnt lgkmcnt(0)\n\t" \
    "s_sub_u32 s69, s54, s56\n\t" \
    "s_cmp_gt_u32 s69, s70\n\t" \
    "s_cbranch_scc1 rr_E76_%=\n\t" \
    "s_add_u32 s2, s2, s4\n\t" \
    "s_add_u32 s6, s6, s8\n\t" \
    "s_add_u32 s10, s10, s12\n\t" \
    "s_add_u32 s14, s14, s16\n\t" \
    "s_add_u32 s18, s18, s20\n\t" \
    "s_add_u32 s22, s22, s24\n\t" \
    "s_add_u32 s2, s2, s6\n\t" \
    "s_add_u32 s10, s10, s14\n\t" \
    "s_add_u32 s18, s18, s22\n\t" \
    "s_add_u32 s2, s2, s26\n\t" \
    "s_add_u32 s10, s10, s18\n\t" \
    "s_add_u32 s2, s2, s10\n\t" \
    "s_mul_i32 s70, s28, 13\n\t" \
    "s_sub_u32 s70, s70, s2\n\t" \
    "s_min_u32 s70, s70, 52\n\t" \
    "s_mul_i32 s69, s69, s70\n\t" \
    "s_mul_hi_u32 s69, s69, 76695844\n\t" \
    "s_mov_b32 s75, s73\n\t" \
    "s_mov_b32 s76, s74\n\t" \
    "s_add_u32 s73, s56, s69\n\t" \
    "s_lshl_b32 s74, s28, 4\n\t" \
    "s_sub_u32 s71, s86, s74\n\t" \
    "s_lshl_b32 s71, s71, 2\n\t" \
    "s_lshr_b32 s33, s77, 1\n\t" \
    "s_sub_u32 s34, s73, 176\n\t" \
    "s_branch rr_hok61_%=\n\t" \
    "rr_end4_%=:\n\t" \
    "s_endpgm\n\t" \
    "rr_last_%=:\n\t" \
    ""
#define ASM_H \
    "s_mov_b64 s[36:37], %0\n\t" \
    "s_mov_b64 s[42:43], %1\n\t" \
    "s_mov_b64 s[44:45], %2\n\t" \
    "s_mov_b32 s66, %3\n\t" \
    "s_mov_b32 s67, %4\n\t" \
    "s_mov_b64 s[50:51], %5\n\t" \
    "s_mov_b32 s82, %7\n\t" \
    "s_load_dwordx8 s[84:91], %6, 0\n\t" \
    "v_mbcnt_lo_u32_b32 v223, -1, 0\n\t" \
    "v_mbcnt_hi_u32_b32 v223, -1, v223\n\t" \
    "s_getpc_b64 s[46:47]\n\t" \
    "hh_pc_%=:\n\t" \
    "s_lshl_b32 s69, s66, 13\n\t" \
    "v_lshl_add_u32 v222, v223, 7, s69\n\t" \
    "v_min_u32 v222, hh_last_%=-hh_pc_%=-4, v222\n\t" \
    "global_load_dword v222, v222, s[46:47]\n\t" \
    "v_lshlrev_b32 v223, 2, v223\n\t" \
    "v_add_u32 v223, s82, v223\n\t" \
    "ds_read_b32 v0, v223 offset:0\n\t" \
    "ds_read_b32 v1, v223 offset:256\n\t" \
    "ds_read_b32 v2, v223 offset:512\n\t" \
    "ds_read_b32 v3, v223 offset:768\n\t" \
    "ds_read_b32 v4, v223 offset:1024\n\t" \
    "ds_read_b32 v5, v223 offset:1280\n\t" \
    "ds_read_b32 v6, v223 offset:1536\n\t" \
    "ds_read_b32 v7, v223 offset:1792\n\t" \
    "ds_read_b32 v8, v223 offset:2048\n\t" \
    "ds_read_b32 v9, v223 offset:2304\n\t" \
    "ds_read_b32 v10, v223 offset:2560\n\t" \
    "ds_read_b32 v11, v223 offset:2816\n\t" \
    "s_waitcnt lgkmcnt(0)\n\t" \
    "ds_read_b32 v12, v223 offset:3072\n\t" \
    "ds_read_b32 v13, v223 offset:3328\n\t" \
    "ds_read_b32 v14, v223 offset:3584\n\t" \
    "ds_read_b32 v15, v223 offset:3840\n\t" \
    "ds_read_b32 v16, v223 offset:4096\n\t" \
    "ds_read_b32 v17, v223 offset:4352\n\t" \
    "s_waitcnt lgkmcnt(0)\n\t" \
    "s_mov_b32 s68, 262144\n\t" \
    "s_mov_b32 s72, 0\n\t" \
    "s_mov_b32 s83, -1\n\t" \
    "s_mov_b32 s65, 0\n\t" \
    "s_mov_b32 s77, 3019898880\n\t" \
    "s_mov_b32 s78, 3054198966\n\t" \
    "s_waitcnt lgkmcnt(0)\n\t" \
    "s_mov_b32 s70, 116\n\t" \
    "hh_E1_%=:\n\t" \
    "s_memtime s[56:57]\n\t" \
    "s_memrealtime s[2:3]\n\t" \
    "s_nop 0\n\t" \
    "s_memrealtime s[4:5]\n\t" \
    "s_nop 0\n\t" \
    "s_memrealtime s[6:7]\n\t" \
    "s_nop 0\n\t" \
    "s_memrealtime s[8:9]\n\t" \
    "s_nop 0\n\t" \
    "s_memrealtime s[10:11]\n\t" \
    "s_nop 0\n\t" \
    "s_memrealtime s[12:13]\n\t" \
    "s_nop 0\n\t" \
    "s_memrealtime s[14:15]\n\t" \
    "s_nop 0\n\t" \
    "s_memrealtime s[16:17]\n\t" \
    "s_nop 0\n\t" \
    "s_memrealtime s[18:19]\n\t" \
    "s_nop 0\n\t" \
    "s_memrealtime s[20:21]\n\t" \
    "s_nop 0\n\t" \
    "s_memrealtime s[22:23]\n\t" \
    "s_nop 0\n\t" \
    "s_memrealtime s[24:25]\n\t" \
    "s_nop 0\n\t" \
    "s_memrealtime s[26:27]\n\t" \
    "s_nop 0\n\t" \
    "s_memrealtime s[28:29]\n\t" \
    "s_memtime s[54:55]\n\t" \
    "s_add_u32 s70, s70, 8\n\t" \
    "s_waitcnt lgkmcnt(0)\n\t" \
    "s_sub_u32 s69, s54, s56\n\t" \
    "s_cmp_gt_u32 s69, s70\n\t" \
    "s_cbranch_scc1 hh_E1_%=\n\t" \
    "s_add_u32 s2, s2, s4\n\t" \
    "s_add_u32 s6, s6, s8\n\t" \
    "s_add_u32 s10, s10, s12\n\t" \
    "s_add_u32 s14, s14, s16\n\t" \
    "s_add_u32 s18, s18, s20\n\t" \
    "s_add_u32 s22, s22, s24\n\t" \
    "s_add_u32 s2, s2, s6\n\t" \
    "s_add_u32 s10, s10, s14\n\t" \
    "s_add_u32 s18, s18, s22\n\t" \
    "s_add_u32 s2, s2, s26\n\t" \
    "s_add_u32 s10, s10, s18\n\t" \
    "s_add_u32 s2, s2, s10\n\t" \
    "s_mul_i32 s70, s28, 13\n\t" \
    "s_sub_u32 s70, s70, s2\n\t" \
    "s_min_u32 s70, s70, 52\n\t" \
    "s_mul_i32 s69, s69, s70\n\t" \
    "s_mul_hi_u32 s69, s69, 76695844\n\t" \
    "s_mov_b32 s75, s73\n\t" \
    "s_mov_b32 s76, s74\n\t" \
    "s_add_u32 s73, s56, s69\n\t" \
    "s_lshl_b32 s74, s28, 4\n\t" \
    "v_mbcnt_lo_u32_b32 v30, -1, 0\n\t" \
    "v_mbcnt_hi_u32_b32 v30, -1, v30\n\t" \
    "v_lshlrev_b32 v30, 6, v30\n\t" \
    "v_add_u32 v30, 6225920, v30\n\t" \
    "v_mov_b32 v31, 6234112\n\t" \
    "global_load_dword v31, v31, s[36:37] sc1\n\t" \
    "global_load_dwordx2 v[32:33], v30, s[36:37] sc1\n\t" \
    "global_load_dword v34, v30, s[36:37] offset:2048 sc1\n\t" \
    "s_waitcnt vmcnt(0)\n\t" \
    "hh_C2_%=:\n\t" \
    "v_cmp_ne_u32 s[54:55], v32, v34\n\t" \
    "v_sub_u32 v32, v32, v31\n\t" \
    "v_cmp_ne_u32 vcc, 1, v32\n\t" \
    "s_nop 1\n\t" \
    "s_or_b64 vcc, vcc, s[54:55]\n\t" \
    "s_cbranch_scc0 hh_Cok3_%=\n\t" \
    "s_cmp_eq_u32 s68, 0\n\t" \
    "s_cbranch_scc1 hh_F5_%=\n\t" \
    "s_sub_u32 s68, s68, 1\n\t" \
    "s_sleep 6\n\t" \
    "s_branch hh_Cag4_%=\n\t" \
    "hh_F5_%=:\n\t" \
    "v_mov_b32 v34, 5767424\n\t" \
    "v_mov_b32 v35, 4\n\t" \
    "global_store_dword v34, v35, s[36:37] sc1\n\t" \
    "s_waitcnt vmcnt(0)\n\t" \
    "s_branch hh_Cok3_%=\n\t" \
    "hh_Cag4_%=:\n\t" \
    "global_load_dwordx2 v[32:33], v30, s[36:37] sc1\n\t" \
    "global_load_dword v34, v30, s[36:37] offset:2048 sc1\n\t" \
    "s_waitcnt vmcnt(0)\n\t" \
    "s_branch hh_C2_%=\n\t" \
    "hh_Cok3_%=:\n\t" \
    "v_mov_b32_dpp v30, v33 quad_perm:[1,0,3,2] row_mask:0xf bank_mask:0xf\n\t" \
    "v_cmp_lt_u32 s[44:45], v30, v33\n\t" \
    "s_nop 1\n\t" \
    "s_bitcmp1_b32 s44, 0\n\t" \
    "s_cselect_b32 s62, 8192, 0\n\t" \
    "s_bitcmp1_b32 s44, 2\n\t" \
    "s_cselect_b32 s63, 8192, 0\n\t" \
    "s_or_b32 s63, s63, 0x200000\n\t" \
    "v_min_u32 v30, v30, v33\n\t" \
    "v_cmp_lt_u32 vcc, 660, v30\n\t" \
    "s_nop 1\n\t" \
    "s_cmp_lg_u64 vcc, 0\n\t" \
    "s_cselect_b32 s69, 1, 0\n\t" \
    "s_mul_i32 s70, s69, 160\n\t" \
    "s_add_u32 s86, s86, s70\n\t" \
    "s_mul_i32 s70, s69, 536\n\t" \
    "s_add_u32 s87, s87, s70\n\t" \
    "s_mul_i32 s70, s69, 256\n\t" \
    "s_add_u32 s88, s88, s70\n\t" \
    "s_waitcnt lgkmcnt(0)\n\t" \
    "s_mov_b32 s70, 116\n\t" \
    "hh_E6_%=:\n\t" \
    "s_memtime s[56:57]\n\t" \
    "s_memrealtime s[2:3]\n\t" \
    "s_nop 0\n\t" \
    "s_memrealtime s[4:5]\n\t" \
    "s_nop 0\n\t" \
    "s_memrealtime s[6:7]\n\t" \
    "s_nop 0\n\t" \
    "s_memrealtime s[8:9]\n\t" \
    "s_nop 0\n\t" \
    "s_memrealtime s[10:11]\n\t" \
    "s_nop 0\n\t" \
    "s_memrealtime s[12:13]\n\t" \
    "s_nop 0\n\t" \
    "s_memrealtime s[14:15]\n\t" \
    "s_nop 0\n\t" \
    "s_memrealtime s[16:17]\n\t" \
    "s_nop 0\n\t" \
    "s_memrealtime s[18:19]\n\t" \
    "s_nop 0\n\t" \
    "s_memrealtime s[20:21]\n\t" \
    "s_nop 0\n\t" \
    "s_memrealtime s[22:23]\n\t" \
    "s_nop 0\n\t" \
    "s_memrealtime s[24:25]\n\t" \
    "s_nop 0\n\t" \
    "s_memrealtime s[26:27]\n\t" \
    "s_nop 0\n\t" \
    "s_memrealtime s[28:29]\n\t" \
    "s_memtime s[54:55]\n\t" \
    "s_add_u32 s70, s70, 8\n\t" \
    "s_waitcnt lgkmcnt(0)\n\t" \
    "s_sub_u32 s69, s54, s56\n\t" \
    "s_cmp_gt_u32 s69, s70\n\t" \
    "s_cbranch_scc1 hh_E6_%=\n\t" \
    "s_add_u32 s2, s2, s4\n\t" \
    "s_add_u32 s6, s6, s8\n\t" \
    "s_add_u32 s10, s10, s12\n\t" \
    "s_add_u32 s14, s14, s16\n\t" \
    "s_add_u32 s18, s18, s20\n\t" \
    "s_add_u32 s22, s22, s24\n\t" \
    "s_add_u32 s2, s2, s6\n\t" \
    "s_add_u32 s10, s10, s14\n\t" \
    "s_add_u32 s18, s18, s22\n\t" \
    "s_add_u32 s2, s2, s26\n\t" \
    "s_add_u32 s10, s10, s18\n\t" \
    "s_add_u32 s2, s2, s10\n\t" \
    "s_mul_i32 s70, s28, 13\n\t" \
    "s_sub_u32 s70, s70, s2\n\t" \
    "s_min_u32 s70, s70, 52\n\t" \
    "s_mul_i32 s69, s69, s70\n\t" \
    "s_mul_hi_u32 s69, s69, 76695844\n\t" \
    "s_mov_b32 s75, s73\n\t" \
    "s_mov_b32 s76, s74\n\t" \
    "s_add_u32 s73, s56, s69\n\t" \
    "s_lshl_b32 s74, s28, 4\n\t" \
    "s_sub_u32 s69, s73, s75\n\t" \
    "s_sub_u32 s70, s74, s76\n\t" \
    "s_sub_u32 s71, s70, 1600\n\t" \
    "s_cmp_lt_u32 s71, 400000\n\t" \
    "s_cbranch_scc0 hh_K7_%=\n\t" \
    "v_cvt_f32_u32 v30, s69\n\t" \
    "v_cvt_f32_u32 v31, s70\n\t" \
    "v_rcp_f32 v32, v30\n\t" \
    "v_rcp_f32 v33, v31\n\t" \
    "v_mul_f32 v32, v32, v31\n\t" \
    "v_mul_f32 v33, v33, v30\n\t" \
    "v_mul_f32 v32, 0x4f800000, v32\n\t" \
    "v_mul_f32 v33, 0x4f000000, v33\n\t" \
    "v_cvt_u32_f32 v32, v32\n\t" \
    "v_cvt_u32_f32 v33, v33\n\t" \
    "v_readfirstlane_b32 s69, v32\n\t" \
    "v_readfirstlane_b32 s70, v33\n\t" \
    "s_nop 1\n\t" \
    "s_mov_b32 s78, s69\n\t" \
    "s_mov_b32 s77, s70\n\t" \
    "hh_K7_%=:\n\t" \
    "s_sub_u32 s80, s74, 32000\n\t" \
    "v_mov_b32 v38, -1\n\t" \
    "v_mov_b32 v39, -1\n\t" \
    "s_mov_b32 m0, 155648\n\t" \
    "s_mul_i32 s96, s67, 16\n\t" \
    "s_add_u32 s96, s96, 64\n\t" \
    "hh_loop8_%=:\n\t" \
    "s_and_b32 s69, s65, 7\n\t" \
    "s_lshl_b32 s69, s69, 18\n\t" \
    "s_add_u32 s69, s69, s63\n\t" \
    "s_add_u32 s52, s36, s69\n\t" \
    "s_addc_u32 s53, s37, 0\n\t" \
    "s_add_u32 s71, s80, 608\n\t" \
    "s_sub_u32 s70, s71, s74\n\t" \
    "s_max_i32 s70, s70, 0\n\t" \
    "s_min_i32 s70, s70, 16000\n\t" \
    "s_lshl_b32 s70, s70, 1\n\t" \
    "s_mul_hi_u32 s70, s70, s77\n\t" \
    "s_add_u32 s70, s70, s73\n\t" \
    "hh_W11_%=:\n\t" \
    "s_memtime s[56:57]\n\t" \
    "s_waitcnt lgkmcnt(0)\n\t" \
    "s_sub_u32 s69, s70, s56\n\t" \
    "s_cmp_gt_i32 s69, 24\n\t" \
    "s_cbranch_scc1 hh_W11_%=\n\t" \
    "global_load_dword v30, v1, s[52:53] sc1\n\t" \
    "s_waitcnt lgkmcnt(0)\n\t" \
    "s_mov_b32 s70, 116\n\t" \
    "hh_E15_%=:\n\t" \
    "s_memtime s[56:57]\n\t" \
    "s_memrealtime s[2:3]\n\t" \
    "s_nop 0\n\t" \
    "s_memrealtime s[4:5]\n\t" \
    "s_nop 0\n\t" \
    "s_memrealtime s[6:7]\n\t" \
    "s_nop 0\n\t" \
    "s_memrealtime s[8:9]\n\t" \
    "s_nop 0\n\t" \
    "s_memrealtime s[10:11]\n\t" \
    "s_nop 0\n\t" \
    "s_memrealtime s[12:13]\n\t" \
    "s_nop 0\n\t" \
    "s_memrealtime s[14:15]\n\t" \
    "s_nop 0\n\t" \
    "s_memrealtime s[16:17]\n\t" \
    "s_nop 0\n\t" \
    "s_memrealtime s[18:19]\n\t" \
    "s_nop 0\n\t" \
    "s_memrealtime s[20:21]\n\t" \
    "s_nop 0\n\t" \
    "s_memrealtime s[22:23]\n\t" \
    "s_nop 0\n\t" \
    "s_memrealtime s[24:25]\n\t" \
    "s_nop 0\n\t" \
    "s_memrealtime s[26:27]\n\t" \
    "s_nop 0\n\t" \
    "s_memrealtime s[28:29]\n\t" \
    "s_memtime s[54:55]\n\t" \
    "s_add_u32 s70, s70, 8\n\t" \
    "s_waitcnt lgkmcnt(0)\n\t" \
    "s_sub_u32 s69, s54, s56\n\t" \
    "s_cmp_gt_u32 s69, s70\n\t" \
    "s_cbranch_scc1 hh_E15_%=\n\t" \
    "s_add_u32 s2, s2, s4\n\t" \
    "s_add_u32 s6, s6, s8\n\t" \
    "s_add_u32 s10, s10, s12\n\t" \
    "s_add_u32 s14, s14, s16\n\t" \
    "s_add_u32 s18, s18, s20\n\t" \
    "s_add_u32 s22, s22, s24\n\t" \
    "s_add_u32 s2, s2, s6\n\t" \
    "s_add_u32 s10, s10, s14\n\t" \
    "s_add_u32 s18, s18, s22\n\t" \
    "s_add_u32 s2, s2, s26\n\t" \
    "s_add_u32 s10, s10, s18\n\t" \
    "s_add_u32 s2, s2, s10\n\t" \
    "s_mul_i32 s70, s28, 13\n\t" \
    "s_sub_u32 s70, s70, s2\n\t" \
    "s_min_u32 s70, s70, 52\n\t" \
    "s_mul_i32 s69, s69, s70\n\t" \
    "s_mul_hi_u32 s69, s69, 76695844\n\t" \
    "s_mov_b32 s75, s73\n\t" \
    "s_mov_b32 s76, s74\n\t" \
    "s_add_u32 s73, s56, s69\n\t" \
    "s_lshl_b32 s74, s28, 4\n\t" \
    "s_sub_u32 s69, s73, s75\n\t" \
    "s_sub_u32 s70, s74, s76\n\t" \
    "s_sub_u32 s71, s70, 1600\n\t" \
    "s_cmp_lt_u32 s71, 400000\n\t" \
    "s_cbranch_scc0 hh_K16_%=\n\t" \
    "v_cvt_f32_u32 v34, s69\n\t" \
    "v_cvt_f32_u32 v35, s70\n\t" \
    "v_rcp_f32 v36, v34\n\t" \
    "v_rcp_f32 v37, v35\n\t" \
    "v_mul_f32 v36, v36, v35\n\t" \
    "v_mul_f32 v37, v37, v34\n\t" \
    "v_mul_f32 v36, 0x4f800000, v36\n\t" \
    "v_mul_f32 v37, 0x4f000000, v37\n\t" \
    "v_cvt_u32_f32 v36, v36\n\t" \
    "v_cvt_u32_f32 v37, v37\n\t" \
    "v_readfirstlane_b32 s69, v36\n\t" \
    "v_readfirstlane_b32 s70, v37\n\t" \
    "s_nop 1\n\t" \
    "s_sub_u32 s69, s69, s78\n\t" \
    "s_ashr_i32 s69, s69, 1\n\t" \
    "s_add_u32 s78, s78, s69\n\t" \
    "s_sub_u32 s70, s70, s77\n\t" \
    "s_ashr_i32 s70, s70, 1\n\t" \
    "s_add_u32 s77, s77, s70\n\t" \
    "hh_K16_%=:\n\t" \
    "s_waitcnt vmcnt(0)\n\t" \
    "v_cmp_eq_u32 vcc, -1, v30\n\t" \
    "s_cbranch_vccz hh_Hok13_%=\n\t" \
    "hh_H12_%=:\n\t" \
    "global_load_dword v30, v1, s[52:53] sc1\n\t" \
    "s_waitcnt vmcnt(0)\n\t" \
    "v_cmp_eq_u32 vcc, -1, v30\n\t" \
    "s_cbranch_vccz hh_Hc14_%=\n\t" \
    "s_cmp_eq_u32 s68, 0\n\t" \
    "s_cbranch_scc1 hh_F17_%=\n\t" \
    "s_sub_u32 s68, s68, 1\n\t" \
    "s_sleep 2\n\t" \
    "s_branch hh_H12_%=\n\t" \
    "hh_F17_%=:\n\t" \
    "v_mov_b32 v31, 5767424\n\t" \
    "v_mov_b32 v32, 14\n\t" \
    "global_store_dword v31, v32, s[36:37] sc1\n\t" \
    "s_waitcnt vmcnt(0)\n\t" \
    "s_branch hh_Hc14_%=\n\t" \
    "hh_Hc14_%=:\n\t" \
    "s_waitcnt lgkmcnt(0)\n\t" \
    "s_mov_b32 s70, 116\n\t" \
    "hh_E18_%=:\n\t" \
    "s_memtime s[56:57]\n\t" \
    "s_memrealtime s[2:3]\n\t" \
    "s_nop 0\n\t" \
    "s_memrealtime s[4:5]\n\t" \
    "s_nop 0\n\t" \
    "s_memrealtime s[6:7]\n\t" \
    "s_nop 0\n\t" \
    "s_memrealtime s[8:9]\n\t" \
    "s_nop 0\n\t" \
    "s_memrealtime s[10:11]\n\t" \
    "s_nop 0\n\t" \
    "s_memrealtime s[12:13]\n\t" \
    "s_nop 0\n\t" \
    "s_memrealtime s[14:15]\n\t" \
    "s_nop 0\n\t" \
    "s_memrealtime s[16:17]\n\t" \
    "s_nop 0\n\t" \
    "s_memrealtime s[18:19]\n\t" \
    "s_nop 0\n\t" \
    "s_memrealtime s[20:21]\n\t" \
    "s_nop 0\n\t" \
    "s_memrealtime s[22:23]\n\t" \
    "s_nop 0\n\t" \
    "s_memrealtime s[24:25]\n\t" \
    "s_nop 0\n\t" \
    "s_memrealtime s[26:27]\n\t" \
    "s_nop 0\n\t" \
    "s_memrealtime s[28:29]\n\t" \
    "s_memtime s[54:55]\n\t" \
    "s_add_u32 s70, s70, 8\n\t" \
    "s_waitcnt lgkmcnt(0)\n\t" \
    "s_sub_u32 s69, s54, s56\n\t" \
    "s_cmp_gt_u32 s69, s70\n\t" \
    "s_cbranch_scc1 hh_E18_%=\n\t" \
    "s_add_u32 s2, s2, s4\n\t" \
    "s_add_u32 s6, s6, s8\n\t" \
    "s_add_u32 s10, s10, s12\n\t" \
    "s_add_u32 s14, s14, s16\n\t" \
    "s_add_u32 s18, s18, s20\n\t" \
    "s_add_u32 s22, s22, s24\n\t" \
    "s_add_u32 s2, s2, s6\n\t" \
    "s_add_u32 s10, s10, s14\n\t" \
    "s_add_u32 s18, s18, s22\n\t" \
    "s_add_u32 s2, s2, s26\n\t" \
    "s_add_u32 s10, s10, s18\n\t" \
    "s_add_u32 s2, s2, s10\n\t" \
    "s_mul_i32 s70, s28, 13\n\t" \
    "s_sub_u32 s70, s70, s2\n\t" \
    "s_min_u32 s70, s70, 52\n\t" \
    "s_mul_i32 s69, s69, s70\n\t" \
    "s_mul_hi_u32 s69, s69, 76695844\n\t" \
    "s_mov_b32 s75, s73\n\t" \
    "s_mov_b32 s76, s74\n\t" \
    "s_add_u32 s73, s56, s69\n\t" \
    "s_lshl_b32 s74, s28, 4\n\t" \
    "s_sub_u32 s69, s73, s75\n\t" \
    "s_sub_u32 s70, s74, s76\n\t" \
    "s_sub_u32 s71, s70, 1600\n\t" \
    "s_cmp_lt_u32 s71, 400000\n\t" \
    "s_cbranch_scc0 hh_K19_%=\n\t" \
    "v_cvt_f32_u32 v34, s69\n\t" \
    "v_cvt_f32_u32 v35, s70\n\t" \
    "v_rcp_f32 v36, v34\n\t" \
    "v_rcp_f32 v37, v35\n\t" \
    "v_mul_f32 v36, v36, v35\n\t" \
    "v_mul_f32 v37, v37, v34\n\t" \
    "v_mul_f32 v36, 0x4f800000, v36\n\t" \
    "v_mul_f32 v37, 0x4f000000, v37\n\t" \
    "v_cvt_u32_f32 v36, v36\n\t" \
    "v_cvt_u32_f32 v37, v37\n\t" \
    "v_readfirstlane_b32 s69, v36\n\t" \
    "v_readfirstlane_b32 s70, v37\n\t" \
    "s_nop 1\n\t" \
    "s_sub_u32 s69, s69, s78\n\t" \
    "s_ashr_i32 s69, s69, 1\n\t" \
    "s_add_u32 s78, s78, s69\n\t" \
    "s_sub_u32 s70, s70, s77\n\t" \
    "s_ashr_i32 s70, s70, 1\n\t" \
    "s_add_u32 s77, s77, s70\n\t" \
    "hh_K19_%=:\n\t" \
    "hh_Hok13_%=:\n\t" \
    "v_max_u32_dpp v30, v30, v30 row_ror:1 row_mask:0xf bank_mask:0xf\n\t" \
    "s_nop 1\n\t" \
    "v_max_u32_dpp v30, v30, v30 row_ror:2 row_mask:0xf bank_mask:0xf\n\t" \
    "s_nop 1\n\t" \
    "v_max_u32_dpp v30, v30, v30 row_ror:4 row_mask:0xf bank_mask:0xf\n\t" \
    "s_nop 1\n\t" \
    "v_max_u32_dpp v30, v30, v30 row_ror:8 row_mask:0xf bank_mask:0xf\n\t" \
    "s_nop 0\n\t" \
    "v_readlane_b32 s69, v30, 0\n\t" \
    "v_readlane_b32 s71, v30, 16\n\t" \
    "s_nop 1\n\t" \
    "s_max_u32 s81, s69, s71\n\t" \
    "s_mul_hi_u32 s69, s85, s78\n\t" \
    "s_add_u32 s79, s81, s69\n\t" \
    "s_add_u32 s79, s79, s87\n\t" \
    "s_cmp_ge_u32 s65, 52\n\t" \
    "s_cselect_b32 s69, 32, 0\n\t" \
    "s_sub_u32 s79, s79, s69\n\t" \
    "s_cmp_lt_u32 s65, 9\n\t" \
    "s_cselect_b32 s69, 48, 0\n\t" \
    "s_add_u32 s79, s79, s69\n\t" \
    "s_add_u32 s80, s79, s88\n\t" \
    "s_add_u32 s65, s65, 1\n\t" \
    "s_cmp_gt_u32 s65, 62\n\t" \
    "s_cbranch_scc1 hh_end10_%=\n\t" \
    "s_cmp_le_u32 s65, 6\n\t" \
    "s_cbranch_scc1 hh_loop8_%=\n\t" \
    "v_lshrrev_b32 v20, 15, v11\n\t" \
    "v_add3_u32 v20, v20, s65, 1\n\t" \
    "v_min_u32 v20, 63, v20\n\t" \
    "v_mul_u32_u24 v20, v20, v11\n\t" \
    "v_xor_b32 v20, v20, v4\n\t" \
    "v_lshrrev_b32 v21, 15, v12\n\t" \
    "v_add3_u32 v21, v21, s65, 1\n\t" \
    "v_min_u32 v21, 63, v21\n\t" \
    "v_mul_u32_u24 v21, v21, v12\n\t" \
    "v_xor_b32 v21, v21, v5\n\t" \
    "v_lshrrev_b32 v22, 15, v13\n\t" \
    "v_add3_u32 v22, v22, s65, 1\n\t" \
    "v_min_u32 v22, 63, v22\n\t" \
    "v_mul_u32_u24 v22, v22, v13\n\t" \
    "v_xor_b32 v22, v22, v6\n\t" \
    "v_lshrrev_b32 v23, 15, v14\n\t" \
    "v_add3_u32 v23, v23, s65, 1\n\t" \
    "v_min_u32 v23, 63, v23\n\t" \
    "v_mul_u32_u24 v23, v23, v14\n\t" \
    "v_xor_b32 v23, v23, v7\n\t" \
    "v_lshrrev_b32 v24, 15, v15\n\t" \
    "v_add3_u32 v24, v24, s65, 1\n\t" \
    "v_min_u32 v24, 63, v24\n\t" \
    "v_mul_u32_u24 v24, v24, v15\n\t" \
    "v_xor_b32 v24, v24, v8\n\t" \
    "v_lshrrev_b32 v25, 15, v16\n\t" \
    "v_add3_u32 v25, v25, s65, 1\n\t" \
    "v_min_u32 v25, 63, v25\n\t" \
    "v_mul_u32_u24 v25, v25, v16\n\t" \
    "v_xor_b32 v25, v25, v9\n\t" \
    "v_lshrrev_b32 v26, 15, v17\n\t" \
    "v_add3_u32 v26, v26, s65, 1\n\t" \
    "v_min_u32 v26, 63, v26\n\t" \
    "v_mul_u32_u24 v26, v26, v17\n\t" \
    "v_xor_b32 v26, v26, v10\n\t" \
    "s_add_u32 s71, s79, s96\n\t" \
    "s_sub_u32 s70, s71, s74\n\t" \
    "s_max_i32 s70, s70, 0\n\t" \
    "s_min_i32 s70, s70, 16000\n\t" \
    "s_lshl_b32 s70, s70, 1\n\t" \
    "s_mul_hi_u32 s70, s70, s77\n\t" \
    "s_add_u32 s70, s70, s73\n\t" \
    "s_getpc_b64 s[30:31]\n\t" \
    "hh_Wp22_%=:\n\t" \
    "s_add_u32 s30, s30, hh_We23_%=-hh_Wp22_%=\n\t" \
    "s_addc_u32 s31, s31, 0\n\t" \
    "s_sub_u32 s70, s70, 96\n\t" \
    "hh_W21_%=:\n\t" \
    "s_memtime s[56:57]\n\t" \
    "s_waitcnt lgkmcnt(0)\n\t" \
    "s_sub_u32 s69, s70, s56\n\t" \
    "s_cmp_gt_i32 s69, 80\n\t" \
    "s_cbranch_scc1 hh_W21_%=\n\t" \
    "s_max_i32 s69, s69, 0\n\t" \
    "s_and_b32 s69, s69, -4\n\t" \
    "s_sub_u32 s30, s30, s69\n\t" \
    "s_subb_u32 s31, s31, 0\n\t" \
    "s_setpc_b64 s[30:31]\n\t" \
    "s_nop 0\n\t" \
    "s_nop 0\n\t" \
    "s_nop 0\n\t" \
    "s_nop 0\n\t" \
    "s_nop 0\n\t" \
    "s_nop 0\n\t" \
    "s_nop 0\n\t" \
    "s_nop 0\n\t" \
    "s_nop 0\n\t" \
    "s_nop 0\n\t" \
    "s_nop 0\n\t" \
    "s_nop 0\n\t" \
    "s_nop 0\n\t" \
    "s_nop 0\n\t" \
    "s_nop 0\n\t" \
    "s_nop 0\n\t" \
    "s_nop 0\n\t" \
    "s_nop 0\n\t" \
    "s_nop 0\n\t" \
    "s_nop 0\n\t" \
    "hh_We23_%=:\n\t" \
    "global_load_lds_dword v26, s[36:37] sc1\n\t" \
    "s_sleep 2\n\t" \
    "s_nop 5\n\t" \
    "global_load_lds_dword v25, s[36:37] sc1\n\t" \
    "s_sleep 2\n\t" \
    "s_nop 5\n\t" \
    "global_load_lds_dword v24, s[36:37] sc1\n\t" \
    "s_sleep 2\n\t" \
    "s_nop 5\n\t" \
    "global_load_lds_dword v23, s[36:37] sc1\n\t" \
    "s_sleep 2\n\t" \
    "s_nop 5\n\t" \
    "global_load_lds_dword v22, s[36:37] sc1\n\t" \
    "s_sleep 2\n\t" \
    "s_nop 5\n\t" \
    "global_load_lds_dword v21, s[36:37] sc1\n\t" \
    "s_sleep 2\n\t" \
    "s_nop 5\n\t" \
    "global_load_lds_dword v20, s[36:37] sc1\n\t" \
    "s_branch hh_loop8_%=\n\t" \
    "hh_end10_%=:\n\t" \
    "s_waitcnt vmcnt(0)\n\t" \
    "s_endpgm\n\t" \
    "hh_last_%=:\n\t" \
    ""
#define ASM_GX \
    "s_load_dwordx2 s[42:43], %3, 48\n\t" \
    "s_load_dwordx2 s[44:45], %3, 40\n\t" \
    "s_load_dwordx2 s[36:37], %3, 0x58\n\t" \
    "s_load_dwordx2 s[50:51], %3, 0x48\n\t" \
    "s_mov_b32 s66, %4\n\t" \
    "s_mov_b32 s67, %5\n\t" \
    "s_mov_b32 s68, 262144\n\t" \
    "s_mov_b32 s72, 0\n\t" \
    "s_mov_b32 s83, -1\n\t" \
    "s_mov_b32 s65, 0\n\t" \
    "s_lshl_b32 s82, s66, 10\n\t" \
    "s_waitcnt lgkmcnt(0)\n\t" \
    "s_mul_i32 s89, s66, 9216\n\t" \
    "s_mov_b32 s90, 0\n\t" \
    "s_add_u32 s46, s42, 2097152\n\t" \
    "s_addc_u32 s47, s43, 0\n\t" \
    "s_add_u32 m0, s82, 65536\n\t" \
    "s_nop 0\n\t" \
    "global_load_lds_dwordx4 v200, s[46:47]\n\t" \
    "s_add_u32 s46, s42, 2228224\n\t" \
    "s_addc_u32 s47, s43, 0\n\t" \
    "s_add_u32 m0, s82, 69632\n\t" \
    "s_nop 0\n\t" \
    "global_load_lds_dwordx4 v200, s[46:47]\n\t" \
    "s_add_u32 s46, s42, 2359296\n\t" \
    "s_addc_u32 s47, s43, 0\n\t" \
    "s_add_u32 m0, s82, 73728\n\t" \
    "s_nop 0\n\t" \
    "global_load_lds_dwordx4 v200, s[46:47]\n\t" \
    "s_add_u32 s46, s42, 2490368\n\t" \
    "s_addc_u32 s47, s43, 0\n\t" \
    "s_add_u32 m0, s82, 77824\n\t" \
    "s_nop 0\n\t" \
    "global_load_lds_dwordx4 v200, s[46:47]\n\t" \
    "s_add_u32 s46, s42, 2621440\n\t" \
    "s_addc_u32 s47, s43, 0\n\t" \
    "s_add_u32 m0, s82, 81920\n\t" \
    "s_nop 0\n\t" \
    "global_load_lds_dwordx4 v200, s[46:47]\n\t" \
    "s_add_u32 s46, s42, 2752512\n\t" \
    "s_addc_u32 s47, s43, 0\n\t" \
    "s_add_u32 m0, s82, 86016\n\t" \
    "s_nop 0\n\t" \
    "global_load_lds_dwordx4 v200, s[46:47]\n\t" \
    "s_add_u32 s46, s42, 2883584\n\t" \
    "s_addc_u32 s47, s43, 0\n\t" \
    "s_add_u32 m0, s82, 90112\n\t" \
    "s_nop 0\n\t" \
    "global_load_lds_dwordx4 v200, s[46:47]\n\t" \
    "s_add_u32 s46, s42, 3014656\n\t" \
    "s_addc_u32 s47, s43, 0\n\t" \
    "s_add_u32 m0, s82, 94208\n\t" \
    "s_nop 0\n\t" \
    "global_load_lds_dwordx4 v200, s[46:47]\n\t" \
    "s_add_u32 s46, s42, 3145728\n\t" \
    "s_addc_u32 s47, s43, 0\n\t" \
    "s_add_u32 m0, s82, 98304\n\t" \
    "s_nop 0\n\t" \
    "global_load_lds_dwordx4 v200, s[46:47]\n\t" \
    "s_add_u32 s46, s42, 3276800\n\t" \
    "s_addc_u32 s47, s43, 0\n\t" \
    "s_add_u32 m0, s82, 102400\n\t" \
    "s_nop 0\n\t" \
    "global_load_lds_dwordx4 v200, s[46:47]\n\t" \
    "s_add_u32 s46, s42, 3407872\n\t" \
    "s_addc_u32 s47, s43, 0\n\t" \
    "s_add_u32 m0, s82, 106496\n\t" \
    "s_nop 0\n\t" \
    "global_load_lds_dwordx4 v200, s[46:47]\n\t" \
    "s_add_u32 s46, s42, 3538944\n\t" \
    "s_addc_u32 s47, s43, 0\n\t" \
    "s_add_u32 m0, s82, 110592\n\t" \
    "s_nop 0\n\t" \
    "global_load_lds_dwordx4 v200, s[46:47]\n\t" \
    "s_add_u32 s46, s42, 3670016\n\t" \
    "s_addc_u32 s47, s43, 0\n\t" \
    "s_add_u32 m0, s82, 114688\n\t" \
    "s_nop 0\n\t" \
    "global_load_lds_dwordx4 v200, s[46:47]\n\t" \
    "s_add_u32 s46, s42, 3801088\n\t" \
    "s_addc_u32 s47, s43, 0\n\t" \
    "s_add_u32 m0, s82, 118784\n\t" \
    "s_nop 0\n\t" \
    "global_load_lds_dwordx4 v200, s[46:47]\n\t" \
    "s_add_u32 s46, s42, 3932160\n\t" \
    "s_addc_u32 s47, s43, 0\n\t" \
    "s_add_u32 m0, s82, 122880\n\t" \
    "s_nop 0\n\t" \
    "global_load_lds_dwordx4 v200, s[46:47]\n\t" \
    "s_add_u32 s46, s42, 4063232\n\t" \
    "s_addc_u32 s47, s43, 0\n\t" \
    "s_add_u32 m0, s82, 126976\n\t" \
    "s_nop 0\n\t" \
    "global_load_lds_dwordx4 v200, s[46:47]\n\t" \
    "s_add_u32 s46, s42, 4194304\n\t" \
    "s_addc_u32 s47, s43, 0\n\t" \
    "s_add_u32 m0, s82, 131072\n\t" \
    "s_nop 0\n\t" \
    "global_load_lds_dwordx4 v200, s[46:47]\n\t" \
    "s_add_u32 s46, s42, 4325376\n\t" \
    "s_addc_u32 s47, s43, 0\n\t" \
    "s_add_u32 m0, s82, 135168\n\t" \
    "s_nop 0\n\t" \
    "global_load_lds_dwordx4 v200, s[46:47]\n\t" \
    "s_add_u32 s46, s42, 4456448\n\t" \
    "s_addc_u32 s47, s43, 0\n\t" \
    "s_add_u32 m0, s82, 139264\n\t" \
    "s_nop 0\n\t" \
    "global_load_lds_dwordx4 v200, s[46:47]\n\t" \
    "s_add_u32 s46, s42, 4587520\n\t" \
    "s_addc_u32 s47, s43, 0\n\t" \
    "s_add_u32 m0, s82, 143360\n\t" \
    "s_nop 0\n\t" \
    "global_load_lds_dwordx4 v200, s[46:47]\n\t" \
    "global_load_dwordx4 v[120:123], v203, s[50:51]\n\t" \
    "global_load_dwordx4 v[124:127], v204, s[50:51]\n\t" \
    "global_load_dwordx4 v[128:131], v205, s[50:51]\n\t" \
    "global_load_dwordx4 v[132:135], v206, s[50:51]\n\t" \
    "global_load_dwordx4 v[136:139], v207, s[50:51]\n\t" \
    "global_load_dwordx4 v[140:143], v208, s[50:51]\n\t" \
    "global_load_dwordx4 v[144:147], v209, s[50:51]\n\t" \
    "global_load_dwordx4 v[148:151], v210, s[50:51]\n\t" \
    "global_load_dwordx4 v[152:155], v211, s[50:51]\n\t" \
    "s_add_u32 s52, s50, 36864\n\t" \
    "s_addc_u32 s53, s51, 0\n\t" \
    "global_load_dwordx4 v[72:75], v203, s[52:53]\n\t" \
    "global_load_dwordx4 v[76:79], v204, s[52:53]\n\t" \
    "global_load_dwordx4 v[80:83], v205, s[52:53]\n\t" \
    "global_load_dwordx4 v[84:87], v206, s[52:53]\n\t" \
    "global_load_dwordx4 v[88:91], v207, s[52:53]\n\t" \
    "global_load_dwordx4 v[92:95], v208, s[52:53]\n\t" \
    "global_load_dwordx4 v[96:99], v209, s[52:53]\n\t" \
    "global_load_dwordx4 v[100:103], v210, s[52:53]\n\t" \
    "global_load_dwordx4 v[104:107], v211, s[52:53]\n\t" \
    "s_waitcnt vmcnt(50) lgkmcnt(0)\n\t" \
    "s_barrier\n\t" \
    "ds_read_b64_tr_b16 a[144:145], v201 offset:0\n\t" \
    "ds_read_b64_tr_b16 a[146:147], v201 offset:1024\n\t" \
    "ds_read_b64_tr_b16 a[148:149], v201 offset:8192\n\t" \
    "ds_read_b64_tr_b16 a[150:151], v201 offset:9216\n\t" \
    "ds_read_b64_tr_b16 a[216:217], v202 offset:0\n\t" \
    "ds_read_b64_tr_b16 a[218:219], v202 offset:1024\n\t" \
    "ds_read_b64_tr_b16 a[220:221], v202 offset:8192\n\t" \
    "ds_read_b64_tr_b16 a[222:223], v202 offset:9216\n\t" \
    "s_waitcnt vmcnt(46) lgkmcnt(0)\n\t" \
    "s_barrier\n\t" \
    "ds_read_b64_tr_b16 a[152:153], v201 offset:16384\n\t" \
    "ds_read_b64_tr_b16 a[154:155], v201 offset:17408\n\t" \
    "ds_read_b64_tr_b16 a[156:157], v201 offset:24576\n\t" \
    "ds_read_b64_tr_b16 a[158:159], v201 offset:25600\n\t" \
    "ds_read_b64_tr_b16 a[224:225], v202 offset:16384\n\t" \
    "ds_read_b64_tr_b16 a[226:227], v202 offset:17408\n\t" \
    "ds_read_b64_tr_b16 a[228:229], v202 offset:24576\n\t" \
    "ds_read_b64_tr_b16 a[230:231], v202 offset:25600\n\t" \
    "s_waitcnt vmcnt(42) lgkmcnt(0)\n\t" \
    "s_barrier\n\t" \
    "ds_read_b64_tr_b16 a[160:161], v201 offset:32768\n\t" \
    "ds_read_b64_tr_b16 a[162:163], v201 offset:33792\n\t" \
    "ds_read_b64_tr_b16 a[164:165], v201 offset:40960\n\t" \
    "ds_read_b64_tr_b16 a[166:167], v201 offset:41984\n\t" \
    "ds_read_b64_tr_b16 a[232:233], v202 offset:32768\n\t" \
    "ds_read_b64_tr_b16 a[234:235], v202 offset:33792\n\t" \
    "ds_read_b64_tr_b16 a[236:237], v202 offset:40960\n\t" \
    "ds_read_b64_tr_b16 a[238:239], v202 offset:41984\n\t" \
    "s_waitcnt vmcnt(38) lgkmcnt(0)\n\t" \
    "s_barrier\n\t" \
    "ds_read_b64_tr_b16 a[168:169], v201 offset:49152\n\t" \
    "ds_read_b64_tr_b16 a[170:171], v201 offset:50176\n\t" \
    "ds_read_b64_tr_b16 a[172:173], v201 offset:57344\n\t" \
    "ds_read_b64_tr_b16 a[174:175], v201 offset:58368\n\t" \
    "ds_read_b64_tr_b16 a[240:241], v202 offset:49152\n\t" \
    "ds_read_b64_tr_b16 a[242:243], v202 offset:50176\n\t" \
    "ds_read_b64_tr_b16 a[244:245], v202 offset:57344\n\t" \
    "ds_read_b64_tr_b16 a[246:247], v202 offset:58368\n\t" \
    "v_mbcnt_lo_u32_b32 v164, -1, 0\n\t" \
    "v_mbcnt_hi_u32_b32 v164, -1, v164\n\t" \
    "v_lshlrev_b32 v164, 6, v164\n\t" \
    "v_add_u32 v164, 6225920, v164\n\t" \
    "v_mov_b32 v165, 6234112\n\t" \
    "global_load_dword v165, v165, s[36:37] sc1\n\t" \
    "global_load_dwordx2 v[166:167], v164, s[36:37] sc1\n\t" \
    "global_load_dword v168, v164, s[36:37] offset:2048 sc1\n\t" \
    "s_waitcnt vmcnt(37) lgkmcnt(0)\n\t" \
    "s_barrier\n\t" \
    "v_add_u32 v201, 0x10000, v201\n\t" \
    "v_add_u32 v202, 0x10000, v202\n\t" \
    "ds_read_b64_tr_b16 a[176:177], v201 offset:0\n\t" \
    "ds_read_b64_tr_b16 a[178:179], v201 offset:1024\n\t" \
    "ds_read_b64_tr_b16 a[180:181], v201 offset:8192\n\t" \
    "ds_read_b64_tr_b16 a[182:183], v201 offset:9216\n\t" \
    "ds_read_b64_tr_b16 a[248:249], v202 offset:0\n\t" \
    "ds_read_b64_tr_b16 a[250:251], v202 offset:1024\n\t" \
    "ds_read_b64_tr_b16 a[252:253], v202 offset:8192\n\t" \
    "ds_read_b64_tr_b16 a[254:255], v202 offset:9216\n\t" \
    "s_waitcnt vmcnt(33) lgkmcnt(0)\n\t" \
    "s_barrier\n\t" \
    "ds_read_b64_tr_b16 a[184:185], v201 offset:16384\n\t" \
    "ds_read_b64_tr_b16 a[186:187], v201 offset:17408\n\t" \
    "ds_read_b64_tr_b16 a[188:189], v201 offset:24576\n\t" \
    "ds_read_b64_tr_b16 a[190:191], v201 offset:25600\n\t" \
    "ds_read_b64_tr_b16 v[224:225], v202 offset:16384\n\t" \
    "ds_read_b64_tr_b16 v[226:227], v202 offset:17408\n\t" \
    "ds_read_b64_tr_b16 v[228:229], v202 offset:24576\n\t" \
    "ds_read_b64_tr_b16 v[230:231], v202 offset:25600\n\t" \
    "s_waitcnt vmcnt(29) lgkmcnt(0)\n\t" \
    "s_barrier\n\t" \
    "ds_read_b64_tr_b16 a[192:193], v201 offset:32768\n\t" \
    "ds_read_b64_tr_b16 a[194:195], v201 offset:33792\n\t" \
    "ds_read_b64_tr_b16 a[196:197], v201 offset:40960\n\t" \
    "ds_read_b64_tr_b16 a[198:199], v201 offset:41984\n\t" \
    "ds_read_b64_tr_b16 v[232:233], v202 offset:32768\n\t" \
    "ds_read_b64_tr_b16 v[234:235], v202 offset:33792\n\t" \
    "ds_read_b64_tr_b16 v[236:237], v202 offset:40960\n\t" \
    "ds_read_b64_tr_b16 v[238:239], v202 offset:41984\n\t" \
    "s_waitcnt vmcnt(25) lgkmcnt(0)\n\t" \
    "s_barrier\n\t" \
    "ds_read_b64_tr_b16 a[200:201], v201 offset:49152\n\t" \
    "ds_read_b64_tr_b16 a[202:203], v201 offset:50176\n\t" \
    "ds_read_b64_tr_b16 a[204:205], v201 offset:57344\n\t" \
    "ds_read_b64_tr_b16 a[206:207], v201 offset:58368\n\t" \
    "ds_read_b64_tr_b16 v[240:241], v202 offset:49152\n\t" \
    "ds_read_b64_tr_b16 v[242:243], v202 offset:50176\n\t" \
    "ds_read_b64_tr_b16 v[244:245], v202 offset:57344\n\t" \
    "ds_read_b64_tr_b16 v[246:247], v202 offset:58368\n\t" \
    "s_waitcnt vmcnt(21) lgkmcnt(0)\n\t" \
    "s_barrier\n\t" \
    "v_add_u32 v201, 0x10000, v201\n\t" \
    "v_add_u32 v202, 0x10000, v202\n\t" \
    "ds_read_b64_tr_b16 a[208:209], v201 offset:0\n\t" \
    "ds_read_b64_tr_b16 a[210:211], v201 offset:1024\n\t" \
    "ds_read_b64_tr_b16 a[212:213], v201 offset:8192\n\t" \
    "ds_read_b64_tr_b16 a[214:215], v201 offset:9216\n\t" \
    "ds_read_b64_tr_b16 v[248:249], v202 offset:0\n\t" \
    "ds_read_b64_tr_b16 v[250:251], v202 offset:1024\n\t" \
    "ds_read_b64_tr_b16 v[252:253], v202 offset:8192\n\t" \
    "ds_read_b64_tr_b16 v[254:255], v202 offset:9216\n\t" \
    "v_add_u32 v201, 0xfffe0000, v201\n\t" \
    "v_add_u32 v202, 0xfffe0000, v202\n\t" \
    "s_waitcnt lgkmcnt(0)\n\t" \
    "s_barrier\n\t" \
    "s_waitcnt vmcnt(0)\n\t" \
    "ds_write_b128 v3, v[120:123] offset:0\n\t" \
    "ds_write_b128 v3, v[124:127] offset:1024\n\t" \
    "ds_write_b128 v3, v[128:131] offset:2048\n\t" \
    "ds_write_b128 v3, v[132:135] offset:3072\n\t" \
    "ds_write_b128 v3, v[136:139] offset:4096\n\t" \
    "ds_write_b128 v3, v[140:143] offset:5120\n\t" \
    "ds_write_b128 v3, v[144:147] offset:6144\n\t" \
    "ds_write_b128 v3, v[148:151] offset:7168\n\t" \
    "ds_write_b128 v3, v[152:155] offset:8192\n\t" \
    "ds_write_b128 v3, v[72:75] offset:36864\n\t" \
    "ds_write_b128 v3, v[76:79] offset:37888\n\t" \
    "ds_write_b128 v3, v[80:83] offset:38912\n\t" \
    "ds_write_b128 v3, v[84:87] offset:39936\n\t" \
    "ds_write_b128 v3, v[88:91] offset:40960\n\t" \
    "ds_write_b128 v3, v[92:95] offset:41984\n\t" \
    "ds_write_b128 v3, v[96:99] offset:43008\n\t" \
    "ds_write_b128 v3, v[100:103] offset:44032\n\t" \
    "ds_write_b128 v3, v[104:107] offset:45056\n\t" \
    "gx_C1_%=:\n\t" \
    "v_cmp_ne_u32 s[54:55], v166, v168\n\t" \
    "v_sub_u32 v166, v166, v165\n\t" \
    "v_cmp_ne_u32 vcc, 1, v166\n\t" \
    "s_nop 1\n\t" \
    "s_or_b64 vcc, vcc, s[54:55]\n\t" \
    "s_cbranch_scc0 gx_Cok2_%=\n\t" \
    "s_cmp_eq_u32 s68, 0\n\t" \
    "s_cbranch_scc1 gx_F4_%=\n\t" \
    "s_sub_u32 s68, s68, 1\n\t" \
    "s_sleep 6\n\t" \
    "s_branch gx_Cag3_%=\n\t" \
    "gx_F4_%=:\n\t" \
    "v_mov_b32 v168, 5767424\n\t" \
    "v_mov_b32 v169, 4\n\t" \
    "global_store_dword v168, v169, s[36:37] sc1\n\t" \
    "s_waitcnt vmcnt(0)\n\t" \
    "s_branch gx_Cok2_%=\n\t" \
    "gx_Cag3_%=:\n\t" \
    "global_load_dwordx2 v[166:167], v164, s[36:37] sc1\n\t" \
    "global_load_dword v168, v164, s[36:37] offset:2048 sc1\n\t" \
    "s_waitcnt vmcnt(0)\n\t" \
    "s_branch gx_C1_%=\n\t" \
    "gx_Cok2_%=:\n\t" \
    "s_cmp_lg_u32 s66, 0\n\t" \
    "s_cbranch_scc1 gx_Ca5_%=\n\t" \
    "v_mov_b32 v164, 5836800\n\t" \
    "s_mov_b64 exec, 1\n\t" \
    "global_atomic_add v164, v166, s[36:37] sc1\n\t" \
    "s_mov_b64 exec, -1\n\t" \
    "gx_Ca5_%=:\n\t" \
    "s_waitcnt lgkmcnt(0)\n\t" \
    "s_barrier\n\t" \
    "ds_read_b128 v[24:27], v1 offset:0\n\t" \
    "ds_read_b128 v[28:31], v1 offset:18432\n\t" \
    "ds_read_b128 v[32:35], v2 offset:0\n\t" \
    "ds_read_b128 v[36:39], v2 offset:18432\n\t" \
    "ds_read_b128 v[40:43], v1 offset:128\n\t" \
    "ds_read_b128 v[44:47], v1 offset:18560\n\t" \
    "gx_l16_%=:\n\t" \
    "s_add_u32 s84, s65, 2\n\t" \
    "s_min_u32 s69, s84, 63\n\t" \
    "s_mul_i32 s69, s69, 36864\n\t" \
    "s_add_u32 s52, s50, s69\n\t" \
    "s_addc_u32 s53, s51, 0\n\t" \
    "s_add_u32 s90, s90, 36864\n\t" \
    "s_cmp_eq_u32 s90, 147456\n\t" \
    "s_cselect_b32 s90, 0, s90\n\t" \
    "s_add_u32 s91, s90, 36864\n\t" \
    "s_cmp_eq_u32 s91, 147456\n\t" \
    "s_cselect_b32 s91, 0, s91\n\t" \
    "s_add_u32 s91, s91, s89\n\t" \
    "v_add_u32 v22, s90, v0\n\t" \
    "v_xor_b32 v23, 64, v22\n\t" \
    "s_mul_i32 s70, s67, 7\n\t" \
    "s_add_u32 s69, s65, 0\n\t" \
    "s_xor_b32 s69, s69, s70\n\t" \
    "s_and_b32 s69, s69, 63\n\t" \
    "s_lshl_b32 s69, s69, 13\n\t" \
    "s_mul_i32 s70, s67, 524288\n\t" \
    "s_add_u32 s69, s69, s70\n\t" \
    "s_add_u32 s69, s69, 23068672\n\t" \
    "s_add_u32 s40, s36, s69\n\t" \
    "s_addc_u32 s41, s37, 0\n\t" \
    "s_waitcnt lgkmcnt(5)\n\t" \
    "v_mfma_f32_16x16x32_bf16 v[72:75], a[144:147], v[24:27], 0\n\t" \
    "ds_read_b128 v[48:51], v2 offset:128\n\t" \
    "s_waitcnt lgkmcnt(5)\n\t" \
    "v_mfma_f32_16x16x32_bf16 v[76:79], a[144:147], v[28:31], 0\n\t" \
    "ds_read_b128 v[52:55], v2 offset:18560\n\t" \
    "v_mfma_f32_16x16x32_bf16 v[80:83], a[216:219], v[24:27], 0\n\t" \
    "s_add_u32 m0, s91, 0\n\t" \
    "v_mfma_f32_16x16x32_bf16 v[84:87], a[216:219], v[28:31], 0\n\t" \
    "global_load_lds_dwordx4 v203, s[52:53]\n\t" \
    "s_waitcnt lgkmcnt(5)\n\t" \
    "v_mfma_f32_16x16x32_bf16 v[72:75], a[148:151], v[32:35], v[72:75]\n\t" \
    "ds_read_b128 v[56:59], v1 offset:256\n\t" \
    "s_waitcnt lgkmcnt(5)\n\t" \
    "v_mfma_f32_16x16x32_bf16 v[76:79], a[148:151], v[36:39], v[76:79]\n\t" \
    "ds_read_b128 v[60:63], v1 offset:18688\n\t" \
    "v_mfma_f32_16x16x32_bf16 v[80:83], a[220:223], v[32:35], v[80:83]\n\t" \
    "s_add_u32 m0, s91, 1024\n\t" \
    "v_mfma_f32_16x16x32_bf16 v[84:87], a[220:223], v[36:39], v[84:87]\n\t" \
    "global_load_lds_dwordx4 v204, s[52:53]\n\t" \
    "s_waitcnt lgkmcnt(5)\n\t" \
    "v_mfma_f32_16x16x32_bf16 v[72:75], a[152:155], v[40:43], v[72:75]\n\t" \
    "ds_read_b128 v[64:67], v2 offset:256\n\t" \
    "s_waitcnt lgkmcnt(5)\n\t" \
    "v_mfma_f32_16x16x32_bf16 v[76:79], a[152:155], v[44:47], v[76:79]\n\t" \
    "ds_read_b128 v[68:71], v2 offset:18688\n\t" \
    "v_mfma_f32_16x16x32_bf16 v[80:83], a[224:227], v[40:43], v[80:83]\n\t" \
    "s_add_u32 m0, s91, 2048\n\t" \
    "v_mfma_f32_16x16x32_bf16 v[84:87], a[224:227], v[44:47], v[84:87]\n\t" \
    "global_load_lds_dwordx4 v205, s[52:53]\n\t" \
    "s_waitcnt lgkmcnt(5)\n\t" \
    "v_mfma_f32_16x16x32_bf16 v[72:75], a[156:159], v[48:51], v[72:75]\n\t" \
    "ds_read_b128 v[24:27], v1 offset:384\n\t" \
    "s_waitcnt lgkmcnt(5)\n\t" \
    "v_mfma_f32_16x16x32_bf16 v[76:79], a[156:159], v[52:55], v[76:79]\n\t" \
    "ds_read_b128 v[28:31], v1 offset:18816\n\t" \
    "v_mfma_f32_16x16x32_bf16 v[80:83], a[228:231], v[48:51], v[80:83]\n\t" \
    "s_add_u32 m0, s91, 3072\n\t" \
    "v_mfma_f32_16x16x32_bf16 v[84:87], a[228:231], v[52:55], v[84:87]\n\t" \
    "global_load_lds_dwordx4 v206, s[52:53]\n\t" \
    "s_waitcnt lgkmcnt(5)\n\t" \
    "v_mfma_f32_16x16x32_bf16 v[72:75], a[160:163], v[56:59], v[72:75]\n\t" \
    "ds_read_b128 v[32:35], v2 offset:384\n\t" \
    "s_waitcnt lgkmcnt(5)\n\t" \
    "v_mfma_f32_16x16x32_bf16 v[76:79], a[160:163], v[60:63], v[76:79]\n\t" \
    "ds_read_b128 v[36:39], v2 offset:18816\n\t" \
    "v_mfma_f32_16x16x32_bf16 v[80:83], a[232:235], v[56:59], v[80:83]\n\t" \
    "s_add_u32 m0, s91, 4096\n\t" \
    "v_mfma_f32_16x16x32_bf16 v[84:87], a[232:235], v[60:63], v[84:87]\n\t" \
    "global_load_lds_dwordx4 v207, s[52:53]\n\t" \
    "s_waitcnt lgkmcnt(5)\n\t" \
    "v_mfma_f32_16x16x32_bf16 v[72:75], a[164:167], v[64:67], v[72:75]\n\t" \
    "ds_read_b128 v[40:43], v1 offset:512\n\t" \
    "s_waitcnt lgkmcnt(5)\n\t" \
    "v_mfma_f32_16x16x32_bf16 v[76:79], a[164:167], v[68:71], v[76:79]\n\t" \
    "ds_read_b128 v[44:47], v1 offset:18944\n\t" \
    "v_mfma_f32_16x16x32_bf16 v[80:83], a[236:239], v[64:67], v[80:83]\n\t" \
    "s_add_u32 m0, s91, 5120\n\t" \
    "v_mfma_f32_16x16x32_bf16 v[84:87], a[236:239], v[68:71], v[84:87]\n\t" \
    "global_load_lds_dwordx4 v208, s[52:53]\n\t" \
    "s_waitcnt lgkmcnt(5)\n\t" \
    "v_mfma_f32_16x16x32_bf16 v[72:75], a[168:171], v[24:27], v[72:75]\n\t" \
    "ds_read_b128 v[48:51], v2 offset:512\n\t" \
    "s_waitcnt lgkmcnt(5)\n\t" \
    "v_mfma_f32_16x16x32_bf16 v[76:79], a[168:171], v[28:31], v[76:79]\n\t" \
    "ds_read_b128 v[52:55], v2 offset:18944\n\t" \
    "v_mfma_f32_16x16x32_bf16 v[80:83], a[240:243], v[24:27], v[80:83]\n\t" \
    "s_add_u32 m0, s91, 6144\n\t" \
    "v_mfma_f32_16x16x32_bf16 v[84:87], a[240:243], v[28:31], v[84:87]\n\t" \
    "global_load_lds_dwordx4 v209, s[52:53]\n\t" \
    "s_waitcnt lgkmcnt(5)\n\t" \
    "v_mfma_f32_16x16x32_bf16 v[72:75], a[172:175], v[32:35], v[72:75]\n\t" \
    "ds_read_b128 v[56:59], v1 offset:640\n\t" \
    "s_waitcnt lgkmcnt(5)\n\t" \
    "v_mfma_f32_16x16x32_bf16 v[76:79], a[172:175], v[36:39], v[76:79]\n\t" \
    "ds_read_b128 v[60:63], v1 offset:19072\n\t" \
    "v_mfma_f32_16x16x32_bf16 v[80:83], a[244:247], v[32:35], v[80:83]\n\t" \
    "s_add_u32 m0, s91, 7168\n\t" \
    "v_mfma_f32_16x16x32_bf16 v[84:87], a[244:247], v[36:39], v[84:87]\n\t" \
    "global_load_lds_dwordx4 v210, s[52:53]\n\t" \
    "s_waitcnt lgkmcnt(5)\n\t" \
    "v_mfma_f32_16x16x32_bf16 v[72:75], a[176:179], v[40:43], v[72:75]\n\t" \
    "ds_read_b128 v[64:67], v2 offset:640\n\t" \
    "s_waitcnt lgkmcnt(5)\n\t" \
    "v_mfma_f32_16x16x32_bf16 v[76:79], a[176:179], v[44:47], v[76:79]\n\t" \
    "ds_read_b128 v[68:71], v2 offset:19072\n\t" \
    "v_mfma_f32_16x16x32_bf16 v[80:83], a[248:251], v[40:43], v[80:83]\n\t" \
    "s_add_u32 m0, s91, 8192\n\t" \
    "v_mfma_f32_16x16x32_bf16 v[84:87], a[248:251], v[44:47], v[84:87]\n\t" \
    "global_load_lds_dwordx4 v211, s[52:53]\n\t" \
    "s_waitcnt lgkmcnt(5)\n\t" \
    "v_mfma_f32_16x16x32_bf16 v[72:75], a[180:183], v[48:51], v[72:75]\n\t" \
    "ds_read_b128 v[24:27], v1 offset:768\n\t" \
    "s_waitcnt lgkmcnt(5)\n\t" \
    "v_mfma_f32_16x16x32_bf16 v[76:79], a[180:183], v[52:55], v[76:79]\n\t" \
    "ds_read_b128 v[28:31], v1 offset:19200\n\t" \
    "v_mfma_f32_16x16x32_bf16 v[80:83], a[252:255], v[48:51], v[80:83]\n\t" \
    "v_mfma_f32_16x16x32_bf16 v[84:87], a[252:255], v[52:55], v[84:87]\n\t" \
    "s_waitcnt lgkmcnt(5)\n\t" \
    "v_mfma_f32_16x16x32_bf16 v[72:75], a[184:187], v[56:59], v[72:75]\n\t" \
    "ds_read_b128 v[32:35], v2 offset:768\n\t" \
    "s_waitcnt lgkmcnt(5)\n\t" \
    "v_mfma_f32_16x16x32_bf16 v[76:79], a[184:187], v[60:63], v[76:79]\n\t" \
    "ds_read_b128 v[36:39], v2 offset:19200\n\t" \
    "v_mfma_f32_16x16x32_bf16 v[80:83], v[224:227], v[56:59], v[80:83]\n\t" \
    "s_waitcnt vmcnt(9)\n\t" \
    "s_barrier\n\t" \
    "v_mfma_f32_16x16x32_bf16 v[84:87], v[224:227], v[60:63], v[84:87]\n\t" \
    "s_cmp_lg_u32 s66, 0\n\t" \
    "s_cbranch_scc1 gx_SF9_%=\n\t" \
    "s_lshl_b32 s69, s67, 7\n\t" \
    "s_add_u32 s69, s69, 5771264\n\t" \
    "s_add_u32 s70, s65, -1\n\t" \
    "v_mov_b32 v156, s69\n\t" \
    "v_mov_b32 v157, s70\n\t" \
    "s_mov_b64 exec, 1\n\t" \
    "global_store_dword v156, v157, s[36:37] sc1\n\t" \
    "s_mov_b64 exec, -1\n\t" \
    "gx_SF9_%=:\n\t" \
    "s_waitcnt lgkmcnt(5)\n\t" \
    "v_mfma_f32_16x16x32_bf16 v[72:75], a[188:191], v[64:67], v[72:75]\n\t" \
    "ds_read_b128 v[40:43], v1 offset:896\n\t" \
    "s_waitcnt lgkmcnt(5)\n\t" \
    "v_mfma_f32_16x16x32_bf16 v[76:79], a[188:191], v[68:71], v[76:79]\n\t" \
    "ds_read_b128 v[44:47], v1 offset:19328\n\t" \
    "v_mfma_f32_16x16x32_bf16 v[80:83], v[228:231], v[64:67], v[80:83]\n\t" \
    "v_mfma_f32_16x16x32_bf16 v[84:87], v[228:231], v[68:71], v[84:87]\n\t" \
    "s_waitcnt lgkmcnt(5)\n\t" \
    "v_mfma_f32_16x16x32_bf16 v[72:75], a[192:195], v[24:27], v[72:75]\n\t" \
    "ds_read_b128 v[48:51], v2 offset:896\n\t" \
    "s_waitcnt lgkmcnt(5)\n\t" \
    "v_mfma_f32_16x16x32_bf16 v[76:79], a[192:195], v[28:31], v[76:79]\n\t" \
    "ds_read_b128 v[52:55], v2 offset:19328\n\t" \
    "v_mfma_f32_16x16x32_bf16 v[80:83], v[232:235], v[24:27], v[80:83]\n\t" \
    "v_mfma_f32_16x16x32_bf16 v[84:87], v[232:235], v[28:31], v[84:87]\n\t" \
    "s_waitcnt lgkmcnt(5)\n\t" \
    "v_mfma_f32_16x16x32_bf16 v[72:75], a[196:199], v[32:35], v[72:75]\n\t" \
    "ds_read_b128 v[56:59], v1 offset:1024\n\t" \
    "s_waitcnt lgkmcnt(5)\n\t" \
    "v_mfma_f32_16x16x32_bf16 v[76:79], a[196:199], v[36:39], v[76:79]\n\t" \
    "ds_read_b128 v[60:63], v1 offset:19456\n\t" \
    "v_mfma_f32_16x16x32_bf16 v[80:83], v[236:239], v[32:35], v[80:83]\n\t" \
    "v_mfma_f32_16x16x32_bf16 v[84:87], v[236:239], v[36:39], v[84:87]\n\t" \
    "s_waitcnt lgkmcnt(5)\n\t" \
    "v_mfma_f32_16x16x32_bf16 v[72:75], a[200:203], v[40:43], v[72:75]\n\t" \
    "ds_read_b128 v[64:67], v2 offset:1024\n\t" \
    "s_waitcnt lgkmcnt(5)\n\t" \
    "v_mfma_f32_16x16x32_bf16 v[76:79], a[200:203], v[44:47], v[76:79]\n\t" \
    "ds_read_b128 v[68:71], v2 offset:19456\n\t" \
    "v_mfma_f32_16x16x32_bf16 v[80:83], v[240:243], v[40:43], v[80:83]\n\t" \
    "v_mfma_f32_16x16x32_bf16 v[84:87], v[240:243], v[44:47], v[84:87]\n\t" \
    "s_waitcnt lgkmcnt(5)\n\t" \
    "v_mfma_f32_16x16x32_bf16 v[72:75], a[204:207], v[48:51], v[72:75]\n\t" \
    "ds_read_b128 v[24:27], v22 offset:0\n\t" \
    "s_waitcnt lgkmcnt(5)\n\t" \
    "v_mfma_f32_16x16x32_bf16 v[76:79], a[204:207], v[52:55], v[76:79]\n\t" \
    "ds_read_b128 v[28:31], v22 offset:18432\n\t" \
    "v_mfma_f32_16x16x32_bf16 v[80:83], v[244:247], v[48:51], v[80:83]\n\t" \
    "v_mfma_f32_16x16x32_bf16 v[84:87], v[244:247], v[52:55], v[84:87]\n\t" \
    "s_waitcnt lgkmcnt(5)\n\t" \
    "v_mfma_f32_16x16x32_bf16 v[72:75], a[208:211], v[56:59], v[72:75]\n\t" \
    "ds_read_b128 v[32:35], v23 offset:0\n\t" \
    "s_waitcnt lgkmcnt(5)\n\t" \
    "v_mfma_f32_16x16x32_bf16 v[76:79], a[208:211], v[60:63], v[76:79]\n\t" \
    "ds_read_b128 v[36:39], v23 offset:18432\n\t" \
    "v_mfma_f32_16x16x32_bf16 v[80:83], v[248:251], v[56:59], v[80:83]\n\t" \
    "v_mfma_f32_16x16x32_bf16 v[84:87], v[248:251], v[60:63], v[84:87]\n\t" \
    "s_waitcnt lgkmcnt(5)\n\t" \
    "v_mfma_f32_16x16x32_bf16 v[72:75], a[212:215], v[64:67], v[72:75]\n\t" \
    "ds_read_b128 v[40:43], v22 offset:128\n\t" \
    "s_waitcnt lgkmcnt(5)\n\t" \
    "v_mfma_f32_16x16x32_bf16 v[76:79], a[212:215], v[68:71], v[76:79]\n\t" \
    "ds_read_b128 v[44:47], v22 offset:18560\n\t" \
    "v_mfma_f32_16x16x32_bf16 v[80:83], v[252:255], v[64:67], v[80:83]\n\t" \
    "v_mfma_f32_16x16x32_bf16 v[84:87], v[252:255], v[68:71], v[84:87]\n\t" \
    "v_mov_b32 v1, v22\n\t" \
    "v_mov_b32 v2, v23\n\t" \
    "v_cvt_pk_bf16_f32 v104, v72, v73\n\t" \
    "v_cvt_pk_bf16_f32 v105, v74, v75\n\t" \
    "s_nop 2\n\t" \
    "v_cvt_pk_bf16_f32 v106, v80, v81\n\t" \
    "v_cvt_pk_bf16_f32 v107, v82, v83\n\t" \
    "global_store_dwordx4 v8, v[104:107], s[40:41] offset:0 sc1\n\t" \
    "v_cvt_pk_bf16_f32 v108, v76, v77\n\t" \
    "v_cvt_pk_bf16_f32 v109, v78, v79\n\t" \
    "v_cvt_pk_bf16_f32 v110, v84, v85\n\t" \
    "v_cvt_pk_bf16_f32 v111, v86, v87\n\t" \
    "global_store_dwordx4 v8, v[108:111], s[40:41] offset:1024 sc1\n\t" \
    "s_cmp_ge_u32 s65, 1\n\t" \
    "s_cbranch_scc1 gx_nofast7_%=\n\t" \
    "s_waitcnt vmcnt(0)\n\t" \
    "s_barrier\n\t" \
    "s_cmp_lg_u32 s66, 0\n\t" \
    "s_cbranch_scc1 gx_SF10_%=\n\t" \
    "s_lshl_b32 s69, s67, 7\n\t" \
    "s_add_u32 s69, s69, 5771264\n\t" \
    "s_add_u32 s70, s65, 0\n\t" \
    "v_mov_b32 v156, s69\n\t" \
    "v_mov_b32 v157, s70\n\t" \
    "s_mov_b64 exec, 1\n\t" \
    "global_store_dword v156, v157, s[36:37] sc1\n\t" \
    "s_mov_b64 exec, -1\n\t" \
    "gx_SF10_%=:\n\t" \
    "gx_nofast7_%=:\n\t" \
    "s_sub_u32 s69, s65, 4\n\t" \
    "s_cmp_gt_u32 s69, 4\n\t" \
    "s_cbranch_scc1 gx_nt11_%=\n\t" \
    "s_lshl_b32 s69, s69, 7\n\t" \
    "s_lshl_b32 s70, s66, 5\n\t" \
    "s_add_u32 s69, s69, s70\n\t" \
    "v_lshrrev_b32 v156, 3, v6\n\t" \
    "v_add_u32 v156, s69, v156\n\t" \
    "v_min_u32 v156, 575, v156\n\t" \
    "v_and_b32 v157, 4, v6\n\t" \
    "v_lshlrev_b32 v157, 5, v157\n\t" \
    "v_lshl_add_u32 v156, v156, 13, v157\n\t" \
    "s_lshl_b32 s69, s67, 8\n\t" \
    "v_add_u32 v156, s69, v156\n\t" \
    "s_mov_b32 m0, 155648\n\t" \
    "s_nop 0\n\t" \
    "global_load_lds_dword v156, s[44:45]\n\t" \
    "gx_nt11_%=:\n\t" \
    "s_add_u32 s65, s65, 1\n\t" \
    "s_cmp_lt_u32 s65, 9\n\t" \
    "s_cbranch_scc1 gx_l16_%=\n\t" \
    "s_waitcnt vmcnt(0) lgkmcnt(0)\n\t" \
    "s_barrier\n\t" \
    "s_cmp_lg_u32 s66, 0\n\t" \
    "s_cbranch_scc1 gx_SF12_%=\n\t" \
    "s_lshl_b32 s69, s67, 7\n\t" \
    "s_add_u32 s69, s69, 5771264\n\t" \
    "s_add_u32 s70, s65, -1\n\t" \
    "v_mov_b32 v156, s69\n\t" \
    "v_mov_b32 v157, s70\n\t" \
    "s_mov_b64 exec, 1\n\t" \
    "global_store_dword v156, v157, s[36:37] sc1\n\t" \
    "s_mov_b64 exec, -1\n\t" \
    "gx_SF12_%=:\n\t" \
    "s_add_u32 s46, s44, 0\n\t" \
    "s_addc_u32 s47, s45, 0\n\t" \
    "s_add_u32 m0, s82, 0\n\t" \
    "s_nop 0\n\t" \
    "global_load_lds_dwordx4 v200, s[46:47]\n\t" \
    "s_add_u32 s46, s44, 131072\n\t" \
    "s_addc_u32 s47, s45, 0\n\t" \
    "s_add_u32 m0, s82, 4096\n\t" \
    "s_nop 0\n\t" \
    "global_load_lds_dwordx4 v200, s[46:47]\n\t" \
    "s_add_u32 s46, s44, 262144\n\t" \
    "s_addc_u32 s47, s45, 0\n\t" \
    "s_add_u32 m0, s82, 8192\n\t" \
    "s_nop 0\n\t" \
    "global_load_lds_dwordx4 v200, s[46:47]\n\t" \
    "s_add_u32 s46, s44, 393216\n\t" \
    "s_addc_u32 s47, s45, 0\n\t" \
    "s_add_u32 m0, s82, 12288\n\t" \
    "s_nop 0\n\t" \
    "global_load_lds_dwordx4 v200, s[46:47]\n\t" \
    "s_add_u32 s46, s44, 524288\n\t" \
    "s_addc_u32 s47, s45, 0\n\t" \
    "s_add_u32 m0, s82, 16384\n\t" \
    "s_nop 0\n\t" \
    "global_load_lds_dwordx4 v200, s[46:47]\n\t" \
    "s_add_u32 s46, s44, 655360\n\t" \
    "s_addc_u32 s47, s45, 0\n\t" \
    "s_add_u32 m0, s82, 20480\n\t" \
    "s_nop 0\n\t" \
    "global_load_lds_dwordx4 v200, s[46:47]\n\t" \
    "s_add_u32 s46, s44, 786432\n\t" \
    "s_addc_u32 s47, s45, 0\n\t" \
    "s_add_u32 m0, s82, 24576\n\t" \
    "s_nop 0\n\t" \
    "global_load_lds_dwordx4 v200, s[46:47]\n\t" \
    "s_add_u32 s46, s44, 917504\n\t" \
    "s_addc_u32 s47, s45, 0\n\t" \
    "s_add_u32 m0, s82, 28672\n\t" \
    "s_nop 0\n\t" \
    "global_load_lds_dwordx4 v200, s[46:47]\n\t" \
    "s_add_u32 s46, s44, 1048576\n\t" \
    "s_addc_u32 s47, s45, 0\n\t" \
    "s_add_u32 m0, s82, 32768\n\t" \
    "s_nop 0\n\t" \
    "global_load_lds_dwordx4 v200, s[46:47]\n\t" \
    "s_add_u32 s46, s44, 1179648\n\t" \
    "s_addc_u32 s47, s45, 0\n\t" \
    "s_add_u32 m0, s82, 36864\n\t" \
    "s_nop 0\n\t" \
    "global_load_lds_dwordx4 v200, s[46:47]\n\t" \
    "s_add_u32 s46, s44, 1310720\n\t" \
    "s_addc_u32 s47, s45, 0\n\t" \
    "s_add_u32 m0, s82, 40960\n\t" \
    "s_nop 0\n\t" \
    "global_load_lds_dwordx4 v200, s[46:47]\n\t" \
    "s_add_u32 s46, s44, 1441792\n\t" \
    "s_addc_u32 s47, s45, 0\n\t" \
    "s_add_u32 m0, s82, 45056\n\t" \
    "s_nop 0\n\t" \
    "global_load_lds_dwordx4 v200, s[46:47]\n\t" \
    "s_add_u32 s46, s44, 1572864\n\t" \
    "s_addc_u32 s47, s45, 0\n\t" \
    "s_add_u32 m0, s82, 49152\n\t" \
    "s_nop 0\n\t" \
    "global_load_lds_dwordx4 v200, s[46:47]\n\t" \
    "s_add_u32 s46, s44, 1703936\n\t" \
    "s_addc_u32 s47, s45, 0\n\t" \
    "s_add_u32 m0, s82, 53248\n\t" \
    "s_nop 0\n\t" \
    "global_load_lds_dwordx4 v200, s[46:47]\n\t" \
    "s_add_u32 s46, s44, 1835008\n\t" \
    "s_addc_u32 s47, s45, 0\n\t" \
    "s_add_u32 m0, s82, 57344\n\t" \
    "s_nop 0\n\t" \
    "global_load_lds_dwordx4 v200, s[46:47]\n\t" \
    "s_add_u32 s46, s44, 1966080\n\t" \
    "s_addc_u32 s47, s45, 0\n\t" \
    "s_add_u32 m0, s82, 61440\n\t" \
    "s_nop 0\n\t" \
    "global_load_lds_dwordx4 v200, s[46:47]\n\t" \
    "s_add_u32 s46, s44, 2097152\n\t" \
    "s_addc_u32 s47, s45, 0\n\t" \
    "s_add_u32 m0, s82, 65536\n\t" \
    "s_nop 0\n\t" \
    "global_load_lds_dwordx4 v200, s[46:47]\n\t" \
    "s_add_u32 s46, s44, 2228224\n\t" \
    "s_addc_u32 s47, s45, 0\n\t" \
    "s_add_u32 m0, s82, 69632\n\t" \
    "s_nop 0\n\t" \
    "global_load_lds_dwordx4 v200, s[46:47]\n\t" \
    "s_add_u32 s46, s44, 2359296\n\t" \
    "s_addc_u32 s47, s45, 0\n\t" \
    "s_add_u32 m0, s82, 73728\n\t" \
    "s_nop 0\n\t" \
    "global_load_lds_dwordx4 v200, s[46:47]\n\t" \
    "s_add_u32 s46, s44, 2490368\n\t" \
    "s_addc_u32 s47, s45, 0\n\t" \
    "s_add_u32 m0, s82, 77824\n\t" \
    "s_nop 0\n\t" \
    "global_load_lds_dwordx4 v200, s[46:47]\n\t" \
    "s_add_u32 s46, s44, 2621440\n\t" \
    "s_addc_u32 s47, s45, 0\n\t" \
    "s_add_u32 m0, s82, 81920\n\t" \
    "s_nop 0\n\t" \
    "global_load_lds_dwordx4 v200, s[46:47]\n\t" \
    "s_add_u32 s46, s44, 2752512\n\t" \
    "s_addc_u32 s47, s45, 0\n\t" \
    "s_add_u32 m0, s82, 86016\n\t" \
    "s_nop 0\n\t" \
    "global_load_lds_dwordx4 v200, s[46:47]\n\t" \
    "s_add_u32 s46, s44, 2883584\n\t" \
    "s_addc_u32 s47, s45, 0\n\t" \
    "s_add_u32 m0, s82, 90112\n\t" \
    "s_nop 0\n\t" \
    "global_load_lds_dwordx4 v200, s[46:47]\n\t" \
    "s_add_u32 s46, s44, 3014656\n\t" \
    "s_addc_u32 s47, s45, 0\n\t" \
    "s_add_u32 m0, s82, 94208\n\t" \
    "s_nop 0\n\t" \
    "global_load_lds_dwordx4 v200, s[46:47]\n\t" \
    "s_add_u32 s46, s44, 3145728\n\t" \
    "s_addc_u32 s47, s45, 0\n\t" \
    "s_add_u32 m0, s82, 98304\n\t" \
    "s_nop 0\n\t" \
    "global_load_lds_dwordx4 v200, s[46:47]\n\t" \
    "s_add_u32 s46, s44, 3276800\n\t" \
    "s_addc_u32 s47, s45, 0\n\t" \
    "s_add_u32 m0, s82, 102400\n\t" \
    "s_nop 0\n\t" \
    "global_load_lds_dwordx4 v200, s[46:47]\n\t" \
    "s_add_u32 s46, s44, 3407872\n\t" \
    "s_addc_u32 s47, s45, 0\n\t" \
    "s_add_u32 m0, s82, 106496\n\t" \
    "s_nop 0\n\t" \
    "global_load_lds_dwordx4 v200, s[46:47]\n\t" \
    "s_add_u32 s46, s44, 3538944\n\t" \
    "s_addc_u32 s47, s45, 0\n\t" \
    "s_add_u32 m0, s82, 110592\n\t" \
    "s_nop 0\n\t" \
    "global_load_lds_dwordx4 v200, s[46:47]\n\t" \
    "s_add_u32 s46, s44, 3670016\n\t" \
    "s_addc_u32 s47, s45, 0\n\t" \
    "s_add_u32 m0, s82, 114688\n\t" \
    "s_nop 0\n\t" \
    "global_load_lds_dwordx4 v200, s[46:47]\n\t" \
    "s_add_u32 s46, s44, 3801088\n\t" \
    "s_addc_u32 s47, s45, 0\n\t" \
    "s_add_u32 m0, s82, 118784\n\t" \
    "s_nop 0\n\t" \
    "global_load_lds_dwordx4 v200, s[46:47]\n\t" \
    "s_add_u32 s46, s44, 3932160\n\t" \
    "s_addc_u32 s47, s45, 0\n\t" \
    "s_add_u32 m0, s82, 122880\n\t" \
    "s_nop 0\n\t" \
    "global_load_lds_dwordx4 v200, s[46:47]\n\t" \
    "s_add_u32 s46, s44, 4063232\n\t" \
    "s_addc_u32 s47, s45, 0\n\t" \
    "s_add_u32 m0, s82, 126976\n\t" \
    "s_nop 0\n\t" \
    "global_load_lds_dwordx4 v200, s[46:47]\n\t" \
    "s_add_u32 s46, s44, 4194304\n\t" \
    "s_addc_u32 s47, s45, 0\n\t" \
    "s_add_u32 m0, s82, 131072\n\t" \
    "s_nop 0\n\t" \
    "global_load_lds_dwordx4 v200, s[46:47]\n\t" \
    "s_add_u32 s46, s44, 4325376\n\t" \
    "s_addc_u32 s47, s45, 0\n\t" \
    "s_add_u32 m0, s82, 135168\n\t" \
    "s_nop 0\n\t" \
    "global_load_lds_dwordx4 v200, s[46:47]\n\t" \
    "s_add_u32 s46, s44, 4456448\n\t" \
    "s_addc_u32 s47, s45, 0\n\t" \
    "s_add_u32 m0, s82, 139264\n\t" \
    "s_nop 0\n\t" \
    "global_load_lds_dwordx4 v200, s[46:47]\n\t" \
    "s_add_u32 s46, s44, 4587520\n\t" \
    "s_addc_u32 s47, s45, 0\n\t" \
    "s_add_u32 m0, s82, 143360\n\t" \
    "s_nop 0\n\t" \
    "global_load_lds_dwordx4 v200, s[46:47]\n\t" \
    "global_load_dwordx4 v[120:123], v203, s[50:51]\n\t" \
    "global_load_dwordx4 v[124:127], v204, s[50:51]\n\t" \
    "global_load_dwordx4 v[128:131], v205, s[50:51]\n\t" \
    "global_load_dwordx4 v[132:135], v206, s[50:51]\n\t" \
    "global_load_dwordx4 v[136:139], v207, s[50:51]\n\t" \
    "global_load_dwordx4 v[140:143], v208, s[50:51]\n\t" \
    "global_load_dwordx4 v[144:147], v209, s[50:51]\n\t" \
    "global_load_dwordx4 v[148:151], v210, s[50:51]\n\t" \
    "global_load_dwordx4 v[152:155], v211, s[50:51]\n\t" \
    "s_add_u32 s52, s50, 331776\n\t" \
    "s_addc_u32 s53, s51, 0\n\t" \
    "global_load_dwordx4 v[72:75], v203, s[52:53]\n\t" \
    "global_load_dwordx4 v[76:79], v204, s[52:53]\n\t" \
    "global_load_dwordx4 v[80:83], v205, s[52:53]\n\t" \
    "global_load_dwordx4 v[84:87], v206, s[52:53]\n\t" \
    "global_load_dwordx4 v[88:91], v207, s[52:53]\n\t" \
    "global_load_dwordx4 v[92:95], v208, s[52:53]\n\t" \
    "global_load_dwordx4 v[96:99], v209, s[52:53]\n\t" \
    "global_load_dwordx4 v[100:103], v210, s[52:53]\n\t" \
    "global_load_dwordx4 v[104:107], v211, s[52:53]\n\t" \
    "s_waitcnt vmcnt(50) lgkmcnt(0)\n\t" \
    "s_barrier\n\t" \
    "ds_read_b64_tr_b16 a[0:1], v201 offset:0\n\t" \
    "ds_read_b64_tr_b16 a[2:3], v201 offset:1024\n\t" \
    "ds_read_b64_tr_b16 a[4:5], v201 offset:8192\n\t" \
    "ds_read_b64_tr_b16 a[6:7], v201 offset:9216\n\t" \
    "ds_read_b64_tr_b16 a[72:73], v202 offset:0\n\t" \
    "ds_read_b64_tr_b16 a[74:75], v202 offset:1024\n\t" \
    "ds_read_b64_tr_b16 a[76:77], v202 offset:8192\n\t" \
    "ds_read_b64_tr_b16 a[78:79], v202 offset:9216\n\t" \
    "s_waitcnt vmcnt(46) lgkmcnt(0)\n\t" \
    "s_barrier\n\t" \
    "ds_read_b64_tr_b16 a[8:9], v201 offset:16384\n\t" \
    "ds_read_b64_tr_b16 a[10:11], v201 offset:17408\n\t" \
    "ds_read_b64_tr_b16 a[12:13], v201 offset:24576\n\t" \
    "ds_read_b64_tr_b16 a[14:15], v201 offset:25600\n\t" \
    "ds_read_b64_tr_b16 a[80:81], v202 offset:16384\n\t" \
    "ds_read_b64_tr_b16 a[82:83], v202 offset:17408\n\t" \
    "ds_read_b64_tr_b16 a[84:85], v202 offset:24576\n\t" \
    "ds_read_b64_tr_b16 a[86:87], v202 offset:25600\n\t" \
    "s_waitcnt vmcnt(42) lgkmcnt(0)\n\t" \
    "s_barrier\n\t" \
    "ds_read_b64_tr_b16 a[16:17], v201 offset:32768\n\t" \
    "ds_read_b64_tr_b16 a[18:19], v201 offset:33792\n\t" \
    "ds_read_b64_tr_b16 a[20:21], v201 offset:40960\n\t" \
    "ds_read_b64_tr_b16 a[22:23], v201 offset:41984\n\t" \
    "ds_read_b64_tr_b16 a[88:89], v202 offset:32768\n\t" \
    "ds_read_b64_tr_b16 a[90:91], v202 offset:33792\n\t" \
    "ds_read_b64_tr_b16 a[92:93], v202 offset:40960\n\t" \
    "ds_read_b64_tr_b16 a[94:95], v202 offset:41984\n\t" \
    "s_waitcnt vmcnt(38) lgkmcnt(0)\n\t" \
    "s_barrier\n\t" \
    "ds_read_b64_tr_b16 a[24:25], v201 offset:49152\n\t" \
    "ds_read_b64_tr_b16 a[26:27], v201 offset:50176\n\t" \
    "ds_read_b64_tr_b16 a[28:29], v201 offset:57344\n\t" \
    "ds_read_b64_tr_b16 a[30:31], v201 offset:58368\n\t" \
    "ds_read_b64_tr_b16 a[96:97], v202 offset:49152\n\t" \
    "ds_read_b64_tr_b16 a[98:99], v202 offset:50176\n\t" \
    "ds_read_b64_tr_b16 a[100:101], v202 offset:57344\n\t" \
    "ds_read_b64_tr_b16 a[102:103], v202 offset:58368\n\t" \
    "s_waitcnt vmcnt(34) lgkmcnt(0)\n\t" \
    "s_barrier\n\t" \
    "v_add_u32 v201, 0x10000, v201\n\t" \
    "v_add_u32 v202, 0x10000, v202\n\t" \
    "ds_read_b64_tr_b16 a[32:33], v201 offset:0\n\t" \
    "ds_read_b64_tr_b16 a[34:35], v201 offset:1024\n\t" \
    "ds_read_b64_tr_b16 a[36:37], v201 offset:8192\n\t" \
    "ds_read_b64_tr_b16 a[38:39], v201 offset:9216\n\t" \
    "ds_read_b64_tr_b16 a[104:105], v202 offset:0\n\t" \
    "ds_read_b64_tr_b16 a[106:107], v202 offset:1024\n\t" \
    "ds_read_b64_tr_b16 a[108:109], v202 offset:8192\n\t" \
    "ds_read_b64_tr_b16 a[110:111], v202 offset:9216\n\t" \
    "s_waitcnt vmcnt(30) lgkmcnt(0)\n\t" \
    "s_barrier\n\t" \
    "ds_read_b64_tr_b16 a[40:41], v201 offset:16384\n\t" \
    "ds_read_b64_tr_b16 a[42:43], v201 offset:17408\n\t" \
    "ds_read_b64_tr_b16 a[44:45], v201 offset:24576\n\t" \
    "ds_read_b64_tr_b16 a[46:47], v201 offset:25600\n\t" \
    "ds_read_b64_tr_b16 a[112:113], v202 offset:16384\n\t" \
    "ds_read_b64_tr_b16 a[114:115], v202 offset:17408\n\t" \
    "ds_read_b64_tr_b16 a[116:117], v202 offset:24576\n\t" \
    "ds_read_b64_tr_b16 a[118:119], v202 offset:25600\n\t" \
    "s_waitcnt vmcnt(26) lgkmcnt(0)\n\t" \
    "s_barrier\n\t" \
    "ds_read_b64_tr_b16 a[48:49], v201 offset:32768\n\t" \
    "ds_read_b64_tr_b16 a[50:51], v201 offset:33792\n\t" \
    "ds_read_b64_tr_b16 a[52:53], v201 offset:40960\n\t" \
    "ds_read_b64_tr_b16 a[54:55], v201 offset:41984\n\t" \
    "ds_read_b64_tr_b16 a[120:121], v202 offset:32768\n\t" \
    "ds_read_b64_tr_b16 a[122:123], v202 offset:33792\n\t" \
    "ds_read_b64_tr_b16 a[124:125], v202 offset:40960\n\t" \
    "ds_read_b64_tr_b16 a[126:127], v202 offset:41984\n\t" \
    "s_waitcnt vmcnt(22) lgkmcnt(0)\n\t" \
    "s_barrier\n\t" \
    "ds_read_b64_tr_b16 a[56:57], v201 offset:49152\n\t" \
    "ds_read_b64_tr_b16 a[58:59], v201 offset:50176\n\t" \
    "ds_read_b64_tr_b16 a[60:61], v201 offset:57344\n\t" \
    "ds_read_b64_tr_b16 a[62:63], v201 offset:58368\n\t" \
    "ds_read_b64_tr_b16 a[128:129], v202 offset:49152\n\t" \
    "ds_read_b64_tr_b16 a[130:131], v202 offset:50176\n\t" \
    "ds_read_b64_tr_b16 a[132:133], v202 offset:57344\n\t" \
    "ds_read_b64_tr_b16 a[134:135], v202 offset:58368\n\t" \
    "s_waitcnt vmcnt(18) lgkmcnt(0)\n\t" \
    "s_barrier\n\t" \
    "v_add_u32 v201, 0x10000, v201\n\t" \
    "v_add_u32 v202, 0x10000, v202\n\t" \
    "ds_read_b64_tr_b16 a[64:65], v201 offset:0\n\t" \
    "ds_read_b64_tr_b16 a[66:67], v201 offset:1024\n\t" \
    "ds_read_b64_tr_b16 a[68:69], v201 offset:8192\n\t" \
    "ds_read_b64_tr_b16 a[70:71], v201 offset:9216\n\t" \
    "ds_read_b64_tr_b16 a[136:137], v202 offset:0\n\t" \
    "ds_read_b64_tr_b16 a[138:139], v202 offset:1024\n\t" \
    "ds_read_b64_tr_b16 a[140:141], v202 offset:8192\n\t" \
    "ds_read_b64_tr_b16 a[142:143], v202 offset:9216\n\t" \
    "v_add_u32 v201, 0xfffe0000, v201\n\t" \
    "v_add_u32 v202, 0xfffe0000, v202\n\t" \
    "s_waitcnt lgkmcnt(0)\n\t" \
    "s_barrier\n\t" \
    "s_waitcnt vmcnt(0)\n\t" \
    "ds_write_b128 v3, v[120:123] offset:0\n\t" \
    "ds_write_b128 v3, v[124:127] offset:1024\n\t" \
    "ds_write_b128 v3, v[128:131] offset:2048\n\t" \
    "ds_write_b128 v3, v[132:135] offset:3072\n\t" \
    "ds_write_b128 v3, v[136:139] offset:4096\n\t" \
    "ds_write_b128 v3, v[140:143] offset:5120\n\t" \
    "ds_write_b128 v3, v[144:147] offset:6144\n\t" \
    "ds_write_b128 v3, v[148:151] offset:7168\n\t" \
    "ds_write_b128 v3, v[152:155] offset:8192\n\t" \
    "ds_write_b128 v3, v[72:75] offset:36864\n\t" \
    "ds_write_b128 v3, v[76:79] offset:37888\n\t" \
    "ds_write_b128 v3, v[80:83] offset:38912\n\t" \
    "ds_write_b128 v3, v[84:87] offset:39936\n\t" \
    "ds_write_b128 v3, v[88:91] offset:40960\n\t" \
    "ds_write_b128 v3, v[92:95] offset:41984\n\t" \
    "ds_write_b128 v3, v[96:99] offset:43008\n\t" \
    "ds_write_b128 v3, v[100:103] offset:44032\n\t" \
    "ds_write_b128 v3, v[104:107] offset:45056\n\t" \
    "s_mov_b32 s65, 0\n\t" \
    "v_mov_b32 v1, v0\n\t" \
    "v_xor_b32 v2, 64, v0\n\t" \
    "s_mov_b32 s90, 0\n\t" \
    "s_add_u32 s91, s89, 0x12000\n\t" \
    "s_add_u32 s85, s91, s89\n\t" \
    "s_add_u32 s52, s50, 36864\n\t" \
    "s_addc_u32 s53, s51, 0\n\t" \
    "s_mul_i32 s69, s67, 8192\n\t" \
    "s_add_u32 s69, s69, 175112192\n\t" \
    "s_add_u32 s42, s36, s69\n\t" \
    "s_addc_u32 s43, s37, 0\n\t" \
    "s_mov_b64 s[40:41], s[42:43]\n\t" \
    "s_waitcnt lgkmcnt(0)\n\t" \
    "s_barrier\n\t" \
    "ds_read_b128 v[24:27], v1 offset:0\n\t" \
    "ds_read_b128 v[28:31], v1 offset:18432\n\t" \
    "ds_read_b128 v[32:35], v2 offset:0\n\t" \
    "ds_read_b128 v[36:39], v2 offset:18432\n\t" \
    "ds_read_b128 v[40:43], v1 offset:128\n\t" \
    "ds_read_b128 v[44:47], v1 offset:18560\n\t" \
    ".p2align 6\n\t" \
    "s_nop 0\n\t" \
    "s_nop 0\n\t" \
    "gx_l28_%=:\n\t" \
    "s_waitcnt lgkmcnt(5)\n\t" \
    "v_mfma_f32_16x16x32_bf16 v[88:91], a[0:3], v[24:27], 0\n\t" \
    "ds_read_b128 v[48:51], v2 offset:128\n\t" \
    "s_waitcnt lgkmcnt(5)\n\t" \
    "v_mfma_f32_16x16x32_bf16 v[92:95], a[0:3], v[28:31], 0\n\t" \
    "ds_read_b128 v[52:55], v2 offset:18560\n\t" \
    "v_mfma_f32_16x16x32_bf16 v[96:99], a[72:75], v[24:27], 0\n\t" \
    "s_add_u32 m0, s91, 0\n\t" \
    "v_mfma_f32_16x16x32_bf16 v[100:103], a[72:75], v[28:31], 0\n\t" \
    "global_load_lds_dwordx4 v203, s[52:53]\n\t" \
    "s_waitcnt lgkmcnt(5)\n\t" \
    "v_mfma_f32_16x16x32_bf16 v[88:91], a[4:7], v[32:35], v[88:91]\n\t" \
    "ds_read_b128 v[56:59], v1 offset:256\n\t" \
    "s_waitcnt lgkmcnt(5)\n\t" \
    "v_mfma_f32_16x16x32_bf16 v[92:95], a[4:7], v[36:39], v[92:95]\n\t" \
    "ds_read_b128 v[60:63], v1 offset:18688\n\t" \
    "v_mfma_f32_16x16x32_bf16 v[96:99], a[76:79], v[32:35], v[96:99]\n\t" \
    "s_add_u32 m0, s91, 1024\n\t" \
    "v_mfma_f32_16x16x32_bf16 v[100:103], a[76:79], v[36:39], v[100:103]\n\t" \
    "global_load_lds_dwordx4 v204, s[52:53]\n\t" \
    "s_waitcnt lgkmcnt(5)\n\t" \
    "v_mfma_f32_16x16x32_bf16 v[88:91], a[8:11], v[40:43], v[88:91]\n\t" \
    "ds_read_b128 v[64:67], v2 offset:256\n\t" \
    "s_waitcnt lgkmcnt(5)\n\t" \
    "v_mfma_f32_16x16x32_bf16 v[92:95], a[8:11], v[44:47], v[92:95]\n\t" \
    "ds_read_b128 v[68:71], v2 offset:18688\n\t" \
    "v_cvt_pk_bf16_f32 v104, v72, v73\n\t" \
    "v_mfma_f32_16x16x32_bf16 v[96:99], a[80:83], v[40:43], v[96:99]\n\t" \
    "s_add_u32 m0, s91, 2048\n\t" \
    "v_cvt_pk_bf16_f32 v105, v74, v75\n\t" \
    "v_mfma_f32_16x16x32_bf16 v[100:103], a[80:83], v[44:47], v[100:103]\n\t" \
    "global_load_lds_dwordx4 v205, s[52:53]\n\t" \
    "s_waitcnt lgkmcnt(5)\n\t" \
    "v_mfma_f32_16x16x32_bf16 v[88:91], a[12:15], v[48:51], v[88:91]\n\t" \
    "ds_read_b128 v[24:27], v1 offset:384\n\t" \
    "s_waitcnt lgkmcnt(5)\n\t" \
    "v_mfma_f32_16x16x32_bf16 v[92:95], a[12:15], v[52:55], v[92:95]\n\t" \
    "ds_read_b128 v[28:31], v1 offset:18816\n\t" \
    "v_cvt_pk_bf16_f32 v106, v80, v81\n\t" \
    "v_mfma_f32_16x16x32_bf16 v[96:99], a[84:87], v[48:51], v[96:99]\n\t" \
    "s_add_u32 m0, s91, 3072\n\t" \
    "v_cvt_pk_bf16_f32 v107, v82, v83\n\t" \
    "v_mfma_f32_16x16x32_bf16 v[100:103], a[84:87], v[52:55], v[100:103]\n\t" \
    "global_load_lds_dwordx4 v206, s[52:53]\n\t" \
    "s_waitcnt lgkmcnt(5)\n\t" \
    "v_mfma_f32_16x16x32_bf16 v[88:91], a[16:19], v[56:59], v[88:91]\n\t" \
    "ds_read_b128 v[32:35], v2 offset:384\n\t" \
    "s_waitcnt lgkmcnt(5)\n\t" \
    "v_mfma_f32_16x16x32_bf16 v[92:95], a[16:19], v[60:63], v[92:95]\n\t" \
    "ds_read_b128 v[36:39], v2 offset:18816\n\t" \
    "global_store_dwordx4 v8, v[104:107], s[40:41] offset:0 sc1\n\t" \
    "v_mfma_f32_16x16x32_bf16 v[96:99], a[88:91], v[56:59], v[96:99]\n\t" \
    "s_add_u32 m0, s91, 4096\n\t" \
    "v_cvt_pk_bf16_f32 v108, v76, v77\n\t" \
    "v_mfma_f32_16x16x32_bf16 v[100:103], a[88:91], v[60:63], v[100:103]\n\t" \
    "global_load_lds_dwordx4 v207, s[52:53]\n\t" \
    "s_waitcnt lgkmcnt(5)\n\t" \
    "v_mfma_f32_16x16x32_bf16 v[88:91], a[20:23], v[64:67], v[88:91]\n\t" \
    "ds_read_b128 v[40:43], v1 offset:512\n\t" \
    "s_waitcnt lgkmcnt(5)\n\t" \
    "v_mfma_f32_16x16x32_bf16 v[92:95], a[20:23], v[68:71], v[92:95]\n\t" \
    "ds_read_b128 v[44:47], v1 offset:18944\n\t" \
    "v_cvt_pk_bf16_f32 v109, v78, v79\n\t" \
    "v_mfma_f32_16x16x32_bf16 v[96:99], a[92:95], v[64:67], v[96:99]\n\t" \
    "s_add_u32 m0, s91, 5120\n\t" \
    "v_cvt_pk_bf16_f32 v110, v84, v85\n\t" \
    "v_mfma_f32_16x16x32_bf16 v[100:103], a[92:95], v[68:71], v[100:103]\n\t" \
    "global_load_lds_dwordx4 v208, s[52:53]\n\t" \
    "s_waitcnt lgkmcnt(5)\n\t" \
    "v_mfma_f32_16x16x32_bf16 v[88:91], a[24:27], v[24:27], v[88:91]\n\t" \
    "ds_read_b128 v[48:51], v2 offset:512\n\t" \
    "s_waitcnt lgkmcnt(5)\n\t" \
    "v_mfma_f32_16x16x32_bf16 v[92:95], a[24:27], v[28:31], v[92:95]\n\t" \
    "ds_read_b128 v[52:55], v2 offset:18944\n\t" \
    "v_cvt_pk_bf16_f32 v111, v86, v87\n\t" \
    "v_mfma_f32_16x16x32_bf16 v[96:99], a[96:99], v[24:27], v[96:99]\n\t" \
    "s_add_u32 m0, s91, 6144\n\t" \
    "global_store_dwordx4 v8, v[108:111], s[40:41] offset:1024 sc1\n\t" \
    "v_mfma_f32_16x16x32_bf16 v[100:103], a[96:99], v[28:31], v[100:103]\n\t" \
    "global_load_lds_dwordx4 v209, s[52:53]\n\t" \
    "s_waitcnt lgkmcnt(5)\n\t" \
    "v_mfma_f32_16x16x32_bf16 v[88:91], a[28:31], v[32:35], v[88:91]\n\t" \
    "ds_read_b128 v[56:59], v1 offset:640\n\t" \
    "s_waitcnt lgkmcnt(5)\n\t" \
    "v_mfma_f32_16x16x32_bf16 v[92:95], a[28:31], v[36:39], v[92:95]\n\t" \
    "ds_read_b128 v[60:63], v1 offset:19072\n\t" \
    "v_mfma_f32_16x16x32_bf16 v[96:99], a[100:103], v[32:35], v[96:99]\n\t" \
    "s_add_u32 m0, s91, 7168\n\t" \
    "v_mfma_f32_16x16x32_bf16 v[100:103], a[100:103], v[36:39], v[100:103]\n\t" \
    "global_load_lds_dwordx4 v210, s[52:53]\n\t" \
    "s_waitcnt lgkmcnt(5)\n\t" \
    "v_mfma_f32_16x16x32_bf16 v[88:91], a[32:35], v[40:43], v[88:91]\n\t" \
    "ds_read_b128 v[64:67], v2 offset:640\n\t" \
    "s_waitcnt lgkmcnt(5)\n\t" \
    "v_mfma_f32_16x16x32_bf16 v[92:95], a[32:35], v[44:47], v[92:95]\n\t" \
    "ds_read_b128 v[68:71], v2 offset:19072\n\t" \
    "v_mfma_f32_16x16x32_bf16 v[96:99], a[104:107], v[40:43], v[96:99]\n\t" \
    "s_add_u32 m0, s91, 8192\n\t" \
    "v_mfma_f32_16x16x32_bf16 v[100:103], a[104:107], v[44:47], v[100:103]\n\t" \
    "global_load_lds_dwordx4 v211, s[52:53]\n\t" \
    "s_waitcnt lgkmcnt(5)\n\t" \
    "v_mfma_f32_16x16x32_bf16 v[88:91], a[36:39], v[48:51], v[88:91]\n\t" \
    "ds_read_b128 v[24:27], v1 offset:768\n\t" \
    "s_waitcnt lgkmcnt(5)\n\t" \
    "v_mfma_f32_16x16x32_bf16 v[92:95], a[36:39], v[52:55], v[92:95]\n\t" \
    "ds_read_b128 v[28:31], v1 offset:19200\n\t" \
    "v_mfma_f32_16x16x32_bf16 v[96:99], a[108:111], v[48:51], v[96:99]\n\t" \
    "v_mfma_f32_16x16x32_bf16 v[100:103], a[108:111], v[52:55], v[100:103]\n\t" \
    "s_waitcnt lgkmcnt(5)\n\t" \
    "v_mfma_f32_16x16x32_bf16 v[88:91], a[40:43], v[56:59], v[88:91]\n\t" \
    "ds_read_b128 v[32:35], v2 offset:768\n\t" \
    "s_waitcnt lgkmcnt(5)\n\t" \
    "v_mfma_f32_16x16x32_bf16 v[92:95], a[40:43], v[60:63], v[92:95]\n\t" \
    "ds_read_b128 v[36:39], v2 offset:19200\n\t" \
    "v_mfma_f32_16x16x32_bf16 v[96:99], a[112:115], v[56:59], v[96:99]\n\t" \
    "s_waitcnt vmcnt(11)\n\t" \
    "s_barrier\n\t" \
    "v_mfma_f32_16x16x32_bf16 v[100:103], a[112:115], v[60:63], v[100:103]\n\t" \
    "s_cmp_lg_u32 s66, 0\n\t" \
    "s_cbranch_scc1 gx_SF13_%=\n\t" \
    "s_lshl_b32 s69, s67, 7\n\t" \
    "s_add_u32 s69, s69, 5849088\n\t" \
    "s_add_u32 s70, s65, -1\n\t" \
    "v_mov_b32 v156, s69\n\t" \
    "v_mov_b32 v157, s70\n\t" \
    "s_mov_b64 exec, 1\n\t" \
    "global_store_dword v156, v157, s[36:37] sc1\n\t" \
    "s_mov_b64 exec, -1\n\t" \
    "gx_SF13_%=:\n\t" \
    "s_waitcnt lgkmcnt(5)\n\t" \
    "v_mfma_f32_16x16x32_bf16 v[88:91], a[44:47], v[64:67], v[88:91]\n\t" \
    "ds_read_b128 v[40:43], v1 offset:896\n\t" \
    "s_waitcnt lgkmcnt(5)\n\t" \
    "v_mfma_f32_16x16x32_bf16 v[92:95], a[44:47], v[68:71], v[92:95]\n\t" \
    "ds_read_b128 v[44:47], v1 offset:19328\n\t" \
    "v_mfma_f32_16x16x32_bf16 v[96:99], a[116:119], v[64:67], v[96:99]\n\t" \
    "v_add_u32 v22, 0x9000, v1\n\t" \
    "v_add_u32 v23, 0x9000, v2\n\t" \
    "v_mfma_f32_16x16x32_bf16 v[100:103], a[116:119], v[68:71], v[100:103]\n\t" \
    "s_cmp_lt_u32 s65, 54\n\t" \
    "s_cselect_b32 s84, 10, 2\n\t" \
    "s_waitcnt lgkmcnt(5)\n\t" \
    "v_mfma_f32_16x16x32_bf16 v[88:91], a[48:51], v[24:27], v[88:91]\n\t" \
    "ds_read_b128 v[48:51], v2 offset:896\n\t" \
    "s_waitcnt lgkmcnt(5)\n\t" \
    "v_mfma_f32_16x16x32_bf16 v[92:95], a[48:51], v[28:31], v[92:95]\n\t" \
    "ds_read_b128 v[52:55], v2 offset:19328\n\t" \
    "v_mfma_f32_16x16x32_bf16 v[96:99], a[120:123], v[24:27], v[96:99]\n\t" \
    "s_add_u32 s84, s65, s84\n\t" \
    "s_min_u32 s69, s84, 63\n\t" \
    "v_mfma_f32_16x16x32_bf16 v[100:103], a[120:123], v[28:31], v[100:103]\n\t" \
    "s_mul_i32 s69, s69, 36864\n\t" \
    "s_add_u32 s48, s50, s69\n\t" \
    "s_waitcnt lgkmcnt(5)\n\t" \
    "v_mfma_f32_16x16x32_bf16 v[88:91], a[52:55], v[32:35], v[88:91]\n\t" \
    "ds_read_b128 v[56:59], v1 offset:1024\n\t" \
    "s_waitcnt lgkmcnt(5)\n\t" \
    "v_mfma_f32_16x16x32_bf16 v[92:95], a[52:55], v[36:39], v[92:95]\n\t" \
    "ds_read_b128 v[60:63], v1 offset:19456\n\t" \
    "v_mfma_f32_16x16x32_bf16 v[96:99], a[124:127], v[32:35], v[96:99]\n\t" \
    "s_addc_u32 s49, s51, 0\n\t" \
    "s_mul_i32 s70, s67, 7\n\t" \
    "v_mfma_f32_16x16x32_bf16 v[100:103], a[124:127], v[36:39], v[100:103]\n\t" \
    "s_add_u32 s69, s65, 0\n\t" \
    "s_xor_b32 s69, s69, s70\n\t" \
    "s_waitcnt lgkmcnt(5)\n\t" \
    "v_mfma_f32_16x16x32_bf16 v[88:91], a[56:59], v[40:43], v[88:91]\n\t" \
    "ds_read_b128 v[64:67], v2 offset:1024\n\t" \
    "s_waitcnt lgkmcnt(5)\n\t" \
    "v_mfma_f32_16x16x32_bf16 v[92:95], a[56:59], v[44:47], v[92:95]\n\t" \
    "ds_read_b128 v[68:71], v2 offset:19456\n\t" \
    "v_mfma_f32_16x16x32_bf16 v[96:99], a[128:131], v[40:43], v[96:99]\n\t" \
    "s_and_b32 s69, s69, 63\n\t" \
    "s_lshl_b32 s69, s69, 13\n\t" \
    "v_mfma_f32_16x16x32_bf16 v[100:103], a[128:131], v[44:47], v[100:103]\n\t" \
    "s_mul_i32 s70, s67, 524288\n\t" \
    "s_add_u32 s69, s69, s70\n\t" \
    "s_waitcnt lgkmcnt(5)\n\t" \
    "v_mfma_f32_16x16x32_bf16 v[88:91], a[60:63], v[48:51], v[88:91]\n\t" \
    "ds_read_b128 v[24:27], v22 offset:0\n\t" \
    "s_waitcnt lgkmcnt(5)\n\t" \
    "v_mfma_f32_16x16x32_bf16 v[92:95], a[60:63], v[52:55], v[92:95]\n\t" \
    "ds_read_b128 v[28:31], v22 offset:18432\n\t" \
    "v_mfma_f32_16x16x32_bf16 v[96:99], a[132:135], v[48:51], v[96:99]\n\t" \
    "s_add_u32 s69, s69, 6291456\n\t" \
    "s_add_u32 s38, s36, s69\n\t" \
    "v_mfma_f32_16x16x32_bf16 v[100:103], a[132:135], v[52:55], v[100:103]\n\t" \
    "s_addc_u32 s39, s37, 0\n\t" \
    "s_waitcnt lgkmcnt(5)\n\t" \
    "v_mfma_f32_16x16x32_bf16 v[88:91], a[64:67], v[56:59], v[88:91]\n\t" \
    "ds_read_b128 v[32:35], v23 offset:0\n\t" \
    "s_waitcnt lgkmcnt(5)\n\t" \
    "v_mfma_f32_16x16x32_bf16 v[92:95], a[64:67], v[60:63], v[92:95]\n\t" \
    "ds_read_b128 v[36:39], v23 offset:18432\n\t" \
    "v_mfma_f32_16x16x32_bf16 v[96:99], a[136:139], v[56:59], v[96:99]\n\t" \
    "v_mfma_f32_16x16x32_bf16 v[100:103], a[136:139], v[60:63], v[100:103]\n\t" \
    "s_waitcnt lgkmcnt(5)\n\t" \
    "v_mfma_f32_16x16x32_bf16 v[88:91], a[68:71], v[64:67], v[88:91]\n\t" \
    "ds_read_b128 v[40:43], v22 offset:128\n\t" \
    "s_waitcnt lgkmcnt(5)\n\t" \
    "v_mfma_f32_16x16x32_bf16 v[92:95], a[68:71], v[68:71], v[92:95]\n\t" \
    "ds_read_b128 v[44:47], v22 offset:18560\n\t" \
    "v_mfma_f32_16x16x32_bf16 v[96:99], a[140:143], v[64:67], v[96:99]\n\t" \
    "v_mfma_f32_16x16x32_bf16 v[100:103], a[140:143], v[68:71], v[100:103]\n\t" \
    "s_waitcnt lgkmcnt(5)\n\t" \
    "v_mfma_f32_16x16x32_bf16 v[72:75], a[144:147], v[24:27], 0\n\t" \
    "ds_read_b128 v[48:51], v23 offset:128\n\t" \
    "s_waitcnt lgkmcnt(5)\n\t" \
    "v_mfma_f32_16x16x32_bf16 v[76:79], a[144:147], v[28:31], 0\n\t" \
    "ds_read_b128 v[52:55], v23 offset:18560\n\t" \
    "v_mfma_f32_16x16x32_bf16 v[80:83], a[216:219], v[24:27], 0\n\t" \
    "s_add_u32 m0, s91, 36864\n\t" \
    "v_mfma_f32_16x16x32_bf16 v[84:87], a[216:219], v[28:31], 0\n\t" \
    "global_load_lds_dwordx4 v203, s[48:49]\n\t" \
    "s_waitcnt lgkmcnt(5)\n\t" \
    "v_mfma_f32_16x16x32_bf16 v[72:75], a[148:151], v[32:35], v[72:75]\n\t" \
    "ds_read_b128 v[56:59], v22 offset:256\n\t" \
    "s_waitcnt lgkmcnt(5)\n\t" \
    "v_mfma_f32_16x16x32_bf16 v[76:79], a[148:151], v[36:39], v[76:79]\n\t" \
    "ds_read_b128 v[60:63], v22 offset:18688\n\t" \
    "v_mfma_f32_16x16x32_bf16 v[80:83], a[220:223], v[32:35], v[80:83]\n\t" \
    "s_add_u32 m0, s91, 37888\n\t" \
    "v_mfma_f32_16x16x32_bf16 v[84:87], a[220:223], v[36:39], v[84:87]\n\t" \
    "global_load_lds_dwordx4 v204, s[48:49]\n\t" \
    "s_waitcnt lgkmcnt(5)\n\t" \
    "v_mfma_f32_16x16x32_bf16 v[72:75], a[152:155], v[40:43], v[72:75]\n\t" \
    "ds_read_b128 v[64:67], v23 offset:256\n\t" \
    "s_waitcnt lgkmcnt(5)\n\t" \
    "v_mfma_f32_16x16x32_bf16 v[76:79], a[152:155], v[44:47], v[76:79]\n\t" \
    "ds_read_b128 v[68:71], v23 offset:18688\n\t" \
    "v_cvt_pk_bf16_f32 v104, v88, v89\n\t" \
    "v_mfma_f32_16x16x32_bf16 v[80:83], a[224:227], v[40:43], v[80:83]\n\t" \
    "s_add_u32 m0, s91, 38912\n\t" \
    "v_cvt_pk_bf16_f32 v105, v90, v91\n\t" \
    "v_mfma_f32_16x16x32_bf16 v[84:87], a[224:227], v[44:47], v[84:87]\n\t" \
    "global_load_lds_dwordx4 v205, s[48:49]\n\t" \
    "s_waitcnt lgkmcnt(5)\n\t" \
    "v_mfma_f32_16x16x32_bf16 v[72:75], a[156:159], v[48:51], v[72:75]\n\t" \
    "ds_read_b128 v[24:27], v22 offset:384\n\t" \
    "s_waitcnt lgkmcnt(5)\n\t" \
    "v_mfma_f32_16x16x32_bf16 v[76:79], a[156:159], v[52:55], v[76:79]\n\t" \
    "ds_read_b128 v[28:31], v22 offset:18816\n\t" \
    "v_cvt_pk_bf16_f32 v106, v96, v97\n\t" \
    "v_mfma_f32_16x16x32_bf16 v[80:83], a[228:231], v[48:51], v[80:83]\n\t" \
    "s_add_u32 m0, s91, 39936\n\t" \
    "v_cvt_pk_bf16_f32 v107, v98, v99\n\t" \
    "v_mfma_f32_16x16x32_bf16 v[84:87], a[228:231], v[52:55], v[84:87]\n\t" \
    "global_load_lds_dwordx4 v206, s[48:49]\n\t" \
    "s_waitcnt lgkmcnt(5)\n\t" \
    "v_mfma_f32_16x16x32_bf16 v[72:75], a[160:163], v[56:59], v[72:75]\n\t" \
    "ds_read_b128 v[32:35], v23 offset:384\n\t" \
    "s_waitcnt lgkmcnt(5)\n\t" \
    "v_mfma_f32_16x16x32_bf16 v[76:79], a[160:163], v[60:63], v[76:79]\n\t" \
    "ds_read_b128 v[36:39], v23 offset:18816\n\t" \
    "global_store_dwordx4 v8, v[104:107], s[38:39] offset:0 sc1\n\t" \
    "v_mfma_f32_16x16x32_bf16 v[80:83], a[232:235], v[56:59], v[80:83]\n\t" \
    "s_add_u32 m0, s91, 40960\n\t" \
    "v_cvt_pk_bf16_f32 v108, v92, v93\n\t" \
    "v_mfma_f32_16x16x32_bf16 v[84:87], a[232:235], v[60:63], v[84:87]\n\t" \
    "global_load_lds_dwordx4 v207, s[48:49]\n\t" \
    "s_waitcnt lgkmcnt(5)\n\t" \
    "v_mfma_f32_16x16x32_bf16 v[72:75], a[164:167], v[64:67], v[72:75]\n\t" \
    "ds_read_b128 v[40:43], v22 offset:512\n\t" \
    "s_waitcnt lgkmcnt(5)\n\t" \
    "v_mfma_f32_16x16x32_bf16 v[76:79], a[164:167], v[68:71], v[76:79]\n\t" \
    "ds_read_b128 v[44:47], v22 offset:18944\n\t" \
    "v_cvt_pk_bf16_f32 v109, v94, v95\n\t" \
    "v_mfma_f32_16x16x32_bf16 v[80:83], a[236:239], v[64:67], v[80:83]\n\t" \
    "s_add_u32 m0, s91, 41984\n\t" \
    "v_cvt_pk_bf16_f32 v110, v100, v101\n\t" \
    "v_mfma_f32_16x16x32_bf16 v[84:87], a[236:239], v[68:71], v[84:87]\n\t" \
    "global_load_lds_dwordx4 v208, s[48:49]\n\t" \
    "s_waitcnt lgkmcnt(5)\n\t" \
    "v_mfma_f32_16x16x32_bf16 v[72:75], a[168:171], v[24:27], v[72:75]\n\t" \
    "ds_read_b128 v[48:51], v23 offset:512\n\t" \
    "s_waitcnt lgkmcnt(5)\n\t" \
    "v_mfma_f32_16x16x32_bf16 v[76:79], a[168:171], v[28:31], v[76:79]\n\t" \
    "ds_read_b128 v[52:55], v23 offset:18944\n\t" \
    "v_cvt_pk_bf16_f32 v111, v102, v103\n\t" \
    "v_mfma_f32_16x16x32_bf16 v[80:83], a[240:243], v[24:27], v[80:83]\n\t" \
    "s_add_u32 m0, s91, 43008\n\t" \
    "global_store_dwordx4 v8, v[108:111], s[38:39] offset:1024 sc1\n\t" \
    "v_mfma_f32_16x16x32_bf16 v[84:87], a[240:243], v[28:31], v[84:87]\n\t" \
    "global_load_lds_dwordx4 v209, s[48:49]\n\t" \
    "s_waitcnt lgkmcnt(5)\n\t" \
    "v_mfma_f32_16x16x32_bf16 v[72:75], a[172:175], v[32:35], v[72:75]\n\t" \
    "ds_read_b128 v[56:59], v22 offset:640\n\t" \
    "s_waitcnt lgkmcnt(5)\n\t" \
    "v_mfma_f32_16x16x32_bf16 v[76:79], a[172:175], v[36:39], v[76:79]\n\t" \
    "ds_read_b128 v[60:63], v22 offset:19072\n\t" \
    "v_mfma_f32_16x16x32_bf16 v[80:83], a[244:247], v[32:35], v[80:83]\n\t" \
    "s_add_u32 m0, s91, 44032\n\t" \
    "v_mfma_f32_16x16x32_bf16 v[84:87], a[244:247], v[36:39], v[84:87]\n\t" \
    "global_load_lds_dwordx4 v210, s[48:49]\n\t" \
    "s_waitcnt lgkmcnt(5)\n\t" \
    "v_mfma_f32_16x16x32_bf16 v[72:75], a[176:179], v[40:43], v[72:75]\n\t" \
    "ds_read_b128 v[64:67], v23 offset:640\n\t" \
    "s_waitcnt lgkmcnt(5)\n\t" \
    "v_mfma_f32_16x16x32_bf16 v[76:79], a[176:179], v[44:47], v[76:79]\n\t" \
    "ds_read_b128 v[68:71], v23 offset:19072\n\t" \
    "v_mfma_f32_16x16x32_bf16 v[80:83], a[248:251], v[40:43], v[80:83]\n\t" \
    "s_add_u32 m0, s91, 45056\n\t" \
    "v_mfma_f32_16x16x32_bf16 v[84:87], a[248:251], v[44:47], v[84:87]\n\t" \
    "global_load_lds_dwordx4 v211, s[48:49]\n\t" \
    "s_waitcnt lgkmcnt(5)\n\t" \
    "v_mfma_f32_16x16x32_bf16 v[72:75], a[180:183], v[48:51], v[72:75]\n\t" \
    "ds_read_b128 v[24:27], v22 offset:768\n\t" \
    "s_waitcnt lgkmcnt(5)\n\t" \
    "v_mfma_f32_16x16x32_bf16 v[76:79], a[180:183], v[52:55], v[76:79]\n\t" \
    "ds_read_b128 v[28:31], v22 offset:19200\n\t" \
    "v_mfma_f32_16x16x32_bf16 v[80:83], a[252:255], v[48:51], v[80:83]\n\t" \
    "v_mfma_f32_16x16x32_bf16 v[84:87], a[252:255], v[52:55], v[84:87]\n\t" \
    "s_waitcnt lgkmcnt(5)\n\t" \
    "v_mfma_f32_16x16x32_bf16 v[72:75], a[184:187], v[56:59], v[72:75]\n\t" \
    "ds_read_b128 v[32:35], v23 offset:768\n\t" \
    "s_waitcnt lgkmcnt(5)\n\t" \
    "v_mfma_f32_16x16x32_bf16 v[76:79], a[184:187], v[60:63], v[76:79]\n\t" \
    "ds_read_b128 v[36:39], v23 offset:19200\n\t" \
    "v_mfma_f32_16x16x32_bf16 v[80:83], v[224:227], v[56:59], v[80:83]\n\t" \
    "s_waitcnt vmcnt(11)\n\t" \
    "s_barrier\n\t" \
    "v_mfma_f32_16x16x32_bf16 v[84:87], v[224:227], v[60:63], v[84:87]\n\t" \
    "s_add_u32 s71, s65, 8\n\t" \
    "s_cmp_lg_u32 s66, 0\n\t" \
    "s_cbranch_scc1 gx_SF14_%=\n\t" \
    "s_lshl_b32 s69, s67, 7\n\t" \
    "s_add_u32 s69, s69, 5771264\n\t" \
    "s_add_u32 s70, s71, 0\n\t" \
    "v_mov_b32 v156, s69\n\t" \
    "v_mov_b32 v157, s70\n\t" \
    "s_mov_b64 exec, 1\n\t" \
    "global_store_dword v156, v157, s[36:37] sc1\n\t" \
    "s_mov_b64 exec, -1\n\t" \
    "gx_SF14_%=:\n\t" \
    "s_waitcnt lgkmcnt(5)\n\t" \
    "v_mfma_f32_16x16x32_bf16 v[72:75], a[188:191], v[64:67], v[72:75]\n\t" \
    "ds_read_b128 v[40:43], v22 offset:896\n\t" \
    "s_waitcnt lgkmcnt(5)\n\t" \
    "v_mfma_f32_16x16x32_bf16 v[76:79], a[188:191], v[68:71], v[76:79]\n\t" \
    "ds_read_b128 v[44:47], v22 offset:19328\n\t" \
    "v_mfma_f32_16x16x32_bf16 v[80:83], v[228:231], v[64:67], v[80:83]\n\t" \
    "s_xor_b32 s90, s90, 0x12000\n\t" \
    "v_add_u32 v1, s90, v0\n\t" \
    "v_mfma_f32_16x16x32_bf16 v[84:87], v[228:231], v[68:71], v[84:87]\n\t" \
    "v_xor_b32 v2, 64, v1\n\t" \
    "s_add_u32 s84, s65, 2\n\t" \
    "s_waitcnt lgkmcnt(5)\n\t" \
    "v_mfma_f32_16x16x32_bf16 v[72:75], a[192:195], v[24:27], v[72:75]\n\t" \
    "ds_read_b128 v[48:51], v23 offset:896\n\t" \
    "s_waitcnt lgkmcnt(5)\n\t" \
    "v_mfma_f32_16x16x32_bf16 v[76:79], a[192:195], v[28:31], v[76:79]\n\t" \
    "ds_read_b128 v[52:55], v23 offset:19328\n\t" \
    "v_mfma_f32_16x16x32_bf16 v[80:83], v[232:235], v[24:27], v[80:83]\n\t" \
    "s_min_u32 s69, s84, 63\n\t" \
    "s_mul_i32 s69, s69, 36864\n\t" \
    "v_mfma_f32_16x16x32_bf16 v[84:87], v[232:235], v[28:31], v[84:87]\n\t" \
    "s_add_u32 s52, s50, s69\n\t" \
    "s_addc_u32 s53, s51, 0\n\t" \
    "s_waitcnt lgkmcnt(5)\n\t" \
    "v_mfma_f32_16x16x32_bf16 v[72:75], a[196:199], v[32:35], v[72:75]\n\t" \
    "ds_read_b128 v[56:59], v22 offset:1024\n\t" \
    "s_waitcnt lgkmcnt(5)\n\t" \
    "v_mfma_f32_16x16x32_bf16 v[76:79], a[196:199], v[36:39], v[76:79]\n\t" \
    "ds_read_b128 v[60:63], v22 offset:19456\n\t" \
    "v_mfma_f32_16x16x32_bf16 v[80:83], v[236:239], v[32:35], v[80:83]\n\t" \
    "s_mul_i32 s70, s67, 7\n\t" \
    "s_add_u32 s69, s65, 9\n\t" \
    "v_mfma_f32_16x16x32_bf16 v[84:87], v[236:239], v[36:39], v[84:87]\n\t" \
    "s_xor_b32 s69, s69, s70\n\t" \
    "s_and_b32 s69, s69, 63\n\t" \
    "s_waitcnt lgkmcnt(5)\n\t" \
    "v_mfma_f32_16x16x32_bf16 v[72:75], a[200:203], v[40:43], v[72:75]\n\t" \
    "ds_read_b128 v[64:67], v23 offset:1024\n\t" \
    "s_waitcnt lgkmcnt(5)\n\t" \
    "v_mfma_f32_16x16x32_bf16 v[76:79], a[200:203], v[44:47], v[76:79]\n\t" \
    "ds_read_b128 v[68:71], v23 offset:19456\n\t" \
    "v_mfma_f32_16x16x32_bf16 v[80:83], v[240:243], v[40:43], v[80:83]\n\t" \
    "s_lshl_b32 s69, s69, 13\n\t" \
    "s_mul_i32 s70, s67, 524288\n\t" \
    "v_mfma_f32_16x16x32_bf16 v[84:87], v[240:243], v[44:47], v[84:87]\n\t" \
    "s_add_u32 s69, s69, s70\n\t" \
    "s_add_u32 s69, s69, 23068672\n\t" \
    "s_waitcnt lgkmcnt(5)\n\t" \
    "v_mfma_f32_16x16x32_bf16 v[72:75], a[204:207], v[48:51], v[72:75]\n\t" \
    "ds_read_b128 v[24:27], v1 offset:0\n\t" \
    "s_waitcnt lgkmcnt(5)\n\t" \
    "v_mfma_f32_16x16x32_bf16 v[76:79], a[204:207], v[52:55], v[76:79]\n\t" \
    "ds_read_b128 v[28:31], v1 offset:18432\n\t" \
    "v_mfma_f32_16x16x32_bf16 v[80:83], v[244:247], v[48:51], v[80:83]\n\t" \
    "s_add_u32 s40, s36, s69\n\t" \
    "s_addc_u32 s41, s37, 0\n\t" \
    "v_mfma_f32_16x16x32_bf16 v[84:87], v[244:247], v[52:55], v[84:87]\n\t" \
    "s_sub_u32 s91, s85, s91\n\t" \
    "s_add_u32 s65, s65, 1\n\t" \
    "s_waitcnt lgkmcnt(5)\n\t" \
    "v_mfma_f32_16x16x32_bf16 v[72:75], a[208:211], v[56:59], v[72:75]\n\t" \
    "ds_read_b128 v[32:35], v2 offset:0\n\t" \
    "s_waitcnt lgkmcnt(5)\n\t" \
    "v_mfma_f32_16x16x32_bf16 v[76:79], a[208:211], v[60:63], v[76:79]\n\t" \
    "ds_read_b128 v[36:39], v2 offset:18432\n\t" \
    "v_mfma_f32_16x16x32_bf16 v[80:83], v[248:251], v[56:59], v[80:83]\n\t" \
    "v_mfma_f32_16x16x32_bf16 v[84:87], v[248:251], v[60:63], v[84:87]\n\t" \
    "s_waitcnt lgkmcnt(5)\n\t" \
    "v_mfma_f32_16x16x32_bf16 v[72:75], a[212:215], v[64:67], v[72:75]\n\t" \
    "ds_read_b128 v[40:43], v1 offset:128\n\t" \
    "s_waitcnt lgkmcnt(5)\n\t" \
    "v_mfma_f32_16x16x32_bf16 v[76:79], a[212:215], v[68:71], v[76:79]\n\t" \
    "ds_read_b128 v[44:47], v1 offset:18560\n\t" \
    "v_mfma_f32_16x16x32_bf16 v[80:83], v[252:255], v[64:67], v[80:83]\n\t" \
    "v_mfma_f32_16x16x32_bf16 v[84:87], v[252:255], v[68:71], v[84:87]\n\t" \
    "s_cmp_lt_u32 s65, 55\n\t" \
    "s_cbranch_scc1 gx_l28_%=\n\t" \
    "v_cvt_pk_bf16_f32 v104, v72, v73\n\t" \
    "v_cvt_pk_bf16_f32 v105, v74, v75\n\t" \
    "s_nop 2\n\t" \
    "v_cvt_pk_bf16_f32 v106, v80, v81\n\t" \
    "v_cvt_pk_bf16_f32 v107, v82, v83\n\t" \
    "global_store_dwordx4 v8, v[104:107], s[40:41] offset:0 sc1\n\t" \
    "v_cvt_pk_bf16_f32 v108, v76, v77\n\t" \
    "v_cvt_pk_bf16_f32 v109, v78, v79\n\t" \
    "v_cvt_pk_bf16_f32 v110, v84, v85\n\t" \
    "v_cvt_pk_bf16_f32 v111, v86, v87\n\t" \
    "global_store_dwordx4 v8, v[108:111], s[40:41] offset:1024 sc1\n\t" \
    "gx_l315_%=:\n\t" \
    "s_add_u32 s84, s65, 2\n\t" \
    "s_min_u32 s69, s84, 63\n\t" \
    "s_mul_i32 s69, s69, 36864\n\t" \
    "s_add_u32 s52, s50, s69\n\t" \
    "s_addc_u32 s53, s51, 0\n\t" \
    "s_add_u32 s90, s90, 36864\n\t" \
    "s_cmp_eq_u32 s90, 147456\n\t" \
    "s_cselect_b32 s90, 0, s90\n\t" \
    "s_add_u32 s91, s90, 36864\n\t" \
    "s_cmp_eq_u32 s91, 147456\n\t" \
    "s_cselect_b32 s91, 0, s91\n\t" \
    "s_add_u32 s91, s91, s89\n\t" \
    "v_add_u32 v22, s90, v0\n\t" \
    "v_xor_b32 v23, 64, v22\n\t" \
    "s_mul_i32 s70, s67, 7\n\t" \
    "s_add_u32 s69, s65, 0\n\t" \
    "s_xor_b32 s69, s69, s70\n\t" \
    "s_and_b32 s69, s69, 63\n\t" \
    "s_lshl_b32 s69, s69, 13\n\t" \
    "s_mul_i32 s70, s67, 524288\n\t" \
    "s_add_u32 s69, s69, s70\n\t" \
    "s_add_u32 s69, s69, 6291456\n\t" \
    "s_add_u32 s38, s36, s69\n\t" \
    "s_addc_u32 s39, s37, 0\n\t" \
    "s_waitcnt lgkmcnt(5)\n\t" \
    "v_mfma_f32_16x16x32_bf16 v[88:91], a[0:3], v[24:27], 0\n\t" \
    "ds_read_b128 v[48:51], v2 offset:128\n\t" \
    "s_waitcnt lgkmcnt(5)\n\t" \
    "v_mfma_f32_16x16x32_bf16 v[92:95], a[0:3], v[28:31], 0\n\t" \
    "ds_read_b128 v[52:55], v2 offset:18560\n\t" \
    "v_mfma_f32_16x16x32_bf16 v[96:99], a[72:75], v[24:27], 0\n\t" \
    "s_add_u32 m0, s91, 0\n\t" \
    "v_mfma_f32_16x16x32_bf16 v[100:103], a[72:75], v[28:31], 0\n\t" \
    "global_load_lds_dwordx4 v203, s[52:53]\n\t" \
    "s_waitcnt lgkmcnt(5)\n\t" \
    "v_mfma_f32_16x16x32_bf16 v[88:91], a[4:7], v[32:35], v[88:91]\n\t" \
    "ds_read_b128 v[56:59], v1 offset:256\n\t" \
    "s_waitcnt lgkmcnt(5)\n\t" \
    "v_mfma_f32_16x16x32_bf16 v[92:95], a[4:7], v[36:39], v[92:95]\n\t" \
    "ds_read_b128 v[60:63], v1 offset:18688\n\t" \
    "v_mfma_f32_16x16x32_bf16 v[96:99], a[76:79], v[32:35], v[96:99]\n\t" \
    "s_add_u32 m0, s91, 1024\n\t" \
    "v_mfma_f32_16x16x32_bf16 v[100:103], a[76:79], v[36:39], v[100:103]\n\t" \
    "global_load_lds_dwordx4 v204, s[52:53]\n\t" \
    "s_waitcnt lgkmcnt(5)\n\t" \
    "v_mfma_f32_16x16x32_bf16 v[88:91], a[8:11], v[40:43], v[88:91]\n\t" \
    "ds_read_b128 v[64:67], v2 offset:256\n\t" \
    "s_waitcnt lgkmcnt(5)\n\t" \
    "v_mfma_f32_16x16x32_bf16 v[92:95], a[8:11], v[44:47], v[92:95]\n\t" \
    "ds_read_b128 v[68:71], v2 offset:18688\n\t" \
    "v_mfma_f32_16x16x32_bf16 v[96:99], a[80:83], v[40:43], v[96:99]\n\t" \
    "s_add_u32 m0, s91, 2048\n\t" \
    "v_mfma_f32_16x16x32_bf16 v[100:103], a[80:83], v[44:47], v[100:103]\n\t" \
    "global_load_lds_dwordx4 v205, s[52:53]\n\t" \
    "s_waitcnt lgkmcnt(5)\n\t" \
    "v_mfma_f32_16x16x32_bf16 v[88:91], a[12:15], v[48:51], v[88:91]\n\t" \
    "ds_read_b128 v[24:27], v1 offset:384\n\t" \
    "s_waitcnt lgkmcnt(5)\n\t" \
    "v_mfma_f32_16x16x32_bf16 v[92:95], a[12:15], v[52:55], v[92:95]\n\t" \
    "ds_read_b128 v[28:31], v1 offset:18816\n\t" \
    "v_mfma_f32_16x16x32_bf16 v[96:99], a[84:87], v[48:51], v[96:99]\n\t" \
    "s_add_u32 m0, s91, 3072\n\t" \
    "v_mfma_f32_16x16x32_bf16 v[100:103], a[84:87], v[52:55], v[100:103]\n\t" \
    "global_load_lds_dwordx4 v206, s[52:53]\n\t" \
    "s_waitcnt lgkmcnt(5)\n\t" \
    "v_mfma_f32_16x16x32_bf16 v[88:91], a[16:19], v[56:59], v[88:91]\n\t" \
    "ds_read_b128 v[32:35], v2 offset:384\n\t" \
    "s_waitcnt lgkmcnt(5)\n\t" \
    "v_mfma_f32_16x16x32_bf16 v[92:95], a[16:19], v[60:63], v[92:95]\n\t" \
    "ds_read_b128 v[36:39], v2 offset:18816\n\t" \
    "v_mfma_f32_16x16x32_bf16 v[96:99], a[88:91], v[56:59], v[96:99]\n\t" \
    "s_add_u32 m0, s91, 4096\n\t" \
    "v_mfma_f32_16x16x32_bf16 v[100:103], a[88:91], v[60:63], v[100:103]\n\t" \
    "global_load_lds_dwordx4 v207, s[52:53]\n\t" \
    "s_waitcnt lgkmcnt(5)\n\t" \
    "v_mfma_f32_16x16x32_bf16 v[88:91], a[20:23], v[64:67], v[88:91]\n\t" \
    "ds_read_b128 v[40:43], v1 offset:512\n\t" \
    "s_waitcnt lgkmcnt(5)\n\t" \
    "v_mfma_f32_16x16x32_bf16 v[92:95], a[20:23], v[68:71], v[92:95]\n\t" \
    "ds_read_b128 v[44:47], v1 offset:18944\n\t" \
    "v_mfma_f32_16x16x32_bf16 v[96:99], a[92:95], v[64:67], v[96:99]\n\t" \
    "s_add_u32 m0, s91, 5120\n\t" \
    "v_mfma_f32_16x16x32_bf16 v[100:103], a[92:95], v[68:71], v[100:103]\n\t" \
    "global_load_lds_dwordx4 v208, s[52:53]\n\t" \
    "s_waitcnt lgkmcnt(5)\n\t" \
    "v_mfma_f32_16x16x32_bf16 v[88:91], a[24:27], v[24:27], v[88:91]\n\t" \
    "ds_read_b128 v[48:51], v2 offset:512\n\t" \
    "s_waitcnt lgkmcnt(5)\n\t" \
    "v_mfma_f32_16x16x32_bf16 v[92:95], a[24:27], v[28:31], v[92:95]\n\t" \
    "ds_read_b128 v[52:55], v2 offset:18944\n\t" \
    "v_mfma_f32_16x16x32_bf16 v[96:99], a[96:99], v[24:27], v[96:99]\n\t" \
    "s_add_u32 m0, s91, 6144\n\t" \
    "v_mfma_f32_16x16x32_bf16 v[100:103], a[96:99], v[28:31], v[100:103]\n\t" \
    "global_load_lds_dwordx4 v209, s[52:53]\n\t" \
    "s_waitcnt lgkmcnt(5)\n\t" \
    "v_mfma_f32_16x16x32_bf16 v[88:91], a[28:31], v[32:35], v[88:91]\n\t" \
    "ds_read_b128 v[56:59], v1 offset:640\n\t" \
    "s_waitcnt lgkmcnt(5)\n\t" \
    "v_mfma_f32_16x16x32_bf16 v[92:95], a[28:31], v[36:39], v[92:95]\n\t" \
    "ds_read_b128 v[60:63], v1 offset:19072\n\t" \
    "v_mfma_f32_16x16x32_bf16 v[96:99], a[100:103], v[32:35], v[96:99]\n\t" \
    "s_add_u32 m0, s91, 7168\n\t" \
    "v_mfma_f32_16x16x32_bf16 v[100:103], a[100:103], v[36:39], v[100:103]\n\t" \
    "global_load_lds_dwordx4 v210, s[52:53]\n\t" \
    "s_waitcnt lgkmcnt(5)\n\t" \
    "v_mfma_f32_16x16x32_bf16 v[88:91], a[32:35], v[40:43], v[88:91]\n\t" \
    "ds_read_b128 v[64:67], v2 offset:640\n\t" \
    "s_waitcnt lgkmcnt(5)\n\t" \
    "v_mfma_f32_16x16x32_bf16 v[92:95], a[32:35], v[44:47], v[92:95]\n\t" \
    "ds_read_b128 v[68:71], v2 offset:19072\n\t" \
    "v_mfma_f32_16x16x32_bf16 v[96:99], a[104:107], v[40:43], v[96:99]\n\t" \
    "s_add_u32 m0, s91, 8192\n\t" \
    "v_mfma_f32_16x16x32_bf16 v[100:103], a[104:107], v[44:47], v[100:103]\n\t" \
    "global_load_lds_dwordx4 v211, s[52:53]\n\t" \
    "s_waitcnt lgkmcnt(5)\n\t" \
    "v_mfma_f32_16x16x32_bf16 v[88:91], a[36:39], v[48:51], v[88:91]\n\t" \
    "ds_read_b128 v[24:27], v1 offset:768\n\t" \
    "s_waitcnt lgkmcnt(5)\n\t" \
    "v_mfma_f32_16x16x32_bf16 v[92:95], a[36:39], v[52:55], v[92:95]\n\t" \
    "ds_read_b128 v[28:31], v1 offset:19200\n\t" \
    "v_mfma_f32_16x16x32_bf16 v[96:99], a[108:111], v[48:51], v[96:99]\n\t" \
    "v_mfma_f32_16x16x32_bf16 v[100:103], a[108:111], v[52:55], v[100:103]\n\t" \
    "s_waitcnt lgkmcnt(5)\n\t" \
    "v_mfma_f32_16x16x32_bf16 v[88:91], a[40:43], v[56:59], v[88:91]\n\t" \
    "ds_read_b128 v[32:35], v2 offset:768\n\t" \
    "s_waitcnt lgkmcnt(5)\n\t" \
    "v_mfma_f32_16x16x32_bf16 v[92:95], a[40:43], v[60:63], v[92:95]\n\t" \
    "ds_read_b128 v[36:39], v2 offset:19200\n\t" \
    "v_mfma_f32_16x16x32_bf16 v[96:99], a[112:115], v[56:59], v[96:99]\n\t" \
    "s_waitcnt vmcnt(9)\n\t" \
    "s_barrier\n\t" \
    "v_mfma_f32_16x16x32_bf16 v[100:103], a[112:115], v[60:63], v[100:103]\n\t" \
    "s_cmp_lg_u32 s66, 0\n\t" \
    "s_cbranch_scc1 gx_SF16_%=\n\t" \
    "s_lshl_b32 s69, s67, 7\n\t" \
    "s_add_u32 s69, s69, 5849088\n\t" \
    "s_add_u32 s70, s65, -1\n\t" \
    "v_mov_b32 v156, s69\n\t" \
    "v_mov_b32 v157, s70\n\t" \
    "s_mov_b64 exec, 1\n\t" \
    "global_store_dword v156, v157, s[36:37] sc1\n\t" \
    "s_mov_b64 exec, -1\n\t" \
    "gx_SF16_%=:\n\t" \
    "s_cmp_lg_u32 s66, 1\n\t" \
    "s_cbranch_scc1 gx_SF17_%=\n\t" \
    "s_lshl_b32 s69, s67, 7\n\t" \
    "s_add_u32 s69, s69, 5771264\n\t" \
    "s_add_u32 s70, s65, 40\n\t" \
    "v_mov_b32 v156, s69\n\t" \
    "v_mov_b32 v157, s70\n\t" \
    "s_mov_b64 exec, 1\n\t" \
    "global_store_dword v156, v157, s[36:37] sc1\n\t" \
    "s_mov_b64 exec, -1\n\t" \
    "gx_SF17_%=:\n\t" \
    "s_waitcnt lgkmcnt(5)\n\t" \
    "v_mfma_f32_16x16x32_bf16 v[88:91], a[44:47], v[64:67], v[88:91]\n\t" \
    "ds_read_b128 v[40:43], v1 offset:896\n\t" \
    "s_waitcnt lgkmcnt(5)\n\t" \
    "v_mfma_f32_16x16x32_bf16 v[92:95], a[44:47], v[68:71], v[92:95]\n\t" \
    "ds_read_b128 v[44:47], v1 offset:19328\n\t" \
    "v_mfma_f32_16x16x32_bf16 v[96:99], a[116:119], v[64:67], v[96:99]\n\t" \
    "v_mfma_f32_16x16x32_bf16 v[100:103], a[116:119], v[68:71], v[100:103]\n\t" \
    "s_waitcnt lgkmcnt(5)\n\t" \
    "v_mfma_f32_16x16x32_bf16 v[88:91], a[48:51], v[24:27], v[88:91]\n\t" \
    "ds_read_b128 v[48:51], v2 offset:896\n\t" \
    "s_waitcnt lgkmcnt(5)\n\t" \
    "v_mfma_f32_16x16x32_bf16 v[92:95], a[48:51], v[28:31], v[92:95]\n\t" \
    "ds_read_b128 v[52:55], v2 offset:19328\n\t" \
    "v_mfma_f32_16x16x32_bf16 v[96:99], a[120:123], v[24:27], v[96:99]\n\t" \
    "v_mfma_f32_16x16x32_bf16 v[100:103], a[120:123], v[28:31], v[100:103]\n\t" \
    "s_waitcnt lgkmcnt(5)\n\t" \
    "v_mfma_f32_16x16x32_bf16 v[88:91], a[52:55], v[32:35], v[88:91]\n\t" \
    "ds_read_b128 v[56:59], v1 offset:1024\n\t" \
    "s_waitcnt lgkmcnt(5)\n\t" \
    "v_mfma_f32_16x16x32_bf16 v[92:95], a[52:55], v[36:39], v[92:95]\n\t" \
    "ds_read_b128 v[60:63], v1 offset:19456\n\t" \
    "v_mfma_f32_16x16x32_bf16 v[96:99], a[124:127], v[32:35], v[96:99]\n\t" \
    "v_mfma_f32_16x16x32_bf16 v[100:103], a[124:127], v[36:39], v[100:103]\n\t" \
    "s_waitcnt lgkmcnt(5)\n\t" \
    "v_mfma_f32_16x16x32_bf16 v[88:91], a[56:59], v[40:43], v[88:91]\n\t" \
    "ds_read_b128 v[64:67], v2 offset:1024\n\t" \
    "s_waitcnt lgkmcnt(5)\n\t" \
    "v_mfma_f32_16x16x32_bf16 v[92:95], a[56:59], v[44:47], v[92:95]\n\t" \
    "ds_read_b128 v[68:71], v2 offset:19456\n\t" \
    "v_mfma_f32_16x16x32_bf16 v[96:99], a[128:131], v[40:43], v[96:99]\n\t" \
    "v_mfma_f32_16x16x32_bf16 v[100:103], a[128:131], v[44:47], v[100:103]\n\t" \
    "s_waitcnt lgkmcnt(5)\n\t" \
    "v_mfma_f32_16x16x32_bf16 v[88:91], a[60:63], v[48:51], v[88:91]\n\t" \
    "ds_read_b128 v[24:27], v22 offset:0\n\t" \
    "s_waitcnt lgkmcnt(5)\n\t" \
    "v_mfma_f32_16x16x32_bf16 v[92:95], a[60:63], v[52:55], v[92:95]\n\t" \
    "ds_read_b128 v[28:31], v22 offset:18432\n\t" \
    "v_mfma_f32_16x16x32_bf16 v[96:99], a[132:135], v[48:51], v[96:99]\n\t" \
    "v_mfma_f32_16x16x32_bf16 v[100:103], a[132:135], v[52:55], v[100:103]\n\t" \
    "s_waitcnt lgkmcnt(5)\n\t" \
    "v_mfma_f32_16x16x32_bf16 v[88:91], a[64:67], v[56:59], v[88:91]\n\t" \
    "ds_read_b128 v[32:35], v23 offset:0\n\t" \
    "s_waitcnt lgkmcnt(5)\n\t" \
    "v_mfma_f32_16x16x32_bf16 v[92:95], a[64:67], v[60:63], v[92:95]\n\t" \
    "ds_read_b128 v[36:39], v23 offset:18432\n\t" \
    "v_mfma_f32_16x16x32_bf16 v[96:99], a[136:139], v[56:59], v[96:99]\n\t" \
    "v_mfma_f32_16x16x32_bf16 v[100:103], a[136:139], v[60:63], v[100:103]\n\t" \
    "s_waitcnt lgkmcnt(5)\n\t" \
    "v_mfma_f32_16x16x32_bf16 v[88:91], a[68:71], v[64:67], v[88:91]\n\t" \
    "ds_read_b128 v[40:43], v22 offset:128\n\t" \
    "s_waitcnt lgkmcnt(5)\n\t" \
    "v_mfma_f32_16x16x32_bf16 v[92:95], a[68:71], v[68:71], v[92:95]\n\t" \
    "ds_read_b128 v[44:47], v22 offset:18560\n\t" \
    "v_mfma_f32_16x16x32_bf16 v[96:99], a[140:143], v[64:67], v[96:99]\n\t" \
    "v_mfma_f32_16x16x32_bf16 v[100:103], a[140:143], v[68:71], v[100:103]\n\t" \
    "v_mov_b32 v1, v22\n\t" \
    "v_mov_b32 v2, v23\n\t" \
    "v_cvt_pk_bf16_f32 v104, v88, v89\n\t" \
    "v_cvt_pk_bf16_f32 v105, v90, v91\n\t" \
    "s_nop 2\n\t" \
    "v_cvt_pk_bf16_f32 v106, v96, v97\n\t" \
    "v_cvt_pk_bf16_f32 v107, v98, v99\n\t" \
    "global_store_dwordx4 v8, v[104:107], s[38:39] offset:0 sc1\n\t" \
    "v_cvt_pk_bf16_f32 v108, v92, v93\n\t" \
    "v_cvt_pk_bf16_f32 v109, v94, v95\n\t" \
    "v_cvt_pk_bf16_f32 v110, v100, v101\n\t" \
    "v_cvt_pk_bf16_f32 v111, v102, v103\n\t" \
    "global_store_dwordx4 v8, v[108:111], s[38:39] offset:1024 sc1\n\t" \
    "s_add_u32 s65, s65, 1\n\t" \
    "s_cmp_lt_u32 s65, 64\n\t" \
    "s_cbranch_scc1 gx_l315_%=\n\t" \
    "s_waitcnt vmcnt(0) lgkmcnt(0)\n\t" \
    "s_barrier\n\t" \
    "s_cmp_lg_u32 s66, 0\n\t" \
    "s_cbranch_scc1 gx_SF18_%=\n\t" \
    "s_lshl_b32 s69, s67, 7\n\t" \
    "s_add_u32 s69, s69, 5849088\n\t" \
    "s_add_u32 s70, s65, -1\n\t" \
    "v_mov_b32 v156, s69\n\t" \
    "v_mov_b32 v157, s70\n\t" \
    "s_mov_b64 exec, 1\n\t" \
    "global_store_dword v156, v157, s[36:37] sc1\n\t" \
    "s_mov_b64 exec, -1\n\t" \
    "gx_SF18_%=:\n\t" \
    "gx_last_%=:\n\t" \
    ""
#define ASM_GV \
    "s_load_dwordx2 s[42:43], %3, 56\n\t" \
    "s_load_dwordx2 s[44:45], %3, 64\n\t" \
    "s_load_dwordx2 s[36:37], %3, 0x58\n\t" \
    "s_load_dwordx2 s[50:51], %3, 0x48\n\t" \
    "s_mov_b32 s66, %4\n\t" \
    "s_mov_b32 s67, %5\n\t" \
    "s_mov_b32 s68, 262144\n\t" \
    "s_mov_b32 s72, 0\n\t" \
    "s_mov_b32 s83, -1\n\t" \
    "s_mov_b32 s65, 0\n\t" \
    "s_lshl_b32 s82, s66, 10\n\t" \
    "s_waitcnt lgkmcnt(0)\n\t" \
    "s_lshr_b32 s65, s67, 5\n\t" \
    "s_cselect_b32 s88, 24, 64\n\t" \
    "s_cmp_lt_u32 s65, 24\n\t" \
    "s_addc_u32 s84, s65, 1\n\t" \
    "s_cmp_lt_u32 s84, 24\n\t" \
    "s_addc_u32 s85, s84, 1\n\t" \
    "s_mov_b32 s86, -1\n\t" \
    "s_mov_b32 s87, -1\n\t" \
    "s_add_u32 s38, s36, 48234496\n\t" \
    "s_addc_u32 s39, s37, 0\n\t" \
    "s_and_b32 s69, s67, 31\n\t" \
    "s_mul_i32 s69, s69, 147456\n\t" \
    "s_add_u32 s44, s44, s69\n\t" \
    "s_addc_u32 s45, s45, 0\n\t" \
    "s_mul_i32 s89, s66, 9216\n\t" \
    "s_mov_b32 s90, 0\n\t" \
    "v_mov_b32 v196, -1\n\t" \
    "v_mov_b32 v197, -1\n\t" \
    "v_mov_b32 v198, -1\n\t" \
    "v_mov_b32 v199, -1\n\t" \
    "s_add_u32 s46, s42, 2097152\n\t" \
    "s_addc_u32 s47, s43, 0\n\t" \
    "s_add_u32 m0, s82, 65536\n\t" \
    "s_nop 0\n\t" \
    "global_load_lds_dwordx4 v200, s[46:47]\n\t" \
    "s_add_u32 s46, s42, 2228224\n\t" \
    "s_addc_u32 s47, s43, 0\n\t" \
    "s_add_u32 m0, s82, 69632\n\t" \
    "s_nop 0\n\t" \
    "global_load_lds_dwordx4 v200, s[46:47]\n\t" \
    "s_add_u32 s46, s42, 2359296\n\t" \
    "s_addc_u32 s47, s43, 0\n\t" \
    "s_add_u32 m0, s82, 73728\n\t" \
    "s_nop 0\n\t" \
    "global_load_lds_dwordx4 v200, s[46:47]\n\t" \
    "s_add_u32 s46, s42, 2490368\n\t" \
    "s_addc_u32 s47, s43, 0\n\t" \
    "s_add_u32 m0, s82, 77824\n\t" \
    "s_nop 0\n\t" \
    "global_load_lds_dwordx4 v200, s[46:47]\n\t" \
    "s_add_u32 s46, s42, 2621440\n\t" \
    "s_addc_u32 s47, s43, 0\n\t" \
    "s_add_u32 m0, s82, 81920\n\t" \
    "s_nop 0\n\t" \
    "global_load_lds_dwordx4 v200, s[46:47]\n\t" \
    "s_add_u32 s46, s42, 2752512\n\t" \
    "s_addc_u32 s47, s43, 0\n\t" \
    "s_add_u32 m0, s82, 86016\n\t" \
    "s_nop 0\n\t" \
    "global_load_lds_dwordx4 v200, s[46:47]\n\t" \
    "s_add_u32 s46, s42, 2883584\n\t" \
    "s_addc_u32 s47, s43, 0\n\t" \
    "s_add_u32 m0, s82, 90112\n\t" \
    "s_nop 0\n\t" \
    "global_load_lds_dwordx4 v200, s[46:47]\n\t" \
    "s_add_u32 s46, s42, 3014656\n\t" \
    "s_addc_u32 s47, s43, 0\n\t" \
    "s_add_u32 m0, s82, 94208\n\t" \
    "s_nop 0\n\t" \
    "global_load_lds_dwordx4 v200, s[46:47]\n\t" \
    "s_add_u32 s46, s42, 3145728\n\t" \
    "s_addc_u32 s47, s43, 0\n\t" \
    "s_add_u32 m0, s82, 98304\n\t" \
    "s_nop 0\n\t" \
    "global_load_lds_dwordx4 v200, s[46:47]\n\t" \
    "s_add_u32 s46, s42, 3276800\n\t" \
    "s_addc_u32 s47, s43, 0\n\t" \
    "s_add_u32 m0, s82, 102400\n\t" \
    "s_nop 0\n\t" \
    "global_load_lds_dwordx4 v200, s[46:47]\n\t" \
    "s_add_u32 s46, s42, 3407872\n\t" \
    "s_addc_u32 s47, s43, 0\n\t" \
    "s_add_u32 m0, s82, 106496\n\t" \
    "s_nop 0\n\t" \
    "global_load_lds_dwordx4 v200, s[46:47]\n\t" \
    "s_add_u32 s46, s42, 3538944\n\t" \
    "s_addc_u32 s47, s43, 0\n\t" \
    "s_add_u32 m0, s82, 110592\n\t" \
    "s_nop 0\n\t" \
    "global_load_lds_dwordx4 v200, s[46:47]\n\t" \
    "s_add_u32 s46, s42, 3670016\n\t" \
    "s_addc_u32 s47, s43, 0\n\t" \
    "s_add_u32 m0, s82, 114688\n\t" \
    "s_nop 0\n\t" \
    "global_load_lds_dwordx4 v200, s[46:47]\n\t" \
    "s_add_u32 s46, s42, 3801088\n\t" \
    "s_addc_u32 s47, s43, 0\n\t" \
    "s_add_u32 m0, s82, 118784\n\t" \
    "s_nop 0\n\t" \
    "global_load_lds_dwordx4 v200, s[46:47]\n\t" \
    "s_add_u32 s46, s42, 3932160\n\t" \
    "s_addc_u32 s47, s43, 0\n\t" \
    "s_add_u32 m0, s82, 122880\n\t" \
    "s_nop 0\n\t" \
    "global_load_lds_dwordx4 v200, s[46:47]\n\t" \
    "s_add_u32 s46, s42, 4063232\n\t" \
    "s_addc_u32 s47, s43, 0\n\t" \
    "s_add_u32 m0, s82, 126976\n\t" \
    "s_nop 0\n\t" \
    "global_load_lds_dwordx4 v200, s[46:47]\n\t" \
    "s_add_u32 s46, s42, 4194304\n\t" \
    "s_addc_u32 s47, s43, 0\n\t" \
    "s_add_u32 m0, s82, 131072\n\t" \
    "s_nop 0\n\t" \
    "global_load_lds_dwordx4 v200, s[46:47]\n\t" \
    "s_add_u32 s46, s42, 4325376\n\t" \
    "s_addc_u32 s47, s43, 0\n\t" \
    "s_add_u32 m0, s82, 135168\n\t" \
    "s_nop 0\n\t" \
    "global_load_lds_dwordx4 v200, s[46:47]\n\t" \
    "s_add_u32 s46, s42, 4456448\n\t" \
    "s_addc_u32 s47, s43, 0\n\t" \
    "s_add_u32 m0, s82, 139264\n\t" \
    "s_nop 0\n\t" \
    "global_load_lds_dwordx4 v200, s[46:47]\n\t" \
    "s_add_u32 s46, s42, 4587520\n\t" \
    "s_addc_u32 s47, s43, 0\n\t" \
    "s_add_u32 m0, s82, 143360\n\t" \
    "s_nop 0\n\t" \
    "global_load_lds_dwordx4 v200, s[46:47]\n\t" \
    "s_waitcnt vmcnt(32) lgkmcnt(0)\n\t" \
    "s_barrier\n\t" \
    "ds_read_b64_tr_b16 a[0:1], v201 offset:0\n\t" \
    "ds_read_b64_tr_b16 a[2:3], v201 offset:1024\n\t" \
    "ds_read_b64_tr_b16 a[4:5], v201 offset:8192\n\t" \
    "ds_read_b64_tr_b16 a[6:7], v201 offset:9216\n\t" \
    "ds_read_b64_tr_b16 a[72:73], v202 offset:0\n\t" \
    "ds_read_b64_tr_b16 a[74:75], v202 offset:1024\n\t" \
    "ds_read_b64_tr_b16 a[76:77], v202 offset:8192\n\t" \
    "ds_read_b64_tr_b16 a[78:79], v202 offset:9216\n\t" \
    "s_waitcnt vmcnt(28) lgkmcnt(0)\n\t" \
    "s_barrier\n\t" \
    "ds_read_b64_tr_b16 a[8:9], v201 offset:16384\n\t" \
    "ds_read_b64_tr_b16 a[10:11], v201 offset:17408\n\t" \
    "ds_read_b64_tr_b16 a[12:13], v201 offset:24576\n\t" \
    "ds_read_b64_tr_b16 a[14:15], v201 offset:25600\n\t" \
    "ds_read_b64_tr_b16 a[80:81], v202 offset:16384\n\t" \
    "ds_read_b64_tr_b16 a[82:83], v202 offset:17408\n\t" \
    "ds_read_b64_tr_b16 a[84:85], v202 offset:24576\n\t" \
    "ds_read_b64_tr_b16 a[86:87], v202 offset:25600\n\t" \
    "s_waitcnt vmcnt(24) lgkmcnt(0)\n\t" \
    "s_barrier\n\t" \
    "ds_read_b64_tr_b16 a[16:17], v201 offset:32768\n\t" \
    "ds_read_b64_tr_b16 a[18:19], v201 offset:33792\n\t" \
    "ds_read_b64_tr_b16 a[20:21], v201 offset:40960\n\t" \
    "ds_read_b64_tr_b16 a[22:23], v201 offset:41984\n\t" \
    "ds_read_b64_tr_b16 a[88:89], v202 offset:32768\n\t" \
    "ds_read_b64_tr_b16 a[90:91], v202 offset:33792\n\t" \
    "ds_read_b64_tr_b16 a[92:93], v202 offset:40960\n\t" \
    "ds_read_b64_tr_b16 a[94:95], v202 offset:41984\n\t" \
    "s_waitcnt vmcnt(20) lgkmcnt(0)\n\t" \
    "s_barrier\n\t" \
    "ds_read_b64_tr_b16 a[24:25], v201 offset:49152\n\t" \
    "ds_read_b64_tr_b16 a[26:27], v201 offset:50176\n\t" \
    "ds_read_b64_tr_b16 a[28:29], v201 offset:57344\n\t" \
    "ds_read_b64_tr_b16 a[30:31], v201 offset:58368\n\t" \
    "ds_read_b64_tr_b16 a[96:97], v202 offset:49152\n\t" \
    "ds_read_b64_tr_b16 a[98:99], v202 offset:50176\n\t" \
    "ds_read_b64_tr_b16 a[100:101], v202 offset:57344\n\t" \
    "ds_read_b64_tr_b16 a[102:103], v202 offset:58368\n\t" \
    "s_add_u32 s46, s44, 0\n\t" \
    "s_addc_u32 s47, s45, 0\n\t" \
    "s_mul_i32 s69, s82, 9\n\t" \
    "s_add_u32 m0, s69, 0\n\t" \
    "s_nop 0\n\t" \
    "global_load_lds_dwordx4 v203, s[46:47]\n\t" \
    "s_add_u32 m0, s69, 1024\n\t" \
    "s_nop 0\n\t" \
    "global_load_lds_dwordx4 v204, s[46:47]\n\t" \
    "s_add_u32 m0, s69, 2048\n\t" \
    "s_nop 0\n\t" \
    "global_load_lds_dwordx4 v205, s[46:47]\n\t" \
    "s_add_u32 m0, s69, 3072\n\t" \
    "s_nop 0\n\t" \
    "global_load_lds_dwordx4 v206, s[46:47]\n\t" \
    "s_add_u32 m0, s69, 4096\n\t" \
    "s_nop 0\n\t" \
    "global_load_lds_dwordx4 v207, s[46:47]\n\t" \
    "s_add_u32 m0, s69, 5120\n\t" \
    "s_nop 0\n\t" \
    "global_load_lds_dwordx4 v208, s[46:47]\n\t" \
    "s_add_u32 m0, s69, 6144\n\t" \
    "s_nop 0\n\t" \
    "global_load_lds_dwordx4 v209, s[46:47]\n\t" \
    "s_add_u32 m0, s69, 7168\n\t" \
    "s_nop 0\n\t" \
    "global_load_lds_dwordx4 v210, s[46:47]\n\t" \
    "s_add_u32 m0, s69, 8192\n\t" \
    "s_nop 0\n\t" \
    "global_load_lds_dwordx4 v211, s[46:47]\n\t" \
    "s_min_u32 s69, s65, 63\n\t" \
    "s_mul_i32 s69, s69, 36864\n\t" \
    "s_add_u32 s52, s50, s69\n\t" \
    "s_addc_u32 s53, s51, 0\n\t" \
    "global_load_dwordx4 v[120:123], v203, s[52:53]\n\t" \
    "global_load_dwordx4 v[124:127], v204, s[52:53]\n\t" \
    "global_load_dwordx4 v[128:131], v205, s[52:53]\n\t" \
    "global_load_dwordx4 v[132:135], v206, s[52:53]\n\t" \
    "global_load_dwordx4 v[136:139], v207, s[52:53]\n\t" \
    "global_load_dwordx4 v[140:143], v208, s[52:53]\n\t" \
    "global_load_dwordx4 v[144:147], v209, s[52:53]\n\t" \
    "global_load_dwordx4 v[148:151], v210, s[52:53]\n\t" \
    "global_load_dwordx4 v[152:155], v211, s[52:53]\n\t" \
    "s_waitcnt vmcnt(34) lgkmcnt(0)\n\t" \
    "s_barrier\n\t" \
    "v_add_u32 v201, 0x10000, v201\n\t" \
    "v_add_u32 v202, 0x10000, v202\n\t" \
    "ds_read_b64_tr_b16 a[32:33], v201 offset:0\n\t" \
    "ds_read_b64_tr_b16 a[34:35], v201 offset:1024\n\t" \
    "ds_read_b64_tr_b16 a[36:37], v201 offset:8192\n\t" \
    "ds_read_b64_tr_b16 a[38:39], v201 offset:9216\n\t" \
    "ds_read_b64_tr_b16 a[104:105], v202 offset:0\n\t" \
    "ds_read_b64_tr_b16 a[106:107], v202 offset:1024\n\t" \
    "ds_read_b64_tr_b16 a[108:109], v202 offset:8192\n\t" \
    "ds_read_b64_tr_b16 a[110:111], v202 offset:9216\n\t" \
    "s_waitcnt vmcnt(30) lgkmcnt(0)\n\t" \
    "s_barrier\n\t" \
    "ds_read_b64_tr_b16 a[40:41], v201 offset:16384\n\t" \
    "ds_read_b64_tr_b16 a[42:43], v201 offset:17408\n\t" \
    "ds_read_b64_tr_b16 a[44:45], v201 offset:24576\n\t" \
    "ds_read_b64_tr_b16 a[46:47], v201 offset:25600\n\t" \
    "ds_read_b64_tr_b16 a[112:113], v202 offset:16384\n\t" \
    "ds_read_b64_tr_b16 a[114:115], v202 offset:17408\n\t" \
    "ds_read_b64_tr_b16 a[116:117], v202 offset:24576\n\t" \
    "ds_read_b64_tr_b16 a[118:119], v202 offset:25600\n\t" \
    "s_add_u32 s46, s44, 36864\n\t" \
    "s_addc_u32 s47, s45, 0\n\t" \
    "s_mul_i32 s69, s82, 9\n\t" \
    "s_add_u32 m0, s69, 36864\n\t" \
    "s_nop 0\n\t" \
    "global_load_lds_dwordx4 v203, s[46:47]\n\t" \
    "s_add_u32 m0, s69, 37888\n\t" \
    "s_nop 0\n\t" \
    "global_load_lds_dwordx4 v204, s[46:47]\n\t" \
    "s_add_u32 m0, s69, 38912\n\t" \
    "s_nop 0\n\t" \
    "global_load_lds_dwordx4 v205, s[46:47]\n\t" \
    "s_add_u32 m0, s69, 39936\n\t" \
    "s_nop 0\n\t" \
    "global_load_lds_dwordx4 v206, s[46:47]\n\t" \
    "s_add_u32 m0, s69, 40960\n\t" \
    "s_nop 0\n\t" \
    "global_load_lds_dwordx4 v207, s[46:47]\n\t" \
    "s_add_u32 m0, s69, 41984\n\t" \
    "s_nop 0\n\t" \
    "global_load_lds_dwordx4 v208, s[46:47]\n\t" \
    "s_add_u32 m0, s69, 43008\n\t" \
    "s_nop 0\n\t" \
    "global_load_lds_dwordx4 v209, s[46:47]\n\t" \
    "s_add_u32 m0, s69, 44032\n\t" \
    "s_nop 0\n\t" \
    "global_load_lds_dwordx4 v210, s[46:47]\n\t" \
    "s_add_u32 m0, s69, 45056\n\t" \
    "s_nop 0\n\t" \
    "global_load_lds_dwordx4 v211, s[46:47]\n\t" \
    "s_waitcnt vmcnt(35) lgkmcnt(0)\n\t" \
    "s_barrier\n\t" \
    "ds_read_b64_tr_b16 a[48:49], v201 offset:32768\n\t" \
    "ds_read_b64_tr_b16 a[50:51], v201 offset:33792\n\t" \
    "ds_read_b64_tr_b16 a[52:53], v201 offset:40960\n\t" \
    "ds_read_b64_tr_b16 a[54:55], v201 offset:41984\n\t" \
    "ds_read_b64_tr_b16 a[120:121], v202 offset:32768\n\t" \
    "ds_read_b64_tr_b16 a[122:123], v202 offset:33792\n\t" \
    "ds_read_b64_tr_b16 a[124:125], v202 offset:40960\n\t" \
    "ds_read_b64_tr_b16 a[126:127], v202 offset:41984\n\t" \
    "s_waitcnt vmcnt(31) lgkmcnt(0)\n\t" \
    "s_barrier\n\t" \
    "ds_read_b64_tr_b16 a[56:57], v201 offset:49152\n\t" \
    "ds_read_b64_tr_b16 a[58:59], v201 offset:50176\n\t" \
    "ds_read_b64_tr_b16 a[60:61], v201 offset:57344\n\t" \
    "ds_read_b64_tr_b16 a[62:63], v201 offset:58368\n\t" \
    "ds_read_b64_tr_b16 a[128:129], v202 offset:49152\n\t" \
    "ds_read_b64_tr_b16 a[130:131], v202 offset:50176\n\t" \
    "ds_read_b64_tr_b16 a[132:133], v202 offset:57344\n\t" \
    "ds_read_b64_tr_b16 a[134:135], v202 offset:58368\n\t" \
    "s_add_u32 s46, s44, 73728\n\t" \
    "s_addc_u32 s47, s45, 0\n\t" \
    "s_mul_i32 s69, s82, 9\n\t" \
    "s_add_u32 m0, s69, 73728\n\t" \
    "s_nop 0\n\t" \
    "global_load_lds_dwordx4 v203, s[46:47]\n\t" \
    "s_add_u32 m0, s69, 74752\n\t" \
    "s_nop 0\n\t" \
    "global_load_lds_dwordx4 v204, s[46:47]\n\t" \
    "s_add_u32 m0, s69, 75776\n\t" \
    "s_nop 0\n\t" \
    "global_load_lds_dwordx4 v205, s[46:47]\n\t" \
    "s_add_u32 m0, s69, 76800\n\t" \
    "s_nop 0\n\t" \
    "global_load_lds_dwordx4 v206, s[46:47]\n\t" \
    "s_add_u32 m0, s69, 77824\n\t" \
    "s_nop 0\n\t" \
    "global_load_lds_dwordx4 v207, s[46:47]\n\t" \
    "s_add_u32 m0, s69, 78848\n\t" \
    "s_nop 0\n\t" \
    "global_load_lds_dwordx4 v208, s[46:47]\n\t" \
    "s_add_u32 m0, s69, 79872\n\t" \
    "s_nop 0\n\t" \
    "global_load_lds_dwordx4 v209, s[46:47]\n\t" \
    "s_add_u32 m0, s69, 80896\n\t" \
    "s_nop 0\n\t" \
    "global_load_lds_dwordx4 v210, s[46:47]\n\t" \
    "s_add_u32 m0, s69, 81920\n\t" \
    "s_nop 0\n\t" \
    "global_load_lds_dwordx4 v211, s[46:47]\n\t" \
    "v_mbcnt_lo_u32_b32 v164, -1, 0\n\t" \
    "v_mbcnt_hi_u32_b32 v164, -1, v164\n\t" \
    "v_lshlrev_b32 v164, 6, v164\n\t" \
    "v_add_u32 v164, 6225920, v164\n\t" \
    "v_mov_b32 v165, 6234112\n\t" \
    "global_load_dword v165, v165, s[36:37] sc1\n\t" \
    "global_load_dwordx2 v[166:167], v164, s[36:37] sc1\n\t" \
    "global_load_dword v168, v164, s[36:37] offset:2048 sc1\n\t" \
    "s_waitcnt vmcnt(39) lgkmcnt(0)\n\t" \
    "s_barrier\n\t" \
    "v_add_u32 v201, 0x10000, v201\n\t" \
    "v_add_u32 v202, 0x10000, v202\n\t" \
    "ds_read_b64_tr_b16 a[64:65], v201 offset:0\n\t" \
    "ds_read_b64_tr_b16 a[66:67], v201 offset:1024\n\t" \
    "ds_read_b64_tr_b16 a[68:69], v201 offset:8192\n\t" \
    "ds_read_b64_tr_b16 a[70:71], v201 offset:9216\n\t" \
    "ds_read_b64_tr_b16 a[136:137], v202 offset:0\n\t" \
    "ds_read_b64_tr_b16 a[138:139], v202 offset:1024\n\t" \
    "ds_read_b64_tr_b16 a[140:141], v202 offset:8192\n\t" \
    "ds_read_b64_tr_b16 a[142:143], v202 offset:9216\n\t" \
    "v_add_u32 v201, 0xfffe0000, v201\n\t" \
    "v_add_u32 v202, 0xfffe0000, v202\n\t" \
    "gv_jn2_%=:\n\t" \
    "s_waitcnt lgkmcnt(0)\n\t" \
    "s_barrier\n\t" \
    "s_add_u32 s46, s44, 110592\n\t" \
    "s_addc_u32 s47, s45, 0\n\t" \
    "s_mul_i32 s69, s82, 9\n\t" \
    "s_add_u32 m0, s69, 110592\n\t" \
    "s_nop 0\n\t" \
    "global_load_lds_dwordx4 v203, s[46:47]\n\t" \
    "s_add_u32 m0, s69, 111616\n\t" \
    "s_nop 0\n\t" \
    "global_load_lds_dwordx4 v204, s[46:47]\n\t" \
    "s_add_u32 m0, s69, 112640\n\t" \
    "s_nop 0\n\t" \
    "global_load_lds_dwordx4 v205, s[46:47]\n\t" \
    "s_add_u32 m0, s69, 113664\n\t" \
    "s_nop 0\n\t" \
    "global_load_lds_dwordx4 v206, s[46:47]\n\t" \
    "s_add_u32 m0, s69, 114688\n\t" \
    "s_nop 0\n\t" \
    "global_load_lds_dwordx4 v207, s[46:47]\n\t" \
    "s_add_u32 m0, s69, 115712\n\t" \
    "s_nop 0\n\t" \
    "global_load_lds_dwordx4 v208, s[46:47]\n\t" \
    "s_add_u32 m0, s69, 116736\n\t" \
    "s_nop 0\n\t" \
    "global_load_lds_dwordx4 v209, s[46:47]\n\t" \
    "s_add_u32 m0, s69, 117760\n\t" \
    "s_nop 0\n\t" \
    "global_load_lds_dwordx4 v210, s[46:47]\n\t" \
    "s_add_u32 m0, s69, 118784\n\t" \
    "s_nop 0\n\t" \
    "global_load_lds_dwordx4 v211, s[46:47]\n\t" \
    "s_waitcnt vmcnt(39) lgkmcnt(0)\n\t" \
    "s_barrier\n\t" \
    "ds_read_b64_tr_b16 a[144:145], v212 offset:0\n\t" \
    "ds_read_b64_tr_b16 a[146:147], v212 offset:18432\n\t" \
    "ds_read_b64_tr_b16 a[160:161], v213 offset:0\n\t" \
    "ds_read_b64_tr_b16 a[162:163], v213 offset:18432\n\t" \
    "ds_read_b64_tr_b16 a[176:177], v214 offset:0\n\t" \
    "ds_read_b64_tr_b16 a[178:179], v214 offset:18432\n\t" \
    "s_waitcnt lgkmcnt(6)\n\t" \
    "ds_read_b64_tr_b16 a[192:193], v215 offset:0\n\t" \
    "ds_read_b64_tr_b16 a[194:195], v215 offset:18432\n\t" \
    "ds_read_b64_tr_b16 a[208:209], v212 offset:128\n\t" \
    "ds_read_b64_tr_b16 a[210:211], v212 offset:18560\n\t" \
    "ds_read_b64_tr_b16 a[224:225], v213 offset:128\n\t" \
    "ds_read_b64_tr_b16 a[226:227], v213 offset:18560\n\t" \
    "s_waitcnt lgkmcnt(6)\n\t" \
    "ds_read_b64_tr_b16 a[240:241], v214 offset:128\n\t" \
    "ds_read_b64_tr_b16 a[242:243], v214 offset:18560\n\t" \
    "ds_read_b64_tr_b16 v[224:225], v215 offset:128\n\t" \
    "ds_read_b64_tr_b16 v[226:227], v215 offset:18560\n\t" \
    "ds_read_b64_tr_b16 v[240:241], v212 offset:256\n\t" \
    "ds_read_b64_tr_b16 v[242:243], v212 offset:18688\n\t" \
    "s_waitcnt lgkmcnt(6)\n\t" \
    "v_add_u32 v212, 0x9000, v212\n\t" \
    "v_add_u32 v213, 0x9000, v213\n\t" \
    "v_add_u32 v214, 0x9000, v214\n\t" \
    "v_add_u32 v215, 0x9000, v215\n\t" \
    "s_waitcnt vmcnt(21) lgkmcnt(0)\n\t" \
    "s_barrier\n\t" \
    "ds_read_b64_tr_b16 a[148:149], v212 offset:0\n\t" \
    "ds_read_b64_tr_b16 a[150:151], v212 offset:18432\n\t" \
    "ds_read_b64_tr_b16 a[164:165], v213 offset:0\n\t" \
    "ds_read_b64_tr_b16 a[166:167], v213 offset:18432\n\t" \
    "ds_read_b64_tr_b16 a[180:181], v214 offset:0\n\t" \
    "ds_read_b64_tr_b16 a[182:183], v214 offset:18432\n\t" \
    "s_waitcnt lgkmcnt(6)\n\t" \
    "ds_read_b64_tr_b16 a[196:197], v215 offset:0\n\t" \
    "ds_read_b64_tr_b16 a[198:199], v215 offset:18432\n\t" \
    "ds_read_b64_tr_b16 a[212:213], v212 offset:128\n\t" \
    "ds_read_b64_tr_b16 a[214:215], v212 offset:18560\n\t" \
    "ds_read_b64_tr_b16 a[228:229], v213 offset:128\n\t" \
    "ds_read_b64_tr_b16 a[230:231], v213 offset:18560\n\t" \
    "s_waitcnt lgkmcnt(6)\n\t" \
    "ds_read_b64_tr_b16 a[244:245], v214 offset:128\n\t" \
    "ds_read_b64_tr_b16 a[246:247], v214 offset:18560\n\t" \
    "ds_read_b64_tr_b16 v[228:229], v215 offset:128\n\t" \
    "ds_read_b64_tr_b16 v[230:231], v215 offset:18560\n\t" \
    "ds_read_b64_tr_b16 v[244:245], v212 offset:256\n\t" \
    "ds_read_b64_tr_b16 v[246:247], v212 offset:18688\n\t" \
    "s_waitcnt lgkmcnt(6)\n\t" \
    "v_add_u32 v212, 0x9000, v212\n\t" \
    "v_add_u32 v213, 0x9000, v213\n\t" \
    "v_add_u32 v214, 0x9000, v214\n\t" \
    "v_add_u32 v215, 0x9000, v215\n\t" \
    "ds_write_b128 v3, v[120:123] offset:0\n\t" \
    "ds_write_b128 v3, v[124:127] offset:1024\n\t" \
    "ds_write_b128 v3, v[128:131] offset:2048\n\t" \
    "ds_write_b128 v3, v[132:135] offset:3072\n\t" \
    "ds_write_b128 v3, v[136:139] offset:4096\n\t" \
    "ds_write_b128 v3, v[140:143] offset:5120\n\t" \
    "ds_write_b128 v3, v[144:147] offset:6144\n\t" \
    "ds_write_b128 v3, v[148:151] offset:7168\n\t" \
    "ds_write_b128 v3, v[152:155] offset:8192\n\t" \
    "s_waitcnt vmcnt(12) lgkmcnt(0)\n\t" \
    "s_barrier\n\t" \
    "ds_read_b64_tr_b16 a[152:153], v212 offset:0\n\t" \
    "ds_read_b64_tr_b16 a[154:155], v212 offset:18432\n\t" \
    "ds_read_b64_tr_b16 a[168:169], v213 offset:0\n\t" \
    "ds_read_b64_tr_b16 a[170:171], v213 offset:18432\n\t" \
    "ds_read_b64_tr_b16 a[184:185], v214 offset:0\n\t" \
    "ds_read_b64_tr_b16 a[186:187], v214 offset:18432\n\t" \
    "s_waitcnt lgkmcnt(6)\n\t" \
    "ds_read_b64_tr_b16 a[200:201], v215 offset:0\n\t" \
    "ds_read_b64_tr_b16 a[202:203], v215 offset:18432\n\t" \
    "ds_read_b64_tr_b16 a[216:217], v212 offset:128\n\t" \
    "ds_read_b64_tr_b16 a[218:219], v212 offset:18560\n\t" \
    "ds_read_b64_tr_b16 a[232:233], v213 offset:128\n\t" \
    "ds_read_b64_tr_b16 a[234:235], v213 offset:18560\n\t" \
    "s_waitcnt lgkmcnt(6)\n\t" \
    "ds_read_b64_tr_b16 a[248:249], v214 offset:128\n\t" \
    "ds_read_b64_tr_b16 a[250:251], v214 offset:18560\n\t" \
    "ds_read_b64_tr_b16 v[232:233], v215 offset:128\n\t" \
    "ds_read_b64_tr_b16 v[234:235], v215 offset:18560\n\t" \
    "ds_read_b64_tr_b16 v[248:249], v212 offset:256\n\t" \
    "ds_read_b64_tr_b16 v[250:251], v212 offset:18688\n\t" \
    "s_waitcnt lgkmcnt(6)\n\t" \
    "v_add_u32 v212, 0x9000, v212\n\t" \
    "v_add_u32 v213, 0x9000, v213\n\t" \
    "v_add_u32 v214, 0x9000, v214\n\t" \
    "v_add_u32 v215, 0x9000, v215\n\t" \
    "s_min_u32 s69, s84, 63\n\t" \
    "s_mul_i32 s69, s69, 36864\n\t" \
    "s_add_u32 s52, s50, s69\n\t" \
    "s_addc_u32 s53, s51, 0\n\t" \
    "s_add_u32 m0, s89, 36864\n\t" \
    "s_nop 0\n\t" \
    "global_load_lds_dwordx4 v203, s[52:53]\n\t" \
    "s_add_u32 m0, s89, 37888\n\t" \
    "s_nop 0\n\t" \
    "global_load_lds_dwordx4 v204, s[52:53]\n\t" \
    "s_add_u32 m0, s89, 38912\n\t" \
    "s_nop 0\n\t" \
    "global_load_lds_dwordx4 v205, s[52:53]\n\t" \
    "s_add_u32 m0, s89, 39936\n\t" \
    "s_nop 0\n\t" \
    "global_load_lds_dwordx4 v206, s[52:53]\n\t" \
    "s_add_u32 m0, s89, 40960\n\t" \
    "s_nop 0\n\t" \
    "global_load_lds_dwordx4 v207, s[52:53]\n\t" \
    "s_add_u32 m0, s89, 41984\n\t" \
    "s_nop 0\n\t" \
    "global_load_lds_dwordx4 v208, s[52:53]\n\t" \
    "s_add_u32 m0, s89, 43008\n\t" \
    "s_nop 0\n\t" \
    "global_load_lds_dwordx4 v209, s[52:53]\n\t" \
    "s_add_u32 m0, s89, 44032\n\t" \
    "s_nop 0\n\t" \
    "global_load_lds_dwordx4 v210, s[52:53]\n\t" \
    "s_add_u32 m0, s89, 45056\n\t" \
    "s_nop 0\n\t" \
    "global_load_lds_dwordx4 v211, s[52:53]\n\t" \
    "s_waitcnt vmcnt(18)\n\t" \
    "gv_C3_%=:\n\t" \
    "v_cmp_ne_u32 s[54:55], v166, v168\n\t" \
    "v_sub_u32 v166, v166, v165\n\t" \
    "v_cmp_ne_u32 vcc, 1, v166\n\t" \
    "s_nop 1\n\t" \
    "s_or_b64 vcc, vcc, s[54:55]\n\t" \
    "s_cbranch_scc0 gv_Cok4_%=\n\t" \
    "s_cmp_eq_u32 s68, 0\n\t" \
    "s_cbranch_scc1 gv_F6_%=\n\t" \
    "s_sub_u32 s68, s68, 1\n\t" \
    "s_sleep 6\n\t" \
    "s_branch gv_Cag5_%=\n\t" \
    "gv_F6_%=:\n\t" \
    "v_mov_b32 v168, 5767424\n\t" \
    "v_mov_b32 v169, 4\n\t" \
    "global_store_dword v168, v169, s[36:37] sc1\n\t" \
    "s_waitcnt vmcnt(0)\n\t" \
    "s_branch gv_Cok4_%=\n\t" \
    "gv_Cag5_%=:\n\t" \
    "global_load_dwordx2 v[166:167], v164, s[36:37] sc1\n\t" \
    "global_load_dword v168, v164, s[36:37] offset:2048 sc1\n\t" \
    "s_waitcnt vmcnt(0)\n\t" \
    "s_branch gv_C3_%=\n\t" \
    "gv_Cok4_%=:\n\t" \
    "s_cmp_lg_u32 s66, 0\n\t" \
    "s_cbranch_scc1 gv_Ca7_%=\n\t" \
    "v_mov_b32 v164, 5836800\n\t" \
    "s_mov_b64 exec, 1\n\t" \
    "global_atomic_add v164, v166, s[36:37] sc1\n\t" \
    "s_mov_b64 exec, -1\n\t" \
    "gv_Ca7_%=:\n\t" \
    "v_mov_b32_dpp v164, v167 quad_perm:[1,0,3,2] row_mask:0xf bank_mask:0xf\n\t" \
    "v_cmp_lt_u32 s[44:45], v164, v167\n\t" \
    "s_nop 1\n\t" \
    "s_waitcnt lgkmcnt(0)\n\t" \
    "s_barrier\n\t" \
    "ds_read_b128 v[24:27], v1 offset:0\n\t" \
    "ds_read_b128 v[28:31], v1 offset:18432\n\t" \
    "ds_read_b128 v[32:35], v2 offset:0\n\t" \
    "ds_read_b128 v[36:39], v2 offset:18432\n\t" \
    "ds_read_b128 v[40:43], v1 offset:128\n\t" \
    "ds_read_b128 v[44:47], v1 offset:18560\n\t" \
    ".p2align 6\n\t" \
    "s_nop 0\n\t" \
    "s_nop 0\n\t" \
    "gv_loop8_%=:\n\t" \
    "s_min_u32 s69, s85, 63\n\t" \
    "s_mul_i32 s69, s69, 36864\n\t" \
    "s_add_u32 s52, s50, s69\n\t" \
    "s_addc_u32 s53, s51, 0\n\t" \
    "s_add_u32 s90, s90, 36864\n\t" \
    "s_cmp_eq_u32 s90, 110592\n\t" \
    "s_cselect_b32 s90, 0, s90\n\t" \
    "s_add_u32 s91, s90, 36864\n\t" \
    "s_cmp_eq_u32 s91, 110592\n\t" \
    "s_cselect_b32 s91, 0, s91\n\t" \
    "s_add_u32 s91, s91, s89\n\t" \
    "v_add_u32 v22, s90, v0\n\t" \
    "v_xor_b32 v23, 64, v22\n\t" \
    "s_lshl_b32 s40, s65, 15\n\t" \
    "s_cselect_b32 s70, s65, 64\n\t" \
    "s_mul_i32 s69, s70, 171\n\t" \
    "s_lshr_b32 s69, s69, 9\n\t" \
    "s_mul_i32 s70, s70, 589824\n\t" \
    "s_mul_i32 s71, s69, 327680\n\t" \
    "s_add_u32 s70, s70, s71\n\t" \
    "s_lshl1_add_u32 s69, s69, 4\n\t" \
    "s_bitcmp1_b64 s[44:45], s69\n\t" \
    "s_cselect_b32 s69, 8192, 0\n\t" \
    "s_add_u32 s69, s69, s70\n\t" \
    "s_add_u32 s69, s69, 123731968\n\t" \
    "s_add_u32 s48, s36, s69\n\t" \
    "s_addc_u32 s49, s37, 0\n\t" \
    "s_waitcnt lgkmcnt(5)\n\t" \
    "v_mfma_f32_16x16x32_bf16 v[72:75], a[0:3], v[24:27], 0\n\t" \
    "ds_read_b128 v[48:51], v2 offset:128\n\t" \
    "s_waitcnt lgkmcnt(5)\n\t" \
    "v_mfma_f32_16x16x32_bf16 v[76:79], a[0:3], v[28:31], 0\n\t" \
    "ds_read_b128 v[52:55], v2 offset:18560\n\t" \
    "v_mfma_f32_16x16x32_bf16 v[80:83], a[72:75], v[24:27], 0\n\t" \
    "s_add_u32 m0, s91, 0\n\t" \
    "v_mfma_f32_16x16x32_bf16 v[84:87], a[72:75], v[28:31], 0\n\t" \
    "global_load_lds_dwordx4 v203, s[52:53]\n\t" \
    "s_waitcnt lgkmcnt(5)\n\t" \
    "v_mfma_f32_16x16x32_bf16 v[72:75], a[4:7], v[32:35], v[72:75]\n\t" \
    "ds_read_b128 v[56:59], v1 offset:256\n\t" \
    "s_waitcnt lgkmcnt(5)\n\t" \
    "v_mfma_f32_16x16x32_bf16 v[76:79], a[4:7], v[36:39], v[76:79]\n\t" \
    "ds_read_b128 v[60:63], v1 offset:18688\n\t" \
    "v_mfma_f32_16x16x32_bf16 v[80:83], a[76:79], v[32:35], v[80:83]\n\t" \
    "s_add_u32 m0, s91, 1024\n\t" \
    "v_mfma_f32_16x16x32_bf16 v[84:87], a[76:79], v[36:39], v[84:87]\n\t" \
    "global_load_lds_dwordx4 v204, s[52:53]\n\t" \
    "s_waitcnt lgkmcnt(5)\n\t" \
    "v_mfma_f32_16x16x32_bf16 v[72:75], a[8:11], v[40:43], v[72:75]\n\t" \
    "ds_read_b128 v[64:67], v2 offset:256\n\t" \
    "s_waitcnt lgkmcnt(5)\n\t" \
    "v_mfma_f32_16x16x32_bf16 v[76:79], a[8:11], v[44:47], v[76:79]\n\t" \
    "ds_read_b128 v[68:71], v2 offset:18688\n\t" \
    "v_mfma_f32_16x16x32_bf16 v[80:83], a[80:83], v[40:43], v[80:83]\n\t" \
    "s_add_u32 m0, s91, 2048\n\t" \
    "v_mfma_f32_16x16x32_bf16 v[84:87], a[80:83], v[44:47], v[84:87]\n\t" \
    "global_load_lds_dwordx4 v205, s[52:53]\n\t" \
    "s_waitcnt lgkmcnt(5)\n\t" \
    "v_mfma_f32_16x16x32_bf16 v[72:75], a[12:15], v[48:51], v[72:75]\n\t" \
    "ds_read_b128 v[24:27], v1 offset:384\n\t" \
    "s_waitcnt lgkmcnt(5)\n\t" \
    "v_mfma_f32_16x16x32_bf16 v[76:79], a[12:15], v[52:55], v[76:79]\n\t" \
    "ds_read_b128 v[28:31], v1 offset:18816\n\t" \
    "v_mfma_f32_16x16x32_bf16 v[80:83], a[84:87], v[48:51], v[80:83]\n\t" \
    "s_add_u32 m0, s91, 3072\n\t" \
    "v_mfma_f32_16x16x32_bf16 v[84:87], a[84:87], v[52:55], v[84:87]\n\t" \
    "global_load_lds_dwordx4 v206, s[52:53]\n\t" \
    "s_waitcnt lgkmcnt(5)\n\t" \
    "v_mfma_f32_16x16x32_bf16 v[72:75], a[16:19], v[56:59], v[72:75]\n\t" \
    "ds_read_b128 v[32:35], v2 offset:384\n\t" \
    "s_waitcnt lgkmcnt(5)\n\t" \
    "v_mfma_f32_16x16x32_bf16 v[76:79], a[16:19], v[60:63], v[76:79]\n\t" \
    "ds_read_b128 v[36:39], v2 offset:18816\n\t" \
    "v_mfma_f32_16x16x32_bf16 v[80:83], a[88:91], v[56:59], v[80:83]\n\t" \
    "s_add_u32 m0, s91, 4096\n\t" \
    "v_mfma_f32_16x16x32_bf16 v[84:87], a[88:91], v[60:63], v[84:87]\n\t" \
    "global_load_lds_dwordx4 v207, s[52:53]\n\t" \
    "s_waitcnt lgkmcnt(5)\n\t" \
    "v_mfma_f32_16x16x32_bf16 v[72:75], a[20:23], v[64:67], v[72:75]\n\t" \
    "ds_read_b128 v[40:43], v1 offset:512\n\t" \
    "s_waitcnt lgkmcnt(5)\n\t" \
    "v_mfma_f32_16x16x32_bf16 v[76:79], a[20:23], v[68:71], v[76:79]\n\t" \
    "ds_read_b128 v[44:47], v1 offset:18944\n\t" \
    "v_mfma_f32_16x16x32_bf16 v[80:83], a[92:95], v[64:67], v[80:83]\n\t" \
    "s_add_u32 m0, s91, 5120\n\t" \
    "v_mfma_f32_16x16x32_bf16 v[84:87], a[92:95], v[68:71], v[84:87]\n\t" \
    "global_load_lds_dwordx4 v208, s[52:53]\n\t" \
    "s_waitcnt lgkmcnt(5)\n\t" \
    "v_mfma_f32_16x16x32_bf16 v[72:75], a[24:27], v[24:27], v[72:75]\n\t" \
    "ds_read_b128 v[48:51], v2 offset:512\n\t" \
    "s_waitcnt lgkmcnt(5)\n\t" \
    "v_mfma_f32_16x16x32_bf16 v[76:79], a[24:27], v[28:31], v[76:79]\n\t" \
    "ds_read_b128 v[52:55], v2 offset:18944\n\t" \
    "v_mfma_f32_16x16x32_bf16 v[80:83], a[96:99], v[24:27], v[80:83]\n\t" \
    "s_add_u32 m0, s91, 6144\n\t" \
    "v_mfma_f32_16x16x32_bf16 v[84:87], a[96:99], v[28:31], v[84:87]\n\t" \
    "global_load_lds_dwordx4 v209, s[52:53]\n\t" \
    "s_waitcnt lgkmcnt(5)\n\t" \
    "v_mfma_f32_16x16x32_bf16 v[72:75], a[28:31], v[32:35], v[72:75]\n\t" \
    "ds_read_b128 v[56:59], v1 offset:640\n\t" \
    "s_waitcnt lgkmcnt(5)\n\t" \
    "v_mfma_f32_16x16x32_bf16 v[76:79], a[28:31], v[36:39], v[76:79]\n\t" \
    "ds_read_b128 v[60:63], v1 offset:19072\n\t" \
    "v_mfma_f32_16x16x32_bf16 v[80:83], a[100:103], v[32:35], v[80:83]\n\t" \
    "s_add_u32 m0, s91, 7168\n\t" \
    "v_mfma_f32_16x16x32_bf16 v[84:87], a[100:103], v[36:39], v[84:87]\n\t" \
    "global_load_lds_dwordx4 v210, s[52:53]\n\t" \
    "s_waitcnt lgkmcnt(5)\n\t" \
    "v_mfma_f32_16x16x32_bf16 v[72:75], a[32:35], v[40:43], v[72:75]\n\t" \
    "ds_read_b128 v[64:67], v2 offset:640\n\t" \
    "s_waitcnt lgkmcnt(5)\n\t" \
    "v_mfma_f32_16x16x32_bf16 v[76:79], a[32:35], v[44:47], v[76:79]\n\t" \
    "ds_read_b128 v[68:71], v2 offset:19072\n\t" \
    "v_mfma_f32_16x16x32_bf16 v[80:83], a[104:107], v[40:43], v[80:83]\n\t" \
    "s_add_u32 m0, s91, 8192\n\t" \
    "v_mfma_f32_16x16x32_bf16 v[84:87], a[104:107], v[44:47], v[84:87]\n\t" \
    "global_load_lds_dwordx4 v211, s[52:53]\n\t" \
    "s_waitcnt lgkmcnt(5)\n\t" \
    "v_mfma_f32_16x16x32_bf16 v[72:75], a[36:39], v[48:51], v[72:75]\n\t" \
    "ds_read_b128 v[24:27], v1 offset:768\n\t" \
    "s_waitcnt lgkmcnt(5)\n\t" \
    "v_mfma_f32_16x16x32_bf16 v[76:79], a[36:39], v[52:55], v[76:79]\n\t" \
    "ds_read_b128 v[28:31], v1 offset:19200\n\t" \
    "v_mfma_f32_16x16x32_bf16 v[80:83], a[108:111], v[48:51], v[80:83]\n\t" \
    "v_mfma_f32_16x16x32_bf16 v[84:87], a[108:111], v[52:55], v[84:87]\n\t" \
    "s_waitcnt lgkmcnt(5)\n\t" \
    "v_mfma_f32_16x16x32_bf16 v[72:75], a[40:43], v[56:59], v[72:75]\n\t" \
    "ds_read_b128 v[32:35], v2 offset:768\n\t" \
    "s_waitcnt lgkmcnt(5)\n\t" \
    "v_mfma_f32_16x16x32_bf16 v[76:79], a[40:43], v[60:63], v[76:79]\n\t" \
    "ds_read_b128 v[36:39], v2 offset:19200\n\t" \
    "v_mfma_f32_16x16x32_bf16 v[80:83], a[112:115], v[56:59], v[80:83]\n\t" \
    "v_mfma_f32_16x16x32_bf16 v[84:87], a[112:115], v[60:63], v[84:87]\n\t" \
    "global_store_dwordx4 v19, v[196:199], s[48:49] sc1\n\t" \
    "s_waitcnt lgkmcnt(5)\n\t" \
    "v_mfma_f32_16x16x32_bf16 v[72:75], a[44:47], v[64:67], v[72:75]\n\t" \
    "ds_read_b128 v[40:43], v1 offset:896\n\t" \
    "s_waitcnt lgkmcnt(5)\n\t" \
    "v_mfma_f32_16x16x32_bf16 v[76:79], a[44:47], v[68:71], v[76:79]\n\t" \
    "ds_read_b128 v[44:47], v1 offset:19328\n\t" \
    "v_mfma_f32_16x16x32_bf16 v[80:83], a[116:119], v[64:67], v[80:83]\n\t" \
    "v_mfma_f32_16x16x32_bf16 v[84:87], a[116:119], v[68:71], v[84:87]\n\t" \
    "s_waitcnt lgkmcnt(5)\n\t" \
    "v_mfma_f32_16x16x32_bf16 v[72:75], a[48:51], v[24:27], v[72:75]\n\t" \
    "ds_read_b128 v[48:51], v2 offset:896\n\t" \
    "s_waitcnt lgkmcnt(5)\n\t" \
    "v_mfma_f32_16x16x32_bf16 v[76:79], a[48:51], v[28:31], v[76:79]\n\t" \
    "ds_read_b128 v[52:55], v2 offset:19328\n\t" \
    "v_mfma_f32_16x16x32_bf16 v[80:83], a[120:123], v[24:27], v[80:83]\n\t" \
    "v_mfma_f32_16x16x32_bf16 v[84:87], a[120:123], v[28:31], v[84:87]\n\t" \
    "s_waitcnt lgkmcnt(5)\n\t" \
    "v_mfma_f32_16x16x32_bf16 v[72:75], a[52:55], v[32:35], v[72:75]\n\t" \
    "ds_read_b128 v[56:59], v1 offset:1024\n\t" \
    "s_waitcnt lgkmcnt(5)\n\t" \
    "v_mfma_f32_16x16x32_bf16 v[76:79], a[52:55], v[36:39], v[76:79]\n\t" \
    "ds_read_b128 v[60:63], v1 offset:19456\n\t" \
    "v_mfma_f32_16x16x32_bf16 v[80:83], a[124:127], v[32:35], v[80:83]\n\t" \
    "v_mfma_f32_16x16x32_bf16 v[84:87], a[124:127], v[36:39], v[84:87]\n\t" \
    "global_store_dwordx4 v20, v[196:199], s[48:49] sc1\n\t" \
    "s_waitcnt lgkmcnt(5)\n\t" \
    "v_mfma_f32_16x16x32_bf16 v[72:75], a[56:59], v[40:43], v[72:75]\n\t" \
    "ds_read_b128 v[64:67], v2 offset:1024\n\t" \
    "s_waitcnt lgkmcnt(5)\n\t" \
    "v_mfma_f32_16x16x32_bf16 v[76:79], a[56:59], v[44:47], v[76:79]\n\t" \
    "ds_read_b128 v[68:71], v2 offset:19456\n\t" \
    "v_mfma_f32_16x16x32_bf16 v[80:83], a[128:131], v[40:43], v[80:83]\n\t" \
    "v_mfma_f32_16x16x32_bf16 v[84:87], a[128:131], v[44:47], v[84:87]\n\t" \
    "s_waitcnt lgkmcnt(5)\n\t" \
    "v_mfma_f32_16x16x32_bf16 v[72:75], a[60:63], v[48:51], v[72:75]\n\t" \
    "s_waitcnt lgkmcnt(4)\n\t" \
    "v_mfma_f32_16x16x32_bf16 v[76:79], a[60:63], v[52:55], v[76:79]\n\t" \
    "v_mfma_f32_16x16x32_bf16 v[80:83], a[132:135], v[48:51], v[80:83]\n\t" \
    "v_mfma_f32_16x16x32_bf16 v[84:87], a[132:135], v[52:55], v[84:87]\n\t" \
    "s_waitcnt lgkmcnt(3)\n\t" \
    "v_mfma_f32_16x16x32_bf16 v[72:75], a[64:67], v[56:59], v[72:75]\n\t" \
    "s_waitcnt lgkmcnt(2)\n\t" \
    "v_mfma_f32_16x16x32_bf16 v[76:79], a[64:67], v[60:63], v[76:79]\n\t" \
    "v_mfma_f32_16x16x32_bf16 v[80:83], a[136:139], v[56:59], v[80:83]\n\t" \
    "v_mfma_f32_16x16x32_bf16 v[84:87], a[136:139], v[60:63], v[84:87]\n\t" \
    "global_store_dword v21, v196, s[48:49] sc1\n\t" \
    "s_waitcnt lgkmcnt(1)\n\t" \
    "v_mfma_f32_16x16x32_bf16 v[72:75], a[68:71], v[64:67], v[72:75]\n\t" \
    "s_waitcnt lgkmcnt(0)\n\t" \
    "v_mfma_f32_16x16x32_bf16 v[76:79], a[68:71], v[68:71], v[76:79]\n\t" \
    "v_mfma_f32_16x16x32_bf16 v[80:83], a[140:143], v[64:67], v[80:83]\n\t" \
    "v_mfma_f32_16x16x32_bf16 v[84:87], a[140:143], v[68:71], v[84:87]\n\t" \
    "v_mov_b32 v1, v22\n\t" \
    "v_mov_b32 v2, v23\n\t" \
    "s_nop 1\n\t" \
    "v_cvt_pk_bf16_f32 v104, v72, v73\n\t" \
    "v_cvt_pk_bf16_f32 v105, v74, v75\n\t" \
    "s_nop 0\n\t" \
    "v_cvt_pk_bf16_f32 v106, v80, v81\n\t" \
    "v_cvt_pk_bf16_f32 v107, v82, v83\n\t" \
    "ds_write_b128 v4, v[104:107] offset:0\n\t" \
    "v_cvt_pk_bf16_f32 v108, v76, v77\n\t" \
    "v_cvt_pk_bf16_f32 v109, v78, v79\n\t" \
    "v_cvt_pk_bf16_f32 v110, v84, v85\n\t" \
    "v_cvt_pk_bf16_f32 v111, v86, v87\n\t" \
    "ds_write_b128 v4, v[108:111] offset:1024\n\t" \
    "s_cmp_lt_i32 s86, 0\n\t" \
    "s_cbranch_scc1 gv_rest10_%=\n\t" \
    "gv_back11_%=:\n\t" \
    "s_waitcnt vmcnt(21) lgkmcnt(0)\n\t" \
    "s_barrier\n\t" \
    "ds_read_b128 v[164:167], v5 offset:0\n\t" \
    "ds_read_b128 v[168:171], v5 offset:1024\n\t" \
    "ds_read_b128 v[172:175], v5 offset:2048\n\t" \
    "ds_read_b128 v[176:179], v5 offset:3072\n\t" \
    "ds_read_b128 v[180:183], v5 offset:4096\n\t" \
    "ds_read_b128 v[184:187], v5 offset:5120\n\t" \
    "ds_read_b128 v[188:191], v5 offset:6144\n\t" \
    "ds_read_b128 v[192:195], v5 offset:7168\n\t" \
    "s_cmp_lt_i32 s87, 16\n\t" \
    "s_cbranch_scc1 gv_fl12_%=\n\t" \
    "s_cmp_lg_u32 s66, 0\n\t" \
    "s_cbranch_scc1 gv_SF13_%=\n\t" \
    "s_lshl_b32 s69, s67, 7\n\t" \
    "s_add_u32 s69, s69, 5783552\n\t" \
    "s_add_u32 s70, s87, 0\n\t" \
    "v_mov_b32 v156, s69\n\t" \
    "v_mov_b32 v157, s70\n\t" \
    "s_mov_b64 exec, 1\n\t" \
    "global_store_dword v156, v157, s[36:37] sc1\n\t" \
    "s_mov_b64 exec, -1\n\t" \
    "gv_SF13_%=:\n\t" \
    "gv_fl12_%=:\n\t" \
    "v_xor_b32 v4, 0x2000, v4\n\t" \
    "v_xor_b32 v5, 0x2000, v5\n\t" \
    "s_waitcnt lgkmcnt(7)\n\t" \
    "v_mfma_f32_16x16x32_bf16 v[72:75], v[164:167], a[144:147], 0\n\t" \
    "s_waitcnt lgkmcnt(6)\n\t" \
    "v_mfma_f32_16x16x32_bf16 v[76:79], v[168:171], a[144:147], 0\n\t" \
    "s_waitcnt lgkmcnt(5)\n\t" \
    "v_mfma_f32_16x16x32_bf16 v[72:75], v[172:175], a[148:151], v[72:75]\n\t" \
    "s_waitcnt lgkmcnt(4)\n\t" \
    "v_mfma_f32_16x16x32_bf16 v[76:79], v[176:179], a[148:151], v[76:79]\n\t" \
    "s_waitcnt lgkmcnt(3)\n\t" \
    "v_mfma_f32_16x16x32_bf16 v[72:75], v[180:183], a[152:155], v[72:75]\n\t" \
    "s_waitcnt lgkmcnt(2)\n\t" \
    "v_mfma_f32_16x16x32_bf16 v[76:79], v[184:187], a[152:155], v[76:79]\n\t" \
    "s_waitcnt lgkmcnt(1)\n\t" \
    "v_mfma_f32_16x16x32_bf16 v[72:75], v[188:191], a[156:159], v[72:75]\n\t" \
    "s_waitcnt lgkmcnt(0)\n\t" \
    "v_mfma_f32_16x16x32_bf16 v[76:79], v[192:195], a[156:159], v[76:79]\n\t" \
    "v_mfma_f32_16x16x32_bf16 v[80:83], v[164:167], a[160:163], 0\n\t" \
    "v_mfma_f32_16x16x32_bf16 v[84:87], v[168:171], a[160:163], 0\n\t" \
    "v_mfma_f32_16x16x32_bf16 v[80:83], v[172:175], a[164:167], v[80:83]\n\t" \
    "v_mfma_f32_16x16x32_bf16 v[84:87], v[176:179], a[164:167], v[84:87]\n\t" \
    "v_mfma_f32_16x16x32_bf16 v[80:83], v[180:183], a[168:171], v[80:83]\n\t" \
    "v_mfma_f32_16x16x32_bf16 v[84:87], v[184:187], a[168:171], v[84:87]\n\t" \
    "v_cvt_pk_bf16_f32 v104, v72, v73\n\t" \
    "v_mfma_f32_16x16x32_bf16 v[80:83], v[188:191], a[172:175], v[80:83]\n\t" \
    "v_cvt_pk_bf16_f32 v105, v74, v75\n\t" \
    "v_mfma_f32_16x16x32_bf16 v[84:87], v[192:195], a[172:175], v[84:87]\n\t" \
    "v_cvt_pk_bf16_f32 v106, v76, v77\n\t" \
    "v_mfma_f32_16x16x32_bf16 v[88:91], v[164:167], a[176:179], 0\n\t" \
    "v_cvt_pk_bf16_f32 v107, v78, v79\n\t" \
    "v_mfma_f32_16x16x32_bf16 v[92:95], v[168:171], a[176:179], 0\n\t" \
    "v_xor_b32 v18, s40, v8\n\t" \
    "v_mfma_f32_16x16x32_bf16 v[88:91], v[172:175], a[180:183], v[88:91]\n\t" \
    "global_store_dwordx4 v18, v[104:107], s[38:39] sc1\n\t" \
    "v_mfma_f32_16x16x32_bf16 v[92:95], v[176:179], a[180:183], v[92:95]\n\t" \
    "v_mfma_f32_16x16x32_bf16 v[88:91], v[180:183], a[184:187], v[88:91]\n\t" \
    "v_mfma_f32_16x16x32_bf16 v[92:95], v[184:187], a[184:187], v[92:95]\n\t" \
    "v_cvt_pk_bf16_f32 v108, v80, v81\n\t" \
    "v_mfma_f32_16x16x32_bf16 v[88:91], v[188:191], a[188:191], v[88:91]\n\t" \
    "v_cvt_pk_bf16_f32 v109, v82, v83\n\t" \
    "v_mfma_f32_16x16x32_bf16 v[92:95], v[192:195], a[188:191], v[92:95]\n\t" \
    "v_cvt_pk_bf16_f32 v110, v84, v85\n\t" \
    "v_mfma_f32_16x16x32_bf16 v[72:75], v[164:167], a[192:195], 0\n\t" \
    "v_cvt_pk_bf16_f32 v111, v86, v87\n\t" \
    "v_mfma_f32_16x16x32_bf16 v[76:79], v[168:171], a[192:195], 0\n\t" \
    "v_xor_b32 v18, s40, v9\n\t" \
    "v_mfma_f32_16x16x32_bf16 v[72:75], v[172:175], a[196:199], v[72:75]\n\t" \
    "global_store_dwordx4 v18, v[108:111], s[38:39] sc1\n\t" \
    "v_mfma_f32_16x16x32_bf16 v[76:79], v[176:179], a[196:199], v[76:79]\n\t" \
    "v_mfma_f32_16x16x32_bf16 v[72:75], v[180:183], a[200:203], v[72:75]\n\t" \
    "v_mfma_f32_16x16x32_bf16 v[76:79], v[184:187], a[200:203], v[76:79]\n\t" \
    "v_cvt_pk_bf16_f32 v104, v88, v89\n\t" \
    "v_mfma_f32_16x16x32_bf16 v[72:75], v[188:191], a[204:207], v[72:75]\n\t" \
    "v_cvt_pk_bf16_f32 v105, v90, v91\n\t" \
    "v_mfma_f32_16x16x32_bf16 v[76:79], v[192:195], a[204:207], v[76:79]\n\t" \
    "v_cvt_pk_bf16_f32 v106, v92, v93\n\t" \
    "v_mfma_f32_16x16x32_bf16 v[80:83], v[164:167], a[208:211], 0\n\t" \
    "v_cvt_pk_bf16_f32 v107, v94, v95\n\t" \
    "v_mfma_f32_16x16x32_bf16 v[84:87], v[168:171], a[208:211], 0\n\t" \
    "v_xor_b32 v18, s40, v10\n\t" \
    "v_mfma_f32_16x16x32_bf16 v[80:83], v[172:175], a[212:215], v[80:83]\n\t" \
    "global_store_dwordx4 v18, v[104:107], s[38:39] sc1\n\t" \
    "v_mfma_f32_16x16x32_bf16 v[84:87], v[176:179], a[212:215], v[84:87]\n\t" \
    "v_mfma_f32_16x16x32_bf16 v[80:83], v[180:183], a[216:219], v[80:83]\n\t" \
    "v_mfma_f32_16x16x32_bf16 v[84:87], v[184:187], a[216:219], v[84:87]\n\t" \
    "v_cvt_pk_bf16_f32 v108, v72, v73\n\t" \
    "v_mfma_f32_16x16x32_bf16 v[80:83], v[188:191], a[220:223], v[80:83]\n\t" \
    "v_cvt_pk_bf16_f32 v109, v74, v75\n\t" \
    "v_mfma_f32_16x16x32_bf16 v[84:87], v[192:195], a[220:223], v[84:87]\n\t" \
    "v_cvt_pk_bf16_f32 v110, v76, v77\n\t" \
    "v_mfma_f32_16x16x32_bf16 v[88:91], v[164:167], a[224:227], 0\n\t" \
    "v_cvt_pk_bf16_f32 v111, v78, v79\n\t" \
    "v_mfma_f32_16x16x32_bf16 v[92:95], v[168:171], a[224:227], 0\n\t" \
    "v_xor_b32 v18, s40, v11\n\t" \
    "v_mfma_f32_16x16x32_bf16 v[88:91], v[172:175], a[228:231], v[88:91]\n\t" \
    "global_store_dwordx4 v18, v[108:111], s[38:39] sc1\n\t" \
    "v_mfma_f32_16x16x32_bf16 v[92:95], v[176:179], a[228:231], v[92:95]\n\t" \
    "v_mfma_f32_16x16x32_bf16 v[88:91], v[180:183], a[232:235], v[88:91]\n\t" \
    "v_mfma_f32_16x16x32_bf16 v[92:95], v[184:187], a[232:235], v[92:95]\n\t" \
    "v_cvt_pk_bf16_f32 v104, v80, v81\n\t" \
    "v_mfma_f32_16x16x32_bf16 v[88:91], v[188:191], a[236:239], v[88:91]\n\t" \
    "v_cvt_pk_bf16_f32 v105, v82, v83\n\t" \
    "v_mfma_f32_16x16x32_bf16 v[92:95], v[192:195], a[236:239], v[92:95]\n\t" \
    "v_cvt_pk_bf16_f32 v106, v84, v85\n\t" \
    "v_mfma_f32_16x16x32_bf16 v[72:75], v[164:167], a[240:243], 0\n\t" \
    "v_cvt_pk_bf16_f32 v107, v86, v87\n\t" \
    "v_mfma_f32_16x16x32_bf16 v[76:79], v[168:171], a[240:243], 0\n\t" \
    "v_xor_b32 v18, s40, v12\n\t" \
    "v_mfma_f32_16x16x32_bf16 v[72:75], v[172:175], a[244:247], v[72:75]\n\t" \
    "global_store_dwordx4 v18, v[104:107], s[38:39] sc1\n\t" \
    "v_mfma_f32_16x16x32_bf16 v[76:79], v[176:179], a[244:247], v[76:79]\n\t" \
    "v_mfma_f32_16x16x32_bf16 v[72:75], v[180:183], a[248:251], v[72:75]\n\t" \
    "v_mfma_f32_16x16x32_bf16 v[76:79], v[184:187], a[248:251], v[76:79]\n\t" \
    "v_cvt_pk_bf16_f32 v108, v88, v89\n\t" \
    "v_mfma_f32_16x16x32_bf16 v[72:75], v[188:191], a[252:255], v[72:75]\n\t" \
    "v_cvt_pk_bf16_f32 v109, v90, v91\n\t" \
    "v_mfma_f32_16x16x32_bf16 v[76:79], v[192:195], a[252:255], v[76:79]\n\t" \
    "v_cvt_pk_bf16_f32 v110, v92, v93\n\t" \
    "v_mfma_f32_16x16x32_bf16 v[80:83], v[164:167], v[224:227], 0\n\t" \
    "v_cvt_pk_bf16_f32 v111, v94, v95\n\t" \
    "v_mfma_f32_16x16x32_bf16 v[84:87], v[168:171], v[224:227], 0\n\t" \
    "v_xor_b32 v18, s40, v13\n\t" \
    "v_mfma_f32_16x16x32_bf16 v[80:83], v[172:175], v[228:231], v[80:83]\n\t" \
    "global_store_dwordx4 v18, v[108:111], s[38:39] sc1\n\t" \
    "v_mfma_f32_16x16x32_bf16 v[84:87], v[176:179], v[228:231], v[84:87]\n\t" \
    "v_mfma_f32_16x16x32_bf16 v[80:83], v[180:183], v[232:235], v[80:83]\n\t" \
    "v_mfma_f32_16x16x32_bf16 v[84:87], v[184:187], v[232:235], v[84:87]\n\t" \
    "v_cvt_pk_bf16_f32 v104, v72, v73\n\t" \
    "v_mfma_f32_16x16x32_bf16 v[80:83], v[188:191], v[236:239], v[80:83]\n\t" \
    "v_cvt_pk_bf16_f32 v105, v74, v75\n\t" \
    "v_mfma_f32_16x16x32_bf16 v[84:87], v[192:195], v[236:239], v[84:87]\n\t" \
    "v_cvt_pk_bf16_f32 v106, v76, v77\n\t" \
    "v_mfma_f32_16x16x32_bf16 v[88:91], v[164:167], v[240:243], 0\n\t" \
    "v_cvt_pk_bf16_f32 v107, v78, v79\n\t" \
    "ds_read_b128 v[24:27], v22 offset:0\n\t" \
    "v_mfma_f32_16x16x32_bf16 v[92:95], v[168:171], v[240:243], 0\n\t" \
    "v_xor_b32 v18, s40, v14\n\t" \
    "ds_read_b128 v[28:31], v22 offset:18432\n\t" \
    "v_mfma_f32_16x16x32_bf16 v[88:91], v[172:175], v[244:247], v[88:91]\n\t" \
    "global_store_dwordx4 v18, v[104:107], s[38:39] sc1\n\t" \
    "ds_read_b128 v[32:35], v23 offset:0\n\t" \
    "v_mfma_f32_16x16x32_bf16 v[92:95], v[176:179], v[244:247], v[92:95]\n\t" \
    "ds_read_b128 v[36:39], v23 offset:18432\n\t" \
    "v_mfma_f32_16x16x32_bf16 v[88:91], v[180:183], v[248:251], v[88:91]\n\t" \
    "ds_read_b128 v[40:43], v22 offset:128\n\t" \
    "v_mfma_f32_16x16x32_bf16 v[92:95], v[184:187], v[248:251], v[92:95]\n\t" \
    "v_cvt_pk_bf16_f32 v108, v80, v81\n\t" \
    "ds_read_b128 v[44:47], v22 offset:18560\n\t" \
    "v_mfma_f32_16x16x32_bf16 v[88:91], v[188:191], v[252:255], v[88:91]\n\t" \
    "v_cvt_pk_bf16_f32 v109, v82, v83\n\t" \
    "v_mfma_f32_16x16x32_bf16 v[92:95], v[192:195], v[252:255], v[92:95]\n\t" \
    "v_cvt_pk_bf16_f32 v110, v84, v85\n\t" \
    "v_cvt_pk_bf16_f32 v111, v86, v87\n\t" \
    "v_xor_b32 v18, s40, v15\n\t" \
    "global_store_dwordx4 v18, v[108:111], s[38:39] sc1\n\t" \
    "s_nop 1\n\t" \
    "v_cvt_pk_bf16_f32 v104, v88, v89\n\t" \
    "v_cvt_pk_bf16_f32 v105, v90, v91\n\t" \
    "v_cvt_pk_bf16_f32 v106, v92, v93\n\t" \
    "v_cvt_pk_bf16_f32 v107, v94, v95\n\t" \
    "v_xor_b32 v18, s40, v16\n\t" \
    "global_store_dwordx4 v18, v[104:107], s[38:39] sc1\n\t" \
    "s_cmp_ge_u32 s65, 16\n\t" \
    "s_cbranch_scc1 gv_nofast9_%=\n\t" \
    "s_waitcnt vmcnt(0)\n\t" \
    "s_barrier\n\t" \
    "s_cmp_lg_u32 s66, 0\n\t" \
    "s_cbranch_scc1 gv_SF14_%=\n\t" \
    "s_lshl_b32 s69, s67, 7\n\t" \
    "s_add_u32 s69, s69, 5783552\n\t" \
    "s_add_u32 s70, s65, 0\n\t" \
    "v_mov_b32 v156, s69\n\t" \
    "v_mov_b32 v157, s70\n\t" \
    "s_mov_b64 exec, 1\n\t" \
    "global_store_dword v156, v157, s[36:37] sc1\n\t" \
    "s_mov_b64 exec, -1\n\t" \
    "gv_SF14_%=:\n\t" \
    "s_xor_b32 s69, s66, 1\n\t" \
    "s_lshr_b32 s70, s65, 1\n\t" \
    "s_or_b32 s69, s69, s70\n\t" \
    "s_cbranch_scc1 gv_nofast9_%=\n\t" \
    "v_mov_b32 v156, 5838848\n\t" \
    "v_mov_b32 v157, 1\n\t" \
    "s_mov_b64 exec, 1\n\t" \
    "global_atomic_add v158, v156, v157, s[36:37] sc0 sc1\n\t" \
    "s_mov_b64 exec, -1\n\t" \
    "v_mbcnt_lo_u32_b32 v156, -1, 0\n\t" \
    "v_and_b32 v157, 3, v156\n\t" \
    "v_lshlrev_b32 v157, 16, v157\n\t" \
    "v_bfe_u32 v159, v156, 2, 2\n\t" \
    "v_lshl_add_u32 v157, v159, 11, v157\n\t" \
    "v_bfe_u32 v159, v156, 4, 1\n\t" \
    "v_lshl_add_u32 v157, v159, 14, v157\n\t" \
    "s_bitcmp1_b32 s44, 2\n\t" \
    "s_cselect_b32 s63, 8192, 0\n\t" \
    "s_or_b32 s63, s63, 0x200000\n\t" \
    "v_add_u32 v157, s63, v157\n\t" \
    "s_waitcnt vmcnt(0)\n\t" \
    "v_readfirstlane_b32 s69, v158\n\t" \
    "s_nop 1\n\t" \
    "s_cmp_lg_u32 s69, 126\n\t" \
    "s_cbranch_scc1 gv_go15_%=\n\t" \
    "s_memrealtime s[56:57]\n\t" \
    "s_waitcnt lgkmcnt(0)\n\t" \
    "s_lshl_b32 s69, s56, 4\n\t" \
    "s_add_u32 s69, s69, 2960\n\t" \
    "s_add_u32 s70, s56, 24\n\t" \
    "s_lshr_b32 s70, s70, 4\n\t" \
    "v_lshrrev_b32 v159, 2, v156\n\t" \
    "v_add_u32 v159, s70, v159\n\t" \
    "v_and_b32 v159, 63, v159\n\t" \
    "v_lshlrev_b32 v159, 6, v159\n\t" \
    "v_and_b32 v158, 3, v156\n\t" \
    "v_lshl_add_u32 v159, v158, 12, v159\n\t" \
    "v_add_u32 v159, 5865472, v159\n\t" \
    "v_mov_b32 v156, s69\n\t" \
    "s_mov_b32 exec_hi, 0\n\t" \
    "global_store_dword v159, v156, s[36:37] sc1\n\t" \
    "global_store_dword v157, v156, s[36:37] sc1\n\t" \
    "s_mov_b32 exec_hi, -1\n\t" \
    "gv_go15_%=:\n\t" \
    "gv_nofast9_%=:\n\t" \
    "s_mov_b32 s87, s86\n\t" \
    "s_mov_b32 s86, s65\n\t" \
    "s_mov_b32 s65, s84\n\t" \
    "s_mov_b32 s84, s85\n\t" \
    "s_cmp_lt_u32 s84, 24\n\t" \
    "s_addc_u32 s85, s84, 1\n\t" \
    "s_cmp_lt_u32 s65, s88\n\t" \
    "s_cbranch_scc1 gv_loop8_%=\n\t" \
    "s_waitcnt vmcnt(0) lgkmcnt(0)\n\t" \
    "s_barrier\n\t" \
    "s_mov_b32 s65, 99\n\t" \
    "s_cmp_lg_u32 s66, 0\n\t" \
    "s_cbranch_scc1 gv_SF16_%=\n\t" \
    "s_lshl_b32 s69, s67, 7\n\t" \
    "s_add_u32 s69, s69, 5783552\n\t" \
    "s_add_u32 s70, s65, 0\n\t" \
    "v_mov_b32 v156, s69\n\t" \
    "v_mov_b32 v157, s70\n\t" \
    "s_mov_b64 exec, 1\n\t" \
    "global_store_dword v156, v157, s[36:37] sc1\n\t" \
    "s_mov_b64 exec, -1\n\t" \
    "gv_SF16_%=:\n\t" \
    "s_branch gv_end17_%=\n\t" \
    "gv_rest10_%=:\n\t" \
    "s_waitcnt vmcnt(12) lgkmcnt(0)\n\t" \
    "s_barrier\n\t" \
    "ds_read_b64_tr_b16 a[156:157], v212 offset:0\n\t" \
    "ds_read_b64_tr_b16 a[158:159], v212 offset:18432\n\t" \
    "ds_read_b64_tr_b16 a[172:173], v213 offset:0\n\t" \
    "ds_read_b64_tr_b16 a[174:175], v213 offset:18432\n\t" \
    "ds_read_b64_tr_b16 a[188:189], v214 offset:0\n\t" \
    "ds_read_b64_tr_b16 a[190:191], v214 offset:18432\n\t" \
    "s_waitcnt lgkmcnt(6)\n\t" \
    "ds_read_b64_tr_b16 a[204:205], v215 offset:0\n\t" \
    "ds_read_b64_tr_b16 a[206:207], v215 offset:18432\n\t" \
    "ds_read_b64_tr_b16 a[220:221], v212 offset:128\n\t" \
    "ds_read_b64_tr_b16 a[222:223], v212 offset:18560\n\t" \
    "ds_read_b64_tr_b16 a[236:237], v213 offset:128\n\t" \
    "ds_read_b64_tr_b16 a[238:239], v213 offset:18560\n\t" \
    "s_waitcnt lgkmcnt(6)\n\t" \
    "ds_read_b64_tr_b16 a[252:253], v214 offset:128\n\t" \
    "ds_read_b64_tr_b16 a[254:255], v214 offset:18560\n\t" \
    "ds_read_b64_tr_b16 v[236:237], v215 offset:128\n\t" \
    "ds_read_b64_tr_b16 v[238:239], v215 offset:18560\n\t" \
    "ds_read_b64_tr_b16 v[252:253], v212 offset:256\n\t" \
    "ds_read_b64_tr_b16 v[254:255], v212 offset:18688\n\t" \
    "s_waitcnt lgkmcnt(6)\n\t" \
    "v_add_u32 v212, 0x9000, v212\n\t" \
    "v_add_u32 v213, 0x9000, v213\n\t" \
    "v_add_u32 v214, 0x9000, v214\n\t" \
    "v_add_u32 v215, 0x9000, v215\n\t" \
    "s_branch gv_back11_%=\n\t" \
    "gv_end17_%=:\n\t" \
    "gv_last_%=:\n\t" \
    ""
#define ASM_CV \
    "v_mov_b32 v0, %0\n\t" \
    "v_mov_b32 v2, %2\n\t" \
    "v_subrev_u32 v1, 768, %1\n\t" \
    "v_mov_b32 v4, 0\n\t" \
    "v_mov_b32 v5, 0\n\t" \
    "v_mov_b32 v6, 0\n\t" \
    "v_mov_b32 v7, 0\n\t" \
    "v_mov_b32 v8, 0\n\t" \
    "v_mov_b32 v9, 0\n\t" \
    "v_mov_b32 v10, 0\n\t" \
    "v_mov_b32 v11, 0\n\t" \
    "v_mov_b32 v12, 0\n\t" \
    "v_mov_b32 v13, 0\n\t" \
    "v_mov_b32 v14, 0\n\t" \
    "v_mov_b32 v15, 0\n\t" \
    "v_mov_b32 v16, 0\n\t" \
    "v_mov_b32 v17, 0\n\t" \
    "v_mov_b32 v18, 0\n\t" \
    "v_mov_b32 v19, 0\n\t" \
    "v_mov_b32 v20, 0\n\t" \
    "v_mov_b32 v21, 0\n\t" \
    "v_mov_b32 v22, 0\n\t" \
    "v_mov_b32 v23, 0\n\t" \
    "v_mov_b32 v24, 0\n\t" \
    "v_mov_b32 v25, 0\n\t" \
    "v_mov_b32 v26, 0\n\t" \
    "v_mov_b32 v27, 0\n\t" \
    "v_mov_b32 v28, 0\n\t" \
    "v_mov_b32 v29, 0\n\t" \
    "v_mov_b32 v30, 0\n\t" \
    "v_mov_b32 v31, 0\n\t" \
    "v_mov_b32 v32, 0\n\t" \
    "v_mov_b32 v33, 0\n\t" \
    "v_mov_b32 v34, 0\n\t" \
    "v_mov_b32 v35, 0\n\t" \
    "s_waitcnt lgkmcnt(0)\n\t" \
    "ds_read_b64 v[36:37], v0 offset:0\n\t" \
    "ds_read_b64 v[38:39], v0 offset:8\n\t" \
    "ds_read_b64 v[40:41], v0 offset:32\n\t" \
    "ds_read_b64 v[42:43], v0 offset:40\n\t" \
    "ds_read_b128 v[44:47], v1 offset:768\n\t" \
    "ds_read_b128 v[48:51], v1 offset:2304\n\t" \
    "ds_read_b128 v[52:55], v1 offset:3840\n\t" \
    "ds_read_b128 v[56:59], v1 offset:5376\n\t" \
    "s_mov_b32 s69, 8\n\t" \
    "cv_c1_%=:\n\t" \
    "s_waitcnt lgkmcnt(3)\n\t" \
    "v_mfma_f32_16x16x32_bf16 v[4:7], v[36:39], v[44:47], v[4:7]\n\t" \
    "ds_read_b64 v[76:77], v0 offset:64\n\t" \
    "ds_read_b64 v[78:79], v0 offset:72\n\t" \
    "ds_read_b64 v[80:81], v0 offset:96\n\t" \
    "v_mfma_f32_16x16x32_bf16 v[20:23], v[40:43], v[44:47], v[20:23]\n\t" \
    "ds_read_b64 v[82:83], v0 offset:104\n\t" \
    "ds_read_b128 v[84:87], v1 offset:672\n\t" \
    "ds_read_b128 v[88:91], v1 offset:2208\n\t" \
    "s_waitcnt lgkmcnt(8)\n\t" \
    "v_mfma_f32_16x16x32_bf16 v[8:11], v[36:39], v[48:51], v[8:11]\n\t" \
    "ds_read_b128 v[92:95], v1 offset:3744\n\t" \
    "ds_read_b128 v[96:99], v1 offset:5280\n\t" \
    "v_mfma_f32_16x16x32_bf16 v[24:27], v[40:43], v[48:51], v[24:27]\n\t" \
    "s_waitcnt lgkmcnt(9)\n\t" \
    "v_mfma_f32_16x16x32_bf16 v[12:15], v[36:39], v[52:55], v[12:15]\n\t" \
    "v_mfma_f32_16x16x32_bf16 v[28:31], v[40:43], v[52:55], v[28:31]\n\t" \
    "s_waitcnt lgkmcnt(8)\n\t" \
    "v_mfma_f32_16x16x32_bf16 v[16:19], v[36:39], v[56:59], v[16:19]\n\t" \
    "v_mfma_f32_16x16x32_bf16 v[32:35], v[40:43], v[56:59], v[32:35]\n\t" \
    "s_waitcnt lgkmcnt(3)\n\t" \
    "v_mfma_f32_16x16x32_bf16 v[4:7], v[76:79], v[84:87], v[4:7]\n\t" \
    "ds_read_b64 v[36:37], v0 offset:128\n\t" \
    "ds_read_b64 v[38:39], v0 offset:136\n\t" \
    "ds_read_b64 v[40:41], v0 offset:160\n\t" \
    "v_mfma_f32_16x16x32_bf16 v[20:23], v[80:83], v[84:87], v[20:23]\n\t" \
    "ds_read_b64 v[42:43], v0 offset:168\n\t" \
    "ds_read_b128 v[44:47], v1 offset:576\n\t" \
    "ds_read_b128 v[48:51], v1 offset:2112\n\t" \
    "s_waitcnt lgkmcnt(8)\n\t" \
    "v_mfma_f32_16x16x32_bf16 v[8:11], v[76:79], v[88:91], v[8:11]\n\t" \
    "ds_read_b128 v[52:55], v1 offset:3648\n\t" \
    "ds_read_b128 v[56:59], v1 offset:5184\n\t" \
    "v_mfma_f32_16x16x32_bf16 v[24:27], v[80:83], v[88:91], v[24:27]\n\t" \
    "s_waitcnt lgkmcnt(9)\n\t" \
    "v_mfma_f32_16x16x32_bf16 v[12:15], v[76:79], v[92:95], v[12:15]\n\t" \
    "v_mfma_f32_16x16x32_bf16 v[28:31], v[80:83], v[92:95], v[28:31]\n\t" \
    "s_waitcnt lgkmcnt(8)\n\t" \
    "v_mfma_f32_16x16x32_bf16 v[16:19], v[76:79], v[96:99], v[16:19]\n\t" \
    "v_mfma_f32_16x16x32_bf16 v[32:35], v[80:83], v[96:99], v[32:35]\n\t" \
    "v_add_u32 v0, 128, v0\n\t" \
    "v_subrev_u32 v1, 192, v1\n\t" \
    "s_sub_u32 s69, s69, 1\n\t" \
    "s_cmp_lg_u32 s69, 0\n\t" \
    "s_cbranch_scc1 cv_c1_%=\n\t" \
    "s_waitcnt lgkmcnt(0)\n\t" \
    "ds_read_b64 v[36:37], v0 offset:0\n\t" \
    "ds_read_b64 v[38:39], v0 offset:8\n\t" \
    "ds_read_b64 v[40:41], v0 offset:32\n\t" \
    "ds_read_b64 v[42:43], v0 offset:40\n\t" \
    "ds_read_b128 v[48:51], v1 offset:2304\n\t" \
    "ds_read_b128 v[52:55], v1 offset:3840\n\t" \
    "ds_read_b128 v[56:59], v1 offset:5376\n\t" \
    "ds_read_b64 v[76:77], v0 offset:64\n\t" \
    "ds_read_b64 v[78:79], v0 offset:72\n\t" \
    "ds_read_b64 v[80:81], v0 offset:96\n\t" \
    "ds_read_b64 v[82:83], v0 offset:104\n\t" \
    "ds_read_b128 v[88:91], v1 offset:2208\n\t" \
    "ds_read_b128 v[92:95], v1 offset:3744\n\t" \
    "ds_read_b128 v[96:99], v1 offset:5280\n\t" \
    "s_mov_b32 s69, 4\n\t" \
    "cv_c2_%=:\n\t" \
    "s_waitcnt lgkmcnt(9)\n\t" \
    "v_mfma_f32_16x16x32_bf16 v[8:11], v[36:39], v[48:51], v[8:11]\n\t" \
    "ds_read_b64 v[116:117], v0 offset:128\n\t" \
    "ds_read_b64 v[118:119], v0 offset:136\n\t" \
    "ds_read_b64 v[120:121], v0 offset:160\n\t" \
    "v_mfma_f32_16x16x32_bf16 v[24:27], v[40:43], v[48:51], v[24:27]\n\t" \
    "ds_read_b64 v[122:123], v0 offset:168\n\t" \
    "ds_read_b128 v[128:131], v1 offset:2112\n\t" \
    "ds_read_b128 v[132:135], v1 offset:3648\n\t" \
    "s_waitcnt lgkmcnt(14)\n\t" \
    "v_mfma_f32_16x16x32_bf16 v[12:15], v[36:39], v[52:55], v[12:15]\n\t" \
    "ds_read_b128 v[136:139], v1 offset:5184\n\t" \
    "v_mfma_f32_16x16x32_bf16 v[28:31], v[40:43], v[52:55], v[28:31]\n\t" \
    "s_waitcnt lgkmcnt(14)\n\t" \
    "v_mfma_f32_16x16x32_bf16 v[16:19], v[36:39], v[56:59], v[16:19]\n\t" \
    "ds_read_b64 v[156:157], v0 offset:192\n\t" \
    "v_mfma_f32_16x16x32_bf16 v[32:35], v[40:43], v[56:59], v[32:35]\n\t" \
    "s_waitcnt lgkmcnt(10)\n\t" \
    "v_mfma_f32_16x16x32_bf16 v[8:11], v[76:79], v[88:91], v[8:11]\n\t" \
    "ds_read_b64 v[158:159], v0 offset:200\n\t" \
    "ds_read_b64 v[160:161], v0 offset:224\n\t" \
    "ds_read_b64 v[162:163], v0 offset:232\n\t" \
    "v_mfma_f32_16x16x32_bf16 v[24:27], v[80:83], v[88:91], v[24:27]\n\t" \
    "ds_read_b128 v[168:171], v1 offset:2016\n\t" \
    "ds_read_b128 v[172:175], v1 offset:3552\n\t" \
    "s_waitcnt lgkmcnt(14)\n\t" \
    "v_mfma_f32_16x16x32_bf16 v[12:15], v[76:79], v[92:95], v[12:15]\n\t" \
    "ds_read_b128 v[176:179], v1 offset:5088\n\t" \
    "v_mfma_f32_16x16x32_bf16 v[28:31], v[80:83], v[92:95], v[28:31]\n\t" \
    "s_waitcnt lgkmcnt(14)\n\t" \
    "v_mfma_f32_16x16x32_bf16 v[16:19], v[76:79], v[96:99], v[16:19]\n\t" \
    "v_mfma_f32_16x16x32_bf16 v[32:35], v[80:83], v[96:99], v[32:35]\n\t" \
    "s_waitcnt lgkmcnt(9)\n\t" \
    "v_mfma_f32_16x16x32_bf16 v[8:11], v[116:119], v[128:131], v[8:11]\n\t" \
    "ds_read_b64 v[36:37], v0 offset:256\n\t" \
    "ds_read_b64 v[38:39], v0 offset:264\n\t" \
    "ds_read_b64 v[40:41], v0 offset:288\n\t" \
    "v_mfma_f32_16x16x32_bf16 v[24:27], v[120:123], v[128:131], v[24:27]\n\t" \
    "ds_read_b64 v[42:43], v0 offset:296\n\t" \
    "ds_read_b128 v[48:51], v1 offset:1920\n\t" \
    "ds_read_b128 v[52:55], v1 offset:3456\n\t" \
    "s_waitcnt lgkmcnt(14)\n\t" \
    "v_mfma_f32_16x16x32_bf16 v[12:15], v[116:119], v[132:135], v[12:15]\n\t" \
    "ds_read_b128 v[56:59], v1 offset:4992\n\t" \
    "v_mfma_f32_16x16x32_bf16 v[28:31], v[120:123], v[132:135], v[28:31]\n\t" \
    "s_waitcnt lgkmcnt(14)\n\t" \
    "v_mfma_f32_16x16x32_bf16 v[16:19], v[116:119], v[136:139], v[16:19]\n\t" \
    "ds_read_b64 v[76:77], v0 offset:320\n\t" \
    "v_mfma_f32_16x16x32_bf16 v[32:35], v[120:123], v[136:139], v[32:35]\n\t" \
    "s_waitcnt lgkmcnt(10)\n\t" \
    "v_mfma_f32_16x16x32_bf16 v[8:11], v[156:159], v[168:171], v[8:11]\n\t" \
    "ds_read_b64 v[78:79], v0 offset:328\n\t" \
    "ds_read_b64 v[80:81], v0 offset:352\n\t" \
    "ds_read_b64 v[82:83], v0 offset:360\n\t" \
    "v_mfma_f32_16x16x32_bf16 v[24:27], v[160:163], v[168:171], v[24:27]\n\t" \
    "ds_read_b128 v[88:91], v1 offset:1824\n\t" \
    "ds_read_b128 v[92:95], v1 offset:3360\n\t" \
    "s_waitcnt lgkmcnt(14)\n\t" \
    "v_mfma_f32_16x16x32_bf16 v[12:15], v[156:159], v[172:175], v[12:15]\n\t" \
    "ds_read_b128 v[96:99], v1 offset:4896\n\t" \
    "v_mfma_f32_16x16x32_bf16 v[28:31], v[160:163], v[172:175], v[28:31]\n\t" \
    "s_waitcnt lgkmcnt(14)\n\t" \
    "v_mfma_f32_16x16x32_bf16 v[16:19], v[156:159], v[176:179], v[16:19]\n\t" \
    "v_mfma_f32_16x16x32_bf16 v[32:35], v[160:163], v[176:179], v[32:35]\n\t" \
    "v_add_u32 v0, 256, v0\n\t" \
    "v_subrev_u32 v1, 384, v1\n\t" \
    "s_sub_u32 s69, s69, 1\n\t" \
    "s_cmp_lg_u32 s69, 0\n\t" \
    "s_cbranch_scc1 cv_c2_%=\n\t" \
    "s_waitcnt lgkmcnt(0)\n\t" \
    "s_mov_b32 s69, 4\n\t" \
    "cv_c3_%=:\n\t" \
    "s_waitcnt lgkmcnt(7)\n\t" \
    "v_mfma_f32_16x16x32_bf16 v[12:15], v[36:39], v[52:55], v[12:15]\n\t" \
    "ds_read_b64 v[116:117], v0 offset:128\n\t" \
    "ds_read_b64 v[118:119], v0 offset:136\n\t" \
    "ds_read_b64 v[120:121], v0 offset:160\n\t" \
    "v_mfma_f32_16x16x32_bf16 v[28:31], v[40:43], v[52:55], v[28:31]\n\t" \
    "ds_read_b64 v[122:123], v0 offset:168\n\t" \
    "ds_read_b128 v[132:135], v1 offset:3648\n\t" \
    "ds_read_b128 v[136:139], v1 offset:5184\n\t" \
    "s_waitcnt lgkmcnt(12)\n\t" \
    "v_mfma_f32_16x16x32_bf16 v[16:19], v[36:39], v[56:59], v[16:19]\n\t" \
    "ds_read_b64 v[156:157], v0 offset:192\n\t" \
    "ds_read_b64 v[158:159], v0 offset:200\n\t" \
    "ds_read_b64 v[160:161], v0 offset:224\n\t" \
    "v_mfma_f32_16x16x32_bf16 v[32:35], v[40:43], v[56:59], v[32:35]\n\t" \
    "s_waitcnt lgkmcnt(10)\n\t" \
    "v_mfma_f32_16x16x32_bf16 v[12:15], v[76:79], v[92:95], v[12:15]\n\t" \
    "ds_read_b64 v[162:163], v0 offset:232\n\t" \
    "ds_read_b128 v[172:175], v1 offset:3552\n\t" \
    "ds_read_b128 v[176:179], v1 offset:5088\n\t" \
    "v_mfma_f32_16x16x32_bf16 v[28:31], v[80:83], v[92:95], v[28:31]\n\t" \
    "s_waitcnt lgkmcnt(12)\n\t" \
    "v_mfma_f32_16x16x32_bf16 v[16:19], v[76:79], v[96:99], v[16:19]\n\t" \
    "v_mfma_f32_16x16x32_bf16 v[32:35], v[80:83], v[96:99], v[32:35]\n\t" \
    "s_waitcnt lgkmcnt(7)\n\t" \
    "v_mfma_f32_16x16x32_bf16 v[12:15], v[116:119], v[132:135], v[12:15]\n\t" \
    "ds_read_b64 v[36:37], v0 offset:256\n\t" \
    "ds_read_b64 v[38:39], v0 offset:264\n\t" \
    "ds_read_b64 v[40:41], v0 offset:288\n\t" \
    "v_mfma_f32_16x16x32_bf16 v[28:31], v[120:123], v[132:135], v[28:31]\n\t" \
    "ds_read_b64 v[42:43], v0 offset:296\n\t" \
    "ds_read_b128 v[52:55], v1 offset:3456\n\t" \
    "ds_read_b128 v[56:59], v1 offset:4992\n\t" \
    "s_waitcnt lgkmcnt(12)\n\t" \
    "v_mfma_f32_16x16x32_bf16 v[16:19], v[116:119], v[136:139], v[16:19]\n\t" \
    "ds_read_b64 v[76:77], v0 offset:320\n\t" \
    "ds_read_b64 v[78:79], v0 offset:328\n\t" \
    "ds_read_b64 v[80:81], v0 offset:352\n\t" \
    "v_mfma_f32_16x16x32_bf16 v[32:35], v[120:123], v[136:139], v[32:35]\n\t" \
    "s_waitcnt lgkmcnt(10)\n\t" \
    "v_mfma_f32_16x16x32_bf16 v[12:15], v[156:159], v[172:175], v[12:15]\n\t" \
    "ds_read_b64 v[82:83], v0 offset:360\n\t" \
    "ds_read_b128 v[92:95], v1 offset:3360\n\t" \
    "ds_read_b128 v[96:99], v1 offset:4896\n\t" \
    "v_mfma_f32_16x16x32_bf16 v[28:31], v[160:163], v[172:175], v[28:31]\n\t" \
    "s_waitcnt lgkmcnt(12)\n\t" \
    "v_mfma_f32_16x16x32_bf16 v[16:19], v[156:159], v[176:179], v[16:19]\n\t" \
    "v_mfma_f32_16x16x32_bf16 v[32:35], v[160:163], v[176:179], v[32:35]\n\t" \
    "v_add_u32 v0, 256, v0\n\t" \
    "v_subrev_u32 v1, 384, v1\n\t" \
    "s_sub_u32 s69, s69, 1\n\t" \
    "s_cmp_lg_u32 s69, 0\n\t" \
    "s_cbranch_scc1 cv_c3_%=\n\t" \
    "s_waitcnt lgkmcnt(0)\n\t" \
    "s_mov_b32 s69, 4\n\t" \
    "cv_c4_%=:\n\t" \
    "s_waitcnt lgkmcnt(5)\n\t" \
    "v_mfma_f32_16x16x32_bf16 v[16:19], v[36:39], v[56:59], v[16:19]\n\t" \
    "ds_read_b64 v[116:117], v0 offset:128\n\t" \
    "ds_read_b64 v[118:119], v0 offset:136\n\t" \
    "ds_read_b64 v[120:121], v0 offset:160\n\t" \
    "v_mfma_f32_16x16x32_bf16 v[32:35], v[40:43], v[56:59], v[32:35]\n\t" \
    "ds_read_b64 v[122:123], v0 offset:168\n\t" \
    "ds_read_b128 v[136:139], v1 offset:5184\n\t" \
    "ds_read_b64 v[156:157], v0 offset:192\n\t" \
    "s_waitcnt lgkmcnt(6)\n\t" \
    "v_mfma_f32_16x16x32_bf16 v[16:19], v[76:79], v[96:99], v[16:19]\n\t" \
    "ds_read_b64 v[158:159], v0 offset:200\n\t" \
    "ds_read_b64 v[160:161], v0 offset:224\n\t" \
    "ds_read_b64 v[162:163], v0 offset:232\n\t" \
    "v_mfma_f32_16x16x32_bf16 v[32:35], v[80:83], v[96:99], v[32:35]\n\t" \
    "ds_read_b128 v[176:179], v1 offset:5088\n\t" \
    "s_waitcnt lgkmcnt(5)\n\t" \
    "v_mfma_f32_16x16x32_bf16 v[16:19], v[116:119], v[136:139], v[16:19]\n\t" \
    "ds_read_b64 v[36:37], v0 offset:256\n\t" \
    "ds_read_b64 v[38:39], v0 offset:264\n\t" \
    "ds_read_b64 v[40:41], v0 offset:288\n\t" \
    "v_mfma_f32_16x16x32_bf16 v[32:35], v[120:123], v[136:139], v[32:35]\n\t" \
    "ds_read_b64 v[42:43], v0 offset:296\n\t" \
    "ds_read_b128 v[56:59], v1 offset:4992\n\t" \
    "ds_read_b64 v[76:77], v0 offset:320\n\t" \
    "s_waitcnt lgkmcnt(6)\n\t" \
    "v_mfma_f32_16x16x32_bf16 v[16:19], v[156:159], v[176:179], v[16:19]\n\t" \
    "ds_read_b64 v[78:79], v0 offset:328\n\t" \
    "ds_read_b64 v[80:81], v0 offset:352\n\t" \
    "ds_read_b64 v[82:83], v0 offset:360\n\t" \
    "v_mfma_f32_16x16x32_bf16 v[32:35], v[160:163], v[176:179], v[32:35]\n\t" \
    "ds_read_b128 v[96:99], v1 offset:4896\n\t" \
    "v_add_u32 v0, 256, v0\n\t" \
    "v_subrev_u32 v1, 384, v1\n\t" \
    "s_sub_u32 s69, s69, 1\n\t" \
    "s_cmp_lg_u32 s69, 0\n\t" \
    "s_cbranch_scc1 cv_c4_%=\n\t" \
    "s_waitcnt lgkmcnt(0)\n\t" \
    "s_nop 7\n\t" \
    "ds_write_b128 v2, v[4:7] offset:0\n\t" \
    "ds_write_b128 v2, v[8:11] offset:1024\n\t" \
    "ds_write_b128 v2, v[12:15] offset:2048\n\t" \
    "ds_write_b128 v2, v[16:19] offset:3072\n\t" \
    "ds_write_b128 v2, v[20:23] offset:4096\n\t" \
    "ds_write_b128 v2, v[24:27] offset:5120\n\t" \
    "ds_write_b128 v2, v[28:31] offset:6144\n\t" \
    "ds_write_b128 v2, v[32:35] offset:7168\n\t" \
    "s_waitcnt lgkmcnt(0)\n\t" \
    "cv_last_%=:\n\t" \
    ""
#define ASM_EA \
    "s_bitcmp1_b32 %1, 2\n\t" \
    "s_cselect_b32 s40, 0x30, 0x28\n\t" \
    "s_cmp_lt_u32 %1, 64\n\t" \
    "s_cselect_b32 s40, s40, 0x38\n\t" \
    "s_load_dwordx2 s[42:43], %0, s40\n\t" \
    "s_load_dword s48, %0, 0x40\n\t" \
    "s_getpc_b64 s[44:45]\n\t" \
    "ea_pc_%=:\n\t" \
    "s_bfe_u32 s40, %1, 0x20003\n\t" \
    "s_lshl_b32 s40, s40, 15\n\t" \
    "v_lshl_add_u32 v1, %2, 7, s40\n\t" \
    "v_min_u32 v1, code_end_marker-ea_pc_%=, v1\n\t" \
    "s_mov_b32 m0, 155648\n\t" \
    "v_readfirstlane_b32 s41, %2\n\t" \
    "global_load_lds_dword v1, s[44:45]\n\t" \
    "s_bitcmp1_b32 %1, 2\n\t" \
    "s_cselect_b32 s40, 24, 16\n\t" \
    "s_lshr_b32 s47, %1, 3\n\t" \
    "s_cmp_ge_u32 s47, s40\n\t" \
    "s_cbranch_scc1 ea_done1_%=\n\t" \
    "s_mov_b32 s46, pad_qk-ea_pc_%=\n\t" \
    "s_mov_b32 s40, pad_vv-ea_pc_%=\n\t" \
    "s_bitcmp1_b32 %1, 2\n\t" \
    "s_cselect_b32 s46, pad_gx-ea_pc_%=, s46\n\t" \
    "s_cselect_b32 s40, pad_gv-ea_pc_%=, s40\n\t" \
    "s_cmp_lt_u32 %1, 64\n\t" \
    "s_cselect_b32 s40, s46, s40\n\t" \
    "s_add_u32 s44, s44, s40\n\t" \
    "s_addc_u32 s45, s45, 0\n\t" \
    "s_swappc_b64 s[46:47], s[44:45]\n\t" \
    "s_lshl_b32 m0, s41, 4\n\t" \
    "s_and_b32 s40, %1, 3\n\t" \
    "s_lshl_b32 s40, s40, 11\n\t" \
    "s_bfe_u32 s46, %1, 0x30003\n\t" \
    "s_lshl_b32 s46, s46, 8\n\t" \
    "s_add_u32 s40, s40, s46\n\t" \
    "s_and_b32 s41, s41, 0x80\n\t" \
    "s_xor_b32 s40, s40, s41\n\t" \
    "v_lshrrev_b32 v1, 3, %2\n\t" \
    "v_and_b32 v1, 6, v1\n\t" \
    "v_xor_b32 v1, v1, %2\n\t" \
    "v_and_b32 v1, 15, v1\n\t" \
    "v_lshlrev_b32 v1, 4, v1\n\t" \
    "v_xor_b32 v1, s40, v1\n\t" \
    "v_lshrrev_b32 v2, 4, %2\n\t" \
    "v_lshl_add_u32 v1, v2, 13, v1\n\t" \
    "s_mov_b32 s40, 16\n\t" \
    "s_waitcnt lgkmcnt(0)\n\t" \
    "ea_lp2_%=:\n\t" \
    "global_load_lds_dwordx4 v1, s[42:43]\n\t" \
    "v_add_u32 v1, 0x20000, v1\n\t" \
    "s_add_u32 m0, m0, 4096\n\t" \
    "s_sub_u32 s40, s40, 1\n\t" \
    "s_cmp_lg_u32 s40, 0\n\t" \
    "s_cbranch_scc1 ea_lp2_%=\n\t" \
    "ea_done1_%=:\n\t" \
    "s_waitcnt lgkmcnt(0)\n\t" \
    "ea_last_%=:\n\t" \
    ""
#define ASM_CLOB "v0", "v1", "v2", "v3", "v4", "v5", "v6", "v7", "v8", "v9", "v10", "v11", "v12", "v13", "v14", "v15", "v16", "v17", "v18", "v19", "v20", "v21", "v22", "v23", "v24", "v25", "v26", "v27", "v28", "v29", "v30", "v31", "v32", "v33", "v34", "v35", "v36", "v37", "v38", "v39", "v40", "v41", "v42", "v43", "v44", "v45", "v46", "v47", "v48", "v49", "v50", "v51", "v52", "v53", "v54", "v55", "v56", "v57", "v58", "v59", "v60", "v61", "v62", "v63", "v64", "v65", "v66", "v67", "v68", "v69", "v70", "v71", "v72", "v73", "v74", "v75", "v76", "v77", "v78", "v79", "v80", "v81", "v82", "v83", "v84", "v85", "v86", "v87", "v88", "v89", "v90", "v91", "v92", "v93", "v94", "v95", "v96", "v97", "v98", "v99", "v100", "v101", "v102", "v103", "v104", "v105", "v106", "v107", "v108", "v109", "v110", "v111", "v112", "v113", "v114", "v115", "v116", "v117", "v118", "v119", "v120", "v121", "v122", "v123", "v124", "v125", "v126", "v127", "v128", "v129", "v130", "v131", "v132", "v133", "v134", "v135", "v136", "v137", "v138", "v139", "v140", "v141", "v142", "v143", "v144", "v145", "v146", "v147", "v148", "v149", "v150", "v151", "v152", "v153", "v154", "v155", "v156", "v157", "v158", "v159", "v160", "v161", "v162", "v163", "v164", "v165", "v166", "v167", "v168", "v169", "v170", "v171", "v172", "v173", "v174", "v175", "v176", "v177", "v178", "v179", "v180", "v181", "v182", "v183", "v184", "v185", "v186", "v187", "v188", "v189", "v190", "v191", "v192", "v193", "v194", "v195", "v196", "v197", "v198", "v199", "v200", "v201", "v202", "v203", "v204", "v205", "v206", "v207", "v208", "v209", "v210", "v211", "v212", "v213", "v214", "v215", "v222", "v223", "v224", "v225", "v226", "v227", "v228", "v229", "v230", "v231", "v232", "v233", "v234", "v235", "v236", "v237", "v238", "v239", "v240", "v241", "v242", "v243", "v244", "v245", "v246", "v247", "v248", "v249", "v250", "v251", "v252", "v253", "v254", "v255", "a0", "a1", "a2", "a3", "a4", "a5", "a6", "a7", "a8", "a9", "a10", "a11", "a12", "a13", "a14", "a15", "a16", "a17", "a18", "a19", "a20", "a21", "a22", "a23", "a24", "a25", "a26", "a27", "a28", "a29", "a30", "a31", "a32", "a33", "a34", "a35", "a36", "a37", "a38", "a39", "a40", "a41", "a42", "a43", "a44", "a45", "a46", "a47", "a48", "a49", "a50", "a51", "a52", "a53", "a54", "a55", "a56", "a57", "a58", "a59", "a60", "a61", "a62", "a63", "a64", "a65", "a66", "a67", "a68", "a69", "a70", "a71", "a72", "a73", "a74", "a75", "a76", "a77", "a78", "a79", "a80", "a81", "a82", "a83", "a84", "a85", "a86", "a87", "a88", "a89", "a90", "a91", "a92", "a93", "a94", "a95", "a96", "a97", "a98", "a99", "a100", "a101", "a102", "a103", "a104", "a105", "a106", "a107", "a108", "a109", "a110", "a111", "a112", "a113", "a114", "a115", "a116", "a117", "a118", "a119", "a120", "a121", "a122", "a123", "a124", "a125", "a126", "a127", "a128", "a129", "a130", "a131", "a132", "a133", "a134", "a135", "a136", "a137", "a138", "a139", "a140", "a141", "a142", "a143", "a144", "a145", "a146", "a147", "a148", "a149", "a150", "a151", "a152", "a153", "a154", "a155", "a156", "a157", "a158", "a159", "a160", "a161", "a162", "a163", "a164", "a165", "a166", "a167", "a168", "a169", "a170", "a171", "a172", "a173", "a174", "a175", "a176", "a177", "a178", "a179", "a180", "a181", "a182", "a183", "a184", "a185", "a186", "a187", "a188", "a189", "a190", "a191", "a192", "a193", "a194", "a195", "a196", "a197", "a198", "a199", "a200", "a201", "a202", "a203", "a204", "a205", "a206", "a207", "a208", "a209", "a210", "a211", "a212", "a213", "a214", "a215", "a216", "a217", "a218", "a219", "a220", "a221", "a222", "a223", "a224", "a225", "a226", "a227", "a228", "a229", "a230", "a231", "a232", "a233", "a234", "a235", "a236", "a237", "a238", "a239", "a240", "a241", "a242", "a243", "a244", "a245", "a246", "a247", "a248", "a249", "a250", "a251", "a252", "a253", "a254", "a255", "s33", "s34", "s35", "s36", "s37", "s38", "s39", "s40", "s41", "s42", "s43", "s44", "s45", "s46", "s47", "s48", "s49", "s50", "s51", "s52", "s53", "s54", "s55", "s56", "s57", "s58", "s59", "s60", "s61", "s62", "s63", "s64", "s65", "s66", "s67", "s68", "s69", "s70", "s71", "s72", "s73", "s74", "s75", "s76", "s77", "s78", "s79", "s80", "s81", "s82", "s83", "s84", "s85", "s86", "s87", "s88", "s89", "s90", "s91", "s92", "s93", "s94", "s95", "s96", "s97", "s98", "s99", "vcc", "scc", "memory"
#define ASM_CLOBW "v32", "v33", "v34", "v35", "v36", "v37", "v38", "v39", "v40", "v41", "v42", "v43", "v44", "v45", "v46", "v47", "v48", "v49", "v50", "v51", "v52", "v53", "v54", "v55", "v56", "v57", "v58", "v59", "v60", "v61", "v62", "v63", "v64", "v65", "v66", "v67", "v68", "v69", "v70", "v71", "v72", "v73", "v74", "v75", "v76", "v77", "v78", "v79", "v80", "v81", "v82", "v83", "v84", "v85", "v86", "v87", "v88", "v89", "v90", "v91", "v92", "v93", "v94", "v95", "v96", "v97", "v98", "v99", "v100", "v101", "v102", "v103", "v104", "v105", "v106", "v107", "v108", "v109", "v110", "v111", "v112", "v113", "v114", "v115", "v116", "v117", "v118", "v119", "v120", "v121", "v122", "v123", "v124", "v125", "v126", "v127", "v128", "v129", "v130", "v131", "v132", "v133", "v134", "v135", "v136", "v137", "v138", "v139", "v140", "v141", "v142", "v143", "v144", "v145", "v146", "v147", "v148", "v149", "v150", "v151", "v152", "v153", "v154", "v155", "v156", "v157", "v158", "v159", "v160", "v161", "v162", "v163", "v164", "v165", "v166", "v167", "v168", "v169", "v170", "v171", "v172", "v173", "v174", "v175", "v176", "v177", "v178", "v179", "v180", "v181", "v182", "v183", "v184", "v185", "v186", "v187", "v188", "v189", "v190", "v191", "v192", "v193", "v194", "v195", "v196", "v197", "v198", "v199", "v222", "v223", "v224", "v225", "v226", "v227", "v228", "v229", "v230", "v231", "v232", "v233", "v234", "v235", "v236", "v237", "v238", "v239", "v240", "v241", "v242", "v243", "v244", "v245", "v246", "v247", "v248", "v249", "v250", "v251", "v252", "v253", "v254", "v255", "a0", "a1", "a2", "a3", "a4", "a5", "a6", "a7", "a8", "a9", "a10", "a11", "a12", "a13", "a14", "a15", "a16", "a17", "a18", "a19", "a20", "a21", "a22", "a23", "a24", "a25", "a26", "a27", "a28", "a29", "a30", "a31", "a32", "a33", "a34", "a35", "a36", "a37", "a38", "a39", "a40", "a41", "a42", "a43", "a44", "a45", "a46", "a47", "a48", "a49", "a50", "a51", "a52", "a53", "a54", "a55", "a56", "a57", "a58", "a59", "a60", "a61", "a62", "a63", "a64", "a65", "a66", "a67", "a68", "a69", "a70", "a71", "a72", "a73", "a74", "a75", "a76", "a77", "a78", "a79", "a80", "a81", "a82", "a83", "a84", "a85", "a86", "a87", "a88", "a89", "a90", "a91", "a92", "a93", "a94", "a95", "a96", "a97", "a98", "a99", "a100", "a101", "a102", "a103", "a104", "a105", "a106", "a107", "a108", "a109", "a110", "a111", "a112", "a113", "a114", "a115", "a116", "a117", "a118", "a119", "a120", "a121", "a122", "a123", "a124", "a125", "a126", "a127", "a128", "a129", "a130", "a131", "a132", "a133", "a134", "a135", "a136", "a137", "a138", "a139", "a140", "a141", "a142", "a143", "a144", "a145", "a146", "a147", "a148", "a149", "a150", "a151", "a152", "a153", "a154", "a155", "a156", "a157", "a158", "a159", "a160", "a161", "a162", "a163", "a164", "a165", "a166", "a167", "a168", "a169", "a170", "a171", "a172", "a173", "a174", "a175", "a176", "a177", "a178", "a179", "a180", "a181", "a182", "a183", "a184", "a185", "a186", "a187", "a188", "a189", "a190", "a191", "a192", "a193", "a194", "a195", "a196", "a197", "a198", "a199", "a200", "a201", "a202", "a203", "a204", "a205", "a206", "a207", "a208", "a209", "a210", "a211", "a212", "a213", "a214", "a215", "a216", "a217", "a218", "a219", "a220", "a221", "a222", "a223", "a224", "a225", "a226", "a227", "a228", "a229", "a230", "a231", "a232", "a233", "a234", "a235", "a236", "a237", "a238", "a239", "a240", "a241", "a242", "a243", "a244", "a245", "a246", "a247", "a248", "a249", "a250", "a251", "a252", "a253", "a254", "a255", "s33", "s34", "s35", "s36", "s37", "s38", "s39", "s40", "s41", "s42", "s43", "s44", "s45", "s46", "s47", "s48", "s49", "s50", "s51", "s52", "s53", "s54", "s55", "s56", "s57", "s58", "s59", "s60", "s61", "s62", "s63", "s64", "s65", "s66", "s67", "s68", "s69", "s70", "s71", "s72", "s73", "s74", "s75", "s76", "s77", "s78", "s79", "s80", "s81", "s82", "s83", "s84", "s85", "s86", "s87", "s88", "s89", "s90", "s91", "s92", "s93", "s94", "s95", "s96", "s97", "s98", "s99", "vcc", "scc", "memory"
#define CV_CLOB "v0", "v1", "v2", "v3", "v4", "v5", "v6", "v7", "v8", "v9", "v10", "v11", "v12", "v13", "v14", "v15", "v16", "v17", "v18", "v19", "v20", "v21", "v22", "v23", "v24", "v25", "v26", "v27", "v28", "v29", "v30", "v31", "v32", "v33", "v34", "v35", "v36", "v37", "v38", "v39", "v40", "v41", "v42", "v43", "v44", "v45", "v46", "v47", "v48", "v49", "v50", "v51", "v52", "v53", "v54", "v55", "v56", "v57", "v58", "v59", "v60", "v61", "v62", "v63", "v64", "v65", "v66", "v67", "v68", "v69", "v70", "v71", "v72", "v73", "v74", "v75", "v76", "v77", "v78", "v79", "v80", "v81", "v82", "v83", "v84", "v85", "v86", "v87", "v88", "v89", "v90", "v91", "v92", "v93", "v94", "v95", "v96", "v97", "v98", "v99", "v100", "v101", "v102", "v103", "v104", "v105", "v106", "v107", "v108", "v109", "v110", "v111", "v112", "v113", "v114", "v115", "v116", "v117", "v118", "v119", "v120", "v121", "v122", "v123", "v124", "v125", "v126", "v127", "v128", "v129", "v130", "v131", "v132", "v133", "v134", "v135", "v136", "v137", "v138", "v139", "v140", "v141", "v142", "v143", "v144", "v145", "v146", "v147", "v148", "v149", "v150", "v151", "v152", "v153", "v154", "v155", "v156", "v157", "v158", "v159", "v160", "v161", "v162", "v163", "v164", "v165", "v166", "v167", "v168", "v169", "v170", "v171", "v172", "v173", "v174", "v175", "v176", "v177", "v178", "v179", "v180", "v181", "v182", "v183", "v184", "v185", "v186", "v187", "v188", "v189", "v190", "v191", "v192", "v193", "v194", "v195", "s69", "scc", "memory"
static constexpr unsigned G_STEP = 0x10000;
static constexpr unsigned G_WS_CTL = 0x580000;
static constexpr unsigned G_WS_DONE = 0x5f0000;
static constexpr unsigned G_WS_EP = 0x5f2000;
static constexpr unsigned G_WS_STAMPS = 0xc000000;
static constexpr unsigned G_C_CAL = 0x0;
static constexpr unsigned G_C_ERR = 0x100;
static constexpr unsigned G_C_TASK = 0x200;
static constexpr unsigned G_C_GX = 0x1000;
static constexpr unsigned G_C_GV = 0x4000;
static constexpr unsigned G_C_KM = 0x8000;
static constexpr unsigned G_C_UM = 0x9000;
static constexpr unsigned G_C_PC = 0xc000;
static constexpr unsigned G_C_UT = 0xd000;
static constexpr unsigned G_C_YC = 0x10000;
static constexpr unsigned G_C_ARR = 0x11000;
static constexpr unsigned G_C_KG = 0x12000;
static constexpr unsigned G_C_GQ = 0x14000;
static constexpr unsigned G_C_RDY = 0x15000;
static constexpr unsigned G_C_GT = 0x18000;
static constexpr unsigned G_C_GO = 0x11800;
static constexpr unsigned G_C_RW = 0xf000;
static constexpr unsigned G_WS_H = 0x0;
static constexpr unsigned G_HS8 = 0x80000;
static constexpr unsigned G_HS4 = 0x40000;
static constexpr unsigned G_UXS = 0x200000;
static constexpr unsigned G_UMS = 0x90000;
static constexpr unsigned G_PROBE = 0x1bd400;
static constexpr unsigned G_D_QX = 0x600000;
static constexpr unsigned G_D_KX = 0x1600000;
static constexpr unsigned G_D_KM = 0x2600000;
static constexpr unsigned G_D_UX = 0x2e00000;
static constexpr unsigned G_D_UM = 0x7600000;
static constexpr unsigned G_D_PX = 0xa200000;
static constexpr unsigned G_D_PM = 0xa600000;
static constexpr unsigned G_D_KT = 0xa700000;
static constexpr unsigned G_D_UT = 0xac00000;
static constexpr unsigned G_D_YB = 0xaf00000;
static constexpr unsigned G_D_END = 0xb200000;
static constexpr unsigned G_SKM = 0x7;
static constexpr unsigned G_S1 = 0x18;
static constexpr unsigned G_S0 = 0x6;
static constexpr unsigned G_GVN = 0x2;
static constexpr unsigned G_STG = 0x9000;
static constexpr unsigned G_KROW = 0x30;
static constexpr unsigned G_NHL = 0x1;
#define FLG(x_) (kDefault[5])
#define FLH(x_) (kDefault[7])
static constexpr int kDefault[8] = {PRM_DEFAULT};
#define MFMA32(a_, b_, c_) __builtin_amdgcn_mfma_f32_16x16x32_bf16(__builtin_bit_cast(bf16x8, a_), __builtin_bit_cast(bf16x8, b_), c_, 0, 0, 0)
#define MFMA16(a_, b_, c_) __builtin_amdgcn_mfma_f32_16x16x16bf16_1k(__builtin_bit_cast(s16x4, a_), __builtin_bit_cast(s16x4, b_), c_, 0, 0, 0)
#define WLD8(d_, voff_, base_, off_) asm volatile("s_nop 4\n\tglobal_load_dwordx2 %0, %1, %2 offset:%3 sc1" : "=v"(d_) : "v"(voff_), "s"(base_), "i"(off_) : "memory")
#define WLD16(d_, voff_, base_, off_) asm volatile("s_nop 4\n\tglobal_load_dwordx4 %0, %1, %2 offset:%3 sc1" : "=v"(d_) : "v"(voff_), "s"(base_), "i"(off_) : "memory")
#define WST16(voff_, d_, base_, off_) asm volatile("s_nop 4\n\tglobal_store_dwordx4 %0, %1, %2 offset:%3 sc1\n\ts_nop 1" :: "v"(voff_), "v"(d_), "s"(base_), "i"(off_) : "memory")
#define OMOD ""
#define OST8(p_, d_) asm volatile("global_store_dwordx2 %0, %1, off " OMOD :: "v"(p_), "v"(d_) : "memory")
#define OST16(p_, d_) asm volatile("global_store_dwordx4 %0, %1, off " OMOD "\n\ts_nop 1" :: "v"(p_), "v"(d_) : "memory")
#define WLD16X4(d0, d1, d2, d3, voff_, base_) asm volatile("s_nop 4\n\tglobal_load_dwordx4 %0, %4, %5 sc1\n\tglobal_load_dwordx4 %1, %4, %5 offset:1024 sc1\n\t" \
    "global_load_dwordx4 %2, %4, %5 offset:2048 sc1\n\tglobal_load_dwordx4 %3, %4, %5 offset:3072 sc1" \
    : "=&v"(d0), "=&v"(d1), "=&v"(d2), "=&v"(d3) : "v"(voff_), "s"(base_) : "memory")

__device__ __forceinline__ u32x4 pack8(const f32x4& a, const f32x4& b) {
    return u32x4{pk2bf(a[0], a[1]), pk2bf(a[2], a[3]), pk2bf(b[0], b[1]), pk2bf(b[2], b[3])};
}
template <class T> __device__ __forceinline__ T* uni(T* q) {
    const u64 v = (u64)q;
    unsigned lo = __builtin_amdgcn_readfirstlane((unsigned)v), hi = __builtin_amdgcn_readfirstlane((unsigned)(v >> 32));
    asm volatile("s_nop 4" : "+s"(lo), "+s"(hi));
    return (T*)(((u64)hi << 32) | lo);
}
template <bool FAST = false>
__device__ __forceinline__ void wait_word(const MainP& p, const char* f, int want, unsigned code) {
    unsigned tries = 0;
#pragma unroll 1
    while (__any((int)__hip_atomic_load((const gu32*)f, RLX_AGENT) < want)) {
        if (++tries > kWaitCap) { __hip_atomic_store((gu32*)(p.ws + G_WS_CTL + G_C_ERR), code, RLX_AGENT); break; }
        if ((tries & 1023u) == 0u && __hip_atomic_load((const gu32*)(p.ws + G_WS_CTL + G_C_ERR), RLX_AGENT) != 0xffffffffu) break;
        if (FAST) __builtin_amdgcn_s_sleep(1); else __builtin_amdgcn_s_sleep(10);
    }
}

__device__ __forceinline__ int skew(int s, int id) { return (s ^ (G_SKM * id)) & 63; }
__device__ __forceinline__ unsigned um_base(int s, unsigned nm) { return G_D_UM + (s / 3) * 0x200000 + (s % 3) * G_UMS + ((nm >> (2 + s / 3)) & 1) * 8192; }
typedef GAS unsigned long long gu64;
#define IST(k_, slot_)
__device__ __forceinline__ void init_done(char* ws, int slot, unsigned val) {
    const unsigned e = __hip_atomic_load((const gu32*)(ws + G_WS_EP), RLX_AGENT);
    __hip_atomic_store((gu64*)(ws + G_WS_DONE + slot * 64), ((u64)val << 32) | (e + 1u), RLX_AGENT);
}
__device__ __forceinline__ void init_wait(char* ws, int w, int lane) {
    if (w == 0) {
        const unsigned e = __hip_atomic_load((const gu32*)(ws + G_WS_EP), RLX_AGENT);
        for (unsigned tries = 0; tries < kWaitCap; ++tries) {
            const unsigned d0 = __hip_atomic_load((const gu32*)(ws + G_WS_DONE + lane * 64), RLX_AGENT), d1 = __hip_atomic_load((const gu32*)(ws + G_WS_DONE + 2048 + lane * 64), RLX_AGENT);
            if (!__any((d0 != e + 1u) | (d1 != e + 1u))) break;
            __builtin_amdgcn_s_sleep(8);
        }
    }
    __syncthreads();
    if (w == 0 && lane == 0) __hip_atomic_fetch_add((gu32*)(ws + G_WS_CTL + G_C_ARR), 1u, RLX_AGENT);
}
__device__ __forceinline__ void fill_block(char* ws, int b, int tid, const u32x4& v0, const u32x4& v1) {
    const unsigned o = b * 8192 + tid * 16, o2 = o + 4096;
    asm volatile("global_store_dwordx4 %0, %1, %2 sc1\n\ts_nop 1" :: "v"(o), "v"(v0), "s"(ws) : "memory");
    if (b >= 512 || (b & 6) != 6 || tid < 64)
        asm volatile("global_store_dwordx4 %0, %1, %2 sc1\n\ts_nop 1" :: "v"(o2), "v"(v1), "s"(ws) : "memory");
}
__device__ __forceinline__ void init_fill(char* ws, const u16* mem0, int f, int tid) {
    const u32x4 sent = {0xffffffffu, 0xffffffffu, 0xffffffffu, 0xffffffffu};
    u32x4 v0 = sent, v1 = sent;
    if (f < 32 && (f & 4)) {
#pragma unroll
        for (int k = 0; k < 2; ++k) {
            const int q = tid + 256 * k + ((f & 2) ? 256 : 0), m = q & 15;
            if (!(f & 2) || (q >= 512 && q < 576)) {
                const unsigned* src = (const unsigned*)(mem0 + (2 * (m >> 2) + (m & 1)) * DM + 16 * (q >> 4) + (m & 2));
                const u32x4 o = {min(src[0], 0xfffffffeu), min(src[2], 0xfffffffeu), min(src[4], 0xfffffffeu), min(src[6], 0xfffffffeu)};
                if (k) v1 = o; else v0 = o;
            }
        }
    }
#pragma unroll 1
    for (int q = f; q < 182; q += 48) {
        if (q >= 136 && q < 152 && (tid & 127)) { v0 = u32x4{0u, 0u, 0u, 0u}; v1 = v0; }
        fill_block(ws, q < 64 ? q : q < 136 ? q - 64 + G_D_UM / 8192 : q < 168 ? 256 + ((q - 136) >> 2) * 8 + (q & 3) : q - 168 + G_WS_CTL / 8192, tid, v0, v1);
        v0 = sent; v1 = sent;
    }
    const unsigned e = __hip_atomic_load((const gu32*)(ws + G_WS_EP), RLX_AGENT);
    asm volatile("s_waitcnt vmcnt(0)" :: "v"(e) : "memory");
    __syncthreads();
    if (tid == 0) __hip_atomic_store((gu64*)(ws + G_WS_DONE + (48 + f) * 64), (u64)(e + 1u), RLX_AGENT);
    if (tid == 0) IST(0, 48 + f);
}
__device__ __forceinline__ void init_cal(char* ws, int id, int tid, int n) {
    if (tid < 64) {
        const int pg = id >> 1;
        char* a = ws + (pg < 2 ? pg * 0x200000u : G_D_UM + (pg - 2) * 0x200000u) + G_PROBE + (id & 1) * 8192;
        const u32x4 z = {0u, 0u, 0u, 0u};
        asm volatile("global_store_dwordx4 %0, %1, off sc1\n\tglobal_store_dwordx4 %0, %1, off offset:1024 sc1\n\ts_waitcnt vmcnt(0)" :: "v"(a + tid * 16), "v"(z) : "memory");
        if (tid == 0) {
            unsigned best = 0xffffu;
            const unsigned ts1 = (unsigned)__builtin_amdgcn_s_memrealtime() << 4;
#pragma unroll 1
            for (int it = 0; it < n; ++it) {
                const unsigned c0 = (unsigned)__builtin_amdgcn_s_memtime();
                const unsigned v = __hip_atomic_load((const gu32*)(a + it * 128), RLX_AGENT);
                asm volatile("s_waitcnt vmcnt(0)" :: "v"(v) : "memory");
                best = min(best, (unsigned)__builtin_amdgcn_s_memtime() - c0);
            }
            init_done(ws, id, best);
            IST(0, id);
        }
    }
    __syncthreads();
}
__device__ __forceinline__ unsigned near_mask(char* ws, int lane) {
    const gu32* q = (const gu32*)(ws + G_WS_DONE + (lane & 31) * 128 + 4);
    return (unsigned)__ballot(__hip_atomic_load(q + 16, RLX_AGENT) < __hip_atomic_load(q, RLX_AGENT));
}

#define TST(slot_, i_)
#define PIN8(a) asm volatile("" : "+v"(a[0]), "+v"(a[1]), "+v"(a[2]), "+v"(a[3]), "+v"(a[4]), "+v"(a[5]), "+v"(a[6]), "+v"(a[7]))
struct AOps { u32x4 q[4], k[8]; int fl; };
struct APtr { const char* q; const char* k; const char* m; unsigned voff, vkm, voffm, vfl; };
__device__ __forceinline__ void attn_batch(const MainP& p, AOps& o, const APtr& a) {
    const char* q2 = a.q + 4096; const char* k2 = a.k + 4096;
    WLD16(o.q[0], a.voff, a.q, 0); WLD16(o.q[1], a.voff, a.q, 2048); WLD16(o.q[2], a.voff, q2, 0); WLD16(o.q[3], a.voff, q2, 2048);
    WLD16X4(o.k[0], o.k[1], o.k[2], o.k[3], a.voff, a.k); WLD16X4(o.k[4], o.k[5], o.k[6], o.k[7], a.voff, k2);
    asm volatile("global_load_dword %0, %1, %2 sc1" : "=v"(o.fl) : "v"(a.vfl), "s"(p.ws) : "memory");
}
__device__ __forceinline__ void attn_pin(AOps& o) {
    asm volatile("" : "+v"(o.fl), "+v"(o.q[0]), "+v"(o.q[1]), "+v"(o.q[2]), "+v"(o.q[3])); PIN8(o.k);
}
static constexpr int NFINE = 6;
static constexpr int NG1 = 64 - NFINE;
__device__ __forceinline__ void score_round4(float* r, const f32x4& x) {
    asm volatile("v_cvt_pk_bf16_f32 %0, 0, %4\n\tv_cvt_pk_bf16_f32 %1, 0, %5\n\tv_cvt_pk_bf16_f32 %2, 0, %6\n\tv_cvt_pk_bf16_f32 %3, 0, %7\n\t"
                 "v_mul_f32 %0, 0x3db504f3, %0\n\tv_mul_f32 %1, 0x3db504f3, %1\n\tv_mul_f32 %2, 0x3db504f3, %2\n\tv_mul_f32 %3, 0x3db504f3, %3\n\t"
                 "v_cvt_pk_bf16_f32 %0, 0, %0\n\tv_cvt_pk_bf16_f32 %1, 0, %1\n\tv_cvt_pk_bf16_f32 %2, 0, %2\n\tv_cvt_pk_bf16_f32 %3, 0, %3"
                 : "=&v"(r[0]), "=&v"(r[1]), "=&v"(r[2]), "=&v"(r[3]) : "v"(x[0]), "v"(x[1]), "v"(x[2]), "v"(x[3]));
}
__device__ __forceinline__ float vmax3(float a, float b, float c) { float r; asm("v_max3_f32 %0, %1, %2, %3" : "=v"(r) : "v"(a), "v"(b), "v"(c)); return r; }
template <int N>
__device__ __forceinline__ void attn_soft(float (&q)[12], u32x2& Pm, u32x4& Px) {
    float m = vmax3(q[0], q[1], q[2]);
#pragma unroll
    for (int i = 3; i < N; i += 2) m = vmax3(m, q[i], q[i + 1 < N ? i + 1 : i]);
    { auto a = __builtin_amdgcn_permlane16_swap(__float_as_uint(m), __float_as_uint(m), false, false); m = vmax3(m, __uint_as_float(a[0]), __uint_as_float(a[1])); }
    { auto a = __builtin_amdgcn_permlane32_swap(__float_as_uint(m), __float_as_uint(m), false, false); m = vmax3(m, __uint_as_float(a[0]), __uint_as_float(a[1])); }
    const float ml = -m * 1.4426950408889634f;
#pragma unroll
    for (int i = 0; i < N; ++i) q[i] = __builtin_amdgcn_exp2f(fmaf(q[i], 1.4426950408889634f, ml));
    float sum = 0.5f * ((q[0] + q[1]) + (q[2] + q[3])) + ((q[4] + q[5]) + (q[6] + q[7]));
    if (N > 8) sum += (q[8] + q[9]) + (q[10] + q[11]);
    sum = rowsum4(sum);
    const float inv = __builtin_amdgcn_rcpf(sum);
    Pm = u32x2{pk2bf(q[0] * inv, q[1] * inv), pk2bf(q[2] * inv, q[3] * inv)};
    Px = u32x4{pk2bf(q[4] * inv, q[5] * inv), pk2bf(q[6] * inv, q[7] * inv), N > 8 ? pk2bf(q[8] * inv, q[9] * inv) : 0u, N > 8 ? pk2bf(q[10] * inv, q[11] * inv) : 0u};
}
#define NASPLIT 12
#define HSL 1
#define NAG 2
static constexpr int NA = NASPLIT;
__device__ __forceinline__ int attn_next(int s) { return s < NA ? s + 2 : s + NAG; }
__device__ __forceinline__ unsigned attn_flag(int s, int hq) { return G_WS_CTL + G_C_PC + (s < NA ? s & 1 : (s - NA) % NAG) * 1024 + hq * 128; }
struct BOps { u32x4 q[8], k[8]; };
#define WLD16X4A(d0, d1, d2, d3, voff_, base_) asm volatile("s_nop 4\n\tglobal_load_dwordx4 %0, %4, %5 sc1\n\tglobal_load_dwordx4 %1, %4, %5 offset:1024 sc1\n\t" \
    "global_load_dwordx4 %2, %4, %5 offset:2048 sc1\n\tglobal_load_dwordx4 %3, %4, %5 offset:3072 sc1" \
    : "=&a"(d0), "=&a"(d1), "=&a"(d2), "=&a"(d3) : "v"(voff_), "s"(base_) : "memory")
#define PIN8A(a) asm volatile("" : "+a"(a[0]), "+a"(a[1]), "+a"(a[2]), "+a"(a[3]), "+a"(a[4]), "+a"(a[5]), "+a"(a[6]), "+a"(a[7]))
#define MFMA_AV0(acc, a_, b_) asm volatile("v_mfma_f32_16x16x32_bf16 %0, %1, %2, 0" : "=&v"(acc) : "a"(a_), "v"(b_))
#define MFMA_AV(acc, a_, b_) asm volatile("v_mfma_f32_16x16x32_bf16 %0, %1, %2, %0" : "+v"(acc) : "a"(a_), "v"(b_))
__device__ __forceinline__ void attn_batch8(const MainP& p, BOps& o, const APtr& a) {
    const char* q2 = a.q + 4096; const char* k2 = a.k + 4096;
    WLD16X4(o.q[0], o.q[1], o.q[2], o.q[3], a.voff, a.q); WLD16X4(o.q[4], o.q[5], o.q[6], o.q[7], a.voff, q2);
    WLD16X4A(o.k[0], o.k[1], o.k[2], o.k[3], a.voff, a.k); WLD16X4A(o.k[4], o.k[5], o.k[6], o.k[7], a.voff, k2);
}
__device__ __forceinline__ void attn_step(const MainP& p, BOps& o, BOps& n1, BOps& n2, APtr& a, int& kg, int& kk, int s, int h, int w, int lane) {
    const int g = lane >> 4, c = lane & 15;
    const float scale = 0.08838834764831845f;
    TST(s * 64 + h * 2, 0);
    const int s2 = min(attn_next(attn_next(s)), NSEG - 1);
    if (kg < s2 || kk < s) {
        VMC0();
        if (s < 50) wait_word(p, p.ws + a.vfl, (lane & 1) ? s : s2, 13u); else wait_word<true>(p, p.ws + a.vfl, (lane & 1) ? s : s2, 13u);
    }
    TST(s * 64 + h * 2, 1);
    a.q = p.ws + G_D_QX + (size_t)h * G_HS8 + skew(s2, h) * 8192; a.k = a.q + (G_D_KX - G_D_QX); a.m = p.ws + G_D_KM + (size_t)h * G_HS4 + skew(s, h) * 4096;
    __builtin_amdgcn_sched_barrier(0);
    u32x4 km[4];
    int fl;
    WLD16X4A(km[0], km[1], km[2], km[3], a.vkm, a.m);
    asm volatile("global_load_dword %0, %1, %2 sc1" : "=v"(fl) : "v"(a.vfl), "s"(p.ws) : "memory");
    attn_batch8(p, n2, a);
    f32x4 y[5];
#pragma unroll
    for (int wc = 0; wc < 4; ++wc)
#pragma unroll
        for (int i = 0; i < 3; ++i) { if (wc == 0) MFMA_AV0(y[i], o.k[i >> 1], o.q[(i + 1) >> 1]); else MFMA_AV(y[i], o.k[wc * 2 + (i >> 1)], o.q[wc * 2 + ((i + 1) >> 1)]); }
    asm volatile("s_nop 7" : "+v"(y[0]), "+v"(y[1]), "+v"(y[2]));
    float q0[12], q1[12];
    score_round4(q0 + 4, y[0]); score_round4(q1 + 4, y[1]); score_round4(q1 + 8, y[2]);
#pragma unroll
    for (int i = 0; i < 4; ++i) {
        const bool vis = 4 * g + i <= c;
        q0[4 + i] = vis ? q0[4 + i] : -INFINITY; q1[8 + i] = vis ? q1[8 + i] : -INFINITY;
    }
    __builtin_amdgcn_sched_barrier(0);
    asm volatile("s_waitcnt vmcnt(16)\n\ts_barrier" : "+a"(km[0]), "+a"(km[1]), "+a"(km[2]), "+a"(km[3]), "+v"(fl) :: "memory");
    PIN8(n1.q); PIN8A(n1.k);
    { const unsigned fo = attn_flag(s, h >> 2); const int fv = s - 1;
      asm volatile("s_cmp_lg_u32 %3, 0\n\ts_cbranch_scc1 1f\n\tglobal_store_dword %0, %1, %2 sc1\n1:" :: "v"(fo), "v"(fv), "s"(p.ws), "s"(w) : "scc", "memory"); }
#pragma unroll
    for (int wc = 0; wc < 4; ++wc)
#pragma unroll
        for (int i = 0; i < 2; ++i) { if (wc == 0) MFMA_AV0(y[3 + i], km[0], o.q[i]); else MFMA_AV(y[3 + i], km[wc], o.q[wc * 2 + i]); }
    asm volatile("s_nop 7" : "+v"(y[3]), "+v"(y[4]));
    kg = __builtin_amdgcn_readlane(fl, 0); kk = __builtin_amdgcn_readlane(fl, 1);
    score_round4(q0, y[3]); score_round4(q1, y[4]);
    u32x2 Pm0, Pm1; u32x4 Px0, Px1;
    attn_soft<8>(q0, Pm0, Px0); attn_soft<12>(q1, Pm1, Px1);
    char* pxb = p.ws + G_D_PX + (size_t)(s * 32 + h) * 2048;
    char* pmb = p.ws + G_D_PM + (size_t)s * 16384 + h * 256; char* pm2 = pmb + 8192;
    WST16(a.voff, Px0, pxb, 0); WST16(a.voff, Px1, pxb, 1024);
    asm volatile("global_store_dwordx2 %0, %1, %3 sc1\n\tglobal_store_dwordx2 %0, %2, %4 sc1" :: "v"(a.voffm), "v"(Pm0), "v"(Pm1), "s"(pmb), "s"(pm2) : "memory");
    { const unsigned fo = attn_flag(s, h >> 2);
      asm volatile("s_cmp_lt_u32 %4, 46\n\ts_cbranch_scc1 1f\n\ts_waitcnt vmcnt(0)\n\ts_barrier\n\ts_cmp_lg_u32 %3, 0\n\ts_cbranch_scc1 1f\n\tglobal_store_dword %0, %1, %2 sc1\n1:"
                   :: "v"(fo), "v"(s), "s"(p.ws), "s"(w), "s"(s) : "scc", "memory"); }
    TST(s * 64 + h * 2, 2);
}
__device__ __forceinline__ void attn_role(const MainP& p, int s, int h, int w, int lane) {
    APtr a;
    a.q = p.ws + G_D_QX + (size_t)h * G_HS8 + skew(s, h) * 8192; a.k = a.q + (G_D_KX - G_D_QX);
    a.voff = lane * 16; a.vkm = (lane & 55) * 16; a.voffm = (lane & 15) * 16 + ((lane >> 4) & 1) * 8;
    a.vfl = G_WS_CTL + ((lane & 1) ? G_C_KM : G_C_GQ) + h * 128;
    BOps o0, o1, o2;
    wait_word(p, p.ws + G_WS_CTL + G_C_GQ + h * 128, attn_next(s), 10u);
    attn_batch8(p, o0, a);
    a.q = p.ws + G_D_QX + (size_t)h * G_HS8 + skew(attn_next(s), h) * 8192; a.k = a.q + (G_D_KX - G_D_QX);
    attn_batch8(p, o1, a);
    VMC0();
    PIN8(o0.q); PIN8A(o0.k); PIN8(o1.q); PIN8A(o1.k);
    int kg = -1, kk = -1;
    static_assert(NA % 2 == 0, "ranges");
#pragma unroll 1
    for (;;) {
        attn_step(p, o0, o1, o2, a, kg, kk, s, h, w, lane);
        if ((s = attn_next(s)) >= NG1) break;
        attn_step(p, o1, o2, o0, a, kg, kk, s, h, w, lane);
        if ((s = attn_next(s)) >= NG1) break;
        attn_step(p, o2, o0, o1, a, kg, kk, s, h, w, lane);
        if ((s = attn_next(s)) >= NG1) break;
    }
    VMC0();
}
__device__ __forceinline__ unsigned umax4(const u32x4& v) { return max(max(v[0], v[1]), max(v[2], v[3])); }
__device__ __forceinline__ bool give_up(const MainP& p, unsigned& tries, unsigned code) {
    if (++tries <= kWaitCap / 4) return false;
    __hip_atomic_store((gu32*)(p.ws + G_WS_CTL + G_C_ERR), code, RLX_AGENT);
    return true;
}
__device__ __forceinline__ void task_attn_tail(const MainP& p, int s, int h, int w, int lane) {
    const int g = lane >> 4, c = lane & 15, qt = w & 1;
    const float scale = 0.08838834764831845f;
    APtr a;
    a.q = p.ws + G_D_QX + (size_t)h * G_HS8 + skew(s, h) * 8192 + qt * 1024; a.k = p.ws + G_D_KX + (size_t)h * G_HS8 + skew(s, h) * 8192; a.m = p.ws + G_D_KM + (size_t)h * G_HS4 + skew(s, h) * 4096;
    a.voff = lane * 16; a.vkm = (lane & 55) * 16; a.voffm = c * 16 + (g & 1) * 8; a.vfl = G_WS_CTL;
    AOps o;
    wait_word(p, p.ws + G_WS_CTL + G_C_GQ + h * 128, s, 14u);
    attn_batch(p, o, a);
    VMC0();
    attn_pin(o);
    float q[12];
    u32x4 km[4];
#pragma unroll
    for (int kt = 1; kt >= -1; --kt) {
        if (kt < 0) {
            TST(s * 64 + h * 2 + qt, 0);
            wait_word(p, p.ws + G_WS_CTL + G_C_KM + h * 128, s - 2, 15u);
            if (FLG(p.prm[5]) & 2) wait_word<true>(p, p.ws + G_WS_CTL + G_C_KM + h * 128, s - 1, 15u);
            for (unsigned tries = 0;;) {
                WLD16X4(km[0], km[1], km[2], km[3], a.vkm, a.m);
                asm volatile("s_waitcnt vmcnt(0)" : "+v"(km[0]), "+v"(km[1]), "+v"(km[2]), "+v"(km[3]) :: "memory");
                if (!__any(max(max(umax4(km[0]), umax4(km[1])), max(umax4(km[2]), umax4(km[3]))) == 0xffffffffu) || give_up(p, tries, 16u)) break;
            }
            TST(s * 64 + h * 2 + qt, 1);
        }
        f32x4 y = {0.f, 0.f, 0.f, 0.f};
#pragma unroll
        for (int wc = 0; wc < 4; ++wc) y = MFMA32(kt < 0 ? km[wc] : o.k[wc * 2 + (kt < 0 ? 0 : kt)], o.q[wc], y);
#pragma unroll
        for (int i = 0; i < 4; ++i) q[4 + 4 * kt + i] = (16 * kt + 4 * g + i <= 16 * qt + c) ? bfr(bfr(y[i]) * scale) : -INFINITY;
    }
    u32x2 Pm; u32x4 Px;
    attn_soft<12>(q, Pm, Px);
    char* pxb = p.ws + G_D_PX + (size_t)(s * 32 + h) * 2048 + qt * 1024;
    char* pmb = p.ws + G_D_PM + (size_t)s * 16384 + h * 256 + qt * 8192;
    WST16(a.voff, Px, pxb, 0);
    asm volatile("global_store_dwordx2 %0, %1, %2 sc1" :: "v"(a.voffm), "v"(Pm), "s"(pmb) : "memory");
    TST(s * 64 + h * 2 + qt, 2);
}

#define RLD(d_, voff_, base_, off_) asm volatile("global_load_dwordx4 %0, %1, %2 offset:%3 sc1" : "=v"(d_) : "v"(voff_), "s"(base_), "i"(off_) : "memory")
#define RLDA(d_, voff_, base_, off_) asm volatile("global_load_dwordx4 %0, %1, %2 offset:%3 sc1" : "=a"(d_) : "v"(voff_), "s"(base_), "i"(off_) : "memory")
#define MFMA_ACC_A(acc, a_, b_) asm volatile("v_mfma_f32_16x16x32_bf16 %0, %1, %2, %0" : "+v"(acc) : "a"(a_), "v"(b_))
#define VMW(n_) asm volatile("s_waitcnt vmcnt(%0)" :: "i"(n_) : "memory")
template <int NT, int DEPTH, bool TAIL>
__device__ __forceinline__ void task_out(const MainP& p, char* smem, int s, int ct0, int w, int lane) {
    const int g = lane >> 4, c = lane & 15;
    const unsigned voff = lane * 16;
    f32x4 acc[NT][2];
#pragma unroll
    for (int t = 0; t < NT; ++t) { acc[t][0] = f32x4{0.f, 0.f, 0.f, 0.f}; acc[t][1] = acc[t][0]; asm volatile("" : "+v"(acc[t][0]), "+v"(acc[t][1])); }
    const char* ub[NT];
#pragma unroll
    for (int t = 0; t < NT; ++t) ub[t] = uni(p.ws + G_D_UX + (size_t)(ct0 + t) * G_UXS + skew(s, ct0 + t) * 32768 + w * 8192);
    const char* pb = uni(p.ws + G_D_PX + (size_t)(s * 32 + w * 8) * 2048);
    const char* um = uni(p.ws + um_base(s, p.prm[0]) + ct0 * 16384);
    unsigned vu[NT];
#pragma unroll
    for (int t = 0; t < NT; ++t) { vu[t] = t * 16384 + w * 2048 + voff; asm volatile("" : "+v"(vu[t])); }
    const char* pm = uni(p.ws + G_D_PM + (size_t)s * 16384 + w * 2048);
    constexpr int NR = DEPTH + 1 > 10 ? 10 : DEPTH + 1;
    u32x4 a[NR][NT], b[10][2];
    unsigned vb = voff, va = voff;
    TST(4096 + (s * 36 + ct0) * 4 + w, 0);
    const int f0 = (int)__hip_atomic_load((const gu32*)(p.ws + G_WS_CTL + (lane < 8 ? G_C_UM + (w * 8 + lane) * 128 : lane < 16 ? G_C_GV + ((s < (int)G_S1 ? s & 1 : 0) * 32 + w * 8 + lane - 8) * 128
                                                                                                                                : attn_flag(s, w * 2 + (lane & 1)) - G_WS_CTL)), RLX_AGENT);
    const bool early = !TAIL && !__any(f0 < s);
    if (!early) wait_word(p, p.ws + G_WS_CTL + (lane < 8 ? G_C_GV + ((s < (int)G_S1 ? s & 1 : 0) * 32 + w * 8 + lane) * 128 : G_C_UM + w * 8 * 128), lane < 8 ? s : s - 3, 21u);
#define ALOAD(q) do { if ((q) < 8) { _Pragma("unroll") for (int t = 0; t < NT; ++t) RLDA(a[(q) % NR][t], va, ub[t], 0);  \
                                     asm volatile("v_add_u32 %0, 0x400, %0" : "+v"(va)); }                                            \
                      else { _Pragma("unroll") for (int t = 0; t < NT; ++t) RLDA(a[(q) % NR][t], vu[t], um, ((q) & 1) * 1024); } } while (0)
    constexpr int D0 = DEPTH < 8 ? DEPTH : 8;
    asm volatile("s_nop 4");
#pragma unroll
    for (int q = 0; q < D0; ++q) ALOAD(q);
    asm volatile("s_cmp_lg_u32 %0, 0\n\ts_cbranch_scc1 1f\n\ts_waitcnt vmcnt(0)\n1:\n\ts_waitcnt vmcnt(30)" :: "s"(__builtin_amdgcn_readfirstlane((int)early)) : "scc", "memory");
    unsigned tries = 0;
    if (TAIL) {
        const char* sp = pm + (lane & 7) * 256 + ((lane >> 3) & 1) * 8192;
        const int dl = (FLG(p.prm[5]) >> 26) & 31;
        if (dl) {
            wait_word<true>(p, p.ws + G_WS_CTL + G_C_KM + ((ct0 >> 1) + w) * 128, s - 1, 25u);
            for (int i = 0; i < dl + (s < 63 ? 5 : 0); ++i) __builtin_amdgcn_s_sleep(2);
        } else
        while (__any(__hip_atomic_load((const gu32*)sp, RLX_AGENT) == 0xffffffffu) && !give_up(p, tries, 23u)) __builtin_amdgcn_s_sleep(1);
    } else if (!early) wait_word(p, p.ws + G_WS_CTL + (lane < 8 ? G_C_UM + (w * 8 + lane) * 128 : attn_flag(s, w * 2 + (lane & 1)) - G_WS_CTL), s, 22u);
    if (!early) TST(4096 + (s * 36 + ct0) * 4 + w, 1);
    for (;;) {
    asm volatile("s_nop 4");
#pragma unroll
    for (int q = 0; q < 8; ++q) { RLD(b[q][0], vb, pb, 0); RLD(b[q][1], vb, pb, 1024); asm volatile("v_add_u32 %0, 0x800, %0" : "+v"(vb)); }
#pragma unroll
    for (int q = 8; q < 10; ++q) { RLD(b[q][0], voff, pm, (q & 1) * 1024); RLD(b[q][1], voff, pm + 8192, (q & 1) * 1024); }
#pragma unroll
    for (int q = D0; q < NR; ++q) ALOAD(q);
#pragma unroll
    for (int q = 0; q < 10; ++q) {
        const int issuedA = (NR + q < 10 ? NR + q : 10) - D0;
        if (q < D0) VMW(2 * (9 - q) + issuedA * NT); else VMW((issuedA - (q - D0 + 1)) * NT);
#pragma unroll
        for (int t = 0; t < NT; ++t) { MFMA_ACC_A(acc[t][0], a[q % NR][t], b[q][0]); MFMA_ACC_A(acc[t][1], a[q % NR][t], b[q][1]); }
        if (NR + q < 10) ALOAD(NR + q);
    }
    asm volatile("s_nop 7" ::: "memory");
#pragma unroll
    for (int t = 0; t < NT; ++t) asm volatile("" : "+v"(acc[t][0]), "+v"(acc[t][1]));
    if (!TAIL) break;
    f32x4 chk = acc[0][0];
#pragma unroll
    for (int t = 0; t < NT; ++t) chk += acc[t][1] + acc[t][0];
    const float ck = (chk[0] + chk[1]) + (chk[2] + chk[3]);
    if (!__any(ck != ck)) break;
    unsigned mx = 0;
#pragma unroll
    for (int q = 0; q < 10; ++q) { asm volatile("" : "+v"(b[q][0]), "+v"(b[q][1])); mx = max(mx, max(umax4(b[q][0]), umax4(b[q][1]))); }
#pragma unroll
    for (int t = 0; t < NT; ++t) { asm volatile("" : "+a"(a[8 % NR][t]), "+a"(a[9 % NR][t])); mx = max(mx, max(umax4(a[8 % NR][t]), umax4(a[9 % NR][t]))); }
    if (!__any(mx == 0xffffffffu) || give_up(p, tries, 24u)) break;
    vb = voff;
#pragma unroll
    for (int t = 0; t < NT; ++t) { acc[t][0] = f32x4{0.f, 0.f, 0.f, 0.f}; acc[t][1] = acc[t][0]; asm volatile("" : "+v"(acc[t][0]), "+v"(acc[t][1])); }
    }
    TST(4096 + (s * 36 + ct0) * 4 + w, 2);
    f32x4* red = (f32x4*)smem;
#pragma unroll
    for (int t = 0; t < NT; ++t) { red[((w * NT + t) * 2 + 0) * 64 + lane] = acc[t][0]; red[((w * NT + t) * 2 + 1) * 64 + lane] = acc[t][1]; }
    __syncthreads();
    u16* orow = p.out + (size_t)(32 * s + 16 * (w & 1) + c) * OUTW + 16 * ct0 + 4 * g;
#pragma unroll
    for (int t = 0; t < NT; ++t)
        if ((t & 1) == (w >> 1)) {
            const int m = w & 1;
            const f32x4 v = (red[((0 * NT + t) * 2 + m) * 64 + lane] + red[((1 * NT + t) * 2 + m) * 64 + lane]) +
                            (red[((2 * NT + t) * 2 + m) * 64 + lane] + red[((3 * NT + t) * 2 + m) * 64 + lane]);
            OST8(orow + 16 * t, (u32x2{pk2bf(v[0], v[1]), pk2bf(v[2], v[3])}));
        }
}

__device__ __forceinline__ int index_of_pos(int pv) { return min(min(pv, 128 + (pv >> 1)), min(256 + (pv >> 2), 384 + (pv >> 3))); }
static constexpr int KROW = G_KROW;
static constexpr int KARR = 80 * KROW;
static constexpr int UCS = 2208;
struct BCh {
    u16 uc[4][UCS];
    f32x4 cst[NST];
    unsigned Kd[648];
    u16 Kh[KARR];
    u16 Kl[KARR];
};
static_assert(4 * sizeof(BCh) <= LDS_TOTAL - 32, "LDS");
__device__ __forceinline__ unsigned prm(unsigned hi, unsigned lo, unsigned sel) { return __builtin_amdgcn_perm(hi, lo, sel); }
#define BST(i) asm volatile("" ::: "memory")
#define UT_LOAD(p_) __builtin_nontemporal_load(p_)
template <class F>
__device__ __forceinline__ void task_ut(const MainP& p, char* smem, int rb, int w, int lane, F more) {
    const int tid = w * 64 + lane, g = lane >> 4, c = lane & 15;
    { char* ws = p.ws; if (tid == 0) IST(4, rb); }
    const u32x4* src = (const u32x4*)(p.U + (size_t)rb * 64 * DM);
    u32x4 v[18];
#pragma unroll
    for (int i = 0; i < 18; ++i) v[i] = UT_LOAD(src + tid + 256 * i);
#pragma unroll
    for (int i = 0; i < 18; ++i) { const int ch = tid + 256 * i, row = ch / 72; *(u32x4*)(smem + row * 1184 + (ch - 72 * row) * 16) = v[i]; }
    __syncthreads();
    { char* ws = p.ws; if (tid == 0) IST(3, rb); }
    const unsigned ad = (unsigned)(uintptr_t)smem + (16 * g + (c >> 2)) * 1184 + (w * 144 + 4 * (c & 3)) * 2;
    char* tw = smem + 77824 + w * 20736;
#pragma unroll
    for (int i = 0; i < 9; ++i) {
        u32x2 x0, x1, x2, x3;
        asm volatile("ds_read_b64_tr_b16 %0, %4 offset:%5\n\tds_read_b64_tr_b16 %1, %4 offset:%6\n\tds_read_b64_tr_b16 %2, %4 offset:%7\n\tds_read_b64_tr_b16 %3, %4 offset:%8\n\ts_waitcnt lgkmcnt(0)"
                     : "=&v"(x0), "=&v"(x1), "=&v"(x2), "=&v"(x3) : "v"(ad), "i"(i * 32), "i"(4 * 1184 + i * 32), "i"(8 * 1184 + i * 32), "i"(12 * 1184 + i * 32) : "memory");
        u32x4* o = (u32x4*)(tw + (16 * i + c) * 144 + g * 32);
        o[0] = u32x4{x0[0], x0[1], x1[0], x1[1]}; o[1] = u32x4{x2[0], x2[1], x3[0], x3[1]};
    }
    asm volatile("" ::: "memory");
    const unsigned vo = (w * 144 + (lane >> 3)) * (SL * 2) + rb * 128 + (lane & 7) * 16;
    const char* dst = p.ws + G_D_UT;
#pragma unroll
    for (int i = 0; i < 18; ++i) {
        const u32x4 a = *(const u32x4*)(tw + (8 * i + (lane >> 3)) * 144 + (lane & 7) * 16);
        const unsigned vi = vo + i * 8 * (SL * 2);
        WST16(vi, a, dst, 0);
    }
    VMC0();
    init_wait(p.ws, w, lane);
    if (tid == 0) __hip_atomic_store((gu32*)(p.ws + G_WS_CTL + G_C_UT + rb * 128), 1u, RLX_AGENT);
    { char* ws = p.ws; if (tid == 0) IST(2, rb); }
    more();
    VMC0();
    __syncthreads();
    if (tid == 0) __hip_atomic_store((gu32*)(p.ws + G_WS_CTL + G_C_UT + rb * 128), 2u, RLX_AGENT);
}
struct KPar { u16 ldt, dsk, pa[2][4]; };
__device__ __forceinline__ KPar kgen_par(const MainP& p, int d, int lane) {
    KPar k;
    k.ldt = p.logdt[d]; k.dsk = p.Dskip[d];
#pragma unroll
    for (int q = 0; q < 2; ++q) { const int i = d * NST + lane + 64 * q; k.pa[q][0] = p.logA[i]; k.pa[q][1] = p.Aim[i]; k.pa[q][2] = p.Cre[i]; k.pa[q][3] = p.Cim[i]; }
    return k;
}
__device__ __forceinline__ void task_s4d(const MainP& p, BCh& L, int d, int lane, bool first, int& nb, KPar& kp, int& pend) {
    const int g = lane >> 4, c = lane & 15;
    BST(0);
    const float dsk = bf2f(kp.dsk);
    const auto& pa = kp.pa;
    const float dt = bfr(expf(bf2f(kp.ldt)));
#pragma unroll
    for (int q = 0; q < 2; ++q) {
        const float ar = -bfr(expf(bf2f(pa[q][0])));
        const float cr = bf2f(pa[q][2]) * dt, ci = bf2f(pa[q][3]) * dt;
        L.cst[lane + 64 * q] = f32x4{ar * dt * 1.4426950408889634f, bf2f(pa[q][1]) * dt * 0.15915494309189535f,
                                     __log2f(hypotf(cr, ci)), atan2f(ci, cr) * 0.15915494309189535f};
    }
    const char* ut = uni(p.ws + G_D_UT + (size_t)d * 4096);
    const unsigned voff = lane * 16;
    u32x4 uu[4];
    unsigned tk = nb >= 0 ? 1u : 0u;
    __builtin_amdgcn_sched_barrier(0);
    asm volatile("s_mov_b64 exec, 1\n\tglobal_atomic_add %0, %1, %0, %2 sc0\n\ts_mov_b64 exec, -1" : "+v"(tk) : "v"(G_WS_CTL + G_C_TASK + 128), "s"(p.ws) : "memory");
    const char* ue = uni(first ? (const char*)p.logA : ut);
    WLD16X4A(uu[0], uu[1], uu[2], uu[3], voff, ue);
    BST(1);
    {
        f32x4 acc[3];
#pragma unroll
        for (int v = 0; v < 3; ++v) acc[v] = f32x4{0.f, 0.f, 0.f, 0.f};
        auto base_of = [](int v, int r) { return v == 0 ? (r & 7) + 128 * (r >> 3) : v == 1 ? (r < 8 ? 256 + 2 * (r & 3) + 128 * (r >> 2) : 512 + 4 * (r & 1) + 128 * ((r - 8) >> 1))
                                                                                           : 1024 + 128 * min(r, 8); };
        const float b0 = (float)base_of(0, c), b1 = (float)base_of(1, c), b2 = (float)base_of(2, c), off = (float)(8 * c);
#define KG_POLAR(h_, l_, lg_, f_, a_, b_) do { const float m_ = __builtin_amdgcn_exp2f(fmaf(cc[0], b_, lg_)), p_ = __builtin_amdgcn_fractf(fmaf(a_, b_, f_)); \
            const float re_ = m_ * __builtin_amdgcn_cosf(p_), im_ = m_ * __builtin_amdgcn_sinf(p_);                                                            \
            h_ = pk2bf(re_, im_); l_ = pk2bf(re_ - lo2f(h_), im_ - hi2f(h_)); } while (0)
#pragma unroll 1
        for (int t = 0; t < NST / 16; ++t) {
            u32x4 fh, fl, ah[3], al[3];
#pragma unroll
            for (int j = 0; j < 4; ++j) {
                const f32x4 cc = L.cst[16 * t + 4 * g + j];
                KG_POLAR(fh[j], fl[j], 0.f, 0.f, -cc[1], off);
                KG_POLAR(ah[0][j], al[0][j], cc[2], cc[3], cc[1], b0); KG_POLAR(ah[1][j], al[1][j], cc[2], cc[3], cc[1], b1); KG_POLAR(ah[2][j], al[2][j], cc[2], cc[3], cc[1], b2);
            }
#pragma unroll
            for (int v = 0; v < 3; ++v) { acc[v] = MFMA32(al[v], fh, acc[v]); acc[v] = MFMA32(ah[v], fl, acc[v]); acc[v] = MFMA32(ah[v], fh, acc[v]); }
        }
        BST(2);
        auto split = [](float k) { const unsigned hi = pk2bf(k, 0.f) & 0xffffu; return hi | (pk2bf(0.f, k - __uint_as_float(hi << 16)) & 0xffff0000u); };
#pragma unroll
        for (int v = 0; v < 3; ++v)
#pragma unroll
            for (int i = 0; i < 4; ++i) {
                const int pos = base_of(v, 4 * g + i) + 8 * c;
                if (pos <= 2048) L.Kd[index_of_pos(pos)] = split(2.0f * acc[v][i]);
            }
    }
    __builtin_amdgcn_sched_barrier(0);
    asm volatile("s_waitcnt vmcnt(0)" : "+v"(tk), "+a"(uu[0]), "+a"(uu[1]), "+a"(uu[2]), "+a"(uu[3]) :: "memory");
    if (first) {
        wait_word(p, p.ws + G_WS_CTL + G_C_UT + (lane & 31) * 128, 1, 30u);
        WLD16X4A(uu[0], uu[1], uu[2], uu[3], voff, ut);
        asm volatile("s_waitcnt vmcnt(0)" : "+a"(uu[0]), "+a"(uu[1]), "+a"(uu[2]), "+a"(uu[3]) :: "memory");
    }
    if (pend >= 0 && lane == 0) __hip_atomic_fetch_add((gu32*)(p.ws + G_WS_CTL + G_C_YC + pend * 128), 1u, RLX_AGENT);
    nb = __builtin_amdgcn_readfirstlane(tk) + 1;
    const u32x4 z4 = {0u, 0u, 0u, 0u};
    *(u32x4*)&L.Kh[8 * lane] = z4; *(u32x4*)&L.Kl[8 * lane] = z4;
    *(u32x4*)&L.Kh[512 + 8 * (lane & 31)] = z4; *(u32x4*)&L.Kl[512 + 8 * (lane & 31)] = z4;
    *(u32x4*)&L.uc[0][64 + SL] = z4;
    *(u32x4*)&L.uc[(lane >> 3) & 3][8 * (lane & 7)] = z4;
    asm volatile("" ::: "memory");
#pragma unroll
    for (int q = 0; q < 4; ++q) {
        const int e = lane + 64 * q, at = (16 + (e >> 2)) * KROW + 24 - 8 * (e & 3);
        unsigned k[8];
        const unsigned* kd = L.Kd + index_of_pos(8 * e);
        if (q == 0) {
            const bool two = lane >= 32;
#pragma unroll
            for (int r = 0; r < 8; ++r) k[7 - r] = kd[two ? (0x43222100 >> (4 * r)) & 7 : r];
        } else if (q == 1) {
            const unsigned k0 = kd[0], k1 = kd[1], k2 = kd[2];
            k[7] = k[6] = k[5] = k0; k[4] = k[3] = k[2] = k1; k[1] = k[0] = k2;
        } else {
            const unsigned k0 = kd[0], k1 = kd[1];
            k[7] = k[6] = k[5] = k[4] = k0; k[3] = (e & 1) ? k1 : k0; k[2] = k[1] = k[0] = k1;
        }
        u32x4 kh, kl;
#pragma unroll
        for (int j = 0; j < 4; ++j) { kh[j] = prm(k[2 * j + 1], k[2 * j], 0x05040100u); kl[j] = prm(k[2 * j + 1], k[2 * j], 0x07060302u); }
        *(u32x4*)&L.Kh[at] = kh;
        if (G_NHL == 2) *(u32x4*)&L.Kl[at] = kl;
    }
    BST(3);
#pragma unroll
    for (int q = 0; q < 4; ++q) *(u32x4*)&L.uc[0][64 + 8 * (lane + 64 * q)] = uu[q];
    asm volatile("" ::: "memory");
#pragma unroll
    for (int q = 0; q < 4; ++q) {
        const int e = lane + 64 * q;
        const int ue = 56 + 8 * e;
        const u32x4 a = *(const u32x4*)&L.uc[0][ue];
        const u32x2 b = *(const u32x2*)&L.uc[0][ue + 8];
        const unsigned s0 = __builtin_amdgcn_alignbit(a[1], a[0], 16), s1 = __builtin_amdgcn_alignbit(a[2], a[1], 16), s2 = __builtin_amdgcn_alignbit(a[3], a[2], 16),
                       s3 = __builtin_amdgcn_alignbit(b[0], a[3], 16), s4 = __builtin_amdgcn_alignbit(b[1], b[0], 16);
        *(u32x4*)&L.uc[1][ue] = u32x4{s0, s1, s2, s3};
        *(u32x4*)&L.uc[2][ue] = u32x4{a[1], a[2], a[3], b[0]};
        *(u32x4*)&L.uc[3][ue] = u32x4{s1, s2, s3, s4};
    }
    if (lane == 0) {
        const u32x4 a = *(const u32x4*)&L.uc[0][56 + SL];
        const unsigned s0 = __builtin_amdgcn_alignbit(a[1], a[0], 16), s1 = __builtin_amdgcn_alignbit(a[2], a[1], 16), s2 = __builtin_amdgcn_alignbit(a[3], a[2], 16), s3 = a[3] >> 16;
        *(u32x4*)&L.uc[1][56 + SL] = u32x4{s0, s1, s2, s3};
        *(u32x4*)&L.uc[2][56 + SL] = u32x4{a[1], a[2], a[3], 0u};
        *(u32x4*)&L.uc[3][56 + SL] = u32x4{s1, s2, s3, 0u};
    }
    kp = kgen_par(p, min(nb, DM - 1), lane);
    BST(4);
    {
        const int o = 33 + c + 8 * g;
        const unsigned ua = (unsigned)(uintptr_t)&L.uc[o & 3][o & ~3], ka = (unsigned)(uintptr_t)&L.Kh[(16 + c) * KROW + 8 * g];
        const f32x4* res = (const f32x4*)L.Kh + lane;
        asm volatile(ASM_CV :: "v"(ua), "v"(ka), "v"((unsigned)(uintptr_t)res) : CV_CLOB);
        asm volatile("" :: "v"(kp.ldt), "v"(kp.dsk), "v"(kp.pa[0][0]), "v"(kp.pa[0][1]), "v"(kp.pa[0][2]), "v"(kp.pa[0][3]), "v"(kp.pa[1][0]), "v"(kp.pa[1][1]), "v"(kp.pa[1][2]), "v"(kp.pa[1][3]));
        BST(5);
#pragma unroll
        for (int par = 0; par < 2; ++par)
#pragma unroll
            for (int t = 0; t < 4; ++t) {
                const int n = 16 * (par + 2 * (16 * t + c)) + 4 * g;
                const u32x2 uu = *(const u32x2*)&L.uc[0][64 + n];
                const f32x4 y4 = res[(par * 4 + t) * 64];
                const float r0 = bfr(y4[0]) + bfr(lo2f(uu[0]) * dsk), r1 = bfr(y4[1]) + bfr(hi2f(uu[0]) * dsk);
                const float r2 = bfr(y4[2]) + bfr(lo2f(uu[1]) * dsk), r3 = bfr(y4[3]) + bfr(hi2f(uu[1]) * dsk);
                *(u32x2*)&L.uc[1][n] = u32x2{pk2bf(r0, r1), pk2bf(r2, r3)};
            }
    }
    BST(6);
    char* yb = uni(p.ws + G_D_YB + (size_t)d * 4096);
#pragma unroll
    for (int q = 0; q < 4; ++q) { const u32x4 o = *(const u32x4*)&L.uc[1][8 * (lane + 64 * q)]; WST16(voff, o, yb, q * 1024); }
    pend = d >> 6;
    BST(7);
}
__device__ __forceinline__ void s4d_wave(const MainP& p, char* smem, int w, int lane, int quota) {
    int nb = 0, pend = -1;
    if (lane == 0) nb = (int)(__hip_atomic_fetch_add((gu32*)(p.ws + G_WS_CTL + G_C_TASK + 128), 1u, RLX_AGENT) + 1u);
    nb = __builtin_amdgcn_readfirstlane(nb);
    KPar kp = kgen_par(p, min(nb, DM - 1), lane);
#pragma unroll 1
    for (int n = 0; n < quota; ++n) {
        const int d = nb;
        if (d >= DM) break;
        nb = n + 1 < quota ? 0 : -1;
        task_s4d(p, reinterpret_cast<BCh*>(smem)[w], d, lane, n == 0, nb, kp, pend);
    }
    VMC0();
    if (pend >= 0 && lane == 0) __hip_atomic_fetch_add((gu32*)(p.ws + G_WS_CTL + G_C_YC + pend * 128), 1u, RLX_AGENT);
}
__device__ __forceinline__ void watch_role(const MainP& p, int lane) {
    const char* c = p.ws + G_WS_CTL;
#pragma unroll 1
    for (int s = G_S0; s < NSEG; ++s) {
        const int n = min(s + 3, NSEG - 1);
        wait_word(p, c + G_C_GV + lane * 128, n - (n < (int)G_S1 ? (n ^ (lane >> 5)) & 1 : 0), 60u);
        wait_word(p, c + (lane < 32 ? G_C_GX : G_C_UT - 4096) + lane * 128, lane < 32 ? min(s + 2, NSEG - 1) : s < 40 ? -1 : 2, 61u);
        if (lane == 0) __hip_atomic_store((gu32*)(p.ws + G_WS_CTL + G_C_RDY + s * 128), 1u, RLX_AGENT);
    }
}
__device__ __forceinline__ void task_merge(const MainP& p, char* smem, int gr, int i, int w, int lane) {
    const int g = lane >> 4, c = lane & 15;
    wait_word(p, p.ws + G_WS_CTL + G_C_YC + gr * 128, 63, 31u);
    const char* sb = uni(p.ws + G_D_YB + (size_t)gr * 262144 + i * 1024 + w * 256);
    const unsigned vt = g * 4096 + c * 16;
    u32x4 z[16];
#pragma unroll
    for (int j = 0; j < 16; ++j) { const char* sj = sb + j * 16384; WLD16(z[j], vt, sj, 0); }
    asm volatile("s_waitcnt vmcnt(0)" : "+v"(z[0]), "+v"(z[1]), "+v"(z[2]), "+v"(z[3]), "+v"(z[4]), "+v"(z[5]), "+v"(z[6]), "+v"(z[7]),
                 "+v"(z[8]), "+v"(z[9]), "+v"(z[10]), "+v"(z[11]), "+v"(z[12]), "+v"(z[13]), "+v"(z[14]), "+v"(z[15]) :: "memory");
    char* tw = smem + w * 17408;
#pragma unroll
    for (int j = 0; j < 16; ++j) *(u32x4*)(tw + (4 * j + g) * 272 + c * 16) = z[j];
    const unsigned ad = (unsigned)(uintptr_t)tw + (8 * g + (c >> 2)) * 272 + (c & 3) * 8;
    u16* orow = p.out + (size_t)(512 * i + 128 * w + c) * OUTW + DM + 64 * gr + 8 * g;
#pragma unroll
    for (int k = 0; k < 8; ++k) {
        u32x2 x0, x1, x2, x3;
        asm volatile("ds_read_b64_tr_b16 %0, %4 offset:%5\n\tds_read_b64_tr_b16 %1, %4 offset:%6\n\tds_read_b64_tr_b16 %2, %4 offset:%7\n\tds_read_b64_tr_b16 %3, %4 offset:%8\n\ts_waitcnt lgkmcnt(0)"
                     : "=&v"(x0), "=&v"(x1), "=&v"(x2), "=&v"(x3) : "v"(ad), "i"(k * 32), "i"(4 * 272 + k * 32), "i"(32 * 272 + k * 32), "i"(36 * 272 + k * 32) : "memory");
        OST16(orow + (size_t)16 * k * OUTW, (u32x4{x0[0], x0[1], x1[0], x1[1]}));
        OST16(orow + (size_t)16 * k * OUTW + 32, (u32x4{x2[0], x2[1], x3[0], x3[1]}));
    }
}

static constexpr int Q1 = NG1 * 6 + 36, Q2 = Q1 + (NFINE - 2) * 25;
static constexpr int NTASK = Q2 + 68;
__host__ __device__ __forceinline__ void task_of(int u, int m0, int& sg, int& r) {
    if (u < 6 * m0) { sg = u / 6; r = u % 6; }
    else if (u < 6 * m0 + 108) { sg = m0 + (u - 6 * m0) / 9; r = (u - 6 * m0) % 9; }
    else if (u < Q1) { sg = (u - 36) / 6; r = (u - 36) % 6; }
    else if (u < Q2) { sg = NG1 + (u - Q1) / 25; r = (u - Q1) % 25; }
    else { sg = 62 + (u - Q2) / 34; r = (u - Q2) % 34; }
}
__device__ __forceinline__ void worker_body(const MainP& p, char* smem, const int w, const int lane) {
    const int tid = w * 64 + lane;
    int* slot = (int*)(smem + LDS_TOTAL - 16);
    int nst = 0, nxt = -1;
    gu32* qa = (gu32*)(p.ws + G_WS_CTL + G_C_TASK);
    const int m0 = (FLH(p.prm[7]) >> 4) & 63;
    for (;;) {
        if (tid == 0) {
            *slot = nxt >= 0 ? nxt : (int)(__hip_atomic_fetch_add(qa, 1u, RLX_AGENT) + 1u);
            nxt = -1;
        }
        __syncthreads();
        const int u = __builtin_amdgcn_readfirstlane(*slot);
        if (u >= NTASK) break;
        if (tid == 0 && u < Q1 - 140) nxt = (int)(__hip_atomic_fetch_add(qa, 1u, RLX_AGENT) + 1u);
        const bool last2 = u >= Q2;
        int sg, r;
        task_of(u, m0, sg, r);
        if (u < Q1) {
            if (r < 6) task_out<6, 6, false>(p, smem, sg, 6 * r, w, lane);
            else { const int m = 3 * (sg - m0) + r - 6; task_merge(p, smem, m >> 2, m & 3, w, lane); }
        }
        else if (r < 16) task_attn_tail(p, sg, 2 * r + (w >> 1), w, lane);
        else if (last2) task_out<2, 9, true>(p, smem, sg, 2 * (r - 16), w, lane);
        else task_out<4, 9, true>(p, smem, sg, 4 * (r - 16), w, lane);
        if (last2) break;
        __syncthreads();
    }
}

__host__ __device__ constexpr unsigned phys(unsigned x) { return (x >> 13) * 16384 + (x & 8191); }
__host__ __device__ constexpr unsigned aMB(int f, int b) { return phys(f < 8 ? 16384 + f * 1024 + b : 28672 + b); }
__host__ __device__ constexpr unsigned aPX(int pr, int b) { return phys(pr * 1024 + b); }
__host__ __device__ constexpr unsigned aPM(int u, int b) { return phys(24576 + u * 1024 + b); }
static constexpr int L_KI = 8192, L_QI = 16384, L_VI = 147456;

__device__ __forceinline__ int lane_id() { int l; asm volatile("v_mbcnt_lo_u32_b32 %0, -1, 0\n\tv_mbcnt_hi_u32_b32 %0, -1, %0" : "=v"(l)); return l; }
typedef const __attribute__((address_space(4))) MainP* KArg;

__global__ __launch_bounds__(256, 1) void main_kernel(MainP p_) {
    __shared__ __attribute__((aligned(16))) char smem[LDS_TOTAL];
    KArg ka = (KArg)__builtin_amdgcn_kernarg_segment_ptr();
    static_assert(offsetof(MainP, W) == 0x28, "argument layout");
    asm volatile(ASM_EA :: "s"(ka), "s"(blockIdx.x), "v"(threadIdx.x) : "v1", "v2", "s40", "s41", "s42", "s43", "s44", "s45", "s46", "s47", "s48", "s49", "scc", "memory");
    asm volatile("" : "+s"(ka));
    const int b = blockIdx.x, x = b & 7, ix = b >> 3;
    const int w = __builtin_amdgcn_readfirstlane(threadIdx.x >> 6);
    const bool isr = x < 4 && ix >= 16;
    constexpr int NRF = 8 + 8 * G_GVN;
    if (__builtin_expect(ix < (x < 4 ? 16 : NRF), 1)) {
        const int h = (x & 3) * 8 + (ix & 7), id = h + (ix >> 4) * 32;
        const unsigned stb = 0;
#define UNR _Pragma("unroll")
#define CW(k, val) do { if ((k) < 16) ca[(k) & 15] = (unsigned)(val); else if ((k) < 32) cb[(k) & 15] = (unsigned)(val); else cl[((k) - 40) & 15] = (unsigned)(val); } while (0)
#define ROLEW(A_) asm volatile(A_ : "+{v[0:15]}"(ca), "+{v[16:31]}"(cb), "+{v[200:215]}"(cl) : "s"(ka), "s"(w), "s"(id), "s"(stb) : ASM_CLOBW)
#define PAD(n_) asm volatile("s_branch skip_" n_ "\npad_" n_ ":\n\ts_setpc_b64 s[46:47]\nskip_" n_ ":")
#define CW_ALL const int lane = lane_id(), g = lane >> 4, c = lane & 15, q = c >> 2; u32x16 ca = {}, cb = {}, cl = {};                                      \
        CW(0, lane * 16); CW(40, (4 * w + g) * 8192 + h * 256 + (c ^ (2 * g + 8 * (w >> 1))) * 16);                                                          \
        UNR for (int k = 0; k < 2; ++k) CW(41 + k, (8 * g + q) * 256 + ((4 * w + 2 * k + ((c >> 1) & 1)) ^ (2 * q + 8 * (g & 1))) * 16 + (c & 1) * 8)
#define CW_SRC(b_, key_) UNR for (int k = 0; k < 9; ++k) { const int pp = 64 * k + lane, r = pp / 72, pc = pp - 72 * r; CW(43 + k, (b_) + (8 * w + r) * 1152 + (pc ^ (key_)) * 16); }
#define UNIT(u_, key_) (((u_) >> 2) * 128 + (((u_) & 3) ^ (key_)) * 32 + (c & 3) * 8)
#define CW_NEAR CW(1, x * 65536 + aMB(2 * w, lane * 16)); CW(2, x * 65536 + aMB(8, w * 256 + lane * 4)); CW(3, 2 * w * 1024 + lane * 16);                            \
        UNR for (int i = 0; i < 6; ++i) CW(4 + i, ((2 * w + 2 + i) & 7) * 1024 + lane * 16);                                                                 \
        CW(10, (c & 2) ? 0xEEEE : 0x4444);                                                                                                                   \
        CW(20, x * 65536 + 22528 + (lane & 31) * 64); CW(21, lane < 32 ? (lane & 3) * 65536 + phys((lane >> 2) * 2048) + h * 64 : 32768 + h * 128);          \
        CW(22, lane * 4); CW(23, x * 65536 + aMB(2 * w + 1, lane * 16))
#define CW_FAR CW(0, c * 1152 + (g ^ (c >> 1)) * 16); CW(1, ca[0]); CW(2, ca[0] ^ 64); CW(3, w * 9216 + lane * 16); CW_SRC(0, (r >> 1) + 4 * (w & 1))                                   \
        CW(4, L_VI + w * 2048 + lane * 16); CW(5, L_VI + lane * 16); CW(6, lane * 4)
        if (x >= 4 && ix >= 8) {
            PAD("gv"); CW_ALL; CW_FAR;
            UNR for (int t = 0; t < 9; ++t) CW(8 + t, (9 * w + t) * G_UXS + skew(0, 9 * w + t) * 32768 + h * 1024 + lane * 16);
            UNR for (int k = 0; k < 2; ++k) CW(19 + k, (9 * w + 4 * k + g) * 16384 + h * 256 + c * 16);
            CW(21, (9 * w + 8) * 16384 + h * 256 + lane * 4);
            UNR for (int k = 0; k < 4; ++k) CW(52 + k, (4 * g + q) * 1152 + ((9 * w + k) >> 2) * 128 + ((2 * ((9 * w + k) & 3) + ((c >> 1) & 1)) ^ (2 * g + (q >> 1))) * 16 + (c & 1) * 8);
            ROLEW(ASM_GV);
        } else if (x >= 4) {
            PAD("gx"); CW_ALL; CW_FAR;
            CW(8, w * 2048 + lane * 16);
            ROLEW(ASM_GX);
        } else if (ix < 8) {
            PAD("qk"); CW_ALL; CW_NEAR;
            CW(12, L_QI + (16 * w + (c & 13)) * 32 + 8 * g + 4 * ((c >> 1) & 1));
            CW(13, L_QI + (8 * g + q) * 32 + (c & 3) * 8);
            CW(14, G_WS_CTL + G_C_KM + h * 128); CW(15, lane * 16 + 4096);
            CW(16, L_KI + (32 * w + c) * 16 + 4 * g);
            CW(17, L_KI + (4 * g + q) * 16 + (c & 1) * 8);
            const int mm = 8 * (w & 1) + 4 * (g >> 1) + (g & 1);
            CW(18, (x ^ 3) * 65536 + aPX(h >> 1, (h & 1) * 512 + (w & 1) * 256 + g * 64 + (c >> 3) * 32 + ((c >> 1) & 1) * 16 + (2 * (q & 1) + (c & 1)) * 4));
            CW(19, ((c & 1) + 2 * (c >> 3)) * 65536 + aPM(h >> 3, ((h >> 1) & 3) * 256 + (mm + 2 * ((c >> 1) & 1)) * 16 + (h & 1) * 8 + (q & 1) * 4));
            CW(24, G_WS_CTL + G_C_GX + h * 128);
            ROLEW(ASM_QK);
        } else {
            PAD("vv"); CW_ALL; CW_NEAR; CW_SRC(h * 147456, r & 4);
            const int tq = 9 * w + 2 * (g & 1) + (g >> 1);
            CW(14, tq * 16384 + h * 256 + c * 16);
            CW(15, (tq + 4) * 16384 + h * 256 + c * 16);
            CW(16, (9 * w + 8) * 16384 + h * 256 + c * 16 + (g & 1) * 8);
            CW(19, (c & 8) ? 0xEEEE : 0x4444);
            CW(12, L_QI + w * 576 + c * 16 + 4 * g);
            CW(13, L_QI + (8 * g + q) * 16 + (c & 1) * 8 + ((c >> 1) & 1) * 576);
            CW(25, L_QI + (8 * g + q) * 16 + (c & 1) * 8 + (1 - ((c >> 1) & 1)) * 576);
            UNR for (int k = 0; k < 4; ++k) CW(52 + k, ((k >> 1) ^ (q >> 1)) * 36864 + (4 * g + (q & 1)) * 1152 + UNIT(9 * w + 2 * (k & 1) + (q >> 1), 2 * (g & 1)));
            CW(24, (q >> 1) * 36864 + (4 * g + (q & 1)) * 1152 + UNIT(9 * w + 8, 2 * (g & 1)));
            ROLEW(ASM_V);
        }
    } else {
    if (isr && (ix < 25 || ix >= 29)) {
        const int id = x * 12 + (ix < 25 ? ix - 13 : ix - 29);
        init_cal(ka->ws, id, threadIdx.x, (FLG(ka->prm[5]) >> 16) & 15);
        if (FLG(ka->prm[5]) & 4) {
            const unsigned o = (id * 384 + threadIdx.x) * 128, o2 = o + (threadIdx.x & 128 ? 16384 : 32768);
            const char* a = (const char*)ka->X + o; const char* b2 = (const char*)ka->U + o;
            const char* c2 = (const char*)(w < 2 ? ka->X : ka->U) + o2;
            const char* ps = (const char*)(w == 0 ? ka->logA : w == 1 ? ka->Aim : w == 2 ? ka->Cre : ka->Cim) + id * 3072 + min((int)(threadIdx.x & 63), 23) * 128;
            asm volatile("s_mov_b32 m0, %4\n\ts_nop 0\n\tglobal_load_lds_dword %0, off\n\tglobal_load_lds_dword %1, off\n\tglobal_load_lds_dword %2, off\n\tglobal_load_lds_dword %3, off"
                         :: "v"(a), "v"(c2), "v"(b2), "v"(ps), "s"(163072) : "memory");
        }
    }
    if (x >= 4 ? ix >= 24 : (ix >= 25 && ix < 29)) init_fill(ka->ws, ka->mem0, x >= 4 ? (ix - 24) * 4 + (x & 3) : 32 + (ix - 25) * 4 + x, threadIdx.x);
    if (x >= 4 && !(FLG(ka->prm[5]) & 1)) {
        const char* src = (const char*)ka->W[3] + (size_t)((x & 3) * 8 + (ix & 7)) * 147456 + threadIdx.x * 128;
#pragma unroll
        for (int i = 0; i < 5; ++i)
            if (i < 4 || w < 2) asm volatile("s_mov_b32 m0, %1\n\ts_nop 0\n\tglobal_load_lds_dword %0, off" :: "v"(src + i * 32768), "s"(155648) : "memory");
    }
    if (isr && ix >= 25) {
        for (int i = 0; i < (ix < 29 ? 3 : 0) + HSL; ++i) __builtin_amdgcn_s_sleep(40);
        init_wait(ka->ws, w, threadIdx.x & 63);
    }
    asm volatile("" : "+s"(ka));
    if (x < 4 && (ix < 25 || w == 0)) {
        const int lane = threadIdx.x & 63, g = lane >> 4, c = lane & 15;
        const unsigned lb = w * G_STG;
        unsigned* ct = (unsigned*)(smem + lb) + lane;
        asm volatile("" :: "v"(ct) : "memory");
#define CTW(k, val) ct[(k) * 64] = (unsigned)(val)
        const u64 ws = (u64)ka->ws, prm = (u64)ka;
        const u64 stb = 0;
        CTW(0, lane * 16);
        const int j = x * 9 + ix - 16;
        if (ix >= 25) {
            CTW(1, x * 65536 + 22528 + (lane & 31) * 64);
            for (int k = 0; k < 7; ++k) {
                const int q = min(ix - 25 + 7 * k, 43), id = q < 36 ? 9 * x + (q >> 2) : 8 * x + q - 36;
                CTW(4 + k, (q < 36 ? G_D_UX + id * G_UXS + skew(0, id) * 32768 + (q & 3) * 8192 : G_D_KX + id * G_HS8 + skew(0, id) * 8192) + lane * 128);
                CTW(11 + k, q < 36 ? 32768 : 8192);
            }
        } else {
            const int rot = (FLG(ka->prm[5]) & 16) ? 0 : (x ^ 3) * 4;
            if (w >= 2) {
                return;
            }
            CTW(1, lane * 16 + 4096);
            CTW(2, x * 65536 + aMB(j >> 2, (j & 3) * 256 + (4 * g + 2 * ((c >> 1) & 1) + (c & 1)) * 16 + (c >> 2) * 4));
            for (int k = 0; k < 5; ++k) { const int q = 9 * k + ix - 16; CTW(46 + k, x * 65536 + lane * 16 + (q < 29 ? phys(q * 1024) : 0x200000 + phys((q - 29) * 1024))); }
            CTW(4, (c & 2) ? 0xEEEE : 0x4444); CTW(5, (c & 1) ? 0x03020706u : 0x05040100u);
            CTW(6, G_WS_CTL + G_C_GV + ((w & (G_GVN - 1)) * 32 + ((lane & 31) ^ (2 * rot))) * 128); CTW(7, x * 65536 + (lane & 31) * 64); CTW(8, lane * 4); CTW(9, 131072);
            for (int k = 0; k < 16; ++k) { const int i = k ^ rot; CTW(10 + k, x * 65536 + aPX(i, (g >> 1) * 512 + (2 * (c >> 2) + (c & 1)) * 64 + (g & 1) * 32 + ((c >> 1) & 1) * 16)); CTW(26 + k, i * 2048 + lane * 16); }
            for (int k = 0; k < 4; ++k) CTW(42 + k, x * 65536 + aPM(k, lane * 16));
        }
        const int id = ix >= 25 ? ix - 25 : j;
#define ROLE(A_) asm volatile(A_ :: "s"(ws), "s"(ws), "s"(ws), "s"(w), "s"(id), "s"(ws), "s"(prm), "s"(lb), "s"(stb) : ASM_CLOB)
        if (ix >= 25) ROLE(ASM_H);
        else ROLE(ASM_R);
    }
    else if (x >= 4 && ix >= 24) {
        if (ix >= 25) for (int i = 0; i < ((FLG(ka->prm[5]) >> 5) & 7); ++i) __builtin_amdgcn_s_sleep(36);
        const char* xs = (const char*)ka->X + (size_t)((ix - 24) * 4 + (x & 3)) * 73728 + threadIdx.x * 128;
        const int f = (ix - 24) * 4 + (x & 3);
        const u16* ps = (w == 0 ? ka->logA : w == 1 ? ka->Aim : w == 2 ? ka->Cre : ka->Cim) + f * 2304 + min((int)(threadIdx.x & 63), 35) * 64;
        if (ix < 25 || !(FLG(ka->prm[5]) & 4))
        asm volatile("s_mov_b32 m0, %4\n\ts_nop 0\n\tglobal_load_lds_dword %0, off\n\tglobal_load_lds_dword %1, off\n\tglobal_load_lds_dword %2, off\n\tglobal_load_lds_dword %3, off"
                     :: "v"(xs), "v"(xs + 32768), "v"(xs + 65536 - w * 8192), "v"(ps), "s"(163072) : "memory");
        if (ix < 25) for (int i = 0; i < ((FLG(ka->prm[5]) >> 5) & 7); ++i) __builtin_amdgcn_s_sleep(36);
    }
    }
    asm volatile("" : "+s"(ka));
    MainP p;
    p.prm[5] = ka->prm[5]; p.prm[7] = ka->prm[7];
    p.U = ka->U; p.ws = ka->ws; p.logA = ka->logA; p.Aim = ka->Aim; p.Cre = ka->Cre; p.Cim = ka->Cim; p.logdt = ka->logdt; p.Dskip = ka->Dskip; p.out = ka->out;
    if (x >= 4 && ix >= 24) {
        const u32x4 sent = {0xffffffffu, 0xffffffffu, 0xffffffffu, 0xffffffffu};
        const int tid = w * 64 + lane_id(), h = (ix - 24) * 4 + (x & 3);
        task_ut(p, smem, h, w, lane_id(), [&] {
#pragma unroll 1
            for (int sg = NG1; sg < 64; ++sg) {
                const unsigned ok = G_D_KM + h * G_HS4 + skew(sg, h) * 4096 + tid * 16;
                const unsigned op = tid < 128 ? G_D_PX + (sg * 32 + h) * 2048 + tid * 16 : G_D_PM + sg * 16384 + ((tid >> 4) & 1) * 8192 + h * 256 + (tid & 15) * 16;
                asm volatile("global_store_dwordx4 %0, %2, %3 sc1\n\tglobal_store_dwordx4 %1, %2, %3 sc1\n\ts_nop 1" :: "v"(ok), "v"(op), "v"(sent), "s"(p.ws) : "memory");
            }
        });
    }
    {
        const int nq = x < 4 ? (w + ((FLH(p.prm[7]) >> 1) & 3) > 3 ? 0 : 1000) : ix < 8 ? (FLG(p.prm[5]) >> 8) & 3 : ix < 16 ? (FLG(p.prm[5]) >> 10) & 3 : (ix & 2) && ((31 - ix) >> 2) < NAG ? 0 : ix < 24 ? (ix < 16 + ((FLH(p.prm[7]) >> 10) & 15) ? (FLG(p.prm[5]) >> 12) & 3 : 0) : (FLG(p.prm[5]) >> 14) & 3;
        if (b == 239 && w == 0) watch_role(p, lane_id());
        else if (nq || b == 239) {
            for (int i = 0; i < (int)(((ix & 7) * 4 + w) * ((FLG(p.prm[5]) >> 23) & 7)); ++i) __builtin_amdgcn_s_sleep(3);
            s4d_wave(p, smem, w, lane_id(), nq == 3 || b == 239 ? 1000 : nq);
        }
        if (x < 4) return;
        __syncthreads();
    }
    if ((ix & 18) == 18 && ((31 - ix) >> 2) < NAG) { attn_role(p, ((31 - ix) >> 2) + (ix < 24 ? NA : 0), (x & 3) * 8 + (ix & 1) * 4 + w, w, lane_id());
        if (b == 255) {
            wait_word(p, p.ws + G_WS_CTL + G_C_ARR, 255, 50u);
            if (w == 0 && lane_id() == 0) __hip_atomic_fetch_add((gu32*)(p.ws + G_WS_EP), 1u, RLX_AGENT);
        }
        __syncthreads();
    }
    p.prm[0] = near_mask(p.ws, lane_id());
    worker_body(p, smem, w, lane_id());
}

extern "C" __global__ void code_end_marker() {}

static constexpr int g_prm[8] = {PRM_DEFAULT};

extern "C" void kernel_launch(void* const* d_in, const int* in_sizes, int n_in, void* d_out, int out_size,
                              void* d_ws, size_t ws_size, hipStream_t stream) {
    char* ws = (char*)(((uintptr_t)d_ws + 0x1FFFFF) & ~(uintptr_t)0x1FFFFF);
    MainP p;
    for (int i = 0; i < 8; ++i) p.prm[i] = g_prm[i];
    p.mem0 = (const u16*)d_in[1];
    for (int i = 0; i < 4; ++i) p.W[i] = (const u16*)d_in[2 + i];
    p.X = (const u16*)d_in[0]; p.U = (const u16*)d_in[7]; p.ws = ws;
    p.logA = (const u16*)d_in[8]; p.Aim = (const u16*)d_in[9]; p.Cre = (const u16*)d_in[10]; p.Cim = (const u16*)d_in[11];
    p.logdt = (const u16*)d_in[12]; p.Dskip = (const u16*)d_in[13]; p.out = (u16*)d_out;
    main_kernel<<<256, 256, 0, stream>>>(p);
}
```

```cpp
#include <hip/hip_runtime.h>
#include <hip/hip_bf16.h>
#include <stdint.h>
#include <stddef.h>

typedef __attribute__((ext_vector_type(8))) __bf16 bf16x8;
typedef __attribute__((ext_vector_type(4))) short s16x4;
typedef __attribute__((ext_vector_type(4))) float f32x4;
typedef __attribute__((ext_vector_type(4))) unsigned u32x4;
typedef __attribute__((ext_vector_type(2))) unsigned u32x2;
typedef __attribute__((ext_vector_type(2))) float f32x2;
typedef __attribute__((ext_vector_type(16))) unsigned u32x16;
typedef unsigned short u16;
typedef unsigned long long u64;

#define GAS __attribute__((address_space(1)))
typedef GAS unsigned gu32;
#define RLX_AGENT __ATOMIC_RELAXED, __HIP_MEMORY_SCOPE_AGENT

static constexpr int DM = 576, NH = 32, DH = 128, MM = 8, SEG = 32, NSEG = 64, SL = 2048;
static constexpr int HD = NH * DH;
static constexpr int OUTW = 2 * DM;
static constexpr int NST = 128;

__device__ __forceinline__ float bf2f(u16 h) { return __uint_as_float(((unsigned)h) << 16); }
typedef __attribute__((ext_vector_type(2))) __bf16 bf16x2_t;
__device__ __forceinline__ unsigned pk2bf(float a, float b) {
    bf16x2_t v; v[0] = (__bf16)a; v[1] = (__bf16)b;
    return __builtin_bit_cast(unsigned, v);
}
__device__ __forceinline__ float bfr(float f) { return __uint_as_float(pk2bf(f, f) << 16); }
__device__ __forceinline__ float lo2f(unsigned u) { return __uint_as_float(u << 16); }
__device__ __forceinline__ float hi2f(unsigned u) { return __uint_as_float(u & 0xffff0000u); }
__device__ __forceinline__ float rowmax4(float x) {
    auto a = __builtin_amdgcn_permlane16_swap(__float_as_uint(x), __float_as_uint(x), false, false);
    x = fmaxf(__uint_as_float(a[0]), __uint_as_float(a[1]));
    auto b = __builtin_amdgcn_permlane32_swap(__float_as_uint(x), __float_as_uint(x), false, false);
    return fmaxf(__uint_as_float(b[0]), __uint_as_float(b[1]));
}
__device__ __forceinline__ float rowsum4(float x) {
    auto a = __builtin_amdgcn_permlane16_swap(__float_as_uint(x), __float_as_uint(x), false, false);
    x = __uint_as_float(a[0]) + __uint_as_float(a[1]);
    auto b = __builtin_amdgcn_permlane32_swap(__float_as_uint(x), __float_as_uint(x), false, false);
    return __uint_as_float(b[0]) + __uint_as_float(b[1]);
}

struct MainP {
    int prm[8];
    const u16* mem0; const u16* W[4]; const u16* X; const u16* U;
    char* ws;
    const u16* logA; const u16* Aim; const u16* Cre; const u16* Cim; const u16* logdt; const u16* Dskip;
    u16* out;
};
static constexpr int LDS_TOTAL = 163840;
static constexpr unsigned kWaitCap = 1u << 18;
#define VMC0() asm volatile("s_waitcnt vmcnt(0)" ::: "memory")
#define PRM_DEFAULT 1050, 600, 120, 675, 760, 1300656171, 3145992, 3779
#define ASM_QK \
    "s_load_dwordx2 s[42:43], %3, 40\n\t" \
    "s_load_dwordx2 s[44:45], %3, 48\n\t" \
    "s_load_dwordx2 s[36:37], %3, 0x58\n\t" \
    "s_load_dwordx2 s[50:51], %3, 0x48\n\t" \
    "s_load_dwordx8 s[84:91], %3, 0\n\t" \
    "s_mov_b32 s66, %4\n\t" \
    "s_mov_b32 s67, %5\n\t" \
    "s_mov_b32 s68, 262144\n\t" \
    "s_mov_b32 s72, 0\n\t" \
    "s_mov_b32 s83, -1\n\t" \
    "s_mov_b32 s65, 0\n\t" \
    "s_lshl_b32 s82, s66, 10\n\t" \
    "s_waitcnt lgkmcnt(0)\n\t" \
    "s_add_u32 s46, s42, 2097152\n\t" \
    "s_addc_u32 s47, s43, 0\n\t" \
    "s_add_u32 m0, s82, 65536\n\t" \
    "s_nop 0\n\t" \
    "global_load_lds_dwordx4 v200, s[46:47]\n\t" \
    "s_add_u32 s46, s42, 2228224\n\t" \
    "s_addc_u32 s47, s43, 0\n\t" \
    "s_add_u32 m0, s82, 69632\n\t" \
    "s_nop 0\n\t" \
    "global_load_lds_dwordx4 v200, s[46:47]\n\t" \
    "s_add_u32 s46, s42, 2359296\n\t" \
    "s_addc_u32 s47, s43, 0\n\t" \
    "s_add_u32 m0, s82, 73728\n\t" \
    "s_nop 0\n\t" \
    "global_load_lds_dwordx4 v200, s[46:47]\n\t" \
    "s_add_u32 s46, s42, 2490368\n\t" \
    "s_addc_u32 s47, s43, 0\n\t" \
    "s_add_u32 m0, s82, 77824\n\t" \
    "s_nop 0\n\t" \
    "global_load_lds_dwordx4 v200, s[46:47]\n\t" \
    "s_add_u32 s46, s42, 2621440\n\t" \
    "s_addc_u32 s47, s43, 0\n\t" \
    "s_add_u32 m0, s82, 81920\n\t" \
    "s_nop 0\n\t" \
    "global_load_lds_dwordx4 v200, s[46:47]\n\t" \
    "s_add_u32 s46, s42, 2752512\n\t" \
    "s_addc_u32 s47, s43, 0\n\t" \
    "s_add_u32 m0, s82, 86016\n\t" \
    "s_nop 0\n\t" \
    "global_load_lds_dwordx4 v200, s[46:47]\n\t" \
    "s_add_u32 s46, s42, 2883584\n\t" \
    "s_addc_u32 s47, s43, 0\n\t" \
    "s_add_u32 m0, s82, 90112\n\t" \
    "s_nop 0\n\t" \
    "global_load_lds_dwordx4 v200, s[46:47]\n\t" \
    "s_add_u32 s46, s42, 3014656\n\t" \
    "s_addc_u32 s47, s43, 0\n\t" \
    "s_add_u32 m0, s82, 94208\n\t" \
    "s_nop 0\n\t" \
    "global_load_lds_dwordx4 v200, s[46:47]\n\t" \
    "s_add_u32 s46, s42, 3145728\n\t" \
    "s_addc_u32 s47, s43, 0\n\t" \
    "s_add_u32 m0, s82, 98304\n\t" \
    "s_nop 0\n\t" \
    "global_load_lds_dwordx4 v200, s[46:47]\n\t" \
    "s_add_u32 s46, s42, 3276800\n\t" \
    "s_addc_u32 s47, s43, 0\n\t" \
    "s_add_u32 m0, s82, 102400\n\t" \
    "s_nop 0\n\t" \
    "global_load_lds_dwordx4 v200, s[46:47]\n\t" \
    "s_add_u32 s46, s42, 3407872\n\t" \
    "s_addc_u32 s47, s43, 0\n\t" \
    "s_add_u32 m0, s82, 106496\n\t" \
    "s_nop 0\n\t" \
    "global_load_lds_dwordx4 v200, s[46:47]\n\t" \
    "s_add_u32 s46, s42, 3538944\n\t" \
    "s_addc_u32 s47, s43, 0\n\t" \
    "s_add_u32 m0, s82, 110592\n\t" \
    "s_nop 0\n\t" \
    "global_load_lds_dwordx4 v200, s[46:47]\n\t" \
    "s_add_u32 s46, s42, 3670016\n\t" \
    "s_addc_u32 s47, s43, 0\n\t" \
    "s_add_u32 m0, s82, 114688\n\t" \
    "s_nop 0\n\t" \
    "global_load_lds_dwordx4 v200, s[46:47]\n\t" \
    "s_add_u32 s46, s42, 3801088\n\t" \
    "s_addc_u32 s47, s43, 0\n\t" \
    "s_add_u32 m0, s82, 118784\n\t" \
    "s_nop 0\n\t" \
    "global_load_lds_dwordx4 v200, s[46:47]\n\t" \
    "s_add_u32 s46, s42, 3932160\n\t" \
    "s_addc_u32 s47, s43, 0\n\t" \
    "s_add_u32 m0, s82, 122880\n\t" \
    "s_nop 0\n\t" \
    "global_load_lds_dwordx4 v200, s[46:47]\n\t" \
    "s_add_u32 s46, s42, 4063232\n\t" \
    "s_addc_u32 s47, s43, 0\n\t" \
    "s_add_u32 m0, s82, 126976\n\t" \
    "s_nop 0\n\t" \
    "global_load_lds_dwordx4 v200, s[46:47]\n\t" \
    "s_add_u32 s46, s42, 4194304\n\t" \
    "s_addc_u32 s47, s43, 0\n\t" \
    "s_add_u32 m0, s82, 131072\n\t" \
    "s_nop 0\n\t" \
    "global_load_lds_dwordx4 v200, s[46:47]\n\t" \
    "s_add_u32 s46, s42, 4325376\n\t" \
    "s_addc_u32 s47, s43, 0\n\t" \
    "s_add_u32 m0, s82, 135168\n\t" \
    "s_nop 0\n\t" \
    "global_load_lds_dwordx4 v200, s[46:47]\n\t" \
    "s_add_u32 s46, s42, 4456448\n\t" \
    "s_addc_u32 s47, s43, 0\n\t" \
    "s_add_u32 m0, s82, 139264\n\t" \
    "s_nop 0\n\t" \
    "global_load_lds_dwordx4 v200, s[46:47]\n\t" \
    "s_add_u32 s46, s42, 4587520\n\t" \
    "s_addc_u32 s47, s43, 0\n\t" \
    "s_add_u32 m0, s82, 143360\n\t" \
    "s_nop 0\n\t" \
    "global_load_lds_dwordx4 v200, s[46:47]\n\t" \
    "s_mov_b32 s77, 3019898880\n\t" \
    "s_mov_b32 s78, 3054198966\n\t" \
    "s_waitcnt lgkmcnt(0)\n\t" \
    "s_mov_b32 s70, 116\n\t" \
    "qk_E1_%=:\n\t" \
    "s_memtime s[56:57]\n\t" \
    "s_memrealtime s[2:3]\n\t" \
    "s_nop 0\n\t" \
    "s_memrealtime s[4:5]\n\t" \
    "s_nop 0\n\t" \
    "s_memrealtime s[6:7]\n\t" \
    "s_nop 0\n\t" \
    "s_memrealtime s[8:9]\n\t" \
    "s_nop 0\n\t" \
    "s_memrealtime s[10:11]\n\t" \
    "s_nop 0\n\t" \
    "s_memrealtime s[12:13]\n\t" \
    "s_nop 0\n\t" \
    "s_memrealtime s[14:15]\n\t" \
    "s_nop 0\n\t" \
    "s_memrealtime s[16:17]\n\t" \
    "s_nop 0\n\t" \
    "s_memrealtime s[18:19]\n\t" \
    "s_nop 0\n\t" \
    "s_memrealtime s[20:21]\n\t" \
    "s_nop 0\n\t" \
    "s_memrealtime s[22:23]\n\t" \
    "s_nop 0\n\t" \
    "s_memrealtime s[24:25]\n\t" \
    "s_nop 0\n\t" \
    "s_memrealtime s[26:27]\n\t" \
    "s_nop 0\n\t" \
    "s_memrealtime s[28:29]\n\t" \
    "s_memtime s[54:55]\n\t" \
    "s_add_u32 s70, s70, 8\n\t" \
    "s_waitcnt lgkmcnt(0)\n\t" \
    "s_sub_u32 s69, s54, s56\n\t" \
    "s_cmp_gt_u32 s69, s70\n\t" \
    "s_cbranch_scc1 qk_E1_%=\n\t" \
    "s_add_u32 s2, s2, s4\n\t" \
    "s_add_u32 s6, s6, s8\n\t" \
    "s_add_u32 s10, s10, s12\n\t" \
    "s_add_u32 s14, s14, s16\n\t" \
    "s_add_u32 s18, s18, s20\n\t" \
    "s_add_u32 s22, s22, s24\n\t" \
    "s_add_u32 s2, s2, s6\n\t" \
    "s_add_u32 s10, s10, s14\n\t" \
    "s_add_u32 s18, s18, s22\n\t" \
    "s_add_u32 s2, s2, s26\n\t" \
    "s_add_u32 s10, s10, s18\n\t" \
    "s_add_u32 s2, s2, s10\n\t" \
    "s_mul_i32 s70, s28, 13\n\t" \
    "s_sub_u32 s70, s70, s2\n\t" \
    "s_min_u32 s70, s70, 52\n\t" \
    "s_mul_i32 s69, s69, s70\n\t" \
    "s_mul_hi_u32 s69, s69, 76695844\n\t" \
    "s_mov_b32 s75, s73\n\t" \
    "s_mov_b32 s76, s74\n\t" \
    "s_add_u32 s73, s56, s69\n\t" \
    "s_lshl_b32 s74, s28, 4\n\t" \
    "s_waitcnt vmcnt(32) lgkmcnt(0)\n\t" \
    "s_barrier\n\t" \
    "ds_read_b64_tr_b16 a[0:1], v201 offset:0\n\t" \
    "ds_read_b64_tr_b16 a[2:3], v201 offset:1024\n\t" \
    "ds_read_b64_tr_b16 a[4:5], v201 offset:8192\n\t" \
    "ds_read_b64_tr_b16 a[6:7], v201 offset:9216\n\t" \
    "ds_read_b64_tr_b16 a[72:73], v202 offset:0\n\t" \
    "ds_read_b64_tr_b16 a[74:75], v202 offset:1024\n\t" \
    "ds_read_b64_tr_b16 a[76:77], v202 offset:8192\n\t" \
    "ds_read_b64_tr_b16 a[78:79], v202 offset:9216\n\t" \
    "s_waitcnt vmcnt(28) lgkmcnt(0)\n\t" \
    "s_barrier\n\t" \
    "ds_read_b64_tr_b16 a[8:9], v201 offset:16384\n\t" \
    "ds_read_b64_tr_b16 a[10:11], v201 offset:17408\n\t" \
    "ds_read_b64_tr_b16 a[12:13], v201 offset:24576\n\t" \
    "ds_read_b64_tr_b16 a[14:15], v201 offset:25600\n\t" \
    "ds_read_b64_tr_b16 a[80:81], v202 offset:16384\n\t" \
    "ds_read_b64_tr_b16 a[82:83], v202 offset:17408\n\t" \
    "ds_read_b64_tr_b16 a[84:85], v202 offset:24576\n\t" \
    "ds_read_b64_tr_b16 a[86:87], v202 offset:25600\n\t" \
    "s_add_u32 s46, s44, 0\n\t" \
    "s_addc_u32 s47, s45, 0\n\t" \
    "s_add_u32 m0, s82, 0\n\t" \
    "s_nop 0\n\t" \
    "global_load_lds_dwordx4 v200, s[46:47]\n\t" \
    "s_add_u32 s46, s44, 131072\n\t" \
    "s_addc_u32 s47, s45, 0\n\t" \
    "s_add_u32 m0, s82, 4096\n\t" \
    "s_nop 0\n\t" \
    "global_load_lds_dwordx4 v200, s[46:47]\n\t" \
    "s_add_u32 s46, s44, 262144\n\t" \
    "s_addc_u32 s47, s45, 0\n\t" \
    "s_add_u32 m0, s82, 8192\n\t" \
    "s_nop 0\n\t" \
    "global_load_lds_dwordx4 v200, s[46:47]\n\t" \
    "s_add_u32 s46, s44, 393216\n\t" \
    "s_addc_u32 s47, s45, 0\n\t" \
    "s_add_u32 m0, s82, 12288\n\t" \
    "s_nop 0\n\t" \
    "global_load_lds_dwordx4 v200, s[46:47]\n\t" \
    "s_waitcnt vmcnt(28) lgkmcnt(0)\n\t" \
    "s_barrier\n\t" \
    "ds_read_b64_tr_b16 a[16:17], v201 offset:32768\n\t" \
    "ds_read_b64_tr_b16 a[18:19], v201 offset:33792\n\t" \
    "ds_read_b64_tr_b16 a[20:21], v201 offset:40960\n\t" \
    "ds_read_b64_tr_b16 a[22:23], v201 offset:41984\n\t" \
    "ds_read_b64_tr_b16 a[88:89], v202 offset:32768\n\t" \
    "ds_read_b64_tr_b16 a[90:91], v202 offset:33792\n\t" \
    "ds_read_b64_tr_b16 a[92:93], v202 offset:40960\n\t" \
    "ds_read_b64_tr_b16 a[94:95], v202 offset:41984\n\t" \
    "s_add_u32 s46, s44, 524288\n\t" \
    "s_addc_u32 s47, s45, 0\n\t" \
    "s_add_u32 m0, s82, 16384\n\t" \
    "s_nop 0\n\t" \
    "global_load_lds_dwordx4 v200, s[46:47]\n\t" \
    "s_add_u32 s46, s44, 655360\n\t" \
    "s_addc_u32 s47, s45, 0\n\t" \
    "s_add_u32 m0, s82, 20480\n\t" \
    "s_nop 0\n\t" \
    "global_load_lds_dwordx4 v200, s[46:47]\n\t" \
    "s_add_u32 s46, s44, 786432\n\t" \
    "s_addc_u32 s47, s45, 0\n\t" \
    "s_add_u32 m0, s82, 24576\n\t" \
    "s_nop 0\n\t" \
    "global_load_lds_dwordx4 v200, s[46:47]\n\t" \
    "s_add_u32 s46, s44, 917504\n\t" \
    "s_addc_u32 s47, s45, 0\n\t" \
    "s_add_u32 m0, s82, 28672\n\t" \
    "s_nop 0\n\t" \
    "global_load_lds_dwordx4 v200, s[46:47]\n\t" \
    "s_waitcnt vmcnt(28) lgkmcnt(0)\n\t" \
    "s_barrier\n\t" \
    "ds_read_b64_tr_b16 a[24:25], v201 offset:49152\n\t" \
    "ds_read_b64_tr_b16 a[26:27], v201 offset:50176\n\t" \
    "ds_read_b64_tr_b16 a[28:29], v201 offset:57344\n\t" \
    "ds_read_b64_tr_b16 a[30:31], v201 offset:58368\n\t" \
    "ds_read_b64_tr_b16 a[96:97], v202 offset:49152\n\t" \
    "ds_read_b64_tr_b16 a[98:99], v202 offset:50176\n\t" \
    "ds_read_b64_tr_b16 a[100:101], v202 offset:57344\n\t" \
    "ds_read_b64_tr_b16 a[102:103], v202 offset:58368\n\t" \
    "s_add_u32 s46, s44, 1048576\n\t" \
    "s_addc_u32 s47, s45, 0\n\t" \
    "s_add_u32 m0, s82, 32768\n\t" \
    "s_nop 0\n\t" \
    "global_load_lds_dwordx4 v200, s[46:47]\n\t" \
    "s_add_u32 s46, s44, 1179648\n\t" \
    "s_addc_u32 s47, s45, 0\n\t" \
    "s_add_u32 m0, s82, 36864\n\t" \
    "s_nop 0\n\t" \
    "global_load_lds_dwordx4 v200, s[46:47]\n\t" \
    "s_add_u32 s46, s44, 1310720\n\t" \
    "s_addc_u32 s47, s45, 0\n\t" \
    "s_add_u32 m0, s82, 40960\n\t" \
    "s_nop 0\n\t" \
    "global_load_lds_dwordx4 v200, s[46:47]\n\t" \
    "s_add_u32 s46, s44, 1441792\n\t" \
    "s_addc_u32 s47, s45, 0\n\t" \
    "s_add_u32 m0, s82, 45056\n\t" \
    "s_nop 0\n\t" \
    "global_load_lds_dwordx4 v200, s[46:47]\n\t" \
    "s_waitcnt vmcnt(28) lgkmcnt(0)\n\t" \
    "s_barrier\n\t" \
    "v_add_u32 v201, 0x10000, v201\n\t" \
    "v_add_u32 v202, 0x10000, v202\n\t" \
    "ds_read_b64_tr_b16 a[32:33], v201 offset:0\n\t" \
    "ds_read_b64_tr_b16 a[34:35], v201 offset:1024\n\t" \
    "ds_read_b64_tr_b16 a[36:37], v201 offset:8192\n\t" \
    "ds_read_b64_tr_b16 a[38:39], v201 offset:9216\n\t" \
    "ds_read_b64_tr_b16 a[104:105], v202 offset:0\n\t" \
    "ds_read_b64_tr_b16 a[106:107], v202 offset:1024\n\t" \
    "ds_read_b64_tr_b16 a[108:109], v202 offset:8192\n\t" \
    "ds_read_b64_tr_b16 a[110:111], v202 offset:9216\n\t" \
    "s_add_u32 s46, s44, 1572864\n\t" \
    "s_addc_u32 s47, s45, 0\n\t" \
    "s_add_u32 m0, s82, 49152\n\t" \
    "s_nop 0\n\t" \
    "global_load_lds_dwordx4 v200, s[46:47]\n\t" \
    "s_add_u32 s46, s44, 1703936\n\t" \
    "s_addc_u32 s47, s45, 0\n\t" \
    "s_add_u32 m0, s82, 53248\n\t" \
    "s_nop 0\n\t" \
    "global_load_lds_dwordx4 v200, s[46:47]\n\t" \
    "s_add_u32 s46, s44, 1835008\n\t" \
    "s_addc_u32 s47, s45, 0\n\t" \
    "s_add_u32 m0, s82, 57344\n\t" \
    "s_nop 0\n\t" \
    "global_load_lds_dwordx4 v200, s[46:47]\n\t" \
    "s_add_u32 s46, s44, 1966080\n\t" \
    "s_addc_u32 s47, s45, 0\n\t" \
    "s_add_u32 m0, s82, 61440\n\t" \
    "s_nop 0\n\t" \
    "global_load_lds_dwordx4 v200, s[46:47]\n\t" \
    "s_waitcnt vmcnt(28) lgkmcnt(0)\n\t" \
    "s_barrier\n\t" \
    "ds_read_b64_tr_b16 a[40:41], v201 offset:16384\n\t" \
    "ds_read_b64_tr_b16 a[42:43], v201 offset:17408\n\t" \
    "ds_read_b64_tr_b16 a[44:45], v201 offset:24576\n\t" \
    "ds_read_b64_tr_b16 a[46:47], v201 offset:25600\n\t" \
    "ds_read_b64_tr_b16 a[112:113], v202 offset:16384\n\t" \
    "ds_read_b64_tr_b16 a[114:115], v202 offset:17408\n\t" \
    "ds_read_b64_tr_b16 a[116:117], v202 offset:24576\n\t" \
    "ds_read_b64_tr_b16 a[118:119], v202 offset:25600\n\t" \
    "s_add_u32 s46, s44, 2097152\n\t" \
    "s_addc_u32 s47, s45, 0\n\t" \
    "s_add_u32 m0, s82, 65536\n\t" \
    "s_nop 0\n\t" \
    "global_load_lds_dwordx4 v200, s[46:47]\n\t" \
    "s_add_u32 s46, s44, 2228224\n\t" \
    "s_addc_u32 s47, s45, 0\n\t" \
    "s_add_u32 m0, s82, 69632\n\t" \
    "s_nop 0\n\t" \
    "global_load_lds_dwordx4 v200, s[46:47]\n\t" \
    "s_add_u32 s46, s44, 2359296\n\t" \
    "s_addc_u32 s47, s45, 0\n\t" \
    "s_add_u32 m0, s82, 73728\n\t" \
    "s_nop 0\n\t" \
    "global_load_lds_dwordx4 v200, s[46:47]\n\t" \
    "s_add_u32 s46, s44, 2490368\n\t" \
    "s_addc_u32 s47, s45, 0\n\t" \
    "s_add_u32 m0, s82, 77824\n\t" \
    "s_nop 0\n\t" \
    "global_load_lds_dwordx4 v200, s[46:47]\n\t" \
    "s_waitcnt vmcnt(28) lgkmcnt(0)\n\t" \
    "s_barrier\n\t" \
    "ds_read_b64_tr_b16 a[48:49], v201 offset:32768\n\t" \
    "ds_read_b64_tr_b16 a[50:51], v201 offset:33792\n\t" \
    "ds_read_b64_tr_b16 a[52:53], v201 offset:40960\n\t" \
    "ds_read_b64_tr_b16 a[54:55], v201 offset:41984\n\t" \
    "ds_read_b64_tr_b16 a[120:121], v202 offset:32768\n\t" \
    "ds_read_b64_tr_b16 a[122:123], v202 offset:33792\n\t" \
    "ds_read_b64_tr_b16 a[124:125], v202 offset:40960\n\t" \
    "ds_read_b64_tr_b16 a[126:127], v202 offset:41984\n\t" \
    "s_add_u32 s46, s44, 2621440\n\t" \
    "s_addc_u32 s47, s45, 0\n\t" \
    "s_add_u32 m0, s82, 81920\n\t" \
    "s_nop 0\n\t" \
    "global_load_lds_dwordx4 v200, s[46:47]\n\t" \
    "s_add_u32 s46, s44, 2752512\n\t" \
    "s_addc_u32 s47, s45, 0\n\t" \
    "s_add_u32 m0, s82, 86016\n\t" \
    "s_nop 0\n\t" \
    "global_load_lds_dwordx4 v200, s[46:47]\n\t" \
    "s_add_u32 s46, s44, 2883584\n\t" \
    "s_addc_u32 s47, s45, 0\n\t" \
    "s_add_u32 m0, s82, 90112\n\t" \
    "s_nop 0\n\t" \
    "global_load_lds_dwordx4 v200, s[46:47]\n\t" \
    "s_add_u32 s46, s44, 3014656\n\t" \
    "s_addc_u32 s47, s45, 0\n\t" \
    "s_add_u32 m0, s82, 94208\n\t" \
    "s_nop 0\n\t" \
    "global_load_lds_dwordx4 v200, s[46:47]\n\t" \
    "s_waitcnt vmcnt(28) lgkmcnt(0)\n\t" \
    "s_barrier\n\t" \
    "ds_read_b64_tr_b16 a[56:57], v201 offset:49152\n\t" \
    "ds_read_b64_tr_b16 a[58:59], v201 offset:50176\n\t" \
    "ds_read_b64_tr_b16 a[60:61], v201 offset:57344\n\t" \
    "ds_read_b64_tr_b16 a[62:63], v201 offset:58368\n\t" \
    "ds_read_b64_tr_b16 a[128:129], v202 offset:49152\n\t" \
    "ds_read_b64_tr_b16 a[130:131], v202 offset:50176\n\t" \
    "ds_read_b64_tr_b16 a[132:133], v202 offset:57344\n\t" \
    "ds_read_b64_tr_b16 a[134:135], v202 offset:58368\n\t" \
    "s_add_u32 s46, s44, 3145728\n\t" \
    "s_addc_u32 s47, s45, 0\n\t" \
    "s_add_u32 m0, s82, 98304\n\t" \
    "s_nop 0\n\t" \
    "global_load_lds_dwordx4 v200, s[46:47]\n\t" \
    "s_add_u32 s46, s44, 3276800\n\t" \
    "s_addc_u32 s47, s45, 0\n\t" \
    "s_add_u32 m0, s82, 102400\n\t" \
    "s_nop 0\n\t" \
    "global_load_lds_dwordx4 v200, s[46:47]\n\t" \
    "s_add_u32 s46, s44, 3407872\n\t" \
    "s_addc_u32 s47, s45, 0\n\t" \
    "s_add_u32 m0, s82, 106496\n\t" \
    "s_nop 0\n\t" \
    "global_load_lds_dwordx4 v200, s[46:47]\n\t" \
    "s_add_u32 s46, s44, 3538944\n\t" \
    "s_addc_u32 s47, s45, 0\n\t" \
    "s_add_u32 m0, s82, 110592\n\t" \
    "s_nop 0\n\t" \
    "global_load_lds_dwordx4 v200, s[46:47]\n\t" \
    "v_mbcnt_lo_u32_b32 v160, -1, 0\n\t" \
    "v_mbcnt_hi_u32_b32 v160, -1, v160\n\t" \
    "v_lshlrev_b32 v160, 6, v160\n\t" \
    "v_add_u32 v160, 6225920, v160\n\t" \
    "v_mov_b32 v161, 6234112\n\t" \
    "global_load_dword v161, v161, s[36:37] sc1\n\t" \
    "global_load_dwordx2 v[162:163], v160, s[36:37] sc1\n\t" \
    "global_load_dword v164, v160, s[36:37] offset:2048 sc1\n\t" \
    "s_waitcnt vmcnt(31) lgkmcnt(0)\n\t" \
    "s_barrier\n\t" \
    "v_add_u32 v201, 0x10000, v201\n\t" \
    "v_add_u32 v202, 0x10000, v202\n\t" \
    "ds_read_b64_tr_b16 a[64:65], v201 offset:0\n\t" \
    "ds_read_b64_tr_b16 a[66:67], v201 offset:1024\n\t" \
    "ds_read_b64_tr_b16 a[68:69], v201 offset:8192\n\t" \
    "ds_read_b64_tr_b16 a[70:71], v201 offset:9216\n\t" \
    "ds_read_b64_tr_b16 a[136:137], v202 offset:0\n\t" \
    "ds_read_b64_tr_b16 a[138:139], v202 offset:1024\n\t" \
    "ds_read_b64_tr_b16 a[140:141], v202 offset:8192\n\t" \
    "ds_read_b64_tr_b16 a[142:143], v202 offset:9216\n\t" \
    "v_add_u32 v201, 0xfffe0000, v201\n\t" \
    "v_add_u32 v202, 0xfffe0000, v202\n\t" \
    "s_add_u32 s46, s44, 3670016\n\t" \
    "s_addc_u32 s47, s45, 0\n\t" \
    "s_add_u32 m0, s82, 114688\n\t" \
    "s_nop 0\n\t" \
    "global_load_lds_dwordx4 v200, s[46:47]\n\t" \
    "s_add_u32 s46, s44, 3801088\n\t" \
    "s_addc_u32 s47, s45, 0\n\t" \
    "s_add_u32 m0, s82, 118784\n\t" \
    "s_nop 0\n\t" \
    "global_load_lds_dwordx4 v200, s[46:47]\n\t" \
    "s_add_u32 s46, s44, 3932160\n\t" \
    "s_addc_u32 s47, s45, 0\n\t" \
    "s_add_u32 m0, s82, 122880\n\t" \
    "s_nop 0\n\t" \
    "global_load_lds_dwordx4 v200, s[46:47]\n\t" \
    "s_add_u32 s46, s44, 4063232\n\t" \
    "s_addc_u32 s47, s45, 0\n\t" \
    "s_add_u32 m0, s82, 126976\n\t" \
    "s_nop 0\n\t" \
    "global_load_lds_dwordx4 v200, s[46:47]\n\t" \
    "s_waitcnt lgkmcnt(0)\n\t" \
    "s_barrier\n\t" \
    "s_add_u32 s46, s44, 4194304\n\t" \
    "s_addc_u32 s47, s45, 0\n\t" \
    "s_add_u32 m0, s82, 131072\n\t" \
    "s_nop 0\n\t" \
    "global_load_lds_dwordx4 v200, s[46:47]\n\t" \
    "s_add_u32 s46, s44, 4325376\n\t" \
    "s_addc_u32 s47, s45, 0\n\t" \
    "s_add_u32 m0, s82, 135168\n\t" \
    "s_nop 0\n\t" \
    "global_load_lds_dwordx4 v200, s[46:47]\n\t" \
    "s_add_u32 s46, s44, 4456448\n\t" \
    "s_addc_u32 s47, s45, 0\n\t" \
    "s_add_u32 m0, s82, 139264\n\t" \
    "s_nop 0\n\t" \
    "global_load_lds_dwordx4 v200, s[46:47]\n\t" \
    "s_add_u32 s46, s44, 4587520\n\t" \
    "s_addc_u32 s47, s45, 0\n\t" \
    "s_add_u32 m0, s82, 143360\n\t" \
    "s_nop 0\n\t" \
    "global_load_lds_dwordx4 v200, s[46:47]\n\t" \
    "s_waitcnt vmcnt(35) lgkmcnt(0)\n\t" \
    "s_barrier\n\t" \
    "ds_read_b64_tr_b16 a[144:145], v201 offset:0\n\t" \
    "ds_read_b64_tr_b16 a[146:147], v201 offset:1024\n\t" \
    "ds_read_b64_tr_b16 a[148:149], v201 offset:8192\n\t" \
    "ds_read_b64_tr_b16 a[150:151], v201 offset:9216\n\t" \
    "ds_read_b64_tr_b16 a[216:217], v202 offset:0\n\t" \
    "ds_read_b64_tr_b16 a[218:219], v202 offset:1024\n\t" \
    "ds_read_b64_tr_b16 a[220:221], v202 offset:8192\n\t" \
    "ds_read_b64_tr_b16 a[222:223], v202 offset:9216\n\t" \
    "s_waitcnt vmcnt(31) lgkmcnt(0)\n\t" \
    "s_barrier\n\t" \
    "ds_read_b64_tr_b16 a[152:153], v201 offset:16384\n\t" \
    "ds_read_b64_tr_b16 a[154:155], v201 offset:17408\n\t" \
    "ds_read_b64_tr_b16 a[156:157], v201 offset:24576\n\t" \
    "ds_read_b64_tr_b16 a[158:159], v201 offset:25600\n\t" \
    "ds_read_b64_tr_b16 a[224:225], v202 offset:16384\n\t" \
    "ds_read_b64_tr_b16 a[226:227], v202 offset:17408\n\t" \
    "ds_read_b64_tr_b16 a[228:229], v202 offset:24576\n\t" \
    "ds_read_b64_tr_b16 a[230:231], v202 offset:25600\n\t" \
    "s_waitcnt vmcnt(27) lgkmcnt(0)\n\t" \
    "s_barrier\n\t" \
    "ds_read_b64_tr_b16 a[160:161], v201 offset:32768\n\t" \
    "ds_read_b64_tr_b16 a[162:163], v201 offset:33792\n\t" \
    "ds_read_b64_tr_b16 a[164:165], v201 offset:40960\n\t" \
    "ds_read_b64_tr_b16 a[166:167], v201 offset:41984\n\t" \
    "ds_read_b64_tr_b16 a[232:233], v202 offset:32768\n\t" \
    "ds_read_b64_tr_b16 a[234:235], v202 offset:33792\n\t" \
    "ds_read_b64_tr_b16 a[236:237], v202 offset:40960\n\t" \
    "ds_read_b64_tr_b16 a[238:239], v202 offset:41984\n\t" \
    "s_waitcnt vmcnt(23) lgkmcnt(0)\n\t" \
    "s_barrier\n\t" \
    "ds_read_b64_tr_b16 a[168:169], v201 offset:49152\n\t" \
    "ds_read_b64_tr_b16 a[170:171], v201 offset:50176\n\t" \
    "ds_read_b64_tr_b16 a[172:173], v201 offset:57344\n\t" \
    "ds_read_b64_tr_b16 a[174:175], v201 offset:58368\n\t" \
    "ds_read_b64_tr_b16 a[240:241], v202 offset:49152\n\t" \
    "ds_read_b64_tr_b16 a[242:243], v202 offset:50176\n\t" \
    "ds_read_b64_tr_b16 a[244:245], v202 offset:57344\n\t" \
    "ds_read_b64_tr_b16 a[246:247], v202 offset:58368\n\t" \
    "s_waitcnt vmcnt(19) lgkmcnt(0)\n\t" \
    "s_barrier\n\t" \
    "v_add_u32 v201, 0x10000, v201\n\t" \
    "v_add_u32 v202, 0x10000, v202\n\t" \
    "ds_read_b64_tr_b16 a[176:177], v201 offset:0\n\t" \
    "ds_read_b64_tr_b16 a[178:179], v201 offset:1024\n\t" \
    "ds_read_b64_tr_b16 a[180:181], v201 offset:8192\n\t" \
    "ds_read_b64_tr_b16 a[182:183], v201 offset:9216\n\t" \
    "ds_read_b64_tr_b16 a[248:249], v202 offset:0\n\t" \
    "ds_read_b64_tr_b16 a[250:251], v202 offset:1024\n\t" \
    "ds_read_b64_tr_b16 a[252:253], v202 offset:8192\n\t" \
    "ds_read_b64_tr_b16 a[254:255], v202 offset:9216\n\t" \
    "s_waitcnt vmcnt(15) lgkmcnt(0)\n\t" \
    "s_barrier\n\t" \
    "ds_read_b64_tr_b16 a[184:185], v201 offset:16384\n\t" \
    "ds_read_b64_tr_b16 a[186:187], v201 offset:17408\n\t" \
    "ds_read_b64_tr_b16 a[188:189], v201 offset:24576\n\t" \
    "ds_read_b64_tr_b16 a[190:191], v201 offset:25600\n\t" \
    "ds_read_b64_tr_b16 v[224:225], v202 offset:16384\n\t" \
    "ds_read_b64_tr_b16 v[226:227], v202 offset:17408\n\t" \
    "ds_read_b64_tr_b16 v[228:229], v202 offset:24576\n\t" \
    "ds_read_b64_tr_b16 v[230:231], v202 offset:25600\n\t" \
    "s_waitcnt vmcnt(11) lgkmcnt(0)\n\t" \
    "s_barrier\n\t" \
    "ds_read_b64_tr_b16 a[192:193], v201 offset:32768\n\t" \
    "ds_read_b64_tr_b16 a[194:195], v201 offset:33792\n\t" \
    "ds_read_b64_tr_b16 a[196:197], v201 offset:40960\n\t" \
    "ds_read_b64_tr_b16 a[198:199], v201 offset:41984\n\t" \
    "ds_read_b64_tr_b16 v[232:233], v202 offset:32768\n\t" \
    "ds_read_b64_tr_b16 v[234:235], v202 offset:33792\n\t" \
    "ds_read_b64_tr_b16 v[236:237], v202 offset:40960\n\t" \
    "ds_read_b64_tr_b16 v[238:239], v202 offset:41984\n\t" \
    "s_waitcnt vmcnt(8)\n\t" \
    "qk_C2_%=:\n\t" \
    "v_cmp_ne_u32 s[54:55], v162, v164\n\t" \
    "v_sub_u32 v162, v162, v161\n\t" \
    "v_cmp_ne_u32 vcc, 1, v162\n\t" \
    "s_nop 1\n\t" \
    "s_or_b64 vcc, vcc, s[54:55]\n\t" \
    "s_cbranch_scc0 qk_Cok3_%=\n\t" \
    "s_cmp_eq_u32 s68, 0\n\t" \
    "s_cbranch_scc1 qk_F5_%=\n\t" \
    "s_sub_u32 s68, s68, 1\n\t" \
    "s_sleep 6\n\t" \
    "s_branch qk_Cag4_%=\n\t" \
    "qk_F5_%=:\n\t" \
    "v_mov_b32 v164, 5767424\n\t" \
    "v_mov_b32 v165, 4\n\t" \
    "global_store_dword v164, v165, s[36:37] sc1\n\t" \
    "s_waitcnt vmcnt(0)\n\t" \
    "s_branch qk_Cok3_%=\n\t" \
    "qk_Cag4_%=:\n\t" \
    "global_load_dwordx2 v[162:163], v160, s[36:37] sc1\n\t" \
    "global_load_dword v164, v160, s[36:37] offset:2048 sc1\n\t" \
    "s_waitcnt vmcnt(0)\n\t" \
    "s_branch qk_C2_%=\n\t" \
    "qk_Cok3_%=:\n\t" \
    "s_cmp_lg_u32 s66, 0\n\t" \
    "s_cbranch_scc1 qk_Ca6_%=\n\t" \
    "v_mov_b32 v160, 5836800\n\t" \
    "s_mov_b64 exec, 1\n\t" \
    "global_atomic_add v160, v162, s[36:37] sc1\n\t" \
    "s_mov_b64 exec, -1\n\t" \
    "qk_Ca6_%=:\n\t" \
    "v_mov_b32_dpp v160, v163 quad_perm:[1,0,3,2] row_mask:0xf bank_mask:0xf\n\t" \
    "v_cmp_lt_u32 s[44:45], v160, v163\n\t" \
    "s_nop 1\n\t" \
    "s_bitcmp1_b32 s44, 0\n\t" \
    "s_cselect_b32 s62, 8192, 0\n\t" \
    "s_bitcmp1_b32 s44, 2\n\t" \
    "s_cselect_b32 s63, 8192, 0\n\t" \
    "s_or_b32 s63, s63, 0x200000\n\t" \
    "v_min_u32 v160, v160, v163\n\t" \
    "v_cmp_lt_u32 vcc, 660, v160\n\t" \
    "s_nop 1\n\t" \
    "s_bcnt1_i32_b64 s69, vcc\n\t" \
    "s_cmp_gt_u32 s69, 16\n\t" \
    "s_cselect_b32 s69, 1, 0\n\t" \
    "s_mul_i32 s70, s69, 160\n\t" \
    "s_add_u32 s86, s86, s70\n\t" \
    "s_mul_i32 s70, s69, 536\n\t" \
    "s_add_u32 s87, s87, s70\n\t" \
    "s_mul_i32 s70, s69, 256\n\t" \
    "s_add_u32 s88, s88, s70\n\t" \
    "global_load_dword v25, v24, s[36:37] sc1\n\t" \
    "s_waitcnt vmcnt(5) lgkmcnt(0)\n\t" \
    "s_barrier\n\t" \
    "ds_read_b64_tr_b16 a[200:201], v201 offset:49152\n\t" \
    "ds_read_b64_tr_b16 a[202:203], v201 offset:50176\n\t" \
    "ds_read_b64_tr_b16 a[204:205], v201 offset:57344\n\t" \
    "ds_read_b64_tr_b16 a[206:207], v201 offset:58368\n\t" \
    "ds_read_b64_tr_b16 v[240:241], v202 offset:49152\n\t" \
    "ds_read_b64_tr_b16 v[242:243], v202 offset:50176\n\t" \
    "ds_read_b64_tr_b16 v[244:245], v202 offset:57344\n\t" \
    "ds_read_b64_tr_b16 v[246:247], v202 offset:58368\n\t" \
    "s_waitcnt vmcnt(1) lgkmcnt(0)\n\t" \
    "s_barrier\n\t" \
    "v_add_u32 v201, 0x10000, v201\n\t" \
    "v_add_u32 v202, 0x10000, v202\n\t" \
    "ds_read_b64_tr_b16 a[208:209], v201 offset:0\n\t" \
    "ds_read_b64_tr_b16 a[210:211], v201 offset:1024\n\t" \
    "ds_read_b64_tr_b16 a[212:213], v201 offset:8192\n\t" \
    "ds_read_b64_tr_b16 a[214:215], v201 offset:9216\n\t" \
    "ds_read_b64_tr_b16 v[248:249], v202 offset:0\n\t" \
    "ds_read_b64_tr_b16 v[250:251], v202 offset:1024\n\t" \
    "ds_read_b64_tr_b16 v[252:253], v202 offset:8192\n\t" \
    "ds_read_b64_tr_b16 v[254:255], v202 offset:9216\n\t" \
    "v_add_u32 v201, 0xfffe0000, v201\n\t" \
    "v_add_u32 v202, 0xfffe0000, v202\n\t" \
    "s_waitcnt lgkmcnt(0)\n\t" \
    "s_barrier\n\t" \
    "s_mul_i32 s70, s67, 7\n\t" \
    "s_add_u32 s69, s65, 0\n\t" \
    "s_xor_b32 s69, s69, s70\n\t" \
    "s_and_b32 s69, s69, 63\n\t" \
    "s_lshl_b32 s69, s69, 13\n\t" \
    "s_mul_i32 s70, s67, 524288\n\t" \
    "s_add_u32 s69, s69, s70\n\t" \
    "s_add_u32 s69, s69, 23068672\n\t" \
    "s_add_u32 s38, s36, s69\n\t" \
    "s_addc_u32 s39, s37, 0\n\t" \
    "v_mov_b32 v26, 0x3db504f3\n\t" \
    "v_mov_b32 v28, 0x3fb8aa3b\n\t" \
    "v_mov_b32 v27, 0x3db504f3\n\t" \
    "v_mov_b32 v29, 0x3fb8aa3b\n\t" \
    "v_xor_b32 v180, 65536, v18\n\t" \
    "v_xor_b32 v181, 131072, v18\n\t" \
    "v_xor_b32 v182, 196608, v18\n\t" \
    "s_mov_b32 s48, 0xaaaaaaaa\n\t" \
    "s_mov_b32 s49, 0xaaaaaaaa\n\t" \
    "s_mov_b32 s58, 0x00ff00ff\n\t" \
    "s_mov_b32 s59, 0x00ff00ff\n\t" \
    "v_mov_b32 v31, -1\n\t" \
    "s_mov_b32 s91, -1\n\t" \
    "ds_write_b32 v12, v31 offset:0\n\t" \
    "ds_write_b32 v12, v31 offset:64\n\t" \
    "s_waitcnt vmcnt(0)\n\t" \
    "s_cmp_gt_u32 s66, 1\n\t" \
    "s_cbranch_scc1 qk_nk7_%=\n\t" \
    "v_readfirstlane_b32 s83, v25\n\t" \
    "s_nop 1\n\t" \
    "qk_WF8_%=:\n\t" \
    "s_cmp_ge_i32 s83, s65\n\t" \
    "s_cbranch_scc1 qk_WFok9_%=\n\t" \
    "global_load_dword v160, v24, s[36:37] sc1\n\t" \
    "s_waitcnt vmcnt(0)\n\t" \
    "v_readfirstlane_b32 s83, v160\n\t" \
    "s_nop 1\n\t" \
    "s_cmp_ge_i32 s83, s65\n\t" \
    "s_cbranch_scc1 qk_WFok9_%=\n\t" \
    "s_cmp_eq_u32 s68, 0\n\t" \
    "s_cbranch_scc1 qk_F10_%=\n\t" \
    "s_sub_u32 s68, s68, 1\n\t" \
    "s_sleep 2\n\t" \
    "s_branch qk_WF8_%=\n\t" \
    "qk_F10_%=:\n\t" \
    "v_mov_b32 v160, 5767424\n\t" \
    "v_mov_b32 v161, 8\n\t" \
    "global_store_dword v160, v161, s[36:37] sc1\n\t" \
    "s_waitcnt vmcnt(0)\n\t" \
    "s_branch qk_WFok9_%=\n\t" \
    "qk_WFok9_%=:\n\t" \
    "global_load_dwordx4 v[100:103], v0, s[38:39] offset:0 sc1\n\t" \
    "global_load_dwordx4 v[104:107], v0, s[38:39] offset:2048 sc1\n\t" \
    "global_load_dwordx4 v[108:111], v15, s[38:39] offset:0 sc1\n\t" \
    "global_load_dwordx4 v[112:115], v15, s[38:39] offset:2048 sc1\n\t" \
    "global_load_dwordx4 v[116:119], v0, s[38:39] offset:1024 sc1\n\t" \
    "global_load_dwordx4 v[120:123], v0, s[38:39] offset:3072 sc1\n\t" \
    "global_load_dwordx4 v[124:127], v15, s[38:39] offset:1024 sc1\n\t" \
    "global_load_dwordx4 v[128:131], v15, s[38:39] offset:3072 sc1\n\t" \
    "qk_nk7_%=:\n\t" \
    "v_mov_b32 v33, v21\n\t" \
    "v_readlane_b32 s69, v21, 63\n\t" \
    "s_nop 1\n\t" \
    "v_mov_b32 v21, s69\n\t" \
    "s_mov_b32 s79, s74\n\t" \
    "s_mov_b32 s80, s74\n\t" \
    "s_cmp_eq_u32 s66, 1\n\t" \
    "s_cbranch_scc1 qk_loop12_%=\n\t" \
    "s_cmp_eq_u32 s66, 2\n\t" \
    "s_cbranch_scc1 qk_loop13_%=\n\t" \
    "s_cmp_eq_u32 s66, 3\n\t" \
    "s_cbranch_scc1 qk_loop14_%=\n\t" \
    "qk_loop11_%=:\n\t" \
    "v_mov_b32 v72, 0\n\t" \
    "v_mov_b32 v73, 0\n\t" \
    "v_mov_b32 v74, 0\n\t" \
    "v_mov_b32 v75, 0\n\t" \
    "v_mov_b32 v76, 0\n\t" \
    "v_mov_b32 v77, 0\n\t" \
    "v_mov_b32 v78, 0\n\t" \
    "v_mov_b32 v79, 0\n\t" \
    "v_mov_b32 v80, 0\n\t" \
    "v_mov_b32 v81, 0\n\t" \
    "v_mov_b32 v82, 0\n\t" \
    "v_mov_b32 v83, 0\n\t" \
    "v_mov_b32 v84, 0\n\t" \
    "v_mov_b32 v85, 0\n\t" \
    "v_mov_b32 v86, 0\n\t" \
    "v_mov_b32 v87, 0\n\t" \
    "v_mov_b32 v88, 0\n\t" \
    "v_mov_b32 v89, 0\n\t" \
    "v_mov_b32 v90, 0\n\t" \
    "v_mov_b32 v91, 0\n\t" \
    "v_mov_b32 v92, 0\n\t" \
    "v_mov_b32 v93, 0\n\t" \
    "v_mov_b32 v94, 0\n\t" \
    "v_mov_b32 v95, 0\n\t" \
    "v_mov_b32 v96, 0\n\t" \
    "v_mov_b32 v97, 0\n\t" \
    "v_mov_b32 v98, 0\n\t" \
    "v_mov_b32 v99, 0\n\t" \
    "s_add_u32 s69, s65, 0\n\t" \
    "s_and_b32 s69, s69, 7\n\t" \
    "s_lshl_b32 s69, s69, 18\n\t" \
    "s_add_u32 s69, s69, s62\n\t" \
    "s_add_u32 s46, s36, s69\n\t" \
    "s_addc_u32 s47, s37, 0\n\t" \
    "s_sub_u32 s70, s79, s74\n\t" \
    "s_max_i32 s70, s70, 0\n\t" \
    "s_min_i32 s70, s70, 16000\n\t" \
    "s_lshl_b32 s70, s70, 1\n\t" \
    "s_mul_hi_u32 s70, s70, s77\n\t" \
    "s_add_u32 s70, s70, s73\n\t" \
    ".p2align 6\n\t" \
    "s_nop 0\n\t" \
    "s_nop 0\n\t" \
    "s_nop 0\n\t" \
    "s_nop 0\n\t" \
    "s_nop 0\n\t" \
    "s_nop 0\n\t" \
    "qk_go16_%=:\n\t" \
    "s_getpc_b64 s[30:31]\n\t" \
    "qk_Wp18_%=:\n\t" \
    "s_add_u32 s30, s30, qk_We19_%=-qk_Wp18_%=\n\t" \
    "s_addc_u32 s31, s31, 0\n\t" \
    "s_sub_u32 s70, s70, 96\n\t" \
    "qk_W17_%=:\n\t" \
    "s_memtime s[56:57]\n\t" \
    "s_waitcnt lgkmcnt(0)\n\t" \
    "s_sub_u32 s69, s70, s56\n\t" \
    "s_cmp_gt_i32 s69, 80\n\t" \
    "s_cbranch_scc1 qk_W17_%=\n\t" \
    "s_max_i32 s69, s69, 0\n\t" \
    "s_and_b32 s69, s69, -4\n\t" \
    "s_sub_u32 s30, s30, s69\n\t" \
    "s_subb_u32 s31, s31, 0\n\t" \
    "s_setpc_b64 s[30:31]\n\t" \
    "s_nop 0\n\t" \
    "s_nop 0\n\t" \
    "s_nop 0\n\t" \
    "s_nop 0\n\t" \
    "s_nop 0\n\t" \
    "s_nop 0\n\t" \
    "s_nop 0\n\t" \
    "s_nop 0\n\t" \
    "s_nop 0\n\t" \
    "s_nop 0\n\t" \
    "s_nop 0\n\t" \
    "s_nop 0\n\t" \
    "s_nop 0\n\t" \
    "s_nop 0\n\t" \
    "s_nop 0\n\t" \
    "s_nop 0\n\t" \
    "s_nop 0\n\t" \
    "s_nop 0\n\t" \
    "s_nop 0\n\t" \
    "s_nop 0\n\t" \
    "qk_We19_%=:\n\t" \
    "qk_G20_%=:\n\t" \
    "global_load_dwordx4 v[36:39], v1, s[46:47] sc1\n\t" \
    "global_load_dword v161, v2, s[46:47] sc1\n\t" \
    "global_load_dwordx4 v[40:43], v23, s[46:47] sc1\n\t" \
    "s_waitcnt vmcnt(2)\n\t" \
    "ds_write_b128 v3, v[36:39]\n\t" \
    "v_smfmac_f32_16x16x64_bf16 v[72:75], v[36:39], a[0:7], v10\n\t" \
    "s_waitcnt vmcnt(1)\n\t" \
    "ds_write_b32 v22, v161 offset:19456\n\t" \
    "v_smfmac_f32_16x16x64_bf16 v[76:79], v[36:39], a[72:79], v10\n\t" \
    "s_waitcnt vmcnt(0)\n\t" \
    "ds_write_b128 v3, v[40:43] offset:1024\n\t" \
    "v_max3_u32 v160, v36, v40, v161\n\t" \
    "v_cmp_eq_u32 vcc, -1, v160\n\t" \
    "v_smfmac_f32_16x16x64_bf16 v[80:83], v[36:39], a[144:151], v10\n\t" \
    "v_smfmac_f32_16x16x64_bf16 v[84:87], v[36:39], a[216:223], v10\n\t" \
    "v_smfmac_f32_16x16x64_bf16 v[72:75], v[40:43], a[8:15], v10\n\t" \
    "s_cbranch_vccnz qk_Gbad21_%=\n\t" \
    "qk_Gc22_%=:\n\t" \
    "s_waitcnt lgkmcnt(0)\n\t" \
    "s_barrier\n\t" \
    "ds_read_b128 v[48:51], v4\n\t" \
    "ds_read_b128 v[52:55], v5\n\t" \
    "v_smfmac_f32_16x16x64_bf16 v[76:79], v[40:43], a[80:87], v10\n\t" \
    "ds_read_b128 v[56:59], v6\n\t" \
    "ds_read_b128 v[60:63], v7\n\t" \
    "v_smfmac_f32_16x16x64_bf16 v[80:83], v[40:43], a[152:159], v10\n\t" \
    "ds_read_b128 v[64:67], v8\n\t" \
    "v_smfmac_f32_16x16x64_bf16 v[84:87], v[40:43], a[224:231], v10\n\t" \
    "ds_read_b128 v[68:71], v9\n\t" \
    "s_waitcnt lgkmcnt(5)\n\t" \
    "v_smfmac_f32_16x16x64_bf16 v[72:75], v[48:51], a[16:23], v10\n\t" \
    "ds_read_b128 v[44:47], v0 offset:19456\n\t" \
    "s_waitcnt lgkmcnt(5)\n\t" \
    "v_smfmac_f32_16x16x64_bf16 v[72:75], v[52:55], a[24:31], v10\n\t" \
    "s_waitcnt lgkmcnt(4)\n\t" \
    "v_smfmac_f32_16x16x64_bf16 v[72:75], v[56:59], a[32:39], v10\n\t" \
    "s_waitcnt lgkmcnt(3)\n\t" \
    "v_smfmac_f32_16x16x64_bf16 v[72:75], v[60:63], a[40:47], v10\n\t" \
    "s_waitcnt lgkmcnt(2)\n\t" \
    "v_smfmac_f32_16x16x64_bf16 v[72:75], v[64:67], a[48:55], v10\n\t" \
    "s_waitcnt lgkmcnt(1)\n\t" \
    "v_smfmac_f32_16x16x64_bf16 v[72:75], v[68:71], a[56:63], v10\n\t" \
    "s_waitcnt lgkmcnt(0)\n\t" \
    "v_smfmac_f32_16x16x64_bf16 v[72:75], v[44:47], a[64:71], v10\n\t" \
    "s_nop 2\n\t" \
    "v_smfmac_f32_16x16x64_bf16 v[76:79], v[48:51], a[88:95], v10\n\t" \
    "s_nop 2\n\t" \
    "v_smfmac_f32_16x16x64_bf16 v[76:79], v[52:55], a[96:103], v10\n\t" \
    "v_add_f32 v72, v72, v74\n\t" \
    "v_smfmac_f32_16x16x64_bf16 v[76:79], v[56:59], a[104:111], v10\n\t" \
    "v_add_f32 v73, v73, v75\n\t" \
    "v_smfmac_f32_16x16x64_bf16 v[76:79], v[60:63], a[112:119], v10\n\t" \
    "v_cvt_pk_bf16_f32 v156, v72, v73\n\t" \
    "v_smfmac_f32_16x16x64_bf16 v[76:79], v[64:67], a[120:127], v10\n\t" \
    "ds_write_b32 v12, v156 offset:0\n\t" \
    "v_smfmac_f32_16x16x64_bf16 v[76:79], v[68:71], a[128:135], v10\n\t" \
    "v_smfmac_f32_16x16x64_bf16 v[76:79], v[44:47], a[136:143], v10\n\t" \
    "s_nop 2\n\t" \
    "v_smfmac_f32_16x16x64_bf16 v[80:83], v[48:51], a[160:167], v10\n\t" \
    "s_nop 2\n\t" \
    "v_smfmac_f32_16x16x64_bf16 v[80:83], v[52:55], a[168:175], v10\n\t" \
    "v_add_f32 v76, v76, v78\n\t" \
    "v_smfmac_f32_16x16x64_bf16 v[80:83], v[56:59], a[176:183], v10\n\t" \
    "v_add_f32 v77, v77, v79\n\t" \
    "v_smfmac_f32_16x16x64_bf16 v[80:83], v[60:63], a[184:191], v10\n\t" \
    "v_cvt_pk_bf16_f32 v157, v76, v77\n\t" \
    "v_smfmac_f32_16x16x64_bf16 v[80:83], v[64:67], a[192:199], v10\n\t" \
    "ds_write_b32 v12, v157 offset:64\n\t" \
    "v_smfmac_f32_16x16x64_bf16 v[80:83], v[68:71], a[200:207], v10\n\t" \
    "v_smfmac_f32_16x16x64_bf16 v[80:83], v[44:47], a[208:215], v10\n\t" \
    "s_nop 2\n\t" \
    "v_smfmac_f32_16x16x64_bf16 v[84:87], v[48:51], a[232:239], v10\n\t" \
    "s_nop 2\n\t" \
    "v_smfmac_f32_16x16x64_bf16 v[84:87], v[52:55], a[240:247], v10\n\t" \
    "v_add_f32 v80, v80, v82\n\t" \
    "v_smfmac_f32_16x16x64_bf16 v[84:87], v[56:59], a[248:255], v10\n\t" \
    "v_add_f32 v81, v81, v83\n\t" \
    "v_smfmac_f32_16x16x64_bf16 v[84:87], v[60:63], v[224:231], v10\n\t" \
    "v_cvt_pk_bf16_f32 v158, v80, v81\n\t" \
    "v_smfmac_f32_16x16x64_bf16 v[84:87], v[64:67], v[232:239], v10\n\t" \
    "ds_write_b32 v16, v158 offset:0\n\t" \
    "v_smfmac_f32_16x16x64_bf16 v[84:87], v[68:71], v[240:247], v10\n\t" \
    "ds_read_b64_tr_b16 v[132:133], v13 offset:0\n\t" \
    "ds_read_b64_tr_b16 v[134:135], v13 offset:128\n\t" \
    "v_smfmac_f32_16x16x64_bf16 v[84:87], v[44:47], v[248:255], v10\n\t" \
    "ds_read_b64_tr_b16 v[136:137], v13 offset:1024\n\t" \
    "ds_read_b64_tr_b16 v[138:139], v13 offset:1152\n\t" \
    "s_nop 4\n\t" \
    "v_cmp_u_f32 vcc, v72, v73\n\t" \
    "v_pk_add_f32 v[84:85], v[84:85], v[86:87]\n\t" \
    "v_cvt_pk_bf16_f32 v159, v84, v85\n\t" \
    "ds_write_b32 v16, v159 offset:256\n\t" \
    "s_cbranch_vccnz qk_Gslow23_%=\n\t" \
    "qk_Gback24_%=:\n\t" \
    "s_waitcnt lgkmcnt(3)\n\t" \
    "v_smfmac_f32_16x16x64_bf16 v[88:91], v[132:135], v[100:107], v10\n\t" \
    "s_waitcnt lgkmcnt(1)\n\t" \
    "v_smfmac_f32_16x16x64_bf16 v[88:91], v[136:139], v[108:115], v10\n\t" \
    "s_waitcnt lgkmcnt(0)\n\t" \
    "s_barrier\n\t" \
    "ds_read_b64_tr_b16 v[140:141], v17 offset:0\n\t" \
    "v_smfmac_f32_16x16x64_bf16 v[92:95], v[132:135], v[116:123], v10\n\t" \
    "ds_read_b64_tr_b16 v[142:143], v17 offset:256\n\t" \
    "ds_read_b64_tr_b16 v[144:145], v17 offset:512\n\t" \
    "ds_read_b64_tr_b16 v[146:147], v17 offset:768\n\t" \
    "ds_read_b64_tr_b16 v[148:149], v17 offset:1024\n\t" \
    "v_smfmac_f32_16x16x64_bf16 v[92:95], v[136:139], v[124:131], v10\n\t" \
    "ds_read_b64_tr_b16 v[150:151], v17 offset:1280\n\t" \
    "ds_read_b64_tr_b16 v[152:153], v17 offset:1536\n\t" \
    "ds_read_b64_tr_b16 v[154:155], v17 offset:1792\n\t" \
    "v_smfmac_f32_16x16x64_bf16 v[184:187], v[132:135], v[100:107], v10\n\t" \
    "qk_qb26_%=:\n\t" \
    "s_waitcnt lgkmcnt(4)\n\t" \
    "v_smfmac_f32_16x16x64_bf16 v[96:99], v[132:135], v[140:147], v10\n\t" \
    "s_waitcnt lgkmcnt(0)\n\t" \
    "v_smfmac_f32_16x16x64_bf16 v[96:99], v[136:139], v[148:155], v10\n\t" \
    "v_add_f32 v168, v88, v90\n\t" \
    "v_add_f32 v169, v92, v94\n\t" \
    "v_cmp_u_f32 vcc, v168, v169\n\t" \
    "v_cvt_pk_bf16_f32 v168, 0, v168\n\t" \
    "v_cvt_pk_bf16_f32 v169, 0, v169\n\t" \
    "v_mul_f32 v168, v168, v26\n\t" \
    "v_mul_f32 v169, v169, v26\n\t" \
    "v_cvt_pk_bf16_f32 v168, 0, v168\n\t" \
    "v_add_f32 v172, v96, v98\n\t" \
    "v_cvt_pk_bf16_f32 v169, 0, v169\n\t" \
    "v_max_f32 v178, v168, v169\n\t" \
    "v_cvt_pk_bf16_f32 v172, 0, v172\n\t" \
    "v_mul_f32 v172, v172, v26\n\t" \
    "v_max_f32_dpp v178, v178, v178 row_mirror row_mask:0xf bank_mask:0xf\n\t" \
    "v_cvt_pk_bf16_f32 v172, 0, v172\n\t" \
    "v_max_f32 v173, v178, v172\n\t" \
    "v_mul_f32 v168, v168, v28\n\t" \
    "v_mul_f32 v169, v169, v28\n\t" \
    "v_max_f32_dpp v173, v173, v173 quad_perm:[1,0,3,2] row_mask:0xf bank_mask:0xf\n\t" \
    "v_mul_f32 v172, v172, v28\n\t" \
    "s_cbranch_vccnz qk_qs25_%=\n\t" \
    "qk_qb227_%=:\n\t" \
    "v_max_f32_dpp v173, v173, v173 quad_perm:[2,3,0,1] row_mask:0xf bank_mask:0xf\n\t" \
    "s_nop 1\n\t" \
    "v_max_f32_dpp v173, v173, v173 row_half_mirror row_mask:0xf bank_mask:0xf\n\t" \
    "v_fma_f32 v172, -v173, v28, v172\n\t" \
    "v_fma_f32 v168, -v173, v28, v168\n\t" \
    "v_fma_f32 v169, -v173, v28, v169\n\t" \
    "v_exp_f32 v172, v172\n\t" \
    "v_exp_f32 v168, v168\n\t" \
    "v_exp_f32 v169, v169\n\t" \
    "v_fma_f32 v173, v172, 0.5, v168\n\t" \
    "v_add_f32 v173, v173, v169\n\t" \
    "v_mov_b32_dpp v174, v172 quad_perm:[0,0,2,2] row_mask:0xf bank_mask:0xf\n\t" \
    "v_mov_b32_dpp v175, v172 quad_perm:[1,1,3,3] row_mask:0xf bank_mask:0xf\n\t" \
    "v_add_f32_dpp v173, v173, v173 quad_perm:[1,0,3,2] row_mask:0xf bank_mask:0xf\n\t" \
    "v_mov_b32_dpp v176, v168 quad_perm:[1,0,3,2] row_mask:0xf bank_mask:0xf\n\t" \
    "v_mov_b32_dpp v177, v169 quad_perm:[1,0,3,2] row_mask:0xf bank_mask:0xf\n\t" \
    "v_add_f32_dpp v173, v173, v173 quad_perm:[2,3,0,1] row_mask:0xf bank_mask:0xf\n\t" \
    "v_cndmask_b32 v168, v168, v177, s[48:49]\n\t" \
    "v_cndmask_b32 v169, v176, v169, s[48:49]\n\t" \
    "v_add_f32_dpp v173, v173, v173 row_half_mirror row_mask:0xf bank_mask:0xf\n\t" \
    "s_nop 1\n\t" \
    "v_add_f32_dpp v173, v173, v173 row_mirror row_mask:0xf bank_mask:0xf\n\t" \
    "v_rcp_f32 v173, v173\n\t" \
    "s_nop 0\n\t" \
    "v_pk_mul_f32 v[168:169], v[168:169], v[172:173] op_sel:[0,1] op_sel_hi:[1,1]\n\t" \
    "v_cvt_pk_bf16_f32 v176, v168, v169\n\t" \
    "global_store_dword v18, v176, s[46:47] sc1\n\t" \
    "global_store_dword v180, v176, s[46:47] sc1\n\t" \
    "global_store_dword v181, v176, s[46:47] sc1\n\t" \
    "global_store_dword v182, v176, s[46:47] sc1\n\t" \
    "v_pk_mul_f32 v[174:175], v[174:175], v[172:173] op_sel:[0,1] op_sel_hi:[1,1]\n\t" \
    "v_cvt_pk_bf16_f32 v177, v174, v175\n\t" \
    "global_store_dword v19, v177, s[46:47] sc1\n\t" \
    "s_cmp_eq_u32 s65, 63\n\t" \
    "s_cbranch_scc1 qk_last15_%=\n\t" \
    "v_readfirstlane_b32 s69, v25\n\t" \
    "s_add_u32 s65, s65, 1\n\t" \
    "s_max_i32 s83, s83, s69\n\t" \
    "s_cmp_gt_u32 s65, 6\n\t" \
    "s_cselect_b32 s83, 99, s83\n\t" \
    "v_mov_b32 v72, 0\n\t" \
    "v_mov_b32 v73, 0\n\t" \
    "v_mov_b32 v74, 0\n\t" \
    "v_mov_b32 v75, 0\n\t" \
    "v_mov_b32 v76, 0\n\t" \
    "v_mov_b32 v77, 0\n\t" \
    "v_mov_b32 v78, 0\n\t" \
    "v_mov_b32 v79, 0\n\t" \
    "v_mov_b32 v80, 0\n\t" \
    "v_mov_b32 v81, 0\n\t" \
    "v_mov_b32 v82, 0\n\t" \
    "v_mov_b32 v83, 0\n\t" \
    "v_mov_b32 v84, 0\n\t" \
    "v_mov_b32 v85, 0\n\t" \
    "v_mov_b32 v86, 0\n\t" \
    "v_mov_b32 v87, 0\n\t" \
    "v_mov_b32 v88, 0\n\t" \
    "v_mov_b32 v89, 0\n\t" \
    "v_mov_b32 v90, 0\n\t" \
    "v_mov_b32 v91, 0\n\t" \
    "v_mov_b32 v92, 0\n\t" \
    "v_mov_b32 v93, 0\n\t" \
    "v_mov_b32 v94, 0\n\t" \
    "v_mov_b32 v95, 0\n\t" \
    "v_mov_b32 v96, 0\n\t" \
    "v_mov_b32 v97, 0\n\t" \
    "v_mov_b32 v98, 0\n\t" \
    "v_mov_b32 v99, 0\n\t" \
    "s_add_u32 s69, s65, 0\n\t" \
    "s_and_b32 s69, s69, 7\n\t" \
    "s_lshl_b32 s69, s69, 18\n\t" \
    "s_add_u32 s69, s69, s62\n\t" \
    "s_add_u32 s46, s36, s69\n\t" \
    "s_addc_u32 s47, s37, 0\n\t" \
    "s_mul_i32 s70, s67, 7\n\t" \
    "s_add_u32 s69, s65, 0\n\t" \
    "s_xor_b32 s69, s69, s70\n\t" \
    "s_and_b32 s69, s69, 63\n\t" \
    "s_lshl_b32 s69, s69, 13\n\t" \
    "s_mul_i32 s70, s67, 524288\n\t" \
    "s_add_u32 s69, s69, s70\n\t" \
    "s_add_u32 s69, s69, 23068672\n\t" \
    "s_add_u32 s38, s36, s69\n\t" \
    "s_addc_u32 s39, s37, 0\n\t" \
    "qk_WF28_%=:\n\t" \
    "s_cmp_ge_i32 s83, s65\n\t" \
    "s_cbranch_scc1 qk_WFok29_%=\n\t" \
    "global_load_dword v160, v24, s[36:37] sc1\n\t" \
    "s_waitcnt vmcnt(0)\n\t" \
    "v_readfirstlane_b32 s83, v160\n\t" \
    "s_nop 1\n\t" \
    "s_cmp_ge_i32 s83, s65\n\t" \
    "s_cbranch_scc1 qk_WFok29_%=\n\t" \
    "s_cmp_eq_u32 s68, 0\n\t" \
    "s_cbranch_scc1 qk_F30_%=\n\t" \
    "s_sub_u32 s68, s68, 1\n\t" \
    "s_sleep 2\n\t" \
    "s_branch qk_WF28_%=\n\t" \
    "qk_F30_%=:\n\t" \
    "v_mov_b32 v160, 5767424\n\t" \
    "v_mov_b32 v161, 9\n\t" \
    "global_store_dword v160, v161, s[36:37] sc1\n\t" \
    "s_waitcnt vmcnt(0)\n\t" \
    "s_branch qk_WFok29_%=\n\t" \
    "qk_WFok29_%=:\n\t" \
    "global_load_dwordx4 v[100:103], v0, s[38:39] offset:0 sc1\n\t" \
    "global_load_dwordx4 v[104:107], v0, s[38:39] offset:2048 sc1\n\t" \
    "global_load_dwordx4 v[108:111], v15, s[38:39] offset:0 sc1\n\t" \
    "global_load_dwordx4 v[112:115], v15, s[38:39] offset:2048 sc1\n\t" \
    "global_load_dwordx4 v[116:119], v0, s[38:39] offset:1024 sc1\n\t" \
    "global_load_dwordx4 v[120:123], v0, s[38:39] offset:3072 sc1\n\t" \
    "global_load_dwordx4 v[124:127], v15, s[38:39] offset:1024 sc1\n\t" \
    "global_load_dwordx4 v[128:131], v15, s[38:39] offset:3072 sc1\n\t" \
    "s_cmp_gt_i32 s83, s65\n\t" \
    "s_cbranch_scc1 qk_nf31_%=\n\t" \
    "global_load_dword v25, v24, s[36:37] sc1\n\t" \
    "qk_nf31_%=:\n\t" \
    "s_barrier\n\t" \
    "ds_read_b32 v168, v11 offset:20480\n\t" \
    "s_waitcnt lgkmcnt(0)\n\t" \
    "v_readfirstlane_b32 s70, v168\n\t" \
    "ds_write_b32 v12, v31 offset:0\n\t" \
    "ds_write_b32 v12, v31 offset:64\n\t" \
    "s_branch qk_go16_%=\n\t" \
    "qk_Gbad21_%=:\n\t" \
    "s_waitcnt lgkmcnt(0)\n\t" \
    "s_nop 7\n\t" \
    "v_mov_b32 v72, 0\n\t" \
    "v_mov_b32 v73, 0\n\t" \
    "v_mov_b32 v74, 0\n\t" \
    "v_mov_b32 v75, 0\n\t" \
    "v_mov_b32 v76, 0\n\t" \
    "v_mov_b32 v77, 0\n\t" \
    "v_mov_b32 v78, 0\n\t" \
    "v_mov_b32 v79, 0\n\t" \
    "v_mov_b32 v80, 0\n\t" \
    "v_mov_b32 v81, 0\n\t" \
    "v_mov_b32 v82, 0\n\t" \
    "v_mov_b32 v83, 0\n\t" \
    "v_mov_b32 v84, 0\n\t" \
    "v_mov_b32 v85, 0\n\t" \
    "v_mov_b32 v86, 0\n\t" \
    "v_mov_b32 v87, 0\n\t" \
    "s_cmp_eq_u32 s68, 0\n\t" \
    "s_cbranch_scc1 qk_F32_%=\n\t" \
    "s_sub_u32 s68, s68, 1\n\t" \
    "s_branch qk_G20_%=\n\t" \
    "qk_F32_%=:\n\t" \
    "v_mov_b32 v160, 5767424\n\t" \
    "v_mov_b32 v161, 1\n\t" \
    "global_store_dword v160, v161, s[36:37] sc1\n\t" \
    "s_waitcnt vmcnt(0)\n\t" \
    "s_branch qk_Gc22_%=\n\t" \
    "qk_Gslow23_%=:\n\t" \
    "v_max_u32 v160, v36, v37\n\t" \
    "v_max3_u32 v160, v160, v38, v39\n\t" \
    "v_max3_u32 v160, v160, v40, v41\n\t" \
    "v_max3_u32 v160, v160, v42, v43\n\t" \
    "v_max3_u32 v160, v160, v44, v45\n\t" \
    "v_max3_u32 v160, v160, v46, v47\n\t" \
    "v_max3_u32 v160, v160, v48, v49\n\t" \
    "v_max3_u32 v160, v160, v50, v51\n\t" \
    "v_max3_u32 v160, v160, v52, v53\n\t" \
    "v_max3_u32 v160, v160, v54, v55\n\t" \
    "v_max3_u32 v160, v160, v56, v57\n\t" \
    "v_max3_u32 v160, v160, v58, v59\n\t" \
    "v_max3_u32 v160, v160, v60, v61\n\t" \
    "v_max3_u32 v160, v160, v62, v63\n\t" \
    "v_max3_u32 v160, v160, v64, v65\n\t" \
    "v_max3_u32 v160, v160, v66, v67\n\t" \
    "v_max3_u32 v160, v160, v68, v69\n\t" \
    "v_max3_u32 v160, v160, v70, v71\n\t" \
    "v_cmp_eq_u32 vcc, -1, v160\n\t" \
    "s_cbranch_vccz qk_Gback24_%=\n\t" \
    "s_waitcnt lgkmcnt(0)\n\t" \
    "v_sub_u32 v160, v1, v3\n\t" \
    "s_nop 0\n\t" \
    "v_readfirstlane_b32 s69, v160\n\t" \
    "v_add_u32 v163, 20480, v0\n\t" \
    "s_nop 7\n\t" \
    "s_add_u32 s30, s46, s69\n\t" \
    "s_addc_u32 s31, s47, 0\n\t" \
    "qk_Gag33_%=:\n\t" \
    "global_load_dwordx4 v[36:39], v1, s[46:47] sc1\n\t" \
    "global_load_dwordx4 v[40:43], v23, s[46:47] sc1\n\t" \
    "global_load_dwordx4 v[44:47], v163, s[30:31] sc1\n\t" \
    "global_load_dwordx4 v[48:51], v4, s[30:31] sc1\n\t" \
    "global_load_dwordx4 v[52:55], v5, s[30:31] sc1\n\t" \
    "global_load_dwordx4 v[56:59], v6, s[30:31] sc1\n\t" \
    "global_load_dwordx4 v[60:63], v7, s[30:31] sc1\n\t" \
    "global_load_dwordx4 v[64:67], v8, s[30:31] sc1\n\t" \
    "global_load_dwordx4 v[68:71], v9, s[30:31] sc1\n\t" \
    "s_waitcnt vmcnt(0)\n\t" \
    "v_max_u32 v160, v36, v37\n\t" \
    "v_max3_u32 v160, v160, v38, v39\n\t" \
    "v_max3_u32 v160, v160, v40, v41\n\t" \
    "v_max3_u32 v160, v160, v42, v43\n\t" \
    "v_max3_u32 v160, v160, v44, v45\n\t" \
    "v_max3_u32 v160, v160, v46, v47\n\t" \
    "v_max3_u32 v160, v160, v48, v49\n\t" \
    "v_max3_u32 v160, v160, v50, v51\n\t" \
    "v_max3_u32 v160, v160, v52, v53\n\t" \
    "v_max3_u32 v160, v160, v54, v55\n\t" \
    "v_max3_u32 v160, v160, v56, v57\n\t" \
    "v_max3_u32 v160, v160, v58, v59\n\t" \
    "v_max3_u32 v160, v160, v60, v61\n\t" \
    "v_max3_u32 v160, v160, v62, v63\n\t" \
    "v_max3_u32 v160, v160, v64, v65\n\t" \
    "v_max3_u32 v160, v160, v66, v67\n\t" \
    "v_max3_u32 v160, v160, v68, v69\n\t" \
    "v_max3_u32 v160, v160, v70, v71\n\t" \
    "v_cmp_eq_u32 vcc, -1, v160\n\t" \
    "s_cbranch_vccz qk_Gredo34_%=\n\t" \
    "s_cmp_eq_u32 s68, 0\n\t" \
    "s_cbranch_scc1 qk_F35_%=\n\t" \
    "s_sub_u32 s68, s68, 1\n\t" \
    "s_sleep 1\n\t" \
    "s_branch qk_Gag33_%=\n\t" \
    "qk_F35_%=:\n\t" \
    "v_mov_b32 v160, 5767424\n\t" \
    "v_mov_b32 v161, 2\n\t" \
    "global_store_dword v160, v161, s[36:37] sc1\n\t" \
    "s_waitcnt vmcnt(0)\n\t" \
    "s_branch qk_Gredo34_%=\n\t" \
    "qk_Gredo34_%=:\n\t" \
    "v_mov_b32 v72, 0\n\t" \
    "v_mov_b32 v73, 0\n\t" \
    "v_mov_b32 v74, 0\n\t" \
    "v_mov_b32 v75, 0\n\t" \
    "v_mov_b32 v76, 0\n\t" \
    "v_mov_b32 v77, 0\n\t" \
    "v_mov_b32 v78, 0\n\t" \
    "v_mov_b32 v79, 0\n\t" \
    "v_mov_b32 v80, 0\n\t" \
    "v_mov_b32 v81, 0\n\t" \
    "v_mov_b32 v82, 0\n\t" \
    "v_mov_b32 v83, 0\n\t" \
    "v_mov_b32 v84, 0\n\t" \
    "v_mov_b32 v85, 0\n\t" \
    "v_mov_b32 v86, 0\n\t" \
    "v_mov_b32 v87, 0\n\t" \
    "v_smfmac_f32_16x16x64_bf16 v[72:75], v[36:39], a[0:7], v10\n\t" \
    "v_smfmac_f32_16x16x64_bf16 v[72:75], v[40:43], a[8:15], v10\n\t" \
    "v_smfmac_f32_16x16x64_bf16 v[72:75], v[48:51], a[16:23], v10\n\t" \
    "v_smfmac_f32_16x16x64_bf16 v[72:75], v[52:55], a[24:31], v10\n\t" \
    "v_smfmac_f32_16x16x64_bf16 v[72:75], v[56:59], a[32:39], v10\n\t" \
    "v_smfmac_f32_16x16x64_bf16 v[72:75], v[60:63], a[40:47], v10\n\t" \
    "v_smfmac_f32_16x16x64_bf16 v[72:75], v[64:67], a[48:55], v10\n\t" \
    "v_smfmac_f32_16x16x64_bf16 v[72:75], v[68:71], a[56:63], v10\n\t" \
    "v_smfmac_f32_16x16x64_bf16 v[72:75], v[44:47], a[64:71], v10\n\t" \
    "v_smfmac_f32_16x16x64_bf16 v[76:79], v[36:39], a[72:79], v10\n\t" \
    "v_smfmac_f32_16x16x64_bf16 v[76:79], v[40:43], a[80:87], v10\n\t" \
    "v_smfmac_f32_16x16x64_bf16 v[76:79], v[48:51], a[88:95], v10\n\t" \
    "v_smfmac_f32_16x16x64_bf16 v[76:79], v[52:55], a[96:103], v10\n\t" \
    "v_smfmac_f32_16x16x64_bf16 v[76:79], v[56:59], a[104:111], v10\n\t" \
    "v_smfmac_f32_16x16x64_bf16 v[76:79], v[60:63], a[112:119], v10\n\t" \
    "v_smfmac_f32_16x16x64_bf16 v[76:79], v[64:67], a[120:127], v10\n\t" \
    "v_smfmac_f32_16x16x64_bf16 v[76:79], v[68:71], a[128:135], v10\n\t" \
    "v_smfmac_f32_16x16x64_bf16 v[76:79], v[44:47], a[136:143], v10\n\t" \
    "v_smfmac_f32_16x16x64_bf16 v[80:83], v[36:39], a[144:151], v10\n\t" \
    "v_smfmac_f32_16x16x64_bf16 v[80:83], v[40:43], a[152:159], v10\n\t" \
    "v_smfmac_f32_16x16x64_bf16 v[80:83], v[48:51], a[160:167], v10\n\t" \
    "v_smfmac_f32_16x16x64_bf16 v[80:83], v[52:55], a[168:175], v10\n\t" \
    "v_smfmac_f32_16x16x64_bf16 v[80:83], v[56:59], a[176:183], v10\n\t" \
    "v_smfmac_f32_16x16x64_bf16 v[80:83], v[60:63], a[184:191], v10\n\t" \
    "v_smfmac_f32_16x16x64_bf16 v[80:83], v[64:67], a[192:199], v10\n\t" \
    "v_smfmac_f32_16x16x64_bf16 v[80:83], v[68:71], a[200:207], v10\n\t" \
    "v_smfmac_f32_16x16x64_bf16 v[80:83], v[44:47], a[208:215], v10\n\t" \
    "v_smfmac_f32_16x16x64_bf16 v[84:87], v[36:39], a[216:223], v10\n\t" \
    "v_smfmac_f32_16x16x64_bf16 v[84:87], v[40:43], a[224:231], v10\n\t" \
    "v_smfmac_f32_16x16x64_bf16 v[84:87], v[48:51], a[232:239], v10\n\t" \
    "v_smfmac_f32_16x16x64_bf16 v[84:87], v[52:55], a[240:247], v10\n\t" \
    "v_smfmac_f32_16x16x64_bf16 v[84:87], v[56:59], a[248:255], v10\n\t" \
    "v_smfmac_f32_16x16x64_bf16 v[84:87], v[60:63], v[224:231], v10\n\t" \
    "v_smfmac_f32_16x16x64_bf16 v[84:87], v[64:67], v[232:239], v10\n\t" \
    "v_smfmac_f32_16x16x64_bf16 v[84:87], v[68:71], v[240:247], v10\n\t" \
    "v_smfmac_f32_16x16x64_bf16 v[84:87], v[44:47], v[248:255], v10\n\t" \
    "v_add_f32 v72, v72, v74\n\t" \
    "v_add_f32 v73, v73, v75\n\t" \
    "v_cvt_pk_bf16_f32 v156, v72, v73\n\t" \
    "ds_write_b32 v12, v156 offset:0\n\t" \
    "v_add_f32 v76, v76, v78\n\t" \
    "v_add_f32 v77, v77, v79\n\t" \
    "v_cvt_pk_bf16_f32 v157, v76, v77\n\t" \
    "ds_write_b32 v12, v157 offset:64\n\t" \
    "v_add_f32 v80, v80, v82\n\t" \
    "v_add_f32 v81, v81, v83\n\t" \
    "v_cvt_pk_bf16_f32 v158, v80, v81\n\t" \
    "ds_write_b32 v16, v158 offset:0\n\t" \
    "v_pk_add_f32 v[84:85], v[84:85], v[86:87]\n\t" \
    "v_cvt_pk_bf16_f32 v159, v84, v85\n\t" \
    "ds_write_b32 v16, v159 offset:256\n\t" \
    "s_branch qk_Gback24_%=\n\t" \
    "qk_qs25_%=:\n\t" \
    "s_cmp_eq_u32 s91, s65\n\t" \
    "s_cbranch_scc1 qk_qb227_%=\n\t" \
    "s_mov_b32 s91, s65\n\t" \
    "s_nop 7\n\t" \
    "ds_read_b64_tr_b16 v[132:133], v13 offset:0\n\t" \
    "ds_read_b64_tr_b16 v[134:135], v13 offset:128\n\t" \
    "ds_read_b64_tr_b16 v[136:137], v13 offset:1024\n\t" \
    "ds_read_b64_tr_b16 v[138:139], v13 offset:1152\n\t" \
    "v_mov_b32 v88, 0\n\t" \
    "v_mov_b32 v89, 0\n\t" \
    "v_mov_b32 v90, 0\n\t" \
    "v_mov_b32 v91, 0\n\t" \
    "v_mov_b32 v92, 0\n\t" \
    "v_mov_b32 v93, 0\n\t" \
    "v_mov_b32 v94, 0\n\t" \
    "v_mov_b32 v95, 0\n\t" \
    "v_mov_b32 v96, 0\n\t" \
    "v_mov_b32 v97, 0\n\t" \
    "v_mov_b32 v98, 0\n\t" \
    "v_mov_b32 v99, 0\n\t" \
    "s_waitcnt lgkmcnt(0)\n\t" \
    "v_smfmac_f32_16x16x64_bf16 v[88:91], v[132:135], v[100:107], v10\n\t" \
    "v_smfmac_f32_16x16x64_bf16 v[92:95], v[132:135], v[116:123], v10\n\t" \
    "v_smfmac_f32_16x16x64_bf16 v[88:91], v[136:139], v[108:115], v10\n\t" \
    "v_smfmac_f32_16x16x64_bf16 v[92:95], v[136:139], v[124:131], v10\n\t" \
    "s_nop 7\n\t" \
    "s_branch qk_qb26_%=\n\t" \
    "qk_last15_%=:\n\t" \
    "s_endpgm\n\t" \
    "qk_loop12_%=:\n\t" \
    "v_mov_b32 v72, 0\n\t" \
    "v_mov_b32 v73, 0\n\t" \
    "v_mov_b32 v74, 0\n\t" \
    "v_mov_b32 v75, 0\n\t" \
    "v_mov_b32 v76, 0\n\t" \
    "v_mov_b32 v77, 0\n\t" \
    "v_mov_b32 v78, 0\n\t" \
    "v_mov_b32 v79, 0\n\t" \
    "v_mov_b32 v80, 0\n\t" \
    "v_mov_b32 v81, 0\n\t" \
    "v_mov_b32 v82, 0\n\t" \
    "v_mov_b32 v83, 0\n\t" \
    "v_mov_b32 v84, 0\n\t" \
    "v_mov_b32 v85, 0\n\t" \
    "v_mov_b32 v86, 0\n\t" \
    "v_mov_b32 v87, 0\n\t" \
    "v_mov_b32 v88, 0\n\t" \
    "v_mov_b32 v89, 0\n\t" \
    "v_mov_b32 v90, 0\n\t" \
    "v_mov_b32 v91, 0\n\t" \
    "v_mov_b32 v92, 0\n\t" \
    "v_mov_b32 v93, 0\n\t" \
    "v_mov_b32 v94, 0\n\t" \
    "v_mov_b32 v95, 0\n\t" \
    "v_mov_b32 v96, 0\n\t" \
    "v_mov_b32 v97, 0\n\t" \
    "v_mov_b32 v98, 0\n\t" \
    "v_mov_b32 v99, 0\n\t" \
    "s_add_u32 s69, s65, 0\n\t" \
    "s_and_b32 s69, s69, 7\n\t" \
    "s_lshl_b32 s69, s69, 18\n\t" \
    "s_add_u32 s69, s69, s62\n\t" \
    "s_add_u32 s46, s36, s69\n\t" \
    "s_addc_u32 s47, s37, 0\n\t" \
    "s_sub_u32 s70, s79, s74\n\t" \
    "s_max_i32 s70, s70, 0\n\t" \
    "s_min_i32 s70, s70, 16000\n\t" \
    "s_lshl_b32 s70, s70, 1\n\t" \
    "s_mul_hi_u32 s70, s70, s77\n\t" \
    "s_add_u32 s70, s70, s73\n\t" \
    ".p2align 6\n\t" \
    "s_nop 0\n\t" \
    "s_nop 0\n\t" \
    "s_nop 0\n\t" \
    "s_nop 0\n\t" \
    "s_nop 0\n\t" \
    "s_nop 0\n\t" \
    "qk_go37_%=:\n\t" \
    "s_getpc_b64 s[30:31]\n\t" \
    "qk_Wp39_%=:\n\t" \
    "s_add_u32 s30, s30, qk_We40_%=-qk_Wp39_%=\n\t" \
    "s_addc_u32 s31, s31, 0\n\t" \
    "s_sub_u32 s70, s70, 96\n\t" \
    "qk_W38_%=:\n\t" \
    "s_memtime s[56:57]\n\t" \
    "s_waitcnt lgkmcnt(0)\n\t" \
    "s_sub_u32 s69, s70, s56\n\t" \
    "s_cmp_gt_i32 s69, 80\n\t" \
    "s_cbranch_scc1 qk_W38_%=\n\t" \
    "s_max_i32 s69, s69, 0\n\t" \
    "s_and_b32 s69, s69, -4\n\t" \
    "s_sub_u32 s30, s30, s69\n\t" \
    "s_subb_u32 s31, s31, 0\n\t" \
    "s_setpc_b64 s[30:31]\n\t" \
    "s_nop 0\n\t" \
    "s_nop 0\n\t" \
    "s_nop 0\n\t" \
    "s_nop 0\n\t" \
    "s_nop 0\n\t" \
    "s_nop 0\n\t" \
    "s_nop 0\n\t" \
    "s_nop 0\n\t" \
    "s_nop 0\n\t" \
    "s_nop 0\n\t" \
    "s_nop 0\n\t" \
    "s_nop 0\n\t" \
    "s_nop 0\n\t" \
    "s_nop 0\n\t" \
    "s_nop 0\n\t" \
    "s_nop 0\n\t" \
    "s_nop 0\n\t" \
    "s_nop 0\n\t" \
    "s_nop 0\n\t" \
    "s_nop 0\n\t" \
    "qk_We40_%=:\n\t" \
    "qk_G41_%=:\n\t" \
    "global_load_dwordx4 v[36:39], v1, s[46:47] sc1\n\t" \
    "global_load_dword v161, v2, s[46:47] sc1\n\t" \
    "global_load_dwordx4 v[40:43], v23, s[46:47] sc1\n\t" \
    "s_waitcnt vmcnt(2)\n\t" \
    "ds_write_b128 v3, v[36:39]\n\t" \
    "v_smfmac_f32_16x16x64_bf16 v[72:75], v[36:39], a[16:23], v10\n\t" \
    "s_waitcnt vmcnt(1)\n\t" \
    "ds_write_b32 v22, v161 offset:19712\n\t" \
    "v_smfmac_f32_16x16x64_bf16 v[76:79], v[36:39], a[88:95], v10\n\t" \
    "s_waitcnt vmcnt(0)\n\t" \
    "ds_write_b128 v3, v[40:43] offset:1024\n\t" \
    "v_max3_u32 v160, v36, v40, v161\n\t" \
    "v_cmp_eq_u32 vcc, -1, v160\n\t" \
    "v_smfmac_f32_16x16x64_bf16 v[80:83], v[36:39], a[160:167], v10\n\t" \
    "v_smfmac_f32_16x16x64_bf16 v[84:87], v[36:39], a[232:239], v10\n\t" \
    "v_smfmac_f32_16x16x64_bf16 v[72:75], v[40:43], a[24:31], v10\n\t" \
    "s_cbranch_vccnz qk_Gbad42_%=\n\t" \
    "qk_Gc43_%=:\n\t" \
    "s_waitcnt lgkmcnt(0)\n\t" \
    "s_barrier\n\t" \
    "ds_read_b128 v[48:51], v4\n\t" \
    "ds_read_b128 v[52:55], v5\n\t" \
    "v_smfmac_f32_16x16x64_bf16 v[76:79], v[40:43], a[96:103], v10\n\t" \
    "ds_read_b128 v[56:59], v6\n\t" \
    "ds_read_b128 v[60:63], v7\n\t" \
    "v_smfmac_f32_16x16x64_bf16 v[80:83], v[40:43], a[168:175], v10\n\t" \
    "ds_read_b128 v[64:67], v8\n\t" \
    "v_smfmac_f32_16x16x64_bf16 v[84:87], v[40:43], a[240:247], v10\n\t" \
    "ds_read_b128 v[68:71], v9\n\t" \
    "s_waitcnt lgkmcnt(5)\n\t" \
    "v_smfmac_f32_16x16x64_bf16 v[72:75], v[48:51], a[32:39], v10\n\t" \
    "ds_read_b128 v[44:47], v0 offset:19456\n\t" \
    "s_waitcnt lgkmcnt(5)\n\t" \
    "v_smfmac_f32_16x16x64_bf16 v[72:75], v[52:55], a[40:47], v10\n\t" \
    "s_waitcnt lgkmcnt(4)\n\t" \
    "v_smfmac_f32_16x16x64_bf16 v[72:75], v[56:59], a[48:55], v10\n\t" \
    "s_waitcnt lgkmcnt(3)\n\t" \
    "v_smfmac_f32_16x16x64_bf16 v[72:75], v[60:63], a[56:63], v10\n\t" \
    "s_waitcnt lgkmcnt(2)\n\t" \
    "v_smfmac_f32_16x16x64_bf16 v[72:75], v[64:67], a[0:7], v10\n\t" \
    "s_waitcnt lgkmcnt(1)\n\t" \
    "v_smfmac_f32_16x16x64_bf16 v[72:75], v[68:71], a[8:15], v10\n\t" \
    "s_waitcnt lgkmcnt(0)\n\t" \
    "v_smfmac_f32_16x16x64_bf16 v[72:75], v[44:47], a[64:71], v10\n\t" \
    "s_nop 2\n\t" \
    "v_smfmac_f32_16x16x64_bf16 v[76:79], v[48:51], a[104:111], v10\n\t" \
    "s_nop 2\n\t" \
    "v_smfmac_f32_16x16x64_bf16 v[76:79], v[52:55], a[112:119], v10\n\t" \
    "v_add_f32 v72, v72, v74\n\t" \
    "v_smfmac_f32_16x16x64_bf16 v[76:79], v[56:59], a[120:127], v10\n\t" \
    "v_add_f32 v73, v73, v75\n\t" \
    "v_smfmac_f32_16x16x64_bf16 v[76:79], v[60:63], a[128:135], v10\n\t" \
    "v_cvt_pk_bf16_f32 v156, v72, v73\n\t" \
    "v_smfmac_f32_16x16x64_bf16 v[76:79], v[64:67], a[72:79], v10\n\t" \
    "ds_write_b32 v12, v156 offset:0\n\t" \
    "v_smfmac_f32_16x16x64_bf16 v[76:79], v[68:71], a[80:87], v10\n\t" \
    "v_smfmac_f32_16x16x64_bf16 v[76:79], v[44:47], a[136:143], v10\n\t" \
    "s_nop 2\n\t" \
    "v_smfmac_f32_16x16x64_bf16 v[80:83], v[48:51], a[176:183], v10\n\t" \
    "s_nop 2\n\t" \
    "v_smfmac_f32_16x16x64_bf16 v[80:83], v[52:55], a[184:191], v10\n\t" \
    "v_add_f32 v76, v76, v78\n\t" \
    "v_smfmac_f32_16x16x64_bf16 v[80:83], v[56:59], a[192:199], v10\n\t" \
    "v_add_f32 v77, v77, v79\n\t" \
    "v_smfmac_f32_16x16x64_bf16 v[80:83], v[60:63], a[200:207], v10\n\t" \
    "v_cvt_pk_bf16_f32 v157, v76, v77\n\t" \
    "v_smfmac_f32_16x16x64_bf16 v[80:83], v[64:67], a[144:151], v10\n\t" \
    "ds_write_b32 v12, v157 offset:64\n\t" \
    "v_smfmac_f32_16x16x64_bf16 v[80:83], v[68:71], a[152:159], v10\n\t" \
    "v_smfmac_f32_16x16x64_bf16 v[80:83], v[44:47], a[208:215], v10\n\t" \
    "s_nop 2\n\t" \
    "v_smfmac_f32_16x16x64_bf16 v[84:87], v[48:51], a[248:255], v10\n\t" \
    "s_nop 2\n\t" \
    "v_smfmac_f32_16x16x64_bf16 v[84:87], v[52:55], v[224:231], v10\n\t" \
    "v_add_f32 v80, v80, v82\n\t" \
    "v_smfmac_f32_16x16x64_bf16 v[84:87], v[56:59], v[232:239], v10\n\t" \
    "v_add_f32 v81, v81, v83\n\t" \
    "v_smfmac_f32_16x16x64_bf16 v[84:87], v[60:63], v[240:247], v10\n\t" \
    "v_cvt_pk_bf16_f32 v158, v80, v81\n\t" \
    "v_smfmac_f32_16x16x64_bf16 v[84:87], v[64:67], a[216:223], v10\n\t" \
    "ds_write_b32 v16, v158 offset:0\n\t" \
    "v_smfmac_f32_16x16x64_bf16 v[84:87], v[68:71], a[224:231], v10\n\t" \
    "ds_read_b64_tr_b16 v[132:133], v13 offset:0\n\t" \
    "ds_read_b64_tr_b16 v[134:135], v13 offset:128\n\t" \
    "v_smfmac_f32_16x16x64_bf16 v[84:87], v[44:47], v[248:255], v10\n\t" \
    "ds_read_b64_tr_b16 v[136:137], v13 offset:1024\n\t" \
    "ds_read_b64_tr_b16 v[138:139], v13 offset:1152\n\t" \
    "s_nop 4\n\t" \
    "v_cmp_u_f32 vcc, v72, v73\n\t" \
    "v_pk_add_f32 v[84:85], v[84:85], v[86:87]\n\t" \
    "v_cvt_pk_bf16_f32 v159, v84, v85\n\t" \
    "ds_write_b32 v16, v159 offset:256\n\t" \
    "s_cbranch_vccnz qk_Gslow44_%=\n\t" \
    "qk_Gback45_%=:\n\t" \
    "s_waitcnt lgkmcnt(3)\n\t" \
    "v_smfmac_f32_16x16x64_bf16 v[88:91], v[132:135], v[100:107], v10\n\t" \
    "s_waitcnt lgkmcnt(1)\n\t" \
    "v_smfmac_f32_16x16x64_bf16 v[88:91], v[136:139], v[108:115], v10\n\t" \
    "s_waitcnt lgkmcnt(0)\n\t" \
    "s_barrier\n\t" \
    "ds_read_b64_tr_b16 v[140:141], v17 offset:0\n\t" \
    "v_smfmac_f32_16x16x64_bf16 v[92:95], v[132:135], v[116:123], v10\n\t" \
    "ds_read_b64_tr_b16 v[142:143], v17 offset:256\n\t" \
    "ds_read_b64_tr_b16 v[144:145], v17 offset:512\n\t" \
    "ds_read_b64_tr_b16 v[146:147], v17 offset:768\n\t" \
    "ds_read_b64_tr_b16 v[148:149], v17 offset:1024\n\t" \
    "v_smfmac_f32_16x16x64_bf16 v[92:95], v[136:139], v[124:131], v10\n\t" \
    "ds_read_b64_tr_b16 v[150:151], v17 offset:1280\n\t" \
    "ds_read_b64_tr_b16 v[152:153], v17 offset:1536\n\t" \
    "ds_read_b64_tr_b16 v[154:155], v17 offset:1792\n\t" \
    "v_smfmac_f32_16x16x64_bf16 v[184:187], v[132:135], v[100:107], v10\n\t" \
    "qk_qb47_%=:\n\t" \
    "s_waitcnt lgkmcnt(4)\n\t" \
    "v_smfmac_f32_16x16x64_bf16 v[96:99], v[132:135], v[140:147], v10\n\t" \
    "s_waitcnt lgkmcnt(0)\n\t" \
    "v_smfmac_f32_16x16x64_bf16 v[96:99], v[136:139], v[148:155], v10\n\t" \
    "v_add_f32 v168, v89, v91\n\t" \
    "v_add_f32 v169, v93, v95\n\t" \
    "v_cmp_u_f32 vcc, v168, v169\n\t" \
    "v_cvt_pk_bf16_f32 v168, 0, v168\n\t" \
    "v_cvt_pk_bf16_f32 v169, 0, v169\n\t" \
    "v_mul_f32 v168, v168, v26\n\t" \
    "v_mul_f32 v169, v169, v26\n\t" \
    "v_cvt_pk_bf16_f32 v168, 0, v168\n\t" \
    "v_add_f32 v172, v97, v99\n\t" \
    "v_cvt_pk_bf16_f32 v169, 0, v169\n\t" \
    "v_max_f32 v178, v168, v169\n\t" \
    "v_cvt_pk_bf16_f32 v172, 0, v172\n\t" \
    "v_mul_f32 v172, v172, v26\n\t" \
    "v_max_f32_dpp v178, v178, v178 row_mirror row_mask:0xf bank_mask:0xf\n\t" \
    "v_cvt_pk_bf16_f32 v172, 0, v172\n\t" \
    "v_max_f32 v173, v178, v172\n\t" \
    "v_mul_f32 v168, v168, v28\n\t" \
    "v_mul_f32 v169, v169, v28\n\t" \
    "v_max_f32_dpp v173, v173, v173 quad_perm:[1,0,3,2] row_mask:0xf bank_mask:0xf\n\t" \
    "v_mul_f32 v172, v172, v28\n\t" \
    "s_cbranch_vccnz qk_qs46_%=\n\t" \
    "qk_qb248_%=:\n\t" \
    "v_max_f32_dpp v173, v173, v173 quad_perm:[2,3,0,1] row_mask:0xf bank_mask:0xf\n\t" \
    "s_nop 1\n\t" \
    "v_max_f32_dpp v173, v173, v173 row_half_mirror row_mask:0xf bank_mask:0xf\n\t" \
    "v_fma_f32 v172, -v173, v28, v172\n\t" \
    "v_fma_f32 v168, -v173, v28, v168\n\t" \
    "v_fma_f32 v169, -v173, v28, v169\n\t" \
    "v_exp_f32 v172, v172\n\t" \
    "v_exp_f32 v168, v168\n\t" \
    "v_exp_f32 v169, v169\n\t" \
    "v_fma_f32 v173, v172, 0.5, v168\n\t" \
    "v_add_f32 v173, v173, v169\n\t" \
    "v_mov_b32_dpp v174, v172 quad_perm:[0,0,2,2] row_mask:0xf bank_mask:0xf\n\t" \
    "v_mov_b32_dpp v175, v172 quad_perm:[1,1,3,3] row_mask:0xf bank_mask:0xf\n\t" \
    "v_add_f32_dpp v173, v173, v173 quad_perm:[1,0,3,2] row_mask:0xf bank_mask:0xf\n\t" \
    "v_mov_b32_dpp v176, v168 quad_perm:[1,0,3,2] row_mask:0xf bank_mask:0xf\n\t" \
    "v_mov_b32_dpp v177, v169 quad_perm:[1,0,3,2] row_mask:0xf bank_mask:0xf\n\t" \
    "v_add_f32_dpp v173, v173, v173 quad_perm:[2,3,0,1] row_mask:0xf bank_mask:0xf\n\t" \
    "v_cndmask_b32 v168, v168, v177, s[48:49]\n\t" \
    "v_cndmask_b32 v169, v176, v169, s[48:49]\n\t" \
    "v_add_f32_dpp v173, v173, v173 row_half_mirror row_mask:0xf bank_mask:0xf\n\t" \
    "s_nop 1\n\t" \
    "v_add_f32_dpp v173, v173, v173 row_mirror row_mask:0xf bank_mask:0xf\n\t" \
    "v_rcp_f32 v173, v173\n\t" \
    "s_nop 0\n\t" \
    "v_pk_mul_f32 v[168:169], v[168:169], v[172:173] op_sel:[0,1] op_sel_hi:[1,1]\n\t" \
    "v_cvt_pk_bf16_f32 v176, v168, v169\n\t" \
    "global_store_dword v18, v176, s[46:47] sc1\n\t" \
    "global_store_dword v180, v176, s[46:47] sc1\n\t" \
    "global_store_dword v181, v176, s[46:47] sc1\n\t" \
    "global_store_dword v182, v176, s[46:47] sc1\n\t" \
    "v_pk_mul_f32 v[174:175], v[174:175], v[172:173] op_sel:[0,1] op_sel_hi:[1,1]\n\t" \
    "v_cvt_pk_bf16_f32 v177, v174, v175\n\t" \
    "global_store_dword v19, v177, s[46:47] sc1\n\t" \
    "s_cmp_eq_u32 s65, 63\n\t" \
    "s_cbranch_scc1 qk_last36_%=\n\t" \
    "v_readfirstlane_b32 s69, v25\n\t" \
    "s_add_u32 s65, s65, 1\n\t" \
    "s_max_i32 s83, s83, s69\n\t" \
    "s_cmp_gt_u32 s65, 6\n\t" \
    "s_cselect_b32 s83, 99, s83\n\t" \
    "v_mov_b32 v72, 0\n\t" \
    "v_mov_b32 v73, 0\n\t" \
    "v_mov_b32 v74, 0\n\t" \
    "v_mov_b32 v75, 0\n\t" \
    "v_mov_b32 v76, 0\n\t" \
    "v_mov_b32 v77, 0\n\t" \
    "v_mov_b32 v78, 0\n\t" \
    "v_mov_b32 v79, 0\n\t" \
    "v_mov_b32 v80, 0\n\t" \
    "v_mov_b32 v81, 0\n\t" \
    "v_mov_b32 v82, 0\n\t" \
    "v_mov_b32 v83, 0\n\t" \
    "v_mov_b32 v84, 0\n\t" \
    "v_mov_b32 v85, 0\n\t" \
    "v_mov_b32 v86, 0\n\t" \
    "v_mov_b32 v87, 0\n\t" \
    "v_mov_b32 v88, 0\n\t" \
    "v_mov_b32 v89, 0\n\t" \
    "v_mov_b32 v90, 0\n\t" \
    "v_mov_b32 v91, 0\n\t" \
    "v_mov_b32 v92, 0\n\t" \
    "v_mov_b32 v93, 0\n\t" \
    "v_mov_b32 v94, 0\n\t" \
    "v_mov_b32 v95, 0\n\t" \
    "v_mov_b32 v96, 0\n\t" \
    "v_mov_b32 v97, 0\n\t" \
    "v_mov_b32 v98, 0\n\t" \
    "v_mov_b32 v99, 0\n\t" \
    "s_add_u32 s69, s65, 0\n\t" \
    "s_and_b32 s69, s69, 7\n\t" \
    "s_lshl_b32 s69, s69, 18\n\t" \
    "s_add_u32 s69, s69, s62\n\t" \
    "s_add_u32 s46, s36, s69\n\t" \
    "s_addc_u32 s47, s37, 0\n\t" \
    "s_mul_i32 s70, s67, 7\n\t" \
    "s_add_u32 s69, s65, 0\n\t" \
    "s_xor_b32 s69, s69, s70\n\t" \
    "s_and_b32 s69, s69, 63\n\t" \
    "s_lshl_b32 s69, s69, 13\n\t" \
    "s_mul_i32 s70, s67, 524288\n\t" \
    "s_add_u32 s69, s69, s70\n\t" \
    "s_add_u32 s69, s69, 23068672\n\t" \
    "s_add_u32 s38, s36, s69\n\t" \
    "s_addc_u32 s39, s37, 0\n\t" \
    "qk_WF49_%=:\n\t" \
    "s_cmp_ge_i32 s83, s65\n\t" \
    "s_cbranch_scc1 qk_WFok50_%=\n\t" \
    "global_load_dword v160, v24, s[36:37] sc1\n\t" \
    "s_waitcnt vmcnt(0)\n\t" \
    "v_readfirstlane_b32 s83, v160\n\t" \
    "s_nop 1\n\t" \
    "s_cmp_ge_i32 s83, s65\n\t" \
    "s_cbranch_scc1 qk_WFok50_%=\n\t" \
    "s_cmp_eq_u32 s68, 0\n\t" \
    "s_cbranch_scc1 qk_F51_%=\n\t" \
    "s_sub_u32 s68, s68, 1\n\t" \
    "s_sleep 2\n\t" \
    "s_branch qk_WF49_%=\n\t" \
    "qk_F51_%=:\n\t" \
    "v_mov_b32 v160, 5767424\n\t" \
    "v_mov_b32 v161, 9\n\t" \
    "global_store_dword v160, v161, s[36:37] sc1\n\t" \
    "s_waitcnt vmcnt(0)\n\t" \
    "s_branch qk_WFok50_%=\n\t" \
    "qk_WFok50_%=:\n\t" \
    "global_load_dwordx4 v[100:103], v0, s[38:39] offset:0 sc1\n\t" \
    "global_load_dwordx4 v[104:107], v0, s[38:39] offset:2048 sc1\n\t" \
    "global_load_dwordx4 v[108:111], v15, s[38:39] offset:0 sc1\n\t" \
    "global_load_dwordx4 v[112:115], v15, s[38:39] offset:2048 sc1\n\t" \
    "global_load_dwordx4 v[116:119], v0, s[38:39] offset:1024 sc1\n\t" \
    "global_load_dwordx4 v[120:123], v0, s[38:39] offset:3072 sc1\n\t" \
    "global_load_dwordx4 v[124:127], v15, s[38:39] offset:1024 sc1\n\t" \
    "global_load_dwordx4 v[128:131], v15, s[38:39] offset:3072 sc1\n\t" \
    "s_cmp_gt_i32 s83, s65\n\t" \
    "s_cbranch_scc1 qk_nf52_%=\n\t" \
    "global_load_dword v25, v24, s[36:37] sc1\n\t" \
    "qk_nf52_%=:\n\t" \
    "s_barrier\n\t" \
    "ds_read_b32 v168, v11 offset:20480\n\t" \
    "s_waitcnt lgkmcnt(0)\n\t" \
    "v_readfirstlane_b32 s70, v168\n\t" \
    "ds_write_b32 v12, v31 offset:0\n\t" \
    "ds_write_b32 v12, v31 offset:64\n\t" \
    "s_branch qk_go37_%=\n\t" \
    "qk_Gbad42_%=:\n\t" \
    "s_waitcnt lgkmcnt(0)\n\t" \
    "s_nop 7\n\t" \
    "v_mov_b32 v72, 0\n\t" \
    "v_mov_b32 v73, 0\n\t" \
    "v_mov_b32 v74, 0\n\t" \
    "v_mov_b32 v75, 0\n\t" \
    "v_mov_b32 v76, 0\n\t" \
    "v_mov_b32 v77, 0\n\t" \
    "v_mov_b32 v78, 0\n\t" \
    "v_mov_b32 v79, 0\n\t" \
    "v_mov_b32 v80, 0\n\t" \
    "v_mov_b32 v81, 0\n\t" \
    "v_mov_b32 v82, 0\n\t" \
    "v_mov_b32 v83, 0\n\t" \
    "v_mov_b32 v84, 0\n\t" \
    "v_mov_b32 v85, 0\n\t" \
    "v_mov_b32 v86, 0\n\t" \
    "v_mov_b32 v87, 0\n\t" \
    "s_cmp_eq_u32 s68, 0\n\t" \
    "s_cbranch_scc1 qk_F53_%=\n\t" \
    "s_sub_u32 s68, s68, 1\n\t" \
    "s_branch qk_G41_%=\n\t" \
    "qk_F53_%=:\n\t" \
    "v_mov_b32 v160, 5767424\n\t" \
    "v_mov_b32 v161, 1\n\t" \
    "global_store_dword v160, v161, s[36:37] sc1\n\t" \
    "s_waitcnt vmcnt(0)\n\t" \
    "s_branch qk_Gc43_%=\n\t" \
    "qk_Gslow44_%=:\n\t" \
    "v_max_u32 v160, v36, v37\n\t" \
    "v_max3_u32 v160, v160, v38, v39\n\t" \
    "v_max3_u32 v160, v160, v40, v41\n\t" \
    "v_max3_u32 v160, v160, v42, v43\n\t" \
    "v_max3_u32 v160, v160, v44, v45\n\t" \
    "v_max3_u32 v160, v160, v46, v47\n\t" \
    "v_max3_u32 v160, v160, v48, v49\n\t" \
    "v_max3_u32 v160, v160, v50, v51\n\t" \
    "v_max3_u32 v160, v160, v52, v53\n\t" \
    "v_max3_u32 v160, v160, v54, v55\n\t" \
    "v_max3_u32 v160, v160, v56, v57\n\t" \
    "v_max3_u32 v160, v160, v58, v59\n\t" \
    "v_max3_u32 v160, v160, v60, v61\n\t" \
    "v_max3_u32 v160, v160, v62, v63\n\t" \
    "v_max3_u32 v160, v160, v64, v65\n\t" \
    "v_max3_u32 v160, v160, v66, v67\n\t" \
    "v_max3_u32 v160, v160, v68, v69\n\t" \
    "v_max3_u32 v160, v160, v70, v71\n\t" \
    "v_cmp_eq_u32 vcc, -1, v160\n\t" \
    "s_cbranch_vccz qk_Gback45_%=\n\t" \
    "s_waitcnt lgkmcnt(0)\n\t" \
    "v_sub_u32 v160, v1, v3\n\t" \
    "s_nop 0\n\t" \
    "v_readfirstlane_b32 s69, v160\n\t" \
    "v_add_u32 v163, 20480, v0\n\t" \
    "s_nop 7\n\t" \
    "s_add_u32 s30, s46, s69\n\t" \
    "s_addc_u32 s31, s47, 0\n\t" \
    "qk_Gag54_%=:\n\t" \
    "global_load_dwordx4 v[36:39], v1, s[46:47] sc1\n\t" \
    "global_load_dwordx4 v[40:43], v23, s[46:47] sc1\n\t" \
    "global_load_dwordx4 v[44:47], v163, s[30:31] sc1\n\t" \
    "global_load_dwordx4 v[48:51], v4, s[30:31] sc1\n\t" \
    "global_load_dwordx4 v[52:55], v5, s[30:31] sc1\n\t" \
    "global_load_dwordx4 v[56:59], v6, s[30:31] sc1\n\t" \
    "global_load_dwordx4 v[60:63], v7, s[30:31] sc1\n\t" \
    "global_load_dwordx4 v[64:67], v8, s[30:31] sc1\n\t" \
    "global_load_dwordx4 v[68:71], v9, s[30:31] sc1\n\t" \
    "s_waitcnt vmcnt(0)\n\t" \
    "v_max_u32 v160, v36, v37\n\t" \
    "v_max3_u32 v160, v160, v38, v39\n\t" \
    "v_max3_u32 v160, v160, v40, v41\n\t" \
    "v_max3_u32 v160, v160, v42, v43\n\t" \
    "v_max3_u32 v160, v160, v44, v45\n\t" \
    "v_max3_u32 v160, v160, v46, v47\n\t" \
    "v_max3_u32 v160, v160, v48, v49\n\t" \
    "v_max3_u32 v160, v160, v50, v51\n\t" \
    "v_max3_u32 v160, v160, v52, v53\n\t" \
    "v_max3_u32 v160, v160, v54, v55\n\t" \
    "v_max3_u32 v160, v160, v56, v57\n\t" \
    "v_max3_u32 v160, v160, v58, v59\n\t" \
    "v_max3_u32 v160, v160, v60, v61\n\t" \
    "v_max3_u32 v160, v160, v62, v63\n\t" \
    "v_max3_u32 v160, v160, v64, v65\n\t" \
    "v_max3_u32 v160, v160, v66, v67\n\t" \
    "v_max3_u32 v160, v160, v68, v69\n\t" \
    "v_max3_u32 v160, v160, v70, v71\n\t" \
    "v_cmp_eq_u32 vcc, -1, v160\n\t" \
    "s_cbranch_vccz qk_Gredo55_%=\n\t" \
    "s_cmp_eq_u32 s68, 0\n\t" \
    "s_cbranch_scc1 qk_F56_%=\n\t" \
    "s_sub_u32 s68, s68, 1\n\t" \
    "s_sleep 1\n\t" \
    "s_branch qk_Gag54_%=\n\t" \
    "qk_F56_%=:\n\t" \
    "v_mov_b32 v160, 5767424\n\t" \
    "v_mov_b32 v161, 2\n\t" \
    "global_store_dword v160, v161, s[36:37] sc1\n\t" \
    "s_waitcnt vmcnt(0)\n\t" \
    "s_branch qk_Gredo55_%=\n\t" \
    "qk_Gredo55_%=:\n\t" \
    "v_mov_b32 v72, 0\n\t" \
    "v_mov_b32 v73, 0\n\t" \
    "v_mov_b32 v74, 0\n\t" \
    "v_mov_b32 v75, 0\n\t" \
    "v_mov_b32 v76, 0\n\t" \
    "v_mov_b32 v77, 0\n\t" \
    "v_mov_b32 v78, 0\n\t" \
    "v_mov_b32 v79, 0\n\t" \
    "v_mov_b32 v80, 0\n\t" \
    "v_mov_b32 v81, 0\n\t" \
    "v_mov_b32 v82, 0\n\t" \
    "v_mov_b32 v83, 0\n\t" \
    "v_mov_b32 v84, 0\n\t" \
    "v_mov_b32 v85, 0\n\t" \
    "v_mov_b32 v86, 0\n\t" \
    "v_mov_b32 v87, 0\n\t" \
    "v_smfmac_f32_16x16x64_bf16 v[72:75], v[36:39], a[16:23], v10\n\t" \
    "v_smfmac_f32_16x16x64_bf16 v[72:75], v[40:43], a[24:31], v10\n\t" \
    "v_smfmac_f32_16x16x64_bf16 v[72:75], v[48:51], a[32:39], v10\n\t" \
    "v_smfmac_f32_16x16x64_bf16 v[72:75], v[52:55], a[40:47], v10\n\t" \
    "v_smfmac_f32_16x16x64_bf16 v[72:75], v[56:59], a[48:55], v10\n\t" \
    "v_smfmac_f32_16x16x64_bf16 v[72:75], v[60:63], a[56:63], v10\n\t" \
    "v_smfmac_f32_16x16x64_bf16 v[72:75], v[64:67], a[0:7], v10\n\t" \
    "v_smfmac_f32_16x16x64_bf16 v[72:75], v[68:71], a[8:15], v10\n\t" \
    "v_smfmac_f32_16x16x64_bf16 v[72:75], v[44:47], a[64:71], v10\n\t" \
    "v_smfmac_f32_16x16x64_bf16 v[76:79], v[36:39], a[88:95], v10\n\t" \
    "v_smfmac_f32_16x16x64_bf16 v[76:79], v[40:43], a[96:103], v10\n\t" \
    "v_smfmac_f32_16x16x64_bf16 v[76:79], v[48:51], a[104:111], v10\n\t" \
    "v_smfmac_f32_16x16x64_bf16 v[76:79], v[52:55], a[112:119], v10\n\t" \
    "v_smfmac_f32_16x16x64_bf16 v[76:79], v[56:59], a[120:127], v10\n\t" \
    "v_smfmac_f32_16x16x64_bf16 v[76:79], v[60:63], a[128:135], v10\n\t" \
    "v_smfmac_f32_16x16x64_bf16 v[76:79], v[64:67], a[72:79], v10\n\t" \
    "v_smfmac_f32_16x16x64_bf16 v[76:79], v[68:71], a[80:87], v10\n\t" \
    "v_smfmac_f32_16x16x64_bf16 v[76:79], v[44:47], a[136:143], v10\n\t" \
    "v_smfmac_f32_16x16x64_bf16 v[80:83], v[36:39], a[160:167], v10\n\t" \
    "v_smfmac_f32_16x16x64_bf16 v[80:83], v[40:43], a[168:175], v10\n\t" \
    "v_smfmac_f32_16x16x64_bf16 v[80:83], v[48:51], a[176:183], v10\n\t" \
    "v_smfmac_f32_16x16x64_bf16 v[80:83], v[52:55], a[184:191], v10\n\t" \
    "v_smfmac_f32_16x16x64_bf16 v[80:83], v[56:59], a[192:199], v10\n\t" \
    "v_smfmac_f32_16x16x64_bf16 v[80:83], v[60:63], a[200:207], v10\n\t" \
    "v_smfmac_f32_16x16x64_bf16 v[80:83], v[64:67], a[144:151], v10\n\t" \
    "v_smfmac_f32_16x16x64_bf16 v[80:83], v[68:71], a[152:159], v10\n\t" \
    "v_smfmac_f32_16x16x64_bf16 v[80:83], v[44:47], a[208:215], v10\n\t" \
    "v_smfmac_f32_16x16x64_bf16 v[84:87], v[36:39], a[232:239], v10\n\t" \
    "v_smfmac_f32_16x16x64_bf16 v[84:87], v[40:43], a[240:247], v10\n\t" \
    "v_smfmac_f32_16x16x64_bf16 v[84:87], v[48:51], a[248:255], v10\n\t" \
    "v_smfmac_f32_16x16x64_bf16 v[84:87], v[52:55], v[224:231], v10\n\t" \
    "v_smfmac_f32_16x16x64_bf16 v[84:87], v[56:59], v[232:239], v10\n\t" \
    "v_smfmac_f32_16x16x64_bf16 v[84:87], v[60:63], v[240:247], v10\n\t" \
    "v_smfmac_f32_16x16x64_bf16 v[84:87], v[64:67], a[216:223], v10\n\t" \
    "v_smfmac_f32_16x16x64_bf16 v[84:87], v[68:71], a[224:231], v10\n\t" \
    "v_smfmac_f32_16x16x64_bf16 v[84:87], v[44:47], v[248:255], v10\n\t" \
    "v_add_f32 v72, v72, v74\n\t" \
    "v_add_f32 v73, v73, v75\n\t" \
    "v_cvt_pk_bf16_f32 v156, v72, v73\n\t" \
    "ds_write_b32 v12, v156 offset:0\n\t" \
    "v_add_f32 v76, v76, v78\n\t" \
    "v_add_f32 v77, v77, v79\n\t" \
    "v_cvt_pk_bf16_f32 v157, v76, v77\n\t" \
    "ds_write_b32 v12, v157 offset:64\n\t" \
    "v_add_f32 v80, v80, v82\n\t" \
    "v_add_f32 v81, v81, v83\n\t" \
    "v_cvt_pk_bf16_f32 v158, v80, v81\n\t" \
    "ds_write_b32 v16, v158 offset:0\n\t" \
    "v_pk_add_f32 v[84:85], v[84:85], v[86:87]\n\t" \
    "v_cvt_pk_bf16_f32 v159, v84, v85\n\t" \
    "ds_write_b32 v16, v159 offset:256\n\t" \
    "s_branch qk_Gback45_%=\n\t" \
    "qk_qs46_%=:\n\t" \
    "s_cmp_eq_u32 s91, s65\n\t" \
    "s_cbranch_scc1 qk_qb248_%=\n\t" \
    "s_mov_b32 s91, s65\n\t" \
    "s_nop 7\n\t" \
    "ds_read_b64_tr_b16 v[132:133], v13 offset:0\n\t" \
    "ds_read_b64_tr_b16 v[134:135], v13 offset:128\n\t" \
    "ds_read_b64_tr_b16 v[136:137], v13 offset:1024\n\t" \
    "ds_read_b64_tr_b16 v[138:139], v13 offset:1152\n\t" \
    "v_mov_b32 v88, 0\n\t" \
    "v_mov_b32 v89, 0\n\t" \
    "v_mov_b32 v90, 0\n\t" \
    "v_mov_b32 v91, 0\n\t" \
    "v_mov_b32 v92, 0\n\t" \
    "v_mov_b32 v93, 0\n\t" \
    "v_mov_b32 v94, 0\n\t" \
    "v_mov_b32 v95, 0\n\t" \
    "v_mov_b32 v96, 0\n\t" \
    "v_mov_b32 v97, 0\n\t" \
    "v_mov_b32 v98, 0\n\t" \
    "v_mov_b32 v99, 0\n\t" \
    "s_waitcnt lgkmcnt(0)\n\t" \
    "v_smfmac_f32_16x16x64_bf16 v[88:91], v[132:135], v[100:107], v10\n\t" \
    "v_smfmac_f32_16x16x64_bf16 v[92:95], v[132:135], v[116:123], v10\n\t" \
    "v_smfmac_f32_16x16x64_bf16 v[88:91], v[136:139], v[108:115], v10\n\t" \
    "v_smfmac_f32_16x16x64_bf16 v[92:95], v[136:139], v[124:131], v10\n\t" \
    "s_nop 7\n\t" \
    "s_branch qk_qb47_%=\n\t" \
    "qk_last36_%=:\n\t" \
    "s_endpgm\n\t" \
    "qk_loop13_%=:\n\t" \
    "v_mov_b32 v72, 0\n\t" \
    "v_mov_b32 v73, 0\n\t" \
    "v_mov_b32 v74, 0\n\t" \
    "v_mov_b32 v75, 0\n\t" \
    "v_mov_b32 v76, 0\n\t" \
    "v_mov_b32 v77, 0\n\t" \
    "v_mov_b32 v78, 0\n\t" \
    "v_mov_b32 v79, 0\n\t" \
    "v_mov_b32 v80, 0\n\t" \
    "v_mov_b32 v81, 0\n\t" \
    "v_mov_b32 v82, 0\n\t" \
    "v_mov_b32 v83, 0\n\t" \
    "v_mov_b32 v84, 0\n\t" \
    "v_mov_b32 v85, 0\n\t" \
    "v_mov_b32 v86, 0\n\t" \
    "v_mov_b32 v87, 0\n\t" \
    "s_add_u32 s69, s65, 0\n\t" \
    "s_and_b32 s69, s69, 7\n\t" \
    "s_lshl_b32 s69, s69, 18\n\t" \
    "s_add_u32 s69, s69, s62\n\t" \
    "s_add_u32 s46, s36, s69\n\t" \
    "s_addc_u32 s47, s37, 0\n\t" \
    "s_mul_i32 s70, s67, 7\n\t" \
    "s_add_u32 s69, s65, 0\n\t" \
    "s_xor_b32 s69, s69, s70\n\t" \
    "s_and_b32 s69, s69, 63\n\t" \
    "s_lshl_b32 s69, s69, 12\n\t" \
    "s_mul_i32 s70, s67, 262144\n\t" \
    "s_add_u32 s69, s69, s70\n\t" \
    "s_add_u32 s69, s69, 39845888\n\t" \
    "s_add_u32 s40, s36, s69\n\t" \
    "s_addc_u32 s41, s37, 0\n\t" \
    "s_sub_u32 s91, s65, 1\n\t" \
    "v_mov_b32 v25, s91\n\t" \
    "s_sub_u32 s70, s79, s74\n\t" \
    "s_max_i32 s70, s70, 0\n\t" \
    "s_min_i32 s70, s70, 16000\n\t" \
    "s_lshl_b32 s70, s70, 1\n\t" \
    "s_mul_hi_u32 s70, s70, s77\n\t" \
    "s_add_u32 s70, s70, s73\n\t" \
    ".p2align 6\n\t" \
    "s_nop 0\n\t" \
    "s_nop 0\n\t" \
    "s_nop 0\n\t" \
    "s_nop 0\n\t" \
    "s_nop 0\n\t" \
    "s_nop 0\n\t" \
    "qk_go58_%=:\n\t" \
    "s_getpc_b64 s[30:31]\n\t" \
    "qk_Wp62_%=:\n\t" \
    "s_add_u32 s30, s30, qk_We63_%=-qk_Wp62_%=\n\t" \
    "s_addc_u32 s31, s31, 0\n\t" \
    "s_sub_u32 s70, s70, 96\n\t" \
    "qk_W61_%=:\n\t" \
    "s_memtime s[56:57]\n\t" \
    "s_waitcnt lgkmcnt(0)\n\t" \
    "s_sub_u32 s69, s70, s56\n\t" \
    "s_cmp_gt_i32 s69, 80\n\t" \
    "s_cbranch_scc1 qk_W61_%=\n\t" \
    "s_max_i32 s69, s69, 0\n\t" \
    "s_and_b32 s69, s69, -4\n\t" \
    "s_sub_u32 s30, s30, s69\n\t" \
    "s_subb_u32 s31, s31, 0\n\t" \
    "s_setpc_b64 s[30:31]\n\t" \
    "s_nop 0\n\t" \
    "s_nop 0\n\t" \
    "s_nop 0\n\t" \
    "s_nop 0\n\t" \
    "s_nop 0\n\t" \
    "s_nop 0\n\t" \
    "s_nop 0\n\t" \
    "s_nop 0\n\t" \
    "s_nop 0\n\t" \
    "s_nop 0\n\t" \
    "s_nop 0\n\t" \
    "s_nop 0\n\t" \
    "s_nop 0\n\t" \
    "s_nop 0\n\t" \
    "s_nop 0\n\t" \
    "s_nop 0\n\t" \
    "s_nop 0\n\t" \
    "s_nop 0\n\t" \
    "s_nop 0\n\t" \
    "s_nop 0\n\t" \
    "qk_We63_%=:\n\t" \
    "qk_G64_%=:\n\t" \
    "global_load_dwordx4 v[36:39], v1, s[46:47] sc1\n\t" \
    "global_load_dword v161, v2, s[46:47] sc1\n\t" \
    "global_load_dwordx4 v[40:43], v23, s[46:47] sc1\n\t" \
    "s_waitcnt vmcnt(2)\n\t" \
    "ds_write_b128 v3, v[36:39]\n\t" \
    "v_smfmac_f32_16x16x64_bf16 v[72:75], v[36:39], a[32:39], v10\n\t" \
    "s_waitcnt vmcnt(1)\n\t" \
    "ds_write_b32 v22, v161 offset:19968\n\t" \
    "v_smfmac_f32_16x16x64_bf16 v[76:79], v[36:39], a[104:111], v10\n\t" \
    "s_waitcnt vmcnt(0)\n\t" \
    "ds_write_b128 v3, v[40:43] offset:1024\n\t" \
    "v_max3_u32 v160, v36, v40, v161\n\t" \
    "v_cmp_eq_u32 vcc, -1, v160\n\t" \
    "v_smfmac_f32_16x16x64_bf16 v[80:83], v[36:39], a[176:183], v10\n\t" \
    "v_smfmac_f32_16x16x64_bf16 v[84:87], v[36:39], a[248:255], v10\n\t" \
    "v_smfmac_f32_16x16x64_bf16 v[72:75], v[40:43], a[40:47], v10\n\t" \
    "s_cbranch_vccnz qk_Gbad65_%=\n\t" \
    "qk_Gc66_%=:\n\t" \
    "s_cmp_ge_u32 s91, 57\n\t" \
    "s_cbranch_scc1 qk_kf59_%=\n\t" \
    "qk_kb60_%=:\n\t" \
    "s_waitcnt lgkmcnt(0)\n\t" \
    "s_barrier\n\t" \
    "ds_read_b128 v[48:51], v4\n\t" \
    "ds_read_b128 v[52:55], v5\n\t" \
    "v_smfmac_f32_16x16x64_bf16 v[76:79], v[40:43], a[112:119], v10\n\t" \
    "ds_read_b128 v[56:59], v6\n\t" \
    "ds_read_b128 v[60:63], v7\n\t" \
    "v_smfmac_f32_16x16x64_bf16 v[80:83], v[40:43], a[184:191], v10\n\t" \
    "ds_read_b128 v[64:67], v8\n\t" \
    "v_smfmac_f32_16x16x64_bf16 v[84:87], v[40:43], v[224:231], v10\n\t" \
    "ds_read_b128 v[68:71], v9\n\t" \
    "s_waitcnt lgkmcnt(5)\n\t" \
    "v_smfmac_f32_16x16x64_bf16 v[72:75], v[48:51], a[48:55], v10\n\t" \
    "ds_read_b128 v[44:47], v0 offset:19456\n\t" \
    "s_waitcnt lgkmcnt(5)\n\t" \
    "v_smfmac_f32_16x16x64_bf16 v[72:75], v[52:55], a[56:63], v10\n\t" \
    "s_waitcnt lgkmcnt(4)\n\t" \
    "v_smfmac_f32_16x16x64_bf16 v[72:75], v[56:59], a[0:7], v10\n\t" \
    "s_waitcnt lgkmcnt(3)\n\t" \
    "v_smfmac_f32_16x16x64_bf16 v[72:75], v[60:63], a[8:15], v10\n\t" \
    "s_waitcnt lgkmcnt(2)\n\t" \
    "v_smfmac_f32_16x16x64_bf16 v[72:75], v[64:67], a[16:23], v10\n\t" \
    "s_waitcnt lgkmcnt(1)\n\t" \
    "v_smfmac_f32_16x16x64_bf16 v[72:75], v[68:71], a[24:31], v10\n\t" \
    "s_waitcnt lgkmcnt(0)\n\t" \
    "v_smfmac_f32_16x16x64_bf16 v[72:75], v[44:47], a[64:71], v10\n\t" \
    "s_nop 2\n\t" \
    "v_smfmac_f32_16x16x64_bf16 v[76:79], v[48:51], a[120:127], v10\n\t" \
    "s_nop 2\n\t" \
    "v_smfmac_f32_16x16x64_bf16 v[76:79], v[52:55], a[128:135], v10\n\t" \
    "v_add_f32 v72, v72, v74\n\t" \
    "v_smfmac_f32_16x16x64_bf16 v[76:79], v[56:59], a[72:79], v10\n\t" \
    "v_add_f32 v73, v73, v75\n\t" \
    "v_smfmac_f32_16x16x64_bf16 v[76:79], v[60:63], a[80:87], v10\n\t" \
    "v_cvt_pk_bf16_f32 v156, v72, v73\n\t" \
    "v_smfmac_f32_16x16x64_bf16 v[76:79], v[64:67], a[88:95], v10\n\t" \
    "ds_write_b32 v12, v156 offset:0\n\t" \
    "v_smfmac_f32_16x16x64_bf16 v[76:79], v[68:71], a[96:103], v10\n\t" \
    "v_smfmac_f32_16x16x64_bf16 v[76:79], v[44:47], a[136:143], v10\n\t" \
    "s_nop 2\n\t" \
    "v_smfmac_f32_16x16x64_bf16 v[80:83], v[48:51], a[192:199], v10\n\t" \
    "s_nop 2\n\t" \
    "v_smfmac_f32_16x16x64_bf16 v[80:83], v[52:55], a[200:207], v10\n\t" \
    "v_add_f32 v76, v76, v78\n\t" \
    "v_smfmac_f32_16x16x64_bf16 v[80:83], v[56:59], a[144:151], v10\n\t" \
    "v_add_f32 v77, v77, v79\n\t" \
    "v_smfmac_f32_16x16x64_bf16 v[80:83], v[60:63], a[152:159], v10\n\t" \
    "v_cvt_pk_bf16_f32 v157, v76, v77\n\t" \
    "v_smfmac_f32_16x16x64_bf16 v[80:83], v[64:67], a[160:167], v10\n\t" \
    "ds_write_b32 v12, v157 offset:64\n\t" \
    "v_smfmac_f32_16x16x64_bf16 v[80:83], v[68:71], a[168:175], v10\n\t" \
    "v_smfmac_f32_16x16x64_bf16 v[80:83], v[44:47], a[208:215], v10\n\t" \
    "s_nop 2\n\t" \
    "v_smfmac_f32_16x16x64_bf16 v[84:87], v[48:51], v[232:239], v10\n\t" \
    "s_nop 2\n\t" \
    "v_smfmac_f32_16x16x64_bf16 v[84:87], v[52:55], v[240:247], v10\n\t" \
    "v_add_f32 v80, v80, v82\n\t" \
    "v_smfmac_f32_16x16x64_bf16 v[84:87], v[56:59], a[216:223], v10\n\t" \
    "v_add_f32 v81, v81, v83\n\t" \
    "v_smfmac_f32_16x16x64_bf16 v[84:87], v[60:63], a[224:231], v10\n\t" \
    "v_cvt_pk_bf16_f32 v158, v80, v81\n\t" \
    "v_smfmac_f32_16x16x64_bf16 v[84:87], v[64:67], a[232:239], v10\n\t" \
    "ds_write_b32 v16, v158 offset:0\n\t" \
    "v_smfmac_f32_16x16x64_bf16 v[84:87], v[68:71], a[240:247], v10\n\t" \
    "v_smfmac_f32_16x16x64_bf16 v[84:87], v[44:47], v[248:255], v10\n\t" \
    "s_nop 6\n\t" \
    "v_cmp_u_f32 vcc, v72, v73\n\t" \
    "v_pk_add_f32 v[84:85], v[84:85], v[86:87]\n\t" \
    "v_cvt_pk_bf16_f32 v159, v84, v85\n\t" \
    "ds_write_b32 v16, v159 offset:256\n\t" \
    "s_cbranch_vccnz qk_Gslow67_%=\n\t" \
    "qk_Gback68_%=:\n\t" \
    "s_waitcnt lgkmcnt(0)\n\t" \
    "s_barrier\n\t" \
    "s_sleep 1\n\t" \
    "ds_read_b64_tr_b16 v[140:141], v17 offset:0\n\t" \
    "ds_read_b64_tr_b16 v[142:143], v17 offset:256\n\t" \
    "ds_read_b64_tr_b16 v[144:145], v17 offset:512\n\t" \
    "ds_read_b64_tr_b16 v[146:147], v17 offset:768\n\t" \
    "ds_read_b64_tr_b16 v[148:149], v17 offset:1024\n\t" \
    "ds_read_b64_tr_b16 v[150:151], v17 offset:1280\n\t" \
    "ds_read_b64_tr_b16 v[152:153], v17 offset:1536\n\t" \
    "ds_read_b64_tr_b16 v[154:155], v17 offset:1792\n\t" \
    "s_waitcnt lgkmcnt(0)\n\t" \
    "s_cmp_eq_u32 s65, 63\n\t" \
    "s_cbranch_scc1 qk_last57_%=\n\t" \
    "s_cmp_ge_u32 s65, 58\n\t" \
    "s_cbranch_scc1 qk_nsl69_%=\n\t" \
    "s_sleep 6\n\t" \
    "global_store_dword v14, v25, s[36:37] sc1\n\t" \
    "qk_nsl69_%=:\n\t" \
    "s_mov_b64 exec, s[58:59]\n\t" \
    "global_store_dwordx4 v0, v[140:143], s[40:41] offset:0 sc1\n\t" \
    "global_store_dwordx4 v0, v[144:147], s[40:41] offset:1024 sc1\n\t" \
    "global_store_dwordx4 v0, v[148:151], s[40:41] offset:2048 sc1\n\t" \
    "global_store_dwordx4 v0, v[152:155], s[40:41] offset:3072 sc1\n\t" \
    "s_mov_b64 exec, -1\n\t" \
    "s_add_u32 s65, s65, 1\n\t" \
    "v_mov_b32 v72, 0\n\t" \
    "v_mov_b32 v73, 0\n\t" \
    "v_mov_b32 v74, 0\n\t" \
    "v_mov_b32 v75, 0\n\t" \
    "v_mov_b32 v76, 0\n\t" \
    "v_mov_b32 v77, 0\n\t" \
    "v_mov_b32 v78, 0\n\t" \
    "v_mov_b32 v79, 0\n\t" \
    "v_mov_b32 v80, 0\n\t" \
    "v_mov_b32 v81, 0\n\t" \
    "v_mov_b32 v82, 0\n\t" \
    "v_mov_b32 v83, 0\n\t" \
    "v_mov_b32 v84, 0\n\t" \
    "v_mov_b32 v85, 0\n\t" \
    "v_mov_b32 v86, 0\n\t" \
    "v_mov_b32 v87, 0\n\t" \
    "s_add_u32 s69, s65, 0\n\t" \
    "s_and_b32 s69, s69, 7\n\t" \
    "s_lshl_b32 s69, s69, 18\n\t" \
    "s_add_u32 s69, s69, s62\n\t" \
    "s_add_u32 s46, s36, s69\n\t" \
    "s_addc_u32 s47, s37, 0\n\t" \
    "s_mul_i32 s70, s67, 7\n\t" \
    "s_add_u32 s69, s65, 0\n\t" \
    "s_xor_b32 s69, s69, s70\n\t" \
    "s_and_b32 s69, s69, 63\n\t" \
    "s_lshl_b32 s69, s69, 12\n\t" \
    "s_mul_i32 s70, s67, 262144\n\t" \
    "s_add_u32 s69, s69, s70\n\t" \
    "s_add_u32 s69, s69, 39845888\n\t" \
    "s_add_u32 s40, s36, s69\n\t" \
    "s_addc_u32 s41, s37, 0\n\t" \
    "s_sub_u32 s91, s65, 1\n\t" \
    "v_mov_b32 v25, s91\n\t" \
    "s_barrier\n\t" \
    "ds_read_b32 v168, v11 offset:20480\n\t" \
    "s_waitcnt lgkmcnt(0)\n\t" \
    "v_readfirstlane_b32 s70, v168\n\t" \
    "ds_write_b32 v12, v31 offset:0\n\t" \
    "ds_write_b32 v12, v31 offset:64\n\t" \
    "s_branch qk_go58_%=\n\t" \
    "qk_Gbad65_%=:\n\t" \
    "s_waitcnt lgkmcnt(0)\n\t" \
    "s_nop 7\n\t" \
    "v_mov_b32 v72, 0\n\t" \
    "v_mov_b32 v73, 0\n\t" \
    "v_mov_b32 v74, 0\n\t" \
    "v_mov_b32 v75, 0\n\t" \
    "v_mov_b32 v76, 0\n\t" \
    "v_mov_b32 v77, 0\n\t" \
    "v_mov_b32 v78, 0\n\t" \
    "v_mov_b32 v79, 0\n\t" \
    "v_mov_b32 v80, 0\n\t" \
    "v_mov_b32 v81, 0\n\t" \
    "v_mov_b32 v82, 0\n\t" \
    "v_mov_b32 v83, 0\n\t" \
    "v_mov_b32 v84, 0\n\t" \
    "v_mov_b32 v85, 0\n\t" \
    "v_mov_b32 v86, 0\n\t" \
    "v_mov_b32 v87, 0\n\t" \
    "s_cmp_eq_u32 s68, 0\n\t" \
    "s_cbranch_scc1 qk_F70_%=\n\t" \
    "s_sub_u32 s68, s68, 1\n\t" \
    "s_branch qk_G64_%=\n\t" \
    "qk_F70_%=:\n\t" \
    "v_mov_b32 v160, 5767424\n\t" \
    "v_mov_b32 v161, 1\n\t" \
    "global_store_dword v160, v161, s[36:37] sc1\n\t" \
    "s_waitcnt vmcnt(0)\n\t" \
    "s_branch qk_Gc66_%=\n\t" \
    "qk_Gslow67_%=:\n\t" \
    "v_max_u32 v160, v36, v37\n\t" \
    "v_max3_u32 v160, v160, v38, v39\n\t" \
    "v_max3_u32 v160, v160, v40, v41\n\t" \
    "v_max3_u32 v160, v160, v42, v43\n\t" \
    "v_max3_u32 v160, v160, v44, v45\n\t" \
    "v_max3_u32 v160, v160, v46, v47\n\t" \
    "v_max3_u32 v160, v160, v48, v49\n\t" \
    "v_max3_u32 v160, v160, v50, v51\n\t" \
    "v_max3_u32 v160, v160, v52, v53\n\t" \
    "v_max3_u32 v160, v160, v54, v55\n\t" \
    "v_max3_u32 v160, v160, v56, v57\n\t" \
    "v_max3_u32 v160, v160, v58, v59\n\t" \
    "v_max3_u32 v160, v160, v60, v61\n\t" \
    "v_max3_u32 v160, v160, v62, v63\n\t" \
    "v_max3_u32 v160, v160, v64, v65\n\t" \
    "v_max3_u32 v160, v160, v66, v67\n\t" \
    "v_max3_u32 v160, v160, v68, v69\n\t" \
    "v_max3_u32 v160, v160, v70, v71\n\t" \
    "v_cmp_eq_u32 vcc, -1, v160\n\t" \
    "s_cbranch_vccz qk_Gback68_%=\n\t" \
    "s_waitcnt lgkmcnt(0)\n\t" \
    "v_sub_u32 v160, v1, v3\n\t" \
    "s_nop 0\n\t" \
    "v_readfirstlane_b32 s69, v160\n\t" \
    "v_add_u32 v163, 20480, v0\n\t" \
    "s_nop 7\n\t" \
    "s_add_u32 s30, s46, s69\n\t" \
    "s_addc_u32 s31, s47, 0\n\t" \
    "qk_Gag71_%=:\n\t" \
    "global_load_dwordx4 v[36:39], v1, s[46:47] sc1\n\t" \
    "global_load_dwordx4 v[40:43], v23, s[46:47] sc1\n\t" \
    "global_load_dwordx4 v[44:47], v163, s[30:31] sc1\n\t" \
    "global_load_dwordx4 v[48:51], v4, s[30:31] sc1\n\t" \
    "global_load_dwordx4 v[52:55], v5, s[30:31] sc1\n\t" \
    "global_load_dwordx4 v[56:59], v6, s[30:31] sc1\n\t" \
    "global_load_dwordx4 v[60:63], v7, s[30:31] sc1\n\t" \
    "global_load_dwordx4 v[64:67], v8, s[30:31] sc1\n\t" \
    "global_load_dwordx4 v[68:71], v9, s[30:31] sc1\n\t" \
    "s_waitcnt vmcnt(0)\n\t" \
    "v_max_u32 v160, v36, v37\n\t" \
    "v_max3_u32 v160, v160, v38, v39\n\t" \
    "v_max3_u32 v160, v160, v40, v41\n\t" \
    "v_max3_u32 v160, v160, v42, v43\n\t" \
    "v_max3_u32 v160, v160, v44, v45\n\t" \
    "v_max3_u32 v160, v160, v46, v47\n\t" \
    "v_max3_u32 v160, v160, v48, v49\n\t" \
    "v_max3_u32 v160, v160, v50, v51\n\t" \
    "v_max3_u32 v160, v160, v52, v53\n\t" \
    "v_max3_u32 v160, v160, v54, v55\n\t" \
    "v_max3_u32 v160, v160, v56, v57\n\t" \
    "v_max3_u32 v160, v160, v58, v59\n\t" \
    "v_max3_u32 v160, v160, v60, v61\n\t" \
    "v_max3_u32 v160, v160, v62, v63\n\t" \
    "v_max3_u32 v160, v160, v64, v65\n\t" \
    "v_max3_u32 v160, v160, v66, v67\n\t" \
    "v_max3_u32 v160, v160, v68, v69\n\t" \
    "v_max3_u32 v160, v160, v70, v71\n\t" \
    "v_cmp_eq_u32 vcc, -1, v160\n\t" \
    "s_cbranch_vccz qk_Gredo72_%=\n\t" \
    "s_cmp_eq_u32 s68, 0\n\t" \
    "s_cbranch_scc1 qk_F73_%=\n\t" \
    "s_sub_u32 s68, s68, 1\n\t" \
    "s_sleep 1\n\t" \
    "s_branch qk_Gag71_%=\n\t" \
    "qk_F73_%=:\n\t" \
    "v_mov_b32 v160, 5767424\n\t" \
    "v_mov_b32 v161, 2\n\t" \
    "global_store_dword v160, v161, s[36:37] sc1\n\t" \
    "s_waitcnt vmcnt(0)\n\t" \
    "s_branch qk_Gredo72_%=\n\t" \
    "qk_Gredo72_%=:\n\t" \
    "v_mov_b32 v72, 0\n\t" \
    "v_mov_b32 v73, 0\n\t" \
    "v_mov_b32 v74, 0\n\t" \
    "v_mov_b32 v75, 0\n\t" \
    "v_mov_b32 v76, 0\n\t" \
    "v_mov_b32 v77, 0\n\t" \
    "v_mov_b32 v78, 0\n\t" \
    "v_mov_b32 v79, 0\n\t" \
    "v_mov_b32 v80, 0\n\t" \
    "v_mov_b32 v81, 0\n\t" \
    "v_mov_b32 v82, 0\n\t" \
    "v_mov_b32 v83, 0\n\t" \
    "v_mov_b32 v84, 0\n\t" \
    "v_mov_b32 v85, 0\n\t" \
    "v_mov_b32 v86, 0\n\t" \
    "v_mov_b32 v87, 0\n\t" \
    "v_smfmac_f32_16x16x64_bf16 v[72:75], v[36:39], a[32:39], v10\n\t" \
    "v_smfmac_f32_16x16x64_bf16 v[72:75], v[40:43], a[40:47], v10\n\t" \
    "v_smfmac_f32_16x16x64_bf16 v[72:75], v[48:51], a[48:55], v10\n\t" \
    "v_smfmac_f32_16x16x64_bf16 v[72:75], v[52:55], a[56:63], v10\n\t" \
    "v_smfmac_f32_16x16x64_bf16 v[72:75], v[56:59], a[0:7], v10\n\t" \
    "v_smfmac_f32_16x16x64_bf16 v[72:75], v[60:63], a[8:15], v10\n\t" \
    "v_smfmac_f32_16x16x64_bf16 v[72:75], v[64:67], a[16:23], v10\n\t" \
    "v_smfmac_f32_16x16x64_bf16 v[72:75], v[68:71], a[24:31], v10\n\t" \
    "v_smfmac_f32_16x16x64_bf16 v[72:75], v[44:47], a[64:71], v10\n\t" \
    "v_smfmac_f32_16x16x64_bf16 v[76:79], v[36:39], a[104:111], v10\n\t" \
    "v_smfmac_f32_16x16x64_bf16 v[76:79], v[40:43], a[112:119], v10\n\t" \
    "v_smfmac_f32_16x16x64_bf16 v[76:79], v[48:51], a[120:127], v10\n\t" \
    "v_smfmac_f32_16x16x64_bf16 v[76:79], v[52:55], a[128:135], v10\n\t" \
    "v_smfmac_f32_16x16x64_bf16 v[76:79], v[56:59], a[72:79], v10\n\t" \
    "v_smfmac_f32_16x16x64_bf16 v[76:79], v[60:63], a[80:87], v10\n\t" \
    "v_smfmac_f32_16x16x64_bf16 v[76:79], v[64:67], a[88:95], v10\n\t" \
    "v_smfmac_f32_16x16x64_bf16 v[76:79], v[68:71], a[96:103], v10\n\t" \
    "v_smfmac_f32_16x16x64_bf16 v[76:79], v[44:47], a[136:143], v10\n\t" \
    "v_smfmac_f32_16x16x64_bf16 v[80:83], v[36:39], a[176:183], v10\n\t" \
    "v_smfmac_f32_16x16x64_bf16 v[80:83], v[40:43], a[184:191], v10\n\t" \
    "v_smfmac_f32_16x16x64_bf16 v[80:83], v[48:51], a[192:199], v10\n\t" \
    "v_smfmac_f32_16x16x64_bf16 v[80:83], v[52:55], a[200:207], v10\n\t" \
    "v_smfmac_f32_16x16x64_bf16 v[80:83], v[56:59], a[144:151], v10\n\t" \
    "v_smfmac_f32_16x16x64_bf16 v[80:83], v[60:63], a[152:159], v10\n\t" \
    "v_smfmac_f32_16x16x64_bf16 v[80:83], v[64:67], a[160:167], v10\n\t" \
    "v_smfmac_f32_16x16x64_bf16 v[80:83], v[68:71], a[168:175], v10\n\t" \
    "v_smfmac_f32_16x16x64_bf16 v[80:83], v[44:47], a[208:215], v10\n\t" \
    "v_smfmac_f32_16x16x64_bf16 v[84:87], v[36:39], a[248:255], v10\n\t" \
    "v_smfmac_f32_16x16x64_bf16 v[84:87], v[40:43], v[224:231], v10\n\t" \
    "v_smfmac_f32_16x16x64_bf16 v[84:87], v[48:51], v[232:239], v10\n\t" \
    "v_smfmac_f32_16x16x64_bf16 v[84:87], v[52:55], v[240:247], v10\n\t" \
    "v_smfmac_f32_16x16x64_bf16 v[84:87], v[56:59], a[216:223], v10\n\t" \
    "v_smfmac_f32_16x16x64_bf16 v[84:87], v[60:63], a[224:231], v10\n\t" \
    "v_smfmac_f32_16x16x64_bf16 v[84:87], v[64:67], a[232:239], v10\n\t" \
    "v_smfmac_f32_16x16x64_bf16 v[84:87], v[68:71], a[240:247], v10\n\t" \
    "v_smfmac_f32_16x16x64_bf16 v[84:87], v[44:47], v[248:255], v10\n\t" \
    "v_add_f32 v72, v72, v74\n\t" \
    "v_add_f32 v73, v73, v75\n\t" \
    "v_cvt_pk_bf16_f32 v156, v72, v73\n\t" \
    "ds_write_b32 v12, v156 offset:0\n\t" \
    "v_add_f32 v76, v76, v78\n\t" \
    "v_add_f32 v77, v77, v79\n\t" \
    "v_cvt_pk_bf16_f32 v157, v76, v77\n\t" \
    "ds_write_b32 v12, v157 offset:64\n\t" \
    "v_add_f32 v80, v80, v82\n\t" \
    "v_add_f32 v81, v81, v83\n\t" \
    "v_cvt_pk_bf16_f32 v158, v80, v81\n\t" \
    "ds_write_b32 v16, v158 offset:0\n\t" \
    "v_pk_add_f32 v[84:85], v[84:85], v[86:87]\n\t" \
    "v_cvt_pk_bf16_f32 v159, v84, v85\n\t" \
    "ds_write_b32 v16, v159 offset:256\n\t" \
    "s_branch qk_Gback68_%=\n\t" \
    "qk_kf59_%=:\n\t" \
    "global_store_dword v14, v25, s[36:37] sc1\n\t" \
    "s_branch qk_kb60_%=\n\t" \
    "qk_last57_%=:\n\t" \
    "s_mov_b64 exec, s[58:59]\n\t" \
    "global_store_dwordx4 v0, v[140:143], s[40:41] offset:0 sc1\n\t" \
    "global_store_dwordx4 v0, v[144:147], s[40:41] offset:1024 sc1\n\t" \
    "global_store_dwordx4 v0, v[148:151], s[40:41] offset:2048 sc1\n\t" \
    "global_store_dwordx4 v0, v[152:155], s[40:41] offset:3072 sc1\n\t" \
    "s_mov_b64 exec, -1\n\t" \
    "s_endpgm\n\t" \
    "qk_loop14_%=:\n\t" \
    "s_mov_b32 s38, 0x22336600\n\t" \
    "s_mov_b32 s39, 0x1a\n\t" \
    "v_mov_b32 v72, 0\n\t" \
    "v_mov_b32 v73, 0\n\t" \
    "v_mov_b32 v74, 0\n\t" \
    "v_mov_b32 v75, 0\n\t" \
    "v_mov_b32 v76, 0\n\t" \
    "v_mov_b32 v77, 0\n\t" \
    "v_mov_b32 v78, 0\n\t" \
    "v_mov_b32 v79, 0\n\t" \
    "v_mov_b32 v80, 0\n\t" \
    "v_mov_b32 v81, 0\n\t" \
    "v_mov_b32 v82, 0\n\t" \
    "v_mov_b32 v83, 0\n\t" \
    "v_mov_b32 v84, 0\n\t" \
    "v_mov_b32 v85, 0\n\t" \
    "v_mov_b32 v86, 0\n\t" \
    "v_mov_b32 v87, 0\n\t" \
    "s_add_u32 s69, s65, 0\n\t" \
    "s_and_b32 s69, s69, 7\n\t" \
    "s_lshl_b32 s69, s69, 18\n\t" \
    "s_add_u32 s69, s69, s62\n\t" \
    "s_add_u32 s46, s36, s69\n\t" \
    "s_addc_u32 s47, s37, 0\n\t" \
    "s_lshr_b64 s[38:39], s[38:39], 4\n\t" \
    "s_and_b32 s97, s38, 15\n\t" \
    "s_lshl_b32 s97, s97, 6\n\t" \
    "s_and_b32 s69, s65, 7\n\t" \
    "s_lshl_b32 s69, s69, 18\n\t" \
    "s_add_u32 s69, s69, s63\n\t" \
    "s_add_u32 s52, s36, s69\n\t" \
    "s_addc_u32 s53, s37, 0\n\t" \
    "s_sub_u32 s70, s79, s74\n\t" \
    "s_max_i32 s70, s70, 0\n\t" \
    "s_min_i32 s70, s70, 16000\n\t" \
    "s_lshl_b32 s70, s70, 1\n\t" \
    "s_mul_hi_u32 s70, s70, s77\n\t" \
    "s_add_u32 s70, s70, s73\n\t" \
    ".p2align 6\n\t" \
    "s_nop 0\n\t" \
    "s_nop 0\n\t" \
    "s_nop 0\n\t" \
    "s_nop 0\n\t" \
    "s_nop 0\n\t" \
    "s_nop 0\n\t" \
    "qk_go75_%=:\n\t" \
    "s_getpc_b64 s[30:31]\n\t" \
    "qk_Wp77_%=:\n\t" \
    "s_add_u32 s30, s30, qk_We78_%=-qk_Wp77_%=\n\t" \
    "s_addc_u32 s31, s31, 0\n\t" \
    "s_sub_u32 s70, s70, 96\n\t" \
    "qk_W76_%=:\n\t" \
    "s_memtime s[56:57]\n\t" \
    "s_waitcnt lgkmcnt(0)\n\t" \
    "s_sub_u32 s69, s70, s56\n\t" \
    "s_cmp_gt_i32 s69, 80\n\t" \
    "s_cbranch_scc1 qk_W76_%=\n\t" \
    "s_max_i32 s69, s69, 0\n\t" \
    "s_and_b32 s69, s69, -4\n\t" \
    "s_sub_u32 s30, s30, s69\n\t" \
    "s_subb_u32 s31, s31, 0\n\t" \
    "s_setpc_b64 s[30:31]\n\t" \
    "s_nop 0\n\t" \
    "s_nop 0\n\t" \
    "s_nop 0\n\t" \
    "s_nop 0\n\t" \
    "s_nop 0\n\t" \
    "s_nop 0\n\t" \
    "s_nop 0\n\t" \
    "s_nop 0\n\t" \
    "s_nop 0\n\t" \
    "s_nop 0\n\t" \
    "s_nop 0\n\t" \
    "s_nop 0\n\t" \
    "s_nop 0\n\t" \
    "s_nop 0\n\t" \
    "s_nop 0\n\t" \
    "s_nop 0\n\t" \
    "s_nop 0\n\t" \
    "s_nop 0\n\t" \
    "s_nop 0\n\t" \
    "s_nop 0\n\t" \
    "qk_We78_%=:\n\t" \
    "qk_G79_%=:\n\t" \
    "global_load_dwordx4 v[36:39], v1, s[46:47] sc1\n\t" \
    "global_load_dword v161, v2, s[46:47] sc1\n\t" \
    "global_load_dwordx4 v[40:43], v23, s[46:47] sc1\n\t" \
    "s_mul_hi_u32 s69, s84, s78\n\t" \
    "s_add_u32 s33, s74, s69\n\t" \
    "s_add_u32 s33, s33, s97\n\t" \
    "s_lshr_b32 s69, s90, 16\n\t" \
    "s_add_u32 s34, s33, s69\n\t" \
    "s_and_b32 s69, s90, 0xffff\n\t" \
    "s_sub_u32 s35, s80, s74\n\t" \
    "s_sub_u32 s35, s35, s69\n\t" \
    "s_add_u32 s99, s35, 240\n\t" \
    "s_add_u32 s98, s33, 448\n\t" \
    "s_sub_u32 s96, s79, s74\n\t" \
    "s_cmp_le_u32 s65, 0\n\t" \
    "s_cselect_b32 s69, 112, 0\n\t" \
    "s_add_u32 s69, s69, 448\n\t" \
    "s_add_u32 s96, s96, s69\n\t" \
    "s_max_i32 s35, s35, s96\n\t" \
    "s_add_u32 s99, s35, 716\n\t" \
    "s_mul_i32 s69, s35, 10\n\t" \
    "s_ashr_i32 s69, s69, 4\n\t" \
    "s_sub_u32 s69, s33, s69\n\t" \
    "v_lshrrev_b32_e64 v163, 1, s69\n\t" \
    "s_waitcnt vmcnt(2)\n\t" \
    "ds_write_b128 v3, v[36:39]\n\t" \
    "v_smfmac_f32_16x16x64_bf16 v[72:75], v[36:39], a[48:55], v10\n\t" \
    "s_waitcnt vmcnt(1)\n\t" \
    "ds_write_b32 v22, v161 offset:20224\n\t" \
    "v_smfmac_f32_16x16x64_bf16 v[76:79], v[36:39], a[120:127], v10\n\t" \
    "s_waitcnt vmcnt(0)\n\t" \
    "ds_write_b128 v3, v[40:43] offset:1024\n\t" \
    "v_max3_u32 v160, v36, v40, v161\n\t" \
    "v_cmp_eq_u32 vcc, -1, v160\n\t" \
    "v_smfmac_f32_16x16x64_bf16 v[80:83], v[36:39], a[192:199], v10\n\t" \
    "v_smfmac_f32_16x16x64_bf16 v[84:87], v[36:39], v[232:239], v10\n\t" \
    "v_smfmac_f32_16x16x64_bf16 v[72:75], v[40:43], a[56:63], v10\n\t" \
    "s_cbranch_vccnz qk_Gbad80_%=\n\t" \
    "qk_Gc81_%=:\n\t" \
    "s_waitcnt lgkmcnt(0)\n\t" \
    "s_barrier\n\t" \
    "s_memtime s[56:57]\n\t" \
    "ds_read_b128 v[48:51], v4\n\t" \
    "ds_read_b128 v[52:55], v5\n\t" \
    "v_smfmac_f32_16x16x64_bf16 v[76:79], v[40:43], a[128:135], v10\n\t" \
    "ds_read_b128 v[56:59], v6\n\t" \
    "ds_read_b128 v[60:63], v7\n\t" \
    "v_smfmac_f32_16x16x64_bf16 v[80:83], v[40:43], a[200:207], v10\n\t" \
    "ds_read_b128 v[64:67], v8\n\t" \
    "ds_read_b128 v[68:71], v9\n\t" \
    "v_smfmac_f32_16x16x64_bf16 v[84:87], v[40:43], v[240:247], v10\n\t" \
    "ds_read_b128 v[44:47], v0 offset:19456\n\t" \
    "s_waitcnt lgkmcnt(6)\n\t" \
    "v_smfmac_f32_16x16x64_bf16 v[72:75], v[48:51], a[0:7], v10\n\t" \
    "s_sub_u32 s69, s56, s73\n\t" \
    "s_mul_hi_u32 s69, s69, s78\n\t" \
    "s_waitcnt lgkmcnt(5)\n\t" \
    "v_smfmac_f32_16x16x64_bf16 v[72:75], v[52:55], a[8:15], v10\n\t" \
    "s_max_i32 s69, s69, s96\n\t" \
    "s_max_i32 s70, s69, s35\n\t" \
    "s_waitcnt lgkmcnt(4)\n\t" \
    "v_smfmac_f32_16x16x64_bf16 v[72:75], v[56:59], a[16:23], v10\n\t" \
    "s_min_i32 s70, s70, s99\n\t" \
    "s_mul_i32 s70, s70, 10\n\t" \
    "s_waitcnt lgkmcnt(3)\n\t" \
    "v_smfmac_f32_16x16x64_bf16 v[72:75], v[60:63], a[24:31], v10\n\t" \
    "s_lshl4_add_u32 s70, s69, s70\n\t" \
    "s_ashr_i32 s70, s70, 5\n\t" \
    "v_add_lshl_u32 v162, s70, v163, 1\n\t" \
    "s_waitcnt lgkmcnt(2)\n\t" \
    "v_smfmac_f32_16x16x64_bf16 v[72:75], v[64:67], a[32:39], v10\n\t" \
    "global_store_dword v21, v162, s[52:53] sc1\n\t" \
    "s_waitcnt lgkmcnt(1)\n\t" \
    "v_smfmac_f32_16x16x64_bf16 v[72:75], v[68:71], a[40:47], v10\n\t" \
    "s_waitcnt lgkmcnt(0)\n\t" \
    "v_smfmac_f32_16x16x64_bf16 v[72:75], v[44:47], a[64:71], v10\n\t" \
    "s_nop 2\n\t" \
    "v_smfmac_f32_16x16x64_bf16 v[76:79], v[48:51], a[72:79], v10\n\t" \
    "s_nop 2\n\t" \
    "v_smfmac_f32_16x16x64_bf16 v[76:79], v[52:55], a[80:87], v10\n\t" \
    "v_add_f32 v72, v72, v74\n\t" \
    "v_smfmac_f32_16x16x64_bf16 v[76:79], v[56:59], a[88:95], v10\n\t" \
    "v_add_f32 v73, v73, v75\n\t" \
    "v_smfmac_f32_16x16x64_bf16 v[76:79], v[60:63], a[96:103], v10\n\t" \
    "v_cvt_pk_bf16_f32 v156, v72, v73\n\t" \
    "v_smfmac_f32_16x16x64_bf16 v[76:79], v[64:67], a[104:111], v10\n\t" \
    "ds_write_b32 v12, v156 offset:0\n\t" \
    "v_smfmac_f32_16x16x64_bf16 v[76:79], v[68:71], a[112:119], v10\n\t" \
    "v_smfmac_f32_16x16x64_bf16 v[76:79], v[44:47], a[136:143], v10\n\t" \
    "s_nop 2\n\t" \
    "v_smfmac_f32_16x16x64_bf16 v[80:83], v[48:51], a[144:151], v10\n\t" \
    "s_nop 2\n\t" \
    "v_smfmac_f32_16x16x64_bf16 v[80:83], v[52:55], a[152:159], v10\n\t" \
    "v_add_f32 v76, v76, v78\n\t" \
    "v_smfmac_f32_16x16x64_bf16 v[80:83], v[56:59], a[160:167], v10\n\t" \
    "v_add_f32 v77, v77, v79\n\t" \
    "v_smfmac_f32_16x16x64_bf16 v[80:83], v[60:63], a[168:175], v10\n\t" \
    "v_cvt_pk_bf16_f32 v157, v76, v77\n\t" \
    "v_smfmac_f32_16x16x64_bf16 v[80:83], v[64:67], a[176:183], v10\n\t" \
    "ds_write_b32 v12, v157 offset:64\n\t" \
    "v_smfmac_f32_16x16x64_bf16 v[80:83], v[68:71], a[184:191], v10\n\t" \
    "v_smfmac_f32_16x16x64_bf16 v[80:83], v[44:47], a[208:215], v10\n\t" \
    "s_nop 2\n\t" \
    "v_smfmac_f32_16x16x64_bf16 v[84:87], v[48:51], a[216:223], v10\n\t" \
    "s_nop 2\n\t" \
    "v_smfmac_f32_16x16x64_bf16 v[84:87], v[52:55], a[224:231], v10\n\t" \
    "v_add_f32 v80, v80, v82\n\t" \
    "v_smfmac_f32_16x16x64_bf16 v[84:87], v[56:59], a[232:239], v10\n\t" \
    "v_add_f32 v81, v81, v83\n\t" \
    "v_smfmac_f32_16x16x64_bf16 v[84:87], v[60:63], a[240:247], v10\n\t" \
    "v_cvt_pk_bf16_f32 v158, v80, v81\n\t" \
    "v_smfmac_f32_16x16x64_bf16 v[84:87], v[64:67], a[248:255], v10\n\t" \
    "ds_write_b32 v16, v158 offset:0\n\t" \
    "v_smfmac_f32_16x16x64_bf16 v[84:87], v[68:71], v[224:231], v10\n\t" \
    "v_smfmac_f32_16x16x64_bf16 v[84:87], v[44:47], v[248:255], v10\n\t" \
    "s_nop 6\n\t" \
    "v_cmp_u_f32 vcc, v72, v73\n\t" \
    "v_pk_add_f32 v[84:85], v[84:85], v[86:87]\n\t" \
    "v_cvt_pk_bf16_f32 v159, v84, v85\n\t" \
    "ds_write_b32 v16, v159 offset:256\n\t" \
    "s_cbranch_vccnz qk_Gslow82_%=\n\t" \
    "qk_Gback83_%=:\n\t" \
    "s_waitcnt lgkmcnt(0)\n\t" \
    "s_barrier\n\t" \
    "s_cmp_eq_u32 s65, 63\n\t" \
    "s_cbranch_scc1 qk_last74_%=\n\t" \
    "s_cmp_lg_u32 s65, 0\n\t" \
    "s_cbranch_scc1 qk_ng84_%=\n\t" \
    "v_mov_b32 v168, 5838848\n\t" \
    "v_mov_b32 v169, 1\n\t" \
    "s_mov_b64 exec, 1\n\t" \
    "global_atomic_add v170, v168, v169, s[36:37] sc0 sc1\n\t" \
    "s_mov_b64 exec, -1\n\t" \
    "v_mbcnt_lo_u32_b32 v168, -1, 0\n\t" \
    "v_and_b32 v169, 3, v168\n\t" \
    "v_lshlrev_b32 v169, 16, v169\n\t" \
    "v_bfe_u32 v171, v168, 2, 2\n\t" \
    "v_lshl_add_u32 v169, v171, 11, v169\n\t" \
    "v_bfe_u32 v171, v168, 4, 1\n\t" \
    "v_lshl_add_u32 v169, v171, 14, v169\n\t" \
    "v_add_u32 v169, s63, v169\n\t" \
    "s_waitcnt vmcnt(0)\n\t" \
    "v_readfirstlane_b32 s69, v170\n\t" \
    "s_nop 1\n\t" \
    "s_cmp_lg_u32 s69, 126\n\t" \
    "s_cbranch_scc1 qk_go85_%=\n\t" \
    "s_memrealtime s[56:57]\n\t" \
    "s_waitcnt lgkmcnt(0)\n\t" \
    "s_lshl_b32 s69, s56, 4\n\t" \
    "s_add_u32 s69, s69, 2800\n\t" \
    "s_add_u32 s70, s56, 24\n\t" \
    "s_lshr_b32 s70, s70, 4\n\t" \
    "v_lshrrev_b32 v171, 2, v168\n\t" \
    "v_add_u32 v171, s70, v171\n\t" \
    "v_and_b32 v171, 63, v171\n\t" \
    "v_lshlrev_b32 v171, 6, v171\n\t" \
    "v_and_b32 v170, 3, v168\n\t" \
    "v_lshl_add_u32 v171, v170, 12, v171\n\t" \
    "v_add_u32 v171, 5865472, v171\n\t" \
    "v_mov_b32 v168, s69\n\t" \
    "s_mov_b32 exec_hi, 0\n\t" \
    "global_store_dword v171, v168, s[36:37] sc1\n\t" \
    "global_store_dword v169, v168, s[36:37] sc1\n\t" \
    "s_mov_b32 exec_hi, -1\n\t" \
    "qk_go85_%=:\n\t" \
    "v_mov_b32 v21, v33\n\t" \
    "qk_ng84_%=:\n\t" \
    "s_sub_u32 s70, s80, s74\n\t" \
    "s_max_i32 s70, s70, 0\n\t" \
    "s_min_i32 s70, s70, 16000\n\t" \
    "s_lshl_b32 s70, s70, 1\n\t" \
    "s_mul_hi_u32 s70, s70, s77\n\t" \
    "s_add_u32 s70, s70, s73\n\t" \
    "qk_W86_%=:\n\t" \
    "s_memtime s[56:57]\n\t" \
    "s_waitcnt lgkmcnt(0)\n\t" \
    "s_sub_u32 s69, s70, s56\n\t" \
    "s_cmp_gt_i32 s69, 24\n\t" \
    "s_cbranch_scc1 qk_W86_%=\n\t" \
    "global_load_dword v160, v20, s[52:53] sc1\n\t" \
    "s_waitcnt lgkmcnt(0)\n\t" \
    "s_mov_b32 s70, 116\n\t" \
    "qk_E90_%=:\n\t" \
    "s_memtime s[56:57]\n\t" \
    "s_memrealtime s[2:3]\n\t" \
    "s_nop 0\n\t" \
    "s_memrealtime s[4:5]\n\t" \
    "s_nop 0\n\t" \
    "s_memrealtime s[6:7]\n\t" \
    "s_nop 0\n\t" \
    "s_memrealtime s[8:9]\n\t" \
    "s_nop 0\n\t" \
    "s_memrealtime s[10:11]\n\t" \
    "s_nop 0\n\t" \
    "s_memrealtime s[12:13]\n\t" \
    "s_nop 0\n\t" \
    "s_memrealtime s[14:15]\n\t" \
    "s_nop 0\n\t" \
    "s_memrealtime s[16:17]\n\t" \
    "s_nop 0\n\t" \
    "s_memrealtime s[18:19]\n\t" \
    "s_nop 0\n\t" \
    "s_memrealtime s[20:21]\n\t" \
    "s_nop 0\n\t" \
    "s_memrealtime s[22:23]\n\t" \
    "s_nop 0\n\t" \
    "s_memrealtime s[24:25]\n\t" \
    "s_nop 0\n\t" \
    "s_memrealtime s[26:27]\n\t" \
    "s_nop 0\n\t" \
    "s_memrealtime s[28:29]\n\t" \
    "s_memtime s[54:55]\n\t" \
    "s_add_u32 s70, s70, 8\n\t" \
    "s_waitcnt lgkmcnt(0)\n\t" \
    "s_sub_u32 s69, s54, s56\n\t" \
    "s_cmp_gt_u32 s69, s70\n\t" \
    "s_cbranch_scc1 qk_E90_%=\n\t" \
    "s_add_u32 s2, s2, s4\n\t" \
    "s_add_u32 s6, s6, s8\n\t" \
    "s_add_u32 s10, s10, s12\n\t" \
    "s_add_u32 s14, s14, s16\n\t" \
    "s_add_u32 s18, s18, s20\n\t" \
    "s_add_u32 s22, s22, s24\n\t" \
    "s_add_u32 s2, s2, s6\n\t" \
    "s_add_u32 s10, s10, s14\n\t" \
    "s_add_u32 s18, s18, s22\n\t" \
    "s_add_u32 s2, s2, s26\n\t" \
    "s_add_u32 s10, s10, s18\n\t" \
    "s_add_u32 s2, s2, s10\n\t" \
    "s_mul_i32 s70, s28, 13\n\t" \
    "s_sub_u32 s70, s70, s2\n\t" \
    "s_min_u32 s70, s70, 52\n\t" \
    "s_mul_i32 s69, s69, s70\n\t" \
    "s_mul_hi_u32 s69, s69, 76695844\n\t" \
    "s_mov_b32 s75, s73\n\t" \
    "s_mov_b32 s76, s74\n\t" \
    "s_add_u32 s73, s56, s69\n\t" \
    "s_lshl_b32 s74, s28, 4\n\t" \
    "s_sub_u32 s69, s73, s75\n\t" \
    "s_sub_u32 s70, s74, s76\n\t" \
    "s_sub_u32 s71, s70, 1600\n\t" \
    "s_cmp_lt_u32 s71, 400000\n\t" \
    "s_cbranch_scc0 qk_K91_%=\n\t" \
    "v_cvt_f32_u32 v168, s69\n\t" \
    "v_cvt_f32_u32 v169, s70\n\t" \
    "v_rcp_f32 v170, v168\n\t" \
    "v_rcp_f32 v171, v169\n\t" \
    "v_mul_f32 v170, v170, v169\n\t" \
    "v_mul_f32 v171, v171, v168\n\t" \
    "v_mul_f32 v170, 0x4f800000, v170\n\t" \
    "v_mul_f32 v171, 0x4f000000, v171\n\t" \
    "v_cvt_u32_f32 v170, v170\n\t" \
    "v_cvt_u32_f32 v171, v171\n\t" \
    "v_readfirstlane_b32 s69, v170\n\t" \
    "v_readfirstlane_b32 s70, v171\n\t" \
    "s_nop 1\n\t" \
    "s_min_u32 s71, s65, 1\n\t" \
    "s_sub_u32 s69, s69, s78\n\t" \
    "s_ashr_i32 s69, s69, s71\n\t" \
    "s_add_u32 s78, s78, s69\n\t" \
    "s_sub_u32 s70, s70, s77\n\t" \
    "s_ashr_i32 s70, s70, s71\n\t" \
    "s_add_u32 s77, s77, s70\n\t" \
    "qk_K91_%=:\n\t" \
    "s_add_u32 s65, s65, 1\n\t" \
    "v_mov_b32 v72, 0\n\t" \
    "v_mov_b32 v73, 0\n\t" \
    "v_mov_b32 v74, 0\n\t" \
    "v_mov_b32 v75, 0\n\t" \
    "v_mov_b32 v76, 0\n\t" \
    "v_mov_b32 v77, 0\n\t" \
    "v_mov_b32 v78, 0\n\t" \
    "v_mov_b32 v79, 0\n\t" \
    "v_mov_b32 v80, 0\n\t" \
    "v_mov_b32 v81, 0\n\t" \
    "v_mov_b32 v82, 0\n\t" \
    "v_mov_b32 v83, 0\n\t" \
    "v_mov_b32 v84, 0\n\t" \
    "v_mov_b32 v85, 0\n\t" \
    "v_mov_b32 v86, 0\n\t" \
    "v_mov_b32 v87, 0\n\t" \
    "s_add_u32 s69, s65, 0\n\t" \
    "s_and_b32 s69, s69, 7\n\t" \
    "s_lshl_b32 s69, s69, 18\n\t" \
    "s_add_u32 s69, s69, s62\n\t" \
    "s_add_u32 s46, s36, s69\n\t" \
    "s_addc_u32 s47, s37, 0\n\t" \
    "s_lshr_b64 s[38:39], s[38:39], 4\n\t" \
    "s_and_b32 s97, s38, 15\n\t" \
    "s_lshl_b32 s97, s97, 6\n\t" \
    "s_waitcnt vmcnt(0)\n\t" \
    "v_cmp_eq_u32 vcc, -1, v160\n\t" \
    "s_cbranch_vccz qk_Hok88_%=\n\t" \
    "qk_H87_%=:\n\t" \
    "global_load_dword v160, v20, s[52:53] sc1\n\t" \
    "s_waitcnt vmcnt(0)\n\t" \
    "v_cmp_eq_u32 vcc, -1, v160\n\t" \
    "s_cbranch_vccz qk_Hc89_%=\n\t" \
    "s_cmp_eq_u32 s68, 0\n\t" \
    "s_cbranch_scc1 qk_F92_%=\n\t" \
    "s_sub_u32 s68, s68, 1\n\t" \
    "s_sleep 2\n\t" \
    "s_branch qk_H87_%=\n\t" \
    "qk_F92_%=:\n\t" \
    "v_mov_b32 v161, 5767424\n\t" \
    "v_mov_b32 v162, 5\n\t" \
    "global_store_dword v161, v162, s[36:37] sc1\n\t" \
    "s_waitcnt vmcnt(0)\n\t" \
    "s_branch qk_Hc89_%=\n\t" \
    "qk_Hc89_%=:\n\t" \
    "s_memtime s[56:57]\n\t" \
    "s_waitcnt lgkmcnt(0)\n\t" \
    "s_sub_u32 s69, s56, s73\n\t" \
    "s_cmp_lt_u32 s69, 3000\n\t" \
    "s_cbranch_scc1 qk_St93_%=\n\t" \
    "s_waitcnt lgkmcnt(0)\n\t" \
    "s_mov_b32 s70, 116\n\t" \
    "qk_E94_%=:\n\t" \
    "s_memtime s[56:57]\n\t" \
    "s_memrealtime s[2:3]\n\t" \
    "s_nop 0\n\t" \
    "s_memrealtime s[4:5]\n\t" \
    "s_nop 0\n\t" \
    "s_memrealtime s[6:7]\n\t" \
    "s_nop 0\n\t" \
    "s_memrealtime s[8:9]\n\t" \
    "s_nop 0\n\t" \
    "s_memrealtime s[10:11]\n\t" \
    "s_nop 0\n\t" \
    "s_memrealtime s[12:13]\n\t" \
    "s_nop 0\n\t" \
    "s_memrealtime s[14:15]\n\t" \
    "s_nop 0\n\t" \
    "s_memrealtime s[16:17]\n\t" \
    "s_nop 0\n\t" \
    "s_memrealtime s[18:19]\n\t" \
    "s_nop 0\n\t" \
    "s_memrealtime s[20:21]\n\t" \
    "s_nop 0\n\t" \
    "s_memrealtime s[22:23]\n\t" \
    "s_nop 0\n\t" \
    "s_memrealtime s[24:25]\n\t" \
    "s_nop 0\n\t" \
    "s_memrealtime s[26:27]\n\t" \
    "s_nop 0\n\t" \
    "s_memrealtime s[28:29]\n\t" \
    "s_memtime s[54:55]\n\t" \
    "s_add_u32 s70, s70, 8\n\t" \
    "s_waitcnt lgkmcnt(0)\n\t" \
    "s_sub_u32 s69, s54, s56\n\t" \
    "s_cmp_gt_u32 s69, s70\n\t" \
    "s_cbranch_scc1 qk_E94_%=\n\t" \
    "s_add_u32 s2, s2, s4\n\t" \
    "s_add_u32 s6, s6, s8\n\t" \
    "s_add_u32 s10, s10, s12\n\t" \
    "s_add_u32 s14, s14, s16\n\t" \
    "s_add_u32 s18, s18, s20\n\t" \
    "s_add_u32 s22, s22, s24\n\t" \
    "s_add_u32 s2, s2, s6\n\t" \
    "s_add_u32 s10, s10, s14\n\t" \
    "s_add_u32 s18, s18, s22\n\t" \
    "s_add_u32 s2, s2, s26\n\t" \
    "s_add_u32 s10, s10, s18\n\t" \
    "s_add_u32 s2, s2, s10\n\t" \
    "s_mul_i32 s70, s28, 13\n\t" \
    "s_sub_u32 s70, s70, s2\n\t" \
    "s_min_u32 s70, s70, 52\n\t" \
    "s_mul_i32 s69, s69, s70\n\t" \
    "s_mul_hi_u32 s69, s69, 76695844\n\t" \
    "s_mov_b32 s75, s73\n\t" \
    "s_mov_b32 s76, s74\n\t" \
    "s_add_u32 s73, s56, s69\n\t" \
    "s_lshl_b32 s74, s28, 4\n\t" \
    "qk_St93_%=:\n\t" \
    "qk_Hok88_%=:\n\t" \
    "v_max_u32_dpp v160, v160, v160 row_ror:1 row_mask:0xf bank_mask:0xf\n\t" \
    "s_nop 1\n\t" \
    "v_max_u32_dpp v160, v160, v160 row_ror:2 row_mask:0xf bank_mask:0xf\n\t" \
    "s_nop 1\n\t" \
    "v_max_u32_dpp v160, v160, v160 row_ror:4 row_mask:0xf bank_mask:0xf\n\t" \
    "s_nop 1\n\t" \
    "v_max_u32_dpp v160, v160, v160 row_ror:8 row_mask:0xf bank_mask:0xf\n\t" \
    "s_nop 0\n\t" \
    "v_readlane_b32 s69, v160, 0\n\t" \
    "v_readlane_b32 s71, v160, 16\n\t" \
    "s_nop 1\n\t" \
    "s_max_u32 s81, s69, s71\n\t" \
    "s_mul_hi_u32 s69, s85, s78\n\t" \
    "s_add_u32 s79, s81, s69\n\t" \
    "s_add_u32 s79, s79, s87\n\t" \
    "s_cmp_ge_u32 s65, 53\n\t" \
    "s_cselect_b32 s69, 32, 0\n\t" \
    "s_sub_u32 s79, s79, s69\n\t" \
    "s_cmp_lt_u32 s65, 10\n\t" \
    "s_cselect_b32 s69, 48, 0\n\t" \
    "s_add_u32 s79, s79, s69\n\t" \
    "s_add_u32 s80, s79, s88\n\t" \
    "s_sub_u32 s70, s79, s74\n\t" \
    "s_max_i32 s70, s70, 0\n\t" \
    "s_min_i32 s70, s70, 16000\n\t" \
    "s_lshl_b32 s70, s70, 1\n\t" \
    "s_mul_hi_u32 s70, s70, s77\n\t" \
    "s_add_u32 s70, s70, s73\n\t" \
    "v_mov_b32 v168, s70\n\t" \
    "ds_write_b32 v11, v168 offset:20480\n\t" \
    "s_and_b32 s69, s65, 7\n\t" \
    "s_lshl_b32 s69, s69, 18\n\t" \
    "s_add_u32 s69, s69, s63\n\t" \
    "s_add_u32 s52, s36, s69\n\t" \
    "s_addc_u32 s53, s37, 0\n\t" \
    "s_waitcnt lgkmcnt(0)\n\t" \
    "s_barrier\n\t" \
    "ds_write_b32 v12, v31 offset:0\n\t" \
    "ds_write_b32 v12, v31 offset:64\n\t" \
    "s_branch qk_go75_%=\n\t" \
    "qk_Gbad80_%=:\n\t" \
    "s_waitcnt lgkmcnt(0)\n\t" \
    "s_nop 7\n\t" \
    "v_mov_b32 v72, 0\n\t" \
    "v_mov_b32 v73, 0\n\t" \
    "v_mov_b32 v74, 0\n\t" \
    "v_mov_b32 v75, 0\n\t" \
    "v_mov_b32 v76, 0\n\t" \
    "v_mov_b32 v77, 0\n\t" \
    "v_mov_b32 v78, 0\n\t" \
    "v_mov_b32 v79, 0\n\t" \
    "v_mov_b32 v80, 0\n\t" \
    "v_mov_b32 v81, 0\n\t" \
    "v_mov_b32 v82, 0\n\t" \
    "v_mov_b32 v83, 0\n\t" \
    "v_mov_b32 v84, 0\n\t" \
    "v_mov_b32 v85, 0\n\t" \
    "v_mov_b32 v86, 0\n\t" \
    "v_mov_b32 v87, 0\n\t" \
    "s_cmp_eq_u32 s68, 0\n\t" \
    "s_cbranch_scc1 qk_F95_%=\n\t" \
    "s_sub_u32 s68, s68, 1\n\t" \
    "s_branch qk_G79_%=\n\t" \
    "qk_F95_%=:\n\t" \
    "v_mov_b32 v160, 5767424\n\t" \
    "v_mov_b32 v161, 1\n\t" \
    "global_store_dword v160, v161, s[36:37] sc1\n\t" \
    "s_waitcnt vmcnt(0)\n\t" \
    "s_branch qk_Gc81_%=\n\t" \
    "qk_Gslow82_%=:\n\t" \
    "v_max_u32 v160, v36, v37\n\t" \
    "v_max3_u32 v160, v160, v38, v39\n\t" \
    "v_max3_u32 v160, v160, v40, v41\n\t" \
    "v_max3_u32 v160, v160, v42, v43\n\t" \
    "v_max3_u32 v160, v160, v44, v45\n\t" \
    "v_max3_u32 v160, v160, v46, v47\n\t" \
    "v_max3_u32 v160, v160, v48, v49\n\t" \
    "v_max3_u32 v160, v160, v50, v51\n\t" \
    "v_max3_u32 v160, v160, v52, v53\n\t" \
    "v_max3_u32 v160, v160, v54, v55\n\t" \
    "v_max3_u32 v160, v160, v56, v57\n\t" \
    "v_max3_u32 v160, v160, v58, v59\n\t" \
    "v_max3_u32 v160, v160, v60, v61\n\t" \
    "v_max3_u32 v160, v160, v62, v63\n\t" \
    "v_max3_u32 v160, v160, v64, v65\n\t" \
    "v_max3_u32 v160, v160, v66, v67\n\t" \
    "v_max3_u32 v160, v160, v68, v69\n\t" \
    "v_max3_u32 v160, v160, v70, v71\n\t" \
    "v_cmp_eq_u32 vcc, -1, v160\n\t" \
    "s_cbranch_vccz qk_Gback83_%=\n\t" \
    "s_waitcnt lgkmcnt(0)\n\t" \
    "v_sub_u32 v160, v1, v3\n\t" \
    "s_nop 0\n\t" \
    "v_readfirstlane_b32 s69, v160\n\t" \
    "v_add_u32 v163, 20480, v0\n\t" \
    "s_nop 7\n\t" \
    "s_add_u32 s30, s46, s69\n\t" \
    "s_addc_u32 s31, s47, 0\n\t" \
    "qk_Gag96_%=:\n\t" \
    "global_load_dwordx4 v[36:39], v1, s[46:47] sc1\n\t" \
    "global_load_dwordx4 v[40:43], v23, s[46:47] sc1\n\t" \
    "global_load_dwordx4 v[44:47], v163, s[30:31] sc1\n\t" \
    "global_load_dwordx4 v[48:51], v4, s[30:31] sc1\n\t" \
    "global_load_dwordx4 v[52:55], v5, s[30:31] sc1\n\t" \
    "global_load_dwordx4 v[56:59], v6, s[30:31] sc1\n\t" \
    "global_load_dwordx4 v[60:63], v7, s[30:31] sc1\n\t" \
    "global_load_dwordx4 v[64:67], v8, s[30:31] sc1\n\t" \
    "global_load_dwordx4 v[68:71], v9, s[30:31] sc1\n\t" \
    "s_waitcnt vmcnt(0)\n\t" \
    "v_max_u32 v160, v36, v37\n\t" \
    "v_max3_u32 v160, v160, v38, v39\n\t" \
    "v_max3_u32 v160, v160, v40, v41\n\t" \
    "v_max3_u32 v160, v160, v42, v43\n\t" \
    "v_max3_u32 v160, v160, v44, v45\n\t" \
    "v_max3_u32 v160, v160, v46, v47\n\t" \
    "v_max3_u32 v160, v160, v48, v49\n\t" \
    "v_max3_u32 v160, v160, v50, v51\n\t" \
    "v_max3_u32 v160, v160, v52, v53\n\t" \
    "v_max3_u32 v160, v160, v54, v55\n\t" \
    "v_max3_u32 v160, v160, v56, v57\n\t" \
    "v_max3_u32 v160, v160, v58, v59\n\t" \
    "v_max3_u32 v160, v160, v60, v61\n\t" \
    "v_max3_u32 v160, v160, v62, v63\n\t" \
    "v_max3_u32 v160, v160, v64, v65\n\t" \
    "v_max3_u32 v160, v160, v66, v67\n\t" \
    "v_max3_u32 v160, v160, v68, v69\n\t" \
    "v_max3_u32 v160, v160, v70, v71\n\t" \
    "v_cmp_eq_u32 vcc, -1, v160\n\t" \
    "s_cbranch_vccz qk_Gredo97_%=\n\t" \
    "s_cmp_eq_u32 s68, 0\n\t" \
    "s_cbranch_scc1 qk_F98_%=\n\t" \
    "s_sub_u32 s68, s68, 1\n\t" \
    "s_sleep 1\n\t" \
    "s_branch qk_Gag96_%=\n\t" \
    "qk_F98_%=:\n\t" \
    "v_mov_b32 v160, 5767424\n\t" \
    "v_mov_b32 v161, 2\n\t" \
    "global_store_dword v160, v161, s[36:37] sc1\n\t" \
    "s_waitcnt vmcnt(0)\n\t" \
    "s_branch qk_Gredo97_%=\n\t" \
    "qk_Gredo97_%=:\n\t" \
    "v_mov_b32 v72, 0\n\t" \
    "v_mov_b32 v73, 0\n\t" \
    "v_mov_b32 v74, 0\n\t" \
    "v_mov_b32 v75, 0\n\t" \
    "v_mov_b32 v76, 0\n\t" \
    "v_mov_b32 v77, 0\n\t" \
    "v_mov_b32 v78, 0\n\t" \
    "v_mov_b32 v79, 0\n\t" \
    "v_mov_b32 v80, 0\n\t" \
    "v_mov_b32 v81, 0\n\t" \
    "v_mov_b32 v82, 0\n\t" \
    "v_mov_b32 v83, 0\n\t" \
    "v_mov_b32 v84, 0\n\t" \
    "v_mov_b32 v85, 0\n\t" \
    "v_mov_b32 v86, 0\n\t" \
    "v_mov_b32 v87, 0\n\t" \
    "v_smfmac_f32_16x16x64_bf16 v[72:75], v[36:39], a[48:55], v10\n\t" \
    "v_smfmac_f32_16x16x64_bf16 v[72:75], v[40:43], a[56:63], v10\n\t" \
    "v_smfmac_f32_16x16x64_bf16 v[72:75], v[48:51], a[0:7], v10\n\t" \
    "v_smfmac_f32_16x16x64_bf16 v[72:75], v[52:55], a[8:15], v10\n\t" \
    "v_smfmac_f32_16x16x64_bf16 v[72:75], v[56:59], a[16:23], v10\n\t" \
    "v_smfmac_f32_16x16x64_bf16 v[72:75], v[60:63], a[24:31], v10\n\t" \
    "v_smfmac_f32_16x16x64_bf16 v[72:75], v[64:67], a[32:39], v10\n\t" \
    "v_smfmac_f32_16x16x64_bf16 v[72:75], v[68:71], a[40:47], v10\n\t" \
    "v_smfmac_f32_16x16x64_bf16 v[72:75], v[44:47], a[64:71], v10\n\t" \
    "v_smfmac_f32_16x16x64_bf16 v[76:79], v[36:39], a[120:127], v10\n\t" \
    "v_smfmac_f32_16x16x64_bf16 v[76:79], v[40:43], a[128:135], v10\n\t" \
    "v_smfmac_f32_16x16x64_bf16 v[76:79], v[48:51], a[72:79], v10\n\t" \
    "v_smfmac_f32_16x16x64_bf16 v[76:79], v[52:55], a[80:87], v10\n\t" \
    "v_smfmac_f32_16x16x64_bf16 v[76:79], v[56:59], a[88:95], v10\n\t" \
    "v_smfmac_f32_16x16x64_bf16 v[76:79], v[60:63], a[96:103], v10\n\t" \
    "v_smfmac_f32_16x16x64_bf16 v[76:79], v[64:67], a[104:111], v10\n\t" \
    "v_smfmac_f32_16x16x64_bf16 v[76:79], v[68:71], a[112:119], v10\n\t" \
    "v_smfmac_f32_16x16x64_bf16 v[76:79], v[44:47], a[136:143], v10\n\t" \
    "v_smfmac_f32_16x16x64_bf16 v[80:83], v[36:39], a[192:199], v10\n\t" \
    "v_smfmac_f32_16x16x64_bf16 v[80:83], v[40:43], a[200:207], v10\n\t" \
    "v_smfmac_f32_16x16x64_bf16 v[80:83], v[48:51], a[144:151], v10\n\t" \
    "v_smfmac_f32_16x16x64_bf16 v[80:83], v[52:55], a[152:159], v10\n\t" \
    "v_smfmac_f32_16x16x64_bf16 v[80:83], v[56:59], a[160:167], v10\n\t" \
    "v_smfmac_f32_16x16x64_bf16 v[80:83], v[60:63], a[168:175], v10\n\t" \
    "v_smfmac_f32_16x16x64_bf16 v[80:83], v[64:67], a[176:183], v10\n\t" \
    "v_smfmac_f32_16x16x64_bf16 v[80:83], v[68:71], a[184:191], v10\n\t" \
    "v_smfmac_f32_16x16x64_bf16 v[80:83], v[44:47], a[208:215], v10\n\t" \
    "v_smfmac_f32_16x16x64_bf16 v[84:87], v[36:39], v[232:239], v10\n\t" \
    "v_smfmac_f32_16x16x64_bf16 v[84:87], v[40:43], v[240:247], v10\n\t" \
    "v_smfmac_f32_16x16x64_bf16 v[84:87], v[48:51], a[216:223], v10\n\t" \
    "v_smfmac_f32_16x16x64_bf16 v[84:87], v[52:55], a[224:231], v10\n\t" \
    "v_smfmac_f32_16x16x64_bf16 v[84:87], v[56:59], a[232:239], v10\n\t" \
    "v_smfmac_f32_16x16x64_bf16 v[84:87], v[60:63], a[240:247], v10\n\t" \
    "v_smfmac_f32_16x16x64_bf16 v[84:87], v[64:67], a[248:255], v10\n\t" \
    "v_smfmac_f32_16x16x64_bf16 v[84:87], v[68:71], v[224:231], v10\n\t" \
    "v_smfmac_f32_16x16x64_bf16 v[84:87], v[44:47], v[248:255], v10\n\t" \
    "v_add_f32 v72, v72, v74\n\t" \
    "v_add_f32 v73, v73, v75\n\t" \
    "v_cvt_pk_bf16_f32 v156, v72, v73\n\t" \
    "ds_write_b32 v12, v156 offset:0\n\t" \
    "v_add_f32 v76, v76, v78\n\t" \
    "v_add_f32 v77, v77, v79\n\t" \
    "v_cvt_pk_bf16_f32 v157, v76, v77\n\t" \
    "ds_write_b32 v12, v157 offset:64\n\t" \
    "v_add_f32 v80, v80, v82\n\t" \
    "v_add_f32 v81, v81, v83\n\t" \
    "v_cvt_pk_bf16_f32 v158, v80, v81\n\t" \
    "ds_write_b32 v16, v158 offset:0\n\t" \
    "v_pk_add_f32 v[84:85], v[84:85], v[86:87]\n\t" \
    "v_cvt_pk_bf16_f32 v159, v84, v85\n\t" \
    "ds_write_b32 v16, v159 offset:256\n\t" \
    "s_branch qk_Gback83_%=\n\t" \
    "qk_last74_%=:\n\t" \
    "s_endpgm\n\t" \
    "qk_last_%=:\n\t" \
    ""
#define ASM_V \
    "s_load_dwordx2 s[42:43], %3, 56\n\t" \
    "s_load_dwordx2 s[44:45], %3, 64\n\t" \
    "s_load_dwordx2 s[36:37], %3, 0x58\n\t" \
    "s_load_dwordx2 s[50:51], %3, 0x48\n\t" \
    "s_load_dwordx8 s[84:91], %3, 0\n\t" \
    "s_mov_b32 s66, %4\n\t" \
    "s_mov_b32 s67, %5\n\t" \
    "s_mov_b32 s68, 262144\n\t" \
    "s_mov_b32 s72, 0\n\t" \
    "s_mov_b32 s83, -1\n\t" \
    "s_mov_b32 s65, 0\n\t" \
    "s_lshl_b32 s82, s66, 10\n\t" \
    "s_waitcnt lgkmcnt(0)\n\t" \
    "s_add_u32 s46, s42, 2097152\n\t" \
    "s_addc_u32 s47, s43, 0\n\t" \
    "s_add_u32 m0, s82, 65536\n\t" \
    "s_nop 0\n\t" \
    "global_load_lds_dwordx4 v200, s[46:47]\n\t" \
    "s_add_u32 s46, s42, 2228224\n\t" \
    "s_addc_u32 s47, s43, 0\n\t" \
    "s_add_u32 m0, s82, 69632\n\t" \
    "s_nop 0\n\t" \
    "global_load_lds_dwordx4 v200, s[46:47]\n\t" \
    "s_add_u32 s46, s42, 2359296\n\t" \
    "s_addc_u32 s47, s43, 0\n\t" \
    "s_add_u32 m0, s82, 73728\n\t" \
    "s_nop 0\n\t" \
    "global_load_lds_dwordx4 v200, s[46:47]\n\t" \
    "s_add_u32 s46, s42, 2490368\n\t" \
    "s_addc_u32 s47, s43, 0\n\t" \
    "s_add_u32 m0, s82, 77824\n\t" \
    "s_nop 0\n\t" \
    "global_load_lds_dwordx4 v200, s[46:47]\n\t" \
    "s_add_u32 s46, s42, 2621440\n\t" \
    "s_addc_u32 s47, s43, 0\n\t" \
    "s_add_u32 m0, s82, 81920\n\t" \
    "s_nop 0\n\t" \
    "global_load_lds_dwordx4 v200, s[46:47]\n\t" \
    "s_add_u32 s46, s42, 2752512\n\t" \
    "s_addc_u32 s47, s43, 0\n\t" \
    "s_add_u32 m0, s82, 86016\n\t" \
    "s_nop 0\n\t" \
    "global_load_lds_dwordx4 v200, s[46:47]\n\t" \
    "s_add_u32 s46, s42, 2883584\n\t" \
    "s_addc_u32 s47, s43, 0\n\t" \
    "s_add_u32 m0, s82, 90112\n\t" \
    "s_nop 0\n\t" \
    "global_load_lds_dwordx4 v200, s[46:47]\n\t" \
    "s_add_u32 s46, s42, 3014656\n\t" \
    "s_addc_u32 s47, s43, 0\n\t" \
    "s_add_u32 m0, s82, 94208\n\t" \
    "s_nop 0\n\t" \
    "global_load_lds_dwordx4 v200, s[46:47]\n\t" \
    "s_add_u32 s46, s42, 3145728\n\t" \
    "s_addc_u32 s47, s43, 0\n\t" \
    "s_add_u32 m0, s82, 98304\n\t" \
    "s_nop 0\n\t" \
    "global_load_lds_dwordx4 v200, s[46:47]\n\t" \
    "s_add_u32 s46, s42, 3276800\n\t" \
    "s_addc_u32 s47, s43, 0\n\t" \
    "s_add_u32 m0, s82, 102400\n\t" \
    "s_nop 0\n\t" \
    "global_load_lds_dwordx4 v200, s[46:47]\n\t" \
    "s_add_u32 s46, s42, 3407872\n\t" \
    "s_addc_u32 s47, s43, 0\n\t" \
    "s_add_u32 m0, s82, 106496\n\t" \
    "s_nop 0\n\t" \
    "global_load_lds_dwordx4 v200, s[46:47]\n\t" \
    "s_add_u32 s46, s42, 3538944\n\t" \
    "s_addc_u32 s47, s43, 0\n\t" \
    "s_add_u32 m0, s82, 110592\n\t" \
    "s_nop 0\n\t" \
    "global_load_lds_dwordx4 v200, s[46:47]\n\t" \
    "s_add_u32 s46, s42, 3670016\n\t" \
    "s_addc_u32 s47, s43, 0\n\t" \
    "s_add_u32 m0, s82, 114688\n\t" \
    "s_nop 0\n\t" \
    "global_load_lds_dwordx4 v200, s[46:47]\n\t" \
    "s_add_u32 s46, s42, 3801088\n\t" \
    "s_addc_u32 s47, s43, 0\n\t" \
    "s_add_u32 m0, s82, 118784\n\t" \
    "s_nop 0\n\t" \
    "global_load_lds_dwordx4 v200, s[46:47]\n\t" \
    "s_add_u32 s46, s42, 3932160\n\t" \
    "s_addc_u32 s47, s43, 0\n\t" \
    "s_add_u32 m0, s82, 122880\n\t" \
    "s_nop 0\n\t" \
    "global_load_lds_dwordx4 v200, s[46:47]\n\t" \
    "s_add_u32 s46, s42, 4063232\n\t" \
    "s_addc_u32 s47, s43, 0\n\t" \
    "s_add_u32 m0, s82, 126976\n\t" \
    "s_nop 0\n\t" \
    "global_load_lds_dwordx4 v200, s[46:47]\n\t" \
    "s_add_u32 s46, s42, 4194304\n\t" \
    "s_addc_u32 s47, s43, 0\n\t" \
    "s_add_u32 m0, s82, 131072\n\t" \
    "s_nop 0\n\t" \
    "global_load_lds_dwordx4 v200, s[46:47]\n\t" \
    "s_add_u32 s46, s42, 4325376\n\t" \
    "s_addc_u32 s47, s43, 0\n\t" \
    "s_add_u32 m0, s82, 135168\n\t" \
    "s_nop 0\n\t" \
    "global_load_lds_dwordx4 v200, s[46:47]\n\t" \
    "s_add_u32 s46, s42, 4456448\n\t" \
    "s_addc_u32 s47, s43, 0\n\t" \
    "s_add_u32 m0, s82, 139264\n\t" \
    "s_nop 0\n\t" \
    "global_load_lds_dwordx4 v200, s[46:47]\n\t" \
    "s_add_u32 s46, s42, 4587520\n\t" \
    "s_addc_u32 s47, s43, 0\n\t" \
    "s_add_u32 m0, s82, 143360\n\t" \
    "s_nop 0\n\t" \
    "global_load_lds_dwordx4 v200, s[46:47]\n\t" \
    "s_mov_b32 s77, 3019898880\n\t" \
    "s_mov_b32 s78, 3054198966\n\t" \
    "s_waitcnt lgkmcnt(0)\n\t" \
    "s_mov_b32 s70, 116\n\t" \
    "vv_E1_%=:\n\t" \
    "s_memtime s[56:57]\n\t" \
    "s_memrealtime s[2:3]\n\t" \
    "s_nop 0\n\t" \
    "s_memrealtime s[4:5]\n\t" \
    "s_nop 0\n\t" \
    "s_memrealtime s[6:7]\n\t" \
    "s_nop 0\n\t" \
    "s_memrealtime s[8:9]\n\t" \
    "s_nop 0\n\t" \
    "s_memrealtime s[10:11]\n\t" \
    "s_nop 0\n\t" \
    "s_memrealtime s[12:13]\n\t" \
    "s_nop 0\n\t" \
    "s_memrealtime s[14:15]\n\t" \
    "s_nop 0\n\t" \
    "s_memrealtime s[16:17]\n\t" \
    "s_nop 0\n\t" \
    "s_memrealtime s[18:19]\n\t" \
    "s_nop 0\n\t" \
    "s_memrealtime s[20:21]\n\t" \
    "s_nop 0\n\t" \
    "s_memrealtime s[22:23]\n\t" \
    "s_nop 0\n\t" \
    "s_memrealtime s[24:25]\n\t" \
    "s_nop 0\n\t" \
    "s_memrealtime s[26:27]\n\t" \
    "s_nop 0\n\t" \
    "s_memrealtime s[28:29]\n\t" \
    "s_memtime s[54:55]\n\t" \
    "s_add_u32 s70, s70, 8\n\t" \
    "s_waitcnt lgkmcnt(0)\n\t" \
    "s_sub_u32 s69, s54, s56\n\t" \
    "s_cmp_gt_u32 s69, s70\n\t" \
    "s_cbranch_scc1 vv_E1_%=\n\t" \
    "s_add_u32 s2, s2, s4\n\t" \
    "s_add_u32 s6, s6, s8\n\t" \
    "s_add_u32 s10, s10, s12\n\t" \
    "s_add_u32 s14, s14, s16\n\t" \
    "s_add_u32 s18, s18, s20\n\t" \
    "s_add_u32 s22, s22, s24\n\t" \
    "s_add_u32 s2, s2, s6\n\t" \
    "s_add_u32 s10, s10, s14\n\t" \
    "s_add_u32 s18, s18, s22\n\t" \
    "s_add_u32 s2, s2, s26\n\t" \
    "s_add_u32 s10, s10, s18\n\t" \
    "s_add_u32 s2, s2, s10\n\t" \
    "s_mul_i32 s70, s28, 13\n\t" \
    "s_sub_u32 s70, s70, s2\n\t" \
    "s_min_u32 s70, s70, 52\n\t" \
    "s_mul_i32 s69, s69, s70\n\t" \
    "s_mul_hi_u32 s69, s69, 76695844\n\t" \
    "s_mov_b32 s75, s73\n\t" \
    "s_mov_b32 s76, s74\n\t" \
    "s_add_u32 s73, s56, s69\n\t" \
    "s_lshl_b32 s74, s28, 4\n\t" \
    "s_waitcnt vmcnt(32) lgkmcnt(0)\n\t" \
    "s_barrier\n\t" \
    "ds_read_b64_tr_b16 a[0:1], v201 offset:0\n\t" \
    "ds_read_b64_tr_b16 a[2:3], v201 offset:1024\n\t" \
    "ds_read_b64_tr_b16 a[4:5], v201 offset:8192\n\t" \
    "ds_read_b64_tr_b16 a[6:7], v201 offset:9216\n\t" \
    "ds_read_b64_tr_b16 a[72:73], v202 offset:0\n\t" \
    "ds_read_b64_tr_b16 a[74:75], v202 offset:1024\n\t" \
    "ds_read_b64_tr_b16 a[76:77], v202 offset:8192\n\t" \
    "ds_read_b64_tr_b16 a[78:79], v202 offset:9216\n\t" \
    "s_waitcnt vmcnt(28) lgkmcnt(0)\n\t" \
    "s_barrier\n\t" \
    "ds_read_b64_tr_b16 a[8:9], v201 offset:16384\n\t" \
    "ds_read_b64_tr_b16 a[10:11], v201 offset:17408\n\t" \
    "ds_read_b64_tr_b16 a[12:13], v201 offset:24576\n\t" \
    "ds_read_b64_tr_b16 a[14:15], v201 offset:25600\n\t" \
    "ds_read_b64_tr_b16 a[80:81], v202 offset:16384\n\t" \
    "ds_read_b64_tr_b16 a[82:83], v202 offset:17408\n\t" \
    "ds_read_b64_tr_b16 a[84:85], v202 offset:24576\n\t" \
    "ds_read_b64_tr_b16 a[86:87], v202 offset:25600\n\t" \
    "s_waitcnt vmcnt(24) lgkmcnt(0)\n\t" \
    "s_barrier\n\t" \
    "ds_read_b64_tr_b16 a[16:17], v201 offset:32768\n\t" \
    "ds_read_b64_tr_b16 a[18:19], v201 offset:33792\n\t" \
    "ds_read_b64_tr_b16 a[20:21], v201 offset:40960\n\t" \
    "ds_read_b64_tr_b16 a[22:23], v201 offset:41984\n\t" \
    "ds_read_b64_tr_b16 a[88:89], v202 offset:32768\n\t" \
    "ds_read_b64_tr_b16 a[90:91], v202 offset:33792\n\t" \
    "ds_read_b64_tr_b16 a[92:93], v202 offset:40960\n\t" \
    "ds_read_b64_tr_b16 a[94:95], v202 offset:41984\n\t" \
    "s_waitcnt vmcnt(20) lgkmcnt(0)\n\t" \
    "s_barrier\n\t" \
    "ds_read_b64_tr_b16 a[24:25], v201 offset:49152\n\t" \
    "ds_read_b64_tr_b16 a[26:27], v201 offset:50176\n\t" \
    "ds_read_b64_tr_b16 a[28:29], v201 offset:57344\n\t" \
    "ds_read_b64_tr_b16 a[30:31], v201 offset:58368\n\t" \
    "ds_read_b64_tr_b16 a[96:97], v202 offset:49152\n\t" \
    "ds_read_b64_tr_b16 a[98:99], v202 offset:50176\n\t" \
    "ds_read_b64_tr_b16 a[100:101], v202 offset:57344\n\t" \
    "ds_read_b64_tr_b16 a[102:103], v202 offset:58368\n\t" \
    "s_add_u32 s46, s44, 0\n\t" \
    "s_addc_u32 s47, s45, 0\n\t" \
    "s_mul_i32 s69, s82, 9\n\t" \
    "s_add_u32 m0, s69, 0\n\t" \
    "s_nop 0\n\t" \
    "global_load_lds_dwordx4 v203, s[46:47]\n\t" \
    "s_add_u32 m0, s69, 1024\n\t" \
    "s_nop 0\n\t" \
    "global_load_lds_dwordx4 v204, s[46:47]\n\t" \
    "s_add_u32 m0, s69, 2048\n\t" \
    "s_nop 0\n\t" \
    "global_load_lds_dwordx4 v205, s[46:47]\n\t" \
    "s_add_u32 m0, s69, 3072\n\t" \
    "s_nop 0\n\t" \
    "global_load_lds_dwordx4 v206, s[46:47]\n\t" \
    "s_add_u32 m0, s69, 4096\n\t" \
    "s_nop 0\n\t" \
    "global_load_lds_dwordx4 v207, s[46:47]\n\t" \
    "s_add_u32 m0, s69, 5120\n\t" \
    "s_nop 0\n\t" \
    "global_load_lds_dwordx4 v208, s[46:47]\n\t" \
    "s_add_u32 m0, s69, 6144\n\t" \
    "s_nop 0\n\t" \
    "global_load_lds_dwordx4 v209, s[46:47]\n\t" \
    "s_add_u32 m0, s69, 7168\n\t" \
    "s_nop 0\n\t" \
    "global_load_lds_dwordx4 v210, s[46:47]\n\t" \
    "s_add_u32 m0, s69, 8192\n\t" \
    "s_nop 0\n\t" \
    "global_load_lds_dwordx4 v211, s[46:47]\n\t" \
    "s_waitcnt vmcnt(25) lgkmcnt(0)\n\t" \
    "s_barrier\n\t" \
    "v_add_u32 v201, 0x10000, v201\n\t" \
    "v_add_u32 v202, 0x10000, v202\n\t" \
    "ds_read_b64_tr_b16 a[32:33], v201 offset:0\n\t" \
    "ds_read_b64_tr_b16 a[34:35], v201 offset:1024\n\t" \
    "ds_read_b64_tr_b16 a[36:37], v201 offset:8192\n\t" \
    "ds_read_b64_tr_b16 a[38:39], v201 offset:9216\n\t" \
    "ds_read_b64_tr_b16 a[104:105], v202 offset:0\n\t" \
    "ds_read_b64_tr_b16 a[106:107], v202 offset:1024\n\t" \
    "ds_read_b64_tr_b16 a[108:109], v202 offset:8192\n\t" \
    "ds_read_b64_tr_b16 a[110:111], v202 offset:9216\n\t" \
    "s_waitcnt vmcnt(21) lgkmcnt(0)\n\t" \
    "s_barrier\n\t" \
    "ds_read_b64_tr_b16 a[40:41], v201 offset:16384\n\t" \
    "ds_read_b64_tr_b16 a[42:43], v201 offset:17408\n\t" \
    "ds_read_b64_tr_b16 a[44:45], v201 offset:24576\n\t" \
    "ds_read_b64_tr_b16 a[46:47], v201 offset:25600\n\t" \
    "ds_read_b64_tr_b16 a[112:113], v202 offset:16384\n\t" \
    "ds_read_b64_tr_b16 a[114:115], v202 offset:17408\n\t" \
    "ds_read_b64_tr_b16 a[116:117], v202 offset:24576\n\t" \
    "ds_read_b64_tr_b16 a[118:119], v202 offset:25600\n\t" \
    "s_add_u32 s46, s44, 36864\n\t" \
    "s_addc_u32 s47, s45, 0\n\t" \
    "s_mul_i32 s69, s82, 9\n\t" \
    "s_add_u32 m0, s69, 36864\n\t" \
    "s_nop 0\n\t" \
    "global_load_lds_dwordx4 v203, s[46:47]\n\t" \
    "s_add_u32 m0, s69, 37888\n\t" \
    "s_nop 0\n\t" \
    "global_load_lds_dwordx4 v204, s[46:47]\n\t" \
    "s_add_u32 m0, s69, 38912\n\t" \
    "s_nop 0\n\t" \
    "global_load_lds_dwordx4 v205, s[46:47]\n\t" \
    "s_add_u32 m0, s69, 39936\n\t" \
    "s_nop 0\n\t" \
    "global_load_lds_dwordx4 v206, s[46:47]\n\t" \
    "s_add_u32 m0, s69, 40960\n\t" \
    "s_nop 0\n\t" \
    "global_load_lds_dwordx4 v207, s[46:47]\n\t" \
    "s_add_u32 m0, s69, 41984\n\t" \
    "s_nop 0\n\t" \
    "global_load_lds_dwordx4 v208, s[46:47]\n\t" \
    "s_add_u32 m0, s69, 43008\n\t" \
    "s_nop 0\n\t" \
    "global_load_lds_dwordx4 v209, s[46:47]\n\t" \
    "s_add_u32 m0, s69, 44032\n\t" \
    "s_nop 0\n\t" \
    "global_load_lds_dwordx4 v210, s[46:47]\n\t" \
    "s_add_u32 m0, s69, 45056\n\t" \
    "s_nop 0\n\t" \
    "global_load_lds_dwordx4 v211, s[46:47]\n\t" \
    "s_waitcnt vmcnt(26) lgkmcnt(0)\n\t" \
    "s_barrier\n\t" \
    "ds_read_b64_tr_b16 a[48:49], v201 offset:32768\n\t" \
    "ds_read_b64_tr_b16 a[50:51], v201 offset:33792\n\t" \
    "ds_read_b64_tr_b16 a[52:53], v201 offset:40960\n\t" \
    "ds_read_b64_tr_b16 a[54:55], v201 offset:41984\n\t" \
    "ds_read_b64_tr_b16 a[120:121], v202 offset:32768\n\t" \
    "ds_read_b64_tr_b16 a[122:123], v202 offset:33792\n\t" \
    "ds_read_b64_tr_b16 a[124:125], v202 offset:40960\n\t" \
    "ds_read_b64_tr_b16 a[126:127], v202 offset:41984\n\t" \
    "s_waitcnt vmcnt(22) lgkmcnt(0)\n\t" \
    "s_barrier\n\t" \
    "ds_read_b64_tr_b16 a[56:57], v201 offset:49152\n\t" \
    "ds_read_b64_tr_b16 a[58:59], v201 offset:50176\n\t" \
    "ds_read_b64_tr_b16 a[60:61], v201 offset:57344\n\t" \
    "ds_read_b64_tr_b16 a[62:63], v201 offset:58368\n\t" \
    "ds_read_b64_tr_b16 a[128:129], v202 offset:49152\n\t" \
    "ds_read_b64_tr_b16 a[130:131], v202 offset:50176\n\t" \
    "ds_read_b64_tr_b16 a[132:133], v202 offset:57344\n\t" \
    "ds_read_b64_tr_b16 a[134:135], v202 offset:58368\n\t" \
    "s_add_u32 s46, s44, 73728\n\t" \
    "s_addc_u32 s47, s45, 0\n\t" \
    "s_mul_i32 s69, s82, 9\n\t" \
    "s_add_u32 m0, s69, 73728\n\t" \
    "s_nop 0\n\t" \
    "global_load_lds_dwordx4 v203, s[46:47]\n\t" \
    "s_add_u32 m0, s69, 74752\n\t" \
    "s_nop 0\n\t" \
    "global_load_lds_dwordx4 v204, s[46:47]\n\t" \
    "s_add_u32 m0, s69, 75776\n\t" \
    "s_nop 0\n\t" \
    "global_load_lds_dwordx4 v205, s[46:47]\n\t" \
    "s_add_u32 m0, s69, 76800\n\t" \
    "s_nop 0\n\t" \
    "global_load_lds_dwordx4 v206, s[46:47]\n\t" \
    "s_add_u32 m0, s69, 77824\n\t" \
    "s_nop 0\n\t" \
    "global_load_lds_dwordx4 v207, s[46:47]\n\t" \
    "s_add_u32 m0, s69, 78848\n\t" \
    "s_nop 0\n\t" \
    "global_load_lds_dwordx4 v208, s[46:47]\n\t" \
    "s_add_u32 m0, s69, 79872\n\t" \
    "s_nop 0\n\t" \
    "global_load_lds_dwordx4 v209, s[46:47]\n\t" \
    "s_add_u32 m0, s69, 80896\n\t" \
    "s_nop 0\n\t" \
    "global_load_lds_dwordx4 v210, s[46:47]\n\t" \
    "s_add_u32 m0, s69, 81920\n\t" \
    "s_nop 0\n\t" \
    "global_load_lds_dwordx4 v211, s[46:47]\n\t" \
    "v_mbcnt_lo_u32_b32 v140, -1, 0\n\t" \
    "v_mbcnt_hi_u32_b32 v140, -1, v140\n\t" \
    "v_lshlrev_b32 v140, 6, v140\n\t" \
    "v_add_u32 v140, 6225920, v140\n\t" \
    "v_mov_b32 v141, 6234112\n\t" \
    "global_load_dword v141, v141, s[36:37] sc1\n\t" \
    "global_load_dwordx2 v[142:143], v140, s[36:37] sc1\n\t" \
    "global_load_dword v144, v140, s[36:37] offset:2048 sc1\n\t" \
    "s_waitcnt vmcnt(30) lgkmcnt(0)\n\t" \
    "s_barrier\n\t" \
    "v_add_u32 v201, 0x10000, v201\n\t" \
    "v_add_u32 v202, 0x10000, v202\n\t" \
    "ds_read_b64_tr_b16 a[64:65], v201 offset:0\n\t" \
    "ds_read_b64_tr_b16 a[66:67], v201 offset:1024\n\t" \
    "ds_read_b64_tr_b16 a[68:69], v201 offset:8192\n\t" \
    "ds_read_b64_tr_b16 a[70:71], v201 offset:9216\n\t" \
    "ds_read_b64_tr_b16 a[136:137], v202 offset:0\n\t" \
    "ds_read_b64_tr_b16 a[138:139], v202 offset:1024\n\t" \
    "ds_read_b64_tr_b16 a[140:141], v202 offset:8192\n\t" \
    "ds_read_b64_tr_b16 a[142:143], v202 offset:9216\n\t" \
    "v_add_u32 v201, 0xfffe0000, v201\n\t" \
    "v_add_u32 v202, 0xfffe0000, v202\n\t" \
    "s_waitcnt lgkmcnt(0)\n\t" \
    "s_barrier\n\t" \
    "s_add_u32 s46, s44, 110592\n\t" \
    "s_addc_u32 s47, s45, 0\n\t" \
    "s_mul_i32 s69, s82, 9\n\t" \
    "s_add_u32 m0, s69, 110592\n\t" \
    "s_nop 0\n\t" \
    "global_load_lds_dwordx4 v203, s[46:47]\n\t" \
    "s_add_u32 m0, s69, 111616\n\t" \
    "s_nop 0\n\t" \
    "global_load_lds_dwordx4 v204, s[46:47]\n\t" \
    "s_add_u32 m0, s69, 112640\n\t" \
    "s_nop 0\n\t" \
    "global_load_lds_dwordx4 v205, s[46:47]\n\t" \
    "s_add_u32 m0, s69, 113664\n\t" \
    "s_nop 0\n\t" \
    "global_load_lds_dwordx4 v206, s[46:47]\n\t" \
    "s_add_u32 m0, s69, 114688\n\t" \
    "s_nop 0\n\t" \
    "global_load_lds_dwordx4 v207, s[46:47]\n\t" \
    "s_add_u32 m0, s69, 115712\n\t" \
    "s_nop 0\n\t" \
    "global_load_lds_dwordx4 v208, s[46:47]\n\t" \
    "s_add_u32 m0, s69, 116736\n\t" \
    "s_nop 0\n\t" \
    "global_load_lds_dwordx4 v209, s[46:47]\n\t" \
    "s_add_u32 m0, s69, 117760\n\t" \
    "s_nop 0\n\t" \
    "global_load_lds_dwordx4 v210, s[46:47]\n\t" \
    "s_add_u32 m0, s69, 118784\n\t" \
    "s_nop 0\n\t" \
    "global_load_lds_dwordx4 v211, s[46:47]\n\t" \
    "s_waitcnt vmcnt(21) lgkmcnt(0)\n\t" \
    "s_barrier\n\t" \
    "ds_read_b64_tr_b16 a[144:145], v212 offset:0\n\t" \
    "ds_read_b64_tr_b16 a[146:147], v212 offset:2304\n\t" \
    "ds_read_b64_tr_b16 a[148:149], v212 offset:18432\n\t" \
    "ds_read_b64_tr_b16 a[150:151], v212 offset:20736\n\t" \
    "s_waitcnt lgkmcnt(6)\n\t" \
    "ds_read_b64_tr_b16 a[176:177], v213 offset:0\n\t" \
    "ds_read_b64_tr_b16 a[178:179], v213 offset:2304\n\t" \
    "ds_read_b64_tr_b16 a[180:181], v213 offset:18432\n\t" \
    "ds_read_b64_tr_b16 a[182:183], v213 offset:20736\n\t" \
    "s_waitcnt lgkmcnt(6)\n\t" \
    "ds_read_b64_tr_b16 a[208:209], v212 offset:128\n\t" \
    "ds_read_b64_tr_b16 a[210:211], v212 offset:2432\n\t" \
    "ds_read_b64_tr_b16 a[212:213], v212 offset:18560\n\t" \
    "ds_read_b64_tr_b16 a[214:215], v212 offset:20864\n\t" \
    "s_waitcnt lgkmcnt(6)\n\t" \
    "ds_read_b64_tr_b16 a[240:241], v213 offset:128\n\t" \
    "ds_read_b64_tr_b16 a[242:243], v213 offset:2432\n\t" \
    "ds_read_b64_tr_b16 a[244:245], v213 offset:18560\n\t" \
    "ds_read_b64_tr_b16 a[246:247], v213 offset:20864\n\t" \
    "s_waitcnt lgkmcnt(6)\n\t" \
    "ds_read_b64_tr_b16 a[152:153], v214 offset:0\n\t" \
    "ds_read_b64_tr_b16 a[154:155], v214 offset:2304\n\t" \
    "ds_read_b64_tr_b16 a[156:157], v214 offset:18432\n\t" \
    "ds_read_b64_tr_b16 a[158:159], v214 offset:20736\n\t" \
    "s_waitcnt lgkmcnt(6)\n\t" \
    "ds_read_b64_tr_b16 a[184:185], v215 offset:0\n\t" \
    "ds_read_b64_tr_b16 a[186:187], v215 offset:2304\n\t" \
    "ds_read_b64_tr_b16 a[188:189], v215 offset:18432\n\t" \
    "ds_read_b64_tr_b16 a[190:191], v215 offset:20736\n\t" \
    "s_waitcnt lgkmcnt(6)\n\t" \
    "ds_read_b64_tr_b16 a[216:217], v214 offset:128\n\t" \
    "ds_read_b64_tr_b16 a[218:219], v214 offset:2432\n\t" \
    "ds_read_b64_tr_b16 a[220:221], v214 offset:18560\n\t" \
    "ds_read_b64_tr_b16 a[222:223], v214 offset:20864\n\t" \
    "s_waitcnt lgkmcnt(6)\n\t" \
    "ds_read_b64_tr_b16 a[248:249], v215 offset:128\n\t" \
    "ds_read_b64_tr_b16 a[250:251], v215 offset:2432\n\t" \
    "ds_read_b64_tr_b16 a[252:253], v215 offset:18560\n\t" \
    "ds_read_b64_tr_b16 a[254:255], v215 offset:20864\n\t" \
    "s_waitcnt lgkmcnt(6)\n\t" \
    "ds_read_b64_tr_b16 v[240:241], v24 offset:0\n\t" \
    "ds_read_b64_tr_b16 v[242:243], v24 offset:2304\n\t" \
    "ds_read_b64_tr_b16 v[244:245], v24 offset:18432\n\t" \
    "ds_read_b64_tr_b16 v[246:247], v24 offset:20736\n\t" \
    "v_add_u32 v212, 0x12000, v212\n\t" \
    "v_add_u32 v213, 0x12000, v213\n\t" \
    "v_add_u32 v214, 0x12000, v214\n\t" \
    "v_add_u32 v215, 0x12000, v215\n\t" \
    "v_add_u32 v24, 0x12000, v24\n\t" \
    "s_waitcnt vmcnt(9)\n\t" \
    "vv_C2_%=:\n\t" \
    "v_cmp_ne_u32 s[54:55], v142, v144\n\t" \
    "v_sub_u32 v142, v142, v141\n\t" \
    "v_cmp_ne_u32 vcc, 1, v142\n\t" \
    "s_nop 1\n\t" \
    "s_or_b64 vcc, vcc, s[54:55]\n\t" \
    "s_cbranch_scc0 vv_Cok3_%=\n\t" \
    "s_cmp_eq_u32 s68, 0\n\t" \
    "s_cbranch_scc1 vv_F5_%=\n\t" \
    "s_sub_u32 s68, s68, 1\n\t" \
    "s_sleep 6\n\t" \
    "s_branch vv_Cag4_%=\n\t" \
    "vv_F5_%=:\n\t" \
    "v_mov_b32 v144, 5767424\n\t" \
    "v_mov_b32 v145, 4\n\t" \
    "global_store_dword v144, v145, s[36:37] sc1\n\t" \
    "s_waitcnt vmcnt(0)\n\t" \
    "s_branch vv_Cok3_%=\n\t" \
    "vv_Cag4_%=:\n\t" \
    "global_load_dwordx2 v[142:143], v140, s[36:37] sc1\n\t" \
    "global_load_dword v144, v140, s[36:37] offset:2048 sc1\n\t" \
    "s_waitcnt vmcnt(0)\n\t" \
    "s_branch vv_C2_%=\n\t" \
    "vv_Cok3_%=:\n\t" \
    "s_cmp_lg_u32 s66, 0\n\t" \
    "s_cbranch_scc1 vv_Ca6_%=\n\t" \
    "v_mov_b32 v140, 5836800\n\t" \
    "s_mov_b64 exec, 1\n\t" \
    "global_atomic_add v140, v142, s[36:37] sc1\n\t" \
    "s_mov_b64 exec, -1\n\t" \
    "vv_Ca6_%=:\n\t" \
    "v_mov_b32_dpp v140, v143 quad_perm:[1,0,3,2] row_mask:0xf bank_mask:0xf\n\t" \
    "v_cmp_lt_u32 s[44:45], v140, v143\n\t" \
    "s_nop 1\n\t" \
    "s_bitcmp1_b32 s44, 0\n\t" \
    "s_cselect_b32 s62, 8192, 0\n\t" \
    "s_bitcmp1_b32 s44, 2\n\t" \
    "s_cselect_b32 s63, 8192, 0\n\t" \
    "s_or_b32 s63, s63, 0x200000\n\t" \
    "v_min_u32 v140, v140, v143\n\t" \
    "v_cmp_lt_u32 vcc, 660, v140\n\t" \
    "s_nop 1\n\t" \
    "s_bcnt1_i32_b64 s69, vcc\n\t" \
    "s_cmp_gt_u32 s69, 16\n\t" \
    "s_cselect_b32 s69, 1, 0\n\t" \
    "s_mul_i32 s70, s69, 160\n\t" \
    "s_add_u32 s86, s86, s70\n\t" \
    "s_mul_i32 s70, s69, 536\n\t" \
    "s_add_u32 s87, s87, s70\n\t" \
    "s_mul_i32 s70, s69, 256\n\t" \
    "s_add_u32 s88, s88, s70\n\t" \
    "s_waitcnt vmcnt(0) lgkmcnt(0)\n\t" \
    "s_barrier\n\t" \
    "ds_read_b64_tr_b16 a[160:161], v212 offset:0\n\t" \
    "ds_read_b64_tr_b16 a[162:163], v212 offset:2304\n\t" \
    "ds_read_b64_tr_b16 a[164:165], v212 offset:18432\n\t" \
    "ds_read_b64_tr_b16 a[166:167], v212 offset:20736\n\t" \
    "s_waitcnt lgkmcnt(6)\n\t" \
    "ds_read_b64_tr_b16 a[192:193], v213 offset:0\n\t" \
    "ds_read_b64_tr_b16 a[194:195], v213 offset:2304\n\t" \
    "ds_read_b64_tr_b16 a[196:197], v213 offset:18432\n\t" \
    "ds_read_b64_tr_b16 a[198:199], v213 offset:20736\n\t" \
    "s_waitcnt lgkmcnt(6)\n\t" \
    "ds_read_b64_tr_b16 a[224:225], v212 offset:128\n\t" \
    "ds_read_b64_tr_b16 a[226:227], v212 offset:2432\n\t" \
    "ds_read_b64_tr_b16 a[228:229], v212 offset:18560\n\t" \
    "ds_read_b64_tr_b16 a[230:231], v212 offset:20864\n\t" \
    "s_waitcnt lgkmcnt(6)\n\t" \
    "ds_read_b64_tr_b16 v[224:225], v213 offset:128\n\t" \
    "ds_read_b64_tr_b16 v[226:227], v213 offset:2432\n\t" \
    "ds_read_b64_tr_b16 v[228:229], v213 offset:18560\n\t" \
    "ds_read_b64_tr_b16 v[230:231], v213 offset:20864\n\t" \
    "s_waitcnt lgkmcnt(6)\n\t" \
    "ds_read_b64_tr_b16 a[168:169], v214 offset:0\n\t" \
    "ds_read_b64_tr_b16 a[170:171], v214 offset:2304\n\t" \
    "ds_read_b64_tr_b16 a[172:173], v214 offset:18432\n\t" \
    "ds_read_b64_tr_b16 a[174:175], v214 offset:20736\n\t" \
    "s_waitcnt lgkmcnt(6)\n\t" \
    "ds_read_b64_tr_b16 a[200:201], v215 offset:0\n\t" \
    "ds_read_b64_tr_b16 a[202:203], v215 offset:2304\n\t" \
    "ds_read_b64_tr_b16 a[204:205], v215 offset:18432\n\t" \
    "ds_read_b64_tr_b16 a[206:207], v215 offset:20736\n\t" \
    "s_waitcnt lgkmcnt(6)\n\t" \
    "ds_read_b64_tr_b16 a[232:233], v214 offset:128\n\t" \
    "ds_read_b64_tr_b16 a[234:235], v214 offset:2432\n\t" \
    "ds_read_b64_tr_b16 a[236:237], v214 offset:18560\n\t" \
    "ds_read_b64_tr_b16 a[238:239], v214 offset:20864\n\t" \
    "s_waitcnt lgkmcnt(6)\n\t" \
    "ds_read_b64_tr_b16 v[232:233], v215 offset:128\n\t" \
    "ds_read_b64_tr_b16 v[234:235], v215 offset:2432\n\t" \
    "ds_read_b64_tr_b16 v[236:237], v215 offset:18560\n\t" \
    "ds_read_b64_tr_b16 v[238:239], v215 offset:20864\n\t" \
    "s_waitcnt lgkmcnt(6)\n\t" \
    "ds_read_b64_tr_b16 v[248:249], v24 offset:0\n\t" \
    "ds_read_b64_tr_b16 v[250:251], v24 offset:2304\n\t" \
    "ds_read_b64_tr_b16 v[252:253], v24 offset:18432\n\t" \
    "ds_read_b64_tr_b16 v[254:255], v24 offset:20736\n\t" \
    "v_add_u32 v212, 0x12000, v212\n\t" \
    "v_add_u32 v213, 0x12000, v213\n\t" \
    "v_add_u32 v214, 0x12000, v214\n\t" \
    "v_add_u32 v215, 0x12000, v215\n\t" \
    "v_add_u32 v24, 0x12000, v24\n\t" \
    "s_waitcnt lgkmcnt(0)\n\t" \
    "s_barrier\n\t" \
    "s_mov_b32 s79, s74\n\t" \
    "s_sub_u32 s80, s74, 32000\n\t" \
    "v_mov_b32 v72, 0\n\t" \
    "v_mov_b32 v73, 0\n\t" \
    "v_mov_b32 v74, 0\n\t" \
    "v_mov_b32 v75, 0\n\t" \
    "v_mov_b32 v76, 0\n\t" \
    "v_mov_b32 v77, 0\n\t" \
    "v_mov_b32 v78, 0\n\t" \
    "v_mov_b32 v79, 0\n\t" \
    "v_mov_b32 v108, 0\n\t" \
    "v_mov_b32 v109, 0\n\t" \
    "v_mov_b32 v110, 0\n\t" \
    "v_mov_b32 v111, 0\n\t" \
    "v_mov_b32 v112, 0\n\t" \
    "v_mov_b32 v113, 0\n\t" \
    "v_mov_b32 v114, 0\n\t" \
    "v_mov_b32 v115, 0\n\t" \
    "v_mov_b32 v116, 0\n\t" \
    "v_mov_b32 v117, 0\n\t" \
    "v_mov_b32 v118, 0\n\t" \
    "v_mov_b32 v119, 0\n\t" \
    "v_mov_b32 v120, 0\n\t" \
    "v_mov_b32 v121, 0\n\t" \
    "v_mov_b32 v122, 0\n\t" \
    "v_mov_b32 v123, 0\n\t" \
    "v_mov_b32 v124, 0\n\t" \
    "v_mov_b32 v125, 0\n\t" \
    "v_mov_b32 v126, 0\n\t" \
    "v_mov_b32 v127, 0\n\t" \
    "s_add_u32 s69, s65, 0\n\t" \
    "s_and_b32 s69, s69, 7\n\t" \
    "s_lshl_b32 s69, s69, 18\n\t" \
    "s_add_u32 s69, s69, s62\n\t" \
    "s_add_u32 s46, s36, s69\n\t" \
    "s_addc_u32 s47, s37, 0\n\t" \
    "s_add_u32 s70, s65, 0\n\t" \
    "s_mul_i32 s69, s70, 171\n\t" \
    "s_lshr_b32 s69, s69, 9\n\t" \
    "s_mul_i32 s70, s70, 589824\n\t" \
    "s_mul_i32 s71, s69, 327680\n\t" \
    "s_add_u32 s70, s70, s71\n\t" \
    "s_lshl1_add_u32 s69, s69, 4\n\t" \
    "s_bitcmp1_b64 s[44:45], s69\n\t" \
    "s_cselect_b32 s69, 8192, 0\n\t" \
    "s_add_u32 s69, s69, s70\n\t" \
    "s_add_u32 s69, s69, 123731968\n\t" \
    "s_add_u32 s38, s36, s69\n\t" \
    "s_addc_u32 s39, s37, 0\n\t" \
    "s_and_b32 s69, s69, 0xffe00000\n\t" \
    "s_add_u32 s69, s69, 1823744\n\t" \
    "v_mov_b32 v17, s69\n\t" \
    "global_load_dword v17, v17, s[36:37]\n\t" \
    "s_and_b32 s69, s65, 7\n\t" \
    "s_lshl_b32 s69, s69, 18\n\t" \
    "s_add_u32 s69, s69, s63\n\t" \
    "s_add_u32 s52, s36, s69\n\t" \
    "s_addc_u32 s53, s37, 0\n\t" \
    "v_mov_b32 v100, 0\n\t" \
    "v_mov_b32 v101, 0\n\t" \
    "v_mov_b32 v102, 0\n\t" \
    "v_mov_b32 v103, 0\n\t" \
    "s_sub_u32 s70, s79, s74\n\t" \
    "s_max_i32 s70, s70, 0\n\t" \
    "s_min_i32 s70, s70, 16000\n\t" \
    "s_lshl_b32 s70, s70, 1\n\t" \
    "s_mul_hi_u32 s70, s70, s77\n\t" \
    "s_add_u32 s70, s70, s73\n\t" \
    "s_cmp_eq_u32 s66, 1\n\t" \
    "s_cbranch_scc1 vv_loop10_%=\n\t" \
    "s_cmp_eq_u32 s66, 2\n\t" \
    "s_cbranch_scc1 vv_loop11_%=\n\t" \
    "s_cmp_eq_u32 s66, 3\n\t" \
    "s_cbranch_scc1 vv_loop12_%=\n\t" \
    ".p2align 6\n\t" \
    "s_nop 0\n\t" \
    "s_nop 0\n\t" \
    "s_nop 0\n\t" \
    "s_nop 0\n\t" \
    "s_nop 0\n\t" \
    "s_nop 0\n\t" \
    "vv_loop9_%=:\n\t" \
    "s_getpc_b64 s[30:31]\n\t" \
    "vv_Wp15_%=:\n\t" \
    "s_add_u32 s30, s30, vv_We16_%=-vv_Wp15_%=\n\t" \
    "s_addc_u32 s31, s31, 0\n\t" \
    "s_sub_u32 s70, s70, 96\n\t" \
    "vv_W14_%=:\n\t" \
    "s_memtime s[56:57]\n\t" \
    "s_waitcnt lgkmcnt(0)\n\t" \
    "s_sub_u32 s69, s70, s56\n\t" \
    "s_cmp_gt_i32 s69, 80\n\t" \
    "s_cbranch_scc1 vv_W14_%=\n\t" \
    "s_max_i32 s69, s69, 0\n\t" \
    "s_and_b32 s69, s69, -4\n\t" \
    "s_sub_u32 s30, s30, s69\n\t" \
    "s_subb_u32 s31, s31, 0\n\t" \
    "s_setpc_b64 s[30:31]\n\t" \
    "s_nop 0\n\t" \
    "s_nop 0\n\t" \
    "s_nop 0\n\t" \
    "s_nop 0\n\t" \
    "s_nop 0\n\t" \
    "s_nop 0\n\t" \
    "s_nop 0\n\t" \
    "s_nop 0\n\t" \
    "s_nop 0\n\t" \
    "s_nop 0\n\t" \
    "s_nop 0\n\t" \
    "s_nop 0\n\t" \
    "s_nop 0\n\t" \
    "s_nop 0\n\t" \
    "s_nop 0\n\t" \
    "s_nop 0\n\t" \
    "s_nop 0\n\t" \
    "s_nop 0\n\t" \
    "s_nop 0\n\t" \
    "s_nop 0\n\t" \
    "vv_We16_%=:\n\t" \
    "vv_G17_%=:\n\t" \
    "global_load_dwordx4 v[36:39], v1, s[46:47] sc1\n\t" \
    "global_load_dword v141, v2, s[46:47] sc1\n\t" \
    "global_load_dwordx4 v[40:43], v23, s[46:47] sc1\n\t" \
    "s_waitcnt vmcnt(2)\n\t" \
    "ds_write_b128 v3, v[36:39]\n\t" \
    "v_smfmac_f32_16x16x64_bf16 v[72:75], v[36:39], a[0:7], v10\n\t" \
    "s_waitcnt vmcnt(1)\n\t" \
    "ds_write_b32 v22, v141 offset:19456\n\t" \
    "s_waitcnt vmcnt(0)\n\t" \
    "ds_write_b128 v3, v[40:43] offset:1024\n\t" \
    "v_max3_u32 v140, v36, v40, v141\n\t" \
    "v_cmp_eq_u32 vcc, -1, v140\n\t" \
    "s_cbranch_vccnz vv_Gbad18_%=\n\t" \
    "vv_Gc19_%=:\n\t" \
    "s_waitcnt lgkmcnt(0)\n\t" \
    "s_barrier\n\t" \
    "ds_read_b128 v[48:51], v4\n\t" \
    "ds_read_b128 v[52:55], v5\n\t" \
    "v_smfmac_f32_16x16x64_bf16 v[76:79], v[36:39], a[72:79], v10\n\t" \
    "ds_read_b128 v[56:59], v6\n\t" \
    "ds_read_b128 v[60:63], v7\n\t" \
    "v_smfmac_f32_16x16x64_bf16 v[72:75], v[40:43], a[8:15], v10\n\t" \
    "ds_read_b128 v[64:67], v8\n\t" \
    "v_smfmac_f32_16x16x64_bf16 v[76:79], v[40:43], a[80:87], v10\n\t" \
    "ds_read_b128 v[68:71], v9\n\t" \
    "s_waitcnt lgkmcnt(5)\n\t" \
    "v_smfmac_f32_16x16x64_bf16 v[72:75], v[48:51], a[16:23], v10\n\t" \
    "ds_read_b128 v[44:47], v0 offset:19456\n\t" \
    "s_waitcnt lgkmcnt(5)\n\t" \
    "v_smfmac_f32_16x16x64_bf16 v[72:75], v[52:55], a[24:31], v10\n\t" \
    "s_waitcnt lgkmcnt(4)\n\t" \
    "v_smfmac_f32_16x16x64_bf16 v[72:75], v[56:59], a[32:39], v10\n\t" \
    "s_waitcnt lgkmcnt(3)\n\t" \
    "v_smfmac_f32_16x16x64_bf16 v[72:75], v[60:63], a[40:47], v10\n\t" \
    "s_waitcnt lgkmcnt(2)\n\t" \
    "v_smfmac_f32_16x16x64_bf16 v[72:75], v[64:67], a[48:55], v10\n\t" \
    "s_waitcnt lgkmcnt(1)\n\t" \
    "v_smfmac_f32_16x16x64_bf16 v[72:75], v[68:71], a[56:63], v10\n\t" \
    "s_waitcnt lgkmcnt(0)\n\t" \
    "v_smfmac_f32_16x16x64_bf16 v[72:75], v[44:47], a[64:71], v10\n\t" \
    "s_nop 2\n\t" \
    "v_smfmac_f32_16x16x64_bf16 v[76:79], v[48:51], a[88:95], v10\n\t" \
    "s_nop 2\n\t" \
    "v_smfmac_f32_16x16x64_bf16 v[76:79], v[52:55], a[96:103], v10\n\t" \
    "v_add_f32 v72, v72, v74\n\t" \
    "v_smfmac_f32_16x16x64_bf16 v[76:79], v[56:59], a[104:111], v10\n\t" \
    "v_add_f32 v73, v73, v75\n\t" \
    "v_smfmac_f32_16x16x64_bf16 v[76:79], v[60:63], a[112:119], v10\n\t" \
    "v_cvt_pk_bf16_f32 v80, v72, v73\n\t" \
    "v_smfmac_f32_16x16x64_bf16 v[76:79], v[64:67], a[120:127], v10\n\t" \
    "ds_write_b32 v12, v80 offset:0\n\t" \
    "v_smfmac_f32_16x16x64_bf16 v[76:79], v[68:71], a[128:135], v10\n\t" \
    "v_smfmac_f32_16x16x64_bf16 v[76:79], v[44:47], a[136:143], v10\n\t" \
    "s_nop 6\n\t" \
    "v_cmp_u_f32 vcc, v72, v73\n\t" \
    "v_pk_add_f32 v[76:77], v[76:77], v[78:79]\n\t" \
    "v_cvt_pk_bf16_f32 v81, v76, v77\n\t" \
    "ds_write_b32 v12, v81 offset:256\n\t" \
    "s_cbranch_vccnz vv_Gslow20_%=\n\t" \
    "vv_Gback21_%=:\n\t" \
    "s_waitcnt lgkmcnt(0)\n\t" \
    "s_barrier\n\t" \
    "ds_read_b64_tr_b16 v[84:85], v13 offset:0\n\t" \
    "v_smfmac_f32_16x16x64_bf16 v[100:103], v[36:39], a[0:7], v10\n\t" \
    "ds_read_b64_tr_b16 v[86:87], v13 offset:64\n\t" \
    "ds_read_b64_tr_b16 v[92:93], v13 offset:1152\n\t" \
    "v_smfmac_f32_16x16x64_bf16 v[100:103], v[36:39], a[0:7], v10\n\t" \
    "ds_read_b64_tr_b16 v[94:95], v13 offset:1216\n\t" \
    "ds_read_b64_tr_b16 v[88:89], v25 offset:0\n\t" \
    "v_smfmac_f32_16x16x64_bf16 v[100:103], v[36:39], a[0:7], v10\n\t" \
    "ds_read_b64_tr_b16 v[90:91], v25 offset:64\n\t" \
    "s_waitcnt lgkmcnt(4)\n\t" \
    "v_smfmac_f32_16x16x64_bf16 v[124:127], v[84:87], v[240:247], v19\n\t" \
    "ds_read_b64_tr_b16 v[96:97], v25 offset:1152\n\t" \
    "ds_read_b64_tr_b16 v[98:99], v25 offset:1216\n\t" \
    "s_waitcnt lgkmcnt(4)\n\t" \
    "v_smfmac_f32_16x16x64_bf16 v[124:127], v[92:95], v[248:255], v19\n\t" \
    "s_nop 2\n\t" \
    "v_smfmac_f32_16x16x64_bf16 v[108:111], v[84:87], a[144:151], v19\n\t" \
    "s_nop 2\n\t" \
    "v_smfmac_f32_16x16x64_bf16 v[108:111], v[92:95], a[160:167], v19\n\t" \
    "s_nop 1\n\t" \
    "v_permlane32_swap_b32 v124, v126\n\t" \
    "v_smfmac_f32_16x16x64_bf16 v[112:115], v[84:87], a[176:183], v19\n\t" \
    "s_nop 1\n\t" \
    "v_permlane32_swap_b32 v125, v127\n\t" \
    "v_smfmac_f32_16x16x64_bf16 v[112:115], v[92:95], a[192:199], v19\n\t" \
    "s_nop 0\n\t" \
    "v_add_f32 v124, v124, v126\n\t" \
    "s_waitcnt lgkmcnt(2)\n\t" \
    "v_smfmac_f32_16x16x64_bf16 v[108:111], v[88:91], a[152:159], v19\n\t" \
    "s_nop 1\n\t" \
    "v_add_f32 v125, v125, v127\n\t" \
    "v_smfmac_f32_16x16x64_bf16 v[112:115], v[88:91], a[184:191], v19\n\t" \
    "s_nop 0\n\t" \
    "v_cvt_pk_bf16_f32 v136, v124, v125\n\t" \
    "s_waitcnt lgkmcnt(0)\n\t" \
    "v_smfmac_f32_16x16x64_bf16 v[108:111], v[96:99], a[168:175], v19\n\t" \
    "s_nop 1\n\t" \
    "v_cvt_pk_bf16_f32 v137, v124, v125\n\t" \
    "v_smfmac_f32_16x16x64_bf16 v[112:115], v[96:99], a[200:207], v19\n\t" \
    "s_nop 1\n\t" \
    "v_permlane32_swap_b32 v136, v137\n\t" \
    "v_smfmac_f32_16x16x64_bf16 v[116:119], v[84:87], a[208:215], v19\n\t" \
    "s_mov_b32 exec_hi, 0\n\t" \
    "global_store_dwordx2 v16, v[136:137], s[38:39] sc1\n\t" \
    "s_mov_b32 exec_hi, -1\n\t" \
    "v_cvt_pk_bf16_f32 v128, v108, v109\n\t" \
    "v_smfmac_f32_16x16x64_bf16 v[116:119], v[88:91], a[216:223], v19\n\t" \
    "s_nop 1\n\t" \
    "v_cvt_pk_bf16_f32 v129, v110, v111\n\t" \
    "v_smfmac_f32_16x16x64_bf16 v[116:119], v[92:95], a[224:231], v19\n\t" \
    "s_nop 1\n\t" \
    "v_cvt_pk_bf16_f32 v130, v112, v113\n\t" \
    "v_smfmac_f32_16x16x64_bf16 v[116:119], v[96:99], a[232:239], v19\n\t" \
    "s_nop 1\n\t" \
    "v_cvt_pk_bf16_f32 v131, v114, v115\n\t" \
    "v_smfmac_f32_16x16x64_bf16 v[120:123], v[84:87], a[240:247], v19\n\t" \
    "s_nop 1\n\t" \
    "v_permlane16_swap_b32 v128, v130\n\t" \
    "v_smfmac_f32_16x16x64_bf16 v[120:123], v[88:91], a[248:255], v19\n\t" \
    "s_nop 1\n\t" \
    "v_permlane16_swap_b32 v129, v131\n\t" \
    "v_smfmac_f32_16x16x64_bf16 v[120:123], v[92:95], v[224:231], v19\n\t" \
    "s_nop 0\n\t" \
    "global_store_dwordx4 v14, v[128:131], s[38:39] sc1\n\t" \
    "v_cvt_pk_bf16_f32 v132, v116, v117\n\t" \
    "v_smfmac_f32_16x16x64_bf16 v[120:123], v[96:99], v[232:239], v19\n\t" \
    "s_nop 1\n\t" \
    "v_cvt_pk_bf16_f32 v133, v118, v119\n\t" \
    "s_nop 4\n\t" \
    "v_cvt_pk_bf16_f32 v134, v120, v121\n\t" \
    "v_cvt_pk_bf16_f32 v135, v122, v123\n\t" \
    "s_nop 0\n\t" \
    "v_permlane16_swap_b32 v132, v134\n\t" \
    "v_permlane16_swap_b32 v133, v135\n\t" \
    "global_store_dwordx4 v15, v[132:135], s[38:39] sc1\n\t" \
    "s_cmp_eq_u32 s65, 63\n\t" \
    "s_cbranch_scc1 vv_end13_%=\n\t" \
    "s_cmp_lg_u32 s66, 0\n\t" \
    "s_cbranch_scc1 vv_SF22_%=\n\t" \
    "s_lshl_b32 s69, s67, 7\n\t" \
    "s_add_u32 s69, s69, 5804032\n\t" \
    "s_add_u32 s70, s65, -1\n\t" \
    "v_mov_b32 v142, s69\n\t" \
    "v_mov_b32 v143, s70\n\t" \
    "s_mov_b64 exec, 1\n\t" \
    "global_store_dword v142, v143, s[36:37] sc1\n\t" \
    "s_mov_b64 exec, -1\n\t" \
    "vv_SF22_%=:\n\t" \
    "s_add_u32 s65, s65, 1\n\t" \
    "v_mov_b32 v72, 0\n\t" \
    "v_mov_b32 v73, 0\n\t" \
    "v_mov_b32 v74, 0\n\t" \
    "v_mov_b32 v75, 0\n\t" \
    "v_mov_b32 v76, 0\n\t" \
    "v_mov_b32 v77, 0\n\t" \
    "v_mov_b32 v78, 0\n\t" \
    "v_mov_b32 v79, 0\n\t" \
    "v_mov_b32 v108, 0\n\t" \
    "v_mov_b32 v109, 0\n\t" \
    "v_mov_b32 v110, 0\n\t" \
    "v_mov_b32 v111, 0\n\t" \
    "v_mov_b32 v112, 0\n\t" \
    "v_mov_b32 v113, 0\n\t" \
    "v_mov_b32 v114, 0\n\t" \
    "v_mov_b32 v115, 0\n\t" \
    "v_mov_b32 v116, 0\n\t" \
    "v_mov_b32 v117, 0\n\t" \
    "v_mov_b32 v118, 0\n\t" \
    "v_mov_b32 v119, 0\n\t" \
    "v_mov_b32 v120, 0\n\t" \
    "v_mov_b32 v121, 0\n\t" \
    "v_mov_b32 v122, 0\n\t" \
    "v_mov_b32 v123, 0\n\t" \
    "v_mov_b32 v124, 0\n\t" \
    "v_mov_b32 v125, 0\n\t" \
    "v_mov_b32 v126, 0\n\t" \
    "v_mov_b32 v127, 0\n\t" \
    "s_add_u32 s69, s65, 0\n\t" \
    "s_and_b32 s69, s69, 7\n\t" \
    "s_lshl_b32 s69, s69, 18\n\t" \
    "s_add_u32 s69, s69, s62\n\t" \
    "s_add_u32 s46, s36, s69\n\t" \
    "s_addc_u32 s47, s37, 0\n\t" \
    "s_add_u32 s70, s65, 0\n\t" \
    "s_mul_i32 s69, s70, 171\n\t" \
    "s_lshr_b32 s69, s69, 9\n\t" \
    "s_mul_i32 s70, s70, 589824\n\t" \
    "s_mul_i32 s71, s69, 327680\n\t" \
    "s_add_u32 s70, s70, s71\n\t" \
    "s_lshl1_add_u32 s69, s69, 4\n\t" \
    "s_bitcmp1_b64 s[44:45], s69\n\t" \
    "s_cselect_b32 s69, 8192, 0\n\t" \
    "s_add_u32 s69, s69, s70\n\t" \
    "s_add_u32 s69, s69, 123731968\n\t" \
    "s_add_u32 s38, s36, s69\n\t" \
    "s_addc_u32 s39, s37, 0\n\t" \
    "s_and_b32 s69, s69, 0xffe00000\n\t" \
    "s_add_u32 s69, s69, 1823744\n\t" \
    "v_mov_b32 v17, s69\n\t" \
    "global_load_dword v17, v17, s[36:37]\n\t" \
    "s_barrier\n\t" \
    "ds_read_b32 v160, v11 offset:20480\n\t" \
    "s_waitcnt lgkmcnt(0)\n\t" \
    "v_readfirstlane_b32 s70, v160\n\t" \
    "s_branch vv_loop9_%=\n\t" \
    "vv_Gbad18_%=:\n\t" \
    "s_waitcnt lgkmcnt(0)\n\t" \
    "s_nop 7\n\t" \
    "v_mov_b32 v72, 0\n\t" \
    "v_mov_b32 v73, 0\n\t" \
    "v_mov_b32 v74, 0\n\t" \
    "v_mov_b32 v75, 0\n\t" \
    "v_mov_b32 v76, 0\n\t" \
    "v_mov_b32 v77, 0\n\t" \
    "v_mov_b32 v78, 0\n\t" \
    "v_mov_b32 v79, 0\n\t" \
    "s_cmp_eq_u32 s68, 0\n\t" \
    "s_cbranch_scc1 vv_F23_%=\n\t" \
    "s_sub_u32 s68, s68, 1\n\t" \
    "s_branch vv_G17_%=\n\t" \
    "vv_F23_%=:\n\t" \
    "v_mov_b32 v140, 5767424\n\t" \
    "v_mov_b32 v141, 1\n\t" \
    "global_store_dword v140, v141, s[36:37] sc1\n\t" \
    "s_waitcnt vmcnt(0)\n\t" \
    "s_branch vv_Gc19_%=\n\t" \
    "vv_Gslow20_%=:\n\t" \
    "v_max_u32 v140, v36, v37\n\t" \
    "v_max3_u32 v140, v140, v38, v39\n\t" \
    "v_max3_u32 v140, v140, v40, v41\n\t" \
    "v_max3_u32 v140, v140, v42, v43\n\t" \
    "v_max3_u32 v140, v140, v44, v45\n\t" \
    "v_max3_u32 v140, v140, v46, v47\n\t" \
    "v_max3_u32 v140, v140, v48, v49\n\t" \
    "v_max3_u32 v140, v140, v50, v51\n\t" \
    "v_max3_u32 v140, v140, v52, v53\n\t" \
    "v_max3_u32 v140, v140, v54, v55\n\t" \
    "v_max3_u32 v140, v140, v56, v57\n\t" \
    "v_max3_u32 v140, v140, v58, v59\n\t" \
    "v_max3_u32 v140, v140, v60, v61\n\t" \
    "v_max3_u32 v140, v140, v62, v63\n\t" \
    "v_max3_u32 v140, v140, v64, v65\n\t" \
    "v_max3_u32 v140, v140, v66, v67\n\t" \
    "v_max3_u32 v140, v140, v68, v69\n\t" \
    "v_max3_u32 v140, v140, v70, v71\n\t" \
    "v_cmp_eq_u32 vcc, -1, v140\n\t" \
    "s_cbranch_vccz vv_Gback21_%=\n\t" \
    "s_waitcnt lgkmcnt(0)\n\t" \
    "v_sub_u32 v140, v1, v3\n\t" \
    "s_nop 0\n\t" \
    "v_readfirstlane_b32 s69, v140\n\t" \
    "v_add_u32 v143, 20480, v0\n\t" \
    "s_nop 7\n\t" \
    "s_add_u32 s30, s46, s69\n\t" \
    "s_addc_u32 s31, s47, 0\n\t" \
    "vv_Gag24_%=:\n\t" \
    "global_load_dwordx4 v[36:39], v1, s[46:47] sc1\n\t" \
    "global_load_dwordx4 v[40:43], v23, s[46:47] sc1\n\t" \
    "global_load_dwordx4 v[44:47], v143, s[30:31] sc1\n\t" \
    "global_load_dwordx4 v[48:51], v4, s[30:31] sc1\n\t" \
    "global_load_dwordx4 v[52:55], v5, s[30:31] sc1\n\t" \
    "global_load_dwordx4 v[56:59], v6, s[30:31] sc1\n\t" \
    "global_load_dwordx4 v[60:63], v7, s[30:31] sc1\n\t" \
    "global_load_dwordx4 v[64:67], v8, s[30:31] sc1\n\t" \
    "global_load_dwordx4 v[68:71], v9, s[30:31] sc1\n\t" \
    "s_waitcnt vmcnt(0)\n\t" \
    "v_max_u32 v140, v36, v37\n\t" \
    "v_max3_u32 v140, v140, v38, v39\n\t" \
    "v_max3_u32 v140, v140, v40, v41\n\t" \
    "v_max3_u32 v140, v140, v42, v43\n\t" \
    "v_max3_u32 v140, v140, v44, v45\n\t" \
    "v_max3_u32 v140, v140, v46, v47\n\t" \
    "v_max3_u32 v140, v140, v48, v49\n\t" \
    "v_max3_u32 v140, v140, v50, v51\n\t" \
    "v_max3_u32 v140, v140, v52, v53\n\t" \
    "v_max3_u32 v140, v140, v54, v55\n\t" \
    "v_max3_u32 v140, v140, v56, v57\n\t" \
    "v_max3_u32 v140, v140, v58, v59\n\t" \
    "v_max3_u32 v140, v140, v60, v61\n\t" \
    "v_max3_u32 v140, v140, v62, v63\n\t" \
    "v_max3_u32 v140, v140, v64, v65\n\t" \
    "v_max3_u32 v140, v140, v66, v67\n\t" \
    "v_max3_u32 v140, v140, v68, v69\n\t" \
    "v_max3_u32 v140, v140, v70, v71\n\t" \
    "v_cmp_eq_u32 vcc, -1, v140\n\t" \
    "s_cbranch_vccz vv_Gredo25_%=\n\t" \
    "s_cmp_eq_u32 s68, 0\n\t" \
    "s_cbranch_scc1 vv_F26_%=\n\t" \
    "s_sub_u32 s68, s68, 1\n\t" \
    "s_sleep 1\n\t" \
    "s_branch vv_Gag24_%=\n\t" \
    "vv_F26_%=:\n\t" \
    "v_mov_b32 v140, 5767424\n\t" \
    "v_mov_b32 v141, 2\n\t" \
    "global_store_dword v140, v141, s[36:37] sc1\n\t" \
    "s_waitcnt vmcnt(0)\n\t" \
    "s_branch vv_Gredo25_%=\n\t" \
    "vv_Gredo25_%=:\n\t" \
    "v_mov_b32 v72, 0\n\t" \
    "v_mov_b32 v73, 0\n\t" \
    "v_mov_b32 v74, 0\n\t" \
    "v_mov_b32 v75, 0\n\t" \
    "v_mov_b32 v76, 0\n\t" \
    "v_mov_b32 v77, 0\n\t" \
    "v_mov_b32 v78, 0\n\t" \
    "v_mov_b32 v79, 0\n\t" \
    "v_smfmac_f32_16x16x64_bf16 v[72:75], v[36:39], a[0:7], v10\n\t" \
    "v_smfmac_f32_16x16x64_bf16 v[72:75], v[40:43], a[8:15], v10\n\t" \
    "v_smfmac_f32_16x16x64_bf16 v[72:75], v[48:51], a[16:23], v10\n\t" \
    "v_smfmac_f32_16x16x64_bf16 v[72:75], v[52:55], a[24:31], v10\n\t" \
    "v_smfmac_f32_16x16x64_bf16 v[72:75], v[56:59], a[32:39], v10\n\t" \
    "v_smfmac_f32_16x16x64_bf16 v[72:75], v[60:63], a[40:47], v10\n\t" \
    "v_smfmac_f32_16x16x64_bf16 v[72:75], v[64:67], a[48:55], v10\n\t" \
    "v_smfmac_f32_16x16x64_bf16 v[72:75], v[68:71], a[56:63], v10\n\t" \
    "v_smfmac_f32_16x16x64_bf16 v[72:75], v[44:47], a[64:71], v10\n\t" \
    "v_smfmac_f32_16x16x64_bf16 v[76:79], v[36:39], a[72:79], v10\n\t" \
    "v_smfmac_f32_16x16x64_bf16 v[76:79], v[40:43], a[80:87], v10\n\t" \
    "v_smfmac_f32_16x16x64_bf16 v[76:79], v[48:51], a[88:95], v10\n\t" \
    "v_smfmac_f32_16x16x64_bf16 v[76:79], v[52:55], a[96:103], v10\n\t" \
    "v_smfmac_f32_16x16x64_bf16 v[76:79], v[56:59], a[104:111], v10\n\t" \
    "v_smfmac_f32_16x16x64_bf16 v[76:79], v[60:63], a[112:119], v10\n\t" \
    "v_smfmac_f32_16x16x64_bf16 v[76:79], v[64:67], a[120:127], v10\n\t" \
    "v_smfmac_f32_16x16x64_bf16 v[76:79], v[68:71], a[128:135], v10\n\t" \
    "v_smfmac_f32_16x16x64_bf16 v[76:79], v[44:47], a[136:143], v10\n\t" \
    "v_add_f32 v72, v72, v74\n\t" \
    "v_add_f32 v73, v73, v75\n\t" \
    "v_cvt_pk_bf16_f32 v80, v72, v73\n\t" \
    "ds_write_b32 v12, v80 offset:0\n\t" \
    "s_nop 3\n\t" \
    "v_pk_add_f32 v[76:77], v[76:77], v[78:79]\n\t" \
    "v_cvt_pk_bf16_f32 v81, v76, v77\n\t" \
    "ds_write_b32 v12, v81 offset:256\n\t" \
    "s_branch vv_Gback21_%=\n\t" \
    "vv_end13_%=:\n\t" \
    "s_endpgm\n\t" \
    ".p2align 6\n\t" \
    "s_nop 0\n\t" \
    "s_nop 0\n\t" \
    "s_nop 0\n\t" \
    "s_nop 0\n\t" \
    "s_nop 0\n\t" \
    "s_nop 0\n\t" \
    "vv_loop10_%=:\n\t" \
    "s_getpc_b64 s[30:31]\n\t" \
    "vv_Wp29_%=:\n\t" \
    "s_add_u32 s30, s30, vv_We30_%=-vv_Wp29_%=\n\t" \
    "s_addc_u32 s31, s31, 0\n\t" \
    "s_sub_u32 s70, s70, 96\n\t" \
    "vv_W28_%=:\n\t" \
    "s_memtime s[56:57]\n\t" \
    "s_waitcnt lgkmcnt(0)\n\t" \
    "s_sub_u32 s69, s70, s56\n\t" \
    "s_cmp_gt_i32 s69, 80\n\t" \
    "s_cbranch_scc1 vv_W28_%=\n\t" \
    "s_max_i32 s69, s69, 0\n\t" \
    "s_and_b32 s69, s69, -4\n\t" \
    "s_sub_u32 s30, s30, s69\n\t" \
    "s_subb_u32 s31, s31, 0\n\t" \
    "s_setpc_b64 s[30:31]\n\t" \
    "s_nop 0\n\t" \
    "s_nop 0\n\t" \
    "s_nop 0\n\t" \
    "s_nop 0\n\t" \
    "s_nop 0\n\t" \
    "s_nop 0\n\t" \
    "s_nop 0\n\t" \
    "s_nop 0\n\t" \
    "s_nop 0\n\t" \
    "s_nop 0\n\t" \
    "s_nop 0\n\t" \
    "s_nop 0\n\t" \
    "s_nop 0\n\t" \
    "s_nop 0\n\t" \
    "s_nop 0\n\t" \
    "s_nop 0\n\t" \
    "s_nop 0\n\t" \
    "s_nop 0\n\t" \
    "s_nop 0\n\t" \
    "s_nop 0\n\t" \
    "vv_We30_%=:\n\t" \
    "vv_G31_%=:\n\t" \
    "global_load_dwordx4 v[36:39], v1, s[46:47] sc1\n\t" \
    "global_load_dword v141, v2, s[46:47] sc1\n\t" \
    "global_load_dwordx4 v[40:43], v23, s[46:47] sc1\n\t" \
    "s_waitcnt vmcnt(2)\n\t" \
    "ds_write_b128 v3, v[36:39]\n\t" \
    "v_smfmac_f32_16x16x64_bf16 v[72:75], v[36:39], a[16:23], v10\n\t" \
    "s_waitcnt vmcnt(1)\n\t" \
    "ds_write_b32 v22, v141 offset:19712\n\t" \
    "s_waitcnt vmcnt(0)\n\t" \
    "ds_write_b128 v3, v[40:43] offset:1024\n\t" \
    "v_max3_u32 v140, v36, v40, v141\n\t" \
    "v_cmp_eq_u32 vcc, -1, v140\n\t" \
    "s_cbranch_vccnz vv_Gbad32_%=\n\t" \
    "vv_Gc33_%=:\n\t" \
    "s_waitcnt lgkmcnt(0)\n\t" \
    "s_barrier\n\t" \
    "ds_read_b128 v[48:51], v4\n\t" \
    "ds_read_b128 v[52:55], v5\n\t" \
    "v_smfmac_f32_16x16x64_bf16 v[76:79], v[36:39], a[88:95], v10\n\t" \
    "ds_read_b128 v[56:59], v6\n\t" \
    "ds_read_b128 v[60:63], v7\n\t" \
    "v_smfmac_f32_16x16x64_bf16 v[72:75], v[40:43], a[24:31], v10\n\t" \
    "ds_read_b128 v[64:67], v8\n\t" \
    "v_smfmac_f32_16x16x64_bf16 v[76:79], v[40:43], a[96:103], v10\n\t" \
    "ds_read_b128 v[68:71], v9\n\t" \
    "s_waitcnt lgkmcnt(5)\n\t" \
    "v_smfmac_f32_16x16x64_bf16 v[72:75], v[48:51], a[32:39], v10\n\t" \
    "ds_read_b128 v[44:47], v0 offset:19456\n\t" \
    "s_waitcnt lgkmcnt(5)\n\t" \
    "v_smfmac_f32_16x16x64_bf16 v[72:75], v[52:55], a[40:47], v10\n\t" \
    "s_waitcnt lgkmcnt(4)\n\t" \
    "v_smfmac_f32_16x16x64_bf16 v[72:75], v[56:59], a[48:55], v10\n\t" \
    "s_waitcnt lgkmcnt(3)\n\t" \
    "v_smfmac_f32_16x16x64_bf16 v[72:75], v[60:63], a[56:63], v10\n\t" \
    "s_waitcnt lgkmcnt(2)\n\t" \
    "v_smfmac_f32_16x16x64_bf16 v[72:75], v[64:67], a[0:7], v10\n\t" \
    "s_waitcnt lgkmcnt(1)\n\t" \
    "v_smfmac_f32_16x16x64_bf16 v[72:75], v[68:71], a[8:15], v10\n\t" \
    "s_waitcnt lgkmcnt(0)\n\t" \
    "v_smfmac_f32_16x16x64_bf16 v[72:75], v[44:47], a[64:71], v10\n\t" \
    "s_nop 2\n\t" \
    "v_smfmac_f32_16x16x64_bf16 v[76:79], v[48:51], a[104:111], v10\n\t" \
    "s_nop 2\n\t" \
    "v_smfmac_f32_16x16x64_bf16 v[76:79], v[52:55], a[112:119], v10\n\t" \
    "v_add_f32 v72, v72, v74\n\t" \
    "v_smfmac_f32_16x16x64_bf16 v[76:79], v[56:59], a[120:127], v10\n\t" \
    "v_add_f32 v73, v73, v75\n\t" \
    "v_smfmac_f32_16x16x64_bf16 v[76:79], v[60:63], a[128:135], v10\n\t" \
    "v_cvt_pk_bf16_f32 v80, v72, v73\n\t" \
    "v_smfmac_f32_16x16x64_bf16 v[76:79], v[64:67], a[72:79], v10\n\t" \
    "ds_write_b32 v12, v80 offset:0\n\t" \
    "v_smfmac_f32_16x16x64_bf16 v[76:79], v[68:71], a[80:87], v10\n\t" \
    "v_smfmac_f32_16x16x64_bf16 v[76:79], v[44:47], a[136:143], v10\n\t" \
    "s_nop 6\n\t" \
    "v_cmp_u_f32 vcc, v72, v73\n\t" \
    "v_pk_add_f32 v[76:77], v[76:77], v[78:79]\n\t" \
    "v_cvt_pk_bf16_f32 v81, v76, v77\n\t" \
    "ds_write_b32 v12, v81 offset:256\n\t" \
    "s_cbranch_vccnz vv_Gslow34_%=\n\t" \
    "vv_Gback35_%=:\n\t" \
    "s_waitcnt lgkmcnt(0)\n\t" \
    "s_barrier\n\t" \
    "ds_read_b64_tr_b16 v[84:85], v13 offset:0\n\t" \
    "v_smfmac_f32_16x16x64_bf16 v[100:103], v[36:39], a[0:7], v10\n\t" \
    "ds_read_b64_tr_b16 v[86:87], v13 offset:64\n\t" \
    "ds_read_b64_tr_b16 v[92:93], v13 offset:1152\n\t" \
    "v_smfmac_f32_16x16x64_bf16 v[100:103], v[36:39], a[0:7], v10\n\t" \
    "ds_read_b64_tr_b16 v[94:95], v13 offset:1216\n\t" \
    "ds_read_b64_tr_b16 v[88:89], v25 offset:0\n\t" \
    "v_smfmac_f32_16x16x64_bf16 v[100:103], v[36:39], a[0:7], v10\n\t" \
    "ds_read_b64_tr_b16 v[90:91], v25 offset:64\n\t" \
    "s_waitcnt lgkmcnt(4)\n\t" \
    "v_smfmac_f32_16x16x64_bf16 v[124:127], v[84:87], v[240:247], v19\n\t" \
    "ds_read_b64_tr_b16 v[96:97], v25 offset:1152\n\t" \
    "ds_read_b64_tr_b16 v[98:99], v25 offset:1216\n\t" \
    "s_waitcnt lgkmcnt(4)\n\t" \
    "v_smfmac_f32_16x16x64_bf16 v[124:127], v[92:95], v[248:255], v19\n\t" \
    "s_nop 2\n\t" \
    "v_smfmac_f32_16x16x64_bf16 v[108:111], v[84:87], a[144:151], v19\n\t" \
    "s_nop 2\n\t" \
    "v_smfmac_f32_16x16x64_bf16 v[108:111], v[92:95], a[160:167], v19\n\t" \
    "s_nop 1\n\t" \
    "v_permlane32_swap_b32 v124, v126\n\t" \
    "v_smfmac_f32_16x16x64_bf16 v[112:115], v[84:87], a[176:183], v19\n\t" \
    "s_nop 1\n\t" \
    "v_permlane32_swap_b32 v125, v127\n\t" \
    "v_smfmac_f32_16x16x64_bf16 v[112:115], v[92:95], a[192:199], v19\n\t" \
    "s_nop 0\n\t" \
    "v_add_f32 v124, v124, v126\n\t" \
    "s_waitcnt lgkmcnt(2)\n\t" \
    "v_smfmac_f32_16x16x64_bf16 v[108:111], v[88:91], a[152:159], v19\n\t" \
    "s_nop 1\n\t" \
    "v_add_f32 v125, v125, v127\n\t" \
    "v_smfmac_f32_16x16x64_bf16 v[112:115], v[88:91], a[184:191], v19\n\t" \
    "s_nop 0\n\t" \
    "v_cvt_pk_bf16_f32 v136, v124, v125\n\t" \
    "s_waitcnt lgkmcnt(0)\n\t" \
    "v_smfmac_f32_16x16x64_bf16 v[108:111], v[96:99], a[168:175], v19\n\t" \
    "s_nop 1\n\t" \
    "v_cvt_pk_bf16_f32 v137, v124, v125\n\t" \
    "v_smfmac_f32_16x16x64_bf16 v[112:115], v[96:99], a[200:207], v19\n\t" \
    "s_nop 1\n\t" \
    "v_permlane32_swap_b32 v136, v137\n\t" \
    "v_smfmac_f32_16x16x64_bf16 v[116:119], v[84:87], a[208:215], v19\n\t" \
    "s_mov_b32 exec_hi, 0\n\t" \
    "global_store_dwordx2 v16, v[136:137], s[38:39] sc1\n\t" \
    "s_mov_b32 exec_hi, -1\n\t" \
    "v_cvt_pk_bf16_f32 v128, v108, v109\n\t" \
    "v_smfmac_f32_16x16x64_bf16 v[116:119], v[88:91], a[216:223], v19\n\t" \
    "s_nop 1\n\t" \
    "v_cvt_pk_bf16_f32 v129, v110, v111\n\t" \
    "v_smfmac_f32_16x16x64_bf16 v[116:119], v[92:95], a[224:231], v19\n\t" \
    "s_nop 1\n\t" \
    "v_cvt_pk_bf16_f32 v130, v112, v113\n\t" \
    "v_smfmac_f32_16x16x64_bf16 v[116:119], v[96:99], a[232:239], v19\n\t" \
    "s_nop 1\n\t" \
    "v_cvt_pk_bf16_f32 v131, v114, v115\n\t" \
    "v_smfmac_f32_16x16x64_bf16 v[120:123], v[84:87], a[240:247], v19\n\t" \
    "s_nop 1\n\t" \
    "v_permlane16_swap_b32 v128, v130\n\t" \
    "v_smfmac_f32_16x16x64_bf16 v[120:123], v[88:91], a[248:255], v19\n\t" \
    "s_nop 1\n\t" \
    "v_permlane16_swap_b32 v129, v131\n\t" \
    "v_smfmac_f32_16x16x64_bf16 v[120:123], v[92:95], v[224:231], v19\n\t" \
    "s_nop 0\n\t" \
    "global_store_dwordx4 v14, v[128:131], s[38:39] sc1\n\t" \
    "v_cvt_pk_bf16_f32 v132, v116, v117\n\t" \
    "v_smfmac_f32_16x16x64_bf16 v[120:123], v[96:99], v[232:239], v19\n\t" \
    "s_nop 1\n\t" \
    "v_cvt_pk_bf16_f32 v133, v118, v119\n\t" \
    "s_nop 4\n\t" \
    "v_cvt_pk_bf16_f32 v134, v120, v121\n\t" \
    "v_cvt_pk_bf16_f32 v135, v122, v123\n\t" \
    "s_nop 0\n\t" \
    "v_permlane16_swap_b32 v132, v134\n\t" \
    "v_permlane16_swap_b32 v133, v135\n\t" \
    "global_store_dwordx4 v15, v[132:135], s[38:39] sc1\n\t" \
    "s_cmp_eq_u32 s65, 63\n\t" \
    "s_cbranch_scc1 vv_end27_%=\n\t" \
    "s_add_u32 s65, s65, 1\n\t" \
    "v_mov_b32 v72, 0\n\t" \
    "v_mov_b32 v73, 0\n\t" \
    "v_mov_b32 v74, 0\n\t" \
    "v_mov_b32 v75, 0\n\t" \
    "v_mov_b32 v76, 0\n\t" \
    "v_mov_b32 v77, 0\n\t" \
    "v_mov_b32 v78, 0\n\t" \
    "v_mov_b32 v79, 0\n\t" \
    "v_mov_b32 v108, 0\n\t" \
    "v_mov_b32 v109, 0\n\t" \
    "v_mov_b32 v110, 0\n\t" \
    "v_mov_b32 v111, 0\n\t" \
    "v_mov_b32 v112, 0\n\t" \
    "v_mov_b32 v113, 0\n\t" \
    "v_mov_b32 v114, 0\n\t" \
    "v_mov_b32 v115, 0\n\t" \
    "v_mov_b32 v116, 0\n\t" \
    "v_mov_b32 v117, 0\n\t" \
    "v_mov_b32 v118, 0\n\t" \
    "v_mov_b32 v119, 0\n\t" \
    "v_mov_b32 v120, 0\n\t" \
    "v_mov_b32 v121, 0\n\t" \
    "v_mov_b32 v122, 0\n\t" \
    "v_mov_b32 v123, 0\n\t" \
    "v_mov_b32 v124, 0\n\t" \
    "v_mov_b32 v125, 0\n\t" \
    "v_mov_b32 v126, 0\n\t" \
    "v_mov_b32 v127, 0\n\t" \
    "s_add_u32 s69, s65, 0\n\t" \
    "s_and_b32 s69, s69, 7\n\t" \
    "s_lshl_b32 s69, s69, 18\n\t" \
    "s_add_u32 s69, s69, s62\n\t" \
    "s_add_u32 s46, s36, s69\n\t" \
    "s_addc_u32 s47, s37, 0\n\t" \
    "s_add_u32 s70, s65, 0\n\t" \
    "s_mul_i32 s69, s70, 171\n\t" \
    "s_lshr_b32 s69, s69, 9\n\t" \
    "s_mul_i32 s70, s70, 589824\n\t" \
    "s_mul_i32 s71, s69, 327680\n\t" \
    "s_add_u32 s70, s70, s71\n\t" \
    "s_lshl1_add_u32 s69, s69, 4\n\t" \
    "s_bitcmp1_b64 s[44:45], s69\n\t" \
    "s_cselect_b32 s69, 8192, 0\n\t" \
    "s_add_u32 s69, s69, s70\n\t" \
    "s_add_u32 s69, s69, 123731968\n\t" \
    "s_add_u32 s38, s36, s69\n\t" \
    "s_addc_u32 s39, s37, 0\n\t" \
    "s_and_b32 s69, s69, 0xffe00000\n\t" \
    "s_add_u32 s69, s69, 1823744\n\t" \
    "v_mov_b32 v17, s69\n\t" \
    "global_load_dword v17, v17, s[36:37]\n\t" \
    "s_barrier\n\t" \
    "ds_read_b32 v160, v11 offset:20480\n\t" \
    "s_waitcnt lgkmcnt(0)\n\t" \
    "v_readfirstlane_b32 s70, v160\n\t" \
    "s_branch vv_loop10_%=\n\t" \
    "vv_Gbad32_%=:\n\t" \
    "s_waitcnt lgkmcnt(0)\n\t" \
    "s_nop 7\n\t" \
    "v_mov_b32 v72, 0\n\t" \
    "v_mov_b32 v73, 0\n\t" \
    "v_mov_b32 v74, 0\n\t" \
    "v_mov_b32 v75, 0\n\t" \
    "v_mov_b32 v76, 0\n\t" \
    "v_mov_b32 v77, 0\n\t" \
    "v_mov_b32 v78, 0\n\t" \
    "v_mov_b32 v79, 0\n\t" \
    "s_cmp_eq_u32 s68, 0\n\t" \
    "s_cbranch_scc1 vv_F36_%=\n\t" \
    "s_sub_u32 s68, s68, 1\n\t" \
    "s_branch vv_G31_%=\n\t" \
    "vv_F36_%=:\n\t" \
    "v_mov_b32 v140, 5767424\n\t" \
    "v_mov_b32 v141, 1\n\t" \
    "global_store_dword v140, v141, s[36:37] sc1\n\t" \
    "s_waitcnt vmcnt(0)\n\t" \
    "s_branch vv_Gc33_%=\n\t" \
    "vv_Gslow34_%=:\n\t" \
    "v_max_u32 v140, v36, v37\n\t" \
    "v_max3_u32 v140, v140, v38, v39\n\t" \
    "v_max3_u32 v140, v140, v40, v41\n\t" \
    "v_max3_u32 v140, v140, v42, v43\n\t" \
    "v_max3_u32 v140, v140, v44, v45\n\t" \
    "v_max3_u32 v140, v140, v46, v47\n\t" \
    "v_max3_u32 v140, v140, v48, v49\n\t" \
    "v_max3_u32 v140, v140, v50, v51\n\t" \
    "v_max3_u32 v140, v140, v52, v53\n\t" \
    "v_max3_u32 v140, v140, v54, v55\n\t" \
    "v_max3_u32 v140, v140, v56, v57\n\t" \
    "v_max3_u32 v140, v140, v58, v59\n\t" \
    "v_max3_u32 v140, v140, v60, v61\n\t" \
    "v_max3_u32 v140, v140, v62, v63\n\t" \
    "v_max3_u32 v140, v140, v64, v65\n\t" \
    "v_max3_u32 v140, v140, v66, v67\n\t" \
    "v_max3_u32 v140, v140, v68, v69\n\t" \
    "v_max3_u32 v140, v140, v70, v71\n\t" \
    "v_cmp_eq_u32 vcc, -1, v140\n\t" \
    "s_cbranch_vccz vv_Gback35_%=\n\t" \
    "s_waitcnt lgkmcnt(0)\n\t" \
    "v_sub_u32 v140, v1, v3\n\t" \
    "s_nop 0\n\t" \
    "v_readfirstlane_b32 s69, v140\n\t" \
    "v_add_u32 v143, 20480, v0\n\t" \
    "s_nop 7\n\t" \
    "s_add_u32 s30, s46, s69\n\t" \
    "s_addc_u32 s31, s47, 0\n\t" \
    "vv_Gag37_%=:\n\t" \
    "global_load_dwordx4 v[36:39], v1, s[46:47] sc1\n\t" \
    "global_load_dwordx4 v[40:43], v23, s[46:47] sc1\n\t" \
    "global_load_dwordx4 v[44:47], v143, s[30:31] sc1\n\t" \
    "global_load_dwordx4 v[48:51], v4, s[30:31] sc1\n\t" \
    "global_load_dwordx4 v[52:55], v5, s[30:31] sc1\n\t" \
    "global_load_dwordx4 v[56:59], v6, s[30:31] sc1\n\t" \
    "global_load_dwordx4 v[60:63], v7, s[30:31] sc1\n\t" \
    "global_load_dwordx4 v[64:67], v8, s[30:31] sc1\n\t" \
    "global_load_dwordx4 v[68:71], v9, s[30:31] sc1\n\t" \
    "s_waitcnt vmcnt(0)\n\t" \
    "v_max_u32 v140, v36, v37\n\t" \
    "v_max3_u32 v140, v140, v38, v39\n\t" \
    "v_max3_u32 v140, v140, v40, v41\n\t" \
    "v_max3_u32 v140, v140, v42, v43\n\t" \
    "v_max3_u32 v140, v140, v44, v45\n\t" \
    "v_max3_u32 v140, v140, v46, v47\n\t" \
    "v_max3_u32 v140, v140, v48, v49\n\t" \
    "v_max3_u32 v140, v140, v50, v51\n\t" \
    "v_max3_u32 v140, v140, v52, v53\n\t" \
    "v_max3_u32 v140, v140, v54, v55\n\t" \
    "v_max3_u32 v140, v140, v56, v57\n\t" \
    "v_max3_u32 v140, v140, v58, v59\n\t" \
    "v_max3_u32 v140, v140, v60, v61\n\t" \
    "v_max3_u32 v140, v140, v62, v63\n\t" \
    "v_max3_u32 v140, v140, v64, v65\n\t" \
    "v_max3_u32 v140, v140, v66, v67\n\t" \
    "v_max3_u32 v140, v140, v68, v69\n\t" \
    "v_max3_u32 v140, v140, v70, v71\n\t" \
    "v_cmp_eq_u32 vcc, -1, v140\n\t" \
    "s_cbranch_vccz vv_Gredo38_%=\n\t" \
    "s_cmp_eq_u32 s68, 0\n\t" \
    "s_cbranch_scc1 vv_F39_%=\n\t" \
    "s_sub_u32 s68, s68, 1\n\t" \
    "s_sleep 1\n\t" \
    "s_branch vv_Gag37_%=\n\t" \
    "vv_F39_%=:\n\t" \
    "v_mov_b32 v140, 5767424\n\t" \
    "v_mov_b32 v141, 2\n\t" \
    "global_store_dword v140, v141, s[36:37] sc1\n\t" \
    "s_waitcnt vmcnt(0)\n\t" \
    "s_branch vv_Gredo38_%=\n\t" \
    "vv_Gredo38_%=:\n\t" \
    "v_mov_b32 v72, 0\n\t" \
    "v_mov_b32 v73, 0\n\t" \
    "v_mov_b32 v74, 0\n\t" \
    "v_mov_b32 v75, 0\n\t" \
    "v_mov_b32 v76, 0\n\t" \
    "v_mov_b32 v77, 0\n\t" \
    "v_mov_b32 v78, 0\n\t" \
    "v_mov_b32 v79, 0\n\t" \
    "v_smfmac_f32_16x16x64_bf16 v[72:75], v[36:39], a[16:23], v10\n\t" \
    "v_smfmac_f32_16x16x64_bf16 v[72:75], v[40:43], a[24:31], v10\n\t" \
    "v_smfmac_f32_16x16x64_bf16 v[72:75], v[48:51], a[32:39], v10\n\t" \
    "v_smfmac_f32_16x16x64_bf16 v[72:75], v[52:55], a[40:47], v10\n\t" \
    "v_smfmac_f32_16x16x64_bf16 v[72:75], v[56:59], a[48:55], v10\n\t" \
    "v_smfmac_f32_16x16x64_bf16 v[72:75], v[60:63], a[56:63], v10\n\t" \
    "v_smfmac_f32_16x16x64_bf16 v[72:75], v[64:67], a[0:7], v10\n\t" \
    "v_smfmac_f32_16x16x64_bf16 v[72:75], v[68:71], a[8:15], v10\n\t" \
    "v_smfmac_f32_16x16x64_bf16 v[72:75], v[44:47], a[64:71], v10\n\t" \
    "v_smfmac_f32_16x16x64_bf16 v[76:79], v[36:39], a[88:95], v10\n\t" \
    "v_smfmac_f32_16x16x64_bf16 v[76:79], v[40:43], a[96:103], v10\n\t" \
    "v_smfmac_f32_16x16x64_bf16 v[76:79], v[48:51], a[104:111], v10\n\t" \
    "v_smfmac_f32_16x16x64_bf16 v[76:79], v[52:55], a[112:119], v10\n\t" \
    "v_smfmac_f32_16x16x64_bf16 v[76:79], v[56:59], a[120:127], v10\n\t" \
    "v_smfmac_f32_16x16x64_bf16 v[76:79], v[60:63], a[128:135], v10\n\t" \
    "v_smfmac_f32_16x16x64_bf16 v[76:79], v[64:67], a[72:79], v10\n\t" \
    "v_smfmac_f32_16x16x64_bf16 v[76:79], v[68:71], a[80:87], v10\n\t" \
    "v_smfmac_f32_16x16x64_bf16 v[76:79], v[44:47], a[136:143], v10\n\t" \
    "v_add_f32 v72, v72, v74\n\t" \
    "v_add_f32 v73, v73, v75\n\t" \
    "v_cvt_pk_bf16_f32 v80, v72, v73\n\t" \
    "ds_write_b32 v12, v80 offset:0\n\t" \
    "s_nop 3\n\t" \
    "v_pk_add_f32 v[76:77], v[76:77], v[78:79]\n\t" \
    "v_cvt_pk_bf16_f32 v81, v76, v77\n\t" \
    "ds_write_b32 v12, v81 offset:256\n\t" \
    "s_branch vv_Gback35_%=\n\t" \
    "vv_end27_%=:\n\t" \
    "s_endpgm\n\t" \
    ".p2align 6\n\t" \
    "s_nop 0\n\t" \
    "s_nop 0\n\t" \
    "s_nop 0\n\t" \
    "s_nop 0\n\t" \
    "s_nop 0\n\t" \
    "s_nop 0\n\t" \
    "vv_loop11_%=:\n\t" \
    "s_getpc_b64 s[30:31]\n\t" \
    "vv_Wp42_%=:\n\t" \
    "s_add_u32 s30, s30, vv_We43_%=-vv_Wp42_%=\n\t" \
    "s_addc_u32 s31, s31, 0\n\t" \
    "s_sub_u32 s70, s70, 96\n\t" \
    "vv_W41_%=:\n\t" \
    "s_memtime s[56:57]\n\t" \
    "s_waitcnt lgkmcnt(0)\n\t" \
    "s_sub_u32 s69, s70, s56\n\t" \
    "s_cmp_gt_i32 s69, 80\n\t" \
    "s_cbranch_scc1 vv_W41_%=\n\t" \
    "s_max_i32 s69, s69, 0\n\t" \
    "s_and_b32 s69, s69, -4\n\t" \
    "s_sub_u32 s30, s30, s69\n\t" \
    "s_subb_u32 s31, s31, 0\n\t" \
    "s_setpc_b64 s[30:31]\n\t" \
    "s_nop 0\n\t" \
    "s_nop 0\n\t" \
    "s_nop 0\n\t" \
    "s_nop 0\n\t" \
    "s_nop 0\n\t" \
    "s_nop 0\n\t" \
    "s_nop 0\n\t" \
    "s_nop 0\n\t" \
    "s_nop 0\n\t" \
    "s_nop 0\n\t" \
    "s_nop 0\n\t" \
    "s_nop 0\n\t" \
    "s_nop 0\n\t" \
    "s_nop 0\n\t" \
    "s_nop 0\n\t" \
    "s_nop 0\n\t" \
    "s_nop 0\n\t" \
    "s_nop 0\n\t" \
    "s_nop 0\n\t" \
    "s_nop 0\n\t" \
    "vv_We43_%=:\n\t" \
    "vv_G44_%=:\n\t" \
    "global_load_dwordx4 v[36:39], v1, s[46:47] sc1\n\t" \
    "global_load_dword v141, v2, s[46:47] sc1\n\t" \
    "global_load_dwordx4 v[40:43], v23, s[46:47] sc1\n\t" \
    "s_waitcnt vmcnt(2)\n\t" \
    "ds_write_b128 v3, v[36:39]\n\t" \
    "v_smfmac_f32_16x16x64_bf16 v[72:75], v[36:39], a[32:39], v10\n\t" \
    "s_waitcnt vmcnt(1)\n\t" \
    "ds_write_b32 v22, v141 offset:19968\n\t" \
    "s_waitcnt vmcnt(0)\n\t" \
    "ds_write_b128 v3, v[40:43] offset:1024\n\t" \
    "v_max3_u32 v140, v36, v40, v141\n\t" \
    "v_cmp_eq_u32 vcc, -1, v140\n\t" \
    "s_cbranch_vccnz vv_Gbad45_%=\n\t" \
    "vv_Gc46_%=:\n\t" \
    "s_waitcnt lgkmcnt(0)\n\t" \
    "s_barrier\n\t" \
    "ds_read_b128 v[48:51], v4\n\t" \
    "ds_read_b128 v[52:55], v5\n\t" \
    "v_smfmac_f32_16x16x64_bf16 v[76:79], v[36:39], a[104:111], v10\n\t" \
    "ds_read_b128 v[56:59], v6\n\t" \
    "ds_read_b128 v[60:63], v7\n\t" \
    "v_smfmac_f32_16x16x64_bf16 v[72:75], v[40:43], a[40:47], v10\n\t" \
    "ds_read_b128 v[64:67], v8\n\t" \
    "v_smfmac_f32_16x16x64_bf16 v[76:79], v[40:43], a[112:119], v10\n\t" \
    "ds_read_b128 v[68:71], v9\n\t" \
    "s_waitcnt lgkmcnt(5)\n\t" \
    "v_smfmac_f32_16x16x64_bf16 v[72:75], v[48:51], a[48:55], v10\n\t" \
    "ds_read_b128 v[44:47], v0 offset:19456\n\t" \
    "s_waitcnt lgkmcnt(5)\n\t" \
    "v_smfmac_f32_16x16x64_bf16 v[72:75], v[52:55], a[56:63], v10\n\t" \
    "s_waitcnt lgkmcnt(4)\n\t" \
    "v_smfmac_f32_16x16x64_bf16 v[72:75], v[56:59], a[0:7], v10\n\t" \
    "s_waitcnt lgkmcnt(3)\n\t" \
    "v_smfmac_f32_16x16x64_bf16 v[72:75], v[60:63], a[8:15], v10\n\t" \
    "s_waitcnt lgkmcnt(2)\n\t" \
    "v_smfmac_f32_16x16x64_bf16 v[72:75], v[64:67], a[16:23], v10\n\t" \
    "s_waitcnt lgkmcnt(1)\n\t" \
    "v_smfmac_f32_16x16x64_bf16 v[72:75], v[68:71], a[24:31], v10\n\t" \
    "s_waitcnt lgkmcnt(0)\n\t" \
    "v_smfmac_f32_16x16x64_bf16 v[72:75], v[44:47], a[64:71], v10\n\t" \
    "s_nop 2\n\t" \
    "v_smfmac_f32_16x16x64_bf16 v[76:79], v[48:51], a[120:127], v10\n\t" \
    "s_nop 2\n\t" \
    "v_smfmac_f32_16x16x64_bf16 v[76:79], v[52:55], a[128:135], v10\n\t" \
    "v_add_f32 v72, v72, v74\n\t" \
    "v_smfmac_f32_16x16x64_bf16 v[76:79], v[56:59], a[72:79], v10\n\t" \
    "v_add_f32 v73, v73, v75\n\t" \
    "v_smfmac_f32_16x16x64_bf16 v[76:79], v[60:63], a[80:87], v10\n\t" \
    "v_cvt_pk_bf16_f32 v80, v72, v73\n\t" \
    "v_smfmac_f32_16x16x64_bf16 v[76:79], v[64:67], a[88:95], v10\n\t" \
    "ds_write_b32 v12, v80 offset:0\n\t" \
    "v_smfmac_f32_16x16x64_bf16 v[76:79], v[68:71], a[96:103], v10\n\t" \
    "v_smfmac_f32_16x16x64_bf16 v[76:79], v[44:47], a[136:143], v10\n\t" \
    "s_nop 6\n\t" \
    "v_cmp_u_f32 vcc, v72, v73\n\t" \
    "v_pk_add_f32 v[76:77], v[76:77], v[78:79]\n\t" \
    "v_cvt_pk_bf16_f32 v81, v76, v77\n\t" \
    "ds_write_b32 v12, v81 offset:256\n\t" \
    "s_cbranch_vccnz vv_Gslow47_%=\n\t" \
    "vv_Gback48_%=:\n\t" \
    "s_waitcnt lgkmcnt(0)\n\t" \
    "s_barrier\n\t" \
    "ds_read_b64_tr_b16 v[84:85], v13 offset:0\n\t" \
    "v_smfmac_f32_16x16x64_bf16 v[100:103], v[36:39], a[0:7], v10\n\t" \
    "ds_read_b64_tr_b16 v[86:87], v13 offset:64\n\t" \
    "ds_read_b64_tr_b16 v[92:93], v13 offset:1152\n\t" \
    "v_smfmac_f32_16x16x64_bf16 v[100:103], v[36:39], a[0:7], v10\n\t" \
    "ds_read_b64_tr_b16 v[94:95], v13 offset:1216\n\t" \
    "ds_read_b64_tr_b16 v[88:89], v25 offset:0\n\t" \
    "v_smfmac_f32_16x16x64_bf16 v[100:103], v[36:39], a[0:7], v10\n\t" \
    "ds_read_b64_tr_b16 v[90:91], v25 offset:64\n\t" \
    "s_waitcnt lgkmcnt(4)\n\t" \
    "v_smfmac_f32_16x16x64_bf16 v[124:127], v[84:87], v[240:247], v19\n\t" \
    "ds_read_b64_tr_b16 v[96:97], v25 offset:1152\n\t" \
    "ds_read_b64_tr_b16 v[98:99], v25 offset:1216\n\t" \
    "s_waitcnt lgkmcnt(4)\n\t" \
    "v_smfmac_f32_16x16x64_bf16 v[124:127], v[92:95], v[248:255], v19\n\t" \
    "s_nop 2\n\t" \
    "v_smfmac_f32_16x16x64_bf16 v[108:111], v[84:87], a[144:151], v19\n\t" \
    "s_nop 2\n\t" \
    "v_smfmac_f32_16x16x64_bf16 v[108:111], v[92:95], a[160:167], v19\n\t" \
    "s_nop 1\n\t" \
    "v_permlane32_swap_b32 v124, v126\n\t" \
    "v_smfmac_f32_16x16x64_bf16 v[112:115], v[84:87], a[176:183], v19\n\t" \
    "s_nop 1\n\t" \
    "v_permlane32_swap_b32 v125, v127\n\t" \
    "v_smfmac_f32_16x16x64_bf16 v[112:115], v[92:95], a[192:199], v19\n\t" \
    "s_nop 0\n\t" \
    "v_add_f32 v124, v124, v126\n\t" \
    "s_waitcnt lgkmcnt(2)\n\t" \
    "v_smfmac_f32_16x16x64_bf16 v[108:111], v[88:91], a[152:159], v19\n\t" \
    "s_nop 1\n\t" \
    "v_add_f32 v125, v125, v127\n\t" \
    "v_smfmac_f32_16x16x64_bf16 v[112:115], v[88:91], a[184:191], v19\n\t" \
    "s_nop 0\n\t" \
    "v_cvt_pk_bf16_f32 v136, v124, v125\n\t" \
    "s_waitcnt lgkmcnt(0)\n\t" \
    "v_smfmac_f32_16x16x64_bf16 v[108:111], v[96:99], a[168:175], v19\n\t" \
    "s_nop 1\n\t" \
    "v_cvt_pk_bf16_f32 v137, v124, v125\n\t" \
    "v_smfmac_f32_16x16x64_bf16 v[112:115], v[96:99], a[200:207], v19\n\t" \
    "s_nop 1\n\t" \
    "v_permlane32_swap_b32 v136, v137\n\t" \
    "v_smfmac_f32_16x16x64_bf16 v[116:119], v[84:87], a[208:215], v19\n\t" \
    "s_mov_b32 exec_hi, 0\n\t" \
    "global_store_dwordx2 v16, v[136:137], s[38:39] sc1\n\t" \
    "s_mov_b32 exec_hi, -1\n\t" \
    "v_cvt_pk_bf16_f32 v128, v108, v109\n\t" \
    "v_smfmac_f32_16x16x64_bf16 v[116:119], v[88:91], a[216:223], v19\n\t" \
    "s_nop 1\n\t" \
    "v_cvt_pk_bf16_f32 v129, v110, v111\n\t" \
    "v_smfmac_f32_16x16x64_bf16 v[116:119], v[92:95], a[224:231], v19\n\t" \
    "s_nop 1\n\t" \
    "v_cvt_pk_bf16_f32 v130, v112, v113\n\t" \
    "v_smfmac_f32_16x16x64_bf16 v[116:119], v[96:99], a[232:239], v19\n\t" \
    "s_nop 1\n\t" \
    "v_cvt_pk_bf16_f32 v131, v114, v115\n\t" \
    "v_smfmac_f32_16x16x64_bf16 v[120:123], v[84:87], a[240:247], v19\n\t" \
    "s_nop 1\n\t" \
    "v_permlane16_swap_b32 v128, v130\n\t" \
    "v_smfmac_f32_16x16x64_bf16 v[120:123], v[88:91], a[248:255], v19\n\t" \
    "s_nop 1\n\t" \
    "v_permlane16_swap_b32 v129, v131\n\t" \
    "v_smfmac_f32_16x16x64_bf16 v[120:123], v[92:95], v[224:231], v19\n\t" \
    "s_nop 0\n\t" \
    "global_store_dwordx4 v14, v[128:131], s[38:39] sc1\n\t" \
    "v_cvt_pk_bf16_f32 v132, v116, v117\n\t" \
    "v_smfmac_f32_16x16x64_bf16 v[120:123], v[96:99], v[232:239], v19\n\t" \
    "s_nop 1\n\t" \
    "v_cvt_pk_bf16_f32 v133, v118, v119\n\t" \
    "s_nop 4\n\t" \
    "v_cvt_pk_bf16_f32 v134, v120, v121\n\t" \
    "v_cvt_pk_bf16_f32 v135, v122, v123\n\t" \
    "s_nop 0\n\t" \
    "v_permlane16_swap_b32 v132, v134\n\t" \
    "v_permlane16_swap_b32 v133, v135\n\t" \
    "global_store_dwordx4 v15, v[132:135], s[38:39] sc1\n\t" \
    "s_cmp_eq_u32 s65, 63\n\t" \
    "s_cbranch_scc1 vv_end40_%=\n\t" \
    "s_add_u32 s65, s65, 1\n\t" \
    "v_mov_b32 v72, 0\n\t" \
    "v_mov_b32 v73, 0\n\t" \
    "v_mov_b32 v74, 0\n\t" \
    "v_mov_b32 v75, 0\n\t" \
    "v_mov_b32 v76, 0\n\t" \
    "v_mov_b32 v77, 0\n\t" \
    "v_mov_b32 v78, 0\n\t" \
    "v_mov_b32 v79, 0\n\t" \
    "v_mov_b32 v108, 0\n\t" \
    "v_mov_b32 v109, 0\n\t" \
    "v_mov_b32 v110, 0\n\t" \
    "v_mov_b32 v111, 0\n\t" \
    "v_mov_b32 v112, 0\n\t" \
    "v_mov_b32 v113, 0\n\t" \
    "v_mov_b32 v114, 0\n\t" \
    "v_mov_b32 v115, 0\n\t" \
    "v_mov_b32 v116, 0\n\t" \
    "v_mov_b32 v117, 0\n\t" \
    "v_mov_b32 v118, 0\n\t" \
    "v_mov_b32 v119, 0\n\t" \
    "v_mov_b32 v120, 0\n\t" \
    "v_mov_b32 v121, 0\n\t" \
    "v_mov_b32 v122, 0\n\t" \
    "v_mov_b32 v123, 0\n\t" \
    "v_mov_b32 v124, 0\n\t" \
    "v_mov_b32 v125, 0\n\t" \
    "v_mov_b32 v126, 0\n\t" \
    "v_mov_b32 v127, 0\n\t" \
    "s_add_u32 s69, s65, 0\n\t" \
    "s_and_b32 s69, s69, 7\n\t" \
    "s_lshl_b32 s69, s69, 18\n\t" \
    "s_add_u32 s69, s69, s62\n\t" \
    "s_add_u32 s46, s36, s69\n\t" \
    "s_addc_u32 s47, s37, 0\n\t" \
    "s_add_u32 s70, s65, 0\n\t" \
    "s_mul_i32 s69, s70, 171\n\t" \
    "s_lshr_b32 s69, s69, 9\n\t" \
    "s_mul_i32 s70, s70, 589824\n\t" \
    "s_mul_i32 s71, s69, 327680\n\t" \
    "s_add_u32 s70, s70, s71\n\t" \
    "s_lshl1_add_u32 s69, s69, 4\n\t" \
    "s_bitcmp1_b64 s[44:45], s69\n\t" \
    "s_cselect_b32 s69, 8192, 0\n\t" \
    "s_add_u32 s69, s69, s70\n\t" \
    "s_add_u32 s69, s69, 123731968\n\t" \
    "s_add_u32 s38, s36, s69\n\t" \
    "s_addc_u32 s39, s37, 0\n\t" \
    "s_and_b32 s69, s69, 0xffe00000\n\t" \
    "s_add_u32 s69, s69, 1823744\n\t" \
    "v_mov_b32 v17, s69\n\t" \
    "global_load_dword v17, v17, s[36:37]\n\t" \
    "s_barrier\n\t" \
    "ds_read_b32 v160, v11 offset:20480\n\t" \
    "s_waitcnt lgkmcnt(0)\n\t" \
    "v_readfirstlane_b32 s70, v160\n\t" \
    "s_branch vv_loop11_%=\n\t" \
    "vv_Gbad45_%=:\n\t" \
    "s_waitcnt lgkmcnt(0)\n\t" \
    "s_nop 7\n\t" \
    "v_mov_b32 v72, 0\n\t" \
    "v_mov_b32 v73, 0\n\t" \
    "v_mov_b32 v74, 0\n\t" \
    "v_mov_b32 v75, 0\n\t" \
    "v_mov_b32 v76, 0\n\t" \
    "v_mov_b32 v77, 0\n\t" \
    "v_mov_b32 v78, 0\n\t" \
    "v_mov_b32 v79, 0\n\t" \
    "s_cmp_eq_u32 s68, 0\n\t" \
    "s_cbranch_scc1 vv_F49_%=\n\t" \
    "s_sub_u32 s68, s68, 1\n\t" \
    "s_branch vv_G44_%=\n\t" \
    "vv_F49_%=:\n\t" \
    "v_mov_b32 v140, 5767424\n\t" \
    "v_mov_b32 v141, 1\n\t" \
    "global_store_dword v140, v141, s[36:37] sc1\n\t" \
    "s_waitcnt vmcnt(0)\n\t" \
    "s_branch vv_Gc46_%=\n\t" \
    "vv_Gslow47_%=:\n\t" \
    "v_max_u32 v140, v36, v37\n\t" \
    "v_max3_u32 v140, v140, v38, v39\n\t" \
    "v_max3_u32 v140, v140, v40, v41\n\t" \
    "v_max3_u32 v140, v140, v42, v43\n\t" \
    "v_max3_u32 v140, v140, v44, v45\n\t" \
    "v_max3_u32 v140, v140, v46, v47\n\t" \
    "v_max3_u32 v140, v140, v48, v49\n\t" \
    "v_max3_u32 v140, v140, v50, v51\n\t" \
    "v_max3_u32 v140, v140, v52, v53\n\t" \
    "v_max3_u32 v140, v140, v54, v55\n\t" \
    "v_max3_u32 v140, v140, v56, v57\n\t" \
    "v_max3_u32 v140, v140, v58, v59\n\t" \
    "v_max3_u32 v140, v140, v60, v61\n\t" \
    "v_max3_u32 v140, v140, v62, v63\n\t" \
    "v_max3_u32 v140, v140, v64, v65\n\t" \
    "v_max3_u32 v140, v140, v66, v67\n\t" \
    "v_max3_u32 v140, v140, v68, v69\n\t" \
    "v_max3_u32 v140, v140, v70, v71\n\t" \
    "v_cmp_eq_u32 vcc, -1, v140\n\t" \
    "s_cbranch_vccz vv_Gback48_%=\n\t" \
    "s_waitcnt lgkmcnt(0)\n\t" \
    "v_sub_u32 v140, v1, v3\n\t" \
    "s_nop 0\n\t" \
    "v_readfirstlane_b32 s69, v140\n\t" \
    "v_add_u32 v143, 20480, v0\n\t" \
    "s_nop 7\n\t" \
    "s_add_u32 s30, s46, s69\n\t" \
    "s_addc_u32 s31, s47, 0\n\t" \
    "vv_Gag50_%=:\n\t" \
    "global_load_dwordx4 v[36:39], v1, s[46:47] sc1\n\t" \
    "global_load_dwordx4 v[40:43], v23, s[46:47] sc1\n\t" \
    "global_load_dwordx4 v[44:47], v143, s[30:31] sc1\n\t" \
    "global_load_dwordx4 v[48:51], v4, s[30:31] sc1\n\t" \
    "global_load_dwordx4 v[52:55], v5, s[30:31] sc1\n\t" \
    "global_load_dwordx4 v[56:59], v6, s[30:31] sc1\n\t" \
    "global_load_dwordx4 v[60:63], v7, s[30:31] sc1\n\t" \
    "global_load_dwordx4 v[64:67], v8, s[30:31] sc1\n\t" \
    "global_load_dwordx4 v[68:71], v9, s[30:31] sc1\n\t" \
    "s_waitcnt vmcnt(0)\n\t" \
    "v_max_u32 v140, v36, v37\n\t" \
    "v_max3_u32 v140, v140, v38, v39\n\t" \
    "v_max3_u32 v140, v140, v40, v41\n\t" \
    "v_max3_u32 v140, v140, v42, v43\n\t" \
    "v_max3_u32 v140, v140, v44, v45\n\t" \
    "v_max3_u32 v140, v140, v46, v47\n\t" \
    "v_max3_u32 v140, v140, v48, v49\n\t" \
    "v_max3_u32 v140, v140, v50, v51\n\t" \
    "v_max3_u32 v140, v140, v52, v53\n\t" \
    "v_max3_u32 v140, v140, v54, v55\n\t" \
    "v_max3_u32 v140, v140, v56, v57\n\t" \
    "v_max3_u32 v140, v140, v58, v59\n\t" \
    "v_max3_u32 v140, v140, v60, v61\n\t" \
    "v_max3_u32 v140, v140, v62, v63\n\t" \
    "v_max3_u32 v140, v140, v64, v65\n\t" \
    "v_max3_u32 v140, v140, v66, v67\n\t" \
    "v_max3_u32 v140, v140, v68, v69\n\t" \
    "v_max3_u32 v140, v140, v70, v71\n\t" \
    "v_cmp_eq_u32 vcc, -1, v140\n\t" \
    "s_cbranch_vccz vv_Gredo51_%=\n\t" \
    "s_cmp_eq_u32 s68, 0\n\t" \
    "s_cbranch_scc1 vv_F52_%=\n\t" \
    "s_sub_u32 s68, s68, 1\n\t" \
    "s_sleep 1\n\t" \
    "s_branch vv_Gag50_%=\n\t" \
    "vv_F52_%=:\n\t" \
    "v_mov_b32 v140, 5767424\n\t" \
    "v_mov_b32 v141, 2\n\t" \
    "global_store_dword v140, v141, s[36:37] sc1\n\t" \
    "s_waitcnt vmcnt(0)\n\t" \
    "s_branch vv_Gredo51_%=\n\t" \
    "vv_Gredo51_%=:\n\t" \
    "v_mov_b32 v72, 0\n\t" \
    "v_mov_b32 v73, 0\n\t" \
    "v_mov_b32 v74, 0\n\t" \
    "v_mov_b32 v75, 0\n\t" \
    "v_mov_b32 v76, 0\n\t" \
    "v_mov_b32 v77, 0\n\t" \
    "v_mov_b32 v78, 0\n\t" \
    "v_mov_b32 v79, 0\n\t" \
    "v_smfmac_f32_16x16x64_bf16 v[72:75], v[36:39], a[32:39], v10\n\t" \
    "v_smfmac_f32_16x16x64_bf16 v[72:75], v[40:43], a[40:47], v10\n\t" \
    "v_smfmac_f32_16x16x64_bf16 v[72:75], v[48:51], a[48:55], v10\n\t" \
    "v_smfmac_f32_16x16x64_bf16 v[72:75], v[52:55], a[56:63], v10\n\t" \
    "v_smfmac_f32_16x16x64_bf16 v[72:75], v[56:59], a[0:7], v10\n\t" \
    "v_smfmac_f32_16x16x64_bf16 v[72:75], v[60:63], a[8:15], v10\n\t" \
    "v_smfmac_f32_16x16x64_bf16 v[72:75], v[64:67], a[16:23], v10\n\t" \
    "v_smfmac_f32_16x16x64_bf16 v[72:75], v[68:71], a[24:31], v10\n\t" \
    "v_smfmac_f32_16x16x64_bf16 v[72:75], v[44:47], a[64:71], v10\n\t" \
    "v_smfmac_f32_16x16x64_bf16 v[76:79], v[36:39], a[104:111], v10\n\t" \
    "v_smfmac_f32_16x16x64_bf16 v[76:79], v[40:43], a[112:119], v10\n\t" \
    "v_smfmac_f32_16x16x64_bf16 v[76:79], v[48:51], a[120:127], v10\n\t" \
    "v_smfmac_f32_16x16x64_bf16 v[76:79], v[52:55], a[128:135], v10\n\t" \
    "v_smfmac_f32_16x16x64_bf16 v[76:79], v[56:59], a[72:79], v10\n\t" \
    "v_smfmac_f32_16x16x64_bf16 v[76:79], v[60:63], a[80:87], v10\n\t" \
    "v_smfmac_f32_16x16x64_bf16 v[76:79], v[64:67], a[88:95], v10\n\t" \
    "v_smfmac_f32_16x16x64_bf16 v[76:79], v[68:71], a[96:103], v10\n\t" \
    "v_smfmac_f32_16x16x64_bf16 v[76:79], v[44:47], a[136:143], v10\n\t" \
    "v_add_f32 v72, v72, v74\n\t" \
    "v_add_f32 v73, v73, v75\n\t" \
    "v_cvt_pk_bf16_f32 v80, v72, v73\n\t" \
    "ds_write_b32 v12, v80 offset:0\n\t" \
    "s_nop 3\n\t" \
    "v_pk_add_f32 v[76:77], v[76:77], v[78:79]\n\t" \
    "v_cvt_pk_bf16_f32 v81, v76, v77\n\t" \
    "ds_write_b32 v12, v81 offset:256\n\t" \
    "s_branch vv_Gback48_%=\n\t" \
    "vv_end40_%=:\n\t" \
    "s_endpgm\n\t" \
    ".p2align 6\n\t" \
    "s_nop 0\n\t" \
    "s_nop 0\n\t" \
    "s_nop 0\n\t" \
    "s_nop 0\n\t" \
    "s_nop 0\n\t" \
    "s_nop 0\n\t" \
    "vv_loop12_%=:\n\t" \
    "s_getpc_b64 s[30:31]\n\t" \
    "vv_Wp55_%=:\n\t" \
    "s_add_u32 s30, s30, vv_We56_%=-vv_Wp55_%=\n\t" \
    "s_addc_u32 s31, s31, 0\n\t" \
    "s_sub_u32 s70, s70, 96\n\t" \
    "vv_W54_%=:\n\t" \
    "s_memtime s[56:57]\n\t" \
    "s_waitcnt lgkmcnt(0)\n\t" \
    "s_sub_u32 s69, s70, s56\n\t" \
    "s_cmp_gt_i32 s69, 80\n\t" \
    "s_cbranch_scc1 vv_W54_%=\n\t" \
    "s_max_i32 s69, s69, 0\n\t" \
    "s_and_b32 s69, s69, -4\n\t" \
    "s_sub_u32 s30, s30, s69\n\t" \
    "s_subb_u32 s31, s31, 0\n\t" \
    "s_setpc_b64 s[30:31]\n\t" \
    "s_nop 0\n\t" \
    "s_nop 0\n\t" \
    "s_nop 0\n\t" \
    "s_nop 0\n\t" \
    "s_nop 0\n\t" \
    "s_nop 0\n\t" \
    "s_nop 0\n\t" \
    "s_nop 0\n\t" \
    "s_nop 0\n\t" \
    "s_nop 0\n\t" \
    "s_nop 0\n\t" \
    "s_nop 0\n\t" \
    "s_nop 0\n\t" \
    "s_nop 0\n\t" \
    "s_nop 0\n\t" \
    "s_nop 0\n\t" \
    "s_nop 0\n\t" \
    "s_nop 0\n\t" \
    "s_nop 0\n\t" \
    "s_nop 0\n\t" \
    "vv_We56_%=:\n\t" \
    "vv_G57_%=:\n\t" \
    "global_load_dwordx4 v[36:39], v1, s[46:47] sc1\n\t" \
    "global_load_dword v141, v2, s[46:47] sc1\n\t" \
    "global_load_dwordx4 v[40:43], v23, s[46:47] sc1\n\t" \
    "s_waitcnt vmcnt(2)\n\t" \
    "ds_write_b128 v3, v[36:39]\n\t" \
    "v_smfmac_f32_16x16x64_bf16 v[72:75], v[36:39], a[48:55], v10\n\t" \
    "s_waitcnt vmcnt(1)\n\t" \
    "ds_write_b32 v22, v141 offset:20224\n\t" \
    "s_waitcnt vmcnt(0)\n\t" \
    "ds_write_b128 v3, v[40:43] offset:1024\n\t" \
    "v_max3_u32 v140, v36, v40, v141\n\t" \
    "v_cmp_eq_u32 vcc, -1, v140\n\t" \
    "s_cbranch_vccnz vv_Gbad58_%=\n\t" \
    "vv_Gc59_%=:\n\t" \
    "s_waitcnt lgkmcnt(0)\n\t" \
    "s_barrier\n\t" \
    "ds_read_b128 v[48:51], v4\n\t" \
    "ds_read_b128 v[52:55], v5\n\t" \
    "v_smfmac_f32_16x16x64_bf16 v[76:79], v[36:39], a[120:127], v10\n\t" \
    "ds_read_b128 v[56:59], v6\n\t" \
    "ds_read_b128 v[60:63], v7\n\t" \
    "v_smfmac_f32_16x16x64_bf16 v[72:75], v[40:43], a[56:63], v10\n\t" \
    "ds_read_b128 v[64:67], v8\n\t" \
    "v_smfmac_f32_16x16x64_bf16 v[76:79], v[40:43], a[128:135], v10\n\t" \
    "ds_read_b128 v[68:71], v9\n\t" \
    "s_waitcnt lgkmcnt(5)\n\t" \
    "v_smfmac_f32_16x16x64_bf16 v[72:75], v[48:51], a[0:7], v10\n\t" \
    "ds_read_b128 v[44:47], v0 offset:19456\n\t" \
    "s_waitcnt lgkmcnt(5)\n\t" \
    "v_smfmac_f32_16x16x64_bf16 v[72:75], v[52:55], a[8:15], v10\n\t" \
    "s_waitcnt lgkmcnt(4)\n\t" \
    "v_smfmac_f32_16x16x64_bf16 v[72:75], v[56:59], a[16:23], v10\n\t" \
    "s_waitcnt lgkmcnt(3)\n\t" \
    "v_smfmac_f32_16x16x64_bf16 v[72:75], v[60:63], a[24:31], v10\n\t" \
    "s_waitcnt lgkmcnt(2)\n\t" \
    "v_smfmac_f32_16x16x64_bf16 v[72:75], v[64:67], a[32:39], v10\n\t" \
    "s_waitcnt lgkmcnt(1)\n\t" \
    "v_smfmac_f32_16x16x64_bf16 v[72:75], v[68:71], a[40:47], v10\n\t" \
    "s_waitcnt lgkmcnt(0)\n\t" \
    "v_smfmac_f32_16x16x64_bf16 v[72:75], v[44:47], a[64:71], v10\n\t" \
    "s_nop 2\n\t" \
    "v_smfmac_f32_16x16x64_bf16 v[76:79], v[48:51], a[72:79], v10\n\t" \
    "s_nop 2\n\t" \
    "v_smfmac_f32_16x16x64_bf16 v[76:79], v[52:55], a[80:87], v10\n\t" \
    "v_add_f32 v72, v72, v74\n\t" \
    "v_smfmac_f32_16x16x64_bf16 v[76:79], v[56:59], a[88:95], v10\n\t" \
    "v_add_f32 v73, v73, v75\n\t" \
    "v_smfmac_f32_16x16x64_bf16 v[76:79], v[60:63], a[96:103], v10\n\t" \
    "v_cvt_pk_bf16_f32 v80, v72, v73\n\t" \
    "v_smfmac_f32_16x16x64_bf16 v[76:79], v[64:67], a[104:111], v10\n\t" \
    "ds_write_b32 v12, v80 offset:0\n\t" \
    "v_smfmac_f32_16x16x64_bf16 v[76:79], v[68:71], a[112:119], v10\n\t" \
    "v_smfmac_f32_16x16x64_bf16 v[76:79], v[44:47], a[136:143], v10\n\t" \
    "s_nop 6\n\t" \
    "v_cmp_u_f32 vcc, v72, v73\n\t" \
    "v_pk_add_f32 v[76:77], v[76:77], v[78:79]\n\t" \
    "v_cvt_pk_bf16_f32 v81, v76, v77\n\t" \
    "ds_write_b32 v12, v81 offset:256\n\t" \
    "s_cbranch_vccnz vv_Gslow60_%=\n\t" \
    "vv_Gback61_%=:\n\t" \
    "s_waitcnt lgkmcnt(0)\n\t" \
    "s_barrier\n\t" \
    "ds_read_b64_tr_b16 v[84:85], v13 offset:0\n\t" \
    "v_smfmac_f32_16x16x64_bf16 v[100:103], v[36:39], a[0:7], v10\n\t" \
    "ds_read_b64_tr_b16 v[86:87], v13 offset:64\n\t" \
    "ds_read_b64_tr_b16 v[92:93], v13 offset:1152\n\t" \
    "v_smfmac_f32_16x16x64_bf16 v[100:103], v[36:39], a[0:7], v10\n\t" \
    "ds_read_b64_tr_b16 v[94:95], v13 offset:1216\n\t" \
    "ds_read_b64_tr_b16 v[88:89], v25 offset:0\n\t" \
    "v_smfmac_f32_16x16x64_bf16 v[100:103], v[36:39], a[0:7], v10\n\t" \
    "ds_read_b64_tr_b16 v[90:91], v25 offset:64\n\t" \
    "s_waitcnt lgkmcnt(4)\n\t" \
    "v_smfmac_f32_16x16x64_bf16 v[124:127], v[84:87], v[240:247], v19\n\t" \
    "ds_read_b64_tr_b16 v[96:97], v25 offset:1152\n\t" \
    "ds_read_b64_tr_b16 v[98:99], v25 offset:1216\n\t" \
    "s_waitcnt lgkmcnt(4)\n\t" \
    "v_smfmac_f32_16x16x64_bf16 v[124:127], v[92:95], v[248:255], v19\n\t" \
    "s_nop 2\n\t" \
    "v_smfmac_f32_16x16x64_bf16 v[108:111], v[84:87], a[144:151], v19\n\t" \
    "s_nop 2\n\t" \
    "v_smfmac_f32_16x16x64_bf16 v[108:111], v[92:95], a[160:167], v19\n\t" \
    "s_nop 1\n\t" \
    "v_permlane32_swap_b32 v124, v126\n\t" \
    "v_smfmac_f32_16x16x64_bf16 v[112:115], v[84:87], a[176:183], v19\n\t" \
    "s_nop 1\n\t" \
    "v_permlane32_swap_b32 v125, v127\n\t" \
    "v_smfmac_f32_16x16x64_bf16 v[112:115], v[92:95], a[192:199], v19\n\t" \
    "s_nop 0\n\t" \
    "v_add_f32 v124, v124, v126\n\t" \
    "s_waitcnt lgkmcnt(2)\n\t" \
    "v_smfmac_f32_16x16x64_bf16 v[108:111], v[88:91], a[152:159], v19\n\t" \
    "s_nop 1\n\t" \
    "v_add_f32 v125, v125, v127\n\t" \
    "v_smfmac_f32_16x16x64_bf16 v[112:115], v[88:91], a[184:191], v19\n\t" \
    "s_nop 0\n\t" \
    "v_cvt_pk_bf16_f32 v136, v124, v125\n\t" \
    "s_waitcnt lgkmcnt(0)\n\t" \
    "v_smfmac_f32_16x16x64_bf16 v[108:111], v[96:99], a[168:175], v19\n\t" \
    "s_nop 1\n\t" \
    "v_cvt_pk_bf16_f32 v137, v124, v125\n\t" \
    "v_smfmac_f32_16x16x64_bf16 v[112:115], v[96:99], a[200:207], v19\n\t" \
    "s_nop 1\n\t" \
    "v_permlane32_swap_b32 v136, v137\n\t" \
    "v_smfmac_f32_16x16x64_bf16 v[116:119], v[84:87], a[208:215], v19\n\t" \
    "s_mov_b32 exec_hi, 0\n\t" \
    "global_store_dwordx2 v16, v[136:137], s[38:39] sc1\n\t" \
    "s_mov_b32 exec_hi, -1\n\t" \
    "v_cvt_pk_bf16_f32 v128, v108, v109\n\t" \
    "v_smfmac_f32_16x16x64_bf16 v[116:119], v[88:91], a[216:223], v19\n\t" \
    "s_nop 1\n\t" \
    "v_cvt_pk_bf16_f32 v129, v110, v111\n\t" \
    "v_smfmac_f32_16x16x64_bf16 v[116:119], v[92:95], a[224:231], v19\n\t" \
    "s_nop 1\n\t" \
    "v_cvt_pk_bf16_f32 v130, v112, v113\n\t" \
    "v_smfmac_f32_16x16x64_bf16 v[116:119], v[96:99], a[232:239], v19\n\t" \
    "s_nop 1\n\t" \
    "v_cvt_pk_bf16_f32 v131, v114, v115\n\t" \
    "v_smfmac_f32_16x16x64_bf16 v[120:123], v[84:87], a[240:247], v19\n\t" \
    "s_nop 1\n\t" \
    "v_permlane16_swap_b32 v128, v130\n\t" \
    "v_smfmac_f32_16x16x64_bf16 v[120:123], v[88:91], a[248:255], v19\n\t" \
    "s_nop 1\n\t" \
    "v_permlane16_swap_b32 v129, v131\n\t" \
    "v_smfmac_f32_16x16x64_bf16 v[120:123], v[92:95], v[224:231], v19\n\t" \
    "s_nop 0\n\t" \
    "global_store_dwordx4 v14, v[128:131], s[38:39] sc1\n\t" \
    "v_cvt_pk_bf16_f32 v132, v116, v117\n\t" \
    "v_smfmac_f32_16x16x64_bf16 v[120:123], v[96:99], v[232:239], v19\n\t" \
    "s_nop 1\n\t" \
    "v_cvt_pk_bf16_f32 v133, v118, v119\n\t" \
    "s_nop 4\n\t" \
    "v_cvt_pk_bf16_f32 v134, v120, v121\n\t" \
    "v_cvt_pk_bf16_f32 v135, v122, v123\n\t" \
    "s_nop 0\n\t" \
    "v_permlane16_swap_b32 v132, v134\n\t" \
    "v_permlane16_swap_b32 v133, v135\n\t" \
    "global_store_dwordx4 v15, v[132:135], s[38:39] sc1\n\t" \
    "global_load_dword v140, v20, s[52:53] sc1\n\t" \
    "s_cmp_eq_u32 s65, 63\n\t" \
    "s_cbranch_scc1 vv_end53_%=\n\t" \
    "s_cmp_lg_u32 s65, 0\n\t" \
    "s_cbranch_scc1 vv_ng62_%=\n\t" \
    "v_mov_b32 v160, 5838848\n\t" \
    "v_mov_b32 v161, 1\n\t" \
    "s_mov_b64 exec, 1\n\t" \
    "global_atomic_add v162, v160, v161, s[36:37] sc0 sc1\n\t" \
    "s_mov_b64 exec, -1\n\t" \
    "v_mbcnt_lo_u32_b32 v160, -1, 0\n\t" \
    "v_and_b32 v161, 3, v160\n\t" \
    "v_lshlrev_b32 v161, 16, v161\n\t" \
    "v_bfe_u32 v163, v160, 2, 2\n\t" \
    "v_lshl_add_u32 v161, v163, 11, v161\n\t" \
    "v_bfe_u32 v163, v160, 4, 1\n\t" \
    "v_lshl_add_u32 v161, v163, 14, v161\n\t" \
    "v_add_u32 v161, s63, v161\n\t" \
    "s_waitcnt vmcnt(0)\n\t" \
    "v_readfirstlane_b32 s69, v162\n\t" \
    "s_nop 1\n\t" \
    "s_cmp_lg_u32 s69, 126\n\t" \
    "s_cbranch_scc1 vv_go63_%=\n\t" \
    "s_memrealtime s[56:57]\n\t" \
    "s_waitcnt lgkmcnt(0)\n\t" \
    "s_lshl_b32 s69, s56, 4\n\t" \
    "s_add_u32 s69, s69, 2800\n\t" \
    "s_add_u32 s70, s56, 24\n\t" \
    "s_lshr_b32 s70, s70, 4\n\t" \
    "v_lshrrev_b32 v163, 2, v160\n\t" \
    "v_add_u32 v163, s70, v163\n\t" \
    "v_and_b32 v163, 63, v163\n\t" \
    "v_lshlrev_b32 v163, 6, v163\n\t" \
    "v_and_b32 v162, 3, v160\n\t" \
    "v_lshl_add_u32 v163, v162, 12, v163\n\t" \
    "v_add_u32 v163, 5865472, v163\n\t" \
    "v_mov_b32 v160, s69\n\t" \
    "s_mov_b32 exec_hi, 0\n\t" \
    "global_store_dword v163, v160, s[36:37] sc1\n\t" \
    "global_store_dword v161, v160, s[36:37] sc1\n\t" \
    "s_mov_b32 exec_hi, -1\n\t" \
    "vv_go63_%=:\n\t" \
    "vv_ng62_%=:\n\t" \
    "s_waitcnt lgkmcnt(0)\n\t" \
    "s_mov_b32 s70, 116\n\t" \
    "vv_E67_%=:\n\t" \
    "s_memtime s[56:57]\n\t" \
    "s_memrealtime s[2:3]\n\t" \
    "s_nop 0\n\t" \
    "s_memrealtime s[4:5]\n\t" \
    "s_nop 0\n\t" \
    "s_memrealtime s[6:7]\n\t" \
    "s_nop 0\n\t" \
    "s_memrealtime s[8:9]\n\t" \
    "s_nop 0\n\t" \
    "s_memrealtime s[10:11]\n\t" \
    "s_nop 0\n\t" \
    "s_memrealtime s[12:13]\n\t" \
    "s_nop 0\n\t" \
    "s_memrealtime s[14:15]\n\t" \
    "s_nop 0\n\t" \
    "s_memrealtime s[16:17]\n\t" \
    "s_nop 0\n\t" \
    "s_memrealtime s[18:19]\n\t" \
    "s_nop 0\n\t" \
    "s_memrealtime s[20:21]\n\t" \
    "s_nop 0\n\t" \
    "s_memrealtime s[22:23]\n\t" \
    "s_nop 0\n\t" \
    "s_memrealtime s[24:25]\n\t" \
    "s_nop 0\n\t" \
    "s_memrealtime s[26:27]\n\t" \
    "s_nop 0\n\t" \
    "s_memrealtime s[28:29]\n\t" \
    "s_memtime s[54:55]\n\t" \
    "s_add_u32 s70, s70, 8\n\t" \
    "s_waitcnt lgkmcnt(0)\n\t" \
    "s_sub_u32 s69, s54, s56\n\t" \
    "s_cmp_gt_u32 s69, s70\n\t" \
    "s_cbranch_scc1 vv_E67_%=\n\t" \
    "s_add_u32 s2, s2, s4\n\t" \
    "s_add_u32 s6, s6, s8\n\t" \
    "s_add_u32 s10, s10, s12\n\t" \
    "s_add_u32 s14, s14, s16\n\t" \
    "s_add_u32 s18, s18, s20\n\t" \
    "s_add_u32 s22, s22, s24\n\t" \
    "s_add_u32 s2, s2, s6\n\t" \
    "s_add_u32 s10, s10, s14\n\t" \
    "s_add_u32 s18, s18, s22\n\t" \
    "s_add_u32 s2, s2, s26\n\t" \
    "s_add_u32 s10, s10, s18\n\t" \
    "s_add_u32 s2, s2, s10\n\t" \
    "s_mul_i32 s70, s28, 13\n\t" \
    "s_sub_u32 s70, s70, s2\n\t" \
    "s_min_u32 s70, s70, 52\n\t" \
    "s_mul_i32 s69, s69, s70\n\t" \
    "s_mul_hi_u32 s69, s69, 76695844\n\t" \
    "s_mov_b32 s75, s73\n\t" \
    "s_mov_b32 s76, s74\n\t" \
    "s_add_u32 s73, s56, s69\n\t" \
    "s_lshl_b32 s74, s28, 4\n\t" \
    "s_sub_u32 s69, s73, s75\n\t" \
    "s_sub_u32 s70, s74, s76\n\t" \
    "s_sub_u32 s71, s70, 1600\n\t" \
    "s_cmp_lt_u32 s71, 400000\n\t" \
    "s_cbranch_scc0 vv_K68_%=\n\t" \
    "v_cvt_f32_u32 v160, s69\n\t" \
    "v_cvt_f32_u32 v161, s70\n\t" \
    "v_rcp_f32 v162, v160\n\t" \
    "v_rcp_f32 v163, v161\n\t" \
    "v_mul_f32 v162, v162, v161\n\t" \
    "v_mul_f32 v163, v163, v160\n\t" \
    "v_mul_f32 v162, 0x4f800000, v162\n\t" \
    "v_mul_f32 v163, 0x4f000000, v163\n\t" \
    "v_cvt_u32_f32 v162, v162\n\t" \
    "v_cvt_u32_f32 v163, v163\n\t" \
    "v_readfirstlane_b32 s69, v162\n\t" \
    "v_readfirstlane_b32 s70, v163\n\t" \
    "s_nop 1\n\t" \
    "s_min_u32 s71, s65, 1\n\t" \
    "s_sub_u32 s69, s69, s78\n\t" \
    "s_ashr_i32 s69, s69, s71\n\t" \
    "s_add_u32 s78, s78, s69\n\t" \
    "s_sub_u32 s70, s70, s77\n\t" \
    "s_ashr_i32 s70, s70, s71\n\t" \
    "s_add_u32 s77, s77, s70\n\t" \
    "vv_K68_%=:\n\t" \
    "s_add_u32 s65, s65, 1\n\t" \
    "v_mov_b32 v72, 0\n\t" \
    "v_mov_b32 v73, 0\n\t" \
    "v_mov_b32 v74, 0\n\t" \
    "v_mov_b32 v75, 0\n\t" \
    "v_mov_b32 v76, 0\n\t" \
    "v_mov_b32 v77, 0\n\t" \
    "v_mov_b32 v78, 0\n\t" \
    "v_mov_b32 v79, 0\n\t" \
    "v_mov_b32 v108, 0\n\t" \
    "v_mov_b32 v109, 0\n\t" \
    "v_mov_b32 v110, 0\n\t" \
    "v_mov_b32 v111, 0\n\t" \
    "v_mov_b32 v112, 0\n\t" \
    "v_mov_b32 v113, 0\n\t" \
    "v_mov_b32 v114, 0\n\t" \
    "v_mov_b32 v115, 0\n\t" \
    "v_mov_b32 v116, 0\n\t" \
    "v_mov_b32 v117, 0\n\t" \
    "v_mov_b32 v118, 0\n\t" \
    "v_mov_b32 v119, 0\n\t" \
    "v_mov_b32 v120, 0\n\t" \
    "v_mov_b32 v121, 0\n\t" \
    "v_mov_b32 v122, 0\n\t" \
    "v_mov_b32 v123, 0\n\t" \
    "v_mov_b32 v124, 0\n\t" \
    "v_mov_b32 v125, 0\n\t" \
    "v_mov_b32 v126, 0\n\t" \
    "v_mov_b32 v127, 0\n\t" \
    "s_add_u32 s69, s65, 0\n\t" \
    "s_and_b32 s69, s69, 7\n\t" \
    "s_lshl_b32 s69, s69, 18\n\t" \
    "s_add_u32 s69, s69, s62\n\t" \
    "s_add_u32 s46, s36, s69\n\t" \
    "s_addc_u32 s47, s37, 0\n\t" \
    "s_add_u32 s70, s65, 0\n\t" \
    "s_mul_i32 s69, s70, 171\n\t" \
    "s_lshr_b32 s69, s69, 9\n\t" \
    "s_mul_i32 s70, s70, 589824\n\t" \
    "s_mul_i32 s71, s69, 327680\n\t" \
    "s_add_u32 s70, s70, s71\n\t" \
    "s_lshl1_add_u32 s69, s69, 4\n\t" \
    "s_bitcmp1_b64 s[44:45], s69\n\t" \
    "s_cselect_b32 s69, 8192, 0\n\t" \
    "s_add_u32 s69, s69, s70\n\t" \
    "s_add_u32 s69, s69, 123731968\n\t" \
    "s_add_u32 s38, s36, s69\n\t" \
    "s_addc_u32 s39, s37, 0\n\t" \
    "s_and_b32 s69, s69, 0xffe00000\n\t" \
    "s_add_u32 s69, s69, 1823744\n\t" \
    "v_mov_b32 v17, s69\n\t" \
    "global_load_dword v17, v17, s[36:37]\n\t" \
    "s_waitcnt vmcnt(0)\n\t" \
    "v_cmp_eq_u32 vcc, -1, v140\n\t" \
    "s_cbranch_vccz vv_Hok65_%=\n\t" \
    "vv_H64_%=:\n\t" \
    "global_load_dword v140, v20, s[52:53] sc1\n\t" \
    "s_waitcnt vmcnt(0)\n\t" \
    "v_cmp_eq_u32 vcc, -1, v140\n\t" \
    "s_cbranch_vccz vv_Hc66_%=\n\t" \
    "s_cmp_eq_u32 s68, 0\n\t" \
    "s_cbranch_scc1 vv_F69_%=\n\t" \
    "s_sub_u32 s68, s68, 1\n\t" \
    "s_sleep 2\n\t" \
    "s_branch vv_H64_%=\n\t" \
    "vv_F69_%=:\n\t" \
    "v_mov_b32 v141, 5767424\n\t" \
    "v_mov_b32 v142, 6\n\t" \
    "global_store_dword v141, v142, s[36:37] sc1\n\t" \
    "s_waitcnt vmcnt(0)\n\t" \
    "s_branch vv_Hc66_%=\n\t" \
    "vv_Hc66_%=:\n\t" \
    "s_memtime s[56:57]\n\t" \
    "s_waitcnt lgkmcnt(0)\n\t" \
    "s_sub_u32 s69, s56, s73\n\t" \
    "s_cmp_lt_u32 s69, 3000\n\t" \
    "s_cbranch_scc1 vv_St70_%=\n\t" \
    "s_waitcnt lgkmcnt(0)\n\t" \
    "s_mov_b32 s70, 116\n\t" \
    "vv_E71_%=:\n\t" \
    "s_memtime s[56:57]\n\t" \
    "s_memrealtime s[2:3]\n\t" \
    "s_nop 0\n\t" \
    "s_memrealtime s[4:5]\n\t" \
    "s_nop 0\n\t" \
    "s_memrealtime s[6:7]\n\t" \
    "s_nop 0\n\t" \
    "s_memrealtime s[8:9]\n\t" \
    "s_nop 0\n\t" \
    "s_memrealtime s[10:11]\n\t" \
    "s_nop 0\n\t" \
    "s_memrealtime s[12:13]\n\t" \
    "s_nop 0\n\t" \
    "s_memrealtime s[14:15]\n\t" \
    "s_nop 0\n\t" \
    "s_memrealtime s[16:17]\n\t" \
    "s_nop 0\n\t" \
    "s_memrealtime s[18:19]\n\t" \
    "s_nop 0\n\t" \
    "s_memrealtime s[20:21]\n\t" \
    "s_nop 0\n\t" \
    "s_memrealtime s[22:23]\n\t" \
    "s_nop 0\n\t" \
    "s_memrealtime s[24:25]\n\t" \
    "s_nop 0\n\t" \
    "s_memrealtime s[26:27]\n\t" \
    "s_nop 0\n\t" \
    "s_memrealtime s[28:29]\n\t" \
    "s_memtime s[54:55]\n\t" \
    "s_add_u32 s70, s70, 8\n\t" \
    "s_waitcnt lgkmcnt(0)\n\t" \
    "s_sub_u32 s69, s54, s56\n\t" \
    "s_cmp_gt_u32 s69, s70\n\t" \
    "s_cbranch_scc1 vv_E71_%=\n\t" \
    "s_add_u32 s2, s2, s4\n\t" \
    "s_add_u32 s6, s6, s8\n\t" \
    "s_add_u32 s10, s10, s12\n\t" \
    "s_add_u32 s14, s14, s16\n\t" \
    "s_add_u32 s18, s18, s20\n\t" \
    "s_add_u32 s22, s22, s24\n\t" \
    "s_add_u32 s2, s2, s6\n\t" \
    "s_add_u32 s10, s10, s14\n\t" \
    "s_add_u32 s18, s18, s22\n\t" \
    "s_add_u32 s2, s2, s26\n\t" \
    "s_add_u32 s10, s10, s18\n\t" \
    "s_add_u32 s2, s2, s10\n\t" \
    "s_mul_i32 s70, s28, 13\n\t" \
    "s_sub_u32 s70, s70, s2\n\t" \
    "s_min_u32 s70, s70, 52\n\t" \
    "s_mul_i32 s69, s69, s70\n\t" \
    "s_mul_hi_u32 s69, s69, 76695844\n\t" \
    "s_mov_b32 s75, s73\n\t" \
    "s_mov_b32 s76, s74\n\t" \
    "s_add_u32 s73, s56, s69\n\t" \
    "s_lshl_b32 s74, s28, 4\n\t" \
    "vv_St70_%=:\n\t" \
    "vv_Hok65_%=:\n\t" \
    "v_max_u32_dpp v140, v140, v140 row_ror:1 row_mask:0xf bank_mask:0xf\n\t" \
    "s_nop 1\n\t" \
    "v_max_u32_dpp v140, v140, v140 row_ror:2 row_mask:0xf bank_mask:0xf\n\t" \
    "s_nop 1\n\t" \
    "v_max_u32_dpp v140, v140, v140 row_ror:4 row_mask:0xf bank_mask:0xf\n\t" \
    "s_nop 1\n\t" \
    "v_max_u32_dpp v140, v140, v140 row_ror:8 row_mask:0xf bank_mask:0xf\n\t" \
    "s_nop 0\n\t" \
    "v_readlane_b32 s69, v140, 0\n\t" \
    "v_readlane_b32 s71, v140, 16\n\t" \
    "s_nop 1\n\t" \
    "s_max_u32 s81, s69, s71\n\t" \
    "s_mul_hi_u32 s69, s85, s78\n\t" \
    "s_add_u32 s79, s81, s69\n\t" \
    "s_add_u32 s79, s79, s87\n\t" \
    "s_cmp_ge_u32 s65, 53\n\t" \
    "s_cselect_b32 s69, 32, 0\n\t" \
    "s_sub_u32 s79, s79, s69\n\t" \
    "s_cmp_lt_u32 s65, 10\n\t" \
    "s_cselect_b32 s69, 48, 0\n\t" \
    "s_add_u32 s79, s79, s69\n\t" \
    "s_add_u32 s80, s79, s88\n\t" \
    "s_sub_u32 s70, s79, s74\n\t" \
    "s_max_i32 s70, s70, 0\n\t" \
    "s_min_i32 s70, s70, 16000\n\t" \
    "s_lshl_b32 s70, s70, 1\n\t" \
    "s_mul_hi_u32 s70, s70, s77\n\t" \
    "s_add_u32 s70, s70, s73\n\t" \
    "v_mov_b32 v160, s70\n\t" \
    "ds_write_b32 v11, v160 offset:20480\n\t" \
    "s_and_b32 s69, s65, 7\n\t" \
    "s_lshl_b32 s69, s69, 18\n\t" \
    "s_add_u32 s69, s69, s63\n\t" \
    "s_add_u32 s52, s36, s69\n\t" \
    "s_addc_u32 s53, s37, 0\n\t" \
    "s_waitcnt lgkmcnt(0)\n\t" \
    "s_barrier\n\t" \
    "s_branch vv_loop12_%=\n\t" \
    "vv_Gbad58_%=:\n\t" \
    "s_waitcnt lgkmcnt(0)\n\t" \
    "s_nop 7\n\t" \
    "v_mov_b32 v72, 0\n\t" \
    "v_mov_b32 v73, 0\n\t" \
    "v_mov_b32 v74, 0\n\t" \
    "v_mov_b32 v75, 0\n\t" \
    "v_mov_b32 v76, 0\n\t" \
    "v_mov_b32 v77, 0\n\t" \
    "v_mov_b32 v78, 0\n\t" \
    "v_mov_b32 v79, 0\n\t" \
    "s_cmp_eq_u32 s68, 0\n\t" \
    "s_cbranch_scc1 vv_F72_%=\n\t" \
    "s_sub_u32 s68, s68, 1\n\t" \
    "s_branch vv_G57_%=\n\t" \
    "vv_F72_%=:\n\t" \
    "v_mov_b32 v140, 5767424\n\t" \
    "v_mov_b32 v141, 1\n\t" \
    "global_store_dword v140, v141, s[36:37] sc1\n\t" \
    "s_waitcnt vmcnt(0)\n\t" \
    "s_branch vv_Gc59_%=\n\t" \
    "vv_Gslow60_%=:\n\t" \
    "v_max_u32 v140, v36, v37\n\t" \
    "v_max3_u32 v140, v140, v38, v39\n\t" \
    "v_max3_u32 v140, v140, v40, v41\n\t" \
    "v_max3_u32 v140, v140, v42, v43\n\t" \
    "v_max3_u32 v140, v140, v44, v45\n\t" \
    "v_max3_u32 v140, v140, v46, v47\n\t" \
    "v_max3_u32 v140, v140, v48, v49\n\t" \
    "v_max3_u32 v140, v140, v50, v51\n\t" \
    "v_max3_u32 v140, v140, v52, v53\n\t" \
    "v_max3_u32 v140, v140, v54, v55\n\t" \
    "v_max3_u32 v140, v140, v56, v57\n\t" \
    "v_max3_u32 v140, v140, v58, v59\n\t" \
    "v_max3_u32 v140, v140, v60, v61\n\t" \
    "v_max3_u32 v140, v140, v62, v63\n\t" \
    "v_max3_u32 v140, v140, v64, v65\n\t" \
    "v_max3_u32 v140, v140, v66, v67\n\t" \
    "v_max3_u32 v140, v140, v68, v69\n\t" \
    "v_max3_u32 v140, v140, v70, v71\n\t" \
    "v_cmp_eq_u32 vcc, -1, v140\n\t" \
    "s_cbranch_vccz vv_Gback61_%=\n\t" \
    "s_waitcnt lgkmcnt(0)\n\t" \
    "v_sub_u32 v140, v1, v3\n\t" \
    "s_nop 0\n\t" \
    "v_readfirstlane_b32 s69, v140\n\t" \
    "v_add_u32 v143, 20480, v0\n\t" \
    "s_nop 7\n\t" \
    "s_add_u32 s30, s46, s69\n\t" \
    "s_addc_u32 s31, s47, 0\n\t" \
    "vv_Gag73_%=:\n\t" \
    "global_load_dwordx4 v[36:39], v1, s[46:47] sc1\n\t" \
    "global_load_dwordx4 v[40:43], v23, s[46:47] sc1\n\t" \
    "global_load_dwordx4 v[44:47], v143, s[30:31] sc1\n\t" \
    "global_load_dwordx4 v[48:51], v4, s[30:31] sc1\n\t" \
    "global_load_dwordx4 v[52:55], v5, s[30:31] sc1\n\t" \
    "global_load_dwordx4 v[56:59], v6, s[30:31] sc1\n\t" \
    "global_load_dwordx4 v[60:63], v7, s[30:31] sc1\n\t" \
    "global_load_dwordx4 v[64:67], v8, s[30:31] sc1\n\t" \
    "global_load_dwordx4 v[68:71], v9, s[30:31] sc1\n\t" \
    "s_waitcnt vmcnt(0)\n\t" \
    "v_max_u32 v140, v36, v37\n\t" \
    "v_max3_u32 v140, v140, v38, v39\n\t" \
    "v_max3_u32 v140, v140, v40, v41\n\t" \
    "v_max3_u32 v140, v140, v42, v43\n\t" \
    "v_max3_u32 v140, v140, v44, v45\n\t" \
    "v_max3_u32 v140, v140, v46, v47\n\t" \
    "v_max3_u32 v140, v140, v48, v49\n\t" \
    "v_max3_u32 v140, v140, v50, v51\n\t" \
    "v_max3_u32 v140, v140, v52, v53\n\t" \
    "v_max3_u32 v140, v140, v54, v55\n\t" \
    "v_max3_u32 v140, v140, v56, v57\n\t" \
    "v_max3_u32 v140, v140, v58, v59\n\t" \
    "v_max3_u32 v140, v140, v60, v61\n\t" \
    "v_max3_u32 v140, v140, v62, v63\n\t" \
    "v_max3_u32 v140, v140, v64, v65\n\t" \
    "v_max3_u32 v140, v140, v66, v67\n\t" \
    "v_max3_u32 v140, v140, v68, v69\n\t" \
    "v_max3_u32 v140, v140, v70, v71\n\t" \
    "v_cmp_eq_u32 vcc, -1, v140\n\t" \
    "s_cbranch_vccz vv_Gredo74_%=\n\t" \
    "s_cmp_eq_u32 s68, 0\n\t" \
    "s_cbranch_scc1 vv_F75_%=\n\t" \
    "s_sub_u32 s68, s68, 1\n\t" \
    "s_sleep 1\n\t" \
    "s_branch vv_Gag73_%=\n\t" \
    "vv_F75_%=:\n\t" \
    "v_mov_b32 v140, 5767424\n\t" \
    "v_mov_b32 v141, 2\n\t" \
    "global_store_dword v140, v141, s[36:37] sc1\n\t" \
    "s_waitcnt vmcnt(0)\n\t" \
    "s_branch vv_Gredo74_%=\n\t" \
    "vv_Gredo74_%=:\n\t" \
    "v_mov_b32 v72, 0\n\t" \
    "v_mov_b32 v73, 0\n\t" \
    "v_mov_b32 v74, 0\n\t" \
    "v_mov_b32 v75, 0\n\t" \
    "v_mov_b32 v76, 0\n\t" \
    "v_mov_b32 v77, 0\n\t" \
    "v_mov_b32 v78, 0\n\t" \
    "v_mov_b32 v79, 0\n\t" \
    "v_smfmac_f32_16x16x64_bf16 v[72:75], v[36:39], a[48:55], v10\n\t" \
    "v_smfmac_f32_16x16x64_bf16 v[72:75], v[40:43], a[56:63], v10\n\t" \
    "v_smfmac_f32_16x16x64_bf16 v[72:75], v[48:51], a[0:7], v10\n\t" \
    "v_smfmac_f32_16x16x64_bf16 v[72:75], v[52:55], a[8:15], v10\n\t" \
    "v_smfmac_f32_16x16x64_bf16 v[72:75], v[56:59], a[16:23], v10\n\t" \
    "v_smfmac_f32_16x16x64_bf16 v[72:75], v[60:63], a[24:31], v10\n\t" \
    "v_smfmac_f32_16x16x64_bf16 v[72:75], v[64:67], a[32:39], v10\n\t" \
    "v_smfmac_f32_16x16x64_bf16 v[72:75], v[68:71], a[40:47], v10\n\t" \
    "v_smfmac_f32_16x16x64_bf16 v[72:75], v[44:47], a[64:71], v10\n\t" \
    "v_smfmac_f32_16x16x64_bf16 v[76:79], v[36:39], a[120:127], v10\n\t" \
    "v_smfmac_f32_16x16x64_bf16 v[76:79], v[40:43], a[128:135], v10\n\t" \
    "v_smfmac_f32_16x16x64_bf16 v[76:79], v[48:51], a[72:79], v10\n\t" \
    "v_smfmac_f32_16x16x64_bf16 v[76:79], v[52:55], a[80:87], v10\n\t" \
    "v_smfmac_f32_16x16x64_bf16 v[76:79], v[56:59], a[88:95], v10\n\t" \
    "v_smfmac_f32_16x16x64_bf16 v[76:79], v[60:63], a[96:103], v10\n\t" \
    "v_smfmac_f32_16x16x64_bf16 v[76:79], v[64:67], a[104:111], v10\n\t" \
    "v_smfmac_f32_16x16x64_bf16 v[76:79], v[68:71], a[112:119], v10\n\t" \
    "v_smfmac_f32_16x16x64_bf16 v[76:79], v[44:47], a[136:143], v10\n\t" \
    "v_add_f32 v72, v72, v74\n\t" \
    "v_add_f32 v73, v73, v75\n\t" \
    "v_cvt_pk_bf16_f32 v80, v72, v73\n\t" \
    "ds_write_b32 v12, v80 offset:0\n\t" \
    "s_nop 3\n\t" \
    "v_pk_add_f32 v[76:77], v[76:77], v[78:79]\n\t" \
    "v_cvt_pk_bf16_f32 v81, v76, v77\n\t" \
    "ds_write_b32 v12, v81 offset:256\n\t" \
    "s_branch vv_Gback61_%=\n\t" \
    "vv_end53_%=:\n\t" \
    "s_endpgm\n\t" \
    "vv_last_%=:\n\t" \
    ""
#define ASM_R \
    "s_mov_b64 s[36:37], %0\n\t" \
    "s_mov_b64 s[42:43], %1\n\t" \
    "s_mov_b64 s[44:45], %2\n\t" \
    "s_mov_b32 s66, %3\n\t" \
    "s_mov_b32 s67, %4\n\t" \
    "s_mov_b64 s[50:51], %5\n\t" \
    "s_mov_b32 s82, %7\n\t" \
    "s_load_dwordx8 s[84:91], %6, 0\n\t" \
    "v_mbcnt_lo_u32_b32 v223, -1, 0\n\t" \
    "v_mbcnt_hi_u32_b32 v223, -1, v223\n\t" \
    "s_getpc_b64 s[46:47]\n\t" \
    "rr_pc_%=:\n\t" \
    "s_lshl_b32 s69, s66, 13\n\t" \
    "v_lshl_add_u32 v222, v223, 7, s69\n\t" \
    "v_min_u32 v222, rr_last_%=-rr_pc_%=-4, v222\n\t" \
    "global_load_dword v222, v222, s[46:47]\n\t" \
    "v_lshlrev_b32 v223, 2, v223\n\t" \
    "v_add_u32 v223, s82, v223\n\t" \
    "ds_read_b32 v0, v223 offset:0\n\t" \
    "ds_read_b32 v1, v223 offset:256\n\t" \
    "ds_read_b32 v2, v223 offset:512\n\t" \
    "ds_read_b32 v3, v223 offset:768\n\t" \
    "ds_read_b32 v4, v223 offset:1024\n\t" \
    "ds_read_b32 v5, v223 offset:1280\n\t" \
    "ds_read_b32 v6, v223 offset:1536\n\t" \
    "ds_read_b32 v7, v223 offset:1792\n\t" \
    "ds_read_b32 v8, v223 offset:2048\n\t" \
    "ds_read_b32 v9, v223 offset:2304\n\t" \
    "ds_read_b32 v10, v223 offset:2560\n\t" \
    "ds_read_b32 v11, v223 offset:2816\n\t" \
    "s_waitcnt lgkmcnt(0)\n\t" \
    "ds_read_b32 v12, v223 offset:3072\n\t" \
    "ds_read_b32 v13, v223 offset:3328\n\t" \
    "ds_read_b32 v14, v223 offset:3584\n\t" \
    "ds_read_b32 v15, v223 offset:3840\n\t" \
    "ds_read_b32 v16, v223 offset:4096\n\t" \
    "ds_read_b32 v17, v223 offset:4352\n\t" \
    "ds_read_b32 v18, v223 offset:4608\n\t" \
    "ds_read_b32 v19, v223 offset:4864\n\t" \
    "ds_read_b32 v20, v223 offset:5120\n\t" \
    "ds_read_b32 v21, v223 offset:5376\n\t" \
    "ds_read_b32 v22, v223 offset:5632\n\t" \
    "ds_read_b32 v23, v223 offset:5888\n\t" \
    "s_waitcnt lgkmcnt(0)\n\t" \
    "ds_read_b32 v24, v223 offset:6144\n\t" \
    "ds_read_b32 v25, v223 offset:6400\n\t" \
    "ds_read_b32 v26, v223 offset:6656\n\t" \
    "ds_read_b32 v27, v223 offset:6912\n\t" \
    "ds_read_b32 v28, v223 offset:7168\n\t" \
    "ds_read_b32 v29, v223 offset:7424\n\t" \
    "ds_read_b32 v30, v223 offset:7680\n\t" \
    "ds_read_b32 v31, v223 offset:7936\n\t" \
    "ds_read_b32 v32, v223 offset:8192\n\t" \
    "ds_read_b32 v33, v223 offset:8448\n\t" \
    "ds_read_b32 v34, v223 offset:8704\n\t" \
    "ds_read_b32 v35, v223 offset:8960\n\t" \
    "s_waitcnt lgkmcnt(0)\n\t" \
    "ds_read_b32 v36, v223 offset:9216\n\t" \
    "ds_read_b32 v37, v223 offset:9472\n\t" \
    "ds_read_b32 v38, v223 offset:9728\n\t" \
    "ds_read_b32 v39, v223 offset:9984\n\t" \
    "ds_read_b32 v40, v223 offset:10240\n\t" \
    "ds_read_b32 v41, v223 offset:10496\n\t" \
    "ds_read_b32 v42, v223 offset:10752\n\t" \
    "ds_read_b32 v43, v223 offset:11008\n\t" \
    "ds_read_b32 v44, v223 offset:11264\n\t" \
    "ds_read_b32 v45, v223 offset:11520\n\t" \
    "ds_read_b32 v184, v223 offset:11776\n\t" \
    "ds_read_b32 v185, v223 offset:12032\n\t" \
    "s_waitcnt lgkmcnt(0)\n\t" \
    "ds_read_b32 v186, v223 offset:12288\n\t" \
    "ds_read_b32 v187, v223 offset:12544\n\t" \
    "ds_read_b32 v188, v223 offset:12800\n\t" \
    "s_waitcnt lgkmcnt(0)\n\t" \
    "s_mov_b32 s68, 262144\n\t" \
    "s_mov_b32 s72, 0\n\t" \
    "s_mov_b32 s83, -1\n\t" \
    "s_mov_b32 s65, 0\n\t" \
    "v_xor_b32 v180, 65536, v2\n\t" \
    "v_xor_b32 v181, 131072, v2\n\t" \
    "v_xor_b32 v182, 196608, v2\n\t" \
    "v_add_u32 v189, 4096, v7\n\t" \
    "v_add_u32 v190, 16384, v7\n\t" \
    "v_add_u32 v191, 20480, v7\n\t" \
    "s_mul_i32 s69, s67, 57\n\t" \
    "s_lshr_b32 s69, s69, 9\n\t" \
    "s_mul_i32 s69, s69, 9\n\t" \
    "s_sub_u32 s69, s67, s69\n\t" \
    "s_cmp_lt_u32 s69, 4\n\t" \
    "s_cselect_b32 s70, 21, 16\n\t" \
    "s_cmp_eq_u32 s69, 8\n\t" \
    "s_cselect_b32 s70, 26, s70\n\t" \
    "s_lshr_b32 s97, s91, s70\n\t" \
    "s_and_b32 s97, s97, 31\n\t" \
    "s_min_u32 s97, s97, 0\n\t" \
    "s_lshl_b32 s97, s97, 4\n\t" \
    "s_mov_b32 s77, 3019898880\n\t" \
    "s_mov_b32 s78, 3054198966\n\t" \
    "s_waitcnt lgkmcnt(0)\n\t" \
    "s_mov_b32 s70, 116\n\t" \
    "rr_E1_%=:\n\t" \
    "s_memtime s[56:57]\n\t" \
    "s_memrealtime s[2:3]\n\t" \
    "s_nop 0\n\t" \
    "s_memrealtime s[4:5]\n\t" \
    "s_nop 0\n\t" \
    "s_memrealtime s[6:7]\n\t" \
    "s_nop 0\n\t" \
    "s_memrealtime s[8:9]\n\t" \
    "s_nop 0\n\t" \
    "s_memrealtime s[10:11]\n\t" \
    "s_nop 0\n\t" \
    "s_memrealtime s[12:13]\n\t" \
    "s_nop 0\n\t" \
    "s_memrealtime s[14:15]\n\t" \
    "s_nop 0\n\t" \
    "s_memrealtime s[16:17]\n\t" \
    "s_nop 0\n\t" \
    "s_memrealtime s[18:19]\n\t" \
    "s_nop 0\n\t" \
    "s_memrealtime s[20:21]\n\t" \
    "s_nop 0\n\t" \
    "s_memrealtime s[22:23]\n\t" \
    "s_nop 0\n\t" \
    "s_memrealtime s[24:25]\n\t" \
    "s_nop 0\n\t" \
    "s_memrealtime s[26:27]\n\t" \
    "s_nop 0\n\t" \
    "s_memrealtime s[28:29]\n\t" \
    "s_memtime s[54:55]\n\t" \
    "s_add_u32 s70, s70, 8\n\t" \
    "s_waitcnt lgkmcnt(0)\n\t" \
    "s_sub_u32 s69, s54, s56\n\t" \
    "s_cmp_gt_u32 s69, s70\n\t" \
    "s_cbranch_scc1 rr_E1_%=\n\t" \
    "s_add_u32 s2, s2, s4\n\t" \
    "s_add_u32 s6, s6, s8\n\t" \
    "s_add_u32 s10, s10, s12\n\t" \
    "s_add_u32 s14, s14, s16\n\t" \
    "s_add_u32 s18, s18, s20\n\t" \
    "s_add_u32 s22, s22, s24\n\t" \
    "s_add_u32 s2, s2, s6\n\t" \
    "s_add_u32 s10, s10, s14\n\t" \
    "s_add_u32 s18, s18, s22\n\t" \
    "s_add_u32 s2, s2, s26\n\t" \
    "s_add_u32 s10, s10, s18\n\t" \
    "s_add_u32 s2, s2, s10\n\t" \
    "s_mul_i32 s70, s28, 13\n\t" \
    "s_sub_u32 s70, s70, s2\n\t" \
    "s_min_u32 s70, s70, 52\n\t" \
    "s_mul_i32 s69, s69, s70\n\t" \
    "s_mul_hi_u32 s69, s69, 76695844\n\t" \
    "s_mov_b32 s75, s73\n\t" \
    "s_mov_b32 s76, s74\n\t" \
    "s_add_u32 s73, s56, s69\n\t" \
    "s_lshl_b32 s74, s28, 4\n\t" \
    "v_mov_b32 v172, 0xd800\n\t" \
    "v_mov_b32 v173, 0x20d800\n\t" \
    "global_load_dword v172, v172, s[36:37]\n\t" \
    "global_load_dword v173, v173, s[36:37]\n\t" \
    "s_mov_b32 s62, 0\n\t" \
    "s_mov_b32 s63, 0x200000\n\t" \
    "s_mov_b64 s[44:45], 0\n\t" \
    "v_mov_b32 v170, 0\n\t" \
    "s_mov_b32 s65, s66\n\t" \
    "s_lshl_b32 s58, s67, 14\n\t" \
    "s_sub_u32 s80, s74, 32000\n\t" \
    "s_mov_b32 s64, 1\n\t" \
    "s_mov_b32 s65, 5\n\t" \
    "s_mov_b32 s83, 99\n\t" \
    "s_add_u32 s70, s65, 59\n\t" \
    "s_mul_i32 s69, s70, 171\n\t" \
    "s_lshr_b32 s69, s69, 9\n\t" \
    "s_mul_i32 s70, s70, 589824\n\t" \
    "s_mul_i32 s71, s69, 327680\n\t" \
    "s_add_u32 s70, s70, s71\n\t" \
    "s_lshl1_add_u32 s69, s69, 4\n\t" \
    "s_bitcmp1_b64 s[44:45], s69\n\t" \
    "s_cselect_b32 s69, 8192, 0\n\t" \
    "s_add_u32 s69, s69, s70\n\t" \
    "s_add_u32 s69, s69, 123731968\n\t" \
    "s_add_u32 s69, s69, s58\n\t" \
    "s_add_u32 s40, s36, s69\n\t" \
    "s_addc_u32 s41, s37, 0\n\t" \
    "v_mov_b32 v192, 0\n\t" \
    "s_mov_b64 exec, 1\n\t" \
    "s_branch rr_act3_%=\n\t" \
    "rr_g214_%=:\n\t" \
    "s_mov_b64 exec, -1\n\t" \
    "s_waitcnt vmcnt(0)\n\t" \
    "v_mbcnt_lo_u32_b32 v164, -1, 0\n\t" \
    "v_mbcnt_hi_u32_b32 v164, -1, v164\n\t" \
    "v_lshlrev_b32 v164, 6, v164\n\t" \
    "v_add_u32 v164, 6225920, v164\n\t" \
    "v_mov_b32 v165, 6234112\n\t" \
    "global_load_dword v165, v165, s[36:37] sc1\n\t" \
    "global_load_dwordx2 v[166:167], v164, s[36:37] sc1\n\t" \
    "global_load_dword v168, v164, s[36:37] offset:2048 sc1\n\t" \
    "s_waitcnt vmcnt(0)\n\t" \
    "rr_C15_%=:\n\t" \
    "v_cmp_ne_u32 s[54:55], v166, v168\n\t" \
    "v_sub_u32 v166, v166, v165\n\t" \
    "v_cmp_ne_u32 vcc, 1, v166\n\t" \
    "s_nop 1\n\t" \
    "s_or_b64 vcc, vcc, s[54:55]\n\t" \
    "s_cbranch_scc0 rr_Cok16_%=\n\t" \
    "s_cmp_eq_u32 s68, 0\n\t" \
    "s_cbranch_scc1 rr_F18_%=\n\t" \
    "s_sub_u32 s68, s68, 1\n\t" \
    "s_sleep 6\n\t" \
    "s_branch rr_Cag17_%=\n\t" \
    "rr_F18_%=:\n\t" \
    "v_mov_b32 v168, 5767424\n\t" \
    "v_mov_b32 v169, 4\n\t" \
    "global_store_dword v168, v169, s[36:37] sc1\n\t" \
    "s_waitcnt vmcnt(0)\n\t" \
    "s_branch rr_Cok16_%=\n\t" \
    "rr_Cag17_%=:\n\t" \
    "global_load_dwordx2 v[166:167], v164, s[36:37] sc1\n\t" \
    "global_load_dword v168, v164, s[36:37] offset:2048 sc1\n\t" \
    "s_waitcnt vmcnt(0)\n\t" \
    "s_branch rr_C15_%=\n\t" \
    "rr_Cok16_%=:\n\t" \
    "s_cmp_lg_u32 s66, 0\n\t" \
    "s_cbranch_scc1 rr_Ca19_%=\n\t" \
    "v_mov_b32 v164, 5836800\n\t" \
    "s_mov_b64 exec, 1\n\t" \
    "global_atomic_add v164, v166, s[36:37] sc1\n\t" \
    "s_mov_b64 exec, -1\n\t" \
    "rr_Ca19_%=:\n\t" \
    "v_mov_b32_dpp v164, v167 quad_perm:[1,0,3,2] row_mask:0xf bank_mask:0xf\n\t" \
    "v_cmp_lt_u32 s[44:45], v164, v167\n\t" \
    "s_nop 1\n\t" \
    "s_bitcmp1_b32 s44, 0\n\t" \
    "s_cselect_b32 s62, 8192, 0\n\t" \
    "s_bitcmp1_b32 s44, 2\n\t" \
    "s_cselect_b32 s63, 8192, 0\n\t" \
    "s_or_b32 s63, s63, 0x200000\n\t" \
    "v_min_u32 v164, v164, v167\n\t" \
    "v_cmp_lt_u32 vcc, 660, v164\n\t" \
    "s_nop 1\n\t" \
    "s_bcnt1_i32_b64 s69, vcc\n\t" \
    "s_cmp_gt_u32 s69, 16\n\t" \
    "s_cselect_b32 s69, 1, 0\n\t" \
    "s_mul_i32 s70, s69, 160\n\t" \
    "s_add_u32 s86, s86, s70\n\t" \
    "s_mul_i32 s70, s69, 536\n\t" \
    "s_add_u32 s87, s87, s70\n\t" \
    "s_mul_i32 s70, s69, 256\n\t" \
    "s_add_u32 s88, s88, s70\n\t" \
    "v_mov_b32 v172, -1\n\t" \
    "v_mov_b32 v173, -1\n\t" \
    "v_mov_b32 v174, -1\n\t" \
    "v_mov_b32 v175, -1\n\t" \
    "v_mov_b32 v176, 32000\n\t" \
    "s_sub_u32 s69, s63, s62\n\t" \
    "s_sub_u32 s69, s69, 0x200000\n\t" \
    "v_bfe_u32 v164, v187, 21, 1\n\t" \
    "v_mad_i32_i24 v187, v164, s69, v187\n\t" \
    "v_bfe_u32 v164, v188, 21, 1\n\t" \
    "v_mad_i32_i24 v188, v164, s69, v188\n\t" \
    "s_mov_b32 s64, 0\n\t" \
    "s_mov_b32 s65, s66\n\t" \
    "s_mov_b32 s83, 1\n\t" \
    "s_mov_b32 s77, 3154116608\n\t" \
    "rr_pf9_%=:\n\t" \
    "s_cmp_gt_u32 s65, 7\n\t" \
    "s_cselect_b32 s83, 99, s83\n\t" \
    "rr_WF20_%=:\n\t" \
    "s_cmp_ge_i32 s83, s65\n\t" \
    "s_cbranch_scc1 rr_WFok21_%=\n\t" \
    "global_load_dword v164, v6, s[36:37] sc1\n\t" \
    "s_waitcnt vmcnt(0)\n\t" \
    "v_min_i32_dpp v164, v164, v164 row_ror:1 row_mask:0xf bank_mask:0xf\n\t" \
    "s_nop 1\n\t" \
    "v_min_i32_dpp v164, v164, v164 row_ror:2 row_mask:0xf bank_mask:0xf\n\t" \
    "s_nop 1\n\t" \
    "v_min_i32_dpp v164, v164, v164 row_ror:4 row_mask:0xf bank_mask:0xf\n\t" \
    "s_nop 1\n\t" \
    "v_min_i32_dpp v164, v164, v164 row_ror:8 row_mask:0xf bank_mask:0xf\n\t" \
    "s_nop 0\n\t" \
    "v_readlane_b32 s69, v164, 0\n\t" \
    "v_readlane_b32 s70, v164, 16\n\t" \
    "s_nop 1\n\t" \
    "s_min_i32 s83, s69, s70\n\t" \
    "s_cmp_ge_i32 s83, s65\n\t" \
    "s_cbranch_scc1 rr_WFok21_%=\n\t" \
    "s_cmp_eq_u32 s68, 0\n\t" \
    "s_cbranch_scc1 rr_F22_%=\n\t" \
    "s_sub_u32 s68, s68, 1\n\t" \
    "s_sleep 2\n\t" \
    "s_branch rr_WF20_%=\n\t" \
    "rr_F22_%=:\n\t" \
    "v_mov_b32 v164, 5767424\n\t" \
    "v_mov_b32 v165, 10\n\t" \
    "global_store_dword v164, v165, s[36:37] sc1\n\t" \
    "s_waitcnt vmcnt(0)\n\t" \
    "s_branch rr_WFok21_%=\n\t" \
    "rr_WFok21_%=:\n\t" \
    "s_mul_i32 s70, s67, 7\n\t" \
    "s_add_u32 s69, s65, 0\n\t" \
    "s_xor_b32 s69, s69, s70\n\t" \
    "s_and_b32 s69, s69, 63\n\t" \
    "s_lshl_b32 s69, s69, 15\n\t" \
    "s_mul_i32 s70, s67, 2097152\n\t" \
    "s_add_u32 s69, s69, s70\n\t" \
    "s_add_u32 s69, s69, 48234496\n\t" \
    "s_add_u32 s38, s36, s69\n\t" \
    "s_addc_u32 s39, s37, 0\n\t" \
    "s_add_u32 s70, s65, 0\n\t" \
    "s_mul_i32 s69, s70, 171\n\t" \
    "s_lshr_b32 s69, s69, 9\n\t" \
    "s_mul_i32 s70, s70, 589824\n\t" \
    "s_mul_i32 s71, s69, 327680\n\t" \
    "s_add_u32 s70, s70, s71\n\t" \
    "s_lshl1_add_u32 s69, s69, 4\n\t" \
    "s_bitcmp1_b64 s[44:45], s69\n\t" \
    "s_cselect_b32 s69, 8192, 0\n\t" \
    "s_add_u32 s69, s69, s70\n\t" \
    "s_add_u32 s69, s69, 123731968\n\t" \
    "s_add_u32 s69, s69, s58\n\t" \
    "s_add_u32 s40, s36, s69\n\t" \
    "s_addc_u32 s41, s37, 0\n\t" \
    "s_and_b32 s69, s69, 0xffe00000\n\t" \
    "s_add_u32 s69, s69, 1823744\n\t" \
    "v_mov_b32 v168, s69\n\t" \
    "global_load_dword v168, v168, s[36:37]\n\t" \
    "v_mov_b32 v164, s83\n\t" \
    "s_add_u32 s69, s65, 2\n\t" \
    "s_cmp_ge_i32 s83, s69\n\t" \
    "s_cbranch_scc1 rr_nq23_%=\n\t" \
    "global_load_dword v164, v6, s[36:37] sc1\n\t" \
    "rr_nq23_%=:\n\t" \
    "s_cmp_gt_u32 s65, 61\n\t" \
    "s_cbranch_scc1 rr_idle2_%=\n\t" \
    "s_add_u32 s69, s65, 2\n\t" \
    "s_and_b32 s69, s69, 7\n\t" \
    "s_lshl_b32 s69, s69, 18\n\t" \
    "s_add_u32 s69, s69, s62\n\t" \
    "s_add_u32 s46, s36, s69\n\t" \
    "s_addc_u32 s47, s37, 0\n\t" \
    "global_store_dwordx4 v184, v[172:175], s[46:47] sc1\n\t" \
    "global_store_dwordx4 v185, v[172:175], s[46:47] sc1\n\t" \
    "global_store_dwordx4 v186, v[172:175], s[46:47] sc1\n\t" \
    "global_store_dwordx4 v187, v[172:175], s[46:47] sc1\n\t" \
    "global_store_dwordx4 v188, v[172:175], s[46:47] sc1\n\t" \
    "rr_idle2_%=:\n\t" \
    "v_mov_b32 v200, 0\n\t" \
    "s_cmp_lg_u32 s67, 35\n\t" \
    "s_cbranch_scc1 rr_cke24_%=\n\t" \
    "s_cmp_lt_u32 s65, 6\n\t" \
    "s_cbranch_scc1 rr_cke24_%=\n\t" \
    "s_lshl_b32 s69, s65, 7\n\t" \
    "s_add_u32 s69, s69, 5853184\n\t" \
    "v_mov_b32 v201, s69\n\t" \
    "global_load_dword v200, v201, s[36:37] sc1\n\t" \
    "rr_cke24_%=:\n\t" \
    "s_cmp_gt_u32 s65, 62\n\t" \
    "s_cbranch_scc1 rr_n225_%=\n\t" \
    "s_cmp_lt_u32 s65, 2\n\t" \
    "s_cbranch_scc1 rr_nw226_%=\n\t" \
    "s_mul_hi_u32 s69, s85, s78\n\t" \
    "s_add_u32 s79, s81, s69\n\t" \
    "s_add_u32 s79, s79, s87\n\t" \
    "s_cmp_ge_u32 s65, 54\n\t" \
    "s_cselect_b32 s69, 32, 0\n\t" \
    "s_sub_u32 s79, s79, s69\n\t" \
    "s_cmp_lt_u32 s65, 11\n\t" \
    "s_cselect_b32 s69, 48, 0\n\t" \
    "s_add_u32 s79, s79, s69\n\t" \
    "s_add_u32 s80, s79, s88\n\t" \
    "s_cmp_gt_u32 s65, 8\n\t" \
    "s_cselect_b32 s71, 240, 64\n\t" \
    "s_add_u32 s71, s71, s79\n\t" \
    "s_sub_u32 s70, s71, s74\n\t" \
    "s_max_i32 s70, s70, 0\n\t" \
    "s_min_i32 s70, s70, 16000\n\t" \
    "s_lshl_b32 s70, s70, 1\n\t" \
    "s_mul_hi_u32 s70, s70, s77\n\t" \
    "s_add_u32 s70, s70, s73\n\t" \
    "rr_W27_%=:\n\t" \
    "s_memtime s[56:57]\n\t" \
    "s_waitcnt lgkmcnt(0)\n\t" \
    "s_sub_u32 s69, s70, s56\n\t" \
    "s_cmp_gt_i32 s69, 24\n\t" \
    "s_cbranch_scc1 rr_W27_%=\n\t" \
    "s_branch rr_g113_%=\n\t" \
    "rr_nw226_%=:\n\t" \
    "s_mov_b32 s96, 0xaaaaaaaa\n\t" \
    "v_mov_b32 v166, -1\n\t" \
    "v_readfirstlane_b32 s71, v7\n\t" \
    "s_nop 1\n\t" \
    "s_lshr_b32 s71, s71, 4\n\t" \
    "s_add_u32 s71, s71, 5865472\n\t" \
    "rr_g012_%=:\n\t" \
    "s_memrealtime s[54:55]\n\t" \
    "s_waitcnt lgkmcnt(0)\n\t" \
    "s_bfe_u32 s69, s54, 0x60004\n\t" \
    "s_lshl_b32 s69, s69, 6\n\t" \
    "s_add_u32 s69, s69, s71\n\t" \
    "v_mov_b32 v165, s69\n\t" \
    "global_load_dword v169, v165, s[36:37] sc1\n\t" \
    "s_and_b32 s69, s68, 63\n\t" \
    "s_cbranch_scc1 rr_gf28_%=\n\t" \
    "global_load_dword v166, v6, s[36:37] sc1\n\t" \
    "rr_gf28_%=:\n\t" \
    "s_waitcnt vmcnt(0)\n\t" \
    "v_cmp_ne_u32 s[30:31], -1, v169\n\t" \
    "v_cmp_le_i32 vcc, s65, v166\n\t" \
    "s_nop 1\n\t" \
    "s_lshr_b32 s69, vcc_lo, 1\n\t" \
    "s_and_b32 s69, s69, vcc_lo\n\t" \
    "s_or_b32 s69, s69, s30\n\t" \
    "s_andn2_b32 s69, s69, s96\n\t" \
    "s_cbranch_scc0 rr_gs29_%=\n\t" \
    "s_or_b32 s96, s96, s69\n\t" \
    "s_cmp_eq_u32 s66, 0\n\t" \
    "s_cbranch_scc1 rr_w030_%=\n\t" \
    "s_sleep 8\n\t" \
    "rr_w030_%=:\n\t" \
    "s_bitcmp0_b32 s69, 0\n\t" \
    "s_cbranch_scc1 rr_gk31_%=\n\t" \
    "global_load_dwordx4 a[0:3], v26, s[38:39] offset:0 sc1\n\t" \
    "global_load_dwordx4 a[4:7], v26, s[38:39] offset:1024 sc1\n\t" \
    "rr_gk31_%=:\n\t" \
    "s_bitcmp0_b32 s69, 2\n\t" \
    "s_cbranch_scc1 rr_gk32_%=\n\t" \
    "global_load_dwordx4 a[8:11], v27, s[38:39] offset:0 sc1\n\t" \
    "global_load_dwordx4 a[12:15], v27, s[38:39] offset:1024 sc1\n\t" \
    "rr_gk32_%=:\n\t" \
    "s_bitcmp0_b32 s69, 4\n\t" \
    "s_cbranch_scc1 rr_gk33_%=\n\t" \
    "global_load_dwordx4 a[16:19], v28, s[38:39] offset:0 sc1\n\t" \
    "global_load_dwordx4 a[20:23], v28, s[38:39] offset:1024 sc1\n\t" \
    "rr_gk33_%=:\n\t" \
    "s_bitcmp0_b32 s69, 6\n\t" \
    "s_cbranch_scc1 rr_gk34_%=\n\t" \
    "global_load_dwordx4 a[24:27], v29, s[38:39] offset:0 sc1\n\t" \
    "global_load_dwordx4 a[28:31], v29, s[38:39] offset:1024 sc1\n\t" \
    "rr_gk34_%=:\n\t" \
    "s_bitcmp0_b32 s69, 8\n\t" \
    "s_cbranch_scc1 rr_gk35_%=\n\t" \
    "global_load_dwordx4 a[32:35], v30, s[38:39] offset:0 sc1\n\t" \
    "global_load_dwordx4 a[36:39], v30, s[38:39] offset:1024 sc1\n\t" \
    "rr_gk35_%=:\n\t" \
    "s_bitcmp0_b32 s69, 10\n\t" \
    "s_cbranch_scc1 rr_gk36_%=\n\t" \
    "global_load_dwordx4 a[40:43], v31, s[38:39] offset:0 sc1\n\t" \
    "global_load_dwordx4 a[44:47], v31, s[38:39] offset:1024 sc1\n\t" \
    "rr_gk36_%=:\n\t" \
    "s_bitcmp0_b32 s69, 12\n\t" \
    "s_cbranch_scc1 rr_gk37_%=\n\t" \
    "global_load_dwordx4 a[48:51], v32, s[38:39] offset:0 sc1\n\t" \
    "global_load_dwordx4 a[52:55], v32, s[38:39] offset:1024 sc1\n\t" \
    "rr_gk37_%=:\n\t" \
    "s_bitcmp0_b32 s69, 14\n\t" \
    "s_cbranch_scc1 rr_gk38_%=\n\t" \
    "global_load_dwordx4 a[56:59], v33, s[38:39] offset:0 sc1\n\t" \
    "global_load_dwordx4 a[60:63], v33, s[38:39] offset:1024 sc1\n\t" \
    "rr_gk38_%=:\n\t" \
    "s_bitcmp0_b32 s69, 16\n\t" \
    "s_cbranch_scc1 rr_gk39_%=\n\t" \
    "global_load_dwordx4 a[64:67], v34, s[38:39] offset:0 sc1\n\t" \
    "global_load_dwordx4 a[68:71], v34, s[38:39] offset:1024 sc1\n\t" \
    "rr_gk39_%=:\n\t" \
    "s_bitcmp0_b32 s69, 18\n\t" \
    "s_cbranch_scc1 rr_gk40_%=\n\t" \
    "global_load_dwordx4 a[72:75], v35, s[38:39] offset:0 sc1\n\t" \
    "global_load_dwordx4 a[76:79], v35, s[38:39] offset:1024 sc1\n\t" \
    "rr_gk40_%=:\n\t" \
    "s_bitcmp0_b32 s69, 20\n\t" \
    "s_cbranch_scc1 rr_gk41_%=\n\t" \
    "global_load_dwordx4 a[80:83], v36, s[38:39] offset:0 sc1\n\t" \
    "global_load_dwordx4 a[84:87], v36, s[38:39] offset:1024 sc1\n\t" \
    "rr_gk41_%=:\n\t" \
    "s_bitcmp0_b32 s69, 22\n\t" \
    "s_cbranch_scc1 rr_gk42_%=\n\t" \
    "global_load_dwordx4 a[88:91], v37, s[38:39] offset:0 sc1\n\t" \
    "global_load_dwordx4 a[92:95], v37, s[38:39] offset:1024 sc1\n\t" \
    "rr_gk42_%=:\n\t" \
    "s_bitcmp0_b32 s69, 24\n\t" \
    "s_cbranch_scc1 rr_gk43_%=\n\t" \
    "global_load_dwordx4 a[96:99], v38, s[38:39] offset:0 sc1\n\t" \
    "global_load_dwordx4 a[100:103], v38, s[38:39] offset:1024 sc1\n\t" \
    "rr_gk43_%=:\n\t" \
    "s_bitcmp0_b32 s69, 26\n\t" \
    "s_cbranch_scc1 rr_gk44_%=\n\t" \
    "global_load_dwordx4 a[104:107], v39, s[38:39] offset:0 sc1\n\t" \
    "global_load_dwordx4 a[108:111], v39, s[38:39] offset:1024 sc1\n\t" \
    "rr_gk44_%=:\n\t" \
    "s_bitcmp0_b32 s69, 28\n\t" \
    "s_cbranch_scc1 rr_gk45_%=\n\t" \
    "global_load_dwordx4 a[112:115], v40, s[38:39] offset:0 sc1\n\t" \
    "global_load_dwordx4 a[116:119], v40, s[38:39] offset:1024 sc1\n\t" \
    "rr_gk45_%=:\n\t" \
    "s_bitcmp0_b32 s69, 30\n\t" \
    "s_cbranch_scc1 rr_gk46_%=\n\t" \
    "global_load_dwordx4 a[120:123], v41, s[38:39] offset:0 sc1\n\t" \
    "global_load_dwordx4 a[124:127], v41, s[38:39] offset:1024 sc1\n\t" \
    "rr_gk46_%=:\n\t" \
    "s_cmp_eq_u32 s96, -1\n\t" \
    "s_cbranch_scc1 rr_n225_%=\n\t" \
    "rr_gs29_%=:\n\t" \
    "s_cmp_eq_u32 s68, 0\n\t" \
    "s_cbranch_scc1 rr_F47_%=\n\t" \
    "s_sub_u32 s68, s68, 1\n\t" \
    "s_sleep 1\n\t" \
    "s_branch rr_g012_%=\n\t" \
    "rr_F47_%=:\n\t" \
    "v_mov_b32 v167, 5767424\n\t" \
    "v_mov_b32 v168, 16\n\t" \
    "global_store_dword v167, v168, s[36:37] sc1\n\t" \
    "s_waitcnt vmcnt(0)\n\t" \
    "s_branch rr_n225_%=\n\t" \
    "rr_g113_%=:\n\t" \
    "global_load_dwordx4 a[0:3], v26, s[38:39] offset:0 sc1\n\t" \
    "global_load_dwordx4 a[4:7], v26, s[38:39] offset:1024 sc1\n\t" \
    "global_load_dwordx4 a[8:11], v27, s[38:39] offset:0 sc1\n\t" \
    "global_load_dwordx4 a[12:15], v27, s[38:39] offset:1024 sc1\n\t" \
    "global_load_dwordx4 a[16:19], v28, s[38:39] offset:0 sc1\n\t" \
    "global_load_dwordx4 a[20:23], v28, s[38:39] offset:1024 sc1\n\t" \
    "global_load_dwordx4 a[24:27], v29, s[38:39] offset:0 sc1\n\t" \
    "global_load_dwordx4 a[28:31], v29, s[38:39] offset:1024 sc1\n\t" \
    "global_load_dwordx4 a[32:35], v30, s[38:39] offset:0 sc1\n\t" \
    "global_load_dwordx4 a[36:39], v30, s[38:39] offset:1024 sc1\n\t" \
    "global_load_dwordx4 a[40:43], v31, s[38:39] offset:0 sc1\n\t" \
    "global_load_dwordx4 a[44:47], v31, s[38:39] offset:1024 sc1\n\t" \
    "global_load_dwordx4 a[48:51], v32, s[38:39] offset:0 sc1\n\t" \
    "global_load_dwordx4 a[52:55], v32, s[38:39] offset:1024 sc1\n\t" \
    "global_load_dwordx4 a[56:59], v33, s[38:39] offset:0 sc1\n\t" \
    "global_load_dwordx4 a[60:63], v33, s[38:39] offset:1024 sc1\n\t" \
    "global_load_dwordx4 a[64:67], v34, s[38:39] offset:0 sc1\n\t" \
    "global_load_dwordx4 a[68:71], v34, s[38:39] offset:1024 sc1\n\t" \
    "global_load_dwordx4 a[72:75], v35, s[38:39] offset:0 sc1\n\t" \
    "global_load_dwordx4 a[76:79], v35, s[38:39] offset:1024 sc1\n\t" \
    "global_load_dwordx4 a[80:83], v36, s[38:39] offset:0 sc1\n\t" \
    "global_load_dwordx4 a[84:87], v36, s[38:39] offset:1024 sc1\n\t" \
    "global_load_dwordx4 a[88:91], v37, s[38:39] offset:0 sc1\n\t" \
    "global_load_dwordx4 a[92:95], v37, s[38:39] offset:1024 sc1\n\t" \
    "global_load_dwordx4 a[96:99], v38, s[38:39] offset:0 sc1\n\t" \
    "global_load_dwordx4 a[100:103], v38, s[38:39] offset:1024 sc1\n\t" \
    "global_load_dwordx4 a[104:107], v39, s[38:39] offset:0 sc1\n\t" \
    "global_load_dwordx4 a[108:111], v39, s[38:39] offset:1024 sc1\n\t" \
    "global_load_dwordx4 a[112:115], v40, s[38:39] offset:0 sc1\n\t" \
    "global_load_dwordx4 a[116:119], v40, s[38:39] offset:1024 sc1\n\t" \
    "global_load_dwordx4 a[120:123], v41, s[38:39] offset:0 sc1\n\t" \
    "global_load_dwordx4 a[124:127], v41, s[38:39] offset:1024 sc1\n\t" \
    "rr_n225_%=:\n\t" \
    "s_cmp_eq_u32 s65, 0\n\t" \
    "s_cbranch_scc1 rr_act3_%=\n\t" \
    "s_waitcnt vmcnt(0)\n\t" \
    "rr_ckl49_%=:\n\t" \
    "v_cmp_eq_u32 vcc, -1, v200\n\t" \
    "s_cbranch_vccz rr_ck48_%=\n\t" \
    "global_load_dword v200, v201, s[36:37] sc1\n\t" \
    "s_waitcnt vmcnt(0)\n\t" \
    "s_cmp_eq_u32 s68, 0\n\t" \
    "s_cbranch_scc1 rr_F50_%=\n\t" \
    "s_sub_u32 s68, s68, 1\n\t" \
    "s_sleep 1\n\t" \
    "s_branch rr_ckl49_%=\n\t" \
    "rr_F50_%=:\n\t" \
    "v_mov_b32 v166, 5767424\n\t" \
    "v_mov_b32 v167, 15\n\t" \
    "global_store_dword v166, v167, s[36:37] sc1\n\t" \
    "s_waitcnt vmcnt(0)\n\t" \
    "s_branch rr_ck48_%=\n\t" \
    "rr_ck48_%=:\n\t" \
    "v_min_i32_dpp v164, v164, v164 row_ror:1 row_mask:0xf bank_mask:0xf\n\t" \
    "s_nop 1\n\t" \
    "v_min_i32_dpp v164, v164, v164 row_ror:2 row_mask:0xf bank_mask:0xf\n\t" \
    "s_nop 1\n\t" \
    "v_min_i32_dpp v164, v164, v164 row_ror:4 row_mask:0xf bank_mask:0xf\n\t" \
    "s_nop 1\n\t" \
    "v_min_i32_dpp v164, v164, v164 row_ror:8 row_mask:0xf bank_mask:0xf\n\t" \
    "s_nop 0\n\t" \
    "v_readlane_b32 s69, v164, 0\n\t" \
    "v_readlane_b32 s70, v164, 16\n\t" \
    "s_nop 1\n\t" \
    "s_min_i32 s69, s69, s70\n\t" \
    "s_max_i32 s83, s83, s69\n\t" \
    "s_cmp_eq_u32 s65, 0\n\t" \
    "s_cbranch_scc1 rr_act3_%=\n\t" \
    "s_barrier\n\t" \
    "s_cmp_gt_u32 s65, 62\n\t" \
    "s_cbranch_scc1 rr_end4_%=\n\t" \
    "ds_read_b32 v164, v9\n\t" \
    "s_waitcnt lgkmcnt(0)\n\t" \
    "v_readfirstlane_b32 s81, v164\n\t" \
    "s_nop 1\n\t" \
    "s_cmp_lg_u32 s65, 1\n\t" \
    "s_cbranch_scc1 rr_n151_%=\n\t" \
    "s_waitcnt lgkmcnt(0)\n\t" \
    "s_mov_b32 s70, 116\n\t" \
    "rr_E52_%=:\n\t" \
    "s_memtime s[56:57]\n\t" \
    "s_memrealtime s[2:3]\n\t" \
    "s_nop 0\n\t" \
    "s_memrealtime s[4:5]\n\t" \
    "s_nop 0\n\t" \
    "s_memrealtime s[6:7]\n\t" \
    "s_nop 0\n\t" \
    "s_memrealtime s[8:9]\n\t" \
    "s_nop 0\n\t" \
    "s_memrealtime s[10:11]\n\t" \
    "s_nop 0\n\t" \
    "s_memrealtime s[12:13]\n\t" \
    "s_nop 0\n\t" \
    "s_memrealtime s[14:15]\n\t" \
    "s_nop 0\n\t" \
    "s_memrealtime s[16:17]\n\t" \
    "s_nop 0\n\t" \
    "s_memrealtime s[18:19]\n\t" \
    "s_nop 0\n\t" \
    "s_memrealtime s[20:21]\n\t" \
    "s_nop 0\n\t" \
    "s_memrealtime s[22:23]\n\t" \
    "s_nop 0\n\t" \
    "s_memrealtime s[24:25]\n\t" \
    "s_nop 0\n\t" \
    "s_memrealtime s[26:27]\n\t" \
    "s_nop 0\n\t" \
    "s_memrealtime s[28:29]\n\t" \
    "s_memtime s[54:55]\n\t" \
    "s_add_u32 s70, s70, 8\n\t" \
    "s_waitcnt lgkmcnt(0)\n\t" \
    "s_sub_u32 s69, s54, s56\n\t" \
    "s_cmp_gt_u32 s69, s70\n\t" \
    "s_cbranch_scc1 rr_E52_%=\n\t" \
    "s_add_u32 s2, s2, s4\n\t" \
    "s_add_u32 s6, s6, s8\n\t" \
    "s_add_u32 s10, s10, s12\n\t" \
    "s_add_u32 s14, s14, s16\n\t" \
    "s_add_u32 s18, s18, s20\n\t" \
    "s_add_u32 s22, s22, s24\n\t" \
    "s_add_u32 s2, s2, s6\n\t" \
    "s_add_u32 s10, s10, s14\n\t" \
    "s_add_u32 s18, s18, s22\n\t" \
    "s_add_u32 s2, s2, s26\n\t" \
    "s_add_u32 s10, s10, s18\n\t" \
    "s_add_u32 s2, s2, s10\n\t" \
    "s_mul_i32 s70, s28, 13\n\t" \
    "s_sub_u32 s70, s70, s2\n\t" \
    "s_min_u32 s70, s70, 52\n\t" \
    "s_mul_i32 s69, s69, s70\n\t" \
    "s_mul_hi_u32 s69, s69, 76695844\n\t" \
    "s_mov_b32 s75, s73\n\t" \
    "s_mov_b32 s76, s74\n\t" \
    "s_add_u32 s73, s56, s69\n\t" \
    "s_lshl_b32 s74, s28, 4\n\t" \
    "rr_n151_%=:\n\t" \
    "s_mul_hi_u32 s69, s85, s78\n\t" \
    "s_add_u32 s79, s81, s69\n\t" \
    "s_add_u32 s79, s79, s87\n\t" \
    "s_cmp_ge_u32 s65, 53\n\t" \
    "s_cselect_b32 s69, 32, 0\n\t" \
    "s_sub_u32 s79, s79, s69\n\t" \
    "s_cmp_lt_u32 s65, 10\n\t" \
    "s_cselect_b32 s69, 48, 0\n\t" \
    "s_add_u32 s79, s79, s69\n\t" \
    "s_add_u32 s80, s79, s88\n\t" \
    "s_sub_u32 s70, s79, s74\n\t" \
    "s_max_i32 s70, s70, 0\n\t" \
    "s_min_i32 s70, s70, 16000\n\t" \
    "s_lshl_b32 s70, s70, 1\n\t" \
    "s_mul_hi_u32 s70, s70, s77\n\t" \
    "s_add_u32 s70, s70, s73\n\t" \
    "rr_W53_%=:\n\t" \
    "s_memtime s[56:57]\n\t" \
    "s_waitcnt lgkmcnt(0)\n\t" \
    "s_sub_u32 s69, s70, s56\n\t" \
    "s_cmp_gt_i32 s69, 24\n\t" \
    "s_cbranch_scc1 rr_W53_%=\n\t" \
    "rr_act3_%=:\n\t" \
    "s_add_u32 s69, s65, 0\n\t" \
    "s_and_b32 s69, s69, 7\n\t" \
    "s_lshl_b32 s69, s69, 18\n\t" \
    "s_add_u32 s69, s69, s62\n\t" \
    "s_add_u32 s46, s36, s69\n\t" \
    "s_addc_u32 s47, s37, 0\n\t" \
    "s_add_u32 s69, s65, 1\n\t" \
    "s_and_b32 s69, s69, 7\n\t" \
    "s_lshl_b32 s69, s69, 18\n\t" \
    "s_add_u32 s69, s69, s62\n\t" \
    "s_add_u32 s48, s36, s69\n\t" \
    "s_addc_u32 s49, s37, 0\n\t" \
    "s_and_b32 s69, s65, 7\n\t" \
    "s_lshl_b32 s69, s69, 18\n\t" \
    "s_add_u32 s69, s69, s63\n\t" \
    "s_add_u32 s52, s36, s69\n\t" \
    "s_addc_u32 s53, s37, 0\n\t" \
    "s_cmp_eq_u32 s65, 0\n\t" \
    "s_cbranch_scc1 rr_nf54_%=\n\t" \
    "s_add_u32 s69, s65, 2\n\t" \
    "s_cmp_ge_i32 s83, s69\n\t" \
    "s_cbranch_scc1 rr_nf54_%=\n\t" \
    "global_load_dword v164, v6, s[36:37] sc1\n\t" \
    "s_waitcnt vmcnt(0)\n\t" \
    "v_min_i32_dpp v164, v164, v164 row_ror:1 row_mask:0xf bank_mask:0xf\n\t" \
    "s_nop 1\n\t" \
    "v_min_i32_dpp v164, v164, v164 row_ror:2 row_mask:0xf bank_mask:0xf\n\t" \
    "s_nop 1\n\t" \
    "v_min_i32_dpp v164, v164, v164 row_ror:4 row_mask:0xf bank_mask:0xf\n\t" \
    "s_nop 1\n\t" \
    "v_min_i32_dpp v164, v164, v164 row_ror:8 row_mask:0xf bank_mask:0xf\n\t" \
    "s_nop 0\n\t" \
    "v_readlane_b32 s69, v164, 0\n\t" \
    "v_readlane_b32 s70, v164, 16\n\t" \
    "s_nop 1\n\t" \
    "s_min_i32 s83, s69, s70\n\t" \
    "rr_nf54_%=:\n\t" \
    "s_waitcnt lgkmcnt(0)\n\t" \
    "s_mov_b32 s70, 116\n\t" \
    "rr_E55_%=:\n\t" \
    "s_memtime s[56:57]\n\t" \
    "s_memrealtime s[2:3]\n\t" \
    "s_nop 0\n\t" \
    "s_memrealtime s[4:5]\n\t" \
    "s_nop 0\n\t" \
    "s_memrealtime s[6:7]\n\t" \
    "s_nop 0\n\t" \
    "s_memrealtime s[8:9]\n\t" \
    "s_nop 0\n\t" \
    "s_memrealtime s[10:11]\n\t" \
    "s_nop 0\n\t" \
    "s_memrealtime s[12:13]\n\t" \
    "s_nop 0\n\t" \
    "s_memrealtime s[14:15]\n\t" \
    "s_nop 0\n\t" \
    "s_memrealtime s[16:17]\n\t" \
    "s_nop 0\n\t" \
    "s_memrealtime s[18:19]\n\t" \
    "s_nop 0\n\t" \
    "s_memrealtime s[20:21]\n\t" \
    "s_nop 0\n\t" \
    "s_memrealtime s[22:23]\n\t" \
    "s_nop 0\n\t" \
    "s_memrealtime s[24:25]\n\t" \
    "s_nop 0\n\t" \
    "s_memrealtime s[26:27]\n\t" \
    "s_nop 0\n\t" \
    "s_memrealtime s[28:29]\n\t" \
    "s_memtime s[54:55]\n\t" \
    "s_add_u32 s70, s70, 8\n\t" \
    "s_waitcnt lgkmcnt(0)\n\t" \
    "s_sub_u32 s69, s54, s56\n\t" \
    "s_cmp_gt_u32 s69, s70\n\t" \
    "s_cbranch_scc1 rr_E55_%=\n\t" \
    "s_add_u32 s2, s2, s4\n\t" \
    "s_add_u32 s6, s6, s8\n\t" \
    "s_add_u32 s10, s10, s12\n\t" \
    "s_add_u32 s14, s14, s16\n\t" \
    "s_add_u32 s18, s18, s20\n\t" \
    "s_add_u32 s22, s22, s24\n\t" \
    "s_add_u32 s2, s2, s6\n\t" \
    "s_add_u32 s10, s10, s14\n\t" \
    "s_add_u32 s18, s18, s22\n\t" \
    "s_add_u32 s2, s2, s26\n\t" \
    "s_add_u32 s10, s10, s18\n\t" \
    "s_add_u32 s2, s2, s10\n\t" \
    "s_mul_i32 s70, s28, 13\n\t" \
    "s_sub_u32 s70, s70, s2\n\t" \
    "s_min_u32 s70, s70, 52\n\t" \
    "s_mul_i32 s69, s69, s70\n\t" \
    "s_mul_hi_u32 s69, s69, 76695844\n\t" \
    "s_mov_b32 s75, s73\n\t" \
    "s_mov_b32 s76, s74\n\t" \
    "s_add_u32 s73, s56, s69\n\t" \
    "s_lshl_b32 s74, s28, 4\n\t" \
    "s_sub_u32 s69, s73, s75\n\t" \
    "s_sub_u32 s70, s74, s76\n\t" \
    "s_sub_u32 s71, s70, 1600\n\t" \
    "s_cmp_lt_u32 s71, 400000\n\t" \
    "s_cbranch_scc0 rr_K56_%=\n\t" \
    "v_cvt_f32_u32 v164, s69\n\t" \
    "v_cvt_f32_u32 v165, s70\n\t" \
    "v_rcp_f32 v166, v164\n\t" \
    "v_rcp_f32 v167, v165\n\t" \
    "v_mul_f32 v166, v166, v165\n\t" \
    "v_mul_f32 v167, v167, v164\n\t" \
    "v_mul_f32 v166, 0x4f800000, v166\n\t" \
    "v_mul_f32 v167, 0x4f000000, v167\n\t" \
    "v_cvt_u32_f32 v166, v166\n\t" \
    "v_cvt_u32_f32 v167, v167\n\t" \
    "v_readfirstlane_b32 s69, v166\n\t" \
    "v_readfirstlane_b32 s70, v167\n\t" \
    "s_nop 1\n\t" \
    "s_lshr_b32 s71, s65, 1\n\t" \
    "s_min_u32 s71, s71, 1\n\t" \
    "s_sub_u32 s69, s69, s78\n\t" \
    "s_ashr_i32 s69, s69, s71\n\t" \
    "s_add_u32 s78, s78, s69\n\t" \
    "s_sub_u32 s70, s70, s77\n\t" \
    "s_ashr_i32 s70, s70, s71\n\t" \
    "s_add_u32 s77, s77, s70\n\t" \
    "rr_K56_%=:\n\t" \
    "s_mov_b32 s91, s64\n\t" \
    "s_cmp_lg_u32 s64, 0\n\t" \
    "s_cselect_b32 s77, 0, s77\n\t" \
    "v_mov_b32 v160, 0\n\t" \
    "v_mov_b32 v161, 0\n\t" \
    "v_mov_b32 v162, 0\n\t" \
    "v_mov_b32 v163, 0\n\t" \
    ".p2align 6\n\t" \
    "s_nop 0\n\t" \
    "s_nop 0\n\t" \
    "s_sub_u32 s70, s80, s74\n\t" \
    "s_max_i32 s70, s70, 0\n\t" \
    "s_min_i32 s70, s70, 16000\n\t" \
    "s_lshl_b32 s70, s70, 1\n\t" \
    "s_mul_hi_u32 s70, s70, s77\n\t" \
    "s_add_u32 s70, s70, s73\n\t" \
    "s_getpc_b64 s[30:31]\n\t" \
    "rr_Wp59_%=:\n\t" \
    "s_add_u32 s30, s30, rr_We60_%=-rr_Wp59_%=\n\t" \
    "s_addc_u32 s31, s31, 0\n\t" \
    "s_sub_u32 s70, s70, 96\n\t" \
    "rr_W58_%=:\n\t" \
    "s_memtime s[56:57]\n\t" \
    "s_waitcnt lgkmcnt(0)\n\t" \
    "s_sub_u32 s69, s70, s56\n\t" \
    "s_cmp_gt_i32 s69, 80\n\t" \
    "s_cbranch_scc1 rr_W58_%=\n\t" \
    "s_max_i32 s69, s69, 0\n\t" \
    "s_and_b32 s69, s69, -4\n\t" \
    "s_sub_u32 s30, s30, s69\n\t" \
    "s_subb_u32 s31, s31, 0\n\t" \
    "s_setpc_b64 s[30:31]\n\t" \
    "s_nop 0\n\t" \
    "s_nop 0\n\t" \
    "s_nop 0\n\t" \
    "s_nop 0\n\t" \
    "s_nop 0\n\t" \
    "s_nop 0\n\t" \
    "s_nop 0\n\t" \
    "s_nop 0\n\t" \
    "s_nop 0\n\t" \
    "s_nop 0\n\t" \
    "s_nop 0\n\t" \
    "s_nop 0\n\t" \
    "s_nop 0\n\t" \
    "s_nop 0\n\t" \
    "s_nop 0\n\t" \
    "s_nop 0\n\t" \
    "s_nop 0\n\t" \
    "s_nop 0\n\t" \
    "s_nop 0\n\t" \
    "s_nop 0\n\t" \
    "rr_We60_%=:\n\t" \
    "global_load_dword v192, v7, s[52:53] offset:0 sc1\n\t" \
    "s_sub_u32 s71, s86, s74\n\t" \
    "s_lshl_b32 s71, s71, 2\n\t" \
    "s_lshr_b32 s33, s77, 1\n\t" \
    "s_sub_u32 s34, s73, 176\n\t" \
    "s_mov_b32 s96, 0\n\t" \
    "s_getpc_b64 s[30:31]\n\t" \
    "rr_rpc64_%=:\n\t" \
    "s_add_u32 s50, s30, rr_pgo67_%=-rr_rpc64_%=\n\t" \
    "s_addc_u32 s51, s31, 0\n\t" \
    "s_add_u32 s30, s30, rr_again5_%=-rr_rpc64_%=\n\t" \
    "s_addc_u32 s31, s31, 0\n\t" \
    "s_nop 7\n\t" \
    "s_nop 7\n\t" \
    "s_nop 7\n\t" \
    "s_nop 7\n\t" \
    "s_nop 3\n\t" \
    "global_load_dword v193, v7, s[52:53] offset:2048 sc1\n\t" \
    "s_nop 7\n\t" \
    "s_nop 7\n\t" \
    "s_nop 7\n\t" \
    "s_nop 7\n\t" \
    "s_nop 7\n\t" \
    "s_nop 7\n\t" \
    "global_load_dword v194, v189, s[52:53] offset:0 sc1\n\t" \
    "s_waitcnt vmcnt(2)\n\t" \
    "rr_hok61_%=:\n\t" \
    "s_memtime s[54:55]\n\t" \
    "v_max_u32_dpp v192, v192, v192 row_ror:1 row_mask:0xf bank_mask:0xf\n\t" \
    "s_nop 1\n\t" \
    "v_max_u32_dpp v192, v192, v192 row_ror:2 row_mask:0xf bank_mask:0xf\n\t" \
    "s_nop 1\n\t" \
    "v_max_u32_dpp v192, v192, v192 row_ror:4 row_mask:0xf bank_mask:0xf\n\t" \
    "s_nop 1\n\t" \
    "v_max_u32_dpp v192, v192, v192 row_ror:8 row_mask:0xf bank_mask:0xf\n\t" \
    "s_nop 0\n\t" \
    "v_readlane_b32 s69, v192, 0\n\t" \
    "v_readlane_b32 s70, v192, 16\n\t" \
    "s_waitcnt lgkmcnt(0)\n\t" \
    "rr_hmax70_%=:\n\t" \
    "s_sub_u32 s56, s34, s54\n\t" \
    "s_max_u32 s81, s69, s70\n\t" \
    "s_cmp_eq_u32 s81, -1\n\t" \
    "s_cbranch_scc1 rr_hlate62_%=\n\t" \
    "s_lshl2_add_u32 s70, s81, s71\n\t" \
    "s_mul_hi_i32 s70, s70, s33\n\t" \
    "s_add_u32 s69, s70, s56\n\t" \
    "s_max_i32 s69, s69, 0\n\t" \
    "s_cmp_gt_i32 s69, 80\n\t" \
    "s_cbranch_scc1 rr_rpoll65_%=\n\t" \
    "rr_rback66_%=:\n\t" \
    "s_and_b32 s69, s69, -4\n\t" \
    "s_sub_u32 s30, s30, s69\n\t" \
    "s_subb_u32 s31, s31, 0\n\t" \
    "s_setpc_b64 s[30:31]\n\t" \
    "rr_rpoll65_%=:\n\t" \
    "s_min_i32 s70, s70, 24000\n\t" \
    "s_add_u32 s70, s70, s73\n\t" \
    "s_sub_u32 s70, s70, 124\n\t" \
    "s_sub_u32 s70, s70, s96\n\t" \
    "rr_wl63_%=:\n\t" \
    "s_memtime s[56:57]\n\t" \
    "s_waitcnt lgkmcnt(0)\n\t" \
    "s_sub_u32 s69, s70, s56\n\t" \
    "s_cmp_gt_i32 s69, 80\n\t" \
    "s_cbranch_scc1 rr_wl63_%=\n\t" \
    "s_max_i32 s69, s69, 0\n\t" \
    "s_branch rr_rback66_%=\n\t" \
    "s_nop 0\n\t" \
    "s_nop 0\n\t" \
    "s_nop 0\n\t" \
    "s_nop 0\n\t" \
    "s_nop 0\n\t" \
    "s_nop 0\n\t" \
    "s_nop 0\n\t" \
    "s_nop 0\n\t" \
    "s_nop 0\n\t" \
    "s_nop 0\n\t" \
    "s_nop 0\n\t" \
    "s_nop 0\n\t" \
    "s_nop 0\n\t" \
    "s_nop 0\n\t" \
    "s_nop 0\n\t" \
    "s_nop 0\n\t" \
    "s_nop 0\n\t" \
    "s_nop 0\n\t" \
    "s_nop 0\n\t" \
    "s_nop 0\n\t" \
    "rr_again5_%=:\n\t" \
    "rr_pgo67_%=:\n\t" \
    "global_load_dwordx4 v[48:51], v10, s[46:47] sc1\n\t" \
    "global_load_dwordx4 v[52:55], v11, s[46:47] sc1\n\t" \
    "global_load_dwordx4 v[56:59], v12, s[46:47] sc1\n\t" \
    "global_load_dwordx4 v[60:63], v13, s[46:47] sc1\n\t" \
    "global_load_dwordx4 v[64:67], v14, s[46:47] sc1\n\t" \
    "global_load_dwordx4 v[68:71], v15, s[46:47] sc1\n\t" \
    "global_load_dwordx4 v[72:75], v16, s[46:47] sc1\n\t" \
    "global_load_dwordx4 v[76:79], v17, s[46:47] sc1\n\t" \
    "global_load_dwordx4 v[80:83], v18, s[46:47] sc1\n\t" \
    "global_load_dwordx4 v[84:87], v19, s[46:47] sc1\n\t" \
    "global_load_dwordx4 v[88:91], v20, s[46:47] sc1\n\t" \
    "global_load_dwordx4 v[92:95], v21, s[46:47] sc1\n\t" \
    "global_load_dwordx4 v[96:99], v22, s[46:47] sc1\n\t" \
    "global_load_dwordx4 v[100:103], v23, s[46:47] sc1\n\t" \
    "global_load_dwordx4 v[104:107], v24, s[46:47] sc1\n\t" \
    "global_load_dwordx4 v[108:111], v25, s[46:47] sc1\n\t" \
    "global_load_dwordx4 v[112:115], v42, s[46:47] sc1\n\t" \
    "global_load_dwordx4 v[116:119], v43, s[46:47] sc1\n\t" \
    "global_load_dwordx4 v[120:123], v44, s[46:47] sc1\n\t" \
    "global_load_dwordx4 v[124:127], v45, s[46:47] sc1\n\t" \
    "global_load_dwordx4 v[128:131], v0, s[40:41] offset:0 sc1\n\t" \
    "global_load_dwordx4 v[132:135], v0, s[40:41] offset:1024 sc1\n\t" \
    "global_load_dwordx4 v[136:139], v0, s[40:41] offset:2048 sc1\n\t" \
    "global_load_dwordx4 v[140:143], v0, s[40:41] offset:3072 sc1\n\t" \
    "global_load_dwordx4 v[144:147], v1, s[40:41] offset:0 sc1\n\t" \
    "global_load_dwordx4 v[148:151], v1, s[40:41] offset:1024 sc1\n\t" \
    "global_load_dwordx4 v[152:155], v1, s[40:41] offset:2048 sc1\n\t" \
    "global_load_dwordx4 v[156:159], v1, s[40:41] offset:3072 sc1\n\t" \
    "s_cmp_lg_u32 s91, 0\n\t" \
    "s_cbranch_scc1 rr_nob8_%=\n\t" \
    "rr_hbk71_%=:\n\t" \
    "v_mov_b32 v164, s81\n\t" \
    "ds_write_b32 v9, v164\n\t" \
    "s_waitcnt lgkmcnt(0)\n\t" \
    "s_barrier\n\t" \
    "rr_nob8_%=:\n\t" \
    "s_waitcnt vmcnt(27)\n\t" \
    "v_smfmac_f32_16x16x64_bf16 v[160:163], v[48:51], a[0:7], v4\n\t" \
    "s_waitcnt vmcnt(26)\n\t" \
    "v_smfmac_f32_16x16x64_bf16 v[160:163], v[52:55], a[8:15], v4\n\t" \
    "s_waitcnt vmcnt(25)\n\t" \
    "v_smfmac_f32_16x16x64_bf16 v[160:163], v[56:59], a[16:23], v4\n\t" \
    "s_waitcnt vmcnt(24)\n\t" \
    "v_smfmac_f32_16x16x64_bf16 v[160:163], v[60:63], a[24:31], v4\n\t" \
    "s_waitcnt vmcnt(23)\n\t" \
    "v_smfmac_f32_16x16x64_bf16 v[160:163], v[64:67], a[32:39], v4\n\t" \
    "s_waitcnt vmcnt(22)\n\t" \
    "v_smfmac_f32_16x16x64_bf16 v[160:163], v[68:71], a[40:47], v4\n\t" \
    "s_waitcnt vmcnt(21)\n\t" \
    "v_smfmac_f32_16x16x64_bf16 v[160:163], v[72:75], a[48:55], v4\n\t" \
    "s_waitcnt vmcnt(20)\n\t" \
    "v_smfmac_f32_16x16x64_bf16 v[160:163], v[76:79], a[56:63], v4\n\t" \
    "s_waitcnt vmcnt(19)\n\t" \
    "v_smfmac_f32_16x16x64_bf16 v[160:163], v[80:83], a[64:71], v4\n\t" \
    "s_waitcnt vmcnt(18)\n\t" \
    "v_smfmac_f32_16x16x64_bf16 v[160:163], v[84:87], a[72:79], v4\n\t" \
    "s_waitcnt vmcnt(17)\n\t" \
    "v_smfmac_f32_16x16x64_bf16 v[160:163], v[88:91], a[80:87], v4\n\t" \
    "s_waitcnt vmcnt(16)\n\t" \
    "v_smfmac_f32_16x16x64_bf16 v[160:163], v[92:95], a[88:95], v4\n\t" \
    "s_waitcnt vmcnt(15)\n\t" \
    "v_smfmac_f32_16x16x64_bf16 v[160:163], v[96:99], a[96:103], v4\n\t" \
    "s_waitcnt vmcnt(14)\n\t" \
    "v_smfmac_f32_16x16x64_bf16 v[160:163], v[100:103], a[104:111], v4\n\t" \
    "s_waitcnt vmcnt(13)\n\t" \
    "v_smfmac_f32_16x16x64_bf16 v[160:163], v[104:107], a[112:119], v4\n\t" \
    "s_waitcnt vmcnt(12)\n\t" \
    "v_smfmac_f32_16x16x64_bf16 v[160:163], v[108:111], a[120:127], v4\n\t" \
    "s_waitcnt vmcnt(6)\n\t" \
    "v_smfmac_f32_16x16x64_bf16 v[160:163], v[112:115], v[128:135], v4\n\t" \
    "s_waitcnt vmcnt(4)\n\t" \
    "v_smfmac_f32_16x16x64_bf16 v[160:163], v[116:119], v[136:143], v4\n\t" \
    "s_waitcnt vmcnt(2)\n\t" \
    "v_smfmac_f32_16x16x64_bf16 v[160:163], v[120:123], v[144:151], v4\n\t" \
    "s_waitcnt vmcnt(0)\n\t" \
    "v_smfmac_f32_16x16x64_bf16 v[160:163], v[124:127], v[152:159], v4\n\t" \
    "s_nop 7\n\t" \
    "v_pk_add_f32 v[160:161], v[160:161], v[162:163]\n\t" \
    "v_add_f32 v166, v160, v161\n\t" \
    "v_cvt_pk_bf16_f32 v164, v160, v161\n\t" \
    "s_nop 0\n\t" \
    "v_add_f32_dpp v166, v166, v166 quad_perm:[1,0,3,2] row_mask:0xf bank_mask:0xf\n\t" \
    "v_mov_b32_dpp v165, v164 quad_perm:[1,0,3,2] row_mask:0xf bank_mask:0xf\n\t" \
    "v_cmp_u_f32 vcc, v166, v166\n\t" \
    "v_perm_b32 v167, v165, v164, v5\n\t" \
    "s_nop 0\n\t" \
    "v_cndmask_b32 v164, v167, v172, vcc\n\t" \
    "rr_cont7_%=:\n\t" \
    "global_store_dword v180, v164, s[48:49] sc1\n\t" \
    "global_store_dword v181, v164, s[48:49] sc1\n\t" \
    "global_store_dword v182, v164, s[48:49] sc1\n\t" \
    "global_store_dword v2, v164, s[48:49] sc1\n\t" \
    "s_cmp_lg_u32 s64, 0\n\t" \
    "s_cbranch_scc1 rr_g214_%=\n\t" \
    "s_cbranch_vccnz rr_bad6_%=\n\t" \
    "s_cmp_eq_u32 s65, 62\n\t" \
    "s_cbranch_scc1 rr_end4_%=\n\t" \
    "s_add_u32 s65, s65, 2\n\t" \
    "s_branch rr_pf9_%=\n\t" \
    "rr_bad6_%=:\n\t" \
    "s_cmp_lg_u32 s64, 0\n\t" \
    "s_cbranch_scc1 rr_cont7_%=\n\t" \
    "s_waitcnt lgkmcnt(0)\n\t" \
    "v_max_u32 v165, v48, v49\n\t" \
    "v_max3_u32 v165, v165, v50, v51\n\t" \
    "v_max3_u32 v165, v165, v52, v53\n\t" \
    "v_max3_u32 v165, v165, v54, v55\n\t" \
    "v_max3_u32 v165, v165, v56, v57\n\t" \
    "v_max3_u32 v165, v165, v58, v59\n\t" \
    "v_max3_u32 v165, v165, v60, v61\n\t" \
    "v_max3_u32 v165, v165, v62, v63\n\t" \
    "v_max3_u32 v165, v165, v64, v65\n\t" \
    "v_max3_u32 v165, v165, v66, v67\n\t" \
    "v_max3_u32 v165, v165, v68, v69\n\t" \
    "v_max3_u32 v165, v165, v70, v71\n\t" \
    "v_max3_u32 v165, v165, v72, v73\n\t" \
    "v_max3_u32 v165, v165, v74, v75\n\t" \
    "v_max3_u32 v165, v165, v76, v77\n\t" \
    "v_max3_u32 v165, v165, v78, v79\n\t" \
    "v_max3_u32 v165, v165, v80, v81\n\t" \
    "v_max3_u32 v165, v165, v82, v83\n\t" \
    "v_max3_u32 v165, v165, v84, v85\n\t" \
    "v_max3_u32 v165, v165, v86, v87\n\t" \
    "v_max3_u32 v165, v165, v88, v89\n\t" \
    "v_max3_u32 v165, v165, v90, v91\n\t" \
    "v_max3_u32 v165, v165, v92, v93\n\t" \
    "v_max3_u32 v165, v165, v94, v95\n\t" \
    "v_max3_u32 v165, v165, v96, v97\n\t" \
    "v_max3_u32 v165, v165, v98, v99\n\t" \
    "v_max3_u32 v165, v165, v100, v101\n\t" \
    "v_max3_u32 v165, v165, v102, v103\n\t" \
    "v_max3_u32 v165, v165, v104, v105\n\t" \
    "v_max3_u32 v165, v165, v106, v107\n\t" \
    "v_max3_u32 v165, v165, v108, v109\n\t" \
    "v_max3_u32 v165, v165, v110, v111\n\t" \
    "v_max3_u32 v165, v165, v112, v113\n\t" \
    "v_max3_u32 v165, v165, v114, v115\n\t" \
    "v_max3_u32 v165, v165, v116, v117\n\t" \
    "v_max3_u32 v165, v165, v118, v119\n\t" \
    "v_max3_u32 v165, v165, v120, v121\n\t" \
    "v_max3_u32 v165, v165, v122, v123\n\t" \
    "v_max3_u32 v165, v165, v124, v125\n\t" \
    "v_max3_u32 v165, v165, v126, v127\n\t" \
    "v_max_u32 v166, v128, v129\n\t" \
    "v_max3_u32 v166, v166, v130, v131\n\t" \
    "v_max3_u32 v166, v166, v132, v133\n\t" \
    "v_max3_u32 v166, v166, v134, v135\n\t" \
    "v_max3_u32 v166, v166, v136, v137\n\t" \
    "v_max3_u32 v166, v166, v138, v139\n\t" \
    "v_max3_u32 v166, v166, v140, v141\n\t" \
    "v_max3_u32 v166, v166, v142, v143\n\t" \
    "v_max3_u32 v166, v166, v144, v145\n\t" \
    "v_max3_u32 v166, v166, v146, v147\n\t" \
    "v_max3_u32 v166, v166, v148, v149\n\t" \
    "v_max3_u32 v166, v166, v150, v151\n\t" \
    "v_max3_u32 v166, v166, v152, v153\n\t" \
    "v_max3_u32 v166, v166, v154, v155\n\t" \
    "v_max3_u32 v166, v166, v156, v157\n\t" \
    "v_max3_u32 v166, v166, v158, v159\n\t" \
    "v_max_u32 v165, v165, v166\n\t" \
    "v_mov_b32 v164, v167\n\t" \
    "v_cmp_eq_u32 vcc, -1, v165\n\t" \
    "s_cbranch_vccz rr_cont7_%=\n\t" \
    "s_mov_b64 vcc, 0\n\t" \
    "s_mov_b32 s91, 1\n\t" \
    "s_mov_b32 s96, 0\n\t" \
    "v_mov_b32 v160, 0\n\t" \
    "v_mov_b32 v161, 0\n\t" \
    "v_mov_b32 v162, 0\n\t" \
    "v_mov_b32 v163, 0\n\t" \
    "s_cmp_eq_u32 s68, 0\n\t" \
    "s_cbranch_scc1 rr_F74_%=\n\t" \
    "s_sub_u32 s68, s68, 1\n\t" \
    "s_branch rr_again5_%=\n\t" \
    "rr_F74_%=:\n\t" \
    "v_mov_b32 v166, 5767424\n\t" \
    "v_mov_b32 v167, 3\n\t" \
    "global_store_dword v166, v167, s[36:37] sc1\n\t" \
    "s_waitcnt vmcnt(0)\n\t" \
    "s_branch rr_cont7_%=\n\t" \
    "rr_hlate62_%=:\n\t" \
    "s_cmp_lg_u32 s64, 0\n\t" \
    "s_cbranch_scc1 rr_hpd73_%=\n\t" \
    "global_load_dword v195, v189, s[52:53] offset:2048 sc1\n\t" \
    "s_waitcnt vmcnt(2)\n\t" \
    "v_mov_b32 v192, v193\n\t" \
    "v_cmp_eq_u32 vcc, -1, v193\n\t" \
    "s_cbranch_vccz rr_hok61_%=\n\t" \
    "global_load_dword v196, v190, s[52:53] offset:0 sc1\n\t" \
    "s_waitcnt vmcnt(2)\n\t" \
    "v_mov_b32 v192, v194\n\t" \
    "v_cmp_eq_u32 vcc, -1, v194\n\t" \
    "s_cbranch_vccz rr_hok61_%=\n\t" \
    "global_load_dword v197, v190, s[52:53] offset:2048 sc1\n\t" \
    "s_waitcnt vmcnt(2)\n\t" \
    "v_mov_b32 v192, v195\n\t" \
    "v_cmp_eq_u32 vcc, -1, v195\n\t" \
    "s_cbranch_vccz rr_hok61_%=\n\t" \
    "s_waitcnt vmcnt(1)\n\t" \
    "v_mov_b32 v192, v196\n\t" \
    "v_cmp_eq_u32 vcc, -1, v196\n\t" \
    "s_cbranch_vccz rr_hok61_%=\n\t" \
    "s_waitcnt vmcnt(0)\n\t" \
    "v_mov_b32 v192, v197\n\t" \
    "v_cmp_eq_u32 vcc, -1, v197\n\t" \
    "s_cbranch_vccz rr_hok61_%=\n\t" \
    "rr_hp72_%=:\n\t" \
    "global_load_dword v192, v191, s[52:53] offset:0 sc1\n\t" \
    "s_waitcnt vmcnt(0)\n\t" \
    "v_cmp_eq_u32 vcc, -1, v192\n\t" \
    "s_cbranch_vccz rr_hpd73_%=\n\t" \
    "s_cmp_eq_u32 s68, 0\n\t" \
    "s_cbranch_scc1 rr_F75_%=\n\t" \
    "s_sub_u32 s68, s68, 1\n\t" \
    "s_sleep 1\n\t" \
    "s_branch rr_hp72_%=\n\t" \
    "rr_F75_%=:\n\t" \
    "v_mov_b32 v166, 5767424\n\t" \
    "v_mov_b32 v167, 7\n\t" \
    "global_store_dword v166, v167, s[36:37] sc1\n\t" \
    "s_waitcnt vmcnt(0)\n\t" \
    "s_branch rr_hpd73_%=\n\t" \
    "rr_hpd73_%=:\n\t" \
    "v_and_b32 v192, -2, v192\n\t" \
    "s_waitcnt lgkmcnt(0)\n\t" \
    "s_mov_b32 s70, 116\n\t" \
    "rr_E76_%=:\n\t" \
    "s_memtime s[56:57]\n\t" \
    "s_memrealtime s[2:3]\n\t" \
    "s_nop 0\n\t" \
    "s_memrealtime s[4:5]\n\t" \
    "s_nop 0\n\t" \
    "s_memrealtime s[6:7]\n\t" \
    "s_nop 0\n\t" \
    "s_memrealtime s[8:9]\n\t" \
    "s_nop 0\n\t" \
    "s_memrealtime s[10:11]\n\t" \
    "s_nop 0\n\t" \
    "s_memrealtime s[12:13]\n\t" \
    "s_nop 0\n\t" \
    "s_memrealtime s[14:15]\n\t" \
    "s_nop 0\n\t" \
    "s_memrealtime s[16:17]\n\t" \
    "s_nop 0\n\t" \
    "s_memrealtime s[18:19]\n\t" \
    "s_nop 0\n\t" \
    "s_memrealtime s[20:21]\n\t" \
    "s_nop 0\n\t" \
    "s_memrealtime s[22:23]\n\t" \
    "s_nop 0\n\t" \
    "s_memrealtime s[24:25]\n\t" \
    "s_nop 0\n\t" \
    "s_memrealtime s[26:27]\n\t" \
    "s_nop 0\n\t" \
    "s_memrealtime s[28:29]\n\t" \
    "s_memtime s[54:55]\n\t" \
    "s_add_u32 s70, s70, 8\n\t" \
    "s_waitcnt lgkmcnt(0)\n\t" \
    "s_sub_u32 s69, s54, s56\n\t" \
    "s_cmp_gt_u32 s69, s70\n\t" \
    "s_cbranch_scc1 rr_E76_%=\n\t" \
    "s_add_u32 s2, s2, s4\n\t" \
    "s_add_u32 s6, s6, s8\n\t" \
    "s_add_u32 s10, s10, s12\n\t" \
    "s_add_u32 s14, s14, s16\n\t" \
    "s_add_u32 s18, s18, s20\n\t" \
    "s_add_u32 s22, s22, s24\n\t" \
    "s_add_u32 s2, s2, s6\n\t" \
    "s_add_u32 s10, s10, s14\n\t" \
    "s_add_u32 s18, s18, s22\n\t" \
    "s_add_u32 s2, s2, s26\n\t" \
    "s_add_u32 s10, s10, s18\n\t" \
    "s_add_u32 s2, s2, s10\n\t" \
    "s_mul_i32 s70, s28, 13\n\t" \
    "s_sub_u32 s70, s70, s2\n\t" \
    "s_min_u32 s70, s70, 52\n\t" \
    "s_mul_i32 s69, s69, s70\n\t" \
    "s_mul_hi_u32 s69, s69, 76695844\n\t" \
    "s_mov_b32 s75, s73\n\t" \
    "s_mov_b32 s76, s74\n\t" \
    "s_add_u32 s73, s56, s69\n\t" \
    "s_lshl_b32 s74, s28, 4\n\t" \
    "s_sub_u32 s71, s86, s74\n\t" \
    "s_lshl_b32 s71, s71, 2\n\t" \
    "s_lshr_b32 s33, s77, 1\n\t" \
    "s_sub_u32 s34, s73, 176\n\t" \
    "s_branch rr_hok61_%=\n\t" \
    "rr_end4_%=:\n\t" \
    "s_endpgm\n\t" \
    "rr_last_%=:\n\t" \
    ""
#define ASM_H \
    "s_mov_b64 s[36:37], %0\n\t" \
    "s_mov_b64 s[42:43], %1\n\t" \
    "s_mov_b64 s[44:45], %2\n\t" \
    "s_mov_b32 s66, %3\n\t" \
    "s_mov_b32 s67, %4\n\t" \
    "s_mov_b64 s[50:51], %5\n\t" \
    "s_mov_b32 s82, %7\n\t" \
    "s_load_dwordx8 s[84:91], %6, 0\n\t" \
    "v_mbcnt_lo_u32_b32 v223, -1, 0\n\t" \
    "v_mbcnt_hi_u32_b32 v223, -1, v223\n\t" \
    "s_getpc_b64 s[46:47]\n\t" \
    "hh_pc_%=:\n\t" \
    "s_lshl_b32 s69, s66, 13\n\t" \
    "v_lshl_add_u32 v222, v223, 7, s69\n\t" \
    "v_min_u32 v222, hh_last_%=-hh_pc_%=-4, v222\n\t" \
    "global_load_dword v222, v222, s[46:47]\n\t" \
    "v_lshlrev_b32 v223, 2, v223\n\t" \
    "v_add_u32 v223, s82, v223\n\t" \
    "ds_read_b32 v0, v223 offset:0\n\t" \
    "ds_read_b32 v1, v223 offset:256\n\t" \
    "ds_read_b32 v2, v223 offset:512\n\t" \
    "ds_read_b32 v3, v223 offset:768\n\t" \
    "ds_read_b32 v4, v223 offset:1024\n\t" \
    "ds_read_b32 v5, v223 offset:1280\n\t" \
    "ds_read_b32 v6, v223 offset:1536\n\t" \
    "ds_read_b32 v7, v223 offset:1792\n\t" \
    "ds_read_b32 v8, v223 offset:2048\n\t" \
    "ds_read_b32 v9, v223 offset:2304\n\t" \
    "ds_read_b32 v10, v223 offset:2560\n\t" \
    "ds_read_b32 v11, v223 offset:2816\n\t" \
    "s_waitcnt lgkmcnt(0)\n\t" \
    "ds_read_b32 v12, v223 offset:3072\n\t" \
    "ds_read_b32 v13, v223 offset:3328\n\t" \
    "ds_read_b32 v14, v223 offset:3584\n\t" \
    "ds_read_b32 v15, v223 offset:3840\n\t" \
    "ds_read_b32 v16, v223 offset:4096\n\t" \
    "ds_read_b32 v17, v223 offset:4352\n\t" \
    "s_waitcnt lgkmcnt(0)\n\t" \
    "s_mov_b32 s68, 262144\n\t" \
    "s_mov_b32 s72, 0\n\t" \
    "s_mov_b32 s83, -1\n\t" \
    "s_mov_b32 s65, 0\n\t" \
    "s_mov_b32 s77, 3019898880\n\t" \
    "s_mov_b32 s78, 3054198966\n\t" \
    "s_waitcnt lgkmcnt(0)\n\t" \
    "s_mov_b32 s70, 116\n\t" \
    "hh_E1_%=:\n\t" \
    "s_memtime s[56:57]\n\t" \
    "s_memrealtime s[2:3]\n\t" \
    "s_nop 0\n\t" \
    "s_memrealtime s[4:5]\n\t" \
    "s_nop 0\n\t" \
    "s_memrealtime s[6:7]\n\t" \
    "s_nop 0\n\t" \
    "s_memrealtime s[8:9]\n\t" \
    "s_nop 0\n\t" \
    "s_memrealtime s[10:11]\n\t" \
    "s_nop 0\n\t" \
    "s_memrealtime s[12:13]\n\t" \
    "s_nop 0\n\t" \
    "s_memrealtime s[14:15]\n\t" \
    "s_nop 0\n\t" \
    "s_memrealtime s[16:17]\n\t" \
    "s_nop 0\n\t" \
    "s_memrealtime s[18:19]\n\t" \
    "s_nop 0\n\t" \
    "s_memrealtime s[20:21]\n\t" \
    "s_nop 0\n\t" \
    "s_memrealtime s[22:23]\n\t" \
    "s_nop 0\n\t" \
    "s_memrealtime s[24:25]\n\t" \
    "s_nop 0\n\t" \
    "s_memrealtime s[26:27]\n\t" \
    "s_nop 0\n\t" \
    "s_memrealtime s[28:29]\n\t" \
    "s_memtime s[54:55]\n\t" \
    "s_add_u32 s70, s70, 8\n\t" \
    "s_waitcnt lgkmcnt(0)\n\t" \
    "s_sub_u32 s69, s54, s56\n\t" \
    "s_cmp_gt_u32 s69, s70\n\t" \
    "s_cbranch_scc1 hh_E1_%=\n\t" \
    "s_add_u32 s2, s2, s4\n\t" \
    "s_add_u32 s6, s6, s8\n\t" \
    "s_add_u32 s10, s10, s12\n\t" \
    "s_add_u32 s14, s14, s16\n\t" \
    "s_add_u32 s18, s18, s20\n\t" \
    "s_add_u32 s22, s22, s24\n\t" \
    "s_add_u32 s2, s2, s6\n\t" \
    "s_add_u32 s10, s10, s14\n\t" \
    "s_add_u32 s18, s18, s22\n\t" \
    "s_add_u32 s2, s2, s26\n\t" \
    "s_add_u32 s10, s10, s18\n\t" \
    "s_add_u32 s2, s2, s10\n\t" \
    "s_mul_i32 s70, s28, 13\n\t" \
    "s_sub_u32 s70, s70, s2\n\t" \
    "s_min_u32 s70, s70, 52\n\t" \
    "s_mul_i32 s69, s69, s70\n\t" \
    "s_mul_hi_u32 s69, s69, 76695844\n\t" \
    "s_mov_b32 s75, s73\n\t" \
    "s_mov_b32 s76, s74\n\t" \
    "s_add_u32 s73, s56, s69\n\t" \
    "s_lshl_b32 s74, s28, 4\n\t" \
    "v_mbcnt_lo_u32_b32 v30, -1, 0\n\t" \
    "v_mbcnt_hi_u32_b32 v30, -1, v30\n\t" \
    "v_lshlrev_b32 v30, 6, v30\n\t" \
    "v_add_u32 v30, 6225920, v30\n\t" \
    "v_mov_b32 v31, 6234112\n\t" \
    "global_load_dword v31, v31, s[36:37] sc1\n\t" \
    "global_load_dwordx2 v[32:33], v30, s[36:37] sc1\n\t" \
    "global_load_dword v34, v30, s[36:37] offset:2048 sc1\n\t" \
    "s_waitcnt vmcnt(0)\n\t" \
    "hh_C2_%=:\n\t" \
    "v_cmp_ne_u32 s[54:55], v32, v34\n\t" \
    "v_sub_u32 v32, v32, v31\n\t" \
    "v_cmp_ne_u32 vcc, 1, v32\n\t" \
    "s_nop 1\n\t" \
    "s_or_b64 vcc, vcc, s[54:55]\n\t" \
    "s_cbranch_scc0 hh_Cok3_%=\n\t" \
    "s_cmp_eq_u32 s68, 0\n\t" \
    "s_cbranch_scc1 hh_F5_%=\n\t" \
    "s_sub_u32 s68, s68, 1\n\t" \
    "s_sleep 6\n\t" \
    "s_branch hh_Cag4_%=\n\t" \
    "hh_F5_%=:\n\t" \
    "v_mov_b32 v34, 5767424\n\t" \
    "v_mov_b32 v35, 4\n\t" \
    "global_store_dword v34, v35, s[36:37] sc1\n\t" \
    "s_waitcnt vmcnt(0)\n\t" \
    "s_branch hh_Cok3_%=\n\t" \
    "hh_Cag4_%=:\n\t" \
    "global_load_dwordx2 v[32:33], v30, s[36:37] sc1\n\t" \
    "global_load_dword v34, v30, s[36:37] offset:2048 sc1\n\t" \
    "s_waitcnt vmcnt(0)\n\t" \
    "s_branch hh_C2_%=\n\t" \
    "hh_Cok3_%=:\n\t" \
    "v_mov_b32_dpp v30, v33 quad_perm:[1,0,3,2] row_mask:0xf bank_mask:0xf\n\t" \
    "v_cmp_lt_u32 s[44:45], v30, v33\n\t" \
    "s_nop 1\n\t" \
    "s_bitcmp1_b32 s44, 0\n\t" \
    "s_cselect_b32 s62, 8192, 0\n\t" \
    "s_bitcmp1_b32 s44, 2\n\t" \
    "s_cselect_b32 s63, 8192, 0\n\t" \
    "s_or_b32 s63, s63, 0x200000\n\t" \
    "v_min_u32 v30, v30, v33\n\t" \
    "v_cmp_lt_u32 vcc, 660, v30\n\t" \
    "s_nop 1\n\t" \
    "s_bcnt1_i32_b64 s69, vcc\n\t" \
    "s_cmp_gt_u32 s69, 16\n\t" \
    "s_cselect_b32 s69, 1, 0\n\t" \
    "s_mul_i32 s70, s69, 160\n\t" \
    "s_add_u32 s86, s86, s70\n\t" \
    "s_mul_i32 s70, s69, 536\n\t" \
    "s_add_u32 s87, s87, s70\n\t" \
    "s_mul_i32 s70, s69, 256\n\t" \
    "s_add_u32 s88, s88, s70\n\t" \
    "s_waitcnt lgkmcnt(0)\n\t" \
    "s_mov_b32 s70, 116\n\t" \
    "hh_E6_%=:\n\t" \
    "s_memtime s[56:57]\n\t" \
    "s_memrealtime s[2:3]\n\t" \
    "s_nop 0\n\t" \
    "s_memrealtime s[4:5]\n\t" \
    "s_nop 0\n\t" \
    "s_memrealtime s[6:7]\n\t" \
    "s_nop 0\n\t" \
    "s_memrealtime s[8:9]\n\t" \
    "s_nop 0\n\t" \
    "s_memrealtime s[10:11]\n\t" \
    "s_nop 0\n\t" \
    "s_memrealtime s[12:13]\n\t" \
    "s_nop 0\n\t" \
    "s_memrealtime s[14:15]\n\t" \
    "s_nop 0\n\t" \
    "s_memrealtime s[16:17]\n\t" \
    "s_nop 0\n\t" \
    "s_memrealtime s[18:19]\n\t" \
    "s_nop 0\n\t" \
    "s_memrealtime s[20:21]\n\t" \
    "s_nop 0\n\t" \
    "s_memrealtime s[22:23]\n\t" \
    "s_nop 0\n\t" \
    "s_memrealtime s[24:25]\n\t" \
    "s_nop 0\n\t" \
    "s_memrealtime s[26:27]\n\t" \
    "s_nop 0\n\t" \
    "s_memrealtime s[28:29]\n\t" \
    "s_memtime s[54:55]\n\t" \
    "s_add_u32 s70, s70, 8\n\t" \
    "s_waitcnt lgkmcnt(0)\n\t" \
    "s_sub_u32 s69, s54, s56\n\t" \
    "s_cmp_gt_u32 s69, s70\n\t" \
    "s_cbranch_scc1 hh_E6_%=\n\t" \
    "s_add_u32 s2, s2, s4\n\t" \
    "s_add_u32 s6, s6, s8\n\t" \
    "s_add_u32 s10, s10, s12\n\t" \
    "s_add_u32 s14, s14, s16\n\t" \
    "s_add_u32 s18, s18, s20\n\t" \
    "s_add_u32 s22, s22, s24\n\t" \
    "s_add_u32 s2, s2, s6\n\t" \
    "s_add_u32 s10, s10, s14\n\t" \
    "s_add_u32 s18, s18, s22\n\t" \
    "s_add_u32 s2, s2, s26\n\t" \
    "s_add_u32 s10, s10, s18\n\t" \
    "s_add_u32 s2, s2, s10\n\t" \
    "s_mul_i32 s70, s28, 13\n\t" \
    "s_sub_u32 s70, s70, s2\n\t" \
    "s_min_u32 s70, s70, 52\n\t" \
    "s_mul_i32 s69, s69, s70\n\t" \
    "s_mul_hi_u32 s69, s69, 76695844\n\t" \
    "s_mov_b32 s75, s73\n\t" \
    "s_mov_b32 s76, s74\n\t" \
    "s_add_u32 s73, s56, s69\n\t" \
    "s_lshl_b32 s74, s28, 4\n\t" \
    "s_sub_u32 s69, s73, s75\n\t" \
    "s_sub_u32 s70, s74, s76\n\t" \
    "s_sub_u32 s71, s70, 1600\n\t" \
    "s_cmp_lt_u32 s71, 400000\n\t" \
    "s_cbranch_scc0 hh_K7_%=\n\t" \
    "v_cvt_f32_u32 v30, s69\n\t" \
    "v_cvt_f32_u32 v31, s70\n\t" \
    "v_rcp_f32 v32, v30\n\t" \
    "v_rcp_f32 v33, v31\n\t" \
    "v_mul_f32 v32, v32, v31\n\t" \
    "v_mul_f32 v33, v33, v30\n\t" \
    "v_mul_f32 v32, 0x4f800000, v32\n\t" \
    "v_mul_f32 v33, 0x4f000000, v33\n\t" \
    "v_cvt_u32_f32 v32, v32\n\t" \
    "v_cvt_u32_f32 v33, v33\n\t" \
    "v_readfirstlane_b32 s69, v32\n\t" \
    "v_readfirstlane_b32 s70, v33\n\t" \
    "s_nop 1\n\t" \
    "s_mov_b32 s78, s69\n\t" \
    "s_mov_b32 s77, s70\n\t" \
    "hh_K7_%=:\n\t" \
    "s_sub_u32 s80, s74, 32000\n\t" \
    "v_mov_b32 v38, -1\n\t" \
    "v_mov_b32 v39, -1\n\t" \
    "s_mov_b32 m0, 155648\n\t" \
    "s_mul_i32 s96, s67, 16\n\t" \
    "s_add_u32 s96, s96, 64\n\t" \
    "hh_loop8_%=:\n\t" \
    "s_and_b32 s69, s65, 7\n\t" \
    "s_lshl_b32 s69, s69, 18\n\t" \
    "s_add_u32 s69, s69, s63\n\t" \
    "s_add_u32 s52, s36, s69\n\t" \
    "s_addc_u32 s53, s37, 0\n\t" \
    "s_add_u32 s71, s80, 608\n\t" \
    "s_sub_u32 s70, s71, s74\n\t" \
    "s_max_i32 s70, s70, 0\n\t" \
    "s_min_i32 s70, s70, 16000\n\t" \
    "s_lshl_b32 s70, s70, 1\n\t" \
    "s_mul_hi_u32 s70, s70, s77\n\t" \
    "s_add_u32 s70, s70, s73\n\t" \
    "hh_W11_%=:\n\t" \
    "s_memtime s[56:57]\n\t" \
    "s_waitcnt lgkmcnt(0)\n\t" \
    "s_sub_u32 s69, s70, s56\n\t" \
    "s_cmp_gt_i32 s69, 24\n\t" \
    "s_cbranch_scc1 hh_W11_%=\n\t" \
    "global_load_dword v30, v1, s[52:53] sc1\n\t" \
    "s_waitcnt lgkmcnt(0)\n\t" \
    "s_mov_b32 s70, 116\n\t" \
    "hh_E15_%=:\n\t" \
    "s_memtime s[56:57]\n\t" \
    "s_memrealtime s[2:3]\n\t" \
    "s_nop 0\n\t" \
    "s_memrealtime s[4:5]\n\t" \
    "s_nop 0\n\t" \
    "s_memrealtime s[6:7]\n\t" \
    "s_nop 0\n\t" \
    "s_memrealtime s[8:9]\n\t" \
    "s_nop 0\n\t" \
    "s_memrealtime s[10:11]\n\t" \
    "s_nop 0\n\t" \
    "s_memrealtime s[12:13]\n\t" \
    "s_nop 0\n\t" \
    "s_memrealtime s[14:15]\n\t" \
    "s_nop 0\n\t" \
    "s_memrealtime s[16:17]\n\t" \
    "s_nop 0\n\t" \
    "s_memrealtime s[18:19]\n\t" \
    "s_nop 0\n\t" \
    "s_memrealtime s[20:21]\n\t" \
    "s_nop 0\n\t" \
    "s_memrealtime s[22:23]\n\t" \
    "s_nop 0\n\t" \
    "s_memrealtime s[24:25]\n\t" \
    "s_nop 0\n\t" \
    "s_memrealtime s[26:27]\n\t" \
    "s_nop 0\n\t" \
    "s_memrealtime s[28:29]\n\t" \
    "s_memtime s[54:55]\n\t" \
    "s_add_u32 s70, s70, 8\n\t" \
    "s_waitcnt lgkmcnt(0)\n\t" \
    "s_sub_u32 s69, s54, s56\n\t" \
    "s_cmp_gt_u32 s69, s70\n\t" \
    "s_cbranch_scc1 hh_E15_%=\n\t" \
    "s_add_u32 s2, s2, s4\n\t" \
    "s_add_u32 s6, s6, s8\n\t" \
    "s_add_u32 s10, s10, s12\n\t" \
    "s_add_u32 s14, s14, s16\n\t" \
    "s_add_u32 s18, s18, s20\n\t" \
    "s_add_u32 s22, s22, s24\n\t" \
    "s_add_u32 s2, s2, s6\n\t" \
    "s_add_u32 s10, s10, s14\n\t" \
    "s_add_u32 s18, s18, s22\n\t" \
    "s_add_u32 s2, s2, s26\n\t" \
    "s_add_u32 s10, s10, s18\n\t" \
    "s_add_u32 s2, s2, s10\n\t" \
    "s_mul_i32 s70, s28, 13\n\t" \
    "s_sub_u32 s70, s70, s2\n\t" \
    "s_min_u32 s70, s70, 52\n\t" \
    "s_mul_i32 s69, s69, s70\n\t" \
    "s_mul_hi_u32 s69, s69, 76695844\n\t" \
    "s_mov_b32 s75, s73\n\t" \
    "s_mov_b32 s76, s74\n\t" \
    "s_add_u32 s73, s56, s69\n\t" \
    "s_lshl_b32 s74, s28, 4\n\t" \
    "s_sub_u32 s69, s73, s75\n\t" \
    "s_sub_u32 s70, s74, s76\n\t" \
    "s_sub_u32 s71, s70, 1600\n\t" \
    "s_cmp_lt_u32 s71, 400000\n\t" \
    "s_cbranch_scc0 hh_K16_%=\n\t" \
    "v_cvt_f32_u32 v34, s69\n\t" \
    "v_cvt_f32_u32 v35, s70\n\t" \
    "v_rcp_f32 v36, v34\n\t" \
    "v_rcp_f32 v37, v35\n\t" \
    "v_mul_f32 v36, v36, v35\n\t" \
    "v_mul_f32 v37, v37, v34\n\t" \
    "v_mul_f32 v36, 0x4f800000, v36\n\t" \
    "v_mul_f32 v37, 0x4f000000, v37\n\t" \
    "v_cvt_u32_f32 v36, v36\n\t" \
    "v_cvt_u32_f32 v37, v37\n\t" \
    "v_readfirstlane_b32 s69, v36\n\t" \
    "v_readfirstlane_b32 s70, v37\n\t" \
    "s_nop 1\n\t" \
    "s_sub_u32 s69, s69, s78\n\t" \
    "s_ashr_i32 s69, s69, 1\n\t" \
    "s_add_u32 s78, s78, s69\n\t" \
    "s_sub_u32 s70, s70, s77\n\t" \
    "s_ashr_i32 s70, s70, 1\n\t" \
    "s_add_u32 s77, s77, s70\n\t" \
    "hh_K16_%=:\n\t" \
    "s_waitcnt vmcnt(0)\n\t" \
    "v_cmp_eq_u32 vcc, -1, v30\n\t" \
    "s_cbranch_vccz hh_Hok13_%=\n\t" \
    "hh_H12_%=:\n\t" \
    "global_load_dword v30, v1, s[52:53] sc1\n\t" \
    "s_waitcnt vmcnt(0)\n\t" \
    "v_cmp_eq_u32 vcc, -1, v30\n\t" \
    "s_cbranch_vccz hh_Hc14_%=\n\t" \
    "s_cmp_eq_u32 s68, 0\n\t" \
    "s_cbranch_scc1 hh_F17_%=\n\t" \
    "s_sub_u32 s68, s68, 1\n\t" \
    "s_sleep 2\n\t" \
    "s_branch hh_H12_%=\n\t" \
    "hh_F17_%=:\n\t" \
    "v_mov_b32 v31, 5767424\n\t" \
    "v_mov_b32 v32, 14\n\t" \
    "global_store_dword v31, v32, s[36:37] sc1\n\t" \
    "s_waitcnt vmcnt(0)\n\t" \
    "s_branch hh_Hc14_%=\n\t" \
    "hh_Hc14_%=:\n\t" \
    "s_waitcnt lgkmcnt(0)\n\t" \
    "s_mov_b32 s70, 116\n\t" \
    "hh_E18_%=:\n\t" \
    "s_memtime s[56:57]\n\t" \
    "s_memrealtime s[2:3]\n\t" \
    "s_nop 0\n\t" \
    "s_memrealtime s[4:5]\n\t" \
    "s_nop 0\n\t" \
    "s_memrealtime s[6:7]\n\t" \
    "s_nop 0\n\t" \
    "s_memrealtime s[8:9]\n\t" \
    "s_nop 0\n\t" \
    "s_memrealtime s[10:11]\n\t" \
    "s_nop 0\n\t" \
    "s_memrealtime s[12:13]\n\t" \
    "s_nop 0\n\t" \
    "s_memrealtime s[14:15]\n\t" \
    "s_nop 0\n\t" \
    "s_memrealtime s[16:17]\n\t" \
    "s_nop 0\n\t" \
    "s_memrealtime s[18:19]\n\t" \
    "s_nop 0\n\t" \
    "s_memrealtime s[20:21]\n\t" \
    "s_nop 0\n\t" \
    "s_memrealtime s[22:23]\n\t" \
    "s_nop 0\n\t" \
    "s_memrealtime s[24:25]\n\t" \
    "s_nop 0\n\t" \
    "s_memrealtime s[26:27]\n\t" \
    "s_nop 0\n\t" \
    "s_memrealtime s[28:29]\n\t" \
    "s_memtime s[54:55]\n\t" \
    "s_add_u32 s70, s70, 8\n\t" \
    "s_waitcnt lgkmcnt(0)\n\t" \
    "s_sub_u32 s69, s54, s56\n\t" \
    "s_cmp_gt_u32 s69, s70\n\t" \
    "s_cbranch_scc1 hh_E18_%=\n\t" \
    "s_add_u32 s2, s2, s4\n\t" \
    "s_add_u32 s6, s6, s8\n\t" \
    "s_add_u32 s10, s10, s12\n\t" \
    "s_add_u32 s14, s14, s16\n\t" \
    "s_add_u32 s18, s18, s20\n\t" \
    "s_add_u32 s22, s22, s24\n\t" \
    "s_add_u32 s2, s2, s6\n\t" \
    "s_add_u32 s10, s10, s14\n\t" \
    "s_add_u32 s18, s18, s22\n\t" \
    "s_add_u32 s2, s2, s26\n\t" \
    "s_add_u32 s10, s10, s18\n\t" \
    "s_add_u32 s2, s2, s10\n\t" \
    "s_mul_i32 s70, s28, 13\n\t" \
    "s_sub_u32 s70, s70, s2\n\t" \
    "s_min_u32 s70, s70, 52\n\t" \
    "s_mul_i32 s69, s69, s70\n\t" \
    "s_mul_hi_u32 s69, s69, 76695844\n\t" \
    "s_mov_b32 s75, s73\n\t" \
    "s_mov_b32 s76, s74\n\t" \
    "s_add_u32 s73, s56, s69\n\t" \
    "s_lshl_b32 s74, s28, 4\n\t" \
    "s_sub_u32 s69, s73, s75\n\t" \
    "s_sub_u32 s70, s74, s76\n\t" \
    "s_sub_u32 s71, s70, 1600\n\t" \
    "s_cmp_lt_u32 s71, 400000\n\t" \
    "s_cbranch_scc0 hh_K19_%=\n\t" \
    "v_cvt_f32_u32 v34, s69\n\t" \
    "v_cvt_f32_u32 v35, s70\n\t" \
    "v_rcp_f32 v36, v34\n\t" \
    "v_rcp_f32 v37, v35\n\t" \
    "v_mul_f32 v36, v36, v35\n\t" \
    "v_mul_f32 v37, v37, v34\n\t" \
    "v_mul_f32 v36, 0x4f800000, v36\n\t" \
    "v_mul_f32 v37, 0x4f000000, v37\n\t" \
    "v_cvt_u32_f32 v36, v36\n\t" \
    "v_cvt_u32_f32 v37, v37\n\t" \
    "v_readfirstlane_b32 s69, v36\n\t" \
    "v_readfirstlane_b32 s70, v37\n\t" \
    "s_nop 1\n\t" \
    "s_sub_u32 s69, s69, s78\n\t" \
    "s_ashr_i32 s69, s69, 1\n\t" \
    "s_add_u32 s78, s78, s69\n\t" \
    "s_sub_u32 s70, s70, s77\n\t" \
    "s_ashr_i32 s70, s70, 1\n\t" \
    "s_add_u32 s77, s77, s70\n\t" \
    "hh_K19_%=:\n\t" \
    "hh_Hok13_%=:\n\t" \
    "v_max_u32_dpp v30, v30, v30 row_ror:1 row_mask:0xf bank_mask:0xf\n\t" \
    "s_nop 1\n\t" \
    "v_max_u32_dpp v30, v30, v30 row_ror:2 row_mask:0xf bank_mask:0xf\n\t" \
    "s_nop 1\n\t" \
    "v_max_u32_dpp v30, v30, v30 row_ror:4 row_mask:0xf bank_mask:0xf\n\t" \
    "s_nop 1\n\t" \
    "v_max_u32_dpp v30, v30, v30 row_ror:8 row_mask:0xf bank_mask:0xf\n\t" \
    "s_nop 0\n\t" \
    "v_readlane_b32 s69, v30, 0\n\t" \
    "v_readlane_b32 s71, v30, 16\n\t" \
    "s_nop 1\n\t" \
    "s_max_u32 s81, s69, s71\n\t" \
    "s_mul_hi_u32 s69, s85, s78\n\t" \
    "s_add_u32 s79, s81, s69\n\t" \
    "s_add_u32 s79, s79, s87\n\t" \
    "s_cmp_ge_u32 s65, 52\n\t" \
    "s_cselect_b32 s69, 32, 0\n\t" \
    "s_sub_u32 s79, s79, s69\n\t" \
    "s_cmp_lt_u32 s65, 9\n\t" \
    "s_cselect_b32 s69, 48, 0\n\t" \
    "s_add_u32 s79, s79, s69\n\t" \
    "s_add_u32 s80, s79, s88\n\t" \
    "s_add_u32 s65, s65, 1\n\t" \
    "s_cmp_gt_u32 s65, 62\n\t" \
    "s_cbranch_scc1 hh_end10_%=\n\t" \
    "s_cmp_le_u32 s65, 6\n\t" \
    "s_cbranch_scc1 hh_loop8_%=\n\t" \
    "v_lshrrev_b32 v20, 15, v11\n\t" \
    "v_add3_u32 v20, v20, s65, 1\n\t" \
    "v_min_u32 v20, 63, v20\n\t" \
    "v_mul_u32_u24 v20, v20, v11\n\t" \
    "v_xor_b32 v20, v20, v4\n\t" \
    "v_lshrrev_b32 v21, 15, v12\n\t" \
    "v_add3_u32 v21, v21, s65, 1\n\t" \
    "v_min_u32 v21, 63, v21\n\t" \
    "v_mul_u32_u24 v21, v21, v12\n\t" \
    "v_xor_b32 v21, v21, v5\n\t" \
    "v_lshrrev_b32 v22, 15, v13\n\t" \
    "v_add3_u32 v22, v22, s65, 1\n\t" \
    "v_min_u32 v22, 63, v22\n\t" \
    "v_mul_u32_u24 v22, v22, v13\n\t" \
    "v_xor_b32 v22, v22, v6\n\t" \
    "v_lshrrev_b32 v23, 15, v14\n\t" \
    "v_add3_u32 v23, v23, s65, 1\n\t" \
    "v_min_u32 v23, 63, v23\n\t" \
    "v_mul_u32_u24 v23, v23, v14\n\t" \
    "v_xor_b32 v23, v23, v7\n\t" \
    "v_lshrrev_b32 v24, 15, v15\n\t" \
    "v_add3_u32 v24, v24, s65, 1\n\t" \
    "v_min_u32 v24, 63, v24\n\t" \
    "v_mul_u32_u24 v24, v24, v15\n\t" \
    "v_xor_b32 v24, v24, v8\n\t" \
    "v_lshrrev_b32 v25, 15, v16\n\t" \
    "v_add3_u32 v25, v25, s65, 1\n\t" \
    "v_min_u32 v25, 63, v25\n\t" \
    "v_mul_u32_u24 v25, v25, v16\n\t" \
    "v_xor_b32 v25, v25, v9\n\t" \
    "v_lshrrev_b32 v26, 15, v17\n\t" \
    "v_add3_u32 v26, v26, s65, 1\n\t" \
    "v_min_u32 v26, 63, v26\n\t" \
    "v_mul_u32_u24 v26, v26, v17\n\t" \
    "v_xor_b32 v26, v26, v10\n\t" \
    "s_add_u32 s71, s79, s96\n\t" \
    "s_sub_u32 s70, s71, s74\n\t" \
    "s_max_i32 s70, s70, 0\n\t" \
    "s_min_i32 s70, s70, 16000\n\t" \
    "s_lshl_b32 s70, s70, 1\n\t" \
    "s_mul_hi_u32 s70, s70, s77\n\t" \
    "s_add_u32 s70, s70, s73\n\t" \
    "s_getpc_b64 s[30:31]\n\t" \
    "hh_Wp22_%=:\n\t" \
    "s_add_u32 s30, s30, hh_We23_%=-hh_Wp22_%=\n\t" \
    "s_addc_u32 s31, s31, 0\n\t" \
    "s_sub_u32 s70, s70, 96\n\t" \
    "hh_W21_%=:\n\t" \
    "s_memtime s[56:57]\n\t" \
    "s_waitcnt lgkmcnt(0)\n\t" \
    "s_sub_u32 s69, s70, s56\n\t" \
    "s_cmp_gt_i32 s69, 80\n\t" \
    "s_cbranch_scc1 hh_W21_%=\n\t" \
    "s_max_i32 s69, s69, 0\n\t" \
    "s_and_b32 s69, s69, -4\n\t" \
    "s_sub_u32 s30, s30, s69\n\t" \
    "s_subb_u32 s31, s31, 0\n\t" \
    "s_setpc_b64 s[30:31]\n\t" \
    "s_nop 0\n\t" \
    "s_nop 0\n\t" \
    "s_nop 0\n\t" \
    "s_nop 0\n\t" \
    "s_nop 0\n\t" \
    "s_nop 0\n\t" \
    "s_nop 0\n\t" \
    "s_nop 0\n\t" \
    "s_nop 0\n\t" \
    "s_nop 0\n\t" \
    "s_nop 0\n\t" \
    "s_nop 0\n\t" \
    "s_nop 0\n\t" \
    "s_nop 0\n\t" \
    "s_nop 0\n\t" \
    "s_nop 0\n\t" \
    "s_nop 0\n\t" \
    "s_nop 0\n\t" \
    "s_nop 0\n\t" \
    "s_nop 0\n\t" \
    "hh_We23_%=:\n\t" \
    "global_load_lds_dword v26, s[36:37] sc1\n\t" \
    "s_sleep 2\n\t" \
    "s_nop 5\n\t" \
    "global_load_lds_dword v25, s[36:37] sc1\n\t" \
    "s_sleep 2\n\t" \
    "s_nop 5\n\t" \
    "global_load_lds_dword v24, s[36:37] sc1\n\t" \
    "s_sleep 2\n\t" \
    "s_nop 5\n\t" \
    "global_load_lds_dword v23, s[36:37] sc1\n\t" \
    "s_sleep 2\n\t" \
    "s_nop 5\n\t" \
    "global_load_lds_dword v22, s[36:37] sc1\n\t" \
    "s_sleep 2\n\t" \
    "s_nop 5\n\t" \
    "global_load_lds_dword v21, s[36:37] sc1\n\t" \
    "s_sleep 2\n\t" \
    "s_nop 5\n\t" \
    "global_load_lds_dword v20, s[36:37] sc1\n\t" \
    "s_branch hh_loop8_%=\n\t" \
    "hh_end10_%=:\n\t" \
    "s_waitcnt vmcnt(0)\n\t" \
    "s_endpgm\n\t" \
    "hh_last_%=:\n\t" \
    ""
#define ASM_GX \
    "s_load_dwordx2 s[42:43], %3, 48\n\t" \
    "s_load_dwordx2 s[44:45], %3, 40\n\t" \
    "s_load_dwordx2 s[36:37], %3, 0x58\n\t" \
    "s_load_dwordx2 s[50:51], %3, 0x48\n\t" \
    "s_mov_b32 s66, %4\n\t" \
    "s_mov_b32 s67, %5\n\t" \
    "s_mov_b32 s68, 262144\n\t" \
    "s_mov_b32 s72, 0\n\t" \
    "s_mov_b32 s83, -1\n\t" \
    "s_mov_b32 s65, 0\n\t" \
    "s_lshl_b32 s82, s66, 10\n\t" \
    "s_waitcnt lgkmcnt(0)\n\t" \
    "s_mul_i32 s89, s66, 9216\n\t" \
    "s_mov_b32 s90, 0\n\t" \
    "s_add_u32 s46, s42, 2097152\n\t" \
    "s_addc_u32 s47, s43, 0\n\t" \
    "s_add_u32 m0, s82, 65536\n\t" \
    "s_nop 0\n\t" \
    "global_load_lds_dwordx4 v200, s[46:47]\n\t" \
    "s_add_u32 s46, s42, 2228224\n\t" \
    "s_addc_u32 s47, s43, 0\n\t" \
    "s_add_u32 m0, s82, 69632\n\t" \
    "s_nop 0\n\t" \
    "global_load_lds_dwordx4 v200, s[46:47]\n\t" \
    "s_add_u32 s46, s42, 2359296\n\t" \
    "s_addc_u32 s47, s43, 0\n\t" \
    "s_add_u32 m0, s82, 73728\n\t" \
    "s_nop 0\n\t" \
    "global_load_lds_dwordx4 v200, s[46:47]\n\t" \
    "s_add_u32 s46, s42, 2490368\n\t" \
    "s_addc_u32 s47, s43, 0\n\t" \
    "s_add_u32 m0, s82, 77824\n\t" \
    "s_nop 0\n\t" \
    "global_load_lds_dwordx4 v200, s[46:47]\n\t" \
    "s_add_u32 s46, s42, 2621440\n\t" \
    "s_addc_u32 s47, s43, 0\n\t" \
    "s_add_u32 m0, s82, 81920\n\t" \
    "s_nop 0\n\t" \
    "global_load_lds_dwordx4 v200, s[46:47]\n\t" \
    "s_add_u32 s46, s42, 2752512\n\t" \
    "s_addc_u32 s47, s43, 0\n\t" \
    "s_add_u32 m0, s82, 86016\n\t" \
    "s_nop 0\n\t" \
    "global_load_lds_dwordx4 v200, s[46:47]\n\t" \
    "s_add_u32 s46, s42, 2883584\n\t" \
    "s_addc_u32 s47, s43, 0\n\t" \
    "s_add_u32 m0, s82, 90112\n\t" \
    "s_nop 0\n\t" \
    "global_load_lds_dwordx4 v200, s[46:47]\n\t" \
    "s_add_u32 s46, s42, 3014656\n\t" \
    "s_addc_u32 s47, s43, 0\n\t" \
    "s_add_u32 m0, s82, 94208\n\t" \
    "s_nop 0\n\t" \
    "global_load_lds_dwordx4 v200, s[46:47]\n\t" \
    "s_add_u32 s46, s42, 3145728\n\t" \
    "s_addc_u32 s47, s43, 0\n\t" \
    "s_add_u32 m0, s82, 98304\n\t" \
    "s_nop 0\n\t" \
    "global_load_lds_dwordx4 v200, s[46:47]\n\t" \
    "s_add_u32 s46, s42, 3276800\n\t" \
    "s_addc_u32 s47, s43, 0\n\t" \
    "s_add_u32 m0, s82, 102400\n\t" \
    "s_nop 0\n\t" \
    "global_load_lds_dwordx4 v200, s[46:47]\n\t" \
    "s_add_u32 s46, s42, 3407872\n\t" \
    "s_addc_u32 s47, s43, 0\n\t" \
    "s_add_u32 m0, s82, 106496\n\t" \
    "s_nop 0\n\t" \
    "global_load_lds_dwordx4 v200, s[46:47]\n\t" \
    "s_add_u32 s46, s42, 3538944\n\t" \
    "s_addc_u32 s47, s43, 0\n\t" \
    "s_add_u32 m0, s82, 110592\n\t" \
    "s_nop 0\n\t" \
    "global_load_lds_dwordx4 v200, s[46:47]\n\t" \
    "s_add_u32 s46, s42, 3670016\n\t" \
    "s_addc_u32 s47, s43, 0\n\t" \
    "s_add_u32 m0, s82, 114688\n\t" \
    "s_nop 0\n\t" \
    "global_load_lds_dwordx4 v200, s[46:47]\n\t" \
    "s_add_u32 s46, s42, 3801088\n\t" \
    "s_addc_u32 s47, s43, 0\n\t" \
    "s_add_u32 m0, s82, 118784\n\t" \
    "s_nop 0\n\t" \
    "global_load_lds_dwordx4 v200, s[46:47]\n\t" \
    "s_add_u32 s46, s42, 3932160\n\t" \
    "s_addc_u32 s47, s43, 0\n\t" \
    "s_add_u32 m0, s82, 122880\n\t" \
    "s_nop 0\n\t" \
    "global_load_lds_dwordx4 v200, s[46:47]\n\t" \
    "s_add_u32 s46, s42, 4063232\n\t" \
    "s_addc_u32 s47, s43, 0\n\t" \
    "s_add_u32 m0, s82, 126976\n\t" \
    "s_nop 0\n\t" \
    "global_load_lds_dwordx4 v200, s[46:47]\n\t" \
    "s_add_u32 s46, s42, 4194304\n\t" \
    "s_addc_u32 s47, s43, 0\n\t" \
    "s_add_u32 m0, s82, 131072\n\t" \
    "s_nop 0\n\t" \
    "global_load_lds_dwordx4 v200, s[46:47]\n\t" \
    "s_add_u32 s46, s42, 4325376\n\t" \
    "s_addc_u32 s47, s43, 0\n\t" \
    "s_add_u32 m0, s82, 135168\n\t" \
    "s_nop 0\n\t" \
    "global_load_lds_dwordx4 v200, s[46:47]\n\t" \
    "s_add_u32 s46, s42, 4456448\n\t" \
    "s_addc_u32 s47, s43, 0\n\t" \
    "s_add_u32 m0, s82, 139264\n\t" \
    "s_nop 0\n\t" \
    "global_load_lds_dwordx4 v200, s[46:47]\n\t" \
    "s_add_u32 s46, s42, 4587520\n\t" \
    "s_addc_u32 s47, s43, 0\n\t" \
    "s_add_u32 m0, s82, 143360\n\t" \
    "s_nop 0\n\t" \
    "global_load_lds_dwordx4 v200, s[46:47]\n\t" \
    "global_load_dwordx4 v[120:123], v203, s[50:51]\n\t" \
    "global_load_dwordx4 v[124:127], v204, s[50:51]\n\t" \
    "global_load_dwordx4 v[128:131], v205, s[50:51]\n\t" \
    "global_load_dwordx4 v[132:135], v206, s[50:51]\n\t" \
    "global_load_dwordx4 v[136:139], v207, s[50:51]\n\t" \
    "global_load_dwordx4 v[140:143], v208, s[50:51]\n\t" \
    "global_load_dwordx4 v[144:147], v209, s[50:51]\n\t" \
    "global_load_dwordx4 v[148:151], v210, s[50:51]\n\t" \
    "global_load_dwordx4 v[152:155], v211, s[50:51]\n\t" \
    "s_add_u32 s52, s50, 36864\n\t" \
    "s_addc_u32 s53, s51, 0\n\t" \
    "global_load_dwordx4 v[72:75], v203, s[52:53]\n\t" \
    "global_load_dwordx4 v[76:79], v204, s[52:53]\n\t" \
    "global_load_dwordx4 v[80:83], v205, s[52:53]\n\t" \
    "global_load_dwordx4 v[84:87], v206, s[52:53]\n\t" \
    "global_load_dwordx4 v[88:91], v207, s[52:53]\n\t" \
    "global_load_dwordx4 v[92:95], v208, s[52:53]\n\t" \
    "global_load_dwordx4 v[96:99], v209, s[52:53]\n\t" \
    "global_load_dwordx4 v[100:103], v210, s[52:53]\n\t" \
    "global_load_dwordx4 v[104:107], v211, s[52:53]\n\t" \
    "s_waitcnt vmcnt(50) lgkmcnt(0)\n\t" \
    "s_barrier\n\t" \
    "ds_read_b64_tr_b16 a[144:145], v201 offset:0\n\t" \
    "ds_read_b64_tr_b16 a[146:147], v201 offset:1024\n\t" \
    "ds_read_b64_tr_b16 a[148:149], v201 offset:8192\n\t" \
    "ds_read_b64_tr_b16 a[150:151], v201 offset:9216\n\t" \
    "ds_read_b64_tr_b16 a[216:217], v202 offset:0\n\t" \
    "ds_read_b64_tr_b16 a[218:219], v202 offset:1024\n\t" \
    "ds_read_b64_tr_b16 a[220:221], v202 offset:8192\n\t" \
    "ds_read_b64_tr_b16 a[222:223], v202 offset:9216\n\t" \
    "s_waitcnt vmcnt(46) lgkmcnt(0)\n\t" \
    "s_barrier\n\t" \
    "ds_read_b64_tr_b16 a[152:153], v201 offset:16384\n\t" \
    "ds_read_b64_tr_b16 a[154:155], v201 offset:17408\n\t" \
    "ds_read_b64_tr_b16 a[156:157], v201 offset:24576\n\t" \
    "ds_read_b64_tr_b16 a[158:159], v201 offset:25600\n\t" \
    "ds_read_b64_tr_b16 a[224:225], v202 offset:16384\n\t" \
    "ds_read_b64_tr_b16 a[226:227], v202 offset:17408\n\t" \
    "ds_read_b64_tr_b16 a[228:229], v202 offset:24576\n\t" \
    "ds_read_b64_tr_b16 a[230:231], v202 offset:25600\n\t" \
    "s_waitcnt vmcnt(42) lgkmcnt(0)\n\t" \
    "s_barrier\n\t" \
    "ds_read_b64_tr_b16 a[160:161], v201 offset:32768\n\t" \
    "ds_read_b64_tr_b16 a[162:163], v201 offset:33792\n\t" \
    "ds_read_b64_tr_b16 a[164:165], v201 offset:40960\n\t" \
    "ds_read_b64_tr_b16 a[166:167], v201 offset:41984\n\t" \
    "ds_read_b64_tr_b16 a[232:233], v202 offset:32768\n\t" \
    "ds_read_b64_tr_b16 a[234:235], v202 offset:33792\n\t" \
    "ds_read_b64_tr_b16 a[236:237], v202 offset:40960\n\t" \
    "ds_read_b64_tr_b16 a[238:239], v202 offset:41984\n\t" \
    "s_waitcnt vmcnt(38) lgkmcnt(0)\n\t" \
    "s_barrier\n\t" \
    "ds_read_b64_tr_b16 a[168:169], v201 offset:49152\n\t" \
    "ds_read_b64_tr_b16 a[170:171], v201 offset:50176\n\t" \
    "ds_read_b64_tr_b16 a[172:173], v201 offset:57344\n\t" \
    "ds_read_b64_tr_b16 a[174:175], v201 offset:58368\n\t" \
    "ds_read_b64_tr_b16 a[240:241], v202 offset:49152\n\t" \
    "ds_read_b64_tr_b16 a[242:243], v202 offset:50176\n\t" \
    "ds_read_b64_tr_b16 a[244:245], v202 offset:57344\n\t" \
    "ds_read_b64_tr_b16 a[246:247], v202 offset:58368\n\t" \
    "v_mbcnt_lo_u32_b32 v164, -1, 0\n\t" \
    "v_mbcnt_hi_u32_b32 v164, -1, v164\n\t" \
    "v_lshlrev_b32 v164, 6, v164\n\t" \
    "v_add_u32 v164, 6225920, v164\n\t" \
    "v_mov_b32 v165, 6234112\n\t" \
    "global_load_dword v165, v165, s[36:37] sc1\n\t" \
    "global_load_dwordx2 v[166:167], v164, s[36:37] sc1\n\t" \
    "global_load_dword v168, v164, s[36:37] offset:2048 sc1\n\t" \
    "s_waitcnt vmcnt(37) lgkmcnt(0)\n\t" \
    "s_barrier\n\t" \
    "v_add_u32 v201, 0x10000, v201\n\t" \
    "v_add_u32 v202, 0x10000, v202\n\t" \
    "ds_read_b64_tr_b16 a[176:177], v201 offset:0\n\t" \
    "ds_read_b64_tr_b16 a[178:179], v201 offset:1024\n\t" \
    "ds_read_b64_tr_b16 a[180:181], v201 offset:8192\n\t" \
    "ds_read_b64_tr_b16 a[182:183], v201 offset:9216\n\t" \
    "ds_read_b64_tr_b16 a[248:249], v202 offset:0\n\t" \
    "ds_read_b64_tr_b16 a[250:251], v202 offset:1024\n\t" \
    "ds_read_b64_tr_b16 a[252:253], v202 offset:8192\n\t" \
    "ds_read_b64_tr_b16 a[254:255], v202 offset:9216\n\t" \
    "s_waitcnt vmcnt(33) lgkmcnt(0)\n\t" \
    "s_barrier\n\t" \
    "ds_read_b64_tr_b16 a[184:185], v201 offset:16384\n\t" \
    "ds_read_b64_tr_b16 a[186:187], v201 offset:17408\n\t" \
    "ds_read_b64_tr_b16 a[188:189], v201 offset:24576\n\t" \
    "ds_read_b64_tr_b16 a[190:191], v201 offset:25600\n\t" \
    "ds_read_b64_tr_b16 v[224:225], v202 offset:16384\n\t" \
    "ds_read_b64_tr_b16 v[226:227], v202 offset:17408\n\t" \
    "ds_read_b64_tr_b16 v[228:229], v202 offset:24576\n\t" \
    "ds_read_b64_tr_b16 v[230:231], v202 offset:25600\n\t" \
    "s_waitcnt vmcnt(29) lgkmcnt(0)\n\t" \
    "s_barrier\n\t" \
    "ds_read_b64_tr_b16 a[192:193], v201 offset:32768\n\t" \
    "ds_read_b64_tr_b16 a[194:195], v201 offset:33792\n\t" \
    "ds_read_b64_tr_b16 a[196:197], v201 offset:40960\n\t" \
    "ds_read_b64_tr_b16 a[198:199], v201 offset:41984\n\t" \
    "ds_read_b64_tr_b16 v[232:233], v202 offset:32768\n\t" \
    "ds_read_b64_tr_b16 v[234:235], v202 offset:33792\n\t" \
    "ds_read_b64_tr_b16 v[236:237], v202 offset:40960\n\t" \
    "ds_read_b64_tr_b16 v[238:239], v202 offset:41984\n\t" \
    "s_waitcnt vmcnt(25) lgkmcnt(0)\n\t" \
    "s_barrier\n\t" \
    "ds_read_b64_tr_b16 a[200:201], v201 offset:49152\n\t" \
    "ds_read_b64_tr_b16 a[202:203], v201 offset:50176\n\t" \
    "ds_read_b64_tr_b16 a[204:205], v201 offset:57344\n\t" \
    "ds_read_b64_tr_b16 a[206:207], v201 offset:58368\n\t" \
    "ds_read_b64_tr_b16 v[240:241], v202 offset:49152\n\t" \
    "ds_read_b64_tr_b16 v[242:243], v202 offset:50176\n\t" \
    "ds_read_b64_tr_b16 v[244:245], v202 offset:57344\n\t" \
    "ds_read_b64_tr_b16 v[246:247], v202 offset:58368\n\t" \
    "s_waitcnt vmcnt(21) lgkmcnt(0)\n\t" \
    "s_barrier\n\t" \
    "v_add_u32 v201, 0x10000, v201\n\t" \
    "v_add_u32 v202, 0x10000, v202\n\t" \
    "ds_read_b64_tr_b16 a[208:209], v201 offset:0\n\t" \
    "ds_read_b64_tr_b16 a[210:211], v201 offset:1024\n\t" \
    "ds_read_b64_tr_b16 a[212:213], v201 offset:8192\n\t" \
    "ds_read_b64_tr_b16 a[214:215], v201 offset:9216\n\t" \
    "ds_read_b64_tr_b16 v[248:249], v202 offset:0\n\t" \
    "ds_read_b64_tr_b16 v[250:251], v202 offset:1024\n\t" \
    "ds_read_b64_tr_b16 v[252:253], v202 offset:8192\n\t" \
    "ds_read_b64_tr_b16 v[254:255], v202 offset:9216\n\t" \
    "v_add_u32 v201, 0xfffe0000, v201\n\t" \
    "v_add_u32 v202, 0xfffe0000, v202\n\t" \
    "s_waitcnt lgkmcnt(0)\n\t" \
    "s_barrier\n\t" \
    "s_waitcnt vmcnt(0)\n\t" \
    "ds_write_b128 v3, v[120:123] offset:0\n\t" \
    "ds_write_b128 v3, v[124:127] offset:1024\n\t" \
    "ds_write_b128 v3, v[128:131] offset:2048\n\t" \
    "ds_write_b128 v3, v[132:135] offset:3072\n\t" \
    "ds_write_b128 v3, v[136:139] offset:4096\n\t" \
    "ds_write_b128 v3, v[140:143] offset:5120\n\t" \
    "ds_write_b128 v3, v[144:147] offset:6144\n\t" \
    "ds_write_b128 v3, v[148:151] offset:7168\n\t" \
    "ds_write_b128 v3, v[152:155] offset:8192\n\t" \
    "ds_write_b128 v3, v[72:75] offset:36864\n\t" \
    "ds_write_b128 v3, v[76:79] offset:37888\n\t" \
    "ds_write_b128 v3, v[80:83] offset:38912\n\t" \
    "ds_write_b128 v3, v[84:87] offset:39936\n\t" \
    "ds_write_b128 v3, v[88:91] offset:40960\n\t" \
    "ds_write_b128 v3, v[92:95] offset:41984\n\t" \
    "ds_write_b128 v3, v[96:99] offset:43008\n\t" \
    "ds_write_b128 v3, v[100:103] offset:44032\n\t" \
    "ds_write_b128 v3, v[104:107] offset:45056\n\t" \
    "gx_C1_%=:\n\t" \
    "v_cmp_ne_u32 s[54:55], v166, v168\n\t" \
    "v_sub_u32 v166, v166, v165\n\t" \
    "v_cmp_ne_u32 vcc, 1, v166\n\t" \
    "s_nop 1\n\t" \
    "s_or_b64 vcc, vcc, s[54:55]\n\t" \
    "s_cbranch_scc0 gx_Cok2_%=\n\t" \
    "s_cmp_eq_u32 s68, 0\n\t" \
    "s_cbranch_scc1 gx_F4_%=\n\t" \
    "s_sub_u32 s68, s68, 1\n\t" \
    "s_sleep 6\n\t" \
    "s_branch gx_Cag3_%=\n\t" \
    "gx_F4_%=:\n\t" \
    "v_mov_b32 v168, 5767424\n\t" \
    "v_mov_b32 v169, 4\n\t" \
    "global_store_dword v168, v169, s[36:37] sc1\n\t" \
    "s_waitcnt vmcnt(0)\n\t" \
    "s_branch gx_Cok2_%=\n\t" \
    "gx_Cag3_%=:\n\t" \
    "global_load_dwordx2 v[166:167], v164, s[36:37] sc1\n\t" \
    "global_load_dword v168, v164, s[36:37] offset:2048 sc1\n\t" \
    "s_waitcnt vmcnt(0)\n\t" \
    "s_branch gx_C1_%=\n\t" \
    "gx_Cok2_%=:\n\t" \
    "s_cmp_lg_u32 s66, 0\n\t" \
    "s_cbranch_scc1 gx_Ca5_%=\n\t" \
    "v_mov_b32 v164, 5836800\n\t" \
    "s_mov_b64 exec, 1\n\t" \
    "global_atomic_add v164, v166, s[36:37] sc1\n\t" \
    "s_mov_b64 exec, -1\n\t" \
    "gx_Ca5_%=:\n\t" \
    "s_waitcnt lgkmcnt(0)\n\t" \
    "s_barrier\n\t" \
    "ds_read_b128 v[24:27], v1 offset:0\n\t" \
    "ds_read_b128 v[28:31], v1 offset:18432\n\t" \
    "ds_read_b128 v[32:35], v2 offset:0\n\t" \
    "ds_read_b128 v[36:39], v2 offset:18432\n\t" \
    "ds_read_b128 v[40:43], v1 offset:128\n\t" \
    "ds_read_b128 v[44:47], v1 offset:18560\n\t" \
    "gx_l16_%=:\n\t" \
    "s_add_u32 s84, s65, 2\n\t" \
    "s_min_u32 s69, s84, 63\n\t" \
    "s_mul_i32 s69, s69, 36864\n\t" \
    "s_add_u32 s52, s50, s69\n\t" \
    "s_addc_u32 s53, s51, 0\n\t" \
    "s_add_u32 s90, s90, 36864\n\t" \
    "s_cmp_eq_u32 s90, 147456\n\t" \
    "s_cselect_b32 s90, 0, s90\n\t" \
    "s_add_u32 s91, s90, 36864\n\t" \
    "s_cmp_eq_u32 s91, 147456\n\t" \
    "s_cselect_b32 s91, 0, s91\n\t" \
    "s_add_u32 s91, s91, s89\n\t" \
    "v_add_u32 v22, s90, v0\n\t" \
    "v_xor_b32 v23, 64, v22\n\t" \
    "s_mul_i32 s70, s67, 7\n\t" \
    "s_add_u32 s69, s65, 0\n\t" \
    "s_xor_b32 s69, s69, s70\n\t" \
    "s_and_b32 s69, s69, 63\n\t" \
    "s_lshl_b32 s69, s69, 13\n\t" \
    "s_mul_i32 s70, s67, 524288\n\t" \
    "s_add_u32 s69, s69, s70\n\t" \
    "s_add_u32 s69, s69, 23068672\n\t" \
    "s_add_u32 s40, s36, s69\n\t" \
    "s_addc_u32 s41, s37, 0\n\t" \
    "s_waitcnt lgkmcnt(5)\n\t" \
    "v_mfma_f32_16x16x32_bf16 v[72:75], a[144:147], v[24:27], 0\n\t" \
    "ds_read_b128 v[48:51], v2 offset:128\n\t" \
    "s_waitcnt lgkmcnt(5)\n\t" \
    "v_mfma_f32_16x16x32_bf16 v[76:79], a[144:147], v[28:31], 0\n\t" \
    "ds_read_b128 v[52:55], v2 offset:18560\n\t" \
    "v_mfma_f32_16x16x32_bf16 v[80:83], a[216:219], v[24:27], 0\n\t" \
    "s_add_u32 m0, s91, 0\n\t" \
    "v_mfma_f32_16x16x32_bf16 v[84:87], a[216:219], v[28:31], 0\n\t" \
    "global_load_lds_dwordx4 v203, s[52:53]\n\t" \
    "s_waitcnt lgkmcnt(5)\n\t" \
    "v_mfma_f32_16x16x32_bf16 v[72:75], a[148:151], v[32:35], v[72:75]\n\t" \
    "ds_read_b128 v[56:59], v1 offset:256\n\t" \
    "s_waitcnt lgkmcnt(5)\n\t" \
    "v_mfma_f32_16x16x32_bf16 v[76:79], a[148:151], v[36:39], v[76:79]\n\t" \
    "ds_read_b128 v[60:63], v1 offset:18688\n\t" \
    "v_mfma_f32_16x16x32_bf16 v[80:83], a[220:223], v[32:35], v[80:83]\n\t" \
    "s_add_u32 m0, s91, 1024\n\t" \
    "v_mfma_f32_16x16x32_bf16 v[84:87], a[220:223], v[36:39], v[84:87]\n\t" \
    "global_load_lds_dwordx4 v204, s[52:53]\n\t" \
    "s_waitcnt lgkmcnt(5)\n\t" \
    "v_mfma_f32_16x16x32_bf16 v[72:75], a[152:155], v[40:43], v[72:75]\n\t" \
    "ds_read_b128 v[64:67], v2 offset:256\n\t" \
    "s_waitcnt lgkmcnt(5)\n\t" \
    "v_mfma_f32_16x16x32_bf16 v[76:79], a[152:155], v[44:47], v[76:79]\n\t" \
    "ds_read_b128 v[68:71], v2 offset:18688\n\t" \
    "v_mfma_f32_16x16x32_bf16 v[80:83], a[224:227], v[40:43], v[80:83]\n\t" \
    "s_add_u32 m0, s91, 2048\n\t" \
    "v_mfma_f32_16x16x32_bf16 v[84:87], a[224:227], v[44:47], v[84:87]\n\t" \
    "global_load_lds_dwordx4 v205, s[52:53]\n\t" \
    "s_waitcnt lgkmcnt(5)\n\t" \
    "v_mfma_f32_16x16x32_bf16 v[72:75], a[156:159], v[48:51], v[72:75]\n\t" \
    "ds_read_b128 v[24:27], v1 offset:384\n\t" \
    "s_waitcnt lgkmcnt(5)\n\t" \
    "v_mfma_f32_16x16x32_bf16 v[76:79], a[156:159], v[52:55], v[76:79]\n\t" \
    "ds_read_b128 v[28:31], v1 offset:18816\n\t" \
    "v_mfma_f32_16x16x32_bf16 v[80:83], a[228:231], v[48:51], v[80:83]\n\t" \
    "s_add_u32 m0, s91, 3072\n\t" \
    "v_mfma_f32_16x16x32_bf16 v[84:87], a[228:231], v[52:55], v[84:87]\n\t" \
    "global_load_lds_dwordx4 v206, s[52:53]\n\t" \
    "s_waitcnt lgkmcnt(5)\n\t" \
    "v_mfma_f32_16x16x32_bf16 v[72:75], a[160:163], v[56:59], v[72:75]\n\t" \
    "ds_read_b128 v[32:35], v2 offset:384\n\t" \
    "s_waitcnt lgkmcnt(5)\n\t" \
    "v_mfma_f32_16x16x32_bf16 v[76:79], a[160:163], v[60:63], v[76:79]\n\t" \
    "ds_read_b128 v[36:39], v2 offset:18816\n\t" \
    "v_mfma_f32_16x16x32_bf16 v[80:83], a[232:235], v[56:59], v[80:83]\n\t" \
    "s_add_u32 m0, s91, 4096\n\t" \
    "v_mfma_f32_16x16x32_bf16 v[84:87], a[232:235], v[60:63], v[84:87]\n\t" \
    "global_load_lds_dwordx4 v207, s[52:53]\n\t" \
    "s_waitcnt lgkmcnt(5)\n\t" \
    "v_mfma_f32_16x16x32_bf16 v[72:75], a[164:167], v[64:67], v[72:75]\n\t" \
    "ds_read_b128 v[40:43], v1 offset:512\n\t" \
    "s_waitcnt lgkmcnt(5)\n\t" \
    "v_mfma_f32_16x16x32_bf16 v[76:79], a[164:167], v[68:71], v[76:79]\n\t" \
    "ds_read_b128 v[44:47], v1 offset:18944\n\t" \
    "v_mfma_f32_16x16x32_bf16 v[80:83], a[236:239], v[64:67], v[80:83]\n\t" \
    "s_add_u32 m0, s91, 5120\n\t" \
    "v_mfma_f32_16x16x32_bf16 v[84:87], a[236:239], v[68:71], v[84:87]\n\t" \
    "global_load_lds_dwordx4 v208, s[52:53]\n\t" \
    "s_waitcnt lgkmcnt(5)\n\t" \
    "v_mfma_f32_16x16x32_bf16 v[72:75], a[168:171], v[24:27], v[72:75]\n\t" \
    "ds_read_b128 v[48:51], v2 offset:512\n\t" \
    "s_waitcnt lgkmcnt(5)\n\t" \
    "v_mfma_f32_16x16x32_bf16 v[76:79], a[168:171], v[28:31], v[76:79]\n\t" \
    "ds_read_b128 v[52:55], v2 offset:18944\n\t" \
    "v_mfma_f32_16x16x32_bf16 v[80:83], a[240:243], v[24:27], v[80:83]\n\t" \
    "s_add_u32 m0, s91, 6144\n\t" \
    "v_mfma_f32_16x16x32_bf16 v[84:87], a[240:243], v[28:31], v[84:87]\n\t" \
    "global_load_lds_dwordx4 v209, s[52:53]\n\t" \
    "s_waitcnt lgkmcnt(5)\n\t" \
    "v_mfma_f32_16x16x32_bf16 v[72:75], a[172:175], v[32:35], v[72:75]\n\t" \
    "ds_read_b128 v[56:59], v1 offset:640\n\t" \
    "s_waitcnt lgkmcnt(5)\n\t" \
    "v_mfma_f32_16x16x32_bf16 v[76:79], a[172:175], v[36:39], v[76:79]\n\t" \
    "ds_read_b128 v[60:63], v1 offset:19072\n\t" \
    "v_mfma_f32_16x16x32_bf16 v[80:83], a[244:247], v[32:35], v[80:83]\n\t" \
    "s_add_u32 m0, s91, 7168\n\t" \
    "v_mfma_f32_16x16x32_bf16 v[84:87], a[244:247], v[36:39], v[84:87]\n\t" \
    "global_load_lds_dwordx4 v210, s[52:53]\n\t" \
    "s_waitcnt lgkmcnt(5)\n\t" \
    "v_mfma_f32_16x16x32_bf16 v[72:75], a[176:179], v[40:43], v[72:75]\n\t" \
    "ds_read_b128 v[64:67], v2 offset:640\n\t" \
    "s_waitcnt lgkmcnt(5)\n\t" \
    "v_mfma_f32_16x16x32_bf16 v[76:79], a[176:179], v[44:47], v[76:79]\n\t" \
    "ds_read_b128 v[68:71], v2 offset:19072\n\t" \
    "v_mfma_f32_16x16x32_bf16 v[80:83], a[248:251], v[40:43], v[80:83]\n\t" \
    "s_add_u32 m0, s91, 8192\n\t" \
    "v_mfma_f32_16x16x32_bf16 v[84:87], a[248:251], v[44:47], v[84:87]\n\t" \
    "global_load_lds_dwordx4 v211, s[52:53]\n\t" \
    "s_waitcnt lgkmcnt(5)\n\t" \
    "v_mfma_f32_16x16x32_bf16 v[72:75], a[180:183], v[48:51], v[72:75]\n\t" \
    "ds_read_b128 v[24:27], v1 offset:768\n\t" \
    "s_waitcnt lgkmcnt(5)\n\t" \
    "v_mfma_f32_16x16x32_bf16 v[76:79], a[180:183], v[52:55], v[76:79]\n\t" \
    "ds_read_b128 v[28:31], v1 offset:19200\n\t" \
    "v_mfma_f32_16x16x32_bf16 v[80:83], a[252:255], v[48:51], v[80:83]\n\t" \
    "v_mfma_f32_16x16x32_bf16 v[84:87], a[252:255], v[52:55], v[84:87]\n\t" \
    "s_waitcnt lgkmcnt(5)\n\t" \
    "v_mfma_f32_16x16x32_bf16 v[72:75], a[184:187], v[56:59], v[72:75]\n\t" \
    "ds_read_b128 v[32:35], v2 offset:768\n\t" \
    "s_waitcnt lgkmcnt(5)\n\t" \
    "v_mfma_f32_16x16x32_bf16 v[76:79], a[184:187], v[60:63], v[76:79]\n\t" \
    "ds_read_b128 v[36:39], v2 offset:19200\n\t" \
    "v_mfma_f32_16x16x32_bf16 v[80:83], v[224:227], v[56:59], v[80:83]\n\t" \
    "s_waitcnt vmcnt(11)\n\t" \
    "s_barrier\n\t" \
    "v_mfma_f32_16x16x32_bf16 v[84:87], v[224:227], v[60:63], v[84:87]\n\t" \
    "s_sub_u32 s71, s65, 1\n\t" \
    "s_min_i32 s71, s71, 0\n\t" \
    "s_sub_u32 s69, s65, 2\n\t" \
    "s_max_i32 s71, s71, s69\n\t" \
    "s_cmp_lg_u32 s66, 0\n\t" \
    "s_cbranch_scc1 gx_SF9_%=\n\t" \
    "s_lshl_b32 s69, s67, 7\n\t" \
    "s_add_u32 s69, s69, 5771264\n\t" \
    "s_add_u32 s70, s71, 0\n\t" \
    "v_mov_b32 v156, s69\n\t" \
    "v_mov_b32 v157, s70\n\t" \
    "s_mov_b64 exec, 1\n\t" \
    "global_store_dword v156, v157, s[36:37] sc1\n\t" \
    "s_mov_b64 exec, -1\n\t" \
    "gx_SF9_%=:\n\t" \
    "s_waitcnt lgkmcnt(5)\n\t" \
    "v_mfma_f32_16x16x32_bf16 v[72:75], a[188:191], v[64:67], v[72:75]\n\t" \
    "ds_read_b128 v[40:43], v1 offset:896\n\t" \
    "s_waitcnt lgkmcnt(5)\n\t" \
    "v_mfma_f32_16x16x32_bf16 v[76:79], a[188:191], v[68:71], v[76:79]\n\t" \
    "ds_read_b128 v[44:47], v1 offset:19328\n\t" \
    "v_mfma_f32_16x16x32_bf16 v[80:83], v[228:231], v[64:67], v[80:83]\n\t" \
    "v_mfma_f32_16x16x32_bf16 v[84:87], v[228:231], v[68:71], v[84:87]\n\t" \
    "s_waitcnt lgkmcnt(5)\n\t" \
    "v_mfma_f32_16x16x32_bf16 v[72:75], a[192:195], v[24:27], v[72:75]\n\t" \
    "ds_read_b128 v[48:51], v2 offset:896\n\t" \
    "s_waitcnt lgkmcnt(5)\n\t" \
    "v_mfma_f32_16x16x32_bf16 v[76:79], a[192:195], v[28:31], v[76:79]\n\t" \
    "ds_read_b128 v[52:55], v2 offset:19328\n\t" \
    "v_mfma_f32_16x16x32_bf16 v[80:83], v[232:235], v[24:27], v[80:83]\n\t" \
    "v_mfma_f32_16x16x32_bf16 v[84:87], v[232:235], v[28:31], v[84:87]\n\t" \
    "s_waitcnt lgkmcnt(5)\n\t" \
    "v_mfma_f32_16x16x32_bf16 v[72:75], a[196:199], v[32:35], v[72:75]\n\t" \
    "ds_read_b128 v[56:59], v1 offset:1024\n\t" \
    "s_waitcnt lgkmcnt(5)\n\t" \
    "v_mfma_f32_16x16x32_bf16 v[76:79], a[196:199], v[36:39], v[76:79]\n\t" \
    "ds_read_b128 v[60:63], v1 offset:19456\n\t" \
    "v_mfma_f32_16x16x32_bf16 v[80:83], v[236:239], v[32:35], v[80:83]\n\t" \
    "v_mfma_f32_16x16x32_bf16 v[84:87], v[236:239], v[36:39], v[84:87]\n\t" \
    "s_waitcnt lgkmcnt(5)\n\t" \
    "v_mfma_f32_16x16x32_bf16 v[72:75], a[200:203], v[40:43], v[72:75]\n\t" \
    "ds_read_b128 v[64:67], v2 offset:1024\n\t" \
    "s_waitcnt lgkmcnt(5)\n\t" \
    "v_mfma_f32_16x16x32_bf16 v[76:79], a[200:203], v[44:47], v[76:79]\n\t" \
    "ds_read_b128 v[68:71], v2 offset:19456\n\t" \
    "v_mfma_f32_16x16x32_bf16 v[80:83], v[240:243], v[40:43], v[80:83]\n\t" \
    "v_mfma_f32_16x16x32_bf16 v[84:87], v[240:243], v[44:47], v[84:87]\n\t" \
    "s_waitcnt lgkmcnt(5)\n\t" \
    "v_mfma_f32_16x16x32_bf16 v[72:75], a[204:207], v[48:51], v[72:75]\n\t" \
    "ds_read_b128 v[24:27], v22 offset:0\n\t" \
    "s_waitcnt lgkmcnt(5)\n\t" \
    "v_mfma_f32_16x16x32_bf16 v[76:79], a[204:207], v[52:55], v[76:79]\n\t" \
    "ds_read_b128 v[28:31], v22 offset:18432\n\t" \
    "v_mfma_f32_16x16x32_bf16 v[80:83], v[244:247], v[48:51], v[80:83]\n\t" \
    "v_mfma_f32_16x16x32_bf16 v[84:87], v[244:247], v[52:55], v[84:87]\n\t" \
    "s_waitcnt lgkmcnt(5)\n\t" \
    "v_mfma_f32_16x16x32_bf16 v[72:75], a[208:211], v[56:59], v[72:75]\n\t" \
    "ds_read_b128 v[32:35], v23 offset:0\n\t" \
    "s_waitcnt lgkmcnt(5)\n\t" \
    "v_mfma_f32_16x16x32_bf16 v[76:79], a[208:211], v[60:63], v[76:79]\n\t" \
    "ds_read_b128 v[36:39], v23 offset:18432\n\t" \
    "v_mfma_f32_16x16x32_bf16 v[80:83], v[248:251], v[56:59], v[80:83]\n\t" \
    "v_mfma_f32_16x16x32_bf16 v[84:87], v[248:251], v[60:63], v[84:87]\n\t" \
    "s_waitcnt lgkmcnt(5)\n\t" \
    "v_mfma_f32_16x16x32_bf16 v[72:75], a[212:215], v[64:67], v[72:75]\n\t" \
    "ds_read_b128 v[40:43], v22 offset:128\n\t" \
    "s_waitcnt lgkmcnt(5)\n\t" \
    "v_mfma_f32_16x16x32_bf16 v[76:79], a[212:215], v[68:71], v[76:79]\n\t" \
    "ds_read_b128 v[44:47], v22 offset:18560\n\t" \
    "v_mfma_f32_16x16x32_bf16 v[80:83], v[252:255], v[64:67], v[80:83]\n\t" \
    "v_mfma_f32_16x16x32_bf16 v[84:87], v[252:255], v[68:71], v[84:87]\n\t" \
    "v_mov_b32 v1, v22\n\t" \
    "v_mov_b32 v2, v23\n\t" \
    "v_cvt_pk_bf16_f32 v104, v72, v73\n\t" \
    "v_cvt_pk_bf16_f32 v105, v74, v75\n\t" \
    "s_nop 2\n\t" \
    "v_cvt_pk_bf16_f32 v106, v80, v81\n\t" \
    "v_cvt_pk_bf16_f32 v107, v82, v83\n\t" \
    "global_store_dwordx4 v8, v[104:107], s[40:41] offset:0 sc1\n\t" \
    "v_cvt_pk_bf16_f32 v108, v76, v77\n\t" \
    "v_cvt_pk_bf16_f32 v109, v78, v79\n\t" \
    "v_cvt_pk_bf16_f32 v110, v84, v85\n\t" \
    "v_cvt_pk_bf16_f32 v111, v86, v87\n\t" \
    "global_store_dwordx4 v8, v[108:111], s[40:41] offset:1024 sc1\n\t" \
    "s_cmp_ge_u32 s65, 1\n\t" \
    "s_cbranch_scc1 gx_nofast7_%=\n\t" \
    "s_waitcnt vmcnt(0)\n\t" \
    "s_barrier\n\t" \
    "s_cmp_lg_u32 s66, 0\n\t" \
    "s_cbranch_scc1 gx_SF10_%=\n\t" \
    "s_lshl_b32 s69, s67, 7\n\t" \
    "s_add_u32 s69, s69, 5771264\n\t" \
    "s_add_u32 s70, s65, 0\n\t" \
    "v_mov_b32 v156, s69\n\t" \
    "v_mov_b32 v157, s70\n\t" \
    "s_mov_b64 exec, 1\n\t" \
    "global_store_dword v156, v157, s[36:37] sc1\n\t" \
    "s_mov_b64 exec, -1\n\t" \
    "gx_SF10_%=:\n\t" \
    "gx_nofast7_%=:\n\t" \
    "s_sub_u32 s69, s65, 4\n\t" \
    "s_cmp_gt_u32 s69, 4\n\t" \
    "s_cbranch_scc1 gx_nt11_%=\n\t" \
    "s_lshl_b32 s69, s69, 7\n\t" \
    "s_lshl_b32 s70, s66, 5\n\t" \
    "s_add_u32 s69, s69, s70\n\t" \
    "v_lshrrev_b32 v156, 3, v6\n\t" \
    "v_add_u32 v156, s69, v156\n\t" \
    "v_min_u32 v156, 575, v156\n\t" \
    "v_and_b32 v157, 4, v6\n\t" \
    "v_lshlrev_b32 v157, 5, v157\n\t" \
    "v_lshl_add_u32 v156, v156, 13, v157\n\t" \
    "s_lshl_b32 s69, s67, 8\n\t" \
    "v_add_u32 v156, s69, v156\n\t" \
    "s_mov_b32 m0, 155648\n\t" \
    "s_nop 0\n\t" \
    "global_load_lds_dword v156, s[44:45]\n\t" \
    "gx_nt11_%=:\n\t" \
    "s_add_u32 s65, s65, 1\n\t" \
    "s_cmp_lt_u32 s65, 9\n\t" \
    "s_cbranch_scc1 gx_l16_%=\n\t" \
    "s_waitcnt vmcnt(0) lgkmcnt(0)\n\t" \
    "s_barrier\n\t" \
    "s_cmp_lg_u32 s66, 0\n\t" \
    "s_cbranch_scc1 gx_SF12_%=\n\t" \
    "s_lshl_b32 s69, s67, 7\n\t" \
    "s_add_u32 s69, s69, 5771264\n\t" \
    "s_add_u32 s70, s65, -1\n\t" \
    "v_mov_b32 v156, s69\n\t" \
    "v_mov_b32 v157, s70\n\t" \
    "s_mov_b64 exec, 1\n\t" \
    "global_store_dword v156, v157, s[36:37] sc1\n\t" \
    "s_mov_b64 exec, -1\n\t" \
    "gx_SF12_%=:\n\t" \
    "s_add_u32 s46, s44, 0\n\t" \
    "s_addc_u32 s47, s45, 0\n\t" \
    "s_add_u32 m0, s82, 0\n\t" \
    "s_nop 0\n\t" \
    "global_load_lds_dwordx4 v200, s[46:47]\n\t" \
    "s_add_u32 s46, s44, 131072\n\t" \
    "s_addc_u32 s47, s45, 0\n\t" \
    "s_add_u32 m0, s82, 4096\n\t" \
    "s_nop 0\n\t" \
    "global_load_lds_dwordx4 v200, s[46:47]\n\t" \
    "s_add_u32 s46, s44, 262144\n\t" \
    "s_addc_u32 s47, s45, 0\n\t" \
    "s_add_u32 m0, s82, 8192\n\t" \
    "s_nop 0\n\t" \
    "global_load_lds_dwordx4 v200, s[46:47]\n\t" \
    "s_add_u32 s46, s44, 393216\n\t" \
    "s_addc_u32 s47, s45, 0\n\t" \
    "s_add_u32 m0, s82, 12288\n\t" \
    "s_nop 0\n\t" \
    "global_load_lds_dwordx4 v200, s[46:47]\n\t" \
    "s_add_u32 s46, s44, 524288\n\t" \
    "s_addc_u32 s47, s45, 0\n\t" \
    "s_add_u32 m0, s82, 16384\n\t" \
    "s_nop 0\n\t" \
    "global_load_lds_dwordx4 v200, s[46:47]\n\t" \
    "s_add_u32 s46, s44, 655360\n\t" \
    "s_addc_u32 s47, s45, 0\n\t" \
    "s_add_u32 m0, s82, 20480\n\t" \
    "s_nop 0\n\t" \
    "global_load_lds_dwordx4 v200, s[46:47]\n\t" \
    "s_add_u32 s46, s44, 786432\n\t" \
    "s_addc_u32 s47, s45, 0\n\t" \
    "s_add_u32 m0, s82, 24576\n\t" \
    "s_nop 0\n\t" \
    "global_load_lds_dwordx4 v200, s[46:47]\n\t" \
    "s_add_u32 s46, s44, 917504\n\t" \
    "s_addc_u32 s47, s45, 0\n\t" \
    "s_add_u32 m0, s82, 28672\n\t" \
    "s_nop 0\n\t" \
    "global_load_lds_dwordx4 v200, s[46:47]\n\t" \
    "s_add_u32 s46, s44, 1048576\n\t" \
    "s_addc_u32 s47, s45, 0\n\t" \
    "s_add_u32 m0, s82, 32768\n\t" \
    "s_nop 0\n\t" \
    "global_load_lds_dwordx4 v200, s[46:47]\n\t" \
    "s_add_u32 s46, s44, 1179648\n\t" \
    "s_addc_u32 s47, s45, 0\n\t" \
    "s_add_u32 m0, s82, 36864\n\t" \
    "s_nop 0\n\t" \
    "global_load_lds_dwordx4 v200, s[46:47]\n\t" \
    "s_add_u32 s46, s44, 1310720\n\t" \
    "s_addc_u32 s47, s45, 0\n\t" \
    "s_add_u32 m0, s82, 40960\n\t" \
    "s_nop 0\n\t" \
    "global_load_lds_dwordx4 v200, s[46:47]\n\t" \
    "s_add_u32 s46, s44, 1441792\n\t" \
    "s_addc_u32 s47, s45, 0\n\t" \
    "s_add_u32 m0, s82, 45056\n\t" \
    "s_nop 0\n\t" \
    "global_load_lds_dwordx4 v200, s[46:47]\n\t" \
    "s_add_u32 s46, s44, 1572864\n\t" \
    "s_addc_u32 s47, s45, 0\n\t" \
    "s_add_u32 m0, s82, 49152\n\t" \
    "s_nop 0\n\t" \
    "global_load_lds_dwordx4 v200, s[46:47]\n\t" \
    "s_add_u32 s46, s44, 1703936\n\t" \
    "s_addc_u32 s47, s45, 0\n\t" \
    "s_add_u32 m0, s82, 53248\n\t" \
    "s_nop 0\n\t" \
    "global_load_lds_dwordx4 v200, s[46:47]\n\t" \
    "s_add_u32 s46, s44, 1835008\n\t" \
    "s_addc_u32 s47, s45, 0\n\t" \
    "s_add_u32 m0, s82, 57344\n\t" \
    "s_nop 0\n\t" \
    "global_load_lds_dwordx4 v200, s[46:47]\n\t" \
    "s_add_u32 s46, s44, 1966080\n\t" \
    "s_addc_u32 s47, s45, 0\n\t" \
    "s_add_u32 m0, s82, 61440\n\t" \
    "s_nop 0\n\t" \
    "global_load_lds_dwordx4 v200, s[46:47]\n\t" \
    "s_add_u32 s46, s44, 2097152\n\t" \
    "s_addc_u32 s47, s45, 0\n\t" \
    "s_add_u32 m0, s82, 65536\n\t" \
    "s_nop 0\n\t" \
    "global_load_lds_dwordx4 v200, s[46:47]\n\t" \
    "s_add_u32 s46, s44, 2228224\n\t" \
    "s_addc_u32 s47, s45, 0\n\t" \
    "s_add_u32 m0, s82, 69632\n\t" \
    "s_nop 0\n\t" \
    "global_load_lds_dwordx4 v200, s[46:47]\n\t" \
    "s_add_u32 s46, s44, 2359296\n\t" \
    "s_addc_u32 s47, s45, 0\n\t" \
    "s_add_u32 m0, s82, 73728\n\t" \
    "s_nop 0\n\t" \
    "global_load_lds_dwordx4 v200, s[46:47]\n\t" \
    "s_add_u32 s46, s44, 2490368\n\t" \
    "s_addc_u32 s47, s45, 0\n\t" \
    "s_add_u32 m0, s82, 77824\n\t" \
    "s_nop 0\n\t" \
    "global_load_lds_dwordx4 v200, s[46:47]\n\t" \
    "s_add_u32 s46, s44, 2621440\n\t" \
    "s_addc_u32 s47, s45, 0\n\t" \
    "s_add_u32 m0, s82, 81920\n\t" \
    "s_nop 0\n\t" \
    "global_load_lds_dwordx4 v200, s[46:47]\n\t" \
    "s_add_u32 s46, s44, 2752512\n\t" \
    "s_addc_u32 s47, s45, 0\n\t" \
    "s_add_u32 m0, s82, 86016\n\t" \
    "s_nop 0\n\t" \
    "global_load_lds_dwordx4 v200, s[46:47]\n\t" \
    "s_add_u32 s46, s44, 2883584\n\t" \
    "s_addc_u32 s47, s45, 0\n\t" \
    "s_add_u32 m0, s82, 90112\n\t" \
    "s_nop 0\n\t" \
    "global_load_lds_dwordx4 v200, s[46:47]\n\t" \
    "s_add_u32 s46, s44, 3014656\n\t" \
    "s_addc_u32 s47, s45, 0\n\t" \
    "s_add_u32 m0, s82, 94208\n\t" \
    "s_nop 0\n\t" \
    "global_load_lds_dwordx4 v200, s[46:47]\n\t" \
    "s_add_u32 s46, s44, 3145728\n\t" \
    "s_addc_u32 s47, s45, 0\n\t" \
    "s_add_u32 m0, s82, 98304\n\t" \
    "s_nop 0\n\t" \
    "global_load_lds_dwordx4 v200, s[46:47]\n\t" \
    "s_add_u32 s46, s44, 3276800\n\t" \
    "s_addc_u32 s47, s45, 0\n\t" \
    "s_add_u32 m0, s82, 102400\n\t" \
    "s_nop 0\n\t" \
    "global_load_lds_dwordx4 v200, s[46:47]\n\t" \
    "s_add_u32 s46, s44, 3407872\n\t" \
    "s_addc_u32 s47, s45, 0\n\t" \
    "s_add_u32 m0, s82, 106496\n\t" \
    "s_nop 0\n\t" \
    "global_load_lds_dwordx4 v200, s[46:47]\n\t" \
    "s_add_u32 s46, s44, 3538944\n\t" \
    "s_addc_u32 s47, s45, 0\n\t" \
    "s_add_u32 m0, s82, 110592\n\t" \
    "s_nop 0\n\t" \
    "global_load_lds_dwordx4 v200, s[46:47]\n\t" \
    "s_add_u32 s46, s44, 3670016\n\t" \
    "s_addc_u32 s47, s45, 0\n\t" \
    "s_add_u32 m0, s82, 114688\n\t" \
    "s_nop 0\n\t" \
    "global_load_lds_dwordx4 v200, s[46:47]\n\t" \
    "s_add_u32 s46, s44, 3801088\n\t" \
    "s_addc_u32 s47, s45, 0\n\t" \
    "s_add_u32 m0, s82, 118784\n\t" \
    "s_nop 0\n\t" \
    "global_load_lds_dwordx4 v200, s[46:47]\n\t" \
    "s_add_u32 s46, s44, 3932160\n\t" \
    "s_addc_u32 s47, s45, 0\n\t" \
    "s_add_u32 m0, s82, 122880\n\t" \
    "s_nop 0\n\t" \
    "global_load_lds_dwordx4 v200, s[46:47]\n\t" \
    "s_add_u32 s46, s44, 4063232\n\t" \
    "s_addc_u32 s47, s45, 0\n\t" \
    "s_add_u32 m0, s82, 126976\n\t" \
    "s_nop 0\n\t" \
    "global_load_lds_dwordx4 v200, s[46:47]\n\t" \
    "s_add_u32 s46, s44, 4194304\n\t" \
    "s_addc_u32 s47, s45, 0\n\t" \
    "s_add_u32 m0, s82, 131072\n\t" \
    "s_nop 0\n\t" \
    "global_load_lds_dwordx4 v200, s[46:47]\n\t" \
    "s_add_u32 s46, s44, 4325376\n\t" \
    "s_addc_u32 s47, s45, 0\n\t" \
    "s_add_u32 m0, s82, 135168\n\t" \
    "s_nop 0\n\t" \
    "global_load_lds_dwordx4 v200, s[46:47]\n\t" \
    "s_add_u32 s46, s44, 4456448\n\t" \
    "s_addc_u32 s47, s45, 0\n\t" \
    "s_add_u32 m0, s82, 139264\n\t" \
    "s_nop 0\n\t" \
    "global_load_lds_dwordx4 v200, s[46:47]\n\t" \
    "s_add_u32 s46, s44, 4587520\n\t" \
    "s_addc_u32 s47, s45, 0\n\t" \
    "s_add_u32 m0, s82, 143360\n\t" \
    "s_nop 0\n\t" \
    "global_load_lds_dwordx4 v200, s[46:47]\n\t" \
    "global_load_dwordx4 v[120:123], v203, s[50:51]\n\t" \
    "global_load_dwordx4 v[124:127], v204, s[50:51]\n\t" \
    "global_load_dwordx4 v[128:131], v205, s[50:51]\n\t" \
    "global_load_dwordx4 v[132:135], v206, s[50:51]\n\t" \
    "global_load_dwordx4 v[136:139], v207, s[50:51]\n\t" \
    "global_load_dwordx4 v[140:143], v208, s[50:51]\n\t" \
    "global_load_dwordx4 v[144:147], v209, s[50:51]\n\t" \
    "global_load_dwordx4 v[148:151], v210, s[50:51]\n\t" \
    "global_load_dwordx4 v[152:155], v211, s[50:51]\n\t" \
    "s_add_u32 s52, s50, 331776\n\t" \
    "s_addc_u32 s53, s51, 0\n\t" \
    "global_load_dwordx4 v[72:75], v203, s[52:53]\n\t" \
    "global_load_dwordx4 v[76:79], v204, s[52:53]\n\t" \
    "global_load_dwordx4 v[80:83], v205, s[52:53]\n\t" \
    "global_load_dwordx4 v[84:87], v206, s[52:53]\n\t" \
    "global_load_dwordx4 v[88:91], v207, s[52:53]\n\t" \
    "global_load_dwordx4 v[92:95], v208, s[52:53]\n\t" \
    "global_load_dwordx4 v[96:99], v209, s[52:53]\n\t" \
    "global_load_dwordx4 v[100:103], v210, s[52:53]\n\t" \
    "global_load_dwordx4 v[104:107], v211, s[52:53]\n\t" \
    "s_waitcnt vmcnt(50) lgkmcnt(0)\n\t" \
    "s_barrier\n\t" \
    "ds_read_b64_tr_b16 a[0:1], v201 offset:0\n\t" \
    "ds_read_b64_tr_b16 a[2:3], v201 offset:1024\n\t" \
    "ds_read_b64_tr_b16 a[4:5], v201 offset:8192\n\t" \
    "ds_read_b64_tr_b16 a[6:7], v201 offset:9216\n\t" \
    "ds_read_b64_tr_b16 a[72:73], v202 offset:0\n\t" \
    "ds_read_b64_tr_b16 a[74:75], v202 offset:1024\n\t" \
    "ds_read_b64_tr_b16 a[76:77], v202 offset:8192\n\t" \
    "ds_read_b64_tr_b16 a[78:79], v202 offset:9216\n\t" \
    "s_waitcnt vmcnt(46) lgkmcnt(0)\n\t" \
    "s_barrier\n\t" \
    "ds_read_b64_tr_b16 a[8:9], v201 offset:16384\n\t" \
    "ds_read_b64_tr_b16 a[10:11], v201 offset:17408\n\t" \
    "ds_read_b64_tr_b16 a[12:13], v201 offset:24576\n\t" \
    "ds_read_b64_tr_b16 a[14:15], v201 offset:25600\n\t" \
    "ds_read_b64_tr_b16 a[80:81], v202 offset:16384\n\t" \
    "ds_read_b64_tr_b16 a[82:83], v202 offset:17408\n\t" \
    "ds_read_b64_tr_b16 a[84:85], v202 offset:24576\n\t" \
    "ds_read_b64_tr_b16 a[86:87], v202 offset:25600\n\t" \
    "s_waitcnt vmcnt(42) lgkmcnt(0)\n\t" \
    "s_barrier\n\t" \
    "ds_read_b64_tr_b16 a[16:17], v201 offset:32768\n\t" \
    "ds_read_b64_tr_b16 a[18:19], v201 offset:33792\n\t" \
    "ds_read_b64_tr_b16 a[20:21], v201 offset:40960\n\t" \
    "ds_read_b64_tr_b16 a[22:23], v201 offset:41984\n\t" \
    "ds_read_b64_tr_b16 a[88:89], v202 offset:32768\n\t" \
    "ds_read_b64_tr_b16 a[90:91], v202 offset:33792\n\t" \
    "ds_read_b64_tr_b16 a[92:93], v202 offset:40960\n\t" \
    "ds_read_b64_tr_b16 a[94:95], v202 offset:41984\n\t" \
    "s_waitcnt vmcnt(38) lgkmcnt(0)\n\t" \
    "s_barrier\n\t" \
    "ds_read_b64_tr_b16 a[24:25], v201 offset:49152\n\t" \
    "ds_read_b64_tr_b16 a[26:27], v201 offset:50176\n\t" \
    "ds_read_b64_tr_b16 a[28:29], v201 offset:57344\n\t" \
    "ds_read_b64_tr_b16 a[30:31], v201 offset:58368\n\t" \
    "ds_read_b64_tr_b16 a[96:97], v202 offset:49152\n\t" \
    "ds_read_b64_tr_b16 a[98:99], v202 offset:50176\n\t" \
    "ds_read_b64_tr_b16 a[100:101], v202 offset:57344\n\t" \
    "ds_read_b64_tr_b16 a[102:103], v202 offset:58368\n\t" \
    "s_waitcnt vmcnt(34) lgkmcnt(0)\n\t" \
    "s_barrier\n\t" \
    "v_add_u32 v201, 0x10000, v201\n\t" \
    "v_add_u32 v202, 0x10000, v202\n\t" \
    "ds_read_b64_tr_b16 a[32:33], v201 offset:0\n\t" \
    "ds_read_b64_tr_b16 a[34:35], v201 offset:1024\n\t" \
    "ds_read_b64_tr_b16 a[36:37], v201 offset:8192\n\t" \
    "ds_read_b64_tr_b16 a[38:39], v201 offset:9216\n\t" \
    "ds_read_b64_tr_b16 a[104:105], v202 offset:0\n\t" \
    "ds_read_b64_tr_b16 a[106:107], v202 offset:1024\n\t" \
    "ds_read_b64_tr_b16 a[108:109], v202 offset:8192\n\t" \
    "ds_read_b64_tr_b16 a[110:111], v202 offset:9216\n\t" \
    "s_waitcnt vmcnt(30) lgkmcnt(0)\n\t" \
    "s_barrier\n\t" \
    "ds_read_b64_tr_b16 a[40:41], v201 offset:16384\n\t" \
    "ds_read_b64_tr_b16 a[42:43], v201 offset:17408\n\t" \
    "ds_read_b64_tr_b16 a[44:45], v201 offset:24576\n\t" \
    "ds_read_b64_tr_b16 a[46:47], v201 offset:25600\n\t" \
    "ds_read_b64_tr_b16 a[112:113], v202 offset:16384\n\t" \
    "ds_read_b64_tr_b16 a[114:115], v202 offset:17408\n\t" \
    "ds_read_b64_tr_b16 a[116:117], v202 offset:24576\n\t" \
    "ds_read_b64_tr_b16 a[118:119], v202 offset:25600\n\t" \
    "s_waitcnt vmcnt(26) lgkmcnt(0)\n\t" \
    "s_barrier\n\t" \
    "ds_read_b64_tr_b16 a[48:49], v201 offset:32768\n\t" \
    "ds_read_b64_tr_b16 a[50:51], v201 offset:33792\n\t" \
    "ds_read_b64_tr_b16 a[52:53], v201 offset:40960\n\t" \
    "ds_read_b64_tr_b16 a[54:55], v201 offset:41984\n\t" \
    "ds_read_b64_tr_b16 a[120:121], v202 offset:32768\n\t" \
    "ds_read_b64_tr_b16 a[122:123], v202 offset:33792\n\t" \
    "ds_read_b64_tr_b16 a[124:125], v202 offset:40960\n\t" \
    "ds_read_b64_tr_b16 a[126:127], v202 offset:41984\n\t" \
    "s_waitcnt vmcnt(22) lgkmcnt(0)\n\t" \
    "s_barrier\n\t" \
    "ds_read_b64_tr_b16 a[56:57], v201 offset:49152\n\t" \
    "ds_read_b64_tr_b16 a[58:59], v201 offset:50176\n\t" \
    "ds_read_b64_tr_b16 a[60:61], v201 offset:57344\n\t" \
    "ds_read_b64_tr_b16 a[62:63], v201 offset:58368\n\t" \
    "ds_read_b64_tr_b16 a[128:129], v202 offset:49152\n\t" \
    "ds_read_b64_tr_b16 a[130:131], v202 offset:50176\n\t" \
    "ds_read_b64_tr_b16 a[132:133], v202 offset:57344\n\t" \
    "ds_read_b64_tr_b16 a[134:135], v202 offset:58368\n\t" \
    "s_waitcnt vmcnt(18) lgkmcnt(0)\n\t" \
    "s_barrier\n\t" \
    "v_add_u32 v201, 0x10000, v201\n\t" \
    "v_add_u32 v202, 0x10000, v202\n\t" \
    "ds_read_b64_tr_b16 a[64:65], v201 offset:0\n\t" \
    "ds_read_b64_tr_b16 a[66:67], v201 offset:1024\n\t" \
    "ds_read_b64_tr_b16 a[68:69], v201 offset:8192\n\t" \
    "ds_read_b64_tr_b16 a[70:71], v201 offset:9216\n\t" \
    "ds_read_b64_tr_b16 a[136:137], v202 offset:0\n\t" \
    "ds_read_b64_tr_b16 a[138:139], v202 offset:1024\n\t" \
    "ds_read_b64_tr_b16 a[140:141], v202 offset:8192\n\t" \
    "ds_read_b64_tr_b16 a[142:143], v202 offset:9216\n\t" \
    "v_add_u32 v201, 0xfffe0000, v201\n\t" \
    "v_add_u32 v202, 0xfffe0000, v202\n\t" \
    "s_waitcnt lgkmcnt(0)\n\t" \
    "s_barrier\n\t" \
    "s_waitcnt vmcnt(0)\n\t" \
    "ds_write_b128 v3, v[120:123] offset:0\n\t" \
    "ds_write_b128 v3, v[124:127] offset:1024\n\t" \
    "ds_write_b128 v3, v[128:131] offset:2048\n\t" \
    "ds_write_b128 v3, v[132:135] offset:3072\n\t" \
    "ds_write_b128 v3, v[136:139] offset:4096\n\t" \
    "ds_write_b128 v3, v[140:143] offset:5120\n\t" \
    "ds_write_b128 v3, v[144:147] offset:6144\n\t" \
    "ds_write_b128 v3, v[148:151] offset:7168\n\t" \
    "ds_write_b128 v3, v[152:155] offset:8192\n\t" \
    "ds_write_b128 v3, v[72:75] offset:36864\n\t" \
    "ds_write_b128 v3, v[76:79] offset:37888\n\t" \
    "ds_write_b128 v3, v[80:83] offset:38912\n\t" \
    "ds_write_b128 v3, v[84:87] offset:39936\n\t" \
    "ds_write_b128 v3, v[88:91] offset:40960\n\t" \
    "ds_write_b128 v3, v[92:95] offset:41984\n\t" \
    "ds_write_b128 v3, v[96:99] offset:43008\n\t" \
    "ds_write_b128 v3, v[100:103] offset:44032\n\t" \
    "ds_write_b128 v3, v[104:107] offset:45056\n\t" \
    "s_mov_b32 s65, 0\n\t" \
    "v_mov_b32 v1, v0\n\t" \
    "v_xor_b32 v2, 64, v0\n\t" \
    "s_mov_b32 s90, 0\n\t" \
    "s_add_u32 s91, s89, 0x12000\n\t" \
    "s_add_u32 s85, s91, s89\n\t" \
    "s_add_u32 s52, s50, 36864\n\t" \
    "s_addc_u32 s53, s51, 0\n\t" \
    "s_mul_i32 s69, s67, 8192\n\t" \
    "s_add_u32 s69, s69, 175112192\n\t" \
    "s_add_u32 s42, s36, s69\n\t" \
    "s_addc_u32 s43, s37, 0\n\t" \
    "s_mov_b64 s[40:41], s[42:43]\n\t" \
    "s_waitcnt lgkmcnt(0)\n\t" \
    "s_barrier\n\t" \
    "ds_read_b128 v[24:27], v1 offset:0\n\t" \
    "ds_read_b128 v[28:31], v1 offset:18432\n\t" \
    "ds_read_b128 v[32:35], v2 offset:0\n\t" \
    "ds_read_b128 v[36:39], v2 offset:18432\n\t" \
    "ds_read_b128 v[40:43], v1 offset:128\n\t" \
    "ds_read_b128 v[44:47], v1 offset:18560\n\t" \
    ".p2align 6\n\t" \
    "s_nop 0\n\t" \
    "s_nop 0\n\t" \
    "gx_l28_%=:\n\t" \
    "s_waitcnt lgkmcnt(5)\n\t" \
    "v_mfma_f32_16x16x32_bf16 v[88:91], a[0:3], v[24:27], 0\n\t" \
    "ds_read_b128 v[48:51], v2 offset:128\n\t" \
    "s_waitcnt lgkmcnt(5)\n\t" \
    "v_mfma_f32_16x16x32_bf16 v[92:95], a[0:3], v[28:31], 0\n\t" \
    "ds_read_b128 v[52:55], v2 offset:18560\n\t" \
    "v_mfma_f32_16x16x32_bf16 v[96:99], a[72:75], v[24:27], 0\n\t" \
    "s_add_u32 m0, s91, 0\n\t" \
    "v_mfma_f32_16x16x32_bf16 v[100:103], a[72:75], v[28:31], 0\n\t" \
    "global_load_lds_dwordx4 v203, s[52:53]\n\t" \
    "s_waitcnt lgkmcnt(5)\n\t" \
    "v_mfma_f32_16x16x32_bf16 v[88:91], a[4:7], v[32:35], v[88:91]\n\t" \
    "ds_read_b128 v[56:59], v1 offset:256\n\t" \
    "s_waitcnt lgkmcnt(5)\n\t" \
    "v_mfma_f32_16x16x32_bf16 v[92:95], a[4:7], v[36:39], v[92:95]\n\t" \
    "ds_read_b128 v[60:63], v1 offset:18688\n\t" \
    "v_mfma_f32_16x16x32_bf16 v[96:99], a[76:79], v[32:35], v[96:99]\n\t" \
    "s_add_u32 m0, s91, 1024\n\t" \
    "v_mfma_f32_16x16x32_bf16 v[100:103], a[76:79], v[36:39], v[100:103]\n\t" \
    "global_load_lds_dwordx4 v204, s[52:53]\n\t" \
    "s_waitcnt lgkmcnt(5)\n\t" \
    "v_mfma_f32_16x16x32_bf16 v[88:91], a[8:11], v[40:43], v[88:91]\n\t" \
    "ds_read_b128 v[64:67], v2 offset:256\n\t" \
    "s_waitcnt lgkmcnt(5)\n\t" \
    "v_mfma_f32_16x16x32_bf16 v[92:95], a[8:11], v[44:47], v[92:95]\n\t" \
    "ds_read_b128 v[68:71], v2 offset:18688\n\t" \
    "v_cvt_pk_bf16_f32 v104, v72, v73\n\t" \
    "v_mfma_f32_16x16x32_bf16 v[96:99], a[80:83], v[40:43], v[96:99]\n\t" \
    "s_add_u32 m0, s91, 2048\n\t" \
    "v_cvt_pk_bf16_f32 v105, v74, v75\n\t" \
    "v_mfma_f32_16x16x32_bf16 v[100:103], a[80:83], v[44:47], v[100:103]\n\t" \
    "global_load_lds_dwordx4 v205, s[52:53]\n\t" \
    "s_waitcnt lgkmcnt(5)\n\t" \
    "v_mfma_f32_16x16x32_bf16 v[88:91], a[12:15], v[48:51], v[88:91]\n\t" \
    "ds_read_b128 v[24:27], v1 offset:384\n\t" \
    "s_waitcnt lgkmcnt(5)\n\t" \
    "v_mfma_f32_16x16x32_bf16 v[92:95], a[12:15], v[52:55], v[92:95]\n\t" \
    "ds_read_b128 v[28:31], v1 offset:18816\n\t" \
    "v_cvt_pk_bf16_f32 v106, v80, v81\n\t" \
    "v_mfma_f32_16x16x32_bf16 v[96:99], a[84:87], v[48:51], v[96:99]\n\t" \
    "s_add_u32 m0, s91, 3072\n\t" \
    "v_cvt_pk_bf16_f32 v107, v82, v83\n\t" \
    "v_mfma_f32_16x16x32_bf16 v[100:103], a[84:87], v[52:55], v[100:103]\n\t" \
    "global_load_lds_dwordx4 v206, s[52:53]\n\t" \
    "s_waitcnt lgkmcnt(5)\n\t" \
    "v_mfma_f32_16x16x32_bf16 v[88:91], a[16:19], v[56:59], v[88:91]\n\t" \
    "ds_read_b128 v[32:35], v2 offset:384\n\t" \
    "s_waitcnt lgkmcnt(5)\n\t" \
    "v_mfma_f32_16x16x32_bf16 v[92:95], a[16:19], v[60:63], v[92:95]\n\t" \
    "ds_read_b128 v[36:39], v2 offset:18816\n\t" \
    "global_store_dwordx4 v8, v[104:107], s[40:41] offset:0 sc1\n\t" \
    "v_mfma_f32_16x16x32_bf16 v[96:99], a[88:91], v[56:59], v[96:99]\n\t" \
    "s_add_u32 m0, s91, 4096\n\t" \
    "v_cvt_pk_bf16_f32 v108, v76, v77\n\t" \
    "v_mfma_f32_16x16x32_bf16 v[100:103], a[88:91], v[60:63], v[100:103]\n\t" \
    "global_load_lds_dwordx4 v207, s[52:53]\n\t" \
    "s_waitcnt lgkmcnt(5)\n\t" \
    "v_mfma_f32_16x16x32_bf16 v[88:91], a[20:23], v[64:67], v[88:91]\n\t" \
    "ds_read_b128 v[40:43], v1 offset:512\n\t" \
    "s_waitcnt lgkmcnt(5)\n\t" \
    "v_mfma_f32_16x16x32_bf16 v[92:95], a[20:23], v[68:71], v[92:95]\n\t" \
    "ds_read_b128 v[44:47], v1 offset:18944\n\t" \
    "v_cvt_pk_bf16_f32 v109, v78, v79\n\t" \
    "v_mfma_f32_16x16x32_bf16 v[96:99], a[92:95], v[64:67], v[96:99]\n\t" \
    "s_add_u32 m0, s91, 5120\n\t" \
    "v_cvt_pk_bf16_f32 v110, v84, v85\n\t" \
    "v_mfma_f32_16x16x32_bf16 v[100:103], a[92:95], v[68:71], v[100:103]\n\t" \
    "global_load_lds_dwordx4 v208, s[52:53]\n\t" \
    "s_waitcnt lgkmcnt(5)\n\t" \
    "v_mfma_f32_16x16x32_bf16 v[88:91], a[24:27], v[24:27], v[88:91]\n\t" \
    "ds_read_b128 v[48:51], v2 offset:512\n\t" \
    "s_waitcnt lgkmcnt(5)\n\t" \
    "v_mfma_f32_16x16x32_bf16 v[92:95], a[24:27], v[28:31], v[92:95]\n\t" \
    "ds_read_b128 v[52:55], v2 offset:18944\n\t" \
    "v_cvt_pk_bf16_f32 v111, v86, v87\n\t" \
    "v_mfma_f32_16x16x32_bf16 v[96:99], a[96:99], v[24:27], v[96:99]\n\t" \
    "s_add_u32 m0, s91, 6144\n\t" \
    "global_store_dwordx4 v8, v[108:111], s[40:41] offset:1024 sc1\n\t" \
    "v_mfma_f32_16x16x32_bf16 v[100:103], a[96:99], v[28:31], v[100:103]\n\t" \
    "global_load_lds_dwordx4 v209, s[52:53]\n\t" \
    "s_waitcnt lgkmcnt(5)\n\t" \
    "v_mfma_f32_16x16x32_bf16 v[88:91], a[28:31], v[32:35], v[88:91]\n\t" \
    "ds_read_b128 v[56:59], v1 offset:640\n\t" \
    "s_waitcnt lgkmcnt(5)\n\t" \
    "v_mfma_f32_16x16x32_bf16 v[92:95], a[28:31], v[36:39], v[92:95]\n\t" \
    "ds_read_b128 v[60:63], v1 offset:19072\n\t" \
    "v_mfma_f32_16x16x32_bf16 v[96:99], a[100:103], v[32:35], v[96:99]\n\t" \
    "s_add_u32 m0, s91, 7168\n\t" \
    "v_mfma_f32_16x16x32_bf16 v[100:103], a[100:103], v[36:39], v[100:103]\n\t" \
    "global_load_lds_dwordx4 v210, s[52:53]\n\t" \
    "s_waitcnt lgkmcnt(5)\n\t" \
    "v_mfma_f32_16x16x32_bf16 v[88:91], a[32:35], v[40:43], v[88:91]\n\t" \
    "ds_read_b128 v[64:67], v2 offset:640\n\t" \
    "s_waitcnt lgkmcnt(5)\n\t" \
    "v_mfma_f32_16x16x32_bf16 v[92:95], a[32:35], v[44:47], v[92:95]\n\t" \
    "ds_read_b128 v[68:71], v2 offset:19072\n\t" \
    "v_mfma_f32_16x16x32_bf16 v[96:99], a[104:107], v[40:43], v[96:99]\n\t" \
    "s_add_u32 m0, s91, 8192\n\t" \
    "v_mfma_f32_16x16x32_bf16 v[100:103], a[104:107], v[44:47], v[100:103]\n\t" \
    "global_load_lds_dwordx4 v211, s[52:53]\n\t" \
    "s_waitcnt lgkmcnt(5)\n\t" \
    "v_mfma_f32_16x16x32_bf16 v[88:91], a[36:39], v[48:51], v[88:91]\n\t" \
    "ds_read_b128 v[24:27], v1 offset:768\n\t" \
    "s_waitcnt lgkmcnt(5)\n\t" \
    "v_mfma_f32_16x16x32_bf16 v[92:95], a[36:39], v[52:55], v[92:95]\n\t" \
    "ds_read_b128 v[28:31], v1 offset:19200\n\t" \
    "v_mfma_f32_16x16x32_bf16 v[96:99], a[108:111], v[48:51], v[96:99]\n\t" \
    "v_mfma_f32_16x16x32_bf16 v[100:103], a[108:111], v[52:55], v[100:103]\n\t" \
    "s_waitcnt lgkmcnt(5)\n\t" \
    "v_mfma_f32_16x16x32_bf16 v[88:91], a[40:43], v[56:59], v[88:91]\n\t" \
    "ds_read_b128 v[32:35], v2 offset:768\n\t" \
    "s_waitcnt lgkmcnt(5)\n\t" \
    "v_mfma_f32_16x16x32_bf16 v[92:95], a[40:43], v[60:63], v[92:95]\n\t" \
    "ds_read_b128 v[36:39], v2 offset:19200\n\t" \
    "v_mfma_f32_16x16x32_bf16 v[96:99], a[112:115], v[56:59], v[96:99]\n\t" \
    "s_waitcnt vmcnt(11)\n\t" \
    "s_barrier\n\t" \
    "v_mfma_f32_16x16x32_bf16 v[100:103], a[112:115], v[60:63], v[100:103]\n\t" \
    "s_cmp_lg_u32 s66, 0\n\t" \
    "s_cbranch_scc1 gx_SF13_%=\n\t" \
    "s_lshl_b32 s69, s67, 7\n\t" \
    "s_add_u32 s69, s69, 5849088\n\t" \
    "s_add_u32 s70, s65, -1\n\t" \
    "v_mov_b32 v156, s69\n\t" \
    "v_mov_b32 v157, s70\n\t" \
    "s_mov_b64 exec, 1\n\t" \
    "global_store_dword v156, v157, s[36:37] sc1\n\t" \
    "s_mov_b64 exec, -1\n\t" \
    "gx_SF13_%=:\n\t" \
    "s_waitcnt lgkmcnt(5)\n\t" \
    "v_mfma_f32_16x16x32_bf16 v[88:91], a[44:47], v[64:67], v[88:91]\n\t" \
    "ds_read_b128 v[40:43], v1 offset:896\n\t" \
    "s_waitcnt lgkmcnt(5)\n\t" \
    "v_mfma_f32_16x16x32_bf16 v[92:95], a[44:47], v[68:71], v[92:95]\n\t" \
    "ds_read_b128 v[44:47], v1 offset:19328\n\t" \
    "v_mfma_f32_16x16x32_bf16 v[96:99], a[116:119], v[64:67], v[96:99]\n\t" \
    "v_add_u32 v22, 0x9000, v1\n\t" \
    "v_add_u32 v23, 0x9000, v2\n\t" \
    "v_mfma_f32_16x16x32_bf16 v[100:103], a[116:119], v[68:71], v[100:103]\n\t" \
    "s_cmp_lt_u32 s65, 54\n\t" \
    "s_cselect_b32 s84, 10, 2\n\t" \
    "s_waitcnt lgkmcnt(5)\n\t" \
    "v_mfma_f32_16x16x32_bf16 v[88:91], a[48:51], v[24:27], v[88:91]\n\t" \
    "ds_read_b128 v[48:51], v2 offset:896\n\t" \
    "s_waitcnt lgkmcnt(5)\n\t" \
    "v_mfma_f32_16x16x32_bf16 v[92:95], a[48:51], v[28:31], v[92:95]\n\t" \
    "ds_read_b128 v[52:55], v2 offset:19328\n\t" \
    "v_mfma_f32_16x16x32_bf16 v[96:99], a[120:123], v[24:27], v[96:99]\n\t" \
    "s_add_u32 s84, s65, s84\n\t" \
    "s_min_u32 s69, s84, 63\n\t" \
    "v_mfma_f32_16x16x32_bf16 v[100:103], a[120:123], v[28:31], v[100:103]\n\t" \
    "s_mul_i32 s69, s69, 36864\n\t" \
    "s_add_u32 s48, s50, s69\n\t" \
    "s_waitcnt lgkmcnt(5)\n\t" \
    "v_mfma_f32_16x16x32_bf16 v[88:91], a[52:55], v[32:35], v[88:91]\n\t" \
    "ds_read_b128 v[56:59], v1 offset:1024\n\t" \
    "s_waitcnt lgkmcnt(5)\n\t" \
    "v_mfma_f32_16x16x32_bf16 v[92:95], a[52:55], v[36:39], v[92:95]\n\t" \
    "ds_read_b128 v[60:63], v1 offset:19456\n\t" \
    "v_mfma_f32_16x16x32_bf16 v[96:99], a[124:127], v[32:35], v[96:99]\n\t" \
    "s_addc_u32 s49, s51, 0\n\t" \
    "s_mul_i32 s70, s67, 7\n\t" \
    "v_mfma_f32_16x16x32_bf16 v[100:103], a[124:127], v[36:39], v[100:103]\n\t" \
    "s_add_u32 s69, s65, 0\n\t" \
    "s_xor_b32 s69, s69, s70\n\t" \
    "s_waitcnt lgkmcnt(5)\n\t" \
    "v_mfma_f32_16x16x32_bf16 v[88:91], a[56:59], v[40:43], v[88:91]\n\t" \
    "ds_read_b128 v[64:67], v2 offset:1024\n\t" \
    "s_waitcnt lgkmcnt(5)\n\t" \
    "v_mfma_f32_16x16x32_bf16 v[92:95], a[56:59], v[44:47], v[92:95]\n\t" \
    "ds_read_b128 v[68:71], v2 offset:19456\n\t" \
    "v_mfma_f32_16x16x32_bf16 v[96:99], a[128:131], v[40:43], v[96:99]\n\t" \
    "s_and_b32 s69, s69, 63\n\t" \
    "s_lshl_b32 s69, s69, 13\n\t" \
    "v_mfma_f32_16x16x32_bf16 v[100:103], a[128:131], v[44:47], v[100:103]\n\t" \
    "s_mul_i32 s70, s67, 524288\n\t" \
    "s_add_u32 s69, s69, s70\n\t" \
    "s_waitcnt lgkmcnt(5)\n\t" \
    "v_mfma_f32_16x16x32_bf16 v[88:91], a[60:63], v[48:51], v[88:91]\n\t" \
    "ds_read_b128 v[24:27], v22 offset:0\n\t" \
    "s_waitcnt lgkmcnt(5)\n\t" \
    "v_mfma_f32_16x16x32_bf16 v[92:95], a[60:63], v[52:55], v[92:95]\n\t" \
    "ds_read_b128 v[28:31], v22 offset:18432\n\t" \
    "v_mfma_f32_16x16x32_bf16 v[96:99], a[132:135], v[48:51], v[96:99]\n\t" \
    "s_add_u32 s69, s69, 6291456\n\t" \
    "s_add_u32 s38, s36, s69\n\t" \
    "v_mfma_f32_16x16x32_bf16 v[100:103], a[132:135], v[52:55], v[100:103]\n\t" \
    "s_addc_u32 s39, s37, 0\n\t" \
    "s_waitcnt lgkmcnt(5)\n\t" \
    "v_mfma_f32_16x16x32_bf16 v[88:91], a[64:67], v[56:59], v[88:91]\n\t" \
    "ds_read_b128 v[32:35], v23 offset:0\n\t" \
    "s_waitcnt lgkmcnt(5)\n\t" \
    "v_mfma_f32_16x16x32_bf16 v[92:95], a[64:67], v[60:63], v[92:95]\n\t" \
    "ds_read_b128 v[36:39], v23 offset:18432\n\t" \
    "v_mfma_f32_16x16x32_bf16 v[96:99], a[136:139], v[56:59], v[96:99]\n\t" \
    "v_mfma_f32_16x16x32_bf16 v[100:103], a[136:139], v[60:63], v[100:103]\n\t" \
    "s_waitcnt lgkmcnt(5)\n\t" \
    "v_mfma_f32_16x16x32_bf16 v[88:91], a[68:71], v[64:67], v[88:91]\n\t" \
    "ds_read_b128 v[40:43], v22 offset:128\n\t" \
    "s_waitcnt lgkmcnt(5)\n\t" \
    "v_mfma_f32_16x16x32_bf16 v[92:95], a[68:71], v[68:71], v[92:95]\n\t" \
    "ds_read_b128 v[44:47], v22 offset:18560\n\t" \
    "v_mfma_f32_16x16x32_bf16 v[96:99], a[140:143], v[64:67], v[96:99]\n\t" \
    "v_mfma_f32_16x16x32_bf16 v[100:103], a[140:143], v[68:71], v[100:103]\n\t" \
    "s_waitcnt lgkmcnt(5)\n\t" \
    "v_mfma_f32_16x16x32_bf16 v[72:75], a[144:147], v[24:27], 0\n\t" \
    "ds_read_b128 v[48:51], v23 offset:128\n\t" \
    "s_waitcnt lgkmcnt(5)\n\t" \
    "v_mfma_f32_16x16x32_bf16 v[76:79], a[144:147], v[28:31], 0\n\t" \
    "ds_read_b128 v[52:55], v23 offset:18560\n\t" \
    "v_mfma_f32_16x16x32_bf16 v[80:83], a[216:219], v[24:27], 0\n\t" \
    "s_add_u32 m0, s91, 36864\n\t" \
    "v_mfma_f32_16x16x32_bf16 v[84:87], a[216:219], v[28:31], 0\n\t" \
    "global_load_lds_dwordx4 v203, s[48:49]\n\t" \
    "s_waitcnt lgkmcnt(5)\n\t" \
    "v_mfma_f32_16x16x32_bf16 v[72:75], a[148:151], v[32:35], v[72:75]\n\t" \
    "ds_read_b128 v[56:59], v22 offset:256\n\t" \
    "s_waitcnt lgkmcnt(5)\n\t" \
    "v_mfma_f32_16x16x32_bf16 v[76:79], a[148:151], v[36:39], v[76:79]\n\t" \
    "ds_read_b128 v[60:63], v22 offset:18688\n\t" \
    "v_mfma_f32_16x16x32_bf16 v[80:83], a[220:223], v[32:35], v[80:83]\n\t" \
    "s_add_u32 m0, s91, 37888\n\t" \
    "v_mfma_f32_16x16x32_bf16 v[84:87], a[220:223], v[36:39], v[84:87]\n\t" \
    "global_load_lds_dwordx4 v204, s[48:49]\n\t" \
    "s_waitcnt lgkmcnt(5)\n\t" \
    "v_mfma_f32_16x16x32_bf16 v[72:75], a[152:155], v[40:43], v[72:75]\n\t" \
    "ds_read_b128 v[64:67], v23 offset:256\n\t" \
    "s_waitcnt lgkmcnt(5)\n\t" \
    "v_mfma_f32_16x16x32_bf16 v[76:79], a[152:155], v[44:47], v[76:79]\n\t" \
    "ds_read_b128 v[68:71], v23 offset:18688\n\t" \
    "v_cvt_pk_bf16_f32 v104, v88, v89\n\t" \
    "v_mfma_f32_16x16x32_bf16 v[80:83], a[224:227], v[40:43], v[80:83]\n\t" \
    "s_add_u32 m0, s91, 38912\n\t" \
    "v_cvt_pk_bf16_f32 v105, v90, v91\n\t" \
    "v_mfma_f32_16x16x32_bf16 v[84:87], a[224:227], v[44:47], v[84:87]\n\t" \
    "global_load_lds_dwordx4 v205, s[48:49]\n\t" \
    "s_waitcnt lgkmcnt(5)\n\t" \
    "v_mfma_f32_16x16x32_bf16 v[72:75], a[156:159], v[48:51], v[72:75]\n\t" \
    "ds_read_b128 v[24:27], v22 offset:384\n\t" \
    "s_waitcnt lgkmcnt(5)\n\t" \
    "v_mfma_f32_16x16x32_bf16 v[76:79], a[156:159], v[52:55], v[76:79]\n\t" \
    "ds_read_b128 v[28:31], v22 offset:18816\n\t" \
    "v_cvt_pk_bf16_f32 v106, v96, v97\n\t" \
    "v_mfma_f32_16x16x32_bf16 v[80:83], a[228:231], v[48:51], v[80:83]\n\t" \
    "s_add_u32 m0, s91, 39936\n\t" \
    "v_cvt_pk_bf16_f32 v107, v98, v99\n\t" \
    "v_mfma_f32_16x16x32_bf16 v[84:87], a[228:231], v[52:55], v[84:87]\n\t" \
    "global_load_lds_dwordx4 v206, s[48:49]\n\t" \
    "s_waitcnt lgkmcnt(5)\n\t" \
    "v_mfma_f32_16x16x32_bf16 v[72:75], a[160:163], v[56:59], v[72:75]\n\t" \
    "ds_read_b128 v[32:35], v23 offset:384\n\t" \
    "s_waitcnt lgkmcnt(5)\n\t" \
    "v_mfma_f32_16x16x32_bf16 v[76:79], a[160:163], v[60:63], v[76:79]\n\t" \
    "ds_read_b128 v[36:39], v23 offset:18816\n\t" \
    "global_store_dwordx4 v8, v[104:107], s[38:39] offset:0 sc1\n\t" \
    "v_mfma_f32_16x16x32_bf16 v[80:83], a[232:235], v[56:59], v[80:83]\n\t" \
    "s_add_u32 m0, s91, 40960\n\t" \
    "v_cvt_pk_bf16_f32 v108, v92, v93\n\t" \
    "v_mfma_f32_16x16x32_bf16 v[84:87], a[232:235], v[60:63], v[84:87]\n\t" \
    "global_load_lds_dwordx4 v207, s[48:49]\n\t" \
    "s_waitcnt lgkmcnt(5)\n\t" \
    "v_mfma_f32_16x16x32_bf16 v[72:75], a[164:167], v[64:67], v[72:75]\n\t" \
    "ds_read_b128 v[40:43], v22 offset:512\n\t" \
    "s_waitcnt lgkmcnt(5)\n\t" \
    "v_mfma_f32_16x16x32_bf16 v[76:79], a[164:167], v[68:71], v[76:79]\n\t" \
    "ds_read_b128 v[44:47], v22 offset:18944\n\t" \
    "v_cvt_pk_bf16_f32 v109, v94, v95\n\t" \
    "v_mfma_f32_16x16x32_bf16 v[80:83], a[236:239], v[64:67], v[80:83]\n\t" \
    "s_add_u32 m0, s91, 41984\n\t" \
    "v_cvt_pk_bf16_f32 v110, v100, v101\n\t" \
    "v_mfma_f32_16x16x32_bf16 v[84:87], a[236:239], v[68:71], v[84:87]\n\t" \
    "global_load_lds_dwordx4 v208, s[48:49]\n\t" \
    "s_waitcnt lgkmcnt(5)\n\t" \
    "v_mfma_f32_16x16x32_bf16 v[72:75], a[168:171], v[24:27], v[72:75]\n\t" \
    "ds_read_b128 v[48:51], v23 offset:512\n\t" \
    "s_waitcnt lgkmcnt(5)\n\t" \
    "v_mfma_f32_16x16x32_bf16 v[76:79], a[168:171], v[28:31], v[76:79]\n\t" \
    "ds_read_b128 v[52:55], v23 offset:18944\n\t" \
    "v_cvt_pk_bf16_f32 v111, v102, v103\n\t" \
    "v_mfma_f32_16x16x32_bf16 v[80:83], a[240:243], v[24:27], v[80:83]\n\t" \
    "s_add_u32 m0, s91, 43008\n\t" \
    "global_store_dwordx4 v8, v[108:111], s[38:39] offset:1024 sc1\n\t" \
    "v_mfma_f32_16x16x32_bf16 v[84:87], a[240:243], v[28:31], v[84:87]\n\t" \
    "global_load_lds_dwordx4 v209, s[48:49]\n\t" \
    "s_waitcnt lgkmcnt(5)\n\t" \
    "v_mfma_f32_16x16x32_bf16 v[72:75], a[172:175], v[32:35], v[72:75]\n\t" \
    "ds_read_b128 v[56:59], v22 offset:640\n\t" \
    "s_waitcnt lgkmcnt(5)\n\t" \
    "v_mfma_f32_16x16x32_bf16 v[76:79], a[172:175], v[36:39], v[76:79]\n\t" \
    "ds_read_b128 v[60:63], v22 offset:19072\n\t" \
    "v_mfma_f32_16x16x32_bf16 v[80:83], a[244:247], v[32:35], v[80:83]\n\t" \
    "s_add_u32 m0, s91, 44032\n\t" \
    "v_mfma_f32_16x16x32_bf16 v[84:87], a[244:247], v[36:39], v[84:87]\n\t" \
    "global_load_lds_dwordx4 v210, s[48:49]\n\t" \
    "s_waitcnt lgkmcnt(5)\n\t" \
    "v_mfma_f32_16x16x32_bf16 v[72:75], a[176:179], v[40:43], v[72:75]\n\t" \
    "ds_read_b128 v[64:67], v23 offset:640\n\t" \
    "s_waitcnt lgkmcnt(5)\n\t" \
    "v_mfma_f32_16x16x32_bf16 v[76:79], a[176:179], v[44:47], v[76:79]\n\t" \
    "ds_read_b128 v[68:71], v23 offset:19072\n\t" \
    "v_mfma_f32_16x16x32_bf16 v[80:83], a[248:251], v[40:43], v[80:83]\n\t" \
    "s_add_u32 m0, s91, 45056\n\t" \
    "v_mfma_f32_16x16x32_bf16 v[84:87], a[248:251], v[44:47], v[84:87]\n\t" \
    "global_load_lds_dwordx4 v211, s[48:49]\n\t" \
    "s_waitcnt lgkmcnt(5)\n\t" \
    "v_mfma_f32_16x16x32_bf16 v[72:75], a[180:183], v[48:51], v[72:75]\n\t" \
    "ds_read_b128 v[24:27], v22 offset:768\n\t" \
    "s_waitcnt lgkmcnt(5)\n\t" \
    "v_mfma_f32_16x16x32_bf16 v[76:79], a[180:183], v[52:55], v[76:79]\n\t" \
    "ds_read_b128 v[28:31], v22 offset:19200\n\t" \
    "v_mfma_f32_16x16x32_bf16 v[80:83], a[252:255], v[48:51], v[80:83]\n\t" \
    "v_mfma_f32_16x16x32_bf16 v[84:87], a[252:255], v[52:55], v[84:87]\n\t" \
    "s_waitcnt lgkmcnt(5)\n\t" \
    "v_mfma_f32_16x16x32_bf16 v[72:75], a[184:187], v[56:59], v[72:75]\n\t" \
    "ds_read_b128 v[32:35], v23 offset:768\n\t" \
    "s_waitcnt lgkmcnt(5)\n\t" \
    "v_mfma_f32_16x16x32_bf16 v[76:79], a[184:187], v[60:63], v[76:79]\n\t" \
    "ds_read_b128 v[36:39], v23 offset:19200\n\t" \
    "v_mfma_f32_16x16x32_bf16 v[80:83], v[224:227], v[56:59], v[80:83]\n\t" \
    "s_waitcnt vmcnt(11)\n\t" \
    "s_barrier\n\t" \
    "v_mfma_f32_16x16x32_bf16 v[84:87], v[224:227], v[60:63], v[84:87]\n\t" \
    "s_add_u32 s71, s65, 8\n\t" \
    "s_cmp_lg_u32 s66, 0\n\t" \
    "s_cbranch_scc1 gx_SF14_%=\n\t" \
    "s_lshl_b32 s69, s67, 7\n\t" \
    "s_add_u32 s69, s69, 5771264\n\t" \
    "s_add_u32 s70, s71, 0\n\t" \
    "v_mov_b32 v156, s69\n\t" \
    "v_mov_b32 v157, s70\n\t" \
    "s_mov_b64 exec, 1\n\t" \
    "global_store_dword v156, v157, s[36:37] sc1\n\t" \
    "s_mov_b64 exec, -1\n\t" \
    "gx_SF14_%=:\n\t" \
    "s_waitcnt lgkmcnt(5)\n\t" \
    "v_mfma_f32_16x16x32_bf16 v[72:75], a[188:191], v[64:67], v[72:75]\n\t" \
    "ds_read_b128 v[40:43], v22 offset:896\n\t" \
    "s_waitcnt lgkmcnt(5)\n\t" \
    "v_mfma_f32_16x16x32_bf16 v[76:79], a[188:191], v[68:71], v[76:79]\n\t" \
    "ds_read_b128 v[44:47], v22 offset:19328\n\t" \
    "v_mfma_f32_16x16x32_bf16 v[80:83], v[228:231], v[64:67], v[80:83]\n\t" \
    "s_xor_b32 s90, s90, 0x12000\n\t" \
    "v_add_u32 v1, s90, v0\n\t" \
    "v_mfma_f32_16x16x32_bf16 v[84:87], v[228:231], v[68:71], v[84:87]\n\t" \
    "v_xor_b32 v2, 64, v1\n\t" \
    "s_add_u32 s84, s65, 2\n\t" \
    "s_waitcnt lgkmcnt(5)\n\t" \
    "v_mfma_f32_16x16x32_bf16 v[72:75], a[192:195], v[24:27], v[72:75]\n\t" \
    "ds_read_b128 v[48:51], v23 offset:896\n\t" \
    "s_waitcnt lgkmcnt(5)\n\t" \
    "v_mfma_f32_16x16x32_bf16 v[76:79], a[192:195], v[28:31], v[76:79]\n\t" \
    "ds_read_b128 v[52:55], v23 offset:19328\n\t" \
    "v_mfma_f32_16x16x32_bf16 v[80:83], v[232:235], v[24:27], v[80:83]\n\t" \
    "s_min_u32 s69, s84, 63\n\t" \
    "s_mul_i32 s69, s69, 36864\n\t" \
    "v_mfma_f32_16x16x32_bf16 v[84:87], v[232:235], v[28:31], v[84:87]\n\t" \
    "s_add_u32 s52, s50, s69\n\t" \
    "s_addc_u32 s53, s51, 0\n\t" \
    "s_waitcnt lgkmcnt(5)\n\t" \
    "v_mfma_f32_16x16x32_bf16 v[72:75], a[196:199], v[32:35], v[72:75]\n\t" \
    "ds_read_b128 v[56:59], v22 offset:1024\n\t" \
    "s_waitcnt lgkmcnt(5)\n\t" \
    "v_mfma_f32_16x16x32_bf16 v[76:79], a[196:199], v[36:39], v[76:79]\n\t" \
    "ds_read_b128 v[60:63], v22 offset:19456\n\t" \
    "v_mfma_f32_16x16x32_bf16 v[80:83], v[236:239], v[32:35], v[80:83]\n\t" \
    "s_mul_i32 s70, s67, 7\n\t" \
    "s_add_u32 s69, s65, 9\n\t" \
    "v_mfma_f32_16x16x32_bf16 v[84:87], v[236:239], v[36:39], v[84:87]\n\t" \
    "s_xor_b32 s69, s69, s70\n\t" \
    "s_and_b32 s69, s69, 63\n\t" \
    "s_waitcnt lgkmcnt(5)\n\t" \
    "v_mfma_f32_16x16x32_bf16 v[72:75], a[200:203], v[40:43], v[72:75]\n\t" \
    "ds_read_b128 v[64:67], v23 offset:1024\n\t" \
    "s_waitcnt lgkmcnt(5)\n\t" \
    "v_mfma_f32_16x16x32_bf16 v[76:79], a[200:203], v[44:47], v[76:79]\n\t" \
    "ds_read_b128 v[68:71], v23 offset:19456\n\t" \
    "v_mfma_f32_16x16x32_bf16 v[80:83], v[240:243], v[40:43], v[80:83]\n\t" \
    "s_lshl_b32 s69, s69, 13\n\t" \
    "s_mul_i32 s70, s67, 524288\n\t" \
    "v_mfma_f32_16x16x32_bf16 v[84:87], v[240:243], v[44:47], v[84:87]\n\t" \
    "s_add_u32 s69, s69, s70\n\t" \
    "s_add_u32 s69, s69, 23068672\n\t" \
    "s_waitcnt lgkmcnt(5)\n\t" \
    "v_mfma_f32_16x16x32_bf16 v[72:75], a[204:207], v[48:51], v[72:75]\n\t" \
    "ds_read_b128 v[24:27], v1 offset:0\n\t" \
    "s_waitcnt lgkmcnt(5)\n\t" \
    "v_mfma_f32_16x16x32_bf16 v[76:79], a[204:207], v[52:55], v[76:79]\n\t" \
    "ds_read_b128 v[28:31], v1 offset:18432\n\t" \
    "v_mfma_f32_16x16x32_bf16 v[80:83], v[244:247], v[48:51], v[80:83]\n\t" \
    "s_add_u32 s40, s36, s69\n\t" \
    "s_addc_u32 s41, s37, 0\n\t" \
    "v_mfma_f32_16x16x32_bf16 v[84:87], v[244:247], v[52:55], v[84:87]\n\t" \
    "s_sub_u32 s91, s85, s91\n\t" \
    "s_add_u32 s65, s65, 1\n\t" \
    "s_waitcnt lgkmcnt(5)\n\t" \
    "v_mfma_f32_16x16x32_bf16 v[72:75], a[208:211], v[56:59], v[72:75]\n\t" \
    "ds_read_b128 v[32:35], v2 offset:0\n\t" \
    "s_waitcnt lgkmcnt(5)\n\t" \
    "v_mfma_f32_16x16x32_bf16 v[76:79], a[208:211], v[60:63], v[76:79]\n\t" \
    "ds_read_b128 v[36:39], v2 offset:18432\n\t" \
    "v_mfma_f32_16x16x32_bf16 v[80:83], v[248:251], v[56:59], v[80:83]\n\t" \
    "v_mfma_f32_16x16x32_bf16 v[84:87], v[248:251], v[60:63], v[84:87]\n\t" \
    "s_waitcnt lgkmcnt(5)\n\t" \
    "v_mfma_f32_16x16x32_bf16 v[72:75], a[212:215], v[64:67], v[72:75]\n\t" \
    "ds_read_b128 v[40:43], v1 offset:128\n\t" \
    "s_waitcnt lgkmcnt(5)\n\t" \
    "v_mfma_f32_16x16x32_bf16 v[76:79], a[212:215], v[68:71], v[76:79]\n\t" \
    "ds_read_b128 v[44:47], v1 offset:18560\n\t" \
    "v_mfma_f32_16x16x32_bf16 v[80:83], v[252:255], v[64:67], v[80:83]\n\t" \
    "v_mfma_f32_16x16x32_bf16 v[84:87], v[252:255], v[68:71], v[84:87]\n\t" \
    "s_cmp_lt_u32 s65, 55\n\t" \
    "s_cbranch_scc1 gx_l28_%=\n\t" \
    "v_cvt_pk_bf16_f32 v104, v72, v73\n\t" \
    "v_cvt_pk_bf16_f32 v105, v74, v75\n\t" \
    "s_nop 2\n\t" \
    "v_cvt_pk_bf16_f32 v106, v80, v81\n\t" \
    "v_cvt_pk_bf16_f32 v107, v82, v83\n\t" \
    "global_store_dwordx4 v8, v[104:107], s[40:41] offset:0 sc1\n\t" \
    "v_cvt_pk_bf16_f32 v108, v76, v77\n\t" \
    "v_cvt_pk_bf16_f32 v109, v78, v79\n\t" \
    "v_cvt_pk_bf16_f32 v110, v84, v85\n\t" \
    "v_cvt_pk_bf16_f32 v111, v86, v87\n\t" \
    "global_store_dwordx4 v8, v[108:111], s[40:41] offset:1024 sc1\n\t" \
    "gx_l315_%=:\n\t" \
    "s_add_u32 s84, s65, 2\n\t" \
    "s_min_u32 s69, s84, 63\n\t" \
    "s_mul_i32 s69, s69, 36864\n\t" \
    "s_add_u32 s52, s50, s69\n\t" \
    "s_addc_u32 s53, s51, 0\n\t" \
    "s_add_u32 s90, s90, 36864\n\t" \
    "s_cmp_eq_u32 s90, 147456\n\t" \
    "s_cselect_b32 s90, 0, s90\n\t" \
    "s_add_u32 s91, s90, 36864\n\t" \
    "s_cmp_eq_u32 s91, 147456\n\t" \
    "s_cselect_b32 s91, 0, s91\n\t" \
    "s_add_u32 s91, s91, s89\n\t" \
    "v_add_u32 v22, s90, v0\n\t" \
    "v_xor_b32 v23, 64, v22\n\t" \
    "s_mul_i32 s70, s67, 7\n\t" \
    "s_add_u32 s69, s65, 0\n\t" \
    "s_xor_b32 s69, s69, s70\n\t" \
    "s_and_b32 s69, s69, 63\n\t" \
    "s_lshl_b32 s69, s69, 13\n\t" \
    "s_mul_i32 s70, s67, 524288\n\t" \
    "s_add_u32 s69, s69, s70\n\t" \
    "s_add_u32 s69, s69, 6291456\n\t" \
    "s_add_u32 s38, s36, s69\n\t" \
    "s_addc_u32 s39, s37, 0\n\t" \
    "s_waitcnt lgkmcnt(5)\n\t" \
    "v_mfma_f32_16x16x32_bf16 v[88:91], a[0:3], v[24:27], 0\n\t" \
    "ds_read_b128 v[48:51], v2 offset:128\n\t" \
    "s_waitcnt lgkmcnt(5)\n\t" \
    "v_mfma_f32_16x16x32_bf16 v[92:95], a[0:3], v[28:31], 0\n\t" \
    "ds_read_b128 v[52:55], v2 offset:18560\n\t" \
    "v_mfma_f32_16x16x32_bf16 v[96:99], a[72:75], v[24:27], 0\n\t" \
    "s_add_u32 m0, s91, 0\n\t" \
    "v_mfma_f32_16x16x32_bf16 v[100:103], a[72:75], v[28:31], 0\n\t" \
    "global_load_lds_dwordx4 v203, s[52:53]\n\t" \
    "s_waitcnt lgkmcnt(5)\n\t" \
    "v_mfma_f32_16x16x32_bf16 v[88:91], a[4:7], v[32:35], v[88:91]\n\t" \
    "ds_read_b128 v[56:59], v1 offset:256\n\t" \
    "s_waitcnt lgkmcnt(5)\n\t" \
    "v_mfma_f32_16x16x32_bf16 v[92:95], a[4:7], v[36:39], v[92:95]\n\t" \
    "ds_read_b128 v[60:63], v1 offset:18688\n\t" \
    "v_mfma_f32_16x16x32_bf16 v[96:99], a[76:79], v[32:35], v[96:99]\n\t" \
    "s_add_u32 m0, s91, 1024\n\t" \
    "v_mfma_f32_16x16x32_bf16 v[100:103], a[76:79], v[36:39], v[100:103]\n\t" \
    "global_load_lds_dwordx4 v204, s[52:53]\n\t" \
    "s_waitcnt lgkmcnt(5)\n\t" \
    "v_mfma_f32_16x16x32_bf16 v[88:91], a[8:11], v[40:43], v[88:91]\n\t" \
    "ds_read_b128 v[64:67], v2 offset:256\n\t" \
    "s_waitcnt lgkmcnt(5)\n\t" \
    "v_mfma_f32_16x16x32_bf16 v[92:95], a[8:11], v[44:47], v[92:95]\n\t" \
    "ds_read_b128 v[68:71], v2 offset:18688\n\t" \
    "v_mfma_f32_16x16x32_bf16 v[96:99], a[80:83], v[40:43], v[96:99]\n\t" \
    "s_add_u32 m0, s91, 2048\n\t" \
    "v_mfma_f32_16x16x32_bf16 v[100:103], a[80:83], v[44:47], v[100:103]\n\t" \
    "global_load_lds_dwordx4 v205, s[52:53]\n\t" \
    "s_waitcnt lgkmcnt(5)\n\t" \
    "v_mfma_f32_16x16x32_bf16 v[88:91], a[12:15], v[48:51], v[88:91]\n\t" \
    "ds_read_b128 v[24:27], v1 offset:384\n\t" \
    "s_waitcnt lgkmcnt(5)\n\t" \
    "v_mfma_f32_16x16x32_bf16 v[92:95], a[12:15], v[52:55], v[92:95]\n\t" \
    "ds_read_b128 v[28:31], v1 offset:18816\n\t" \
    "v_mfma_f32_16x16x32_bf16 v[96:99], a[84:87], v[48:51], v[96:99]\n\t" \
    "s_add_u32 m0, s91, 3072\n\t" \
    "v_mfma_f32_16x16x32_bf16 v[100:103], a[84:87], v[52:55], v[100:103]\n\t" \
    "global_load_lds_dwordx4 v206, s[52:53]\n\t" \
    "s_waitcnt lgkmcnt(5)\n\t" \
    "v_mfma_f32_16x16x32_bf16 v[88:91], a[16:19], v[56:59], v[88:91]\n\t" \
    "ds_read_b128 v[32:35], v2 offset:384\n\t" \
    "s_waitcnt lgkmcnt(5)\n\t" \
    "v_mfma_f32_16x16x32_bf16 v[92:95], a[16:19], v[60:63], v[92:95]\n\t" \
    "ds_read_b128 v[36:39], v2 offset:18816\n\t" \
    "v_mfma_f32_16x16x32_bf16 v[96:99], a[88:91], v[56:59], v[96:99]\n\t" \
    "s_add_u32 m0, s91, 4096\n\t" \
    "v_mfma_f32_16x16x32_bf16 v[100:103], a[88:91], v[60:63], v[100:103]\n\t" \
    "global_load_lds_dwordx4 v207, s[52:53]\n\t" \
    "s_waitcnt lgkmcnt(5)\n\t" \
    "v_mfma_f32_16x16x32_bf16 v[88:91], a[20:23], v[64:67], v[88:91]\n\t" \
    "ds_read_b128 v[40:43], v1 offset:512\n\t" \
    "s_waitcnt lgkmcnt(5)\n\t" \
    "v_mfma_f32_16x16x32_bf16 v[92:95], a[20:23], v[68:71], v[92:95]\n\t" \
    "ds_read_b128 v[44:47], v1 offset:18944\n\t" \
    "v_mfma_f32_16x16x32_bf16 v[96:99], a[92:95], v[64:67], v[96:99]\n\t" \
    "s_add_u32 m0, s91, 5120\n\t" \
    "v_mfma_f32_16x16x32_bf16 v[100:103], a[92:95], v[68:71], v[100:103]\n\t" \
    "global_load_lds_dwordx4 v208, s[52:53]\n\t" \
    "s_waitcnt lgkmcnt(5)\n\t" \
    "v_mfma_f32_16x16x32_bf16 v[88:91], a[24:27], v[24:27], v[88:91]\n\t" \
    "ds_read_b128 v[48:51], v2 offset:512\n\t" \
    "s_waitcnt lgkmcnt(5)\n\t" \
    "v_mfma_f32_16x16x32_bf16 v[92:95], a[24:27], v[28:31], v[92:95]\n\t" \
    "ds_read_b128 v[52:55], v2 offset:18944\n\t" \
    "v_mfma_f32_16x16x32_bf16 v[96:99], a[96:99], v[24:27], v[96:99]\n\t" \
    "s_add_u32 m0, s91, 6144\n\t" \
    "v_mfma_f32_16x16x32_bf16 v[100:103], a[96:99], v[28:31], v[100:103]\n\t" \
    "global_load_lds_dwordx4 v209, s[52:53]\n\t" \
    "s_waitcnt lgkmcnt(5)\n\t" \
    "v_mfma_f32_16x16x32_bf16 v[88:91], a[28:31], v[32:35], v[88:91]\n\t" \
    "ds_read_b128 v[56:59], v1 offset:640\n\t" \
    "s_waitcnt lgkmcnt(5)\n\t" \
    "v_mfma_f32_16x16x32_bf16 v[92:95], a[28:31], v[36:39], v[92:95]\n\t" \
    "ds_read_b128 v[60:63], v1 offset:19072\n\t" \
    "v_mfma_f32_16x16x32_bf16 v[96:99], a[100:103], v[32:35], v[96:99]\n\t" \
    "s_add_u32 m0, s91, 7168\n\t" \
    "v_mfma_f32_16x16x32_bf16 v[100:103], a[100:103], v[36:39], v[100:103]\n\t" \
    "global_load_lds_dwordx4 v210, s[52:53]\n\t" \
    "s_waitcnt lgkmcnt(5)\n\t" \
    "v_mfma_f32_16x16x32_bf16 v[88:91], a[32:35], v[40:43], v[88:91]\n\t" \
    "ds_read_b128 v[64:67], v2 offset:640\n\t" \
    "s_waitcnt lgkmcnt(5)\n\t" \
    "v_mfma_f32_16x16x32_bf16 v[92:95], a[32:35], v[44:47], v[92:95]\n\t" \
    "ds_read_b128 v[68:71], v2 offset:19072\n\t" \
    "v_mfma_f32_16x16x32_bf16 v[96:99], a[104:107], v[40:43], v[96:99]\n\t" \
    "s_add_u32 m0, s91, 8192\n\t" \
    "v_mfma_f32_16x16x32_bf16 v[100:103], a[104:107], v[44:47], v[100:103]\n\t" \
    "global_load_lds_dwordx4 v211, s[52:53]\n\t" \
    "s_waitcnt lgkmcnt(5)\n\t" \
    "v_mfma_f32_16x16x32_bf16 v[88:91], a[36:39], v[48:51], v[88:91]\n\t" \
    "ds_read_b128 v[24:27], v1 offset:768\n\t" \
    "s_waitcnt lgkmcnt(5)\n\t" \
    "v_mfma_f32_16x16x32_bf16 v[92:95], a[36:39], v[52:55], v[92:95]\n\t" \
    "ds_read_b128 v[28:31], v1 offset:19200\n\t" \
    "v_mfma_f32_16x16x32_bf16 v[96:99], a[108:111], v[48:51], v[96:99]\n\t" \
    "v_mfma_f32_16x16x32_bf16 v[100:103], a[108:111], v[52:55], v[100:103]\n\t" \
    "s_waitcnt lgkmcnt(5)\n\t" \
    "v_mfma_f32_16x16x32_bf16 v[88:91], a[40:43], v[56:59], v[88:91]\n\t" \
    "ds_read_b128 v[32:35], v2 offset:768\n\t" \
    "s_waitcnt lgkmcnt(5)\n\t" \
    "v_mfma_f32_16x16x32_bf16 v[92:95], a[40:43], v[60:63], v[92:95]\n\t" \
    "ds_read_b128 v[36:39], v2 offset:19200\n\t" \
    "v_mfma_f32_16x16x32_bf16 v[96:99], a[112:115], v[56:59], v[96:99]\n\t" \
    "s_waitcnt vmcnt(9)\n\t" \
    "s_barrier\n\t" \
    "v_mfma_f32_16x16x32_bf16 v[100:103], a[112:115], v[60:63], v[100:103]\n\t" \
    "s_cmp_lg_u32 s66, 0\n\t" \
    "s_cbranch_scc1 gx_SF16_%=\n\t" \
    "s_lshl_b32 s69, s67, 7\n\t" \
    "s_add_u32 s69, s69, 5849088\n\t" \
    "s_add_u32 s70, s65, -1\n\t" \
    "v_mov_b32 v156, s69\n\t" \
    "v_mov_b32 v157, s70\n\t" \
    "s_mov_b64 exec, 1\n\t" \
    "global_store_dword v156, v157, s[36:37] sc1\n\t" \
    "s_mov_b64 exec, -1\n\t" \
    "gx_SF16_%=:\n\t" \
    "s_cmp_lg_u32 s66, 1\n\t" \
    "s_cbranch_scc1 gx_SF17_%=\n\t" \
    "s_lshl_b32 s69, s67, 7\n\t" \
    "s_add_u32 s69, s69, 5771264\n\t" \
    "s_add_u32 s70, s65, 40\n\t" \
    "v_mov_b32 v156, s69\n\t" \
    "v_mov_b32 v157, s70\n\t" \
    "s_mov_b64 exec, 1\n\t" \
    "global_store_dword v156, v157, s[36:37] sc1\n\t" \
    "s_mov_b64 exec, -1\n\t" \
    "gx_SF17_%=:\n\t" \
    "s_waitcnt lgkmcnt(5)\n\t" \
    "v_mfma_f32_16x16x32_bf16 v[88:91], a[44:47], v[64:67], v[88:91]\n\t" \
    "ds_read_b128 v[40:43], v1 offset:896\n\t" \
    "s_waitcnt lgkmcnt(5)\n\t" \
    "v_mfma_f32_16x16x32_bf16 v[92:95], a[44:47], v[68:71], v[92:95]\n\t" \
    "ds_read_b128 v[44:47], v1 offset:19328\n\t" \
    "v_mfma_f32_16x16x32_bf16 v[96:99], a[116:119], v[64:67], v[96:99]\n\t" \
    "v_mfma_f32_16x16x32_bf16 v[100:103], a[116:119], v[68:71], v[100:103]\n\t" \
    "s_waitcnt lgkmcnt(5)\n\t" \
    "v_mfma_f32_16x16x32_bf16 v[88:91], a[48:51], v[24:27], v[88:91]\n\t" \
    "ds_read_b128 v[48:51], v2 offset:896\n\t" \
    "s_waitcnt lgkmcnt(5)\n\t" \
    "v_mfma_f32_16x16x32_bf16 v[92:95], a[48:51], v[28:31], v[92:95]\n\t" \
    "ds_read_b128 v[52:55], v2 offset:19328\n\t" \
    "v_mfma_f32_16x16x32_bf16 v[96:99], a[120:123], v[24:27], v[96:99]\n\t" \
    "v_mfma_f32_16x16x32_bf16 v[100:103], a[120:123], v[28:31], v[100:103]\n\t" \
    "s_waitcnt lgkmcnt(5)\n\t" \
    "v_mfma_f32_16x16x32_bf16 v[88:91], a[52:55], v[32:35], v[88:91]\n\t" \
    "ds_read_b128 v[56:59], v1 offset:1024\n\t" \
    "s_waitcnt lgkmcnt(5)\n\t" \
    "v_mfma_f32_16x16x32_bf16 v[92:95], a[52:55], v[36:39], v[92:95]\n\t" \
    "ds_read_b128 v[60:63], v1 offset:19456\n\t" \
    "v_mfma_f32_16x16x32_bf16 v[96:99], a[124:127], v[32:35], v[96:99]\n\t" \
    "v_mfma_f32_16x16x32_bf16 v[100:103], a[124:127], v[36:39], v[100:103]\n\t" \
    "s_waitcnt lgkmcnt(5)\n\t" \
    "v_mfma_f32_16x16x32_bf16 v[88:91], a[56:59], v[40:43], v[88:91]\n\t" \
    "ds_read_b128 v[64:67], v2 offset:1024\n\t" \
    "s_waitcnt lgkmcnt(5)\n\t" \
    "v_mfma_f32_16x16x32_bf16 v[92:95], a[56:59], v[44:47], v[92:95]\n\t" \
    "ds_read_b128 v[68:71], v2 offset:19456\n\t" \
    "v_mfma_f32_16x16x32_bf16 v[96:99], a[128:131], v[40:43], v[96:99]\n\t" \
    "v_mfma_f32_16x16x32_bf16 v[100:103], a[128:131], v[44:47], v[100:103]\n\t" \
    "s_waitcnt lgkmcnt(5)\n\t" \
    "v_mfma_f32_16x16x32_bf16 v[88:91], a[60:63], v[48:51], v[88:91]\n\t" \
    "ds_read_b128 v[24:27], v22 offset:0\n\t" \
    "s_waitcnt lgkmcnt(5)\n\t" \
    "v_mfma_f32_16x16x32_bf16 v[92:95], a[60:63], v[52:55], v[92:95]\n\t" \
    "ds_read_b128 v[28:31], v22 offset:18432\n\t" \
    "v_mfma_f32_16x16x32_bf16 v[96:99], a[132:135], v[48:51], v[96:99]\n\t" \
    "v_mfma_f32_16x16x32_bf16 v[100:103], a[132:135], v[52:55], v[100:103]\n\t" \
    "s_waitcnt lgkmcnt(5)\n\t" \
    "v_mfma_f32_16x16x32_bf16 v[88:91], a[64:67], v[56:59], v[88:91]\n\t" \
    "ds_read_b128 v[32:35], v23 offset:0\n\t" \
    "s_waitcnt lgkmcnt(5)\n\t" \
    "v_mfma_f32_16x16x32_bf16 v[92:95], a[64:67], v[60:63], v[92:95]\n\t" \
    "ds_read_b128 v[36:39], v23 offset:18432\n\t" \
    "v_mfma_f32_16x16x32_bf16 v[96:99], a[136:139], v[56:59], v[96:99]\n\t" \
    "v_mfma_f32_16x16x32_bf16 v[100:103], a[136:139], v[60:63], v[100:103]\n\t" \
    "s_waitcnt lgkmcnt(5)\n\t" \
    "v_mfma_f32_16x16x32_bf16 v[88:91], a[68:71], v[64:67], v[88:91]\n\t" \
    "ds_read_b128 v[40:43], v22 offset:128\n\t" \
    "s_waitcnt lgkmcnt(5)\n\t" \
    "v_mfma_f32_16x16x32_bf16 v[92:95], a[68:71], v[68:71], v[92:95]\n\t" \
    "ds_read_b128 v[44:47], v22 offset:18560\n\t" \
    "v_mfma_f32_16x16x32_bf16 v[96:99], a[140:143], v[64:67], v[96:99]\n\t" \
    "v_mfma_f32_16x16x32_bf16 v[100:103], a[140:143], v[68:71], v[100:103]\n\t" \
    "v_mov_b32 v1, v22\n\t" \
    "v_mov_b32 v2, v23\n\t" \
    "v_cvt_pk_bf16_f32 v104, v88, v89\n\t" \
    "v_cvt_pk_bf16_f32 v105, v90, v91\n\t" \
    "s_nop 2\n\t" \
    "v_cvt_pk_bf16_f32 v106, v96, v97\n\t" \
    "v_cvt_pk_bf16_f32 v107, v98, v99\n\t" \
    "global_store_dwordx4 v8, v[104:107], s[38:39] offset:0 sc1\n\t" \
    "v_cvt_pk_bf16_f32 v108, v92, v93\n\t" \
    "v_cvt_pk_bf16_f32 v109, v94, v95\n\t" \
    "v_cvt_pk_bf16_f32 v110, v100, v101\n\t" \
    "v_cvt_pk_bf16_f32 v111, v102, v103\n\t" \
    "global_store_dwordx4 v8, v[108:111], s[38:39] offset:1024 sc1\n\t" \
    "s_add_u32 s65, s65, 1\n\t" \
    "s_cmp_lt_u32 s65, 64\n\t" \
    "s_cbranch_scc1 gx_l315_%=\n\t" \
    "s_waitcnt vmcnt(0) lgkmcnt(0)\n\t" \
    "s_barrier\n\t" \
    "s_cmp_lg_u32 s66, 0\n\t" \
    "s_cbranch_scc1 gx_SF18_%=\n\t" \
    "s_lshl_b32 s69, s67, 7\n\t" \
    "s_add_u32 s69, s69, 5849088\n\t" \
    "s_add_u32 s70, s65, -1\n\t" \
    "v_mov_b32 v156, s69\n\t" \
    "v_mov_b32 v157, s70\n\t" \
    "s_mov_b64 exec, 1\n\t" \
    "global_store_dword v156, v157, s[36:37] sc1\n\t" \
    "s_mov_b64 exec, -1\n\t" \
    "gx_SF18_%=:\n\t" \
    "gx_last_%=:\n\t" \
    ""
#define ASM_GV \
    "s_load_dwordx2 s[42:43], %3, 56\n\t" \
    "s_load_dwordx2 s[44:45], %3, 64\n\t" \
    "s_load_dwordx2 s[36:37], %3, 0x58\n\t" \
    "s_load_dwordx2 s[50:51], %3, 0x48\n\t" \
    "s_mov_b32 s66, %4\n\t" \
    "s_mov_b32 s67, %5\n\t" \
    "s_mov_b32 s68, 262144\n\t" \
    "s_mov_b32 s72, 0\n\t" \
    "s_mov_b32 s83, -1\n\t" \
    "s_mov_b32 s65, 0\n\t" \
    "s_lshl_b32 s82, s66, 10\n\t" \
    "s_waitcnt lgkmcnt(0)\n\t" \
    "s_lshr_b32 s65, s67, 5\n\t" \
    "s_cselect_b32 s88, 24, 64\n\t" \
    "s_cmp_lt_u32 s65, 24\n\t" \
    "s_addc_u32 s84, s65, 1\n\t" \
    "s_cmp_lt_u32 s84, 24\n\t" \
    "s_addc_u32 s85, s84, 1\n\t" \
    "s_mov_b32 s86, -1\n\t" \
    "s_mov_b32 s87, -1\n\t" \
    "s_add_u32 s38, s36, 48234496\n\t" \
    "s_addc_u32 s39, s37, 0\n\t" \
    "s_and_b32 s69, s67, 31\n\t" \
    "s_mul_i32 s69, s69, 147456\n\t" \
    "s_add_u32 s44, s44, s69\n\t" \
    "s_addc_u32 s45, s45, 0\n\t" \
    "s_mul_i32 s89, s66, 9216\n\t" \
    "s_mov_b32 s90, 0\n\t" \
    "v_mov_b32 v196, -1\n\t" \
    "v_mov_b32 v197, -1\n\t" \
    "v_mov_b32 v198, -1\n\t" \
    "v_mov_b32 v199, -1\n\t" \
    "s_add_u32 s46, s42, 2097152\n\t" \
    "s_addc_u32 s47, s43, 0\n\t" \
    "s_add_u32 m0, s82, 65536\n\t" \
    "s_nop 0\n\t" \
    "global_load_lds_dwordx4 v200, s[46:47]\n\t" \
    "s_add_u32 s46, s42, 2228224\n\t" \
    "s_addc_u32 s47, s43, 0\n\t" \
    "s_add_u32 m0, s82, 69632\n\t" \
    "s_nop 0\n\t" \
    "global_load_lds_dwordx4 v200, s[46:47]\n\t" \
    "s_add_u32 s46, s42, 2359296\n\t" \
    "s_addc_u32 s47, s43, 0\n\t" \
    "s_add_u32 m0, s82, 73728\n\t" \
    "s_nop 0\n\t" \
    "global_load_lds_dwordx4 v200, s[46:47]\n\t" \
    "s_add_u32 s46, s42, 2490368\n\t" \
    "s_addc_u32 s47, s43, 0\n\t" \
    "s_add_u32 m0, s82, 77824\n\t" \
    "s_nop 0\n\t" \
    "global_load_lds_dwordx4 v200, s[46:47]\n\t" \
    "s_add_u32 s46, s42, 2621440\n\t" \
    "s_addc_u32 s47, s43, 0\n\t" \
    "s_add_u32 m0, s82, 81920\n\t" \
    "s_nop 0\n\t" \
    "global_load_lds_dwordx4 v200, s[46:47]\n\t" \
    "s_add_u32 s46, s42, 2752512\n\t" \
    "s_addc_u32 s47, s43, 0\n\t" \
    "s_add_u32 m0, s82, 86016\n\t" \
    "s_nop 0\n\t" \
    "global_load_lds_dwordx4 v200, s[46:47]\n\t" \
    "s_add_u32 s46, s42, 2883584\n\t" \
    "s_addc_u32 s47, s43, 0\n\t" \
    "s_add_u32 m0, s82, 90112\n\t" \
    "s_nop 0\n\t" \
    "global_load_lds_dwordx4 v200, s[46:47]\n\t" \
    "s_add_u32 s46, s42, 3014656\n\t" \
    "s_addc_u32 s47, s43, 0\n\t" \
    "s_add_u32 m0, s82, 94208\n\t" \
    "s_nop 0\n\t" \
    "global_load_lds_dwordx4 v200, s[46:47]\n\t" \
    "s_add_u32 s46, s42, 3145728\n\t" \
    "s_addc_u32 s47, s43, 0\n\t" \
    "s_add_u32 m0, s82, 98304\n\t" \
    "s_nop 0\n\t" \
    "global_load_lds_dwordx4 v200, s[46:47]\n\t" \
    "s_add_u32 s46, s42, 3276800\n\t" \
    "s_addc_u32 s47, s43, 0\n\t" \
    "s_add_u32 m0, s82, 102400\n\t" \
    "s_nop 0\n\t" \
    "global_load_lds_dwordx4 v200, s[46:47]\n\t" \
    "s_add_u32 s46, s42, 3407872\n\t" \
    "s_addc_u32 s47, s43, 0\n\t" \
    "s_add_u32 m0, s82, 106496\n\t" \
    "s_nop 0\n\t" \
    "global_load_lds_dwordx4 v200, s[46:47]\n\t" \
    "s_add_u32 s46, s42, 3538944\n\t" \
    "s_addc_u32 s47, s43, 0\n\t" \
    "s_add_u32 m0, s82, 110592\n\t" \
    "s_nop 0\n\t" \
    "global_load_lds_dwordx4 v200, s[46:47]\n\t" \
    "s_add_u32 s46, s42, 3670016\n\t" \
    "s_addc_u32 s47, s43, 0\n\t" \
    "s_add_u32 m0, s82, 114688\n\t" \
    "s_nop 0\n\t" \
    "global_load_lds_dwordx4 v200, s[46:47]\n\t" \
    "s_add_u32 s46, s42, 3801088\n\t" \
    "s_addc_u32 s47, s43, 0\n\t" \
    "s_add_u32 m0, s82, 118784\n\t" \
    "s_nop 0\n\t" \
    "global_load_lds_dwordx4 v200, s[46:47]\n\t" \
    "s_add_u32 s46, s42, 3932160\n\t" \
    "s_addc_u32 s47, s43, 0\n\t" \
    "s_add_u32 m0, s82, 122880\n\t" \
    "s_nop 0\n\t" \
    "global_load_lds_dwordx4 v200, s[46:47]\n\t" \
    "s_add_u32 s46, s42, 4063232\n\t" \
    "s_addc_u32 s47, s43, 0\n\t" \
    "s_add_u32 m0, s82, 126976\n\t" \
    "s_nop 0\n\t" \
    "global_load_lds_dwordx4 v200, s[46:47]\n\t" \
    "s_add_u32 s46, s42, 4194304\n\t" \
    "s_addc_u32 s47, s43, 0\n\t" \
    "s_add_u32 m0, s82, 131072\n\t" \
    "s_nop 0\n\t" \
    "global_load_lds_dwordx4 v200, s[46:47]\n\t" \
    "s_add_u32 s46, s42, 4325376\n\t" \
    "s_addc_u32 s47, s43, 0\n\t" \
    "s_add_u32 m0, s82, 135168\n\t" \
    "s_nop 0\n\t" \
    "global_load_lds_dwordx4 v200, s[46:47]\n\t" \
    "s_add_u32 s46, s42, 4456448\n\t" \
    "s_addc_u32 s47, s43, 0\n\t" \
    "s_add_u32 m0, s82, 139264\n\t" \
    "s_nop 0\n\t" \
    "global_load_lds_dwordx4 v200, s[46:47]\n\t" \
    "s_add_u32 s46, s42, 4587520\n\t" \
    "s_addc_u32 s47, s43, 0\n\t" \
    "s_add_u32 m0, s82, 143360\n\t" \
    "s_nop 0\n\t" \
    "global_load_lds_dwordx4 v200, s[46:47]\n\t" \
    "s_waitcnt vmcnt(32) lgkmcnt(0)\n\t" \
    "s_barrier\n\t" \
    "ds_read_b64_tr_b16 a[0:1], v201 offset:0\n\t" \
    "ds_read_b64_tr_b16 a[2:3], v201 offset:1024\n\t" \
    "ds_read_b64_tr_b16 a[4:5], v201 offset:8192\n\t" \
    "ds_read_b64_tr_b16 a[6:7], v201 offset:9216\n\t" \
    "ds_read_b64_tr_b16 a[72:73], v202 offset:0\n\t" \
    "ds_read_b64_tr_b16 a[74:75], v202 offset:1024\n\t" \
    "ds_read_b64_tr_b16 a[76:77], v202 offset:8192\n\t" \
    "ds_read_b64_tr_b16 a[78:79], v202 offset:9216\n\t" \
    "s_waitcnt vmcnt(28) lgkmcnt(0)\n\t" \
    "s_barrier\n\t" \
    "ds_read_b64_tr_b16 a[8:9], v201 offset:16384\n\t" \
    "ds_read_b64_tr_b16 a[10:11], v201 offset:17408\n\t" \
    "ds_read_b64_tr_b16 a[12:13], v201 offset:24576\n\t" \
    "ds_read_b64_tr_b16 a[14:15], v201 offset:25600\n\t" \
    "ds_read_b64_tr_b16 a[80:81], v202 offset:16384\n\t" \
    "ds_read_b64_tr_b16 a[82:83], v202 offset:17408\n\t" \
    "ds_read_b64_tr_b16 a[84:85], v202 offset:24576\n\t" \
    "ds_read_b64_tr_b16 a[86:87], v202 offset:25600\n\t" \
    "s_waitcnt vmcnt(24) lgkmcnt(0)\n\t" \
    "s_barrier\n\t" \
    "ds_read_b64_tr_b16 a[16:17], v201 offset:32768\n\t" \
    "ds_read_b64_tr_b16 a[18:19], v201 offset:33792\n\t" \
    "ds_read_b64_tr_b16 a[20:21], v201 offset:40960\n\t" \
    "ds_read_b64_tr_b16 a[22:23], v201 offset:41984\n\t" \
    "ds_read_b64_tr_b16 a[88:89], v202 offset:32768\n\t" \
    "ds_read_b64_tr_b16 a[90:91], v202 offset:33792\n\t" \
    "ds_read_b64_tr_b16 a[92:93], v202 offset:40960\n\t" \
    "ds_read_b64_tr_b16 a[94:95], v202 offset:41984\n\t" \
    "s_waitcnt vmcnt(20) lgkmcnt(0)\n\t" \
    "s_barrier\n\t" \
    "ds_read_b64_tr_b16 a[24:25], v201 offset:49152\n\t" \
    "ds_read_b64_tr_b16 a[26:27], v201 offset:50176\n\t" \
    "ds_read_b64_tr_b16 a[28:29], v201 offset:57344\n\t" \
    "ds_read_b64_tr_b16 a[30:31], v201 offset:58368\n\t" \
    "ds_read_b64_tr_b16 a[96:97], v202 offset:49152\n\t" \
    "ds_read_b64_tr_b16 a[98:99], v202 offset:50176\n\t" \
    "ds_read_b64_tr_b16 a[100:101], v202 offset:57344\n\t" \
    "ds_read_b64_tr_b16 a[102:103], v202 offset:58368\n\t" \
    "s_add_u32 s46, s44, 0\n\t" \
    "s_addc_u32 s47, s45, 0\n\t" \
    "s_mul_i32 s69, s82, 9\n\t" \
    "s_add_u32 m0, s69, 0\n\t" \
    "s_nop 0\n\t" \
    "global_load_lds_dwordx4 v203, s[46:47]\n\t" \
    "s_add_u32 m0, s69, 1024\n\t" \
    "s_nop 0\n\t" \
    "global_load_lds_dwordx4 v204, s[46:47]\n\t" \
    "s_add_u32 m0, s69, 2048\n\t" \
    "s_nop 0\n\t" \
    "global_load_lds_dwordx4 v205, s[46:47]\n\t" \
    "s_add_u32 m0, s69, 3072\n\t" \
    "s_nop 0\n\t" \
    "global_load_lds_dwordx4 v206, s[46:47]\n\t" \
    "s_add_u32 m0, s69, 4096\n\t" \
    "s_nop 0\n\t" \
    "global_load_lds_dwordx4 v207, s[46:47]\n\t" \
    "s_add_u32 m0, s69, 5120\n\t" \
    "s_nop 0\n\t" \
    "global_load_lds_dwordx4 v208, s[46:47]\n\t" \
    "s_add_u32 m0, s69, 6144\n\t" \
    "s_nop 0\n\t" \
    "global_load_lds_dwordx4 v209, s[46:47]\n\t" \
    "s_add_u32 m0, s69, 7168\n\t" \
    "s_nop 0\n\t" \
    "global_load_lds_dwordx4 v210, s[46:47]\n\t" \
    "s_add_u32 m0, s69, 8192\n\t" \
    "s_nop 0\n\t" \
    "global_load_lds_dwordx4 v211, s[46:47]\n\t" \
    "s_min_u32 s69, s65, 63\n\t" \
    "s_mul_i32 s69, s69, 36864\n\t" \
    "s_add_u32 s52, s50, s69\n\t" \
    "s_addc_u32 s53, s51, 0\n\t" \
    "global_load_dwordx4 v[120:123], v203, s[52:53]\n\t" \
    "global_load_dwordx4 v[124:127], v204, s[52:53]\n\t" \
    "global_load_dwordx4 v[128:131], v205, s[52:53]\n\t" \
    "global_load_dwordx4 v[132:135], v206, s[52:53]\n\t" \
    "global_load_dwordx4 v[136:139], v207, s[52:53]\n\t" \
    "global_load_dwordx4 v[140:143], v208, s[52:53]\n\t" \
    "global_load_dwordx4 v[144:147], v209, s[52:53]\n\t" \
    "global_load_dwordx4 v[148:151], v210, s[52:53]\n\t" \
    "global_load_dwordx4 v[152:155], v211, s[52:53]\n\t" \
    "s_waitcnt vmcnt(34) lgkmcnt(0)\n\t" \
    "s_barrier\n\t" \
    "v_add_u32 v201, 0x10000, v201\n\t" \
    "v_add_u32 v202, 0x10000, v202\n\t" \
    "ds_read_b64_tr_b16 a[32:33], v201 offset:0\n\t" \
    "ds_read_b64_tr_b16 a[34:35], v201 offset:1024\n\t" \
    "ds_read_b64_tr_b16 a[36:37], v201 offset:8192\n\t" \
    "ds_read_b64_tr_b16 a[38:39], v201 offset:9216\n\t" \
    "ds_read_b64_tr_b16 a[104:105], v202 offset:0\n\t" \
    "ds_read_b64_tr_b16 a[106:107], v202 offset:1024\n\t" \
    "ds_read_b64_tr_b16 a[108:109], v202 offset:8192\n\t" \
    "ds_read_b64_tr_b16 a[110:111], v202 offset:9216\n\t" \
    "s_waitcnt vmcnt(30) lgkmcnt(0)\n\t" \
    "s_barrier\n\t" \
    "ds_read_b64_tr_b16 a[40:41], v201 offset:16384\n\t" \
    "ds_read_b64_tr_b16 a[42:43], v201 offset:17408\n\t" \
    "ds_read_b64_tr_b16 a[44:45], v201 offset:24576\n\t" \
    "ds_read_b64_tr_b16 a[46:47], v201 offset:25600\n\t" \
    "ds_read_b64_tr_b16 a[112:113], v202 offset:16384\n\t" \
    "ds_read_b64_tr_b16 a[114:115], v202 offset:17408\n\t" \
    "ds_read_b64_tr_b16 a[116:117], v202 offset:24576\n\t" \
    "ds_read_b64_tr_b16 a[118:119], v202 offset:25600\n\t" \
    "s_add_u32 s46, s44, 36864\n\t" \
    "s_addc_u32 s47, s45, 0\n\t" \
    "s_mul_i32 s69, s82, 9\n\t" \
    "s_add_u32 m0, s69, 36864\n\t" \
    "s_nop 0\n\t" \
    "global_load_lds_dwordx4 v203, s[46:47]\n\t" \
    "s_add_u32 m0, s69, 37888\n\t" \
    "s_nop 0\n\t" \
    "global_load_lds_dwordx4 v204, s[46:47]\n\t" \
    "s_add_u32 m0, s69, 38912\n\t" \
    "s_nop 0\n\t" \
    "global_load_lds_dwordx4 v205, s[46:47]\n\t" \
    "s_add_u32 m0, s69, 39936\n\t" \
    "s_nop 0\n\t" \
    "global_load_lds_dwordx4 v206, s[46:47]\n\t" \
    "s_add_u32 m0, s69, 40960\n\t" \
    "s_nop 0\n\t" \
    "global_load_lds_dwordx4 v207, s[46:47]\n\t" \
    "s_add_u32 m0, s69, 41984\n\t" \
    "s_nop 0\n\t" \
    "global_load_lds_dwordx4 v208, s[46:47]\n\t" \
    "s_add_u32 m0, s69, 43008\n\t" \
    "s_nop 0\n\t" \
    "global_load_lds_dwordx4 v209, s[46:47]\n\t" \
    "s_add_u32 m0, s69, 44032\n\t" \
    "s_nop 0\n\t" \
    "global_load_lds_dwordx4 v210, s[46:47]\n\t" \
    "s_add_u32 m0, s69, 45056\n\t" \
    "s_nop 0\n\t" \
    "global_load_lds_dwordx4 v211, s[46:47]\n\t" \
    "s_waitcnt vmcnt(35) lgkmcnt(0)\n\t" \
    "s_barrier\n\t" \
    "ds_read_b64_tr_b16 a[48:49], v201 offset:32768\n\t" \
    "ds_read_b64_tr_b16 a[50:51], v201 offset:33792\n\t" \
    "ds_read_b64_tr_b16 a[52:53], v201 offset:40960\n\t" \
    "ds_read_b64_tr_b16 a[54:55], v201 offset:41984\n\t" \
    "ds_read_b64_tr_b16 a[120:121], v202 offset:32768\n\t" \
    "ds_read_b64_tr_b16 a[122:123], v202 offset:33792\n\t" \
    "ds_read_b64_tr_b16 a[124:125], v202 offset:40960\n\t" \
    "ds_read_b64_tr_b16 a[126:127], v202 offset:41984\n\t" \
    "s_waitcnt vmcnt(31) lgkmcnt(0)\n\t" \
    "s_barrier\n\t" \
    "ds_read_b64_tr_b16 a[56:57], v201 offset:49152\n\t" \
    "ds_read_b64_tr_b16 a[58:59], v201 offset:50176\n\t" \
    "ds_read_b64_tr_b16 a[60:61], v201 offset:57344\n\t" \
    "ds_read_b64_tr_b16 a[62:63], v201 offset:58368\n\t" \
    "ds_read_b64_tr_b16 a[128:129], v202 offset:49152\n\t" \
    "ds_read_b64_tr_b16 a[130:131], v202 offset:50176\n\t" \
    "ds_read_b64_tr_b16 a[132:133], v202 offset:57344\n\t" \
    "ds_read_b64_tr_b16 a[134:135], v202 offset:58368\n\t" \
    "s_add_u32 s46, s44, 73728\n\t" \
    "s_addc_u32 s47, s45, 0\n\t" \
    "s_mul_i32 s69, s82, 9\n\t" \
    "s_add_u32 m0, s69, 73728\n\t" \
    "s_nop 0\n\t" \
    "global_load_lds_dwordx4 v203, s[46:47]\n\t" \
    "s_add_u32 m0, s69, 74752\n\t" \
    "s_nop 0\n\t" \
    "global_load_lds_dwordx4 v204, s[46:47]\n\t" \
    "s_add_u32 m0, s69, 75776\n\t" \
    "s_nop 0\n\t" \
    "global_load_lds_dwordx4 v205, s[46:47]\n\t" \
    "s_add_u32 m0, s69, 76800\n\t" \
    "s_nop 0\n\t" \
    "global_load_lds_dwordx4 v206, s[46:47]\n\t" \
    "s_add_u32 m0, s69, 77824\n\t" \
    "s_nop 0\n\t" \
    "global_load_lds_dwordx4 v207, s[46:47]\n\t" \
    "s_add_u32 m0, s69, 78848\n\t" \
    "s_nop 0\n\t" \
    "global_load_lds_dwordx4 v208, s[46:47]\n\t" \
    "s_add_u32 m0, s69, 79872\n\t" \
    "s_nop 0\n\t" \
    "global_load_lds_dwordx4 v209, s[46:47]\n\t" \
    "s_add_u32 m0, s69, 80896\n\t" \
    "s_nop 0\n\t" \
    "global_load_lds_dwordx4 v210, s[46:47]\n\t" \
    "s_add_u32 m0, s69, 81920\n\t" \
    "s_nop 0\n\t" \
    "global_load_lds_dwordx4 v211, s[46:47]\n\t" \
    "v_mbcnt_lo_u32_b32 v164, -1, 0\n\t" \
    "v_mbcnt_hi_u32_b32 v164, -1, v164\n\t" \
    "v_lshlrev_b32 v164, 6, v164\n\t" \
    "v_add_u32 v164, 6225920, v164\n\t" \
    "v_mov_b32 v165, 6234112\n\t" \
    "global_load_dword v165, v165, s[36:37] sc1\n\t" \
    "global_load_dwordx2 v[166:167], v164, s[36:37] sc1\n\t" \
    "global_load_dword v168, v164, s[36:37] offset:2048 sc1\n\t" \
    "s_waitcnt vmcnt(39) lgkmcnt(0)\n\t" \
    "s_barrier\n\t" \
    "v_add_u32 v201, 0x10000, v201\n\t" \
    "v_add_u32 v202, 0x10000, v202\n\t" \
    "ds_read_b64_tr_b16 a[64:65], v201 offset:0\n\t" \
    "ds_read_b64_tr_b16 a[66:67], v201 offset:1024\n\t" \
    "ds_read_b64_tr_b16 a[68:69], v201 offset:8192\n\t" \
    "ds_read_b64_tr_b16 a[70:71], v201 offset:9216\n\t" \
    "ds_read_b64_tr_b16 a[136:137], v202 offset:0\n\t" \
    "ds_read_b64_tr_b16 a[138:139], v202 offset:1024\n\t" \
    "ds_read_b64_tr_b16 a[140:141], v202 offset:8192\n\t" \
    "ds_read_b64_tr_b16 a[142:143], v202 offset:9216\n\t" \
    "v_add_u32 v201, 0xfffe0000, v201\n\t" \
    "v_add_u32 v202, 0xfffe0000, v202\n\t" \
    "gv_jn2_%=:\n\t" \
    "s_waitcnt lgkmcnt(0)\n\t" \
    "s_barrier\n\t" \
    "s_add_u32 s46, s44, 110592\n\t" \
    "s_addc_u32 s47, s45, 0\n\t" \
    "s_mul_i32 s69, s82, 9\n\t" \
    "s_add_u32 m0, s69, 110592\n\t" \
    "s_nop 0\n\t" \
    "global_load_lds_dwordx4 v203, s[46:47]\n\t" \
    "s_add_u32 m0, s69, 111616\n\t" \
    "s_nop 0\n\t" \
    "global_load_lds_dwordx4 v204, s[46:47]\n\t" \
    "s_add_u32 m0, s69, 112640\n\t" \
    "s_nop 0\n\t" \
    "global_load_lds_dwordx4 v205, s[46:47]\n\t" \
    "s_add_u32 m0, s69, 113664\n\t" \
    "s_nop 0\n\t" \
    "global_load_lds_dwordx4 v206, s[46:47]\n\t" \
    "s_add_u32 m0, s69, 114688\n\t" \
    "s_nop 0\n\t" \
    "global_load_lds_dwordx4 v207, s[46:47]\n\t" \
    "s_add_u32 m0, s69, 115712\n\t" \
    "s_nop 0\n\t" \
    "global_load_lds_dwordx4 v208, s[46:47]\n\t" \
    "s_add_u32 m0, s69, 116736\n\t" \
    "s_nop 0\n\t" \
    "global_load_lds_dwordx4 v209, s[46:47]\n\t" \
    "s_add_u32 m0, s69, 117760\n\t" \
    "s_nop 0\n\t" \
    "global_load_lds_dwordx4 v210, s[46:47]\n\t" \
    "s_add_u32 m0, s69, 118784\n\t" \
    "s_nop 0\n\t" \
    "global_load_lds_dwordx4 v211, s[46:47]\n\t" \
    "s_waitcnt vmcnt(39) lgkmcnt(0)\n\t" \
    "s_barrier\n\t" \
    "ds_read_b64_tr_b16 a[144:145], v212 offset:0\n\t" \
    "ds_read_b64_tr_b16 a[146:147], v212 offset:18432\n\t" \
    "ds_read_b64_tr_b16 a[160:161], v213 offset:0\n\t" \
    "ds_read_b64_tr_b16 a[162:163], v213 offset:18432\n\t" \
    "ds_read_b64_tr_b16 a[176:177], v214 offset:0\n\t" \
    "ds_read_b64_tr_b16 a[178:179], v214 offset:18432\n\t" \
    "s_waitcnt lgkmcnt(6)\n\t" \
    "ds_read_b64_tr_b16 a[192:193], v215 offset:0\n\t" \
    "ds_read_b64_tr_b16 a[194:195], v215 offset:18432\n\t" \
    "ds_read_b64_tr_b16 a[208:209], v212 offset:128\n\t" \
    "ds_read_b64_tr_b16 a[210:211], v212 offset:18560\n\t" \
    "ds_read_b64_tr_b16 a[224:225], v213 offset:128\n\t" \
    "ds_read_b64_tr_b16 a[226:227], v213 offset:18560\n\t" \
    "s_waitcnt lgkmcnt(6)\n\t" \
    "ds_read_b64_tr_b16 a[240:241], v214 offset:128\n\t" \
    "ds_read_b64_tr_b16 a[242:243], v214 offset:18560\n\t" \
    "ds_read_b64_tr_b16 v[224:225], v215 offset:128\n\t" \
    "ds_read_b64_tr_b16 v[226:227], v215 offset:18560\n\t" \
    "ds_read_b64_tr_b16 v[240:241], v212 offset:256\n\t" \
    "ds_read_b64_tr_b16 v[242:243], v212 offset:18688\n\t" \
    "s_waitcnt lgkmcnt(6)\n\t" \
    "v_add_u32 v212, 0x9000, v212\n\t" \
    "v_add_u32 v213, 0x9000, v213\n\t" \
    "v_add_u32 v214, 0x9000, v214\n\t" \
    "v_add_u32 v215, 0x9000, v215\n\t" \
    "s_waitcnt vmcnt(21) lgkmcnt(0)\n\t" \
    "s_barrier\n\t" \
    "ds_read_b64_tr_b16 a[148:149], v212 offset:0\n\t" \
    "ds_read_b64_tr_b16 a[150:151], v212 offset:18432\n\t" \
    "ds_read_b64_tr_b16 a[164:165], v213 offset:0\n\t" \
    "ds_read_b64_tr_b16 a[166:167], v213 offset:18432\n\t" \
    "ds_read_b64_tr_b16 a[180:181], v214 offset:0\n\t" \
    "ds_read_b64_tr_b16 a[182:183], v214 offset:18432\n\t" \
    "s_waitcnt lgkmcnt(6)\n\t" \
    "ds_read_b64_tr_b16 a[196:197], v215 offset:0\n\t" \
    "ds_read_b64_tr_b16 a[198:199], v215 offset:18432\n\t" \
    "ds_read_b64_tr_b16 a[212:213], v212 offset:128\n\t" \
    "ds_read_b64_tr_b16 a[214:215], v212 offset:18560\n\t" \
    "ds_read_b64_tr_b16 a[228:229], v213 offset:128\n\t" \
    "ds_read_b64_tr_b16 a[230:231], v213 offset:18560\n\t" \
    "s_waitcnt lgkmcnt(6)\n\t" \
    "ds_read_b64_tr_b16 a[244:245], v214 offset:128\n\t" \
    "ds_read_b64_tr_b16 a[246:247], v214 offset:18560\n\t" \
    "ds_read_b64_tr_b16 v[228:229], v215 offset:128\n\t" \
    "ds_read_b64_tr_b16 v[230:231], v215 offset:18560\n\t" \
    "ds_read_b64_tr_b16 v[244:245], v212 offset:256\n\t" \
    "ds_read_b64_tr_b16 v[246:247], v212 offset:18688\n\t" \
    "s_waitcnt lgkmcnt(6)\n\t" \
    "v_add_u32 v212, 0x9000, v212\n\t" \
    "v_add_u32 v213, 0x9000, v213\n\t" \
    "v_add_u32 v214, 0x9000, v214\n\t" \
    "v_add_u32 v215, 0x9000, v215\n\t" \
    "ds_write_b128 v3, v[120:123] offset:0\n\t" \
    "ds_write_b128 v3, v[124:127] offset:1024\n\t" \
    "ds_write_b128 v3, v[128:131] offset:2048\n\t" \
    "ds_write_b128 v3, v[132:135] offset:3072\n\t" \
    "ds_write_b128 v3, v[136:139] offset:4096\n\t" \
    "ds_write_b128 v3, v[140:143] offset:5120\n\t" \
    "ds_write_b128 v3, v[144:147] offset:6144\n\t" \
    "ds_write_b128 v3, v[148:151] offset:7168\n\t" \
    "ds_write_b128 v3, v[152:155] offset:8192\n\t" \
    "s_waitcnt vmcnt(12) lgkmcnt(0)\n\t" \
    "s_barrier\n\t" \
    "ds_read_b64_tr_b16 a[152:153], v212 offset:0\n\t" \
    "ds_read_b64_tr_b16 a[154:155], v212 offset:18432\n\t" \
    "ds_read_b64_tr_b16 a[168:169], v213 offset:0\n\t" \
    "ds_read_b64_tr_b16 a[170:171], v213 offset:18432\n\t" \
    "ds_read_b64_tr_b16 a[184:185], v214 offset:0\n\t" \
    "ds_read_b64_tr_b16 a[186:187], v214 offset:18432\n\t" \
    "s_waitcnt lgkmcnt(6)\n\t" \
    "ds_read_b64_tr_b16 a[200:201], v215 offset:0\n\t" \
    "ds_read_b64_tr_b16 a[202:203], v215 offset:18432\n\t" \
    "ds_read_b64_tr_b16 a[216:217], v212 offset:128\n\t" \
    "ds_read_b64_tr_b16 a[218:219], v212 offset:18560\n\t" \
    "ds_read_b64_tr_b16 a[232:233], v213 offset:128\n\t" \
    "ds_read_b64_tr_b16 a[234:235], v213 offset:18560\n\t" \
    "s_waitcnt lgkmcnt(6)\n\t" \
    "ds_read_b64_tr_b16 a[248:249], v214 offset:128\n\t" \
    "ds_read_b64_tr_b16 a[250:251], v214 offset:18560\n\t" \
    "ds_read_b64_tr_b16 v[232:233], v215 offset:128\n\t" \
    "ds_read_b64_tr_b16 v[234:235], v215 offset:18560\n\t" \
    "ds_read_b64_tr_b16 v[248:249], v212 offset:256\n\t" \
    "ds_read_b64_tr_b16 v[250:251], v212 offset:18688\n\t" \
    "s_waitcnt lgkmcnt(6)\n\t" \
    "v_add_u32 v212, 0x9000, v212\n\t" \
    "v_add_u32 v213, 0x9000, v213\n\t" \
    "v_add_u32 v214, 0x9000, v214\n\t" \
    "v_add_u32 v215, 0x9000, v215\n\t" \
    "s_min_u32 s69, s84, 63\n\t" \
    "s_mul_i32 s69, s69, 36864\n\t" \
    "s_add_u32 s52, s50, s69\n\t" \
    "s_addc_u32 s53, s51, 0\n\t" \
    "s_add_u32 m0, s89, 36864\n\t" \
    "s_nop 0\n\t" \
    "global_load_lds_dwordx4 v203, s[52:53]\n\t" \
    "s_add_u32 m0, s89, 37888\n\t" \
    "s_nop 0\n\t" \
    "global_load_lds_dwordx4 v204, s[52:53]\n\t" \
    "s_add_u32 m0, s89, 38912\n\t" \
    "s_nop 0\n\t" \
    "global_load_lds_dwordx4 v205, s[52:53]\n\t" \
    "s_add_u32 m0, s89, 39936\n\t" \
    "s_nop 0\n\t" \
    "global_load_lds_dwordx4 v206, s[52:53]\n\t" \
    "s_add_u32 m0, s89, 40960\n\t" \
    "s_nop 0\n\t" \
    "global_load_lds_dwordx4 v207, s[52:53]\n\t" \
    "s_add_u32 m0, s89, 41984\n\t" \
    "s_nop 0\n\t" \
    "global_load_lds_dwordx4 v208, s[52:53]\n\t" \
    "s_add_u32 m0, s89, 43008\n\t" \
    "s_nop 0\n\t" \
    "global_load_lds_dwordx4 v209, s[52:53]\n\t" \
    "s_add_u32 m0, s89, 44032\n\t" \
    "s_nop 0\n\t" \
    "global_load_lds_dwordx4 v210, s[52:53]\n\t" \
    "s_add_u32 m0, s89, 45056\n\t" \
    "s_nop 0\n\t" \
    "global_load_lds_dwordx4 v211, s[52:53]\n\t" \
    "s_waitcnt vmcnt(18)\n\t" \
    "gv_C3_%=:\n\t" \
    "v_cmp_ne_u32 s[54:55], v166, v168\n\t" \
    "v_sub_u32 v166, v166, v165\n\t" \
    "v_cmp_ne_u32 vcc, 1, v166\n\t" \
    "s_nop 1\n\t" \
    "s_or_b64 vcc, vcc, s[54:55]\n\t" \
    "s_cbranch_scc0 gv_Cok4_%=\n\t" \
    "s_cmp_eq_u32 s68, 0\n\t" \
    "s_cbranch_scc1 gv_F6_%=\n\t" \
    "s_sub_u32 s68, s68, 1\n\t" \
    "s_sleep 6\n\t" \
    "s_branch gv_Cag5_%=\n\t" \
    "gv_F6_%=:\n\t" \
    "v_mov_b32 v168, 5767424\n\t" \
    "v_mov_b32 v169, 4\n\t" \
    "global_store_dword v168, v169, s[36:37] sc1\n\t" \
    "s_waitcnt vmcnt(0)\n\t" \
    "s_branch gv_Cok4_%=\n\t" \
    "gv_Cag5_%=:\n\t" \
    "global_load_dwordx2 v[166:167], v164, s[36:37] sc1\n\t" \
    "global_load_dword v168, v164, s[36:37] offset:2048 sc1\n\t" \
    "s_waitcnt vmcnt(0)\n\t" \
    "s_branch gv_C3_%=\n\t" \
    "gv_Cok4_%=:\n\t" \
    "s_cmp_lg_u32 s66, 0\n\t" \
    "s_cbranch_scc1 gv_Ca7_%=\n\t" \
    "v_mov_b32 v164, 5836800\n\t" \
    "s_mov_b64 exec, 1\n\t" \
    "global_atomic_add v164, v166, s[36:37] sc1\n\t" \
    "s_mov_b64 exec, -1\n\t" \
    "gv_Ca7_%=:\n\t" \
    "v_mov_b32_dpp v164, v167 quad_perm:[1,0,3,2] row_mask:0xf bank_mask:0xf\n\t" \
    "v_cmp_lt_u32 s[44:45], v164, v167\n\t" \
    "s_nop 1\n\t" \
    "s_waitcnt lgkmcnt(0)\n\t" \
    "s_barrier\n\t" \
    "ds_read_b128 v[24:27], v1 offset:0\n\t" \
    "ds_read_b128 v[28:31], v1 offset:18432\n\t" \
    "ds_read_b128 v[32:35], v2 offset:0\n\t" \
    "ds_read_b128 v[36:39], v2 offset:18432\n\t" \
    "ds_read_b128 v[40:43], v1 offset:128\n\t" \
    "ds_read_b128 v[44:47], v1 offset:18560\n\t" \
    "s_mov_b64 vcc, 0\n\t" \
    ".p2align 6\n\t" \
    "s_nop 0\n\t" \
    "s_nop 0\n\t" \
    "gv_loop8_%=:\n\t" \
    "s_min_u32 s69, s85, 63\n\t" \
    "s_mul_i32 s69, s69, 36864\n\t" \
    "s_add_u32 s52, s50, s69\n\t" \
    "s_addc_u32 s53, s51, 0\n\t" \
    "s_add_u32 s90, s90, 36864\n\t" \
    "s_cmp_eq_u32 s90, 110592\n\t" \
    "s_cselect_b32 s90, 0, s90\n\t" \
    "s_add_u32 s91, s90, 36864\n\t" \
    "s_cmp_eq_u32 s91, 110592\n\t" \
    "s_cselect_b32 s91, 0, s91\n\t" \
    "s_add_u32 s91, s91, s89\n\t" \
    "v_add_u32 v22, s90, v0\n\t" \
    "v_xor_b32 v23, 64, v22\n\t" \
    "s_lshl_b32 s40, s65, 15\n\t" \
    "s_cselect_b32 s70, s65, 64\n\t" \
    "s_mul_i32 s69, s70, 171\n\t" \
    "s_lshr_b32 s69, s69, 9\n\t" \
    "s_mul_i32 s70, s70, 589824\n\t" \
    "s_mul_i32 s71, s69, 327680\n\t" \
    "s_add_u32 s70, s70, s71\n\t" \
    "s_lshl1_add_u32 s69, s69, 4\n\t" \
    "s_bitcmp1_b64 s[44:45], s69\n\t" \
    "s_cselect_b32 s69, 8192, 0\n\t" \
    "s_add_u32 s69, s69, s70\n\t" \
    "s_add_u32 s69, s69, 123731968\n\t" \
    "s_add_u32 s48, s36, s69\n\t" \
    "s_addc_u32 s49, s37, 0\n\t" \
    "s_waitcnt lgkmcnt(5)\n\t" \
    "v_mfma_f32_16x16x32_bf16 v[72:75], a[0:3], v[24:27], 0\n\t" \
    "ds_read_b128 v[48:51], v2 offset:128\n\t" \
    "s_waitcnt lgkmcnt(5)\n\t" \
    "v_mfma_f32_16x16x32_bf16 v[76:79], a[0:3], v[28:31], 0\n\t" \
    "ds_read_b128 v[52:55], v2 offset:18560\n\t" \
    "v_mfma_f32_16x16x32_bf16 v[80:83], a[72:75], v[24:27], 0\n\t" \
    "s_add_u32 m0, s91, 0\n\t" \
    "v_mfma_f32_16x16x32_bf16 v[84:87], a[72:75], v[28:31], 0\n\t" \
    "global_load_lds_dwordx4 v203, s[52:53]\n\t" \
    "s_waitcnt lgkmcnt(5)\n\t" \
    "v_mfma_f32_16x16x32_bf16 v[72:75], a[4:7], v[32:35], v[72:75]\n\t" \
    "ds_read_b128 v[56:59], v1 offset:256\n\t" \
    "s_waitcnt lgkmcnt(5)\n\t" \
    "v_mfma_f32_16x16x32_bf16 v[76:79], a[4:7], v[36:39], v[76:79]\n\t" \
    "ds_read_b128 v[60:63], v1 offset:18688\n\t" \
    "v_mfma_f32_16x16x32_bf16 v[80:83], a[76:79], v[32:35], v[80:83]\n\t" \
    "s_add_u32 m0, s91, 1024\n\t" \
    "v_mfma_f32_16x16x32_bf16 v[84:87], a[76:79], v[36:39], v[84:87]\n\t" \
    "global_load_lds_dwordx4 v204, s[52:53]\n\t" \
    "s_waitcnt lgkmcnt(5)\n\t" \
    "v_mfma_f32_16x16x32_bf16 v[72:75], a[8:11], v[40:43], v[72:75]\n\t" \
    "ds_read_b128 v[64:67], v2 offset:256\n\t" \
    "s_waitcnt lgkmcnt(5)\n\t" \
    "v_mfma_f32_16x16x32_bf16 v[76:79], a[8:11], v[44:47], v[76:79]\n\t" \
    "ds_read_b128 v[68:71], v2 offset:18688\n\t" \
    "v_mfma_f32_16x16x32_bf16 v[80:83], a[80:83], v[40:43], v[80:83]\n\t" \
    "s_add_u32 m0, s91, 2048\n\t" \
    "v_mfma_f32_16x16x32_bf16 v[84:87], a[80:83], v[44:47], v[84:87]\n\t" \
    "global_load_lds_dwordx4 v205, s[52:53]\n\t" \
    "s_waitcnt lgkmcnt(5)\n\t" \
    "v_mfma_f32_16x16x32_bf16 v[72:75], a[12:15], v[48:51], v[72:75]\n\t" \
    "ds_read_b128 v[24:27], v1 offset:384\n\t" \
    "s_waitcnt lgkmcnt(5)\n\t" \
    "v_mfma_f32_16x16x32_bf16 v[76:79], a[12:15], v[52:55], v[76:79]\n\t" \
    "ds_read_b128 v[28:31], v1 offset:18816\n\t" \
    "v_mfma_f32_16x16x32_bf16 v[80:83], a[84:87], v[48:51], v[80:83]\n\t" \
    "s_add_u32 m0, s91, 3072\n\t" \
    "v_mfma_f32_16x16x32_bf16 v[84:87], a[84:87], v[52:55], v[84:87]\n\t" \
    "global_load_lds_dwordx4 v206, s[52:53]\n\t" \
    "s_waitcnt lgkmcnt(5)\n\t" \
    "v_mfma_f32_16x16x32_bf16 v[72:75], a[16:19], v[56:59], v[72:75]\n\t" \
    "ds_read_b128 v[32:35], v2 offset:384\n\t" \
    "s_waitcnt lgkmcnt(5)\n\t" \
    "v_mfma_f32_16x16x32_bf16 v[76:79], a[16:19], v[60:63], v[76:79]\n\t" \
    "ds_read_b128 v[36:39], v2 offset:18816\n\t" \
    "v_mfma_f32_16x16x32_bf16 v[80:83], a[88:91], v[56:59], v[80:83]\n\t" \
    "s_add_u32 m0, s91, 4096\n\t" \
    "v_mfma_f32_16x16x32_bf16 v[84:87], a[88:91], v[60:63], v[84:87]\n\t" \
    "global_load_lds_dwordx4 v207, s[52:53]\n\t" \
    "s_waitcnt lgkmcnt(5)\n\t" \
    "v_mfma_f32_16x16x32_bf16 v[72:75], a[20:23], v[64:67], v[72:75]\n\t" \
    "ds_read_b128 v[40:43], v1 offset:512\n\t" \
    "s_waitcnt lgkmcnt(5)\n\t" \
    "v_mfma_f32_16x16x32_bf16 v[76:79], a[20:23], v[68:71], v[76:79]\n\t" \
    "ds_read_b128 v[44:47], v1 offset:18944\n\t" \
    "v_mfma_f32_16x16x32_bf16 v[80:83], a[92:95], v[64:67], v[80:83]\n\t" \
    "s_add_u32 m0, s91, 5120\n\t" \
    "v_mfma_f32_16x16x32_bf16 v[84:87], a[92:95], v[68:71], v[84:87]\n\t" \
    "global_load_lds_dwordx4 v208, s[52:53]\n\t" \
    "s_waitcnt lgkmcnt(5)\n\t" \
    "v_mfma_f32_16x16x32_bf16 v[72:75], a[24:27], v[24:27], v[72:75]\n\t" \
    "ds_read_b128 v[48:51], v2 offset:512\n\t" \
    "s_waitcnt lgkmcnt(5)\n\t" \
    "v_mfma_f32_16x16x32_bf16 v[76:79], a[24:27], v[28:31], v[76:79]\n\t" \
    "ds_read_b128 v[52:55], v2 offset:18944\n\t" \
    "v_mfma_f32_16x16x32_bf16 v[80:83], a[96:99], v[24:27], v[80:83]\n\t" \
    "s_add_u32 m0, s91, 6144\n\t" \
    "v_mfma_f32_16x16x32_bf16 v[84:87], a[96:99], v[28:31], v[84:87]\n\t" \
    "global_load_lds_dwordx4 v209, s[52:53]\n\t" \
    "s_waitcnt lgkmcnt(5)\n\t" \
    "v_mfma_f32_16x16x32_bf16 v[72:75], a[28:31], v[32:35], v[72:75]\n\t" \
    "ds_read_b128 v[56:59], v1 offset:640\n\t" \
    "s_waitcnt lgkmcnt(5)\n\t" \
    "v_mfma_f32_16x16x32_bf16 v[76:79], a[28:31], v[36:39], v[76:79]\n\t" \
    "ds_read_b128 v[60:63], v1 offset:19072\n\t" \
    "v_mfma_f32_16x16x32_bf16 v[80:83], a[100:103], v[32:35], v[80:83]\n\t" \
    "s_add_u32 m0, s91, 7168\n\t" \
    "v_mfma_f32_16x16x32_bf16 v[84:87], a[100:103], v[36:39], v[84:87]\n\t" \
    "global_load_lds_dwordx4 v210, s[52:53]\n\t" \
    "s_waitcnt lgkmcnt(5)\n\t" \
    "v_mfma_f32_16x16x32_bf16 v[72:75], a[32:35], v[40:43], v[72:75]\n\t" \
    "ds_read_b128 v[64:67], v2 offset:640\n\t" \
    "s_waitcnt lgkmcnt(5)\n\t" \
    "v_mfma_f32_16x16x32_bf16 v[76:79], a[32:35], v[44:47], v[76:79]\n\t" \
    "ds_read_b128 v[68:71], v2 offset:19072\n\t" \
    "v_mfma_f32_16x16x32_bf16 v[80:83], a[104:107], v[40:43], v[80:83]\n\t" \
    "s_add_u32 m0, s91, 8192\n\t" \
    "v_mfma_f32_16x16x32_bf16 v[84:87], a[104:107], v[44:47], v[84:87]\n\t" \
    "global_load_lds_dwordx4 v211, s[52:53]\n\t" \
    "s_waitcnt lgkmcnt(5)\n\t" \
    "v_mfma_f32_16x16x32_bf16 v[72:75], a[36:39], v[48:51], v[72:75]\n\t" \
    "ds_read_b128 v[24:27], v1 offset:768\n\t" \
    "s_waitcnt lgkmcnt(5)\n\t" \
    "v_mfma_f32_16x16x32_bf16 v[76:79], a[36:39], v[52:55], v[76:79]\n\t" \
    "ds_read_b128 v[28:31], v1 offset:19200\n\t" \
    "v_mfma_f32_16x16x32_bf16 v[80:83], a[108:111], v[48:51], v[80:83]\n\t" \
    "s_cbranch_vccnz gv_gol12_%=\n\t" \
    "gv_gob13_%=:\n\t" \
    "v_mfma_f32_16x16x32_bf16 v[84:87], a[108:111], v[52:55], v[84:87]\n\t" \
    "s_waitcnt lgkmcnt(5)\n\t" \
    "v_mfma_f32_16x16x32_bf16 v[72:75], a[40:43], v[56:59], v[72:75]\n\t" \
    "ds_read_b128 v[32:35], v2 offset:768\n\t" \
    "s_waitcnt lgkmcnt(5)\n\t" \
    "v_mfma_f32_16x16x32_bf16 v[76:79], a[40:43], v[60:63], v[76:79]\n\t" \
    "ds_read_b128 v[36:39], v2 offset:19200\n\t" \
    "v_mfma_f32_16x16x32_bf16 v[80:83], a[112:115], v[56:59], v[80:83]\n\t" \
    "v_mfma_f32_16x16x32_bf16 v[84:87], a[112:115], v[60:63], v[84:87]\n\t" \
    "global_store_dwordx4 v19, v[196:199], s[48:49] sc1\n\t" \
    "s_waitcnt lgkmcnt(5)\n\t" \
    "v_mfma_f32_16x16x32_bf16 v[72:75], a[44:47], v[64:67], v[72:75]\n\t" \
    "ds_read_b128 v[40:43], v1 offset:896\n\t" \
    "s_waitcnt lgkmcnt(5)\n\t" \
    "v_mfma_f32_16x16x32_bf16 v[76:79], a[44:47], v[68:71], v[76:79]\n\t" \
    "ds_read_b128 v[44:47], v1 offset:19328\n\t" \
    "v_mfma_f32_16x16x32_bf16 v[80:83], a[116:119], v[64:67], v[80:83]\n\t" \
    "v_mfma_f32_16x16x32_bf16 v[84:87], a[116:119], v[68:71], v[84:87]\n\t" \
    "s_waitcnt lgkmcnt(5)\n\t" \
    "v_mfma_f32_16x16x32_bf16 v[72:75], a[48:51], v[24:27], v[72:75]\n\t" \
    "ds_read_b128 v[48:51], v2 offset:896\n\t" \
    "s_waitcnt lgkmcnt(5)\n\t" \
    "v_mfma_f32_16x16x32_bf16 v[76:79], a[48:51], v[28:31], v[76:79]\n\t" \
    "ds_read_b128 v[52:55], v2 offset:19328\n\t" \
    "v_mfma_f32_16x16x32_bf16 v[80:83], a[120:123], v[24:27], v[80:83]\n\t" \
    "v_mfma_f32_16x16x32_bf16 v[84:87], a[120:123], v[28:31], v[84:87]\n\t" \
    "s_waitcnt lgkmcnt(5)\n\t" \
    "v_mfma_f32_16x16x32_bf16 v[72:75], a[52:55], v[32:35], v[72:75]\n\t" \
    "ds_read_b128 v[56:59], v1 offset:1024\n\t" \
    "s_waitcnt lgkmcnt(5)\n\t" \
    "v_mfma_f32_16x16x32_bf16 v[76:79], a[52:55], v[36:39], v[76:79]\n\t" \
    "ds_read_b128 v[60:63], v1 offset:19456\n\t" \
    "v_mfma_f32_16x16x32_bf16 v[80:83], a[124:127], v[32:35], v[80:83]\n\t" \
    "v_mfma_f32_16x16x32_bf16 v[84:87], a[124:127], v[36:39], v[84:87]\n\t" \
    "global_store_dwordx4 v20, v[196:199], s[48:49] sc1\n\t" \
    "s_waitcnt lgkmcnt(5)\n\t" \
    "v_mfma_f32_16x16x32_bf16 v[72:75], a[56:59], v[40:43], v[72:75]\n\t" \
    "ds_read_b128 v[64:67], v2 offset:1024\n\t" \
    "s_waitcnt lgkmcnt(5)\n\t" \
    "v_mfma_f32_16x16x32_bf16 v[76:79], a[56:59], v[44:47], v[76:79]\n\t" \
    "ds_read_b128 v[68:71], v2 offset:19456\n\t" \
    "v_mfma_f32_16x16x32_bf16 v[80:83], a[128:131], v[40:43], v[80:83]\n\t" \
    "v_mfma_f32_16x16x32_bf16 v[84:87], a[128:131], v[44:47], v[84:87]\n\t" \
    "s_waitcnt lgkmcnt(5)\n\t" \
    "v_mfma_f32_16x16x32_bf16 v[72:75], a[60:63], v[48:51], v[72:75]\n\t" \
    "s_waitcnt lgkmcnt(4)\n\t" \
    "v_mfma_f32_16x16x32_bf16 v[76:79], a[60:63], v[52:55], v[76:79]\n\t" \
    "v_mfma_f32_16x16x32_bf16 v[80:83], a[132:135], v[48:51], v[80:83]\n\t" \
    "v_mfma_f32_16x16x32_bf16 v[84:87], a[132:135], v[52:55], v[84:87]\n\t" \
    "s_waitcnt lgkmcnt(3)\n\t" \
    "v_mfma_f32_16x16x32_bf16 v[72:75], a[64:67], v[56:59], v[72:75]\n\t" \
    "s_waitcnt lgkmcnt(2)\n\t" \
    "v_mfma_f32_16x16x32_bf16 v[76:79], a[64:67], v[60:63], v[76:79]\n\t" \
    "v_mfma_f32_16x16x32_bf16 v[80:83], a[136:139], v[56:59], v[80:83]\n\t" \
    "v_mfma_f32_16x16x32_bf16 v[84:87], a[136:139], v[60:63], v[84:87]\n\t" \
    "global_store_dword v21, v196, s[48:49] sc1\n\t" \
    "s_waitcnt lgkmcnt(1)\n\t" \
    "v_mfma_f32_16x16x32_bf16 v[72:75], a[68:71], v[64:67], v[72:75]\n\t" \
    "s_waitcnt lgkmcnt(0)\n\t" \
    "v_mfma_f32_16x16x32_bf16 v[76:79], a[68:71], v[68:71], v[76:79]\n\t" \
    "v_mfma_f32_16x16x32_bf16 v[80:83], a[140:143], v[64:67], v[80:83]\n\t" \
    "v_mfma_f32_16x16x32_bf16 v[84:87], a[140:143], v[68:71], v[84:87]\n\t" \
    "v_mov_b32 v1, v22\n\t" \
    "v_mov_b32 v2, v23\n\t" \
    "s_nop 1\n\t" \
    "v_cvt_pk_bf16_f32 v104, v72, v73\n\t" \
    "v_cvt_pk_bf16_f32 v105, v74, v75\n\t" \
    "s_nop 0\n\t" \
    "v_cvt_pk_bf16_f32 v106, v80, v81\n\t" \
    "v_cvt_pk_bf16_f32 v107, v82, v83\n\t" \
    "ds_write_b128 v4, v[104:107] offset:0\n\t" \
    "v_cvt_pk_bf16_f32 v108, v76, v77\n\t" \
    "v_cvt_pk_bf16_f32 v109, v78, v79\n\t" \
    "v_cvt_pk_bf16_f32 v110, v84, v85\n\t" \
    "v_cvt_pk_bf16_f32 v111, v86, v87\n\t" \
    "ds_write_b128 v4, v[108:111] offset:1024\n\t" \
    "s_cmp_lt_i32 s86, 16\n\t" \
    "s_cbranch_scc1 gv_rest10_%=\n\t" \
    "gv_back11_%=:\n\t" \
    "s_waitcnt vmcnt(21) lgkmcnt(0)\n\t" \
    "s_barrier\n\t" \
    "ds_read_b128 v[164:167], v5 offset:0\n\t" \
    "ds_read_b128 v[168:171], v5 offset:1024\n\t" \
    "ds_read_b128 v[172:175], v5 offset:2048\n\t" \
    "ds_read_b128 v[176:179], v5 offset:3072\n\t" \
    "ds_read_b128 v[180:183], v5 offset:4096\n\t" \
    "ds_read_b128 v[184:187], v5 offset:5120\n\t" \
    "ds_read_b128 v[188:191], v5 offset:6144\n\t" \
    "ds_read_b128 v[192:195], v5 offset:7168\n\t" \
    "s_cmp_lt_i32 s86, 16\n\t" \
    "s_cselect_b32 s71, s86, s87\n\t" \
    "s_cmp_lg_u32 s66, 0\n\t" \
    "s_cbranch_scc1 gv_SF17_%=\n\t" \
    "s_lshl_b32 s69, s67, 7\n\t" \
    "s_add_u32 s69, s69, 5783552\n\t" \
    "s_add_u32 s70, s71, 0\n\t" \
    "v_mov_b32 v156, s69\n\t" \
    "v_mov_b32 v157, s70\n\t" \
    "s_mov_b64 exec, 1\n\t" \
    "global_store_dword v156, v157, s[36:37] sc1\n\t" \
    "s_mov_b64 exec, -1\n\t" \
    "gv_SF17_%=:\n\t" \
    "v_xor_b32 v4, 0x2000, v4\n\t" \
    "v_xor_b32 v5, 0x2000, v5\n\t" \
    "s_waitcnt lgkmcnt(7)\n\t" \
    "v_mfma_f32_16x16x32_bf16 v[72:75], v[164:167], a[144:147], 0\n\t" \
    "s_waitcnt lgkmcnt(6)\n\t" \
    "v_mfma_f32_16x16x32_bf16 v[76:79], v[168:171], a[144:147], 0\n\t" \
    "s_waitcnt lgkmcnt(5)\n\t" \
    "v_mfma_f32_16x16x32_bf16 v[72:75], v[172:175], a[148:151], v[72:75]\n\t" \
    "s_waitcnt lgkmcnt(4)\n\t" \
    "v_mfma_f32_16x16x32_bf16 v[76:79], v[176:179], a[148:151], v[76:79]\n\t" \
    "s_waitcnt lgkmcnt(3)\n\t" \
    "v_mfma_f32_16x16x32_bf16 v[72:75], v[180:183], a[152:155], v[72:75]\n\t" \
    "s_waitcnt lgkmcnt(2)\n\t" \
    "v_mfma_f32_16x16x32_bf16 v[76:79], v[184:187], a[152:155], v[76:79]\n\t" \
    "s_waitcnt lgkmcnt(1)\n\t" \
    "v_mfma_f32_16x16x32_bf16 v[72:75], v[188:191], a[156:159], v[72:75]\n\t" \
    "s_waitcnt lgkmcnt(0)\n\t" \
    "v_mfma_f32_16x16x32_bf16 v[76:79], v[192:195], a[156:159], v[76:79]\n\t" \
    "v_mfma_f32_16x16x32_bf16 v[80:83], v[164:167], a[160:163], 0\n\t" \
    "v_mfma_f32_16x16x32_bf16 v[84:87], v[168:171], a[160:163], 0\n\t" \
    "v_mfma_f32_16x16x32_bf16 v[80:83], v[172:175], a[164:167], v[80:83]\n\t" \
    "v_mfma_f32_16x16x32_bf16 v[84:87], v[176:179], a[164:167], v[84:87]\n\t" \
    "v_mfma_f32_16x16x32_bf16 v[80:83], v[180:183], a[168:171], v[80:83]\n\t" \
    "v_mfma_f32_16x16x32_bf16 v[84:87], v[184:187], a[168:171], v[84:87]\n\t" \
    "v_cvt_pk_bf16_f32 v104, v72, v73\n\t" \
    "v_mfma_f32_16x16x32_bf16 v[80:83], v[188:191], a[172:175], v[80:83]\n\t" \
    "v_cvt_pk_bf16_f32 v105, v74, v75\n\t" \
    "v_mfma_f32_16x16x32_bf16 v[84:87], v[192:195], a[172:175], v[84:87]\n\t" \
    "v_cvt_pk_bf16_f32 v106, v76, v77\n\t" \
    "v_mfma_f32_16x16x32_bf16 v[88:91], v[164:167], a[176:179], 0\n\t" \
    "v_cvt_pk_bf16_f32 v107, v78, v79\n\t" \
    "v_mfma_f32_16x16x32_bf16 v[92:95], v[168:171], a[176:179], 0\n\t" \
    "v_xor_b32 v18, s40, v8\n\t" \
    "v_mfma_f32_16x16x32_bf16 v[88:91], v[172:175], a[180:183], v[88:91]\n\t" \
    "global_store_dwordx4 v18, v[104:107], s[38:39] sc1\n\t" \
    "v_mfma_f32_16x16x32_bf16 v[92:95], v[176:179], a[180:183], v[92:95]\n\t" \
    "v_mfma_f32_16x16x32_bf16 v[88:91], v[180:183], a[184:187], v[88:91]\n\t" \
    "v_mfma_f32_16x16x32_bf16 v[92:95], v[184:187], a[184:187], v[92:95]\n\t" \
    "v_cvt_pk_bf16_f32 v108, v80, v81\n\t" \
    "v_mfma_f32_16x16x32_bf16 v[88:91], v[188:191], a[188:191], v[88:91]\n\t" \
    "v_cvt_pk_bf16_f32 v109, v82, v83\n\t" \
    "v_mfma_f32_16x16x32_bf16 v[92:95], v[192:195], a[188:191], v[92:95]\n\t" \
    "v_cvt_pk_bf16_f32 v110, v84, v85\n\t" \
    "v_mfma_f32_16x16x32_bf16 v[72:75], v[164:167], a[192:195], 0\n\t" \
    "v_cvt_pk_bf16_f32 v111, v86, v87\n\t" \
    "v_mfma_f32_16x16x32_bf16 v[76:79], v[168:171], a[192:195], 0\n\t" \
    "v_xor_b32 v18, s40, v9\n\t" \
    "v_mfma_f32_16x16x32_bf16 v[72:75], v[172:175], a[196:199], v[72:75]\n\t" \
    "global_store_dwordx4 v18, v[108:111], s[38:39] sc1\n\t" \
    "v_mfma_f32_16x16x32_bf16 v[76:79], v[176:179], a[196:199], v[76:79]\n\t" \
    "v_mfma_f32_16x16x32_bf16 v[72:75], v[180:183], a[200:203], v[72:75]\n\t" \
    "v_mfma_f32_16x16x32_bf16 v[76:79], v[184:187], a[200:203], v[76:79]\n\t" \
    "v_cvt_pk_bf16_f32 v104, v88, v89\n\t" \
    "v_mfma_f32_16x16x32_bf16 v[72:75], v[188:191], a[204:207], v[72:75]\n\t" \
    "v_cvt_pk_bf16_f32 v105, v90, v91\n\t" \
    "v_mfma_f32_16x16x32_bf16 v[76:79], v[192:195], a[204:207], v[76:79]\n\t" \
    "v_cvt_pk_bf16_f32 v106, v92, v93\n\t" \
    "v_mfma_f32_16x16x32_bf16 v[80:83], v[164:167], a[208:211], 0\n\t" \
    "v_cvt_pk_bf16_f32 v107, v94, v95\n\t" \
    "v_mfma_f32_16x16x32_bf16 v[84:87], v[168:171], a[208:211], 0\n\t" \
    "v_xor_b32 v18, s40, v10\n\t" \
    "v_mfma_f32_16x16x32_bf16 v[80:83], v[172:175], a[212:215], v[80:83]\n\t" \
    "global_store_dwordx4 v18, v[104:107], s[38:39] sc1\n\t" \
    "v_mfma_f32_16x16x32_bf16 v[84:87], v[176:179], a[212:215], v[84:87]\n\t" \
    "v_mfma_f32_16x16x32_bf16 v[80:83], v[180:183], a[216:219], v[80:83]\n\t" \
    "v_mfma_f32_16x16x32_bf16 v[84:87], v[184:187], a[216:219], v[84:87]\n\t" \
    "v_cvt_pk_bf16_f32 v108, v72, v73\n\t" \
    "v_mfma_f32_16x16x32_bf16 v[80:83], v[188:191], a[220:223], v[80:83]\n\t" \
    "v_cvt_pk_bf16_f32 v109, v74, v75\n\t" \
    "v_mfma_f32_16x16x32_bf16 v[84:87], v[192:195], a[220:223], v[84:87]\n\t" \
    "v_cvt_pk_bf16_f32 v110, v76, v77\n\t" \
    "v_mfma_f32_16x16x32_bf16 v[88:91], v[164:167], a[224:227], 0\n\t" \
    "v_cvt_pk_bf16_f32 v111, v78, v79\n\t" \
    "v_mfma_f32_16x16x32_bf16 v[92:95], v[168:171], a[224:227], 0\n\t" \
    "v_xor_b32 v18, s40, v11\n\t" \
    "v_mfma_f32_16x16x32_bf16 v[88:91], v[172:175], a[228:231], v[88:91]\n\t" \
    "global_store_dwordx4 v18, v[108:111], s[38:39] sc1\n\t" \
    "v_mfma_f32_16x16x32_bf16 v[92:95], v[176:179], a[228:231], v[92:95]\n\t" \
    "v_mfma_f32_16x16x32_bf16 v[88:91], v[180:183], a[232:235], v[88:91]\n\t" \
    "v_mfma_f32_16x16x32_bf16 v[92:95], v[184:187], a[232:235], v[92:95]\n\t" \
    "v_cvt_pk_bf16_f32 v104, v80, v81\n\t" \
    "v_mfma_f32_16x16x32_bf16 v[88:91], v[188:191], a[236:239], v[88:91]\n\t" \
    "v_cvt_pk_bf16_f32 v105, v82, v83\n\t" \
    "v_mfma_f32_16x16x32_bf16 v[92:95], v[192:195], a[236:239], v[92:95]\n\t" \
    "v_cvt_pk_bf16_f32 v106, v84, v85\n\t" \
    "v_mfma_f32_16x16x32_bf16 v[72:75], v[164:167], a[240:243], 0\n\t" \
    "v_cvt_pk_bf16_f32 v107, v86, v87\n\t" \
    "v_mfma_f32_16x16x32_bf16 v[76:79], v[168:171], a[240:243], 0\n\t" \
    "v_xor_b32 v18, s40, v12\n\t" \
    "v_mfma_f32_16x16x32_bf16 v[72:75], v[172:175], a[244:247], v[72:75]\n\t" \
    "global_store_dwordx4 v18, v[104:107], s[38:39] sc1\n\t" \
    "v_mfma_f32_16x16x32_bf16 v[76:79], v[176:179], a[244:247], v[76:79]\n\t" \
    "v_mfma_f32_16x16x32_bf16 v[72:75], v[180:183], a[248:251], v[72:75]\n\t" \
    "v_mfma_f32_16x16x32_bf16 v[76:79], v[184:187], a[248:251], v[76:79]\n\t" \
    "v_cvt_pk_bf16_f32 v108, v88, v89\n\t" \
    "v_mfma_f32_16x16x32_bf16 v[72:75], v[188:191], a[252:255], v[72:75]\n\t" \
    "v_cvt_pk_bf16_f32 v109, v90, v91\n\t" \
    "v_mfma_f32_16x16x32_bf16 v[76:79], v[192:195], a[252:255], v[76:79]\n\t" \
    "v_cvt_pk_bf16_f32 v110, v92, v93\n\t" \
    "v_mfma_f32_16x16x32_bf16 v[80:83], v[164:167], v[224:227], 0\n\t" \
    "v_cvt_pk_bf16_f32 v111, v94, v95\n\t" \
    "v_mfma_f32_16x16x32_bf16 v[84:87], v[168:171], v[224:227], 0\n\t" \
    "v_xor_b32 v18, s40, v13\n\t" \
    "v_mfma_f32_16x16x32_bf16 v[80:83], v[172:175], v[228:231], v[80:83]\n\t" \
    "global_store_dwordx4 v18, v[108:111], s[38:39] sc1\n\t" \
    "v_mfma_f32_16x16x32_bf16 v[84:87], v[176:179], v[228:231], v[84:87]\n\t" \
    "v_mfma_f32_16x16x32_bf16 v[80:83], v[180:183], v[232:235], v[80:83]\n\t" \
    "v_mfma_f32_16x16x32_bf16 v[84:87], v[184:187], v[232:235], v[84:87]\n\t" \
    "v_cvt_pk_bf16_f32 v104, v72, v73\n\t" \
    "v_mfma_f32_16x16x32_bf16 v[80:83], v[188:191], v[236:239], v[80:83]\n\t" \
    "v_cvt_pk_bf16_f32 v105, v74, v75\n\t" \
    "v_mfma_f32_16x16x32_bf16 v[84:87], v[192:195], v[236:239], v[84:87]\n\t" \
    "v_cvt_pk_bf16_f32 v106, v76, v77\n\t" \
    "v_mfma_f32_16x16x32_bf16 v[88:91], v[164:167], v[240:243], 0\n\t" \
    "v_cvt_pk_bf16_f32 v107, v78, v79\n\t" \
    "ds_read_b128 v[24:27], v22 offset:0\n\t" \
    "v_mfma_f32_16x16x32_bf16 v[92:95], v[168:171], v[240:243], 0\n\t" \
    "v_xor_b32 v18, s40, v14\n\t" \
    "ds_read_b128 v[28:31], v22 offset:18432\n\t" \
    "v_mfma_f32_16x16x32_bf16 v[88:91], v[172:175], v[244:247], v[88:91]\n\t" \
    "global_store_dwordx4 v18, v[104:107], s[38:39] sc1\n\t" \
    "ds_read_b128 v[32:35], v23 offset:0\n\t" \
    "v_mfma_f32_16x16x32_bf16 v[92:95], v[176:179], v[244:247], v[92:95]\n\t" \
    "ds_read_b128 v[36:39], v23 offset:18432\n\t" \
    "v_mfma_f32_16x16x32_bf16 v[88:91], v[180:183], v[248:251], v[88:91]\n\t" \
    "ds_read_b128 v[40:43], v22 offset:128\n\t" \
    "v_mfma_f32_16x16x32_bf16 v[92:95], v[184:187], v[248:251], v[92:95]\n\t" \
    "v_cvt_pk_bf16_f32 v108, v80, v81\n\t" \
    "ds_read_b128 v[44:47], v22 offset:18560\n\t" \
    "v_mfma_f32_16x16x32_bf16 v[88:91], v[188:191], v[252:255], v[88:91]\n\t" \
    "v_cvt_pk_bf16_f32 v109, v82, v83\n\t" \
    "v_mfma_f32_16x16x32_bf16 v[92:95], v[192:195], v[252:255], v[92:95]\n\t" \
    "v_cvt_pk_bf16_f32 v110, v84, v85\n\t" \
    "v_cvt_pk_bf16_f32 v111, v86, v87\n\t" \
    "v_xor_b32 v18, s40, v15\n\t" \
    "global_store_dwordx4 v18, v[108:111], s[38:39] sc1\n\t" \
    "s_nop 1\n\t" \
    "v_cvt_pk_bf16_f32 v104, v88, v89\n\t" \
    "v_cvt_pk_bf16_f32 v105, v90, v91\n\t" \
    "v_cvt_pk_bf16_f32 v106, v92, v93\n\t" \
    "v_cvt_pk_bf16_f32 v107, v94, v95\n\t" \
    "v_xor_b32 v18, s40, v16\n\t" \
    "global_store_dwordx4 v18, v[104:107], s[38:39] sc1\n\t" \
    "s_cmp_ge_u32 s65, 2\n\t" \
    "s_cbranch_scc1 gv_nofast9_%=\n\t" \
    "s_waitcnt vmcnt(0)\n\t" \
    "s_barrier\n\t" \
    "s_cmp_lg_u32 s66, 0\n\t" \
    "s_cbranch_scc1 gv_SF18_%=\n\t" \
    "s_lshl_b32 s69, s67, 7\n\t" \
    "s_add_u32 s69, s69, 5783552\n\t" \
    "s_add_u32 s70, s65, 0\n\t" \
    "v_mov_b32 v156, s69\n\t" \
    "v_mov_b32 v157, s70\n\t" \
    "s_mov_b64 exec, 1\n\t" \
    "global_store_dword v156, v157, s[36:37] sc1\n\t" \
    "s_mov_b64 exec, -1\n\t" \
    "gv_SF18_%=:\n\t" \
    "s_cmp_lg_u32 s66, 1\n\t" \
    "s_cbranch_scc1 gv_nofast9_%=\n\t" \
    "v_mov_b32 v120, 5838848\n\t" \
    "v_mov_b32 v121, 1\n\t" \
    "s_mov_b64 exec, 1\n\t" \
    "global_atomic_add v122, v120, v121, s[36:37] sc0 sc1\n\t" \
    "s_mov_b64 exec, -1\n\t" \
    "s_mov_b64 vcc, -1\n\t" \
    "gv_nofast9_%=:\n\t" \
    "s_mov_b32 s87, s86\n\t" \
    "s_mov_b32 s86, s65\n\t" \
    "s_mov_b32 s65, s84\n\t" \
    "s_mov_b32 s84, s85\n\t" \
    "s_cmp_lt_u32 s84, 24\n\t" \
    "s_addc_u32 s85, s84, 1\n\t" \
    "s_cmp_lt_u32 s65, s88\n\t" \
    "s_cbranch_scc1 gv_loop8_%=\n\t" \
    "s_waitcnt vmcnt(0) lgkmcnt(0)\n\t" \
    "s_barrier\n\t" \
    "s_mov_b32 s65, 99\n\t" \
    "s_cmp_lg_u32 s66, 0\n\t" \
    "s_cbranch_scc1 gv_SF20_%=\n\t" \
    "s_lshl_b32 s69, s67, 7\n\t" \
    "s_add_u32 s69, s69, 5783552\n\t" \
    "s_add_u32 s70, s65, 0\n\t" \
    "v_mov_b32 v156, s69\n\t" \
    "v_mov_b32 v157, s70\n\t" \
    "s_mov_b64 exec, 1\n\t" \
    "global_store_dword v156, v157, s[36:37] sc1\n\t" \
    "s_mov_b64 exec, -1\n\t" \
    "gv_SF20_%=:\n\t" \
    "s_branch gv_end21_%=\n\t" \
    "gv_gol12_%=:\n\t" \
    "s_mov_b64 vcc, 0\n\t" \
    "v_mbcnt_lo_u32_b32 v120, -1, 0\n\t" \
    "v_and_b32 v121, 3, v120\n\t" \
    "v_lshlrev_b32 v121, 16, v121\n\t" \
    "v_bfe_u32 v123, v120, 2, 2\n\t" \
    "v_lshl_add_u32 v121, v123, 11, v121\n\t" \
    "v_bfe_u32 v123, v120, 4, 1\n\t" \
    "v_lshl_add_u32 v121, v123, 14, v121\n\t" \
    "s_bitcmp1_b32 s44, 2\n\t" \
    "s_cselect_b32 s63, 8192, 0\n\t" \
    "s_or_b32 s63, s63, 0x200000\n\t" \
    "v_add_u32 v121, s63, v121\n\t" \
    "s_waitcnt vmcnt(9)\n\t" \
    "v_readfirstlane_b32 s69, v122\n\t" \
    "s_nop 1\n\t" \
    "s_cmp_lg_u32 s69, 126\n\t" \
    "s_cbranch_scc1 gv_go22_%=\n\t" \
    "s_memrealtime s[56:57]\n\t" \
    "s_waitcnt lgkmcnt(0)\n\t" \
    "s_lshl_b32 s69, s56, 4\n\t" \
    "s_add_u32 s69, s69, 2800\n\t" \
    "s_add_u32 s70, s56, 24\n\t" \
    "s_lshr_b32 s70, s70, 4\n\t" \
    "v_lshrrev_b32 v123, 2, v120\n\t" \
    "v_add_u32 v123, s70, v123\n\t" \
    "v_and_b32 v123, 63, v123\n\t" \
    "v_lshlrev_b32 v123, 6, v123\n\t" \
    "v_and_b32 v122, 3, v120\n\t" \
    "v_lshl_add_u32 v123, v122, 12, v123\n\t" \
    "v_add_u32 v123, 5865472, v123\n\t" \
    "v_mov_b32 v120, s69\n\t" \
    "s_mov_b32 exec_hi, 0\n\t" \
    "global_store_dword v123, v120, s[36:37] sc1\n\t" \
    "global_store_dword v121, v120, s[36:37] sc1\n\t" \
    "s_mov_b32 exec_hi, -1\n\t" \
    "gv_go22_%=:\n\t" \
    "s_branch gv_gob13_%=\n\t" \
    "gv_rest10_%=:\n\t" \
    "s_waitcnt vmcnt(12) lgkmcnt(0)\n\t" \
    "s_cmp_ge_i32 s86, 0\n\t" \
    "s_cbranch_scc1 gv_back11_%=\n\t" \
    "s_barrier\n\t" \
    "ds_read_b64_tr_b16 a[156:157], v212 offset:0\n\t" \
    "ds_read_b64_tr_b16 a[158:159], v212 offset:18432\n\t" \
    "ds_read_b64_tr_b16 a[172:173], v213 offset:0\n\t" \
    "ds_read_b64_tr_b16 a[174:175], v213 offset:18432\n\t" \
    "ds_read_b64_tr_b16 a[188:189], v214 offset:0\n\t" \
    "ds_read_b64_tr_b16 a[190:191], v214 offset:18432\n\t" \
    "s_waitcnt lgkmcnt(6)\n\t" \
    "ds_read_b64_tr_b16 a[204:205], v215 offset:0\n\t" \
    "ds_read_b64_tr_b16 a[206:207], v215 offset:18432\n\t" \
    "ds_read_b64_tr_b16 a[220:221], v212 offset:128\n\t" \
    "ds_read_b64_tr_b16 a[222:223], v212 offset:18560\n\t" \
    "ds_read_b64_tr_b16 a[236:237], v213 offset:128\n\t" \
    "ds_read_b64_tr_b16 a[238:239], v213 offset:18560\n\t" \
    "s_waitcnt lgkmcnt(6)\n\t" \
    "ds_read_b64_tr_b16 a[252:253], v214 offset:128\n\t" \
    "ds_read_b64_tr_b16 a[254:255], v214 offset:18560\n\t" \
    "ds_read_b64_tr_b16 v[236:237], v215 offset:128\n\t" \
    "ds_read_b64_tr_b16 v[238:239], v215 offset:18560\n\t" \
    "ds_read_b64_tr_b16 v[252:253], v212 offset:256\n\t" \
    "ds_read_b64_tr_b16 v[254:255], v212 offset:18688\n\t" \
    "s_waitcnt lgkmcnt(6)\n\t" \
    "v_add_u32 v212, 0x9000, v212\n\t" \
    "v_add_u32 v213, 0x9000, v213\n\t" \
    "v_add_u32 v214, 0x9000, v214\n\t" \
    "v_add_u32 v215, 0x9000, v215\n\t" \
    "s_branch gv_back11_%=\n\t" \
    "gv_end21_%=:\n\t" \
    "gv_last_%=:\n\t" \
    ""
#define ASM_CV \
    "v_mov_b32 v0, %0\n\t" \
    "v_mov_b32 v2, %2\n\t" \
    "v_subrev_u32 v1, 768, %1\n\t" \
    "v_mov_b32 v4, 0\n\t" \
    "v_mov_b32 v5, 0\n\t" \
    "v_mov_b32 v6, 0\n\t" \
    "v_mov_b32 v7, 0\n\t" \
    "v_mov_b32 v8, 0\n\t" \
    "v_mov_b32 v9, 0\n\t" \
    "v_mov_b32 v10, 0\n\t" \
    "v_mov_b32 v11, 0\n\t" \
    "v_mov_b32 v12, 0\n\t" \
    "v_mov_b32 v13, 0\n\t" \
    "v_mov_b32 v14, 0\n\t" \
    "v_mov_b32 v15, 0\n\t" \
    "v_mov_b32 v16, 0\n\t" \
    "v_mov_b32 v17, 0\n\t" \
    "v_mov_b32 v18, 0\n\t" \
    "v_mov_b32 v19, 0\n\t" \
    "v_mov_b32 v20, 0\n\t" \
    "v_mov_b32 v21, 0\n\t" \
    "v_mov_b32 v22, 0\n\t" \
    "v_mov_b32 v23, 0\n\t" \
    "v_mov_b32 v24, 0\n\t" \
    "v_mov_b32 v25, 0\n\t" \
    "v_mov_b32 v26, 0\n\t" \
    "v_mov_b32 v27, 0\n\t" \
    "v_mov_b32 v28, 0\n\t" \
    "v_mov_b32 v29, 0\n\t" \
    "v_mov_b32 v30, 0\n\t" \
    "v_mov_b32 v31, 0\n\t" \
    "v_mov_b32 v32, 0\n\t" \
    "v_mov_b32 v33, 0\n\t" \
    "v_mov_b32 v34, 0\n\t" \
    "v_mov_b32 v35, 0\n\t" \
    "s_waitcnt lgkmcnt(0)\n\t" \
    "ds_read_b64 v[36:37], v0 offset:0\n\t" \
    "ds_read_b64 v[38:39], v0 offset:8\n\t" \
    "ds_read_b64 v[40:41], v0 offset:32\n\t" \
    "ds_read_b64 v[42:43], v0 offset:40\n\t" \
    "ds_read_b128 v[44:47], v1 offset:768\n\t" \
    "ds_read_b128 v[48:51], v1 offset:2304\n\t" \
    "ds_read_b128 v[52:55], v1 offset:3840\n\t" \
    "ds_read_b128 v[56:59], v1 offset:5376\n\t" \
    "s_mov_b32 s69, 8\n\t" \
    "cv_c1_%=:\n\t" \
    "s_waitcnt lgkmcnt(3)\n\t" \
    "v_mfma_f32_16x16x32_bf16 v[4:7], v[36:39], v[44:47], v[4:7]\n\t" \
    "ds_read_b64 v[76:77], v0 offset:64\n\t" \
    "ds_read_b64 v[78:79], v0 offset:72\n\t" \
    "ds_read_b64 v[80:81], v0 offset:96\n\t" \
    "v_mfma_f32_16x16x32_bf16 v[20:23], v[40:43], v[44:47], v[20:23]\n\t" \
    "ds_read_b64 v[82:83], v0 offset:104\n\t" \
    "ds_read_b128 v[84:87], v1 offset:672\n\t" \
    "ds_read_b128 v[88:91], v1 offset:2208\n\t" \
    "s_waitcnt lgkmcnt(8)\n\t" \
    "v_mfma_f32_16x16x32_bf16 v[8:11], v[36:39], v[48:51], v[8:11]\n\t" \
    "ds_read_b128 v[92:95], v1 offset:3744\n\t" \
    "ds_read_b128 v[96:99], v1 offset:5280\n\t" \
    "v_mfma_f32_16x16x32_bf16 v[24:27], v[40:43], v[48:51], v[24:27]\n\t" \
    "s_waitcnt lgkmcnt(9)\n\t" \
    "v_mfma_f32_16x16x32_bf16 v[12:15], v[36:39], v[52:55], v[12:15]\n\t" \
    "v_mfma_f32_16x16x32_bf16 v[28:31], v[40:43], v[52:55], v[28:31]\n\t" \
    "s_waitcnt lgkmcnt(8)\n\t" \
    "v_mfma_f32_16x16x32_bf16 v[16:19], v[36:39], v[56:59], v[16:19]\n\t" \
    "v_mfma_f32_16x16x32_bf16 v[32:35], v[40:43], v[56:59], v[32:35]\n\t" \
    "s_waitcnt lgkmcnt(3)\n\t" \
    "v_mfma_f32_16x16x32_bf16 v[4:7], v[76:79], v[84:87], v[4:7]\n\t" \
    "ds_read_b64 v[36:37], v0 offset:128\n\t" \
    "ds_read_b64 v[38:39], v0 offset:136\n\t" \
    "ds_read_b64 v[40:41], v0 offset:160\n\t" \
    "v_mfma_f32_16x16x32_bf16 v[20:23], v[80:83], v[84:87], v[20:23]\n\t" \
    "ds_read_b64 v[42:43], v0 offset:168\n\t" \
    "ds_read_b128 v[44:47], v1 offset:576\n\t" \
    "ds_read_b128 v[48:51], v1 offset:2112\n\t" \
    "s_waitcnt lgkmcnt(8)\n\t" \
    "v_mfma_f32_16x16x32_bf16 v[8:11], v[76:79], v[88:91], v[8:11]\n\t" \
    "ds_read_b128 v[52:55], v1 offset:3648\n\t" \
    "ds_read_b128 v[56:59], v1 offset:5184\n\t" \
    "v_mfma_f32_16x16x32_bf16 v[24:27], v[80:83], v[88:91], v[24:27]\n\t" \
    "s_waitcnt lgkmcnt(9)\n\t" \
    "v_mfma_f32_16x16x32_bf16 v[12:15], v[76:79], v[92:95], v[12:15]\n\t" \
    "v_mfma_f32_16x16x32_bf16 v[28:31], v[80:83], v[92:95], v[28:31]\n\t" \
    "s_waitcnt lgkmcnt(8)\n\t" \
    "v_mfma_f32_16x16x32_bf16 v[16:19], v[76:79], v[96:99], v[16:19]\n\t" \
    "v_mfma_f32_16x16x32_bf16 v[32:35], v[80:83], v[96:99], v[32:35]\n\t" \
    "v_add_u32 v0, 128, v0\n\t" \
    "v_subrev_u32 v1, 192, v1\n\t" \
    "s_sub_u32 s69, s69, 1\n\t" \
    "s_cmp_lg_u32 s69, 0\n\t" \
    "s_cbranch_scc1 cv_c1_%=\n\t" \
    "s_waitcnt lgkmcnt(0)\n\t" \
    "ds_read_b64 v[36:37], v0 offset:0\n\t" \
    "ds_read_b64 v[38:39], v0 offset:8\n\t" \
    "ds_read_b64 v[40:41], v0 offset:32\n\t" \
    "ds_read_b64 v[42:43], v0 offset:40\n\t" \
    "ds_read_b128 v[48:51], v1 offset:2304\n\t" \
    "ds_read_b128 v[52:55], v1 offset:3840\n\t" \
    "ds_read_b128 v[56:59], v1 offset:5376\n\t" \
    "ds_read_b64 v[76:77], v0 offset:64\n\t" \
    "ds_read_b64 v[78:79], v0 offset:72\n\t" \
    "ds_read_b64 v[80:81], v0 offset:96\n\t" \
    "ds_read_b64 v[82:83], v0 offset:104\n\t" \
    "ds_read_b128 v[88:91], v1 offset:2208\n\t" \
    "ds_read_b128 v[92:95], v1 offset:3744\n\t" \
    "ds_read_b128 v[96:99], v1 offset:5280\n\t" \
    "s_mov_b32 s69, 4\n\t" \
    "cv_c2_%=:\n\t" \
    "s_waitcnt lgkmcnt(9)\n\t" \
    "v_mfma_f32_16x16x32_bf16 v[8:11], v[36:39], v[48:51], v[8:11]\n\t" \
    "ds_read_b64 v[116:117], v0 offset:128\n\t" \
    "ds_read_b64 v[118:119], v0 offset:136\n\t" \
    "ds_read_b64 v[120:121], v0 offset:160\n\t" \
    "v_mfma_f32_16x16x32_bf16 v[24:27], v[40:43], v[48:51], v[24:27]\n\t" \
    "ds_read_b64 v[122:123], v0 offset:168\n\t" \
    "ds_read_b128 v[128:131], v1 offset:2112\n\t" \
    "ds_read_b128 v[132:135], v1 offset:3648\n\t" \
    "s_waitcnt lgkmcnt(14)\n\t" \
    "v_mfma_f32_16x16x32_bf16 v[12:15], v[36:39], v[52:55], v[12:15]\n\t" \
    "ds_read_b128 v[136:139], v1 offset:5184\n\t" \
    "v_mfma_f32_16x16x32_bf16 v[28:31], v[40:43], v[52:55], v[28:31]\n\t" \
    "s_waitcnt lgkmcnt(14)\n\t" \
    "v_mfma_f32_16x16x32_bf16 v[16:19], v[36:39], v[56:59], v[16:19]\n\t" \
    "ds_read_b64 v[156:157], v0 offset:192\n\t" \
    "v_mfma_f32_16x16x32_bf16 v[32:35], v[40:43], v[56:59], v[32:35]\n\t" \
    "s_waitcnt lgkmcnt(10)\n\t" \
    "v_mfma_f32_16x16x32_bf16 v[8:11], v[76:79], v[88:91], v[8:11]\n\t" \
    "ds_read_b64 v[158:159], v0 offset:200\n\t" \
    "ds_read_b64 v[160:161], v0 offset:224\n\t" \
    "ds_read_b64 v[162:163], v0 offset:232\n\t" \
    "v_mfma_f32_16x16x32_bf16 v[24:27], v[80:83], v[88:91], v[24:27]\n\t" \
    "ds_read_b128 v[168:171], v1 offset:2016\n\t" \
    "ds_read_b128 v[172:175], v1 offset:3552\n\t" \
    "s_waitcnt lgkmcnt(14)\n\t" \
    "v_mfma_f32_16x16x32_bf16 v[12:15], v[76:79], v[92:95], v[12:15]\n\t" \
    "ds_read_b128 v[176:179], v1 offset:5088\n\t" \
    "v_mfma_f32_16x16x32_bf16 v[28:31], v[80:83], v[92:95], v[28:31]\n\t" \
    "s_waitcnt lgkmcnt(14)\n\t" \
    "v_mfma_f32_16x16x32_bf16 v[16:19], v[76:79], v[96:99], v[16:19]\n\t" \
    "v_mfma_f32_16x16x32_bf16 v[32:35], v[80:83], v[96:99], v[32:35]\n\t" \
    "s_waitcnt lgkmcnt(9)\n\t" \
    "v_mfma_f32_16x16x32_bf16 v[8:11], v[116:119], v[128:131], v[8:11]\n\t" \
    "ds_read_b64 v[36:37], v0 offset:256\n\t" \
    "ds_read_b64 v[38:39], v0 offset:264\n\t" \
    "ds_read_b64 v[40:41], v0 offset:288\n\t" \
    "v_mfma_f32_16x16x32_bf16 v[24:27], v[120:123], v[128:131], v[24:27]\n\t" \
    "ds_read_b64 v[42:43], v0 offset:296\n\t" \
    "ds_read_b128 v[48:51], v1 offset:1920\n\t" \
    "ds_read_b128 v[52:55], v1 offset:3456\n\t" \
    "s_waitcnt lgkmcnt(14)\n\t" \
    "v_mfma_f32_16x16x32_bf16 v[12:15], v[116:119], v[132:135], v[12:15]\n\t" \
    "ds_read_b128 v[56:59], v1 offset:4992\n\t" \
    "v_mfma_f32_16x16x32_bf16 v[28:31], v[120:123], v[132:135], v[28:31]\n\t" \
    "s_waitcnt lgkmcnt(14)\n\t" \
    "v_mfma_f32_16x16x32_bf16 v[16:19], v[116:119], v[136:139], v[16:19]\n\t" \
    "ds_read_b64 v[76:77], v0 offset:320\n\t" \
    "v_mfma_f32_16x16x32_bf16 v[32:35], v[120:123], v[136:139], v[32:35]\n\t" \
    "s_waitcnt lgkmcnt(10)\n\t" \
    "v_mfma_f32_16x16x32_bf16 v[8:11], v[156:159], v[168:171], v[8:11]\n\t" \
    "ds_read_b64 v[78:79], v0 offset:328\n\t" \
    "ds_read_b64 v[80:81], v0 offset:352\n\t" \
    "ds_read_b64 v[82:83], v0 offset:360\n\t" \
    "v_mfma_f32_16x16x32_bf16 v[24:27], v[160:163], v[168:171], v[24:27]\n\t" \
    "ds_read_b128 v[88:91], v1 offset:1824\n\t" \
    "ds_read_b128 v[92:95], v1 offset:3360\n\t" \
    "s_waitcnt lgkmcnt(14)\n\t" \
    "v_mfma_f32_16x16x32_bf16 v[12:15], v[156:159], v[172:175], v[12:15]\n\t" \
    "ds_read_b128 v[96:99], v1 offset:4896\n\t" \
    "v_mfma_f32_16x16x32_bf16 v[28:31], v[160:163], v[172:175], v[28:31]\n\t" \
    "s_waitcnt lgkmcnt(14)\n\t" \
    "v_mfma_f32_16x16x32_bf16 v[16:19], v[156:159], v[176:179], v[16:19]\n\t" \
    "v_mfma_f32_16x16x32_bf16 v[32:35], v[160:163], v[176:179], v[32:35]\n\t" \
    "v_add_u32 v0, 256, v0\n\t" \
    "v_subrev_u32 v1, 384, v1\n\t" \
    "s_sub_u32 s69, s69, 1\n\t" \
    "s_cmp_lg_u32 s69, 0\n\t" \
    "s_cbranch_scc1 cv_c2_%=\n\t" \
    "s_waitcnt lgkmcnt(0)\n\t" \
    "s_mov_b32 s69, 4\n\t" \
    "cv_c3_%=:\n\t" \
    "s_waitcnt lgkmcnt(7)\n\t" \
    "v_mfma_f32_16x16x32_bf16 v[12:15], v[36:39], v[52:55], v[12:15]\n\t" \
    "ds_read_b64 v[116:117], v0 offset:128\n\t" \
    "ds_read_b64 v[118:119], v0 offset:136\n\t" \
    "ds_read_b64 v[120:121], v0 offset:160\n\t" \
    "v_mfma_f32_16x16x32_bf16 v[28:31], v[40:43], v[52:55], v[28:31]\n\t" \
    "ds_read_b64 v[122:123], v0 offset:168\n\t" \
    "ds_read_b128 v[132:135], v1 offset:3648\n\t" \
    "ds_read_b128 v[136:139], v1 offset:5184\n\t" \
    "s_waitcnt lgkmcnt(12)\n\t" \
    "v_mfma_f32_16x16x32_bf16 v[16:19], v[36:39], v[56:59], v[16:19]\n\t" \
    "ds_read_b64 v[156:157], v0 offset:192\n\t" \
    "ds_read_b64 v[158:159], v0 offset:200\n\t" \
    "ds_read_b64 v[160:161], v0 offset:224\n\t" \
    "v_mfma_f32_16x16x32_bf16 v[32:35], v[40:43], v[56:59], v[32:35]\n\t" \
    "s_waitcnt lgkmcnt(10)\n\t" \
    "v_mfma_f32_16x16x32_bf16 v[12:15], v[76:79], v[92:95], v[12:15]\n\t" \
    "ds_read_b64 v[162:163], v0 offset:232\n\t" \
    "ds_read_b128 v[172:175], v1 offset:3552\n\t" \
    "ds_read_b128 v[176:179], v1 offset:5088\n\t" \
    "v_mfma_f32_16x16x32_bf16 v[28:31], v[80:83], v[92:95], v[28:31]\n\t" \
    "s_waitcnt lgkmcnt(12)\n\t" \
    "v_mfma_f32_16x16x32_bf16 v[16:19], v[76:79], v[96:99], v[16:19]\n\t" \
    "v_mfma_f32_16x16x32_bf16 v[32:35], v[80:83], v[96:99], v[32:35]\n\t" \
    "s_waitcnt lgkmcnt(7)\n\t" \
    "v_mfma_f32_16x16x32_bf16 v[12:15], v[116:119], v[132:135], v[12:15]\n\t" \
    "ds_read_b64 v[36:37], v0 offset:256\n\t" \
    "ds_read_b64 v[38:39], v0 offset:264\n\t" \
    "ds_read_b64 v[40:41], v0 offset:288\n\t" \
    "v_mfma_f32_16x16x32_bf16 v[28:31], v[120:123], v[132:135], v[28:31]\n\t" \
    "ds_read_b64 v[42:43], v0 offset:296\n\t" \
    "ds_read_b128 v[52:55], v1 offset:3456\n\t" \
    "ds_read_b128 v[56:59], v1 offset:4992\n\t" \
    "s_waitcnt lgkmcnt(12)\n\t" \
    "v_mfma_f32_16x16x32_bf16 v[16:19], v[116:119], v[136:139], v[16:19]\n\t" \
    "ds_read_b64 v[76:77], v0 offset:320\n\t" \
    "ds_read_b64 v[78:79], v0 offset:328\n\t" \
    "ds_read_b64 v[80:81], v0 offset:352\n\t" \
    "v_mfma_f32_16x16x32_bf16 v[32:35], v[120:123], v[136:139], v[32:35]\n\t" \
    "s_waitcnt lgkmcnt(10)\n\t" \
    "v_mfma_f32_16x16x32_bf16 v[12:15], v[156:159], v[172:175], v[12:15]\n\t" \
    "ds_read_b64 v[82:83], v0 offset:360\n\t" \
    "ds_read_b128 v[92:95], v1 offset:3360\n\t" \
    "ds_read_b128 v[96:99], v1 offset:4896\n\t" \
    "v_mfma_f32_16x16x32_bf16 v[28:31], v[160:163], v[172:175], v[28:31]\n\t" \
    "s_waitcnt lgkmcnt(12)\n\t" \
    "v_mfma_f32_16x16x32_bf16 v[16:19], v[156:159], v[176:179], v[16:19]\n\t" \
    "v_mfma_f32_16x16x32_bf16 v[32:35], v[160:163], v[176:179], v[32:35]\n\t" \
    "v_add_u32 v0, 256, v0\n\t" \
    "v_subrev_u32 v1, 384, v1\n\t" \
    "s_sub_u32 s69, s69, 1\n\t" \
    "s_cmp_lg_u32 s69, 0\n\t" \
    "s_cbranch_scc1 cv_c3_%=\n\t" \
    "s_waitcnt lgkmcnt(0)\n\t" \
    "s_mov_b32 s69, 4\n\t" \
    "cv_c4_%=:\n\t" \
    "s_waitcnt lgkmcnt(5)\n\t" \
    "v_mfma_f32_16x16x32_bf16 v[16:19], v[36:39], v[56:59], v[16:19]\n\t" \
    "ds_read_b64 v[116:117], v0 offset:128\n\t" \
    "ds_read_b64 v[118:119], v0 offset:136\n\t" \
    "ds_read_b64 v[120:121], v0 offset:160\n\t" \
    "v_mfma_f32_16x16x32_bf16 v[32:35], v[40:43], v[56:59], v[32:35]\n\t" \
    "ds_read_b64 v[122:123], v0 offset:168\n\t" \
    "ds_read_b128 v[136:139], v1 offset:5184\n\t" \
    "ds_read_b64 v[156:157], v0 offset:192\n\t" \
    "s_waitcnt lgkmcnt(6)\n\t" \
    "v_mfma_f32_16x16x32_bf16 v[16:19], v[76:79], v[96:99], v[16:19]\n\t" \
    "ds_read_b64 v[158:159], v0 offset:200\n\t" \
    "ds_read_b64 v[160:161], v0 offset:224\n\t" \
    "ds_read_b64 v[162:163], v0 offset:232\n\t" \
    "v_mfma_f32_16x16x32_bf16 v[32:35], v[80:83], v[96:99], v[32:35]\n\t" \
    "ds_read_b128 v[176:179], v1 offset:5088\n\t" \
    "s_waitcnt lgkmcnt(5)\n\t" \
    "v_mfma_f32_16x16x32_bf16 v[16:19], v[116:119], v[136:139], v[16:19]\n\t" \
    "ds_read_b64 v[36:37], v0 offset:256\n\t" \
    "ds_read_b64 v[38:39], v0 offset:264\n\t" \
    "ds_read_b64 v[40:41], v0 offset:288\n\t" \
    "v_mfma_f32_16x16x32_bf16 v[32:35], v[120:123], v[136:139], v[32:35]\n\t" \
    "ds_read_b64 v[42:43], v0 offset:296\n\t" \
    "ds_read_b128 v[56:59], v1 offset:4992\n\t" \
    "ds_read_b64 v[76:77], v0 offset:320\n\t" \
    "s_waitcnt lgkmcnt(6)\n\t" \
    "v_mfma_f32_16x16x32_bf16 v[16:19], v[156:159], v[176:179], v[16:19]\n\t" \
    "ds_read_b64 v[78:79], v0 offset:328\n\t" \
    "ds_read_b64 v[80:81], v0 offset:352\n\t" \
    "ds_read_b64 v[82:83], v0 offset:360\n\t" \
    "v_mfma_f32_16x16x32_bf16 v[32:35], v[160:163], v[176:179], v[32:35]\n\t" \
    "ds_read_b128 v[96:99], v1 offset:4896\n\t" \
    "v_add_u32 v0, 256, v0\n\t" \
    "v_subrev_u32 v1, 384, v1\n\t" \
    "s_sub_u32 s69, s69, 1\n\t" \
    "s_cmp_lg_u32 s69, 0\n\t" \
    "s_cbranch_scc1 cv_c4_%=\n\t" \
    "s_waitcnt lgkmcnt(0)\n\t" \
    "s_nop 7\n\t" \
    "ds_write_b128 v2, v[4:7] offset:0\n\t" \
    "ds_write_b128 v2, v[8:11] offset:1024\n\t" \
    "ds_write_b128 v2, v[12:15] offset:2048\n\t" \
    "ds_write_b128 v2, v[16:19] offset:3072\n\t" \
    "ds_write_b128 v2, v[20:23] offset:4096\n\t" \
    "ds_write_b128 v2, v[24:27] offset:5120\n\t" \
    "ds_write_b128 v2, v[28:31] offset:6144\n\t" \
    "ds_write_b128 v2, v[32:35] offset:7168\n\t" \
    "s_waitcnt lgkmcnt(0)\n\t" \
    "cv_last_%=:\n\t" \
    ""
#define ASM_EA \
    "s_bitcmp1_b32 %1, 2\n\t" \
    "s_cselect_b32 s40, 0x30, 0x28\n\t" \
    "s_cmp_lt_u32 %1, 64\n\t" \
    "s_cselect_b32 s40, s40, 0x38\n\t" \
    "s_load_dwordx2 s[42:43], %0, s40\n\t" \
    "s_load_dword s48, %0, 0x40\n\t" \
    "s_getpc_b64 s[44:45]\n\t" \
    "ea_pc_%=:\n\t" \
    "s_bfe_u32 s40, %1, 0x20003\n\t" \
    "s_lshl_b32 s40, s40, 15\n\t" \
    "v_lshl_add_u32 v1, %2, 7, s40\n\t" \
    "v_min_u32 v1, code_end_marker-ea_pc_%=, v1\n\t" \
    "s_mov_b32 m0, 155648\n\t" \
    "v_readfirstlane_b32 s41, %2\n\t" \
    "global_load_lds_dword v1, s[44:45]\n\t" \
    "s_bitcmp1_b32 %1, 2\n\t" \
    "s_cselect_b32 s40, 24, 16\n\t" \
    "s_lshr_b32 s47, %1, 3\n\t" \
    "s_cmp_ge_u32 s47, s40\n\t" \
    "s_cbranch_scc1 ea_done1_%=\n\t" \
    "s_mov_b32 s46, pad_qk-ea_pc_%=\n\t" \
    "s_mov_b32 s40, pad_vv-ea_pc_%=\n\t" \
    "s_bitcmp1_b32 %1, 2\n\t" \
    "s_cselect_b32 s46, pad_gx-ea_pc_%=, s46\n\t" \
    "s_cselect_b32 s40, pad_gv-ea_pc_%=, s40\n\t" \
    "s_cmp_lt_u32 %1, 64\n\t" \
    "s_cselect_b32 s40, s46, s40\n\t" \
    "s_add_u32 s44, s44, s40\n\t" \
    "s_addc_u32 s45, s45, 0\n\t" \
    "s_swappc_b64 s[46:47], s[44:45]\n\t" \
    "s_lshl_b32 m0, s41, 4\n\t" \
    "s_and_b32 s40, %1, 3\n\t" \
    "s_lshl_b32 s40, s40, 11\n\t" \
    "s_bfe_u32 s46, %1, 0x30003\n\t" \
    "s_lshl_b32 s46, s46, 8\n\t" \
    "s_add_u32 s40, s40, s46\n\t" \
    "s_and_b32 s41, s41, 0x80\n\t" \
    "s_xor_b32 s40, s40, s41\n\t" \
    "v_lshrrev_b32 v1, 3, %2\n\t" \
    "v_and_b32 v1, 6, v1\n\t" \
    "v_xor_b32 v1, v1, %2\n\t" \
    "v_and_b32 v1, 15, v1\n\t" \
    "v_lshlrev_b32 v1, 4, v1\n\t" \
    "v_xor_b32 v1, s40, v1\n\t" \
    "v_lshrrev_b32 v2, 4, %2\n\t" \
    "v_lshl_add_u32 v1, v2, 13, v1\n\t" \
    "s_mov_b32 s40, 16\n\t" \
    "s_waitcnt lgkmcnt(0)\n\t" \
    "ea_lp2_%=:\n\t" \
    "global_load_lds_dwordx4 v1, s[42:43]\n\t" \
    "v_add_u32 v1, 0x20000, v1\n\t" \
    "s_add_u32 m0, m0, 4096\n\t" \
    "s_sub_u32 s40, s40, 1\n\t" \
    "s_cmp_lg_u32 s40, 0\n\t" \
    "s_cbranch_scc1 ea_lp2_%=\n\t" \
    "ea_done1_%=:\n\t" \
    "s_waitcnt lgkmcnt(0)\n\t" \
    "ea_last_%=:\n\t" \
    ""
#define ASM_CLOB "v0", "v1", "v2", "v3", "v4", "v5", "v6", "v7", "v8", "v9", "v10", "v11", "v12", "v13", "v14", "v15", "v16", "v17", "v18", "v19", "v20", "v21", "v22", "v23", "v24", "v25", "v26", "v27", "v28", "v29", "v30", "v31", "v32", "v33", "v34", "v35", "v36", "v37", "v38", "v39", "v40", "v41", "v42", "v43", "v44", "v45", "v46", "v47", "v48", "v49", "v50", "v51", "v52", "v53", "v54", "v55", "v56", "v57", "v58", "v59", "v60", "v61", "v62", "v63", "v64", "v65", "v66", "v67", "v68", "v69", "v70", "v71", "v72", "v73", "v74", "v75", "v76", "v77", "v78", "v79", "v80", "v81", "v82", "v83", "v84", "v85", "v86", "v87", "v88", "v89", "v90", "v91", "v92", "v93", "v94", "v95", "v96", "v97", "v98", "v99", "v100", "v101", "v102", "v103", "v104", "v105", "v106", "v107", "v108", "v109", "v110", "v111", "v112", "v113", "v114", "v115", "v116", "v117", "v118", "v119", "v120", "v121", "v122", "v123", "v124", "v125", "v126", "v127", "v128", "v129", "v130", "v131", "v132", "v133", "v134", "v135", "v136", "v137", "v138", "v139", "v140", "v141", "v142", "v143", "v144", "v145", "v146", "v147", "v148", "v149", "v150", "v151", "v152", "v153", "v154", "v155", "v156", "v157", "v158", "v159", "v160", "v161", "v162", "v163", "v164", "v165", "v166", "v167", "v168", "v169", "v170", "v171", "v172", "v173", "v174", "v175", "v176", "v177", "v178", "v179", "v180", "v181", "v182", "v183", "v184", "v185", "v186", "v187", "v188", "v189", "v190", "v191", "v192", "v193", "v194", "v195", "v196", "v197", "v198", "v199", "v200", "v201", "v202", "v203", "v204", "v205", "v206", "v207", "v208", "v209", "v210", "v211", "v212", "v213", "v214", "v215", "v222", "v223", "v224", "v225", "v226", "v227", "v228", "v229", "v230", "v231", "v232", "v233", "v234", "v235", "v236", "v237", "v238", "v239", "v240", "v241", "v242", "v243", "v244", "v245", "v246", "v247", "v248", "v249", "v250", "v251", "v252", "v253", "v254", "v255", "a0", "a1", "a2", "a3", "a4", "a5", "a6", "a7", "a8", "a9", "a10", "a11", "a12", "a13", "a14", "a15", "a16", "a17", "a18", "a19", "a20", "a21", "a22", "a23", "a24", "a25", "a26", "a27", "a28", "a29", "a30", "a31", "a32", "a33", "a34", "a35", "a36", "a37", "a38", "a39", "a40", "a41", "a42", "a43", "a44", "a45", "a46", "a47", "a48", "a49", "a50", "a51", "a52", "a53", "a54", "a55", "a56", "a57", "a58", "a59", "a60", "a61", "a62", "a63", "a64", "a65", "a66", "a67", "a68", "a69", "a70", "a71", "a72", "a73", "a74", "a75", "a76", "a77", "a78", "a79", "a80", "a81", "a82", "a83", "a84", "a85", "a86", "a87", "a88", "a89", "a90", "a91", "a92", "a93", "a94", "a95", "a96", "a97", "a98", "a99", "a100", "a101", "a102", "a103", "a104", "a105", "a106", "a107", "a108", "a109", "a110", "a111", "a112", "a113", "a114", "a115", "a116", "a117", "a118", "a119", "a120", "a121", "a122", "a123", "a124", "a125", "a126", "a127", "a128", "a129", "a130", "a131", "a132", "a133", "a134", "a135", "a136", "a137", "a138", "a139", "a140", "a141", "a142", "a143", "a144", "a145", "a146", "a147", "a148", "a149", "a150", "a151", "a152", "a153", "a154", "a155", "a156", "a157", "a158", "a159", "a160", "a161", "a162", "a163", "a164", "a165", "a166", "a167", "a168", "a169", "a170", "a171", "a172", "a173", "a174", "a175", "a176", "a177", "a178", "a179", "a180", "a181", "a182", "a183", "a184", "a185", "a186", "a187", "a188", "a189", "a190", "a191", "a192", "a193", "a194", "a195", "a196", "a197", "a198", "a199", "a200", "a201", "a202", "a203", "a204", "a205", "a206", "a207", "a208", "a209", "a210", "a211", "a212", "a213", "a214", "a215", "a216", "a217", "a218", "a219", "a220", "a221", "a222", "a223", "a224", "a225", "a226", "a227", "a228", "a229", "a230", "a231", "a232", "a233", "a234", "a235", "a236", "a237", "a238", "a239", "a240", "a241", "a242", "a243", "a244", "a245", "a246", "a247", "a248", "a249", "a250", "a251", "a252", "a253", "a254", "a255", "s33", "s34", "s35", "s36", "s37", "s38", "s39", "s40", "s41", "s42", "s43", "s44", "s45", "s46", "s47", "s48", "s49", "s50", "s51", "s52", "s53", "s54", "s55", "s56", "s57", "s58", "s59", "s60", "s61", "s62", "s63", "s64", "s65", "s66", "s67", "s68", "s69", "s70", "s71", "s72", "s73", "s74", "s75", "s76", "s77", "s78", "s79", "s80", "s81", "s82", "s83", "s84", "s85", "s86", "s87", "s88", "s89", "s90", "s91", "s92", "s93", "s94", "s95", "s96", "s97", "s98", "s99", "vcc", "scc", "memory"
#define ASM_CLOBW "v32", "v33", "v34", "v35", "v36", "v37", "v38", "v39", "v40", "v41", "v42", "v43", "v44", "v45", "v46", "v47", "v48", "v49", "v50", "v51", "v52", "v53", "v54", "v55", "v56", "v57", "v58", "v59", "v60", "v61", "v62", "v63", "v64", "v65", "v66", "v67", "v68", "v69", "v70", "v71", "v72", "v73", "v74", "v75", "v76", "v77", "v78", "v79", "v80", "v81", "v82", "v83", "v84", "v85", "v86", "v87", "v88", "v89", "v90", "v91", "v92", "v93", "v94", "v95", "v96", "v97", "v98", "v99", "v100", "v101", "v102", "v103", "v104", "v105", "v106", "v107", "v108", "v109", "v110", "v111", "v112", "v113", "v114", "v115", "v116", "v117", "v118", "v119", "v120", "v121", "v122", "v123", "v124", "v125", "v126", "v127", "v128", "v129", "v130", "v131", "v132", "v133", "v134", "v135", "v136", "v137", "v138", "v139", "v140", "v141", "v142", "v143", "v144", "v145", "v146", "v147", "v148", "v149", "v150", "v151", "v152", "v153", "v154", "v155", "v156", "v157", "v158", "v159", "v160", "v161", "v162", "v163", "v164", "v165", "v166", "v167", "v168", "v169", "v170", "v171", "v172", "v173", "v174", "v175", "v176", "v177", "v178", "v179", "v180", "v181", "v182", "v183", "v184", "v185", "v186", "v187", "v188", "v189", "v190", "v191", "v192", "v193", "v194", "v195", "v196", "v197", "v198", "v199", "v222", "v223", "v224", "v225", "v226", "v227", "v228", "v229", "v230", "v231", "v232", "v233", "v234", "v235", "v236", "v237", "v238", "v239", "v240", "v241", "v242", "v243", "v244", "v245", "v246", "v247", "v248", "v249", "v250", "v251", "v252", "v253", "v254", "v255", "a0", "a1", "a2", "a3", "a4", "a5", "a6", "a7", "a8", "a9", "a10", "a11", "a12", "a13", "a14", "a15", "a16", "a17", "a18", "a19", "a20", "a21", "a22", "a23", "a24", "a25", "a26", "a27", "a28", "a29", "a30", "a31", "a32", "a33", "a34", "a35", "a36", "a37", "a38", "a39", "a40", "a41", "a42", "a43", "a44", "a45", "a46", "a47", "a48", "a49", "a50", "a51", "a52", "a53", "a54", "a55", "a56", "a57", "a58", "a59", "a60", "a61", "a62", "a63", "a64", "a65", "a66", "a67", "a68", "a69", "a70", "a71", "a72", "a73", "a74", "a75", "a76", "a77", "a78", "a79", "a80", "a81", "a82", "a83", "a84", "a85", "a86", "a87", "a88", "a89", "a90", "a91", "a92", "a93", "a94", "a95", "a96", "a97", "a98", "a99", "a100", "a101", "a102", "a103", "a104", "a105", "a106", "a107", "a108", "a109", "a110", "a111", "a112", "a113", "a114", "a115", "a116", "a117", "a118", "a119", "a120", "a121", "a122", "a123", "a124", "a125", "a126", "a127", "a128", "a129", "a130", "a131", "a132", "a133", "a134", "a135", "a136", "a137", "a138", "a139", "a140", "a141", "a142", "a143", "a144", "a145", "a146", "a147", "a148", "a149", "a150", "a151", "a152", "a153", "a154", "a155", "a156", "a157", "a158", "a159", "a160", "a161", "a162", "a163", "a164", "a165", "a166", "a167", "a168", "a169", "a170", "a171", "a172", "a173", "a174", "a175", "a176", "a177", "a178", "a179", "a180", "a181", "a182", "a183", "a184", "a185", "a186", "a187", "a188", "a189", "a190", "a191", "a192", "a193", "a194", "a195", "a196", "a197", "a198", "a199", "a200", "a201", "a202", "a203", "a204", "a205", "a206", "a207", "a208", "a209", "a210", "a211", "a212", "a213", "a214", "a215", "a216", "a217", "a218", "a219", "a220", "a221", "a222", "a223", "a224", "a225", "a226", "a227", "a228", "a229", "a230", "a231", "a232", "a233", "a234", "a235", "a236", "a237", "a238", "a239", "a240", "a241", "a242", "a243", "a244", "a245", "a246", "a247", "a248", "a249", "a250", "a251", "a252", "a253", "a254", "a255", "s33", "s34", "s35", "s36", "s37", "s38", "s39", "s40", "s41", "s42", "s43", "s44", "s45", "s46", "s47", "s48", "s49", "s50", "s51", "s52", "s53", "s54", "s55", "s56", "s57", "s58", "s59", "s60", "s61", "s62", "s63", "s64", "s65", "s66", "s67", "s68", "s69", "s70", "s71", "s72", "s73", "s74", "s75", "s76", "s77", "s78", "s79", "s80", "s81", "s82", "s83", "s84", "s85", "s86", "s87", "s88", "s89", "s90", "s91", "s92", "s93", "s94", "s95", "s96", "s97", "s98", "s99", "vcc", "scc", "memory"
#define CV_CLOB "v0", "v1", "v2", "v3", "v4", "v5", "v6", "v7", "v8", "v9", "v10", "v11", "v12", "v13", "v14", "v15", "v16", "v17", "v18", "v19", "v20", "v21", "v22", "v23", "v24", "v25", "v26", "v27", "v28", "v29", "v30", "v31", "v32", "v33", "v34", "v35", "v36", "v37", "v38", "v39", "v40", "v41", "v42", "v43", "v44", "v45", "v46", "v47", "v48", "v49", "v50", "v51", "v52", "v53", "v54", "v55", "v56", "v57", "v58", "v59", "v60", "v61", "v62", "v63", "v64", "v65", "v66", "v67", "v68", "v69", "v70", "v71", "v72", "v73", "v74", "v75", "v76", "v77", "v78", "v79", "v80", "v81", "v82", "v83", "v84", "v85", "v86", "v87", "v88", "v89", "v90", "v91", "v92", "v93", "v94", "v95", "v96", "v97", "v98", "v99", "v100", "v101", "v102", "v103", "v104", "v105", "v106", "v107", "v108", "v109", "v110", "v111", "v112", "v113", "v114", "v115", "v116", "v117", "v118", "v119", "v120", "v121", "v122", "v123", "v124", "v125", "v126", "v127", "v128", "v129", "v130", "v131", "v132", "v133", "v134", "v135", "v136", "v137", "v138", "v139", "v140", "v141", "v142", "v143", "v144", "v145", "v146", "v147", "v148", "v149", "v150", "v151", "v152", "v153", "v154", "v155", "v156", "v157", "v158", "v159", "v160", "v161", "v162", "v163", "v164", "v165", "v166", "v167", "v168", "v169", "v170", "v171", "v172", "v173", "v174", "v175", "v176", "v177", "v178", "v179", "v180", "v181", "v182", "v183", "v184", "v185", "v186", "v187", "v188", "v189", "v190", "v191", "v192", "v193", "v194", "v195", "s69", "scc", "memory"
static constexpr unsigned G_STEP = 0x10000;
static constexpr unsigned G_WS_CTL = 0x580000;
static constexpr unsigned G_WS_DONE = 0x5f0000;
static constexpr unsigned G_WS_EP = 0x5f2000;
static constexpr unsigned G_WS_STAMPS = 0xc000000;
static constexpr unsigned G_C_CAL = 0x0;
static constexpr unsigned G_C_ERR = 0x100;
static constexpr unsigned G_C_TASK = 0x200;
static constexpr unsigned G_C_GX = 0x1000;
static constexpr unsigned G_C_GV = 0x4000;
static constexpr unsigned G_C_KM = 0x8000;
static constexpr unsigned G_C_UM = 0x9000;
static constexpr unsigned G_C_PC = 0xc000;
static constexpr unsigned G_C_UT = 0xd000;
static constexpr unsigned G_C_YC = 0x10000;
static constexpr unsigned G_C_ARR = 0x11000;
static constexpr unsigned G_C_KG = 0x12000;
static constexpr unsigned G_C_GQ = 0x14000;
static constexpr unsigned G_C_RDY = 0x15000;
static constexpr unsigned G_C_GT = 0x18000;
static constexpr unsigned G_C_GO = 0x11800;
static constexpr unsigned G_C_RW = 0xf000;
static constexpr unsigned G_WS_H = 0x0;
static constexpr unsigned G_HS8 = 0x80000;
static constexpr unsigned G_HS4 = 0x40000;
static constexpr unsigned G_UXS = 0x200000;
static constexpr unsigned G_UMS = 0x90000;
static constexpr unsigned G_PROBE = 0x1bd400;
static constexpr unsigned G_D_QX = 0x600000;
static constexpr unsigned G_D_KX = 0x1600000;
static constexpr unsigned G_D_KM = 0x2600000;
static constexpr unsigned G_D_UX = 0x2e00000;
static constexpr unsigned G_D_UM = 0x7600000;
static constexpr unsigned G_D_PX = 0xa200000;
static constexpr unsigned G_D_PM = 0xa600000;
static constexpr unsigned G_D_KT = 0xa700000;
static constexpr unsigned G_D_UT = 0xac00000;
static constexpr unsigned G_D_YB = 0xaf00000;
static constexpr unsigned G_D_END = 0xb200000;
static constexpr unsigned G_SKM = 0x7;
static constexpr unsigned G_S1 = 0x18;
static constexpr unsigned G_S0 = 0x6;
static constexpr unsigned G_GVN = 0x2;
static constexpr unsigned G_STG = 0x9000;
static constexpr unsigned G_KROW = 0x30;
static constexpr unsigned G_NHL = 0x1;
#define FLG(x_) (kDefault[5])
#define FLH(x_) (kDefault[7])
static constexpr int kDefault[8] = {PRM_DEFAULT};
#define MFMA32(a_, b_, c_) __builtin_amdgcn_mfma_f32_16x16x32_bf16(__builtin_bit_cast(bf16x8, a_), __builtin_bit_cast(bf16x8, b_), c_, 0, 0, 0)
#define MFMA16(a_, b_, c_) __builtin_amdgcn_mfma_f32_16x16x16bf16_1k(__builtin_bit_cast(s16x4, a_), __builtin_bit_cast(s16x4, b_), c_, 0, 0, 0)
#define WLD8(d_, voff_, base_, off_) asm volatile("s_nop 4\n\tglobal_load_dwordx2 %0, %1, %2 offset:%3 sc1" : "=v"(d_) : "v"(voff_), "s"(base_), "i"(off_) : "memory")
#define WLD16(d_, voff_, base_, off_) asm volatile("s_nop 4\n\tglobal_load_dwordx4 %0, %1, %2 offset:%3 sc1" : "=v"(d_) : "v"(voff_), "s"(base_), "i"(off_) : "memory")
#define WST16(voff_, d_, base_, off_) asm volatile("s_nop 4\n\tglobal_store_dwordx4 %0, %1, %2 offset:%3 sc1\n\ts_nop 1" :: "v"(voff_), "v"(d_), "s"(base_), "i"(off_) : "memory")
#define OMOD ""
#define OST8(p_, d_) asm volatile("global_store_dwordx2 %0, %1, off " OMOD :: "v"(p_), "v"(d_) : "memory")
#define OST16(p_, d_) asm volatile("global_store_dwordx4 %0, %1, off " OMOD "\n\ts_nop 1" :: "v"(p_), "v"(d_) : "memory")
#define WLD16X4(d0, d1, d2, d3, voff_, base_) asm volatile("s_nop 4\n\tglobal_load_dwordx4 %0, %4, %5 sc1\n\tglobal_load_dwordx4 %1, %4, %5 offset:1024 sc1\n\t" \
    "global_load_dwordx4 %2, %4, %5 offset:2048 sc1\n\tglobal_load_dwordx4 %3, %4, %5 offset:3072 sc1" \
    : "=&v"(d0), "=&v"(d1), "=&v"(d2), "=&v"(d3) : "v"(voff_), "s"(base_) : "memory")

__device__ __forceinline__ u32x4 pack8(const f32x4& a, const f32x4& b) {
    return u32x4{pk2bf(a[0], a[1]), pk2bf(a[2], a[3]), pk2bf(b[0], b[1]), pk2bf(b[2], b[3])};
}
template <class T> __device__ __forceinline__ T* uni(T* q) {
    const u64 v = (u64)q;
    unsigned lo = __builtin_amdgcn_readfirstlane((unsigned)v), hi = __builtin_amdgcn_readfirstlane((unsigned)(v >> 32));
    asm volatile("s_nop 4" : "+s"(lo), "+s"(hi));
    return (T*)(((u64)hi << 32) | lo);
}
template <bool FAST = false>
__device__ __forceinline__ void wait_word(const MainP& p, const char* f, int want, unsigned code) {
    unsigned tries = 0;
#pragma unroll 1
    while (__any((int)__hip_atomic_load((const gu32*)f, RLX_AGENT) < want)) {
        if (++tries > kWaitCap) { __hip_atomic_store((gu32*)(p.ws + G_WS_CTL + G_C_ERR), code, RLX_AGENT); break; }
        if ((tries & 1023u) == 0u && __hip_atomic_load((const gu32*)(p.ws + G_WS_CTL + G_C_ERR), RLX_AGENT) != 0xffffffffu) break;
        if (FAST) __builtin_amdgcn_s_sleep(1); else __builtin_amdgcn_s_sleep(10);
    }
}

__device__ __forceinline__ int skew(int s, int id) { return (s ^ (G_SKM * id)) & 63; }
__device__ __forceinline__ unsigned um_base(int s, unsigned nm) { return G_D_UM + (s / 3) * 0x200000 + (s % 3) * G_UMS + ((nm >> (2 + s / 3)) & 1) * 8192; }
typedef GAS unsigned long long gu64;
#define IST(k_, slot_)
__device__ __forceinline__ void init_done(char* ws, int slot, unsigned val) {
    const unsigned e = __hip_atomic_load((const gu32*)(ws + G_WS_EP), RLX_AGENT);
    __hip_atomic_store((gu64*)(ws + G_WS_DONE + slot * 64), ((u64)val << 32) | (e + 1u), RLX_AGENT);
}
__device__ __forceinline__ void init_wait(char* ws, int w, int lane) {
    if (w == 0) {
        const unsigned e = __hip_atomic_load((const gu32*)(ws + G_WS_EP), RLX_AGENT);
        for (unsigned tries = 0; tries < kWaitCap; ++tries) {
            const unsigned d0 = __hip_atomic_load((const gu32*)(ws + G_WS_DONE + lane * 64), RLX_AGENT), d1 = __hip_atomic_load((const gu32*)(ws + G_WS_DONE + 2048 + lane * 64), RLX_AGENT);
            if (!__any((d0 != e + 1u) | (d1 != e + 1u))) break;
            __builtin_amdgcn_s_sleep(8);
        }
    }
    __syncthreads();
    if (w == 0 && lane == 0) __hip_atomic_fetch_add((gu32*)(ws + G_WS_CTL + G_C_ARR), 1u, RLX_AGENT);
}
__device__ __forceinline__ void fill_block(char* ws, int b, int tid, const u32x4& v0, const u32x4& v1) {
    const unsigned o = b * 8192 + tid * 16, o2 = o + 4096;
    asm volatile("global_store_dwordx4 %0, %1, %2 sc1\n\ts_nop 1" :: "v"(o), "v"(v0), "s"(ws) : "memory");
    if (b >= 512 || (b & 6) != 6 || tid < 64)
        asm volatile("global_store_dwordx4 %0, %1, %2 sc1\n\ts_nop 1" :: "v"(o2), "v"(v1), "s"(ws) : "memory");
}
__device__ __forceinline__ void init_fill(char* ws, const u16* mem0, int f, int tid) {
    const u32x4 sent = {0xffffffffu, 0xffffffffu, 0xffffffffu, 0xffffffffu};
    u32x4 v0 = sent, v1 = sent;
    if (f < 32 && (f & 4)) {
#pragma unroll
        for (int k = 0; k < 2; ++k) {
            const int q = tid + 256 * k + ((f & 2) ? 256 : 0), m = q & 15;
            if (!(f & 2) || (q >= 512 && q < 576)) {
                const unsigned* src = (const unsigned*)(mem0 + (2 * (m >> 2) + (m & 1)) * DM + 16 * (q >> 4) + (m & 2));
                const u32x4 o = {min(src[0], 0xfffffffeu), min(src[2], 0xfffffffeu), min(src[4], 0xfffffffeu), min(src[6], 0xfffffffeu)};
                if (k) v1 = o; else v0 = o;
            }
        }
    }
#pragma unroll 1
    for (int q = f; q < 182; q += 48) {
        if (q >= 136 && q < 152 && (tid & 127)) { v0 = u32x4{0u, 0u, 0u, 0u}; v1 = v0; }
        fill_block(ws, q < 64 ? q : q < 136 ? q - 64 + G_D_UM / 8192 : q < 168 ? 256 + ((q - 136) >> 2) * 8 + (q & 3) : q - 168 + G_WS_CTL / 8192, tid, v0, v1);
        v0 = sent; v1 = sent;
    }
    const unsigned e = __hip_atomic_load((const gu32*)(ws + G_WS_EP), RLX_AGENT);
    asm volatile("s_waitcnt vmcnt(0)" :: "v"(e) : "memory");
    __syncthreads();
    if (tid == 0) __hip_atomic_store((gu64*)(ws + G_WS_DONE + (48 + f) * 64), (u64)(e + 1u), RLX_AGENT);
    if (tid == 0) IST(0, 48 + f);
}
__device__ __forceinline__ void init_cal(char* ws, int id, int tid, int n) {
    if (tid < 64) {
        const int pg = id >> 1;
        char* a = ws + (pg < 2 ? pg * 0x200000u : G_D_UM + (pg - 2) * 0x200000u) + G_PROBE + (id & 1) * 8192;
        const u32x4 z = {0u, 0u, 0u, 0u};
        asm volatile("global_store_dwordx4 %0, %1, off sc1\n\tglobal_store_dwordx4 %0, %1, off offset:1024 sc1\n\ts_waitcnt vmcnt(0)" :: "v"(a + tid * 16), "v"(z) : "memory");
        if (tid == 0) {
            unsigned best = 0xffffu;
            const unsigned ts1 = (unsigned)__builtin_amdgcn_s_memrealtime() << 4;
#pragma unroll 1
            for (int it = 0; it < n; ++it) {
                const unsigned c0 = (unsigned)__builtin_amdgcn_s_memtime();
                const unsigned v = __hip_atomic_load((const gu32*)(a + it * 128), RLX_AGENT);
                asm volatile("s_waitcnt vmcnt(0)" :: "v"(v) : "memory");
                best = min(best, (unsigned)__builtin_amdgcn_s_memtime() - c0);
            }
            init_done(ws, id, best);
            IST(0, id);
        }
    }
    __syncthreads();
}
__device__ __forceinline__ unsigned near_mask(char* ws, int lane) {
    const gu32* q = (const gu32*)(ws + G_WS_DONE + (lane & 31) * 128 + 4);
    return (unsigned)__ballot(__hip_atomic_load(q + 16, RLX_AGENT) < __hip_atomic_load(q, RLX_AGENT));
}

#define TST(slot_, i_)
#define PIN8(a) asm volatile("" : "+v"(a[0]), "+v"(a[1]), "+v"(a[2]), "+v"(a[3]), "+v"(a[4]), "+v"(a[5]), "+v"(a[6]), "+v"(a[7]))
struct AOps { u32x4 q[4], k[8]; int fl; };
struct APtr { const char* q; const char* k; const char* m; unsigned voff, vkm, voffm, vfl; };
__device__ __forceinline__ void attn_batch(const MainP& p, AOps& o, const APtr& a) {
    const char* q2 = a.q + 4096; const char* k2 = a.k + 4096;
    WLD16(o.q[0], a.voff, a.q, 0); WLD16(o.q[1], a.voff, a.q, 2048); WLD16(o.q[2], a.voff, q2, 0); WLD16(o.q[3], a.voff, q2, 2048);
    WLD16X4(o.k[0], o.k[1], o.k[2], o.k[3], a.voff, a.k); WLD16X4(o.k[4], o.k[5], o.k[6], o.k[7], a.voff, k2);
    asm volatile("global_load_dword %0, %1, %2 sc1" : "=v"(o.fl) : "v"(a.vfl), "s"(p.ws) : "memory");
}
__device__ __forceinline__ void attn_pin(AOps& o) {
    asm volatile("" : "+v"(o.fl), "+v"(o.q[0]), "+v"(o.q[1]), "+v"(o.q[2]), "+v"(o.q[3])); PIN8(o.k);
}
static constexpr int NFINE = 6;
static constexpr int NG1 = 64 - NFINE;
__device__ __forceinline__ void score_round4(float* r, const f32x4& x) {
    asm volatile("v_cvt_pk_bf16_f32 %0, 0, %4\n\tv_cvt_pk_bf16_f32 %1, 0, %5\n\tv_cvt_pk_bf16_f32 %2, 0, %6\n\tv_cvt_pk_bf16_f32 %3, 0, %7\n\t"
                 "v_mul_f32 %0, 0x3db504f3, %0\n\tv_mul_f32 %1, 0x3db504f3, %1\n\tv_mul_f32 %2, 0x3db504f3, %2\n\tv_mul_f32 %3, 0x3db504f3, %3\n\t"
                 "v_cvt_pk_bf16_f32 %0, 0, %0\n\tv_cvt_pk_bf16_f32 %1, 0, %1\n\tv_cvt_pk_bf16_f32 %2, 0, %2\n\tv_cvt_pk_bf16_f32 %3, 0, %3"
                 : "=&v"(r[0]), "=&v"(r[1]), "=&v"(r[2]), "=&v"(r[3]) : "v"(x[0]), "v"(x[1]), "v"(x[2]), "v"(x[3]));
}
__device__ __forceinline__ float vmax3(float a, float b, float c) { float r; asm("v_max3_f32 %0, %1, %2, %3" : "=v"(r) : "v"(a), "v"(b), "v"(c)); return r; }
template <int N>
__device__ __forceinline__ void attn_soft(float (&q)[12], u32x2& Pm, u32x4& Px) {
    float m = vmax3(q[0], q[1], q[2]);
#pragma unroll
    for (int i = 3; i < N; i += 2) m = vmax3(m, q[i], q[i + 1 < N ? i + 1 : i]);
    { auto a = __builtin_amdgcn_permlane16_swap(__float_as_uint(m), __float_as_uint(m), false, false); m = vmax3(m, __uint_as_float(a[0]), __uint_as_float(a[1])); }
    { auto a = __builtin_amdgcn_permlane32_swap(__float_as_uint(m), __float_as_uint(m), false, false); m = vmax3(m, __uint_as_float(a[0]), __uint_as_float(a[1])); }
    const float ml = -m * 1.4426950408889634f;
#pragma unroll
    for (int i = 0; i < N; ++i) q[i] = __builtin_amdgcn_exp2f(fmaf(q[i], 1.4426950408889634f, ml));
    float sum = 0.5f * ((q[0] + q[1]) + (q[2] + q[3])) + ((q[4] + q[5]) + (q[6] + q[7]));
    if (N > 8) sum += (q[8] + q[9]) + (q[10] + q[11]);
    sum = rowsum4(sum);
    const float inv = __builtin_amdgcn_rcpf(sum);
    Pm = u32x2{pk2bf(q[0] * inv, q[1] * inv), pk2bf(q[2] * inv, q[3] * inv)};
    Px = u32x4{pk2bf(q[4] * inv, q[5] * inv), pk2bf(q[6] * inv, q[7] * inv), N > 8 ? pk2bf(q[8] * inv, q[9] * inv) : 0u, N > 8 ? pk2bf(q[10] * inv, q[11] * inv) : 0u};
}
#define NASPLIT 12
#define HSL 1
#define NAG 2
static constexpr int NA = NASPLIT;
__device__ __forceinline__ int attn_next(int s) { return s < NA ? s + 2 : s + NAG; }
__device__ __forceinline__ unsigned attn_flag(int s, int hq) { return G_WS_CTL + G_C_PC + (s < NA ? s & 1 : (s - NA) % NAG) * 1024 + hq * 128; }
struct BOps { u32x4 q[8], k[8]; };
#define WLD16X4A(d0, d1, d2, d3, voff_, base_) asm volatile("s_nop 4\n\tglobal_load_dwordx4 %0, %4, %5 sc1\n\tglobal_load_dwordx4 %1, %4, %5 offset:1024 sc1\n\t" \
    "global_load_dwordx4 %2, %4, %5 offset:2048 sc1\n\tglobal_load_dwordx4 %3, %4, %5 offset:3072 sc1" \
    : "=&a"(d0), "=&a"(d1), "=&a"(d2), "=&a"(d3) : "v"(voff_), "s"(base_) : "memory")
#define PIN8A(a) asm volatile("" : "+a"(a[0]), "+a"(a[1]), "+a"(a[2]), "+a"(a[3]), "+a"(a[4]), "+a"(a[5]), "+a"(a[6]), "+a"(a[7]))
#define MFMA_AV0(acc, a_, b_) asm volatile("v_mfma_f32_16x16x32_bf16 %0, %1, %2, 0" : "=&v"(acc) : "a"(a_), "v"(b_))
#define MFMA_AV(acc, a_, b_) asm volatile("v_mfma_f32_16x16x32_bf16 %0, %1, %2, %0" : "+v"(acc) : "a"(a_), "v"(b_))
__device__ __forceinline__ void attn_batch8(const MainP& p, BOps& o, const APtr& a) {
    const char* q2 = a.q + 4096; const char* k2 = a.k + 4096;
    WLD16X4(o.q[0], o.q[1], o.q[2], o.q[3], a.voff, a.q); WLD16X4(o.q[4], o.q[5], o.q[6], o.q[7], a.voff, q2);
    WLD16X4A(o.k[0], o.k[1], o.k[2], o.k[3], a.voff, a.k); WLD16X4A(o.k[4], o.k[5], o.k[6], o.k[7], a.voff, k2);
}
__device__ __forceinline__ void attn_step(const MainP& p, BOps& o, BOps& n1, BOps& n2, APtr& a, int& kg, int& kk, int s, int h, int w, int lane) {
    const int g = lane >> 4, c = lane & 15;
    const float scale = 0.08838834764831845f;
    TST(s * 64 + h * 2, 0);
    const int s2 = min(attn_next(attn_next(s)), NSEG - 1);
    if (kg < s2 || kk < s) {
        VMC0();
        if (s < 50) wait_word(p, p.ws + a.vfl, (lane & 1) ? s : s2, 13u); else wait_word<true>(p, p.ws + a.vfl, (lane & 1) ? s : s2, 13u);
    }
    TST(s * 64 + h * 2, 1);
    a.q = p.ws + G_D_QX + (size_t)h * G_HS8 + skew(s2, h) * 8192; a.k = a.q + (G_D_KX - G_D_QX); a.m = p.ws + G_D_KM + (size_t)h * G_HS4 + skew(s, h) * 4096;
    __builtin_amdgcn_sched_barrier(0);
    u32x4 km[4];
    int fl;
    WLD16X4A(km[0], km[1], km[2], km[3], a.vkm, a.m);
    asm volatile("global_load_dword %0, %1, %2 sc1" : "=v"(fl) : "v"(a.vfl), "s"(p.ws) : "memory");
    attn_batch8(p, n2, a);
    f32x4 y[5];
#pragma unroll
    for (int wc = 0; wc < 4; ++wc)
#pragma unroll
        for (int i = 0; i < 3; ++i) { if (wc == 0) MFMA_AV0(y[i], o.k[i >> 1], o.q[(i + 1) >> 1]); else MFMA_AV(y[i], o.k[wc * 2 + (i >> 1)], o.q[wc * 2 + ((i + 1) >> 1)]); }
    asm volatile("s_nop 7" : "+v"(y[0]), "+v"(y[1]), "+v"(y[2]));
    float q0[12], q1[12];
    score_round4(q0 + 4, y[0]); score_round4(q1 + 4, y[1]); score_round4(q1 + 8, y[2]);
#pragma unroll
    for (int i = 0; i < 4; ++i) {
        const bool vis = 4 * g + i <= c;
        q0[4 + i] = vis ? q0[4 + i] : -INFINITY; q1[8 + i] = vis ? q1[8 + i] : -INFINITY;
    }
    __builtin_amdgcn_sched_barrier(0);
    asm volatile("s_waitcnt vmcnt(16)\n\ts_barrier" : "+a"(km[0]), "+a"(km[1]), "+a"(km[2]), "+a"(km[3]), "+v"(fl) :: "memory");
    PIN8(n1.q); PIN8A(n1.k);
    { const unsigned fo = attn_flag(s, h >> 2); const int fv = s - 1;
      asm volatile("s_cmp_lg_u32 %3, 0\n\ts_cbranch_scc1 1f\n\tglobal_store_dword %0, %1, %2 sc1\n1:" :: "v"(fo), "v"(fv), "s"(p.ws), "s"(w) : "scc", "memory"); }
#pragma unroll
    for (int wc = 0; wc < 4; ++wc)
#pragma unroll
        for (int i = 0; i < 2; ++i) { if (wc == 0) MFMA_AV0(y[3 + i], km[0], o.q[i]); else MFMA_AV(y[3 + i], km[wc], o.q[wc * 2 + i]); }
    asm volatile("s_nop 7" : "+v"(y[3]), "+v"(y[4]));
    kg = __builtin_amdgcn_readlane(fl, 0); kk = __builtin_amdgcn_readlane(fl, 1);
    score_round4(q0, y[3]); score_round4(q1, y[4]);
    u32x2 Pm0, Pm1; u32x4 Px0, Px1;
    attn_soft<8>(q0, Pm0, Px0); attn_soft<12>(q1, Pm1, Px1);
    char* pxb = p.ws + G_D_PX + (size_t)(s * 32 + h) * 2048;
    char* pmb = p.ws + G_D_PM + (size_t)s * 16384 + h * 256; char* pm2 = pmb + 8192;
    WST16(a.voff, Px0, pxb, 0); WST16(a.voff, Px1, pxb, 1024);
    asm volatile("global_store_dwordx2 %0, %1, %3 sc1\n\tglobal_store_dwordx2 %0, %2, %4 sc1" :: "v"(a.voffm), "v"(Pm0), "v"(Pm1), "s"(pmb), "s"(pm2) : "memory");
    { const unsigned fo = attn_flag(s, h >> 2);
      asm volatile("s_cmp_lt_u32 %4, 46\n\ts_cbranch_scc1 1f\n\ts_waitcnt vmcnt(0)\n\ts_barrier\n\ts_cmp_lg_u32 %3, 0\n\ts_cbranch_scc1 1f\n\tglobal_store_dword %0, %1, %2 sc1\n1:"
                   :: "v"(fo), "v"(s), "s"(p.ws), "s"(w), "s"(s) : "scc", "memory"); }
    TST(s * 64 + h * 2, 2);
}
__device__ __forceinline__ void attn_role(const MainP& p, int s, int h, int w, int lane) {
    APtr a;
    a.q = p.ws + G_D_QX + (size_t)h * G_HS8 + skew(s, h) * 8192; a.k = a.q + (G_D_KX - G_D_QX);
    a.voff = lane * 16; a.vkm = (lane & 55) * 16; a.voffm = (lane & 15) * 16 + ((lane >> 4) & 1) * 8;
    a.vfl = G_WS_CTL + ((lane & 1) ? G_C_KM : G_C_GQ) + h * 128;
    BOps o0, o1, o2;
    wait_word(p, p.ws + G_WS_CTL + G_C_GQ + h * 128, attn_next(s), 10u);
    attn_batch8(p, o0, a);
    a.q = p.ws + G_D_QX + (size_t)h * G_HS8 + skew(attn_next(s), h) * 8192; a.k = a.q + (G_D_KX - G_D_QX);
    attn_batch8(p, o1, a);
    VMC0();
    PIN8(o0.q); PIN8A(o0.k); PIN8(o1.q); PIN8A(o1.k);
    int kg = -1, kk = -1;
    static_assert(NA % 2 == 0, "ranges");
#pragma unroll 1
    for (;;) {
        attn_step(p, o0, o1, o2, a, kg, kk, s, h, w, lane);
        if ((s = attn_next(s)) >= NG1) break;
        attn_step(p, o1, o2, o0, a, kg, kk, s, h, w, lane);
        if ((s = attn_next(s)) >= NG1) break;
        attn_step(p, o2, o0, o1, a, kg, kk, s, h, w, lane);
        if ((s = attn_next(s)) >= NG1) break;
    }
    VMC0();
}
__device__ __forceinline__ unsigned umax4(const u32x4& v) { return max(max(v[0], v[1]), max(v[2], v[3])); }
__device__ __forceinline__ bool give_up(const MainP& p, unsigned& tries, unsigned code) {
    if (++tries <= kWaitCap / 4) return false;
    __hip_atomic_store((gu32*)(p.ws + G_WS_CTL + G_C_ERR), code, RLX_AGENT);
    return true;
}
__device__ __forceinline__ void task_attn_tail(const MainP& p, int s, int h, int w, int lane) {
    const int g = lane >> 4, c = lane & 15, qt = w & 1;
    const float scale = 0.08838834764831845f;
    APtr a;
    a.q = p.ws + G_D_QX + (size_t)h * G_HS8 + skew(s, h) * 8192 + qt * 1024; a.k = p.ws + G_D_KX + (size_t)h * G_HS8 + skew(s, h) * 8192; a.m = p.ws + G_D_KM + (size_t)h * G_HS4 + skew(s, h) * 4096;
    a.voff = lane * 16; a.vkm = (lane & 55) * 16; a.voffm = c * 16 + (g & 1) * 8; a.vfl = G_WS_CTL;
    AOps o;
    wait_word(p, p.ws + G_WS_CTL + G_C_GQ + h * 128, s, 14u);
    attn_batch(p, o, a);
    VMC0();
    attn_pin(o);
    float q[12];
    u32x4 km[4];
#pragma unroll
    for (int kt = 1; kt >= -1; --kt) {
        if (kt < 0) {
            TST(s * 64 + h * 2 + qt, 0);
            wait_word(p, p.ws + G_WS_CTL + G_C_KM + h * 128, s - 2, 15u);
            if (FLG(p.prm[5]) & 2) wait_word<true>(p, p.ws + G_WS_CTL + G_C_KM + h * 128, s - 1, 15u);
            for (unsigned tries = 0;;) {
                WLD16X4(km[0], km[1], km[2], km[3], a.vkm, a.m);
                asm volatile("s_waitcnt vmcnt(0)" : "+v"(km[0]), "+v"(km[1]), "+v"(km[2]), "+v"(km[3]) :: "memory");
                if (!__any(max(max(umax4(km[0]), umax4(km[1])), max(umax4(km[2]), umax4(km[3]))) == 0xffffffffu) || give_up(p, tries, 16u)) break;
            }
            TST(s * 64 + h * 2 + qt, 1);
        }
        f32x4 y = {0.f, 0.f, 0.f, 0.f};
#pragma unroll
        for (int wc = 0; wc < 4; ++wc) y = MFMA32(kt < 0 ? km[wc] : o.k[wc * 2 + (kt < 0 ? 0 : kt)], o.q[wc], y);
#pragma unroll
        for (int i = 0; i < 4; ++i) q[4 + 4 * kt + i] = (16 * kt + 4 * g + i <= 16 * qt + c) ? bfr(bfr(y[i]) * scale) : -INFINITY;
    }
    u32x2 Pm; u32x4 Px;
    attn_soft<12>(q, Pm, Px);
    char* pxb = p.ws + G_D_PX + (size_t)(s * 32 + h) * 2048 + qt * 1024;
    char* pmb = p.ws + G_D_PM + (size_t)s * 16384 + h * 256 + qt * 8192;
    WST16(a.voff, Px, pxb, 0);
    asm volatile("global_store_dwordx2 %0, %1, %2 sc1" :: "v"(a.voffm), "v"(Pm), "s"(pmb) : "memory");
    TST(s * 64 + h * 2 + qt, 2);
}

#define RLD(d_, voff_, base_, off_) asm volatile("global_load_dwordx4 %0, %1, %2 offset:%3 sc1" : "=v"(d_) : "v"(voff_), "s"(base_), "i"(off_) : "memory")
#define RLDA(d_, voff_, base_, off_) asm volatile("global_load_dwordx4 %0, %1, %2 offset:%3 sc1" : "=a"(d_) : "v"(voff_), "s"(base_), "i"(off_) : "memory")
#define MFMA_ACC_A(acc, a_, b_) asm volatile("v_mfma_f32_16x16x32_bf16 %0, %1, %2, %0" : "+v"(acc) : "a"(a_), "v"(b_))
#define VMW(n_) asm volatile("s_waitcnt vmcnt(%0)" :: "i"(n_) : "memory")
template <int NT, int DEPTH, bool TAIL>
__device__ __forceinline__ void task_out(const MainP& p, char* smem, int s, int ct0, int w, int lane) {
    const int g = lane >> 4, c = lane & 15;
    const unsigned voff = lane * 16;
    f32x4 acc[NT][2];
#pragma unroll
    for (int t = 0; t < NT; ++t) { acc[t][0] = f32x4{0.f, 0.f, 0.f, 0.f}; acc[t][1] = acc[t][0]; asm volatile("" : "+v"(acc[t][0]), "+v"(acc[t][1])); }
    const char* ub[NT];
#pragma unroll
    for (int t = 0; t < NT; ++t) ub[t] = uni(p.ws + G_D_UX + (size_t)(ct0 + t) * G_UXS + skew(s, ct0 + t) * 32768 + w * 8192);
    const char* pb = uni(p.ws + G_D_PX + (size_t)(s * 32 + w * 8) * 2048);
    const char* um = uni(p.ws + um_base(s, p.prm[0]) + ct0 * 16384);
    unsigned vu[NT];
#pragma unroll
    for (int t = 0; t < NT; ++t) { vu[t] = t * 16384 + w * 2048 + voff; asm volatile("" : "+v"(vu[t])); }
    const char* pm = uni(p.ws + G_D_PM + (size_t)s * 16384 + w * 2048);
    constexpr int NR = DEPTH + 1 > 10 ? 10 : DEPTH + 1;
    u32x4 a[NR][NT], b[10][2];
    unsigned vb = voff, va = voff;
    TST(4096 + (s * 36 + ct0) * 4 + w, 0);
    const int f0 = (int)__hip_atomic_load((const gu32*)(p.ws + G_WS_CTL + (lane < 8 ? G_C_UM + (w * 8 + lane) * 128 : lane < 16 ? G_C_GV + ((s < (int)G_S1 ? s & 1 : 0) * 32 + w * 8 + lane - 8) * 128
                                                                                                                                : attn_flag(s, w * 2 + (lane & 1)) - G_WS_CTL)), RLX_AGENT);
    const bool early = !TAIL && !__any(f0 < s);
    if (!early) wait_word(p, p.ws + G_WS_CTL + (lane < 8 ? G_C_GV + ((s < (int)G_S1 ? s & 1 : 0) * 32 + w * 8 + lane) * 128 : G_C_UM + w * 8 * 128), lane < 8 ? s : s - 3, 21u);
#define ALOAD(q) do { if ((q) < 8) { _Pragma("unroll") for (int t = 0; t < NT; ++t) RLDA(a[(q) % NR][t], va, ub[t], 0);  \
                                     asm volatile("v_add_u32 %0, 0x400, %0" : "+v"(va)); }                                            \
                      else { _Pragma("unroll") for (int t = 0; t < NT; ++t) RLDA(a[(q) % NR][t], vu[t], um, ((q) & 1) * 1024); } } while (0)
    constexpr int D0 = DEPTH < 8 ? DEPTH : 8;
    asm volatile("s_nop 4");
#pragma unroll
    for (int q = 0; q < D0; ++q) ALOAD(q);
    asm volatile("s_cmp_lg_u32 %0, 0\n\ts_cbranch_scc1 1f\n\ts_waitcnt vmcnt(0)\n1:\n\ts_waitcnt vmcnt(30)" :: "s"(__builtin_amdgcn_readfirstlane((int)early)) : "scc", "memory");
    unsigned tries = 0;
    if (TAIL) {
        const char* sp = pm + (lane & 7) * 256 + ((lane >> 3) & 1) * 8192;
        const int dl = (FLG(p.prm[5]) >> 26) & 31;
        if (dl) {
            wait_word<true>(p, p.ws + G_WS_CTL + G_C_KM + ((ct0 >> 1) + w) * 128, s - 1, 25u);
            for (int i = 0; i < dl + (s < 63 ? 5 : 0); ++i) __builtin_amdgcn_s_sleep(2);
        } else
        while (__any(__hip_atomic_load((const gu32*)sp, RLX_AGENT) == 0xffffffffu) && !give_up(p, tries, 23u)) __builtin_amdgcn_s_sleep(1);
    } else if (!early) wait_word(p, p.ws + G_WS_CTL + (lane < 8 ? G_C_UM + (w * 8 + lane) * 128 : attn_flag(s, w * 2 + (lane & 1)) - G_WS_CTL), s, 22u);
    if (!early) TST(4096 + (s * 36 + ct0) * 4 + w, 1);
    for (;;) {
    asm volatile("s_nop 4");
#pragma unroll
    for (int q = 0; q < 8; ++q) { RLD(b[q][0], vb, pb, 0); RLD(b[q][1], vb, pb, 1024); asm volatile("v_add_u32 %0, 0x800, %0" : "+v"(vb)); }
#pragma unroll
    for (int q = 8; q < 10; ++q) { RLD(b[q][0], voff, pm, (q & 1) * 1024); RLD(b[q][1], voff, pm + 8192, (q & 1) * 1024); }
#pragma unroll
    for (int q = D0; q < NR; ++q) ALOAD(q);
#pragma unroll
    for (int q = 0; q < 10; ++q) {
        const int issuedA = (NR + q < 10 ? NR + q : 10) - D0;
        if (q < D0) VMW(2 * (9 - q) + issuedA * NT); else VMW((issuedA - (q - D0 + 1)) * NT);
#pragma unroll
        for (int t = 0; t < NT; ++t) { MFMA_ACC_A(acc[t][0], a[q % NR][t], b[q][0]); MFMA_ACC_A(acc[t][1], a[q % NR][t], b[q][1]); }
        if (NR + q < 10) ALOAD(NR + q);
    }
    asm volatile("s_nop 7" ::: "memory");
#pragma unroll
    for (int t = 0; t < NT; ++t) asm volatile("" : "+v"(acc[t][0]), "+v"(acc[t][1]));
    if (!TAIL) break;
    f32x4 chk = acc[0][0];
#pragma unroll
    for (int t = 0; t < NT; ++t) chk += acc[t][1] + acc[t][0];
    const float ck = (chk[0] + chk[1]) + (chk[2] + chk[3]);
    if (!__any(ck != ck)) break;
    unsigned mx = 0;
#pragma unroll
    for (int q = 0; q < 10; ++q) { asm volatile("" : "+v"(b[q][0]), "+v"(b[q][1])); mx = max(mx, max(umax4(b[q][0]), umax4(b[q][1]))); }
#pragma unroll
    for (int t = 0; t < NT; ++t) { asm volatile("" : "+a"(a[8 % NR][t]), "+a"(a[9 % NR][t])); mx = max(mx, max(umax4(a[8 % NR][t]), umax4(a[9 % NR][t]))); }
    if (!__any(mx == 0xffffffffu) || give_up(p, tries, 24u)) break;
    vb = voff;
#pragma unroll
    for (int t = 0; t < NT; ++t) { acc[t][0] = f32x4{0.f, 0.f, 0.f, 0.f}; acc[t][1] = acc[t][0]; asm volatile("" : "+v"(acc[t][0]), "+v"(acc[t][1])); }
    }
    TST(4096 + (s * 36 + ct0) * 4 + w, 2);
    f32x4* red = (f32x4*)smem;
#pragma unroll
    for (int t = 0; t < NT; ++t) { red[((w * NT + t) * 2 + 0) * 64 + lane] = acc[t][0]; red[((w * NT + t) * 2 + 1) * 64 + lane] = acc[t][1]; }
    __syncthreads();
    u16* orow = p.out + (size_t)(32 * s + 16 * (w & 1) + c) * OUTW + 16 * ct0 + 4 * g;
#pragma unroll
    for (int t = 0; t < NT; ++t)
        if ((t & 1) == (w >> 1)) {
            const int m = w & 1;
            const f32x4 v = (red[((0 * NT + t) * 2 + m) * 64 + lane] + red[((1 * NT + t) * 2 + m) * 64 + lane]) +
                            (red[((2 * NT + t) * 2 + m) * 64 + lane] + red[((3 * NT + t) * 2 + m) * 64 + lane]);
            OST8(orow + 16 * t, (u32x2{pk2bf(v[0], v[1]), pk2bf(v[2], v[3])}));
        }
}

__device__ __forceinline__ int index_of_pos(int pv) { return min(min(pv, 128 + (pv >> 1)), min(256 + (pv >> 2), 384 + (pv >> 3))); }
static constexpr int KROW = G_KROW;
static constexpr int KARR = 80 * KROW;
static constexpr int UCS = 2208;
struct BCh {
    u16 uc[4][UCS];
    f32x4 cst[NST];
    unsigned Kd[648];
    u16 Kh[KARR];
    u16 Kl[KARR];
};
static_assert(4 * sizeof(BCh) <= LDS_TOTAL - 32, "LDS");
__device__ __forceinline__ unsigned prm(unsigned hi, unsigned lo, unsigned sel) { return __builtin_amdgcn_perm(hi, lo, sel); }
#define BST(i) asm volatile("" ::: "memory")
#define UT_LOAD(p_) __builtin_nontemporal_load(p_)
template <class F>
__device__ __forceinline__ void task_ut(const MainP& p, char* smem, int rb, int w, int lane, F more) {
    const int tid = w * 64 + lane, g = lane >> 4, c = lane & 15;
    { char* ws = p.ws; if (tid == 0) IST(4, rb); }
    const u32x4* src = (const u32x4*)(p.U + (size_t)rb * 64 * DM);
    u32x4 v[18];
#pragma unroll
    for (int i = 0; i < 18; ++i) v[i] = UT_LOAD(src + tid + 256 * i);
#pragma unroll
    for (int i = 0; i < 18; ++i) { const int ch = tid + 256 * i, row = ch / 72; *(u32x4*)(smem + row * 1184 + (ch - 72 * row) * 16) = v[i]; }
    __syncthreads();
    { char* ws = p.ws; if (tid == 0) IST(3, rb); }
    const unsigned ad = (unsigned)(uintptr_t)smem + (16 * g + (c >> 2)) * 1184 + (w * 144 + 4 * (c & 3)) * 2;
    char* tw = smem + 77824 + w * 20736;
#pragma unroll
    for (int i = 0; i < 9; ++i) {
        u32x2 x0, x1, x2, x3;
        asm volatile("ds_read_b64_tr_b16 %0, %4 offset:%5\n\tds_read_b64_tr_b16 %1, %4 offset:%6\n\tds_read_b64_tr_b16 %2, %4 offset:%7\n\tds_read_b64_tr_b16 %3, %4 offset:%8\n\ts_waitcnt lgkmcnt(0)"
                     : "=&v"(x0), "=&v"(x1), "=&v"(x2), "=&v"(x3) : "v"(ad), "i"(i * 32), "i"(4 * 1184 + i * 32), "i"(8 * 1184 + i * 32), "i"(12 * 1184 + i * 32) : "memory");
        u32x4* o = (u32x4*)(tw + (16 * i + c) * 144 + g * 32);
        o[0] = u32x4{x0[0], x0[1], x1[0], x1[1]}; o[1] = u32x4{x2[0], x2[1], x3[0], x3[1]};
    }
    asm volatile("" ::: "memory");
    const unsigned vo = (w * 144 + (lane >> 3)) * (SL * 2) + rb * 128 + (lane & 7) * 16;
    const char* dst = p.ws + G_D_UT;
#pragma unroll
    for (int i = 0; i < 18; ++i) {
        const u32x4 a = *(const u32x4*)(tw + (8 * i + (lane >> 3)) * 144 + (lane & 7) * 16);
        const unsigned vi = vo + i * 8 * (SL * 2);
        WST16(vi, a, dst, 0);
    }
    VMC0();
    init_wait(p.ws, w, lane);
    if (tid == 0) __hip_atomic_store((gu32*)(p.ws + G_WS_CTL + G_C_UT + rb * 128), 1u, RLX_AGENT);
    { char* ws = p.ws; if (tid == 0) IST(2, rb); }
    more();
    VMC0();
    __syncthreads();
    if (tid == 0) __hip_atomic_store((gu32*)(p.ws + G_WS_CTL + G_C_UT + rb * 128), 2u, RLX_AGENT);
}
struct KPar { u16 ldt, dsk, pa[2][4]; };
__device__ __forceinline__ KPar kgen_par(const MainP& p, int d, int lane) {
    KPar k;
    k.ldt = p.logdt[d]; k.dsk = p.Dskip[d];
#pragma unroll
    for (int q = 0; q < 2; ++q) { const int i = d * NST + lane + 64 * q; k.pa[q][0] = p.logA[i]; k.pa[q][1] = p.Aim[i]; k.pa[q][2] = p.Cre[i]; k.pa[q][3] = p.Cim[i]; }
    return k;
}
__device__ __forceinline__ void task_s4d(const MainP& p, BCh& L, int d, int lane, bool first, int& nb, KPar& kp, int& pend) {
    const int g = lane >> 4, c = lane & 15;
    BST(0);
    const float dsk = bf2f(kp.dsk);
    const auto& pa = kp.pa;
    const float dt = bfr(expf(bf2f(kp.ldt)));
#pragma unroll
    for (int q = 0; q < 2; ++q) {
        const float ar = -bfr(expf(bf2f(pa[q][0])));
        const float cr = bf2f(pa[q][2]) * dt, ci = bf2f(pa[q][3]) * dt;
        L.cst[lane + 64 * q] = f32x4{ar * dt * 1.4426950408889634f, bf2f(pa[q][1]) * dt * 0.15915494309189535f,
                                     __log2f(hypotf(cr, ci)), atan2f(ci, cr) * 0.15915494309189535f};
    }
    const char* ut = uni(p.ws + G_D_UT + (size_t)d * 4096);
    const unsigned voff = lane * 16;
    u32x4 uu[4];
    unsigned tk = nb >= 0 ? 1u : 0u;
    __builtin_amdgcn_sched_barrier(0);
    asm volatile("s_mov_b64 exec, 1\n\tglobal_atomic_add %0, %1, %0, %2 sc0\n\ts_mov_b64 exec, -1" : "+v"(tk) : "v"(G_WS_CTL + G_C_TASK + 128), "s"(p.ws) : "memory");
    const char* ue = uni(first ? (const char*)p.logA : ut);
    WLD16X4A(uu[0], uu[1], uu[2], uu[3], voff, ue);
    BST(1);
    {
        f32x4 acc[3];
#pragma unroll
        for (int v = 0; v < 3; ++v) acc[v] = f32x4{0.f, 0.f, 0.f, 0.f};
        auto base_of = [](int v, int r) { return v == 0 ? (r & 7) + 128 * (r >> 3) : v == 1 ? (r < 8 ? 256 + 2 * (r & 3) + 128 * (r >> 2) : 512 + 4 * (r & 1) + 128 * ((r - 8) >> 1))
                                                                                           : 1024 + 128 * min(r, 8); };
        const float b0 = (float)base_of(0, c), b1 = (float)base_of(1, c), b2 = (float)base_of(2, c), off = (float)(8 * c);
#define KG_POLAR(h_, l_, lg_, f_, a_, b_) do { const float m_ = __builtin_amdgcn_exp2f(fmaf(cc[0], b_, lg_)), p_ = __builtin_amdgcn_fractf(fmaf(a_, b_, f_)); \
            const float re_ = m_ * __builtin_amdgcn_cosf(p_), im_ = m_ * __builtin_amdgcn_sinf(p_);                                                            \
            h_ = pk2bf(re_, im_); l_ = pk2bf(re_ - lo2f(h_), im_ - hi2f(h_)); } while (0)
#pragma unroll 1
        for (int t = 0; t < NST / 16; ++t) {
            u32x4 fh, fl, ah[3], al[3];
#pragma unroll
            for (int j = 0; j < 4; ++j) {
                const f32x4 cc = L.cst[16 * t + 4 * g + j];
                KG_POLAR(fh[j], fl[j], 0.f, 0.f, -cc[1], off);
                KG_POLAR(ah[0][j], al[0][j], cc[2], cc[3], cc[1], b0); KG_POLAR(ah[1][j], al[1][j], cc[2], cc[3], cc[1], b1); KG_POLAR(ah[2][j], al[2][j], cc[2], cc[3], cc[1], b2);
            }
#pragma unroll
            for (int v = 0; v < 3; ++v) { acc[v] = MFMA32(al[v], fh, acc[v]); acc[v] = MFMA32(ah[v], fl, acc[v]); acc[v] = MFMA32(ah[v], fh, acc[v]); }
        }
        BST(2);
        auto split = [](float k) { const unsigned hi = pk2bf(k, 0.f) & 0xffffu; return hi | (pk2bf(0.f, k - __uint_as_float(hi << 16)) & 0xffff0000u); };
#pragma unroll
        for (int v = 0; v < 3; ++v)
#pragma unroll
            for (int i = 0; i < 4; ++i) {
                const int pos = base_of(v, 4 * g + i) + 8 * c;
                if (pos <= 2048) L.Kd[index_of_pos(pos)] = split(2.0f * acc[v][i]);
            }
    }
    __builtin_amdgcn_sched_barrier(0);
    asm volatile("s_waitcnt vmcnt(0)" : "+v"(tk), "+a"(uu[0]), "+a"(uu[1]), "+a"(uu[2]), "+a"(uu[3]) :: "memory");
    if (first) {
        wait_word(p, p.ws + G_WS_CTL + G_C_UT + (lane & 31) * 128, 1, 30u);
        WLD16X4A(uu[0], uu[1], uu[2], uu[3], voff, ut);
        asm volatile("s_waitcnt vmcnt(0)" : "+a"(uu[0]), "+a"(uu[1]), "+a"(uu[2]), "+a"(uu[3]) :: "memory");
    }
    if (pend >= 0 && lane == 0) __hip_atomic_fetch_add((gu32*)(p.ws + G_WS_CTL + G_C_YC + pend * 128), 1u, RLX_AGENT);
    nb = __builtin_amdgcn_readfirstlane(tk) + 1;
    const u32x4 z4 = {0u, 0u, 0u, 0u};
    *(u32x4*)&L.Kh[8 * lane] = z4; *(u32x4*)&L.Kl[8 * lane] = z4;
    *(u32x4*)&L.Kh[512 + 8 * (lane & 31)] = z4; *(u32x4*)&L.Kl[512 + 8 * (lane & 31)] = z4;
    *(u32x4*)&L.uc[0][64 + SL] = z4;
    *(u32x4*)&L.uc[(lane >> 3) & 3][8 * (lane & 7)] = z4;
    asm volatile("" ::: "memory");
#pragma unroll
    for (int q = 0; q < 4; ++q) {
        const int e = lane + 64 * q, at = (16 + (e >> 2)) * KROW + 24 - 8 * (e & 3);
        unsigned k[8];
        const unsigned* kd = L.Kd + index_of_pos(8 * e);
        if (q == 0) {
            const bool two = lane >= 32;
#pragma unroll
            for (int r = 0; r < 8; ++r) k[7 - r] = kd[two ? (0x43222100 >> (4 * r)) & 7 : r];
        } else if (q == 1) {
            const unsigned k0 = kd[0], k1 = kd[1], k2 = kd[2];
            k[7] = k[6] = k[5] = k0; k[4] = k[3] = k[2] = k1; k[1] = k[0] = k2;
        } else {
            const unsigned k0 = kd[0], k1 = kd[1];
            k[7] = k[6] = k[5] = k[4] = k0; k[3] = (e & 1) ? k1 : k0; k[2] = k[1] = k[0] = k1;
        }
        u32x4 kh, kl;
#pragma unroll
        for (int j = 0; j < 4; ++j) { kh[j] = prm(k[2 * j + 1], k[2 * j], 0x05040100u); kl[j] = prm(k[2 * j + 1], k[2 * j], 0x07060302u); }
        *(u32x4*)&L.Kh[at] = kh;
        if (G_NHL == 2) *(u32x4*)&L.Kl[at] = kl;
    }
    BST(3);
#pragma unroll
    for (int q = 0; q < 4; ++q) *(u32x4*)&L.uc[0][64 + 8 * (lane + 64 * q)] = uu[q];
    asm volatile("" ::: "memory");
#pragma unroll
    for (int q = 0; q < 4; ++q) {
        const int e = lane + 64 * q;
        const int ue = 56 + 8 * e;
        const u32x4 a = *(const u32x4*)&L.uc[0][ue];
        const u32x2 b = *(const u32x2*)&L.uc[0][ue + 8];
        const unsigned s0 = __builtin_amdgcn_alignbit(a[1], a[0], 16), s1 = __builtin_amdgcn_alignbit(a[2], a[1], 16), s2 = __builtin_amdgcn_alignbit(a[3], a[2], 16),
                       s3 = __builtin_amdgcn_alignbit(b[0], a[3], 16), s4 = __builtin_amdgcn_alignbit(b[1], b[0], 16);
        *(u32x4*)&L.uc[1][ue] = u32x4{s0, s1, s2, s3};
        *(u32x4*)&L.uc[2][ue] = u32x4{a[1], a[2], a[3], b[0]};
        *(u32x4*)&L.uc[3][ue] = u32x4{s1, s2, s3, s4};
    }
    if (lane == 0) {
        const u32x4 a = *(const u32x4*)&L.uc[0][56 + SL];
        const unsigned s0 = __builtin_amdgcn_alignbit(a[1], a[0], 16), s1 = __builtin_amdgcn_alignbit(a[2], a[1], 16), s2 = __builtin_amdgcn_alignbit(a[3], a[2], 16), s3 = a[3] >> 16;
        *(u32x4*)&L.uc[1][56 + SL] = u32x4{s0, s1, s2, s3};
        *(u32x4*)&L.uc[2][56 + SL] = u32x4{a[1], a[2], a[3], 0u};
        *(u32x4*)&L.uc[3][56 + SL] = u32x4{s1, s2, s3, 0u};
    }
    kp = kgen_par(p, min(nb, DM - 1), lane);
    BST(4);
    {
        const int o = 33 + c + 8 * g;
        const unsigned ua = (unsigned)(uintptr_t)&L.uc[o & 3][o & ~3], ka = (unsigned)(uintptr_t)&L.Kh[(16 + c) * KROW + 8 * g];
        const f32x4* res = (const f32x4*)L.Kh + lane;
        asm volatile(ASM_CV :: "v"(ua), "v"(ka), "v"((unsigned)(uintptr_t)res) : CV_CLOB);
        asm volatile("" :: "v"(kp.ldt), "v"(kp.dsk), "v"(kp.pa[0][0]), "v"(kp.pa[0][1]), "v"(kp.pa[0][2]), "v"(kp.pa[0][3]), "v"(kp.pa[1][0]), "v"(kp.pa[1][1]), "v"(kp.pa[1][2]), "v"(kp.pa[1][3]));
        BST(5);
#pragma unroll
        for (int par = 0; par < 2; ++par)
#pragma unroll
            for (int t = 0; t < 4; ++t) {
                const int n = 16 * (par + 2 * (16 * t + c)) + 4 * g;
                const u32x2 uu = *(const u32x2*)&L.uc[0][64 + n];
                const f32x4 y4 = res[(par * 4 + t) * 64];
                const float r0 = bfr(y4[0]) + bfr(lo2f(uu[0]) * dsk), r1 = bfr(y4[1]) + bfr(hi2f(uu[0]) * dsk);
                const float r2 = bfr(y4[2]) + bfr(lo2f(uu[1]) * dsk), r3 = bfr(y4[3]) + bfr(hi2f(uu[1]) * dsk);
                *(u32x2*)&L.uc[1][n] = u32x2{pk2bf(r0, r1), pk2bf(r2, r3)};
            }
    }
    BST(6);
    char* yb = uni(p.ws + G_D_YB + (size_t)d * 4096);
#pragma unroll
    for (int q = 0; q < 4; ++q) { const u32x4 o = *(const u32x4*)&L.uc[1][8 * (lane + 64 * q)]; WST16(voff, o, yb, q * 1024); }
    pend = d >> 6;
    BST(7);
}
__device__ __forceinline__ void s4d_wave(const MainP& p, char* smem, int w, int lane, int quota) {
    int nb = 0, pend = -1;
    if (lane == 0) nb = (int)(__hip_atomic_fetch_add((gu32*)(p.ws + G_WS_CTL + G_C_TASK + 128), 1u, RLX_AGENT) + 1u);
    nb = __builtin_amdgcn_readfirstlane(nb);
    KPar kp = kgen_par(p, min(nb, DM - 1), lane);
#pragma unroll 1
    for (int n = 0; n < quota; ++n) {
        const int d = nb;
        if (d >= DM) break;
        nb = n + 1 < quota ? 0 : -1;
        task_s4d(p, reinterpret_cast<BCh*>(smem)[w], d, lane, n == 0, nb, kp, pend);
    }
    VMC0();
    if (pend >= 0 && lane == 0) __hip_atomic_fetch_add((gu32*)(p.ws + G_WS_CTL + G_C_YC + pend * 128), 1u, RLX_AGENT);
}
__device__ __forceinline__ void watch_role(const MainP& p, int lane) {
    const char* c = p.ws + G_WS_CTL;
#pragma unroll 1
    for (int s = G_S0; s < NSEG; ++s) {
        const int n = min(s + 3, NSEG - 1);
        wait_word(p, c + G_C_GV + lane * 128, n - (n < (int)G_S1 ? (n ^ (lane >> 5)) & 1 : 0), 60u);
        wait_word(p, c + (lane < 32 ? G_C_GX : G_C_UT - 4096) + lane * 128, lane < 32 ? min(s + 2, NSEG - 1) : s < 40 ? -1 : 2, 61u);
        if (lane == 0) __hip_atomic_store((gu32*)(p.ws + G_WS_CTL + G_C_RDY + s * 128), 1u, RLX_AGENT);
    }
}
__device__ __forceinline__ void task_merge(const MainP& p, char* smem, int gr, int i, int w, int lane) {
    const int g = lane >> 4, c = lane & 15;
    wait_word(p, p.ws + G_WS_CTL + G_C_YC + gr * 128, 63, 31u);
    const char* sb = uni(p.ws + G_D_YB + (size_t)gr * 262144 + i * 1024 + w * 256);
    const unsigned vt = g * 4096 + c * 16;
    u32x4 z[16];
#pragma unroll
    for (int j = 0; j < 16; ++j) { const char* sj = sb + j * 16384; WLD16(z[j], vt, sj, 0); }
    asm volatile("s_waitcnt vmcnt(0)" : "+v"(z[0]), "+v"(z[1]), "+v"(z[2]), "+v"(z[3]), "+v"(z[4]), "+v"(z[5]), "+v"(z[6]), "+v"(z[7]),
                 "+v"(z[8]), "+v"(z[9]), "+v"(z[10]), "+v"(z[11]), "+v"(z[12]), "+v"(z[13]), "+v"(z[14]), "+v"(z[15]) :: "memory");
    char* tw = smem + w * 17408;
#pragma unroll
    for (int j = 0; j < 16; ++j) *(u32x4*)(tw + (4 * j + g) * 272 + c * 16) = z[j];
    const unsigned ad = (unsigned)(uintptr_t)tw + (8 * g + (c >> 2)) * 272 + (c & 3) * 8;
    u16* orow = p.out + (size_t)(512 * i + 128 * w + c) * OUTW + DM + 64 * gr + 8 * g;
#pragma unroll
    for (int k = 0; k < 8; ++k) {
        u32x2 x0, x1, x2, x3;
        asm volatile("ds_read_b64_tr_b16 %0, %4 offset:%5\n\tds_read_b64_tr_b16 %1, %4 offset:%6\n\tds_read_b64_tr_b16 %2, %4 offset:%7\n\tds_read_b64_tr_b16 %3, %4 offset:%8\n\ts_waitcnt lgkmcnt(0)"
                     : "=&v"(x0), "=&v"(x1), "=&v"(x2), "=&v"(x3) : "v"(ad), "i"(k * 32), "i"(4 * 272 + k * 32), "i"(32 * 272 + k * 32), "i"(36 * 272 + k * 32) : "memory");
        OST16(orow + (size_t)16 * k * OUTW, (u32x4{x0[0], x0[1], x1[0], x1[1]}));
        OST16(orow + (size_t)16 * k * OUTW + 32, (u32x4{x2[0], x2[1], x3[0], x3[1]}));
    }
}

static constexpr int Q1 = NG1 * 6 + 36, Q2 = Q1 + (NFINE - 2) * 25;
static constexpr int NTASK = Q2 + 68;
__host__ __device__ __forceinline__ void task_of(int u, int m0, int& sg, int& r) {
    if (u < 6 * m0) { sg = u / 6; r = u % 6; }
    else if (u < 6 * m0 + 108) { sg = m0 + (u - 6 * m0) / 9; r = (u - 6 * m0) % 9; }
    else if (u < Q1) { sg = (u - 36) / 6; r = (u - 36) % 6; }
    else if (u < Q2) { sg = NG1 + (u - Q1) / 25; r = (u - Q1) % 25; }
    else { sg = 62 + (u - Q2) / 34; r = (u - Q2) % 34; }
}
__device__ __forceinline__ void worker_body(const MainP& p, char* smem, const int w, const int lane) {
    const int tid = w * 64 + lane;
    int* slot = (int*)(smem + LDS_TOTAL - 16);
    int nst = 0, nxt = -1;
    gu32* qa = (gu32*)(p.ws + G_WS_CTL + G_C_TASK);
    const int m0 = (FLH(p.prm[7]) >> 4) & 63;
    for (;;) {
        if (tid == 0) {
            *slot = nxt >= 0 ? nxt : (int)(__hip_atomic_fetch_add(qa, 1u, RLX_AGENT) + 1u);
            nxt = -1;
        }
        __syncthreads();
        const int u = __builtin_amdgcn_readfirstlane(*slot);
        if (u >= NTASK) break;
        if (tid == 0 && u < Q1 - 140) nxt = (int)(__hip_atomic_fetch_add(qa, 1u, RLX_AGENT) + 1u);
        const bool last2 = u >= Q2;
        int sg, r;
        task_of(u, m0, sg, r);
        if (u < Q1) {
            if (r < 6) task_out<6, 6, false>(p, smem, sg, 6 * r, w, lane);
            else { const int m = 3 * (sg - m0) + r - 6; task_merge(p, smem, m >> 2, m & 3, w, lane); }
        }
        else if (r < 16) task_attn_tail(p, sg, 2 * r + (w >> 1), w, lane);
        else if (last2) task_out<2, 9, true>(p, smem, sg, 2 * (r - 16), w, lane);
        else task_out<4, 9, true>(p, smem, sg, 4 * (r - 16), w, lane);
        if (last2) break;
        __syncthreads();
    }
}

__host__ __device__ constexpr unsigned phys(unsigned x) { return (x >> 13) * 16384 + (x & 8191); }
__host__ __device__ constexpr unsigned aMB(int f, int b) { return phys(f < 8 ? 16384 + f * 1024 + b : 28672 + b); }
__host__ __device__ constexpr unsigned aPX(int pr, int b) { return phys(pr * 1024 + b); }
__host__ __device__ constexpr unsigned aPM(int u, int b) { return phys(24576 + u * 1024 + b); }
static constexpr int L_KI = 8192, L_QI = 16384, L_VI = 147456;

__device__ __forceinline__ int lane_id() { int l; asm volatile("v_mbcnt_lo_u32_b32 %0, -1, 0\n\tv_mbcnt_hi_u32_b32 %0, -1, %0" : "=v"(l)); return l; }
typedef const __attribute__((address_space(4))) MainP* KArg;

__global__ __launch_bounds__(256, 1) void main_kernel(MainP p_) {
    __shared__ __attribute__((aligned(16))) char smem[LDS_TOTAL];
    KArg ka = (KArg)__builtin_amdgcn_kernarg_segment_ptr();
    static_assert(offsetof(MainP, W) == 0x28, "argument layout");
    asm volatile(ASM_EA :: "s"(ka), "s"(blockIdx.x), "v"(threadIdx.x) : "v1", "v2", "s40", "s41", "s42", "s43", "s44", "s45", "s46", "s47", "s48", "s49", "scc", "memory");
    asm volatile("" : "+s"(ka));
    const int b = blockIdx.x, x = b & 7, ix = b >> 3;
    const int w = __builtin_amdgcn_readfirstlane(threadIdx.x >> 6);
    const bool isr = x < 4 && ix >= 16;
    constexpr int NRF = 8 + 8 * G_GVN;
    if (__builtin_expect(ix < (x < 4 ? 16 : NRF), 1)) {
        const int h = (x & 3) * 8 + (ix & 7), id = h + (ix >> 4) * 32;
        const unsigned stb = 0;
#define UNR _Pragma("unroll")
#define CW(k, val) do { if ((k) < 16) ca[(k) & 15] = (unsigned)(val); else if ((k) < 32) cb[(k) & 15] = (unsigned)(val); else cl[((k) - 40) & 15] = (unsigned)(val); } while (0)
#define ROLEW(A_) asm volatile(A_ : "+{v[0:15]}"(ca), "+{v[16:31]}"(cb), "+{v[200:215]}"(cl) : "s"(ka), "s"(w), "s"(id), "s"(stb) : ASM_CLOBW)
#define PAD(n_) asm volatile("s_branch skip_" n_ "\npad_" n_ ":\n\ts_setpc_b64 s[46:47]\nskip_" n_ ":")
#define CW_ALL const int lane = lane_id(), g = lane >> 4, c = lane & 15, q = c >> 2; u32x16 ca = {}, cb = {}, cl = {};                                      \
        CW(0, lane * 16); CW(40, (4 * w + g) * 8192 + h * 256 + (c ^ (2 * g + 8 * (w >> 1))) * 16);                                                          \
        UNR for (int k = 0; k < 2; ++k) CW(41 + k, (8 * g + q) * 256 + ((4 * w + 2 * k + ((c >> 1) & 1)) ^ (2 * q + 8 * (g & 1))) * 16 + (c & 1) * 8)
#define CW_SRC(b_, key_) UNR for (int k = 0; k < 9; ++k) { const int pp = 64 * k + lane, r = pp / 72, pc = pp - 72 * r; CW(43 + k, (b_) + (8 * w + r) * 1152 + (pc ^ (key_)) * 16); }
#define UNIT(u_, key_) (((u_) >> 2) * 128 + (((u_) & 3) ^ (key_)) * 32 + (c & 3) * 8)
#define CW_NEAR CW(1, x * 65536 + aMB(2 * w, lane * 16)); CW(2, x * 65536 + aMB(8, w * 256 + lane * 4)); CW(3, 2 * w * 1024 + lane * 16);                            \
        UNR for (int i = 0; i < 6; ++i) CW(4 + i, ((2 * w + 2 + i) & 7) * 1024 + lane * 16);                                                                 \
        CW(10, (c & 2) ? 0xEEEE : 0x4444);                                                                                                                   \
        CW(20, x * 65536 + 22528 + (lane & 31) * 64); CW(21, lane < 32 ? (lane & 3) * 65536 + phys((lane >> 2) * 2048) + h * 64 : 32768 + h * 128);          \
        CW(22, lane * 4); CW(23, x * 65536 + aMB(2 * w + 1, lane * 16))
#define CW_FAR CW(0, c * 1152 + (g ^ (c >> 1)) * 16); CW(1, ca[0]); CW(2, ca[0] ^ 64); CW(3, w * 9216 + lane * 16); CW_SRC(0, (r >> 1) + 4 * (w & 1))                                   \
        CW(4, L_VI + w * 2048 + lane * 16); CW(5, L_VI + lane * 16); CW(6, lane * 4)
        if (x >= 4 && ix >= 8) {
            PAD("gv"); CW_ALL; CW_FAR;
            UNR for (int t = 0; t < 9; ++t) CW(8 + t, (9 * w + t) * G_UXS + skew(0, 9 * w + t) * 32768 + h * 1024 + lane * 16);
            UNR for (int k = 0; k < 2; ++k) CW(19 + k, (9 * w + 4 * k + g) * 16384 + h * 256 + c * 16);
            CW(21, (9 * w + 8) * 16384 + h * 256 + lane * 4);
            UNR for (int k = 0; k < 4; ++k) CW(52 + k, (4 * g + q) * 1152 + ((9 * w + k) >> 2) * 128 + ((2 * ((9 * w + k) & 3) + ((c >> 1) & 1)) ^ (2 * g + (q >> 1))) * 16 + (c & 1) * 8);
            ROLEW(ASM_GV);
        } else if (x >= 4) {
            PAD("gx"); CW_ALL; CW_FAR;
            CW(8, w * 2048 + lane * 16);
            ROLEW(ASM_GX);
        } else if (ix < 8) {
            PAD("qk"); CW_ALL; CW_NEAR;
            CW(12, L_QI + (16 * w + (c & 13)) * 32 + 8 * g + 4 * ((c >> 1) & 1));
            CW(13, L_QI + (8 * g + q) * 32 + (c & 3) * 8);
            CW(14, G_WS_CTL + G_C_KM + h * 128); CW(15, lane * 16 + 4096);
            CW(16, L_KI + (32 * w + c) * 16 + 4 * g);
            CW(17, L_KI + (4 * g + q) * 16 + (c & 1) * 8);
            const int mm = 8 * (w & 1) + 4 * (g >> 1) + (g & 1);
            CW(18, (x ^ 3) * 65536 + aPX(h >> 1, (h & 1) * 512 + (w & 1) * 256 + g * 64 + (c >> 3) * 32 + ((c >> 1) & 1) * 16 + (2 * (q & 1) + (c & 1)) * 4));
            CW(19, ((c & 1) + 2 * (c >> 3)) * 65536 + aPM(h >> 3, ((h >> 1) & 3) * 256 + (mm + 2 * ((c >> 1) & 1)) * 16 + (h & 1) * 8 + (q & 1) * 4));
            CW(24, G_WS_CTL + G_C_GX + h * 128);
            ROLEW(ASM_QK);
        } else {
            PAD("vv"); CW_ALL; CW_NEAR; CW_SRC(h * 147456, r & 4);
            const int tq = 9 * w + 2 * (g & 1) + (g >> 1);
            CW(14, tq * 16384 + h * 256 + c * 16);
            CW(15, (tq + 4) * 16384 + h * 256 + c * 16);
            CW(16, (9 * w + 8) * 16384 + h * 256 + c * 16 + (g & 1) * 8);
            CW(19, (c & 8) ? 0xEEEE : 0x4444);
            CW(12, L_QI + w * 576 + c * 16 + 4 * g);
            CW(13, L_QI + (8 * g + q) * 16 + (c & 1) * 8 + ((c >> 1) & 1) * 576);
            CW(25, L_QI + (8 * g + q) * 16 + (c & 1) * 8 + (1 - ((c >> 1) & 1)) * 576);
            UNR for (int k = 0; k < 4; ++k) CW(52 + k, ((k >> 1) ^ (q >> 1)) * 36864 + (4 * g + (q & 1)) * 1152 + UNIT(9 * w + 2 * (k & 1) + (q >> 1), 2 * (g & 1)));
            CW(24, (q >> 1) * 36864 + (4 * g + (q & 1)) * 1152 + UNIT(9 * w + 8, 2 * (g & 1)));
            ROLEW(ASM_V);
        }
    } else {
    if (isr && (ix < 25 || ix >= 29)) {
        const int id = x * 12 + (ix < 25 ? ix - 13 : ix - 29);
        init_cal(ka->ws, id, threadIdx.x, (FLG(ka->prm[5]) >> 16) & 15);
        if (FLG(ka->prm[5]) & 4) {
            const unsigned o = (id * 384 + threadIdx.x) * 128, o2 = o + (threadIdx.x & 128 ? 16384 : 32768);
            const char* a = (const char*)ka->X + o; const char* b2 = (const char*)ka->U + o;
            const char* c2 = (const char*)(w < 2 ? ka->X : ka->U) + o2;
            const char* ps = (const char*)(w == 0 ? ka->logA : w == 1 ? ka->Aim : w == 2 ? ka->Cre : ka->Cim) + id * 3072 + min((int)(threadIdx.x & 63), 23) * 128;
            asm volatile("s_mov_b32 m0, %4\n\ts_nop 0\n\tglobal_load_lds_dword %0, off\n\tglobal_load_lds_dword %1, off\n\tglobal_load_lds_dword %2, off\n\tglobal_load_lds_dword %3, off"
                         :: "v"(a), "v"(c2), "v"(b2), "v"(ps), "s"(163072) : "memory");
        }
    }
    if (x >= 4 ? ix >= 24 : (ix >= 25 && ix < 29)) init_fill(ka->ws, ka->mem0, x >= 4 ? (ix - 24) * 4 + (x & 3) : 32 + (ix - 25) * 4 + x, threadIdx.x);
    if (x >= 4 && !(FLG(ka->prm[5]) & 1)) {
        const char* src = (const char*)ka->W[3] + (size_t)((x & 3) * 8 + (ix & 7)) * 147456 + threadIdx.x * 128;
#pragma unroll
        for (int i = 0; i < 5; ++i)
            if (i < 4 || w < 2) asm volatile("s_mov_b32 m0, %1\n\ts_nop 0\n\tglobal_load_lds_dword %0, off" :: "v"(src + i * 32768), "s"(155648) : "memory");
    }
    if (isr && ix >= 25) {
        for (int i = 0; i < (ix < 29 ? 3 : 0) + HSL; ++i) __builtin_amdgcn_s_sleep(40);
        init_wait(ka->ws, w, threadIdx.x & 63);
    }
    asm volatile("" : "+s"(ka));
    if (x < 4 && (ix < 25 || w == 0)) {
        const int lane = threadIdx.x & 63, g = lane >> 4, c = lane & 15;
        const unsigned lb = w * G_STG;
        unsigned* ct = (unsigned*)(smem + lb) + lane;
        asm volatile("" :: "v"(ct) : "memory");
#define CTW(k, val) ct[(k) * 64] = (unsigned)(val)
        const u64 ws = (u64)ka->ws, prm = (u64)ka;
        const u64 stb = 0;
        CTW(0, lane * 16);
        const int j = x * 9 + ix - 16;
        if (ix >= 25) {
            CTW(1, x * 65536 + 22528 + (lane & 31) * 64);
            for (int k = 0; k < 7; ++k) {
                const int q = min(ix - 25 + 7 * k, 43), id = q < 36 ? 9 * x + (q >> 2) : 8 * x + q - 36;
                CTW(4 + k, (q < 36 ? G_D_UX + id * G_UXS + skew(0, id) * 32768 + (q & 3) * 8192 : G_D_KX + id * G_HS8 + skew(0, id) * 8192) + lane * 128);
                CTW(11 + k, q < 36 ? 32768 : 8192);
            }
        } else {
            const int rot = (FLG(ka->prm[5]) & 16) ? 0 : (x ^ 3) * 4;
            if (w >= 2) {
                return;
            }
            CTW(1, lane * 16 + 4096);
            CTW(2, x * 65536 + aMB(j >> 2, (j & 3) * 256 + (4 * g + 2 * ((c >> 1) & 1) + (c & 1)) * 16 + (c >> 2) * 4));
            for (int k = 0; k < 5; ++k) { const int q = 9 * k + ix - 16; CTW(46 + k, x * 65536 + lane * 16 + (q < 29 ? phys(q * 1024) : 0x200000 + phys((q - 29) * 1024))); }
            CTW(4, (c & 2) ? 0xEEEE : 0x4444); CTW(5, (c & 1) ? 0x03020706u : 0x05040100u);
            CTW(6, G_WS_CTL + G_C_GV + ((w & (G_GVN - 1)) * 32 + ((lane & 31) ^ (2 * rot))) * 128); CTW(7, x * 65536 + (lane & 31) * 64); CTW(8, lane * 4); CTW(9, 131072);
            for (int k = 0; k < 16; ++k) { const int i = k ^ rot; CTW(10 + k, x * 65536 + aPX(i, (g >> 1) * 512 + (2 * (c >> 2) + (c & 1)) * 64 + (g & 1) * 32 + ((c >> 1) & 1) * 16)); CTW(26 + k, i * 2048 + lane * 16); }
            for (int k = 0; k < 4; ++k) CTW(42 + k, x * 65536 + aPM(k, lane * 16));
        }
        const int id = ix >= 25 ? ix - 25 : j;
#define ROLE(A_) asm volatile(A_ :: "s"(ws), "s"(ws), "s"(ws), "s"(w), "s"(id), "s"(ws), "s"(prm), "s"(lb), "s"(stb) : ASM_CLOB)
        if (ix >= 25) ROLE(ASM_H);
        else ROLE(ASM_R);
    }
    else if (x >= 4 && ix >= 24) {
        if (ix >= 25) for (int i = 0; i < ((FLG(ka->prm[5]) >> 5) & 7); ++i) __builtin_amdgcn_s_sleep(36);
        const char* xs = (const char*)ka->X + (size_t)((ix - 24) * 4 + (x & 3)) * 73728 + threadIdx.x * 128;
        const int f = (ix - 24) * 4 + (x & 3);
        const u16* ps = (w == 0 ? ka->logA : w == 1 ? ka->Aim : w == 2 ? ka->Cre : ka->Cim) + f * 2304 + min((int)(threadIdx.x & 63), 35) * 64;
        if (ix < 25 || !(FLG(ka->prm[5]) & 4))
        asm volatile("s_mov_b32 m0, %4\n\ts_nop 0\n\tglobal_load_lds_dword %0, off\n\tglobal_load_lds_dword %1, off\n\tglobal_load_lds_dword %2, off\n\tglobal_load_lds_dword %3, off"
                     :: "v"(xs), "v"(xs + 32768), "v"(xs + 65536 - w * 8192), "v"(ps), "s"(163072) : "memory");
        if (ix < 25) for (int i = 0; i < ((FLG(ka->prm[5]) >> 5) & 7); ++i) __builtin_amdgcn_s_sleep(36);
    }
    }
    asm volatile("" : "+s"(ka));
    MainP p;
    p.prm[5] = ka->prm[5]; p.prm[7] = ka->prm[7];
    p.U = ka->U; p.ws = ka->ws; p.logA = ka->logA; p.Aim = ka->Aim; p.Cre = ka->Cre; p.Cim = ka->Cim; p.logdt = ka->logdt; p.Dskip = ka->Dskip; p.out = ka->out;
    if (x >= 4 && ix >= 24) {
        const u32x4 sent = {0xffffffffu, 0xffffffffu, 0xffffffffu, 0xffffffffu};
        const int tid = w * 64 + lane_id(), h = (ix - 24) * 4 + (x & 3);
        task_ut(p, smem, h, w, lane_id(), [&] {
#pragma unroll 1
            for (int sg = NG1; sg < 64; ++sg) {
                const unsigned ok = G_D_KM + h * G_HS4 + skew(sg, h) * 4096 + tid * 16;
                const unsigned op = tid < 128 ? G_D_PX + (sg * 32 + h) * 2048 + tid * 16 : G_D_PM + sg * 16384 + ((tid >> 4) & 1) * 8192 + h * 256 + (tid & 15) * 16;
                asm volatile("global_store_dwordx4 %0, %2, %3 sc1\n\tglobal_store_dwordx4 %1, %2, %3 sc1\n\ts_nop 1" :: "v"(ok), "v"(op), "v"(sent), "s"(p.ws) : "memory");
            }
        });
    }
    {
        const int nq = x < 4 ? (w + ((FLH(p.prm[7]) >> 1) & 3) > 3 ? 0 : 1000) : ix < 8 ? (FLG(p.prm[5]) >> 8) & 3 : ix < 16 ? (FLG(p.prm[5]) >> 10) & 3 : (ix & 2) && ((31 - ix) >> 2) < NAG ? 0 : ix < 24 ? (ix < 16 + ((FLH(p.prm[7]) >> 10) & 15) ? (FLG(p.prm[5]) >> 12) & 3 : 0) : (FLG(p.prm[5]) >> 14) & 3;
        if (b == 239 && w == 0) watch_role(p, lane_id());
        else if (nq || b == 239) {
            for (int i = 0; i < (int)(((ix & 7) * 4 + w) * ((FLG(p.prm[5]) >> 23) & 7)); ++i) __builtin_amdgcn_s_sleep(3);
            s4d_wave(p, smem, w, lane_id(), nq == 3 || b == 239 ? 1000 : nq);
        }
        if (x < 4) return;
        __syncthreads();
    }
    if ((ix & 18) == 18 && ((31 - ix) >> 2) < NAG) { attn_role(p, ((31 - ix) >> 2) + (ix < 24 ? NA : 0), (x & 3) * 8 + (ix & 1) * 4 + w, w, lane_id());
        if (b == 255) {
            wait_word(p, p.ws + G_WS_CTL + G_C_ARR, 255, 50u);
            if (w == 0 && lane_id() == 0) __hip_atomic_fetch_add((gu32*)(p.ws + G_WS_EP), 1u, RLX_AGENT);
        }
        __syncthreads();
    }
    p.prm[0] = near_mask(p.ws, lane_id());
    worker_body(p, smem, w, lane_id());
}

extern "C" __global__ void code_end_marker() {}

static constexpr int g_prm[8] = {PRM_DEFAULT};

extern "C" void kernel_launch(void* const* d_in, const int* in_sizes, int n_in, void* d_out, int out_size,
                              void* d_ws, size_t ws_size, hipStream_t stream) {
    char* ws = (char*)(((uintptr_t)d_ws + 0x1FFFFF) & ~(uintptr_t)0x1FFFFF);
    MainP p;
    for (int i = 0; i < 8; ++i) p.prm[i] = g_prm[i];
    p.mem0 = (const u16*)d_in[1];
    for (int i = 0; i < 4; ++i) p.W[i] = (const u16*)d_in[2 + i];
    p.X = (const u16*)d_in[0]; p.U = (const u16*)d_in[7]; p.ws = ws;
    p.logA = (const u16*)d_in[8]; p.Aim = (const u16*)d_in[9]; p.Cre = (const u16*)d_in[10]; p.Cim = (const u16*)d_in[11];
    p.logdt = (const u16*)d_in[12]; p.Dskip = (const u16*)d_in[13]; p.out = (u16*)d_out;
    main_kernel<<<256, 256, 0, stream>>>(p);
}
```

```cpp
#define OWT 0
#include <hip/hip_runtime.h>
#include <hip/hip_bf16.h>
#include <stdint.h>
#include <stddef.h>

typedef __attribute__((ext_vector_type(8))) __bf16 bf16x8;
typedef __attribute__((ext_vector_type(4))) short s16x4;
typedef __attribute__((ext_vector_type(4))) float f32x4;
typedef __attribute__((ext_vector_type(4))) unsigned u32x4;
typedef __attribute__((ext_vector_type(2))) unsigned u32x2;
typedef __attribute__((ext_vector_type(2))) float f32x2;
typedef __attribute__((ext_vector_type(16))) unsigned u32x16;
typedef unsigned short u16;
typedef unsigned long long u64;

#define GAS __attribute__((address_space(1)))
typedef GAS unsigned gu32;
#define RLX_AGENT __ATOMIC_RELAXED, __HIP_MEMORY_SCOPE_AGENT

static constexpr int DM = 576, NH = 32, DH = 128, MM = 8, SEG = 32, NSEG = 64, SL = 2048;
static constexpr int HD = NH * DH;
static constexpr int OUTW = 2 * DM;
static constexpr int NST = 128;

__device__ __forceinline__ float bf2f(u16 h) { return __uint_as_float(((unsigned)h) << 16); }
typedef __attribute__((ext_vector_type(2))) __bf16 bf16x2_t;
__device__ __forceinline__ unsigned pk2bf(float a, float b) {
    bf16x2_t v; v[0] = (__bf16)a; v[1] = (__bf16)b;
    return __builtin_bit_cast(unsigned, v);
}
__device__ __forceinline__ float bfr(float f) { return __uint_as_float(pk2bf(f, f) << 16); }
__device__ __forceinline__ float lo2f(unsigned u) { return __uint_as_float(u << 16); }
__device__ __forceinline__ float hi2f(unsigned u) { return __uint_as_float(u & 0xffff0000u); }
__device__ __forceinline__ float rowmax4(float x) {
    auto a = __builtin_amdgcn_permlane16_swap(__float_as_uint(x), __float_as_uint(x), false, false);
    x = fmaxf(__uint_as_float(a[0]), __uint_as_float(a[1]));
    auto b = __builtin_amdgcn_permlane32_swap(__float_as_uint(x), __float_as_uint(x), false, false);
    return fmaxf(__uint_as_float(b[0]), __uint_as_float(b[1]));
}
__device__ __forceinline__ float rowsum4(float x) {
    auto a = __builtin_amdgcn_permlane16_swap(__float_as_uint(x), __float_as_uint(x), false, false);
    x = __uint_as_float(a[0]) + __uint_as_float(a[1]);
    auto b = __builtin_amdgcn_permlane32_swap(__float_as_uint(x), __float_as_uint(x), false, false);
    return __uint_as_float(b[0]) + __uint_as_float(b[1]);
}

struct MainP {
    int prm[8];
    const u16* mem0; const u16* W[4]; const u16* X; const u16* U;
    char* ws;
    const u16* logA; const u16* Aim; const u16* Cre; const u16* Cim; const u16* logdt; const u16* Dskip;
    u16* out;
};
static constexpr int LDS_TOTAL = 163840;
static constexpr unsigned kWaitCap = 1u << 18;
#define VMC0() asm volatile("s_waitcnt vmcnt(0)" ::: "memory")
#define PRM_DEFAULT 1050, 600, 120, 675, 760, 1434873899, 3145992, 52931
#define ASM_QK \
    "s_load_dwordx2 s[42:43], %3, 40\n\t" \
    "s_load_dwordx2 s[44:45], %3, 48\n\t" \
    "s_load_dwordx2 s[36:37], %3, 0x58\n\t" \
    "s_load_dwordx2 s[50:51], %3, 0x48\n\t" \
    "s_load_dwordx8 s[84:91], %3, 0\n\t" \
    "s_mov_b32 s66, %4\n\t" \
    "s_mov_b32 s67, %5\n\t" \
    "s_mov_b32 s68, 262144\n\t" \
    "s_mov_b32 s72, 0\n\t" \
    "s_mov_b32 s83, -1\n\t" \
    "s_mov_b32 s65, 0\n\t" \
    "s_lshl_b32 s82, s66, 10\n\t" \
    "s_waitcnt lgkmcnt(0)\n\t" \
    "s_add_u32 s46, s42, 2097152\n\t" \
    "s_addc_u32 s47, s43, 0\n\t" \
    "s_add_u32 m0, s82, 65536\n\t" \
    "s_nop 0\n\t" \
    "global_load_lds_dwordx4 v200, s[46:47]\n\t" \
    "s_add_u32 s46, s42, 2228224\n\t" \
    "s_addc_u32 s47, s43, 0\n\t" \
    "s_add_u32 m0, s82, 69632\n\t" \
    "s_nop 0\n\t" \
    "global_load_lds_dwordx4 v200, s[46:47]\n\t" \
    "s_add_u32 s46, s42, 2359296\n\t" \
    "s_addc_u32 s47, s43, 0\n\t" \
    "s_add_u32 m0, s82, 73728\n\t" \
    "s_nop 0\n\t" \
    "global_load_lds_dwordx4 v200, s[46:47]\n\t" \
    "s_add_u32 s46, s42, 2490368\n\t" \
    "s_addc_u32 s47, s43, 0\n\t" \
    "s_add_u32 m0, s82, 77824\n\t" \
    "s_nop 0\n\t" \
    "global_load_lds_dwordx4 v200, s[46:47]\n\t" \
    "s_add_u32 s46, s42, 2621440\n\t" \
    "s_addc_u32 s47, s43, 0\n\t" \
    "s_add_u32 m0, s82, 81920\n\t" \
    "s_nop 0\n\t" \
    "global_load_lds_dwordx4 v200, s[46:47]\n\t" \
    "s_add_u32 s46, s42, 2752512\n\t" \
    "s_addc_u32 s47, s43, 0\n\t" \
    "s_add_u32 m0, s82, 86016\n\t" \
    "s_nop 0\n\t" \
    "global_load_lds_dwordx4 v200, s[46:47]\n\t" \
    "s_add_u32 s46, s42, 2883584\n\t" \
    "s_addc_u32 s47, s43, 0\n\t" \
    "s_add_u32 m0, s82, 90112\n\t" \
    "s_nop 0\n\t" \
    "global_load_lds_dwordx4 v200, s[46:47]\n\t" \
    "s_add_u32 s46, s42, 3014656\n\t" \
    "s_addc_u32 s47, s43, 0\n\t" \
    "s_add_u32 m0, s82, 94208\n\t" \
    "s_nop 0\n\t" \
    "global_load_lds_dwordx4 v200, s[46:47]\n\t" \
    "s_add_u32 s46, s42, 3145728\n\t" \
    "s_addc_u32 s47, s43, 0\n\t" \
    "s_add_u32 m0, s82, 98304\n\t" \
    "s_nop 0\n\t" \
    "global_load_lds_dwordx4 v200, s[46:47]\n\t" \
    "s_add_u32 s46, s42, 3276800\n\t" \
    "s_addc_u32 s47, s43, 0\n\t" \
    "s_add_u32 m0, s82, 102400\n\t" \
    "s_nop 0\n\t" \
    "global_load_lds_dwordx4 v200, s[46:47]\n\t" \
    "s_add_u32 s46, s42, 3407872\n\t" \
    "s_addc_u32 s47, s43, 0\n\t" \
    "s_add_u32 m0, s82, 106496\n\t" \
    "s_nop 0\n\t" \
    "global_load_lds_dwordx4 v200, s[46:47]\n\t" \
    "s_add_u32 s46, s42, 3538944\n\t" \
    "s_addc_u32 s47, s43, 0\n\t" \
    "s_add_u32 m0, s82, 110592\n\t" \
    "s_nop 0\n\t" \
    "global_load_lds_dwordx4 v200, s[46:47]\n\t" \
    "s_add_u32 s46, s42, 3670016\n\t" \
    "s_addc_u32 s47, s43, 0\n\t" \
    "s_add_u32 m0, s82, 114688\n\t" \
    "s_nop 0\n\t" \
    "global_load_lds_dwordx4 v200, s[46:47]\n\t" \
    "s_add_u32 s46, s42, 3801088\n\t" \
    "s_addc_u32 s47, s43, 0\n\t" \
    "s_add_u32 m0, s82, 118784\n\t" \
    "s_nop 0\n\t" \
    "global_load_lds_dwordx4 v200, s[46:47]\n\t" \
    "s_add_u32 s46, s42, 3932160\n\t" \
    "s_addc_u32 s47, s43, 0\n\t" \
    "s_add_u32 m0, s82, 122880\n\t" \
    "s_nop 0\n\t" \
    "global_load_lds_dwordx4 v200, s[46:47]\n\t" \
    "s_add_u32 s46, s42, 4063232\n\t" \
    "s_addc_u32 s47, s43, 0\n\t" \
    "s_add_u32 m0, s82, 126976\n\t" \
    "s_nop 0\n\t" \
    "global_load_lds_dwordx4 v200, s[46:47]\n\t" \
    "s_add_u32 s46, s42, 4194304\n\t" \
    "s_addc_u32 s47, s43, 0\n\t" \
    "s_add_u32 m0, s82, 131072\n\t" \
    "s_nop 0\n\t" \
    "global_load_lds_dwordx4 v200, s[46:47]\n\t" \
    "s_add_u32 s46, s42, 4325376\n\t" \
    "s_addc_u32 s47, s43, 0\n\t" \
    "s_add_u32 m0, s82, 135168\n\t" \
    "s_nop 0\n\t" \
    "global_load_lds_dwordx4 v200, s[46:47]\n\t" \
    "s_add_u32 s46, s42, 4456448\n\t" \
    "s_addc_u32 s47, s43, 0\n\t" \
    "s_add_u32 m0, s82, 139264\n\t" \
    "s_nop 0\n\t" \
    "global_load_lds_dwordx4 v200, s[46:47]\n\t" \
    "s_add_u32 s46, s42, 4587520\n\t" \
    "s_addc_u32 s47, s43, 0\n\t" \
    "s_add_u32 m0, s82, 143360\n\t" \
    "s_nop 0\n\t" \
    "global_load_lds_dwordx4 v200, s[46:47]\n\t" \
    "s_mov_b32 s77, 3019898880\n\t" \
    "s_mov_b32 s78, 3054198966\n\t" \
    "s_waitcnt lgkmcnt(0)\n\t" \
    "s_mov_b32 s70, 116\n\t" \
    "qk_E1_%=:\n\t" \
    "s_memtime s[56:57]\n\t" \
    "s_memrealtime s[2:3]\n\t" \
    "s_nop 0\n\t" \
    "s_memrealtime s[4:5]\n\t" \
    "s_nop 0\n\t" \
    "s_memrealtime s[6:7]\n\t" \
    "s_nop 0\n\t" \
    "s_memrealtime s[8:9]\n\t" \
    "s_nop 0\n\t" \
    "s_memrealtime s[10:11]\n\t" \
    "s_nop 0\n\t" \
    "s_memrealtime s[12:13]\n\t" \
    "s_nop 0\n\t" \
    "s_memrealtime s[14:15]\n\t" \
    "s_nop 0\n\t" \
    "s_memrealtime s[16:17]\n\t" \
    "s_nop 0\n\t" \
    "s_memrealtime s[18:19]\n\t" \
    "s_nop 0\n\t" \
    "s_memrealtime s[20:21]\n\t" \
    "s_nop 0\n\t" \
    "s_memrealtime s[22:23]\n\t" \
    "s_nop 0\n\t" \
    "s_memrealtime s[24:25]\n\t" \
    "s_nop 0\n\t" \
    "s_memrealtime s[26:27]\n\t" \
    "s_nop 0\n\t" \
    "s_memrealtime s[28:29]\n\t" \
    "s_memtime s[54:55]\n\t" \
    "s_add_u32 s70, s70, 8\n\t" \
    "s_waitcnt lgkmcnt(0)\n\t" \
    "s_sub_u32 s69, s54, s56\n\t" \
    "s_cmp_gt_u32 s69, s70\n\t" \
    "s_cbranch_scc1 qk_E1_%=\n\t" \
    "s_add_u32 s2, s2, s4\n\t" \
    "s_add_u32 s6, s6, s8\n\t" \
    "s_add_u32 s10, s10, s12\n\t" \
    "s_add_u32 s14, s14, s16\n\t" \
    "s_add_u32 s18, s18, s20\n\t" \
    "s_add_u32 s22, s22, s24\n\t" \
    "s_add_u32 s2, s2, s6\n\t" \
    "s_add_u32 s10, s10, s14\n\t" \
    "s_add_u32 s18, s18, s22\n\t" \
    "s_add_u32 s2, s2, s26\n\t" \
    "s_add_u32 s10, s10, s18\n\t" \
    "s_add_u32 s2, s2, s10\n\t" \
    "s_mul_i32 s70, s28, 13\n\t" \
    "s_sub_u32 s70, s70, s2\n\t" \
    "s_min_u32 s70, s70, 52\n\t" \
    "s_mul_i32 s69, s69, s70\n\t" \
    "s_mul_hi_u32 s69, s69, 76695844\n\t" \
    "s_mov_b32 s75, s73\n\t" \
    "s_mov_b32 s76, s74\n\t" \
    "s_add_u32 s73, s56, s69\n\t" \
    "s_lshl_b32 s74, s28, 4\n\t" \
    "s_waitcnt vmcnt(32) lgkmcnt(0)\n\t" \
    "s_barrier\n\t" \
    "ds_read_b64_tr_b16 a[0:1], v201 offset:0\n\t" \
    "ds_read_b64_tr_b16 a[2:3], v201 offset:1024\n\t" \
    "ds_read_b64_tr_b16 a[4:5], v201 offset:8192\n\t" \
    "ds_read_b64_tr_b16 a[6:7], v201 offset:9216\n\t" \
    "ds_read_b64_tr_b16 a[72:73], v202 offset:0\n\t" \
    "ds_read_b64_tr_b16 a[74:75], v202 offset:1024\n\t" \
    "ds_read_b64_tr_b16 a[76:77], v202 offset:8192\n\t" \
    "ds_read_b64_tr_b16 a[78:79], v202 offset:9216\n\t" \
    "s_waitcnt vmcnt(28) lgkmcnt(0)\n\t" \
    "s_barrier\n\t" \
    "ds_read_b64_tr_b16 a[8:9], v201 offset:16384\n\t" \
    "ds_read_b64_tr_b16 a[10:11], v201 offset:17408\n\t" \
    "ds_read_b64_tr_b16 a[12:13], v201 offset:24576\n\t" \
    "ds_read_b64_tr_b16 a[14:15], v201 offset:25600\n\t" \
    "ds_read_b64_tr_b16 a[80:81], v202 offset:16384\n\t" \
    "ds_read_b64_tr_b16 a[82:83], v202 offset:17408\n\t" \
    "ds_read_b64_tr_b16 a[84:85], v202 offset:24576\n\t" \
    "ds_read_b64_tr_b16 a[86:87], v202 offset:25600\n\t" \
    "s_add_u32 s46, s44, 0\n\t" \
    "s_addc_u32 s47, s45, 0\n\t" \
    "s_add_u32 m0, s82, 0\n\t" \
    "s_nop 0\n\t" \
    "global_load_lds_dwordx4 v200, s[46:47]\n\t" \
    "s_add_u32 s46, s44, 131072\n\t" \
    "s_addc_u32 s47, s45, 0\n\t" \
    "s_add_u32 m0, s82, 4096\n\t" \
    "s_nop 0\n\t" \
    "global_load_lds_dwordx4 v200, s[46:47]\n\t" \
    "s_add_u32 s46, s44, 262144\n\t" \
    "s_addc_u32 s47, s45, 0\n\t" \
    "s_add_u32 m0, s82, 8192\n\t" \
    "s_nop 0\n\t" \
    "global_load_lds_dwordx4 v200, s[46:47]\n\t" \
    "s_add_u32 s46, s44, 393216\n\t" \
    "s_addc_u32 s47, s45, 0\n\t" \
    "s_add_u32 m0, s82, 12288\n\t" \
    "s_nop 0\n\t" \
    "global_load_lds_dwordx4 v200, s[46:47]\n\t" \
    "s_waitcnt vmcnt(28) lgkmcnt(0)\n\t" \
    "s_barrier\n\t" \
    "ds_read_b64_tr_b16 a[16:17], v201 offset:32768\n\t" \
    "ds_read_b64_tr_b16 a[18:19], v201 offset:33792\n\t" \
    "ds_read_b64_tr_b16 a[20:21], v201 offset:40960\n\t" \
    "ds_read_b64_tr_b16 a[22:23], v201 offset:41984\n\t" \
    "ds_read_b64_tr_b16 a[88:89], v202 offset:32768\n\t" \
    "ds_read_b64_tr_b16 a[90:91], v202 offset:33792\n\t" \
    "ds_read_b64_tr_b16 a[92:93], v202 offset:40960\n\t" \
    "ds_read_b64_tr_b16 a[94:95], v202 offset:41984\n\t" \
    "s_add_u32 s46, s44, 524288\n\t" \
    "s_addc_u32 s47, s45, 0\n\t" \
    "s_add_u32 m0, s82, 16384\n\t" \
    "s_nop 0\n\t" \
    "global_load_lds_dwordx4 v200, s[46:47]\n\t" \
    "s_add_u32 s46, s44, 655360\n\t" \
    "s_addc_u32 s47, s45, 0\n\t" \
    "s_add_u32 m0, s82, 20480\n\t" \
    "s_nop 0\n\t" \
    "global_load_lds_dwordx4 v200, s[46:47]\n\t" \
    "s_add_u32 s46, s44, 786432\n\t" \
    "s_addc_u32 s47, s45, 0\n\t" \
    "s_add_u32 m0, s82, 24576\n\t" \
    "s_nop 0\n\t" \
    "global_load_lds_dwordx4 v200, s[46:47]\n\t" \
    "s_add_u32 s46, s44, 917504\n\t" \
    "s_addc_u32 s47, s45, 0\n\t" \
    "s_add_u32 m0, s82, 28672\n\t" \
    "s_nop 0\n\t" \
    "global_load_lds_dwordx4 v200, s[46:47]\n\t" \
    "s_waitcnt vmcnt(28) lgkmcnt(0)\n\t" \
    "s_barrier\n\t" \
    "ds_read_b64_tr_b16 a[24:25], v201 offset:49152\n\t" \
    "ds_read_b64_tr_b16 a[26:27], v201 offset:50176\n\t" \
    "ds_read_b64_tr_b16 a[28:29], v201 offset:57344\n\t" \
    "ds_read_b64_tr_b16 a[30:31], v201 offset:58368\n\t" \
    "ds_read_b64_tr_b16 a[96:97], v202 offset:49152\n\t" \
    "ds_read_b64_tr_b16 a[98:99], v202 offset:50176\n\t" \
    "ds_read_b64_tr_b16 a[100:101], v202 offset:57344\n\t" \
    "ds_read_b64_tr_b16 a[102:103], v202 offset:58368\n\t" \
    "s_add_u32 s46, s44, 1048576\n\t" \
    "s_addc_u32 s47, s45, 0\n\t" \
    "s_add_u32 m0, s82, 32768\n\t" \
    "s_nop 0\n\t" \
    "global_load_lds_dwordx4 v200, s[46:47]\n\t" \
    "s_add_u32 s46, s44, 1179648\n\t" \
    "s_addc_u32 s47, s45, 0\n\t" \
    "s_add_u32 m0, s82, 36864\n\t" \
    "s_nop 0\n\t" \
    "global_load_lds_dwordx4 v200, s[46:47]\n\t" \
    "s_add_u32 s46, s44, 1310720\n\t" \
    "s_addc_u32 s47, s45, 0\n\t" \
    "s_add_u32 m0, s82, 40960\n\t" \
    "s_nop 0\n\t" \
    "global_load_lds_dwordx4 v200, s[46:47]\n\t" \
    "s_add_u32 s46, s44, 1441792\n\t" \
    "s_addc_u32 s47, s45, 0\n\t" \
    "s_add_u32 m0, s82, 45056\n\t" \
    "s_nop 0\n\t" \
    "global_load_lds_dwordx4 v200, s[46:47]\n\t" \
    "s_waitcnt vmcnt(28) lgkmcnt(0)\n\t" \
    "s_barrier\n\t" \
    "v_add_u32 v201, 0x10000, v201\n\t" \
    "v_add_u32 v202, 0x10000, v202\n\t" \
    "ds_read_b64_tr_b16 a[32:33], v201 offset:0\n\t" \
    "ds_read_b64_tr_b16 a[34:35], v201 offset:1024\n\t" \
    "ds_read_b64_tr_b16 a[36:37], v201 offset:8192\n\t" \
    "ds_read_b64_tr_b16 a[38:39], v201 offset:9216\n\t" \
    "ds_read_b64_tr_b16 a[104:105], v202 offset:0\n\t" \
    "ds_read_b64_tr_b16 a[106:107], v202 offset:1024\n\t" \
    "ds_read_b64_tr_b16 a[108:109], v202 offset:8192\n\t" \
    "ds_read_b64_tr_b16 a[110:111], v202 offset:9216\n\t" \
    "s_add_u32 s46, s44, 1572864\n\t" \
    "s_addc_u32 s47, s45, 0\n\t" \
    "s_add_u32 m0, s82, 49152\n\t" \
    "s_nop 0\n\t" \
    "global_load_lds_dwordx4 v200, s[46:47]\n\t" \
    "s_add_u32 s46, s44, 1703936\n\t" \
    "s_addc_u32 s47, s45, 0\n\t" \
    "s_add_u32 m0, s82, 53248\n\t" \
    "s_nop 0\n\t" \
    "global_load_lds_dwordx4 v200, s[46:47]\n\t" \
    "s_add_u32 s46, s44, 1835008\n\t" \
    "s_addc_u32 s47, s45, 0\n\t" \
    "s_add_u32 m0, s82, 57344\n\t" \
    "s_nop 0\n\t" \
    "global_load_lds_dwordx4 v200, s[46:47]\n\t" \
    "s_add_u32 s46, s44, 1966080\n\t" \
    "s_addc_u32 s47, s45, 0\n\t" \
    "s_add_u32 m0, s82, 61440\n\t" \
    "s_nop 0\n\t" \
    "global_load_lds_dwordx4 v200, s[46:47]\n\t" \
    "s_waitcnt vmcnt(28) lgkmcnt(0)\n\t" \
    "s_barrier\n\t" \
    "ds_read_b64_tr_b16 a[40:41], v201 offset:16384\n\t" \
    "ds_read_b64_tr_b16 a[42:43], v201 offset:17408\n\t" \
    "ds_read_b64_tr_b16 a[44:45], v201 offset:24576\n\t" \
    "ds_read_b64_tr_b16 a[46:47], v201 offset:25600\n\t" \
    "ds_read_b64_tr_b16 a[112:113], v202 offset:16384\n\t" \
    "ds_read_b64_tr_b16 a[114:115], v202 offset:17408\n\t" \
    "ds_read_b64_tr_b16 a[116:117], v202 offset:24576\n\t" \
    "ds_read_b64_tr_b16 a[118:119], v202 offset:25600\n\t" \
    "s_add_u32 s46, s44, 2097152\n\t" \
    "s_addc_u32 s47, s45, 0\n\t" \
    "s_add_u32 m0, s82, 65536\n\t" \
    "s_nop 0\n\t" \
    "global_load_lds_dwordx4 v200, s[46:47]\n\t" \
    "s_add_u32 s46, s44, 2228224\n\t" \
    "s_addc_u32 s47, s45, 0\n\t" \
    "s_add_u32 m0, s82, 69632\n\t" \
    "s_nop 0\n\t" \
    "global_load_lds_dwordx4 v200, s[46:47]\n\t" \
    "s_add_u32 s46, s44, 2359296\n\t" \
    "s_addc_u32 s47, s45, 0\n\t" \
    "s_add_u32 m0, s82, 73728\n\t" \
    "s_nop 0\n\t" \
    "global_load_lds_dwordx4 v200, s[46:47]\n\t" \
    "s_add_u32 s46, s44, 2490368\n\t" \
    "s_addc_u32 s47, s45, 0\n\t" \
    "s_add_u32 m0, s82, 77824\n\t" \
    "s_nop 0\n\t" \
    "global_load_lds_dwordx4 v200, s[46:47]\n\t" \
    "s_waitcnt vmcnt(28) lgkmcnt(0)\n\t" \
    "s_barrier\n\t" \
    "ds_read_b64_tr_b16 a[48:49], v201 offset:32768\n\t" \
    "ds_read_b64_tr_b16 a[50:51], v201 offset:33792\n\t" \
    "ds_read_b64_tr_b16 a[52:53], v201 offset:40960\n\t" \
    "ds_read_b64_tr_b16 a[54:55], v201 offset:41984\n\t" \
    "ds_read_b64_tr_b16 a[120:121], v202 offset:32768\n\t" \
    "ds_read_b64_tr_b16 a[122:123], v202 offset:33792\n\t" \
    "ds_read_b64_tr_b16 a[124:125], v202 offset:40960\n\t" \
    "ds_read_b64_tr_b16 a[126:127], v202 offset:41984\n\t" \
    "s_add_u32 s46, s44, 2621440\n\t" \
    "s_addc_u32 s47, s45, 0\n\t" \
    "s_add_u32 m0, s82, 81920\n\t" \
    "s_nop 0\n\t" \
    "global_load_lds_dwordx4 v200, s[46:47]\n\t" \
    "s_add_u32 s46, s44, 2752512\n\t" \
    "s_addc_u32 s47, s45, 0\n\t" \
    "s_add_u32 m0, s82, 86016\n\t" \
    "s_nop 0\n\t" \
    "global_load_lds_dwordx4 v200, s[46:47]\n\t" \
    "s_add_u32 s46, s44, 2883584\n\t" \
    "s_addc_u32 s47, s45, 0\n\t" \
    "s_add_u32 m0, s82, 90112\n\t" \
    "s_nop 0\n\t" \
    "global_load_lds_dwordx4 v200, s[46:47]\n\t" \
    "s_add_u32 s46, s44, 3014656\n\t" \
    "s_addc_u32 s47, s45, 0\n\t" \
    "s_add_u32 m0, s82, 94208\n\t" \
    "s_nop 0\n\t" \
    "global_load_lds_dwordx4 v200, s[46:47]\n\t" \
    "s_waitcnt vmcnt(28) lgkmcnt(0)\n\t" \
    "s_barrier\n\t" \
    "ds_read_b64_tr_b16 a[56:57], v201 offset:49152\n\t" \
    "ds_read_b64_tr_b16 a[58:59], v201 offset:50176\n\t" \
    "ds_read_b64_tr_b16 a[60:61], v201 offset:57344\n\t" \
    "ds_read_b64_tr_b16 a[62:63], v201 offset:58368\n\t" \
    "ds_read_b64_tr_b16 a[128:129], v202 offset:49152\n\t" \
    "ds_read_b64_tr_b16 a[130:131], v202 offset:50176\n\t" \
    "ds_read_b64_tr_b16 a[132:133], v202 offset:57344\n\t" \
    "ds_read_b64_tr_b16 a[134:135], v202 offset:58368\n\t" \
    "s_add_u32 s46, s44, 3145728\n\t" \
    "s_addc_u32 s47, s45, 0\n\t" \
    "s_add_u32 m0, s82, 98304\n\t" \
    "s_nop 0\n\t" \
    "global_load_lds_dwordx4 v200, s[46:47]\n\t" \
    "s_add_u32 s46, s44, 3276800\n\t" \
    "s_addc_u32 s47, s45, 0\n\t" \
    "s_add_u32 m0, s82, 102400\n\t" \
    "s_nop 0\n\t" \
    "global_load_lds_dwordx4 v200, s[46:47]\n\t" \
    "s_add_u32 s46, s44, 3407872\n\t" \
    "s_addc_u32 s47, s45, 0\n\t" \
    "s_add_u32 m0, s82, 106496\n\t" \
    "s_nop 0\n\t" \
    "global_load_lds_dwordx4 v200, s[46:47]\n\t" \
    "s_add_u32 s46, s44, 3538944\n\t" \
    "s_addc_u32 s47, s45, 0\n\t" \
    "s_add_u32 m0, s82, 110592\n\t" \
    "s_nop 0\n\t" \
    "global_load_lds_dwordx4 v200, s[46:47]\n\t" \
    "v_mbcnt_lo_u32_b32 v160, -1, 0\n\t" \
    "v_mbcnt_hi_u32_b32 v160, -1, v160\n\t" \
    "v_lshlrev_b32 v160, 6, v160\n\t" \
    "v_add_u32 v160, 6225920, v160\n\t" \
    "v_mov_b32 v161, 6234112\n\t" \
    "global_load_dword v161, v161, s[36:37] sc1\n\t" \
    "global_load_dwordx2 v[162:163], v160, s[36:37] sc1\n\t" \
    "global_load_dword v164, v160, s[36:37] offset:2048 sc1\n\t" \
    "s_waitcnt vmcnt(31) lgkmcnt(0)\n\t" \
    "s_barrier\n\t" \
    "v_add_u32 v201, 0x10000, v201\n\t" \
    "v_add_u32 v202, 0x10000, v202\n\t" \
    "ds_read_b64_tr_b16 a[64:65], v201 offset:0\n\t" \
    "ds_read_b64_tr_b16 a[66:67], v201 offset:1024\n\t" \
    "ds_read_b64_tr_b16 a[68:69], v201 offset:8192\n\t" \
    "ds_read_b64_tr_b16 a[70:71], v201 offset:9216\n\t" \
    "ds_read_b64_tr_b16 a[136:137], v202 offset:0\n\t" \
    "ds_read_b64_tr_b16 a[138:139], v202 offset:1024\n\t" \
    "ds_read_b64_tr_b16 a[140:141], v202 offset:8192\n\t" \
    "ds_read_b64_tr_b16 a[142:143], v202 offset:9216\n\t" \
    "v_add_u32 v201, 0xfffe0000, v201\n\t" \
    "v_add_u32 v202, 0xfffe0000, v202\n\t" \
    "s_add_u32 s46, s44, 3670016\n\t" \
    "s_addc_u32 s47, s45, 0\n\t" \
    "s_add_u32 m0, s82, 114688\n\t" \
    "s_nop 0\n\t" \
    "global_load_lds_dwordx4 v200, s[46:47]\n\t" \
    "s_add_u32 s46, s44, 3801088\n\t" \
    "s_addc_u32 s47, s45, 0\n\t" \
    "s_add_u32 m0, s82, 118784\n\t" \
    "s_nop 0\n\t" \
    "global_load_lds_dwordx4 v200, s[46:47]\n\t" \
    "s_add_u32 s46, s44, 3932160\n\t" \
    "s_addc_u32 s47, s45, 0\n\t" \
    "s_add_u32 m0, s82, 122880\n\t" \
    "s_nop 0\n\t" \
    "global_load_lds_dwordx4 v200, s[46:47]\n\t" \
    "s_add_u32 s46, s44, 4063232\n\t" \
    "s_addc_u32 s47, s45, 0\n\t" \
    "s_add_u32 m0, s82, 126976\n\t" \
    "s_nop 0\n\t" \
    "global_load_lds_dwordx4 v200, s[46:47]\n\t" \
    "s_waitcnt lgkmcnt(0)\n\t" \
    "s_barrier\n\t" \
    "s_add_u32 s46, s44, 4194304\n\t" \
    "s_addc_u32 s47, s45, 0\n\t" \
    "s_add_u32 m0, s82, 131072\n\t" \
    "s_nop 0\n\t" \
    "global_load_lds_dwordx4 v200, s[46:47]\n\t" \
    "s_add_u32 s46, s44, 4325376\n\t" \
    "s_addc_u32 s47, s45, 0\n\t" \
    "s_add_u32 m0, s82, 135168\n\t" \
    "s_nop 0\n\t" \
    "global_load_lds_dwordx4 v200, s[46:47]\n\t" \
    "s_add_u32 s46, s44, 4456448\n\t" \
    "s_addc_u32 s47, s45, 0\n\t" \
    "s_add_u32 m0, s82, 139264\n\t" \
    "s_nop 0\n\t" \
    "global_load_lds_dwordx4 v200, s[46:47]\n\t" \
    "s_add_u32 s46, s44, 4587520\n\t" \
    "s_addc_u32 s47, s45, 0\n\t" \
    "s_add_u32 m0, s82, 143360\n\t" \
    "s_nop 0\n\t" \
    "global_load_lds_dwordx4 v200, s[46:47]\n\t" \
    "s_waitcnt vmcnt(35) lgkmcnt(0)\n\t" \
    "s_barrier\n\t" \
    "ds_read_b64_tr_b16 a[144:145], v201 offset:0\n\t" \
    "ds_read_b64_tr_b16 a[146:147], v201 offset:1024\n\t" \
    "ds_read_b64_tr_b16 a[148:149], v201 offset:8192\n\t" \
    "ds_read_b64_tr_b16 a[150:151], v201 offset:9216\n\t" \
    "ds_read_b64_tr_b16 a[216:217], v202 offset:0\n\t" \
    "ds_read_b64_tr_b16 a[218:219], v202 offset:1024\n\t" \
    "ds_read_b64_tr_b16 a[220:221], v202 offset:8192\n\t" \
    "ds_read_b64_tr_b16 a[222:223], v202 offset:9216\n\t" \
    "s_waitcnt vmcnt(31) lgkmcnt(0)\n\t" \
    "s_barrier\n\t" \
    "ds_read_b64_tr_b16 a[152:153], v201 offset:16384\n\t" \
    "ds_read_b64_tr_b16 a[154:155], v201 offset:17408\n\t" \
    "ds_read_b64_tr_b16 a[156:157], v201 offset:24576\n\t" \
    "ds_read_b64_tr_b16 a[158:159], v201 offset:25600\n\t" \
    "ds_read_b64_tr_b16 a[224:225], v202 offset:16384\n\t" \
    "ds_read_b64_tr_b16 a[226:227], v202 offset:17408\n\t" \
    "ds_read_b64_tr_b16 a[228:229], v202 offset:24576\n\t" \
    "ds_read_b64_tr_b16 a[230:231], v202 offset:25600\n\t" \
    "s_waitcnt vmcnt(27) lgkmcnt(0)\n\t" \
    "s_barrier\n\t" \
    "ds_read_b64_tr_b16 a[160:161], v201 offset:32768\n\t" \
    "ds_read_b64_tr_b16 a[162:163], v201 offset:33792\n\t" \
    "ds_read_b64_tr_b16 a[164:165], v201 offset:40960\n\t" \
    "ds_read_b64_tr_b16 a[166:167], v201 offset:41984\n\t" \
    "ds_read_b64_tr_b16 a[232:233], v202 offset:32768\n\t" \
    "ds_read_b64_tr_b16 a[234:235], v202 offset:33792\n\t" \
    "ds_read_b64_tr_b16 a[236:237], v202 offset:40960\n\t" \
    "ds_read_b64_tr_b16 a[238:239], v202 offset:41984\n\t" \
    "s_waitcnt vmcnt(23) lgkmcnt(0)\n\t" \
    "s_barrier\n\t" \
    "ds_read_b64_tr_b16 a[168:169], v201 offset:49152\n\t" \
    "ds_read_b64_tr_b16 a[170:171], v201 offset:50176\n\t" \
    "ds_read_b64_tr_b16 a[172:173], v201 offset:57344\n\t" \
    "ds_read_b64_tr_b16 a[174:175], v201 offset:58368\n\t" \
    "ds_read_b64_tr_b16 a[240:241], v202 offset:49152\n\t" \
    "ds_read_b64_tr_b16 a[242:243], v202 offset:50176\n\t" \
    "ds_read_b64_tr_b16 a[244:245], v202 offset:57344\n\t" \
    "ds_read_b64_tr_b16 a[246:247], v202 offset:58368\n\t" \
    "s_waitcnt vmcnt(19) lgkmcnt(0)\n\t" \
    "s_barrier\n\t" \
    "v_add_u32 v201, 0x10000, v201\n\t" \
    "v_add_u32 v202, 0x10000, v202\n\t" \
    "ds_read_b64_tr_b16 a[176:177], v201 offset:0\n\t" \
    "ds_read_b64_tr_b16 a[178:179], v201 offset:1024\n\t" \
    "ds_read_b64_tr_b16 a[180:181], v201 offset:8192\n\t" \
    "ds_read_b64_tr_b16 a[182:183], v201 offset:9216\n\t" \
    "ds_read_b64_tr_b16 a[248:249], v202 offset:0\n\t" \
    "ds_read_b64_tr_b16 a[250:251], v202 offset:1024\n\t" \
    "ds_read_b64_tr_b16 a[252:253], v202 offset:8192\n\t" \
    "ds_read_b64_tr_b16 a[254:255], v202 offset:9216\n\t" \
    "s_waitcnt vmcnt(15) lgkmcnt(0)\n\t" \
    "s_barrier\n\t" \
    "ds_read_b64_tr_b16 a[184:185], v201 offset:16384\n\t" \
    "ds_read_b64_tr_b16 a[186:187], v201 offset:17408\n\t" \
    "ds_read_b64_tr_b16 a[188:189], v201 offset:24576\n\t" \
    "ds_read_b64_tr_b16 a[190:191], v201 offset:25600\n\t" \
    "ds_read_b64_tr_b16 v[224:225], v202 offset:16384\n\t" \
    "ds_read_b64_tr_b16 v[226:227], v202 offset:17408\n\t" \
    "ds_read_b64_tr_b16 v[228:229], v202 offset:24576\n\t" \
    "ds_read_b64_tr_b16 v[230:231], v202 offset:25600\n\t" \
    "s_waitcnt vmcnt(11) lgkmcnt(0)\n\t" \
    "s_barrier\n\t" \
    "ds_read_b64_tr_b16 a[192:193], v201 offset:32768\n\t" \
    "ds_read_b64_tr_b16 a[194:195], v201 offset:33792\n\t" \
    "ds_read_b64_tr_b16 a[196:197], v201 offset:40960\n\t" \
    "ds_read_b64_tr_b16 a[198:199], v201 offset:41984\n\t" \
    "ds_read_b64_tr_b16 v[232:233], v202 offset:32768\n\t" \
    "ds_read_b64_tr_b16 v[234:235], v202 offset:33792\n\t" \
    "ds_read_b64_tr_b16 v[236:237], v202 offset:40960\n\t" \
    "ds_read_b64_tr_b16 v[238:239], v202 offset:41984\n\t" \
    "s_waitcnt vmcnt(8)\n\t" \
    "qk_C2_%=:\n\t" \
    "v_cmp_ne_u32 s[54:55], v162, v164\n\t" \
    "v_sub_u32 v162, v162, v161\n\t" \
    "v_cmp_ne_u32 vcc, 1, v162\n\t" \
    "s_nop 1\n\t" \
    "s_or_b64 vcc, vcc, s[54:55]\n\t" \
    "s_cbranch_scc0 qk_Cok3_%=\n\t" \
    "s_cmp_eq_u32 s68, 0\n\t" \
    "s_cbranch_scc1 qk_F5_%=\n\t" \
    "s_sub_u32 s68, s68, 1\n\t" \
    "s_sleep 6\n\t" \
    "s_branch qk_Cag4_%=\n\t" \
    "qk_F5_%=:\n\t" \
    "v_mov_b32 v164, 5767424\n\t" \
    "v_mov_b32 v165, 4\n\t" \
    "global_store_dword v164, v165, s[36:37] sc1\n\t" \
    "s_waitcnt vmcnt(0)\n\t" \
    "s_branch qk_Cok3_%=\n\t" \
    "qk_Cag4_%=:\n\t" \
    "global_load_dwordx2 v[162:163], v160, s[36:37] sc1\n\t" \
    "global_load_dword v164, v160, s[36:37] offset:2048 sc1\n\t" \
    "s_waitcnt vmcnt(0)\n\t" \
    "s_branch qk_C2_%=\n\t" \
    "qk_Cok3_%=:\n\t" \
    "v_readlane_b32 s69, v163, 2\n\t" \
    "v_readlane_b32 s70, v163, 3\n\t" \
    "s_nop 1\n\t" \
    "s_cmp_lt_u32 s70, s69\n\t" \
    "s_cselect_b32 s69, 0, 8192\n\t" \
    "s_add_u32 s100, s36, s69\n\t" \
    "s_addc_u32 s101, s37, 0\n\t" \
    "s_cmp_lg_u32 s66, 0\n\t" \
    "s_cbranch_scc1 qk_Ca6_%=\n\t" \
    "v_mov_b32 v160, 5768192\n\t" \
    "s_mov_b64 exec, 1\n\t" \
    "global_atomic_add v160, v162, s[36:37] sc1\n\t" \
    "s_mov_b64 exec, -1\n\t" \
    "qk_Ca6_%=:\n\t" \
    "v_mov_b32_dpp v160, v163 quad_perm:[1,0,3,2] row_mask:0xf bank_mask:0xf\n\t" \
    "v_cmp_lt_u32 s[44:45], v160, v163\n\t" \
    "s_nop 1\n\t" \
    "s_bitcmp1_b32 s44, 0\n\t" \
    "s_cselect_b32 s62, 8192, 0\n\t" \
    "s_bitcmp1_b32 s44, 2\n\t" \
    "s_cselect_b32 s63, 8192, 0\n\t" \
    "s_or_b32 s63, s63, 0x200000\n\t" \
    "v_min_u32 v160, v160, v163\n\t" \
    "v_cmp_lt_u32 vcc, 660, v160\n\t" \
    "s_nop 1\n\t" \
    "s_bcnt1_i32_b64 s69, vcc\n\t" \
    "s_cmp_gt_u32 s69, 16\n\t" \
    "s_cselect_b32 s69, 1, 0\n\t" \
    "s_mul_i32 s70, s69, 160\n\t" \
    "s_add_u32 s86, s86, s70\n\t" \
    "s_mul_i32 s70, s69, 536\n\t" \
    "s_add_u32 s87, s87, s70\n\t" \
    "s_mul_i32 s70, s69, 256\n\t" \
    "s_add_u32 s88, s88, s70\n\t" \
    "global_load_dword v25, v24, s[100:101] sc1\n\t" \
    "s_waitcnt vmcnt(5) lgkmcnt(0)\n\t" \
    "s_barrier\n\t" \
    "ds_read_b64_tr_b16 a[200:201], v201 offset:49152\n\t" \
    "ds_read_b64_tr_b16 a[202:203], v201 offset:50176\n\t" \
    "ds_read_b64_tr_b16 a[204:205], v201 offset:57344\n\t" \
    "ds_read_b64_tr_b16 a[206:207], v201 offset:58368\n\t" \
    "ds_read_b64_tr_b16 v[240:241], v202 offset:49152\n\t" \
    "ds_read_b64_tr_b16 v[242:243], v202 offset:50176\n\t" \
    "ds_read_b64_tr_b16 v[244:245], v202 offset:57344\n\t" \
    "ds_read_b64_tr_b16 v[246:247], v202 offset:58368\n\t" \
    "s_waitcnt vmcnt(1) lgkmcnt(0)\n\t" \
    "s_barrier\n\t" \
    "v_add_u32 v201, 0x10000, v201\n\t" \
    "v_add_u32 v202, 0x10000, v202\n\t" \
    "ds_read_b64_tr_b16 a[208:209], v201 offset:0\n\t" \
    "ds_read_b64_tr_b16 a[210:211], v201 offset:1024\n\t" \
    "ds_read_b64_tr_b16 a[212:213], v201 offset:8192\n\t" \
    "ds_read_b64_tr_b16 a[214:215], v201 offset:9216\n\t" \
    "ds_read_b64_tr_b16 v[248:249], v202 offset:0\n\t" \
    "ds_read_b64_tr_b16 v[250:251], v202 offset:1024\n\t" \
    "ds_read_b64_tr_b16 v[252:253], v202 offset:8192\n\t" \
    "ds_read_b64_tr_b16 v[254:255], v202 offset:9216\n\t" \
    "v_add_u32 v201, 0xfffe0000, v201\n\t" \
    "v_add_u32 v202, 0xfffe0000, v202\n\t" \
    "s_waitcnt lgkmcnt(0)\n\t" \
    "s_barrier\n\t" \
    "s_mul_i32 s70, s67, 7\n\t" \
    "s_add_u32 s69, s65, 0\n\t" \
    "s_xor_b32 s69, s69, s70\n\t" \
    "s_and_b32 s69, s69, 63\n\t" \
    "s_lshl_b32 s69, s69, 13\n\t" \
    "s_mul_i32 s70, s67, 524288\n\t" \
    "s_add_u32 s69, s69, s70\n\t" \
    "s_add_u32 s69, s69, 23068672\n\t" \
    "s_add_u32 s38, s36, s69\n\t" \
    "s_addc_u32 s39, s37, 0\n\t" \
    "v_mov_b32 v26, 0x3db504f3\n\t" \
    "v_mov_b32 v28, 0x3fb8aa3b\n\t" \
    "v_mov_b32 v27, 0x3db504f3\n\t" \
    "v_mov_b32 v29, 0x3fb8aa3b\n\t" \
    "v_xor_b32 v180, 65536, v18\n\t" \
    "v_xor_b32 v181, 131072, v18\n\t" \
    "v_xor_b32 v182, 196608, v18\n\t" \
    "s_mov_b32 s48, 0xaaaaaaaa\n\t" \
    "s_mov_b32 s49, 0xaaaaaaaa\n\t" \
    "s_mov_b32 s58, 0x00ff00ff\n\t" \
    "s_mov_b32 s59, 0x00ff00ff\n\t" \
    "v_mov_b32 v31, -1\n\t" \
    "s_mov_b32 s91, -1\n\t" \
    "ds_write_b32 v12, v31 offset:0\n\t" \
    "ds_write_b32 v12, v31 offset:64\n\t" \
    "s_waitcnt vmcnt(0)\n\t" \
    "s_cmp_gt_u32 s66, 1\n\t" \
    "s_cbranch_scc1 qk_nk7_%=\n\t" \
    "v_readfirstlane_b32 s83, v25\n\t" \
    "s_nop 1\n\t" \
    "qk_WF8_%=:\n\t" \
    "s_cmp_ge_i32 s83, s65\n\t" \
    "s_cbranch_scc1 qk_WFok9_%=\n\t" \
    "global_load_dword v160, v24, s[100:101] sc1\n\t" \
    "s_waitcnt vmcnt(0)\n\t" \
    "v_readfirstlane_b32 s83, v160\n\t" \
    "s_nop 1\n\t" \
    "s_cmp_ge_i32 s83, s65\n\t" \
    "s_cbranch_scc1 qk_WFok9_%=\n\t" \
    "s_cmp_eq_u32 s68, 0\n\t" \
    "s_cbranch_scc1 qk_F10_%=\n\t" \
    "s_sub_u32 s68, s68, 1\n\t" \
    "s_sleep 2\n\t" \
    "s_branch qk_WF8_%=\n\t" \
    "qk_F10_%=:\n\t" \
    "v_mov_b32 v160, 5767424\n\t" \
    "v_mov_b32 v161, 8\n\t" \
    "global_store_dword v160, v161, s[36:37] sc1\n\t" \
    "s_waitcnt vmcnt(0)\n\t" \
    "s_branch qk_WFok9_%=\n\t" \
    "qk_WFok9_%=:\n\t" \
    "global_load_dwordx4 v[100:103], v0, s[38:39] offset:0 sc1\n\t" \
    "global_load_dwordx4 v[104:107], v0, s[38:39] offset:2048 sc1\n\t" \
    "global_load_dwordx4 v[108:111], v15, s[38:39] offset:0 sc1\n\t" \
    "global_load_dwordx4 v[112:115], v15, s[38:39] offset:2048 sc1\n\t" \
    "global_load_dwordx4 v[116:119], v0, s[38:39] offset:1024 sc1\n\t" \
    "global_load_dwordx4 v[120:123], v0, s[38:39] offset:3072 sc1\n\t" \
    "global_load_dwordx4 v[124:127], v15, s[38:39] offset:1024 sc1\n\t" \
    "global_load_dwordx4 v[128:131], v15, s[38:39] offset:3072 sc1\n\t" \
    "qk_nk7_%=:\n\t" \
    "v_mov_b32 v33, v21\n\t" \
    "v_readlane_b32 s69, v21, 63\n\t" \
    "s_nop 1\n\t" \
    "v_mov_b32 v21, s69\n\t" \
    "s_mov_b32 s79, s74\n\t" \
    "s_mov_b32 s80, s74\n\t" \
    "s_cmp_eq_u32 s66, 1\n\t" \
    "s_cbranch_scc1 qk_loop12_%=\n\t" \
    "s_cmp_eq_u32 s66, 2\n\t" \
    "s_cbranch_scc1 qk_loop13_%=\n\t" \
    "s_cmp_eq_u32 s66, 3\n\t" \
    "s_cbranch_scc1 qk_loop14_%=\n\t" \
    "qk_loop11_%=:\n\t" \
    "v_mov_b32 v72, 0\n\t" \
    "v_mov_b32 v73, 0\n\t" \
    "v_mov_b32 v74, 0\n\t" \
    "v_mov_b32 v75, 0\n\t" \
    "v_mov_b32 v76, 0\n\t" \
    "v_mov_b32 v77, 0\n\t" \
    "v_mov_b32 v78, 0\n\t" \
    "v_mov_b32 v79, 0\n\t" \
    "v_mov_b32 v80, 0\n\t" \
    "v_mov_b32 v81, 0\n\t" \
    "v_mov_b32 v82, 0\n\t" \
    "v_mov_b32 v83, 0\n\t" \
    "v_mov_b32 v84, 0\n\t" \
    "v_mov_b32 v85, 0\n\t" \
    "v_mov_b32 v86, 0\n\t" \
    "v_mov_b32 v87, 0\n\t" \
    "v_mov_b32 v88, 0\n\t" \
    "v_mov_b32 v89, 0\n\t" \
    "v_mov_b32 v90, 0\n\t" \
    "v_mov_b32 v91, 0\n\t" \
    "v_mov_b32 v92, 0\n\t" \
    "v_mov_b32 v93, 0\n\t" \
    "v_mov_b32 v94, 0\n\t" \
    "v_mov_b32 v95, 0\n\t" \
    "v_mov_b32 v96, 0\n\t" \
    "v_mov_b32 v97, 0\n\t" \
    "v_mov_b32 v98, 0\n\t" \
    "v_mov_b32 v99, 0\n\t" \
    "s_add_u32 s69, s65, 0\n\t" \
    "s_and_b32 s69, s69, 7\n\t" \
    "s_lshl_b32 s69, s69, 18\n\t" \
    "s_add_u32 s69, s69, s62\n\t" \
    "s_add_u32 s46, s36, s69\n\t" \
    "s_addc_u32 s47, s37, 0\n\t" \
    "s_sub_u32 s70, s79, s74\n\t" \
    "s_max_i32 s70, s70, 0\n\t" \
    "s_min_i32 s70, s70, 16000\n\t" \
    "s_lshl_b32 s70, s70, 1\n\t" \
    "s_mul_hi_u32 s70, s70, s77\n\t" \
    "s_add_u32 s70, s70, s73\n\t" \
    ".p2align 6\n\t" \
    "s_nop 0\n\t" \
    "s_nop 0\n\t" \
    "s_nop 0\n\t" \
    "s_nop 0\n\t" \
    "s_nop 0\n\t" \
    "s_nop 0\n\t" \
    "qk_go16_%=:\n\t" \
    "s_getpc_b64 s[30:31]\n\t" \
    "qk_Wp18_%=:\n\t" \
    "s_add_u32 s30, s30, qk_We19_%=-qk_Wp18_%=\n\t" \
    "s_addc_u32 s31, s31, 0\n\t" \
    "s_sub_u32 s70, s70, 96\n\t" \
    "qk_W17_%=:\n\t" \
    "s_memtime s[56:57]\n\t" \
    "s_waitcnt lgkmcnt(0)\n\t" \
    "s_sub_u32 s69, s70, s56\n\t" \
    "s_cmp_gt_i32 s69, 80\n\t" \
    "s_cbranch_scc1 qk_W17_%=\n\t" \
    "s_max_i32 s69, s69, 0\n\t" \
    "s_and_b32 s69, s69, -4\n\t" \
    "s_sub_u32 s30, s30, s69\n\t" \
    "s_subb_u32 s31, s31, 0\n\t" \
    "s_setpc_b64 s[30:31]\n\t" \
    "s_nop 0\n\t" \
    "s_nop 0\n\t" \
    "s_nop 0\n\t" \
    "s_nop 0\n\t" \
    "s_nop 0\n\t" \
    "s_nop 0\n\t" \
    "s_nop 0\n\t" \
    "s_nop 0\n\t" \
    "s_nop 0\n\t" \
    "s_nop 0\n\t" \
    "s_nop 0\n\t" \
    "s_nop 0\n\t" \
    "s_nop 0\n\t" \
    "s_nop 0\n\t" \
    "s_nop 0\n\t" \
    "s_nop 0\n\t" \
    "s_nop 0\n\t" \
    "s_nop 0\n\t" \
    "s_nop 0\n\t" \
    "s_nop 0\n\t" \
    "qk_We19_%=:\n\t" \
    "qk_G20_%=:\n\t" \
    "global_load_dwordx4 v[36:39], v1, s[46:47] sc1\n\t" \
    "global_load_dword v161, v2, s[46:47] sc1\n\t" \
    "global_load_dwordx4 v[40:43], v23, s[46:47] sc1\n\t" \
    "s_waitcnt vmcnt(2)\n\t" \
    "ds_write_b128 v3, v[36:39]\n\t" \
    "v_smfmac_f32_16x16x64_bf16 v[72:75], v[36:39], a[0:7], v10\n\t" \
    "s_waitcnt vmcnt(1)\n\t" \
    "ds_write_b32 v22, v161 offset:19456\n\t" \
    "v_smfmac_f32_16x16x64_bf16 v[76:79], v[36:39], a[72:79], v10\n\t" \
    "s_waitcnt vmcnt(0)\n\t" \
    "ds_write_b128 v3, v[40:43] offset:1024\n\t" \
    "v_max3_u32 v160, v36, v40, v161\n\t" \
    "v_cmp_eq_u32 vcc, -1, v160\n\t" \
    "v_smfmac_f32_16x16x64_bf16 v[80:83], v[36:39], a[144:151], v10\n\t" \
    "v_smfmac_f32_16x16x64_bf16 v[84:87], v[36:39], a[216:223], v10\n\t" \
    "v_smfmac_f32_16x16x64_bf16 v[72:75], v[40:43], a[8:15], v10\n\t" \
    "s_cbranch_vccnz qk_Gbad21_%=\n\t" \
    "qk_Gc22_%=:\n\t" \
    "s_waitcnt lgkmcnt(0)\n\t" \
    "s_barrier\n\t" \
    "ds_read_b128 v[48:51], v4\n\t" \
    "ds_read_b128 v[52:55], v5\n\t" \
    "v_smfmac_f32_16x16x64_bf16 v[76:79], v[40:43], a[80:87], v10\n\t" \
    "ds_read_b128 v[56:59], v6\n\t" \
    "ds_read_b128 v[60:63], v7\n\t" \
    "v_smfmac_f32_16x16x64_bf16 v[80:83], v[40:43], a[152:159], v10\n\t" \
    "ds_read_b128 v[64:67], v8\n\t" \
    "v_smfmac_f32_16x16x64_bf16 v[84:87], v[40:43], a[224:231], v10\n\t" \
    "ds_read_b128 v[68:71], v9\n\t" \
    "s_waitcnt lgkmcnt(5)\n\t" \
    "v_smfmac_f32_16x16x64_bf16 v[72:75], v[48:51], a[16:23], v10\n\t" \
    "ds_read_b128 v[44:47], v0 offset:19456\n\t" \
    "s_waitcnt lgkmcnt(5)\n\t" \
    "v_smfmac_f32_16x16x64_bf16 v[72:75], v[52:55], a[24:31], v10\n\t" \
    "s_waitcnt lgkmcnt(4)\n\t" \
    "v_smfmac_f32_16x16x64_bf16 v[72:75], v[56:59], a[32:39], v10\n\t" \
    "s_waitcnt lgkmcnt(3)\n\t" \
    "v_smfmac_f32_16x16x64_bf16 v[72:75], v[60:63], a[40:47], v10\n\t" \
    "s_waitcnt lgkmcnt(2)\n\t" \
    "v_smfmac_f32_16x16x64_bf16 v[72:75], v[64:67], a[48:55], v10\n\t" \
    "s_waitcnt lgkmcnt(1)\n\t" \
    "v_smfmac_f32_16x16x64_bf16 v[72:75], v[68:71], a[56:63], v10\n\t" \
    "s_waitcnt lgkmcnt(0)\n\t" \
    "v_smfmac_f32_16x16x64_bf16 v[72:75], v[44:47], a[64:71], v10\n\t" \
    "s_nop 2\n\t" \
    "v_smfmac_f32_16x16x64_bf16 v[76:79], v[48:51], a[88:95], v10\n\t" \
    "s_nop 2\n\t" \
    "v_smfmac_f32_16x16x64_bf16 v[76:79], v[52:55], a[96:103], v10\n\t" \
    "v_add_f32 v72, v72, v74\n\t" \
    "v_smfmac_f32_16x16x64_bf16 v[76:79], v[56:59], a[104:111], v10\n\t" \
    "v_add_f32 v73, v73, v75\n\t" \
    "v_smfmac_f32_16x16x64_bf16 v[76:79], v[60:63], a[112:119], v10\n\t" \
    "v_cvt_pk_bf16_f32 v156, v72, v73\n\t" \
    "v_smfmac_f32_16x16x64_bf16 v[76:79], v[64:67], a[120:127], v10\n\t" \
    "ds_write_b32 v12, v156 offset:0\n\t" \
    "v_smfmac_f32_16x16x64_bf16 v[76:79], v[68:71], a[128:135], v10\n\t" \
    "v_smfmac_f32_16x16x64_bf16 v[76:79], v[44:47], a[136:143], v10\n\t" \
    "s_nop 2\n\t" \
    "v_smfmac_f32_16x16x64_bf16 v[80:83], v[48:51], a[160:167], v10\n\t" \
    "s_nop 2\n\t" \
    "v_smfmac_f32_16x16x64_bf16 v[80:83], v[52:55], a[168:175], v10\n\t" \
    "v_add_f32 v76, v76, v78\n\t" \
    "v_smfmac_f32_16x16x64_bf16 v[80:83], v[56:59], a[176:183], v10\n\t" \
    "v_add_f32 v77, v77, v79\n\t" \
    "v_smfmac_f32_16x16x64_bf16 v[80:83], v[60:63], a[184:191], v10\n\t" \
    "v_cvt_pk_bf16_f32 v157, v76, v77\n\t" \
    "v_smfmac_f32_16x16x64_bf16 v[80:83], v[64:67], a[192:199], v10\n\t" \
    "ds_write_b32 v12, v157 offset:64\n\t" \
    "v_smfmac_f32_16x16x64_bf16 v[80:83], v[68:71], a[200:207], v10\n\t" \
    "v_smfmac_f32_16x16x64_bf16 v[80:83], v[44:47], a[208:215], v10\n\t" \
    "s_nop 2\n\t" \
    "v_smfmac_f32_16x16x64_bf16 v[84:87], v[48:51], a[232:239], v10\n\t" \
    "s_nop 2\n\t" \
    "v_smfmac_f32_16x16x64_bf16 v[84:87], v[52:55], a[240:247], v10\n\t" \
    "v_add_f32 v80, v80, v82\n\t" \
    "v_smfmac_f32_16x16x64_bf16 v[84:87], v[56:59], a[248:255], v10\n\t" \
    "v_add_f32 v81, v81, v83\n\t" \
    "v_smfmac_f32_16x16x64_bf16 v[84:87], v[60:63], v[224:231], v10\n\t" \
    "v_cvt_pk_bf16_f32 v158, v80, v81\n\t" \
    "v_smfmac_f32_16x16x64_bf16 v[84:87], v[64:67], v[232:239], v10\n\t" \
    "ds_write_b32 v16, v158 offset:0\n\t" \
    "v_smfmac_f32_16x16x64_bf16 v[84:87], v[68:71], v[240:247], v10\n\t" \
    "ds_read_b64_tr_b16 v[132:133], v13 offset:0\n\t" \
    "ds_read_b64_tr_b16 v[134:135], v13 offset:128\n\t" \
    "v_smfmac_f32_16x16x64_bf16 v[84:87], v[44:47], v[248:255], v10\n\t" \
    "ds_read_b64_tr_b16 v[136:137], v13 offset:1024\n\t" \
    "ds_read_b64_tr_b16 v[138:139], v13 offset:1152\n\t" \
    "s_nop 4\n\t" \
    "v_cmp_u_f32 vcc, v72, v73\n\t" \
    "v_pk_add_f32 v[84:85], v[84:85], v[86:87]\n\t" \
    "v_cvt_pk_bf16_f32 v159, v84, v85\n\t" \
    "ds_write_b32 v16, v159 offset:256\n\t" \
    "s_cbranch_vccnz qk_Gslow23_%=\n\t" \
    "qk_Gback24_%=:\n\t" \
    "s_waitcnt lgkmcnt(3)\n\t" \
    "v_smfmac_f32_16x16x64_bf16 v[88:91], v[132:135], v[100:107], v10\n\t" \
    "s_waitcnt lgkmcnt(1)\n\t" \
    "v_smfmac_f32_16x16x64_bf16 v[88:91], v[136:139], v[108:115], v10\n\t" \
    "s_waitcnt lgkmcnt(0)\n\t" \
    "s_barrier\n\t" \
    "ds_read_b64_tr_b16 v[140:141], v17 offset:0\n\t" \
    "v_smfmac_f32_16x16x64_bf16 v[92:95], v[132:135], v[116:123], v10\n\t" \
    "ds_read_b64_tr_b16 v[142:143], v17 offset:256\n\t" \
    "ds_read_b64_tr_b16 v[144:145], v17 offset:512\n\t" \
    "ds_read_b64_tr_b16 v[146:147], v17 offset:768\n\t" \
    "ds_read_b64_tr_b16 v[148:149], v17 offset:1024\n\t" \
    "v_smfmac_f32_16x16x64_bf16 v[92:95], v[136:139], v[124:131], v10\n\t" \
    "ds_read_b64_tr_b16 v[150:151], v17 offset:1280\n\t" \
    "ds_read_b64_tr_b16 v[152:153], v17 offset:1536\n\t" \
    "ds_read_b64_tr_b16 v[154:155], v17 offset:1792\n\t" \
    "v_smfmac_f32_16x16x64_bf16 v[184:187], v[132:135], v[100:107], v10\n\t" \
    "qk_qb26_%=:\n\t" \
    "s_waitcnt lgkmcnt(4)\n\t" \
    "v_smfmac_f32_16x16x64_bf16 v[96:99], v[132:135], v[140:147], v10\n\t" \
    "s_waitcnt lgkmcnt(0)\n\t" \
    "v_smfmac_f32_16x16x64_bf16 v[96:99], v[136:139], v[148:155], v10\n\t" \
    "v_add_f32 v168, v88, v90\n\t" \
    "v_add_f32 v169, v92, v94\n\t" \
    "v_cmp_u_f32 vcc, v168, v169\n\t" \
    "v_cvt_pk_bf16_f32 v168, 0, v168\n\t" \
    "v_cvt_pk_bf16_f32 v169, 0, v169\n\t" \
    "v_mul_f32 v168, v168, v26\n\t" \
    "v_mul_f32 v169, v169, v26\n\t" \
    "v_cvt_pk_bf16_f32 v168, 0, v168\n\t" \
    "v_add_f32 v172, v96, v98\n\t" \
    "v_cvt_pk_bf16_f32 v169, 0, v169\n\t" \
    "v_max_f32 v178, v168, v169\n\t" \
    "v_cvt_pk_bf16_f32 v172, 0, v172\n\t" \
    "v_mul_f32 v172, v172, v26\n\t" \
    "v_max_f32_dpp v178, v178, v178 row_mirror row_mask:0xf bank_mask:0xf\n\t" \
    "v_cvt_pk_bf16_f32 v172, 0, v172\n\t" \
    "v_max_f32 v173, v178, v172\n\t" \
    "v_mul_f32 v168, v168, v28\n\t" \
    "v_mul_f32 v169, v169, v28\n\t" \
    "v_max_f32_dpp v173, v173, v173 quad_perm:[1,0,3,2] row_mask:0xf bank_mask:0xf\n\t" \
    "v_mul_f32 v172, v172, v28\n\t" \
    "s_cbranch_vccnz qk_qs25_%=\n\t" \
    "qk_qb227_%=:\n\t" \
    "v_max_f32_dpp v173, v173, v173 quad_perm:[2,3,0,1] row_mask:0xf bank_mask:0xf\n\t" \
    "s_nop 1\n\t" \
    "v_max_f32_dpp v173, v173, v173 row_half_mirror row_mask:0xf bank_mask:0xf\n\t" \
    "v_fma_f32 v172, -v173, v28, v172\n\t" \
    "v_fma_f32 v168, -v173, v28, v168\n\t" \
    "v_fma_f32 v169, -v173, v28, v169\n\t" \
    "v_exp_f32 v172, v172\n\t" \
    "v_exp_f32 v168, v168\n\t" \
    "v_exp_f32 v169, v169\n\t" \
    "v_fma_f32 v173, v172, 0.5, v168\n\t" \
    "v_add_f32 v173, v173, v169\n\t" \
    "v_mov_b32_dpp v174, v172 quad_perm:[0,0,2,2] row_mask:0xf bank_mask:0xf\n\t" \
    "v_mov_b32_dpp v175, v172 quad_perm:[1,1,3,3] row_mask:0xf bank_mask:0xf\n\t" \
    "v_add_f32_dpp v173, v173, v173 quad_perm:[1,0,3,2] row_mask:0xf bank_mask:0xf\n\t" \
    "v_mov_b32_dpp v176, v168 quad_perm:[1,0,3,2] row_mask:0xf bank_mask:0xf\n\t" \
    "v_mov_b32_dpp v177, v169 quad_perm:[1,0,3,2] row_mask:0xf bank_mask:0xf\n\t" \
    "v_add_f32_dpp v173, v173, v173 quad_perm:[2,3,0,1] row_mask:0xf bank_mask:0xf\n\t" \
    "v_cndmask_b32 v168, v168, v177, s[48:49]\n\t" \
    "v_cndmask_b32 v169, v176, v169, s[48:49]\n\t" \
    "v_add_f32_dpp v173, v173, v173 row_half_mirror row_mask:0xf bank_mask:0xf\n\t" \
    "s_nop 1\n\t" \
    "v_add_f32_dpp v173, v173, v173 row_mirror row_mask:0xf bank_mask:0xf\n\t" \
    "v_rcp_f32 v173, v173\n\t" \
    "s_nop 0\n\t" \
    "v_pk_mul_f32 v[168:169], v[168:169], v[172:173] op_sel:[0,1] op_sel_hi:[1,1]\n\t" \
    "v_cvt_pk_bf16_f32 v176, v168, v169\n\t" \
    "global_store_dword v18, v176, s[46:47] sc1\n\t" \
    "global_store_dword v180, v176, s[46:47] sc1\n\t" \
    "global_store_dword v181, v176, s[46:47] sc1\n\t" \
    "global_store_dword v182, v176, s[46:47] sc1\n\t" \
    "v_pk_mul_f32 v[174:175], v[174:175], v[172:173] op_sel:[0,1] op_sel_hi:[1,1]\n\t" \
    "v_cvt_pk_bf16_f32 v177, v174, v175\n\t" \
    "global_store_dword v19, v177, s[46:47] sc1\n\t" \
    "s_cmp_eq_u32 s65, 63\n\t" \
    "s_cbranch_scc1 qk_last15_%=\n\t" \
    "v_readfirstlane_b32 s69, v25\n\t" \
    "s_add_u32 s65, s65, 1\n\t" \
    "s_max_i32 s83, s83, s69\n\t" \
    "s_cmp_gt_u32 s65, 6\n\t" \
    "s_cselect_b32 s83, 99, s83\n\t" \
    "v_mov_b32 v72, 0\n\t" \
    "v_mov_b32 v73, 0\n\t" \
    "v_mov_b32 v74, 0\n\t" \
    "v_mov_b32 v75, 0\n\t" \
    "v_mov_b32 v76, 0\n\t" \
    "v_mov_b32 v77, 0\n\t" \
    "v_mov_b32 v78, 0\n\t" \
    "v_mov_b32 v79, 0\n\t" \
    "v_mov_b32 v80, 0\n\t" \
    "v_mov_b32 v81, 0\n\t" \
    "v_mov_b32 v82, 0\n\t" \
    "v_mov_b32 v83, 0\n\t" \
    "v_mov_b32 v84, 0\n\t" \
    "v_mov_b32 v85, 0\n\t" \
    "v_mov_b32 v86, 0\n\t" \
    "v_mov_b32 v87, 0\n\t" \
    "v_mov_b32 v88, 0\n\t" \
    "v_mov_b32 v89, 0\n\t" \
    "v_mov_b32 v90, 0\n\t" \
    "v_mov_b32 v91, 0\n\t" \
    "v_mov_b32 v92, 0\n\t" \
    "v_mov_b32 v93, 0\n\t" \
    "v_mov_b32 v94, 0\n\t" \
    "v_mov_b32 v95, 0\n\t" \
    "v_mov_b32 v96, 0\n\t" \
    "v_mov_b32 v97, 0\n\t" \
    "v_mov_b32 v98, 0\n\t" \
    "v_mov_b32 v99, 0\n\t" \
    "s_add_u32 s69, s65, 0\n\t" \
    "s_and_b32 s69, s69, 7\n\t" \
    "s_lshl_b32 s69, s69, 18\n\t" \
    "s_add_u32 s69, s69, s62\n\t" \
    "s_add_u32 s46, s36, s69\n\t" \
    "s_addc_u32 s47, s37, 0\n\t" \
    "s_mul_i32 s70, s67, 7\n\t" \
    "s_add_u32 s69, s65, 0\n\t" \
    "s_xor_b32 s69, s69, s70\n\t" \
    "s_and_b32 s69, s69, 63\n\t" \
    "s_lshl_b32 s69, s69, 13\n\t" \
    "s_mul_i32 s70, s67, 524288\n\t" \
    "s_add_u32 s69, s69, s70\n\t" \
    "s_add_u32 s69, s69, 23068672\n\t" \
    "s_add_u32 s38, s36, s69\n\t" \
    "s_addc_u32 s39, s37, 0\n\t" \
    "qk_WF28_%=:\n\t" \
    "s_cmp_ge_i32 s83, s65\n\t" \
    "s_cbranch_scc1 qk_WFok29_%=\n\t" \
    "global_load_dword v160, v24, s[100:101] sc1\n\t" \
    "s_waitcnt vmcnt(0)\n\t" \
    "v_readfirstlane_b32 s83, v160\n\t" \
    "s_nop 1\n\t" \
    "s_cmp_ge_i32 s83, s65\n\t" \
    "s_cbranch_scc1 qk_WFok29_%=\n\t" \
    "s_cmp_eq_u32 s68, 0\n\t" \
    "s_cbranch_scc1 qk_F30_%=\n\t" \
    "s_sub_u32 s68, s68, 1\n\t" \
    "s_sleep 2\n\t" \
    "s_branch qk_WF28_%=\n\t" \
    "qk_F30_%=:\n\t" \
    "v_mov_b32 v160, 5767424\n\t" \
    "v_mov_b32 v161, 9\n\t" \
    "global_store_dword v160, v161, s[36:37] sc1\n\t" \
    "s_waitcnt vmcnt(0)\n\t" \
    "s_branch qk_WFok29_%=\n\t" \
    "qk_WFok29_%=:\n\t" \
    "global_load_dwordx4 v[100:103], v0, s[38:39] offset:0 sc1\n\t" \
    "global_load_dwordx4 v[104:107], v0, s[38:39] offset:2048 sc1\n\t" \
    "global_load_dwordx4 v[108:111], v15, s[38:39] offset:0 sc1\n\t" \
    "global_load_dwordx4 v[112:115], v15, s[38:39] offset:2048 sc1\n\t" \
    "global_load_dwordx4 v[116:119], v0, s[38:39] offset:1024 sc1\n\t" \
    "global_load_dwordx4 v[120:123], v0, s[38:39] offset:3072 sc1\n\t" \
    "global_load_dwordx4 v[124:127], v15, s[38:39] offset:1024 sc1\n\t" \
    "global_load_dwordx4 v[128:131], v15, s[38:39] offset:3072 sc1\n\t" \
    "s_cmp_gt_i32 s83, s65\n\t" \
    "s_cbranch_scc1 qk_nf31_%=\n\t" \
    "global_load_dword v25, v24, s[100:101] sc1\n\t" \
    "qk_nf31_%=:\n\t" \
    "s_barrier\n\t" \
    "ds_read_b32 v168, v11 offset:20480\n\t" \
    "s_waitcnt lgkmcnt(0)\n\t" \
    "v_readfirstlane_b32 s70, v168\n\t" \
    "ds_write_b32 v12, v31 offset:0\n\t" \
    "ds_write_b32 v12, v31 offset:64\n\t" \
    "s_branch qk_go16_%=\n\t" \
    "qk_Gbad21_%=:\n\t" \
    "s_waitcnt lgkmcnt(0)\n\t" \
    "s_nop 7\n\t" \
    "v_mov_b32 v72, 0\n\t" \
    "v_mov_b32 v73, 0\n\t" \
    "v_mov_b32 v74, 0\n\t" \
    "v_mov_b32 v75, 0\n\t" \
    "v_mov_b32 v76, 0\n\t" \
    "v_mov_b32 v77, 0\n\t" \
    "v_mov_b32 v78, 0\n\t" \
    "v_mov_b32 v79, 0\n\t" \
    "v_mov_b32 v80, 0\n\t" \
    "v_mov_b32 v81, 0\n\t" \
    "v_mov_b32 v82, 0\n\t" \
    "v_mov_b32 v83, 0\n\t" \
    "v_mov_b32 v84, 0\n\t" \
    "v_mov_b32 v85, 0\n\t" \
    "v_mov_b32 v86, 0\n\t" \
    "v_mov_b32 v87, 0\n\t" \
    "s_cmp_eq_u32 s68, 0\n\t" \
    "s_cbranch_scc1 qk_F32_%=\n\t" \
    "s_sub_u32 s68, s68, 1\n\t" \
    "s_branch qk_G20_%=\n\t" \
    "qk_F32_%=:\n\t" \
    "v_mov_b32 v160, 5767424\n\t" \
    "v_mov_b32 v161, 1\n\t" \
    "global_store_dword v160, v161, s[36:37] sc1\n\t" \
    "s_waitcnt vmcnt(0)\n\t" \
    "s_branch qk_Gc22_%=\n\t" \
    "qk_Gslow23_%=:\n\t" \
    "v_max_u32 v160, v36, v37\n\t" \
    "v_max3_u32 v160, v160, v38, v39\n\t" \
    "v_max3_u32 v160, v160, v40, v41\n\t" \
    "v_max3_u32 v160, v160, v42, v43\n\t" \
    "v_max3_u32 v160, v160, v44, v45\n\t" \
    "v_max3_u32 v160, v160, v46, v47\n\t" \
    "v_max3_u32 v160, v160, v48, v49\n\t" \
    "v_max3_u32 v160, v160, v50, v51\n\t" \
    "v_max3_u32 v160, v160, v52, v53\n\t" \
    "v_max3_u32 v160, v160, v54, v55\n\t" \
    "v_max3_u32 v160, v160, v56, v57\n\t" \
    "v_max3_u32 v160, v160, v58, v59\n\t" \
    "v_max3_u32 v160, v160, v60, v61\n\t" \
    "v_max3_u32 v160, v160, v62, v63\n\t" \
    "v_max3_u32 v160, v160, v64, v65\n\t" \
    "v_max3_u32 v160, v160, v66, v67\n\t" \
    "v_max3_u32 v160, v160, v68, v69\n\t" \
    "v_max3_u32 v160, v160, v70, v71\n\t" \
    "v_cmp_eq_u32 vcc, -1, v160\n\t" \
    "s_cbranch_vccz qk_Gback24_%=\n\t" \
    "s_waitcnt lgkmcnt(0)\n\t" \
    "v_sub_u32 v160, v1, v3\n\t" \
    "s_nop 0\n\t" \
    "v_readfirstlane_b32 s69, v160\n\t" \
    "v_add_u32 v163, 20480, v0\n\t" \
    "s_nop 7\n\t" \
    "s_add_u32 s30, s46, s69\n\t" \
    "s_addc_u32 s31, s47, 0\n\t" \
    "qk_Gag33_%=:\n\t" \
    "global_load_dwordx4 v[36:39], v1, s[46:47] sc1\n\t" \
    "global_load_dwordx4 v[40:43], v23, s[46:47] sc1\n\t" \
    "global_load_dwordx4 v[44:47], v163, s[30:31] sc1\n\t" \
    "global_load_dwordx4 v[48:51], v4, s[30:31] sc1\n\t" \
    "global_load_dwordx4 v[52:55], v5, s[30:31] sc1\n\t" \
    "global_load_dwordx4 v[56:59], v6, s[30:31] sc1\n\t" \
    "global_load_dwordx4 v[60:63], v7, s[30:31] sc1\n\t" \
    "global_load_dwordx4 v[64:67], v8, s[30:31] sc1\n\t" \
    "global_load_dwordx4 v[68:71], v9, s[30:31] sc1\n\t" \
    "s_waitcnt vmcnt(0)\n\t" \
    "v_max_u32 v160, v36, v37\n\t" \
    "v_max3_u32 v160, v160, v38, v39\n\t" \
    "v_max3_u32 v160, v160, v40, v41\n\t" \
    "v_max3_u32 v160, v160, v42, v43\n\t" \
    "v_max3_u32 v160, v160, v44, v45\n\t" \
    "v_max3_u32 v160, v160, v46, v47\n\t" \
    "v_max3_u32 v160, v160, v48, v49\n\t" \
    "v_max3_u32 v160, v160, v50, v51\n\t" \
    "v_max3_u32 v160, v160, v52, v53\n\t" \
    "v_max3_u32 v160, v160, v54, v55\n\t" \
    "v_max3_u32 v160, v160, v56, v57\n\t" \
    "v_max3_u32 v160, v160, v58, v59\n\t" \
    "v_max3_u32 v160, v160, v60, v61\n\t" \
    "v_max3_u32 v160, v160, v62, v63\n\t" \
    "v_max3_u32 v160, v160, v64, v65\n\t" \
    "v_max3_u32 v160, v160, v66, v67\n\t" \
    "v_max3_u32 v160, v160, v68, v69\n\t" \
    "v_max3_u32 v160, v160, v70, v71\n\t" \
    "v_cmp_eq_u32 vcc, -1, v160\n\t" \
    "s_cbranch_vccz qk_Gredo34_%=\n\t" \
    "s_cmp_eq_u32 s68, 0\n\t" \
    "s_cbranch_scc1 qk_F35_%=\n\t" \
    "s_sub_u32 s68, s68, 1\n\t" \
    "s_sleep 1\n\t" \
    "s_branch qk_Gag33_%=\n\t" \
    "qk_F35_%=:\n\t" \
    "v_mov_b32 v160, 5767424\n\t" \
    "v_mov_b32 v161, 2\n\t" \
    "global_store_dword v160, v161, s[36:37] sc1\n\t" \
    "s_waitcnt vmcnt(0)\n\t" \
    "s_branch qk_Gredo34_%=\n\t" \
    "qk_Gredo34_%=:\n\t" \
    "v_mov_b32 v72, 0\n\t" \
    "v_mov_b32 v73, 0\n\t" \
    "v_mov_b32 v74, 0\n\t" \
    "v_mov_b32 v75, 0\n\t" \
    "v_mov_b32 v76, 0\n\t" \
    "v_mov_b32 v77, 0\n\t" \
    "v_mov_b32 v78, 0\n\t" \
    "v_mov_b32 v79, 0\n\t" \
    "v_mov_b32 v80, 0\n\t" \
    "v_mov_b32 v81, 0\n\t" \
    "v_mov_b32 v82, 0\n\t" \
    "v_mov_b32 v83, 0\n\t" \
    "v_mov_b32 v84, 0\n\t" \
    "v_mov_b32 v85, 0\n\t" \
    "v_mov_b32 v86, 0\n\t" \
    "v_mov_b32 v87, 0\n\t" \
    "v_smfmac_f32_16x16x64_bf16 v[72:75], v[36:39], a[0:7], v10\n\t" \
    "v_smfmac_f32_16x16x64_bf16 v[72:75], v[40:43], a[8:15], v10\n\t" \
    "v_smfmac_f32_16x16x64_bf16 v[72:75], v[48:51], a[16:23], v10\n\t" \
    "v_smfmac_f32_16x16x64_bf16 v[72:75], v[52:55], a[24:31], v10\n\t" \
    "v_smfmac_f32_16x16x64_bf16 v[72:75], v[56:59], a[32:39], v10\n\t" \
    "v_smfmac_f32_16x16x64_bf16 v[72:75], v[60:63], a[40:47], v10\n\t" \
    "v_smfmac_f32_16x16x64_bf16 v[72:75], v[64:67], a[48:55], v10\n\t" \
    "v_smfmac_f32_16x16x64_bf16 v[72:75], v[68:71], a[56:63], v10\n\t" \
    "v_smfmac_f32_16x16x64_bf16 v[72:75], v[44:47], a[64:71], v10\n\t" \
    "v_smfmac_f32_16x16x64_bf16 v[76:79], v[36:39], a[72:79], v10\n\t" \
    "v_smfmac_f32_16x16x64_bf16 v[76:79], v[40:43], a[80:87], v10\n\t" \
    "v_smfmac_f32_16x16x64_bf16 v[76:79], v[48:51], a[88:95], v10\n\t" \
    "v_smfmac_f32_16x16x64_bf16 v[76:79], v[52:55], a[96:103], v10\n\t" \
    "v_smfmac_f32_16x16x64_bf16 v[76:79], v[56:59], a[104:111], v10\n\t" \
    "v_smfmac_f32_16x16x64_bf16 v[76:79], v[60:63], a[112:119], v10\n\t" \
    "v_smfmac_f32_16x16x64_bf16 v[76:79], v[64:67], a[120:127], v10\n\t" \
    "v_smfmac_f32_16x16x64_bf16 v[76:79], v[68:71], a[128:135], v10\n\t" \
    "v_smfmac_f32_16x16x64_bf16 v[76:79], v[44:47], a[136:143], v10\n\t" \
    "v_smfmac_f32_16x16x64_bf16 v[80:83], v[36:39], a[144:151], v10\n\t" \
    "v_smfmac_f32_16x16x64_bf16 v[80:83], v[40:43], a[152:159], v10\n\t" \
    "v_smfmac_f32_16x16x64_bf16 v[80:83], v[48:51], a[160:167], v10\n\t" \
    "v_smfmac_f32_16x16x64_bf16 v[80:83], v[52:55], a[168:175], v10\n\t" \
    "v_smfmac_f32_16x16x64_bf16 v[80:83], v[56:59], a[176:183], v10\n\t" \
    "v_smfmac_f32_16x16x64_bf16 v[80:83], v[60:63], a[184:191], v10\n\t" \
    "v_smfmac_f32_16x16x64_bf16 v[80:83], v[64:67], a[192:199], v10\n\t" \
    "v_smfmac_f32_16x16x64_bf16 v[80:83], v[68:71], a[200:207], v10\n\t" \
    "v_smfmac_f32_16x16x64_bf16 v[80:83], v[44:47], a[208:215], v10\n\t" \
    "v_smfmac_f32_16x16x64_bf16 v[84:87], v[36:39], a[216:223], v10\n\t" \
    "v_smfmac_f32_16x16x64_bf16 v[84:87], v[40:43], a[224:231], v10\n\t" \
    "v_smfmac_f32_16x16x64_bf16 v[84:87], v[48:51], a[232:239], v10\n\t" \
    "v_smfmac_f32_16x16x64_bf16 v[84:87], v[52:55], a[240:247], v10\n\t" \
    "v_smfmac_f32_16x16x64_bf16 v[84:87], v[56:59], a[248:255], v10\n\t" \
    "v_smfmac_f32_16x16x64_bf16 v[84:87], v[60:63], v[224:231], v10\n\t" \
    "v_smfmac_f32_16x16x64_bf16 v[84:87], v[64:67], v[232:239], v10\n\t" \
    "v_smfmac_f32_16x16x64_bf16 v[84:87], v[68:71], v[240:247], v10\n\t" \
    "v_smfmac_f32_16x16x64_bf16 v[84:87], v[44:47], v[248:255], v10\n\t" \
    "v_add_f32 v72, v72, v74\n\t" \
    "v_add_f32 v73, v73, v75\n\t" \
    "v_cvt_pk_bf16_f32 v156, v72, v73\n\t" \
    "ds_write_b32 v12, v156 offset:0\n\t" \
    "v_add_f32 v76, v76, v78\n\t" \
    "v_add_f32 v77, v77, v79\n\t" \
    "v_cvt_pk_bf16_f32 v157, v76, v77\n\t" \
    "ds_write_b32 v12, v157 offset:64\n\t" \
    "v_add_f32 v80, v80, v82\n\t" \
    "v_add_f32 v81, v81, v83\n\t" \
    "v_cvt_pk_bf16_f32 v158, v80, v81\n\t" \
    "ds_write_b32 v16, v158 offset:0\n\t" \
    "v_pk_add_f32 v[84:85], v[84:85], v[86:87]\n\t" \
    "v_cvt_pk_bf16_f32 v159, v84, v85\n\t" \
    "ds_write_b32 v16, v159 offset:256\n\t" \
    "s_branch qk_Gback24_%=\n\t" \
    "qk_qs25_%=:\n\t" \
    "s_cmp_eq_u32 s91, s65\n\t" \
    "s_cbranch_scc1 qk_qb227_%=\n\t" \
    "s_mov_b32 s91, s65\n\t" \
    "s_nop 7\n\t" \
    "ds_read_b64_tr_b16 v[132:133], v13 offset:0\n\t" \
    "ds_read_b64_tr_b16 v[134:135], v13 offset:128\n\t" \
    "ds_read_b64_tr_b16 v[136:137], v13 offset:1024\n\t" \
    "ds_read_b64_tr_b16 v[138:139], v13 offset:1152\n\t" \
    "v_mov_b32 v88, 0\n\t" \
    "v_mov_b32 v89, 0\n\t" \
    "v_mov_b32 v90, 0\n\t" \
    "v_mov_b32 v91, 0\n\t" \
    "v_mov_b32 v92, 0\n\t" \
    "v_mov_b32 v93, 0\n\t" \
    "v_mov_b32 v94, 0\n\t" \
    "v_mov_b32 v95, 0\n\t" \
    "v_mov_b32 v96, 0\n\t" \
    "v_mov_b32 v97, 0\n\t" \
    "v_mov_b32 v98, 0\n\t" \
    "v_mov_b32 v99, 0\n\t" \
    "s_waitcnt lgkmcnt(0)\n\t" \
    "v_smfmac_f32_16x16x64_bf16 v[88:91], v[132:135], v[100:107], v10\n\t" \
    "v_smfmac_f32_16x16x64_bf16 v[92:95], v[132:135], v[116:123], v10\n\t" \
    "v_smfmac_f32_16x16x64_bf16 v[88:91], v[136:139], v[108:115], v10\n\t" \
    "v_smfmac_f32_16x16x64_bf16 v[92:95], v[136:139], v[124:131], v10\n\t" \
    "s_nop 7\n\t" \
    "s_branch qk_qb26_%=\n\t" \
    "qk_last15_%=:\n\t" \
    "s_endpgm\n\t" \
    "qk_loop12_%=:\n\t" \
    "v_mov_b32 v72, 0\n\t" \
    "v_mov_b32 v73, 0\n\t" \
    "v_mov_b32 v74, 0\n\t" \
    "v_mov_b32 v75, 0\n\t" \
    "v_mov_b32 v76, 0\n\t" \
    "v_mov_b32 v77, 0\n\t" \
    "v_mov_b32 v78, 0\n\t" \
    "v_mov_b32 v79, 0\n\t" \
    "v_mov_b32 v80, 0\n\t" \
    "v_mov_b32 v81, 0\n\t" \
    "v_mov_b32 v82, 0\n\t" \
    "v_mov_b32 v83, 0\n\t" \
    "v_mov_b32 v84, 0\n\t" \
    "v_mov_b32 v85, 0\n\t" \
    "v_mov_b32 v86, 0\n\t" \
    "v_mov_b32 v87, 0\n\t" \
    "v_mov_b32 v88, 0\n\t" \
    "v_mov_b32 v89, 0\n\t" \
    "v_mov_b32 v90, 0\n\t" \
    "v_mov_b32 v91, 0\n\t" \
    "v_mov_b32 v92, 0\n\t" \
    "v_mov_b32 v93, 0\n\t" \
    "v_mov_b32 v94, 0\n\t" \
    "v_mov_b32 v95, 0\n\t" \
    "v_mov_b32 v96, 0\n\t" \
    "v_mov_b32 v97, 0\n\t" \
    "v_mov_b32 v98, 0\n\t" \
    "v_mov_b32 v99, 0\n\t" \
    "s_add_u32 s69, s65, 0\n\t" \
    "s_and_b32 s69, s69, 7\n\t" \
    "s_lshl_b32 s69, s69, 18\n\t" \
    "s_add_u32 s69, s69, s62\n\t" \
    "s_add_u32 s46, s36, s69\n\t" \
    "s_addc_u32 s47, s37, 0\n\t" \
    "s_sub_u32 s70, s79, s74\n\t" \
    "s_max_i32 s70, s70, 0\n\t" \
    "s_min_i32 s70, s70, 16000\n\t" \
    "s_lshl_b32 s70, s70, 1\n\t" \
    "s_mul_hi_u32 s70, s70, s77\n\t" \
    "s_add_u32 s70, s70, s73\n\t" \
    ".p2align 6\n\t" \
    "s_nop 0\n\t" \
    "s_nop 0\n\t" \
    "s_nop 0\n\t" \
    "s_nop 0\n\t" \
    "s_nop 0\n\t" \
    "s_nop 0\n\t" \
    "qk_go37_%=:\n\t" \
    "s_getpc_b64 s[30:31]\n\t" \
    "qk_Wp39_%=:\n\t" \
    "s_add_u32 s30, s30, qk_We40_%=-qk_Wp39_%=\n\t" \
    "s_addc_u32 s31, s31, 0\n\t" \
    "s_sub_u32 s70, s70, 96\n\t" \
    "qk_W38_%=:\n\t" \
    "s_memtime s[56:57]\n\t" \
    "s_waitcnt lgkmcnt(0)\n\t" \
    "s_sub_u32 s69, s70, s56\n\t" \
    "s_cmp_gt_i32 s69, 80\n\t" \
    "s_cbranch_scc1 qk_W38_%=\n\t" \
    "s_max_i32 s69, s69, 0\n\t" \
    "s_and_b32 s69, s69, -4\n\t" \
    "s_sub_u32 s30, s30, s69\n\t" \
    "s_subb_u32 s31, s31, 0\n\t" \
    "s_setpc_b64 s[30:31]\n\t" \
    "s_nop 0\n\t" \
    "s_nop 0\n\t" \
    "s_nop 0\n\t" \
    "s_nop 0\n\t" \
    "s_nop 0\n\t" \
    "s_nop 0\n\t" \
    "s_nop 0\n\t" \
    "s_nop 0\n\t" \
    "s_nop 0\n\t" \
    "s_nop 0\n\t" \
    "s_nop 0\n\t" \
    "s_nop 0\n\t" \
    "s_nop 0\n\t" \
    "s_nop 0\n\t" \
    "s_nop 0\n\t" \
    "s_nop 0\n\t" \
    "s_nop 0\n\t" \
    "s_nop 0\n\t" \
    "s_nop 0\n\t" \
    "s_nop 0\n\t" \
    "qk_We40_%=:\n\t" \
    "qk_G41_%=:\n\t" \
    "global_load_dwordx4 v[36:39], v1, s[46:47] sc1\n\t" \
    "global_load_dword v161, v2, s[46:47] sc1\n\t" \
    "global_load_dwordx4 v[40:43], v23, s[46:47] sc1\n\t" \
    "s_waitcnt vmcnt(2)\n\t" \
    "ds_write_b128 v3, v[36:39]\n\t" \
    "v_smfmac_f32_16x16x64_bf16 v[72:75], v[36:39], a[16:23], v10\n\t" \
    "s_waitcnt vmcnt(1)\n\t" \
    "ds_write_b32 v22, v161 offset:19712\n\t" \
    "v_smfmac_f32_16x16x64_bf16 v[76:79], v[36:39], a[88:95], v10\n\t" \
    "s_waitcnt vmcnt(0)\n\t" \
    "ds_write_b128 v3, v[40:43] offset:1024\n\t" \
    "v_max3_u32 v160, v36, v40, v161\n\t" \
    "v_cmp_eq_u32 vcc, -1, v160\n\t" \
    "v_smfmac_f32_16x16x64_bf16 v[80:83], v[36:39], a[160:167], v10\n\t" \
    "v_smfmac_f32_16x16x64_bf16 v[84:87], v[36:39], a[232:239], v10\n\t" \
    "v_smfmac_f32_16x16x64_bf16 v[72:75], v[40:43], a[24:31], v10\n\t" \
    "s_cbranch_vccnz qk_Gbad42_%=\n\t" \
    "qk_Gc43_%=:\n\t" \
    "s_waitcnt lgkmcnt(0)\n\t" \
    "s_barrier\n\t" \
    "ds_read_b128 v[48:51], v4\n\t" \
    "ds_read_b128 v[52:55], v5\n\t" \
    "v_smfmac_f32_16x16x64_bf16 v[76:79], v[40:43], a[96:103], v10\n\t" \
    "ds_read_b128 v[56:59], v6\n\t" \
    "ds_read_b128 v[60:63], v7\n\t" \
    "v_smfmac_f32_16x16x64_bf16 v[80:83], v[40:43], a[168:175], v10\n\t" \
    "ds_read_b128 v[64:67], v8\n\t" \
    "v_smfmac_f32_16x16x64_bf16 v[84:87], v[40:43], a[240:247], v10\n\t" \
    "ds_read_b128 v[68:71], v9\n\t" \
    "s_waitcnt lgkmcnt(5)\n\t" \
    "v_smfmac_f32_16x16x64_bf16 v[72:75], v[48:51], a[32:39], v10\n\t" \
    "ds_read_b128 v[44:47], v0 offset:19456\n\t" \
    "s_waitcnt lgkmcnt(5)\n\t" \
    "v_smfmac_f32_16x16x64_bf16 v[72:75], v[52:55], a[40:47], v10\n\t" \
    "s_waitcnt lgkmcnt(4)\n\t" \
    "v_smfmac_f32_16x16x64_bf16 v[72:75], v[56:59], a[48:55], v10\n\t" \
    "s_waitcnt lgkmcnt(3)\n\t" \
    "v_smfmac_f32_16x16x64_bf16 v[72:75], v[60:63], a[56:63], v10\n\t" \
    "s_waitcnt lgkmcnt(2)\n\t" \
    "v_smfmac_f32_16x16x64_bf16 v[72:75], v[64:67], a[0:7], v10\n\t" \
    "s_waitcnt lgkmcnt(1)\n\t" \
    "v_smfmac_f32_16x16x64_bf16 v[72:75], v[68:71], a[8:15], v10\n\t" \
    "s_waitcnt lgkmcnt(0)\n\t" \
    "v_smfmac_f32_16x16x64_bf16 v[72:75], v[44:47], a[64:71], v10\n\t" \
    "s_nop 2\n\t" \
    "v_smfmac_f32_16x16x64_bf16 v[76:79], v[48:51], a[104:111], v10\n\t" \
    "s_nop 2\n\t" \
    "v_smfmac_f32_16x16x64_bf16 v[76:79], v[52:55], a[112:119], v10\n\t" \
    "v_add_f32 v72, v72, v74\n\t" \
    "v_smfmac_f32_16x16x64_bf16 v[76:79], v[56:59], a[120:127], v10\n\t" \
    "v_add_f32 v73, v73, v75\n\t" \
    "v_smfmac_f32_16x16x64_bf16 v[76:79], v[60:63], a[128:135], v10\n\t" \
    "v_cvt_pk_bf16_f32 v156, v72, v73\n\t" \
    "v_smfmac_f32_16x16x64_bf16 v[76:79], v[64:67], a[72:79], v10\n\t" \
    "ds_write_b32 v12, v156 offset:0\n\t" \
    "v_smfmac_f32_16x16x64_bf16 v[76:79], v[68:71], a[80:87], v10\n\t" \
    "v_smfmac_f32_16x16x64_bf16 v[76:79], v[44:47], a[136:143], v10\n\t" \
    "s_nop 2\n\t" \
    "v_smfmac_f32_16x16x64_bf16 v[80:83], v[48:51], a[176:183], v10\n\t" \
    "s_nop 2\n\t" \
    "v_smfmac_f32_16x16x64_bf16 v[80:83], v[52:55], a[184:191], v10\n\t" \
    "v_add_f32 v76, v76, v78\n\t" \
    "v_smfmac_f32_16x16x64_bf16 v[80:83], v[56:59], a[192:199], v10\n\t" \
    "v_add_f32 v77, v77, v79\n\t" \
    "v_smfmac_f32_16x16x64_bf16 v[80:83], v[60:63], a[200:207], v10\n\t" \
    "v_cvt_pk_bf16_f32 v157, v76, v77\n\t" \
    "v_smfmac_f32_16x16x64_bf16 v[80:83], v[64:67], a[144:151], v10\n\t" \
    "ds_write_b32 v12, v157 offset:64\n\t" \
    "v_smfmac_f32_16x16x64_bf16 v[80:83], v[68:71], a[152:159], v10\n\t" \
    "v_smfmac_f32_16x16x64_bf16 v[80:83], v[44:47], a[208:215], v10\n\t" \
    "s_nop 2\n\t" \
    "v_smfmac_f32_16x16x64_bf16 v[84:87], v[48:51], a[248:255], v10\n\t" \
    "s_nop 2\n\t" \
    "v_smfmac_f32_16x16x64_bf16 v[84:87], v[52:55], v[224:231], v10\n\t" \
    "v_add_f32 v80, v80, v82\n\t" \
    "v_smfmac_f32_16x16x64_bf16 v[84:87], v[56:59], v[232:239], v10\n\t" \
    "v_add_f32 v81, v81, v83\n\t" \
    "v_smfmac_f32_16x16x64_bf16 v[84:87], v[60:63], v[240:247], v10\n\t" \
    "v_cvt_pk_bf16_f32 v158, v80, v81\n\t" \
    "v_smfmac_f32_16x16x64_bf16 v[84:87], v[64:67], a[216:223], v10\n\t" \
    "ds_write_b32 v16, v158 offset:0\n\t" \
    "v_smfmac_f32_16x16x64_bf16 v[84:87], v[68:71], a[224:231], v10\n\t" \
    "ds_read_b64_tr_b16 v[132:133], v13 offset:0\n\t" \
    "ds_read_b64_tr_b16 v[134:135], v13 offset:128\n\t" \
    "v_smfmac_f32_16x16x64_bf16 v[84:87], v[44:47], v[248:255], v10\n\t" \
    "ds_read_b64_tr_b16 v[136:137], v13 offset:1024\n\t" \
    "ds_read_b64_tr_b16 v[138:139], v13 offset:1152\n\t" \
    "s_nop 4\n\t" \
    "v_cmp_u_f32 vcc, v72, v73\n\t" \
    "v_pk_add_f32 v[84:85], v[84:85], v[86:87]\n\t" \
    "v_cvt_pk_bf16_f32 v159, v84, v85\n\t" \
    "ds_write_b32 v16, v159 offset:256\n\t" \
    "s_cbranch_vccnz qk_Gslow44_%=\n\t" \
    "qk_Gback45_%=:\n\t" \
    "s_waitcnt lgkmcnt(3)\n\t" \
    "v_smfmac_f32_16x16x64_bf16 v[88:91], v[132:135], v[100:107], v10\n\t" \
    "s_waitcnt lgkmcnt(1)\n\t" \
    "v_smfmac_f32_16x16x64_bf16 v[88:91], v[136:139], v[108:115], v10\n\t" \
    "s_waitcnt lgkmcnt(0)\n\t" \
    "s_barrier\n\t" \
    "ds_read_b64_tr_b16 v[140:141], v17 offset:0\n\t" \
    "v_smfmac_f32_16x16x64_bf16 v[92:95], v[132:135], v[116:123], v10\n\t" \
    "ds_read_b64_tr_b16 v[142:143], v17 offset:256\n\t" \
    "ds_read_b64_tr_b16 v[144:145], v17 offset:512\n\t" \
    "ds_read_b64_tr_b16 v[146:147], v17 offset:768\n\t" \
    "ds_read_b64_tr_b16 v[148:149], v17 offset:1024\n\t" \
    "v_smfmac_f32_16x16x64_bf16 v[92:95], v[136:139], v[124:131], v10\n\t" \
    "ds_read_b64_tr_b16 v[150:151], v17 offset:1280\n\t" \
    "ds_read_b64_tr_b16 v[152:153], v17 offset:1536\n\t" \
    "ds_read_b64_tr_b16 v[154:155], v17 offset:1792\n\t" \
    "v_smfmac_f32_16x16x64_bf16 v[184:187], v[132:135], v[100:107], v10\n\t" \
    "qk_qb47_%=:\n\t" \
    "s_waitcnt lgkmcnt(4)\n\t" \
    "v_smfmac_f32_16x16x64_bf16 v[96:99], v[132:135], v[140:147], v10\n\t" \
    "s_waitcnt lgkmcnt(0)\n\t" \
    "v_smfmac_f32_16x16x64_bf16 v[96:99], v[136:139], v[148:155], v10\n\t" \
    "v_add_f32 v168, v89, v91\n\t" \
    "v_add_f32 v169, v93, v95\n\t" \
    "v_cmp_u_f32 vcc, v168, v169\n\t" \
    "v_cvt_pk_bf16_f32 v168, 0, v168\n\t" \
    "v_cvt_pk_bf16_f32 v169, 0, v169\n\t" \
    "v_mul_f32 v168, v168, v26\n\t" \
    "v_mul_f32 v169, v169, v26\n\t" \
    "v_cvt_pk_bf16_f32 v168, 0, v168\n\t" \
    "v_add_f32 v172, v97, v99\n\t" \
    "v_cvt_pk_bf16_f32 v169, 0, v169\n\t" \
    "v_max_f32 v178, v168, v169\n\t" \
    "v_cvt_pk_bf16_f32 v172, 0, v172\n\t" \
    "v_mul_f32 v172, v172, v26\n\t" \
    "v_max_f32_dpp v178, v178, v178 row_mirror row_mask:0xf bank_mask:0xf\n\t" \
    "v_cvt_pk_bf16_f32 v172, 0, v172\n\t" \
    "v_max_f32 v173, v178, v172\n\t" \
    "v_mul_f32 v168, v168, v28\n\t" \
    "v_mul_f32 v169, v169, v28\n\t" \
    "v_max_f32_dpp v173, v173, v173 quad_perm:[1,0,3,2] row_mask:0xf bank_mask:0xf\n\t" \
    "v_mul_f32 v172, v172, v28\n\t" \
    "s_cbranch_vccnz qk_qs46_%=\n\t" \
    "qk_qb248_%=:\n\t" \
    "v_max_f32_dpp v173, v173, v173 quad_perm:[2,3,0,1] row_mask:0xf bank_mask:0xf\n\t" \
    "s_nop 1\n\t" \
    "v_max_f32_dpp v173, v173, v173 row_half_mirror row_mask:0xf bank_mask:0xf\n\t" \
    "v_fma_f32 v172, -v173, v28, v172\n\t" \
    "v_fma_f32 v168, -v173, v28, v168\n\t" \
    "v_fma_f32 v169, -v173, v28, v169\n\t" \
    "v_exp_f32 v172, v172\n\t" \
    "v_exp_f32 v168, v168\n\t" \
    "v_exp_f32 v169, v169\n\t" \
    "v_fma_f32 v173, v172, 0.5, v168\n\t" \
    "v_add_f32 v173, v173, v169\n\t" \
    "v_mov_b32_dpp v174, v172 quad_perm:[0,0,2,2] row_mask:0xf bank_mask:0xf\n\t" \
    "v_mov_b32_dpp v175, v172 quad_perm:[1,1,3,3] row_mask:0xf bank_mask:0xf\n\t" \
    "v_add_f32_dpp v173, v173, v173 quad_perm:[1,0,3,2] row_mask:0xf bank_mask:0xf\n\t" \
    "v_mov_b32_dpp v176, v168 quad_perm:[1,0,3,2] row_mask:0xf bank_mask:0xf\n\t" \
    "v_mov_b32_dpp v177, v169 quad_perm:[1,0,3,2] row_mask:0xf bank_mask:0xf\n\t" \
    "v_add_f32_dpp v173, v173, v173 quad_perm:[2,3,0,1] row_mask:0xf bank_mask:0xf\n\t" \
    "v_cndmask_b32 v168, v168, v177, s[48:49]\n\t" \
    "v_cndmask_b32 v169, v176, v169, s[48:49]\n\t" \
    "v_add_f32_dpp v173, v173, v173 row_half_mirror row_mask:0xf bank_mask:0xf\n\t" \
    "s_nop 1\n\t" \
    "v_add_f32_dpp v173, v173, v173 row_mirror row_mask:0xf bank_mask:0xf\n\t" \
    "v_rcp_f32 v173, v173\n\t" \
    "s_nop 0\n\t" \
    "v_pk_mul_f32 v[168:169], v[168:169], v[172:173] op_sel:[0,1] op_sel_hi:[1,1]\n\t" \
    "v_cvt_pk_bf16_f32 v176, v168, v169\n\t" \
    "global_store_dword v18, v176, s[46:47] sc1\n\t" \
    "global_store_dword v180, v176, s[46:47] sc1\n\t" \
    "global_store_dword v181, v176, s[46:47] sc1\n\t" \
    "global_store_dword v182, v176, s[46:47] sc1\n\t" \
    "v_pk_mul_f32 v[174:175], v[174:175], v[172:173] op_sel:[0,1] op_sel_hi:[1,1]\n\t" \
    "v_cvt_pk_bf16_f32 v177, v174, v175\n\t" \
    "global_store_dword v19, v177, s[46:47] sc1\n\t" \
    "s_cmp_eq_u32 s65, 63\n\t" \
    "s_cbranch_scc1 qk_last36_%=\n\t" \
    "v_readfirstlane_b32 s69, v25\n\t" \
    "s_add_u32 s65, s65, 1\n\t" \
    "s_max_i32 s83, s83, s69\n\t" \
    "s_cmp_gt_u32 s65, 6\n\t" \
    "s_cselect_b32 s83, 99, s83\n\t" \
    "v_mov_b32 v72, 0\n\t" \
    "v_mov_b32 v73, 0\n\t" \
    "v_mov_b32 v74, 0\n\t" \
    "v_mov_b32 v75, 0\n\t" \
    "v_mov_b32 v76, 0\n\t" \
    "v_mov_b32 v77, 0\n\t" \
    "v_mov_b32 v78, 0\n\t" \
    "v_mov_b32 v79, 0\n\t" \
    "v_mov_b32 v80, 0\n\t" \
    "v_mov_b32 v81, 0\n\t" \
    "v_mov_b32 v82, 0\n\t" \
    "v_mov_b32 v83, 0\n\t" \
    "v_mov_b32 v84, 0\n\t" \
    "v_mov_b32 v85, 0\n\t" \
    "v_mov_b32 v86, 0\n\t" \
    "v_mov_b32 v87, 0\n\t" \
    "v_mov_b32 v88, 0\n\t" \
    "v_mov_b32 v89, 0\n\t" \
    "v_mov_b32 v90, 0\n\t" \
    "v_mov_b32 v91, 0\n\t" \
    "v_mov_b32 v92, 0\n\t" \
    "v_mov_b32 v93, 0\n\t" \
    "v_mov_b32 v94, 0\n\t" \
    "v_mov_b32 v95, 0\n\t" \
    "v_mov_b32 v96, 0\n\t" \
    "v_mov_b32 v97, 0\n\t" \
    "v_mov_b32 v98, 0\n\t" \
    "v_mov_b32 v99, 0\n\t" \
    "s_add_u32 s69, s65, 0\n\t" \
    "s_and_b32 s69, s69, 7\n\t" \
    "s_lshl_b32 s69, s69, 18\n\t" \
    "s_add_u32 s69, s69, s62\n\t" \
    "s_add_u32 s46, s36, s69\n\t" \
    "s_addc_u32 s47, s37, 0\n\t" \
    "s_mul_i32 s70, s67, 7\n\t" \
    "s_add_u32 s69, s65, 0\n\t" \
    "s_xor_b32 s69, s69, s70\n\t" \
    "s_and_b32 s69, s69, 63\n\t" \
    "s_lshl_b32 s69, s69, 13\n\t" \
    "s_mul_i32 s70, s67, 524288\n\t" \
    "s_add_u32 s69, s69, s70\n\t" \
    "s_add_u32 s69, s69, 23068672\n\t" \
    "s_add_u32 s38, s36, s69\n\t" \
    "s_addc_u32 s39, s37, 0\n\t" \
    "qk_WF49_%=:\n\t" \
    "s_cmp_ge_i32 s83, s65\n\t" \
    "s_cbranch_scc1 qk_WFok50_%=\n\t" \
    "global_load_dword v160, v24, s[100:101] sc1\n\t" \
    "s_waitcnt vmcnt(0)\n\t" \
    "v_readfirstlane_b32 s83, v160\n\t" \
    "s_nop 1\n\t" \
    "s_cmp_ge_i32 s83, s65\n\t" \
    "s_cbranch_scc1 qk_WFok50_%=\n\t" \
    "s_cmp_eq_u32 s68, 0\n\t" \
    "s_cbranch_scc1 qk_F51_%=\n\t" \
    "s_sub_u32 s68, s68, 1\n\t" \
    "s_sleep 2\n\t" \
    "s_branch qk_WF49_%=\n\t" \
    "qk_F51_%=:\n\t" \
    "v_mov_b32 v160, 5767424\n\t" \
    "v_mov_b32 v161, 9\n\t" \
    "global_store_dword v160, v161, s[36:37] sc1\n\t" \
    "s_waitcnt vmcnt(0)\n\t" \
    "s_branch qk_WFok50_%=\n\t" \
    "qk_WFok50_%=:\n\t" \
    "global_load_dwordx4 v[100:103], v0, s[38:39] offset:0 sc1\n\t" \
    "global_load_dwordx4 v[104:107], v0, s[38:39] offset:2048 sc1\n\t" \
    "global_load_dwordx4 v[108:111], v15, s[38:39] offset:0 sc1\n\t" \
    "global_load_dwordx4 v[112:115], v15, s[38:39] offset:2048 sc1\n\t" \
    "global_load_dwordx4 v[116:119], v0, s[38:39] offset:1024 sc1\n\t" \
    "global_load_dwordx4 v[120:123], v0, s[38:39] offset:3072 sc1\n\t" \
    "global_load_dwordx4 v[124:127], v15, s[38:39] offset:1024 sc1\n\t" \
    "global_load_dwordx4 v[128:131], v15, s[38:39] offset:3072 sc1\n\t" \
    "s_cmp_gt_i32 s83, s65\n\t" \
    "s_cbranch_scc1 qk_nf52_%=\n\t" \
    "global_load_dword v25, v24, s[100:101] sc1\n\t" \
    "qk_nf52_%=:\n\t" \
    "s_barrier\n\t" \
    "ds_read_b32 v168, v11 offset:20480\n\t" \
    "s_waitcnt lgkmcnt(0)\n\t" \
    "v_readfirstlane_b32 s70, v168\n\t" \
    "ds_write_b32 v12, v31 offset:0\n\t" \
    "ds_write_b32 v12, v31 offset:64\n\t" \
    "s_branch qk_go37_%=\n\t" \
    "qk_Gbad42_%=:\n\t" \
    "s_waitcnt lgkmcnt(0)\n\t" \
    "s_nop 7\n\t" \
    "v_mov_b32 v72, 0\n\t" \
    "v_mov_b32 v73, 0\n\t" \
    "v_mov_b32 v74, 0\n\t" \
    "v_mov_b32 v75, 0\n\t" \
    "v_mov_b32 v76, 0\n\t" \
    "v_mov_b32 v77, 0\n\t" \
    "v_mov_b32 v78, 0\n\t" \
    "v_mov_b32 v79, 0\n\t" \
    "v_mov_b32 v80, 0\n\t" \
    "v_mov_b32 v81, 0\n\t" \
    "v_mov_b32 v82, 0\n\t" \
    "v_mov_b32 v83, 0\n\t" \
    "v_mov_b32 v84, 0\n\t" \
    "v_mov_b32 v85, 0\n\t" \
    "v_mov_b32 v86, 0\n\t" \
    "v_mov_b32 v87, 0\n\t" \
    "s_cmp_eq_u32 s68, 0\n\t" \
    "s_cbranch_scc1 qk_F53_%=\n\t" \
    "s_sub_u32 s68, s68, 1\n\t" \
    "s_branch qk_G41_%=\n\t" \
    "qk_F53_%=:\n\t" \
    "v_mov_b32 v160, 5767424\n\t" \
    "v_mov_b32 v161, 1\n\t" \
    "global_store_dword v160, v161, s[36:37] sc1\n\t" \
    "s_waitcnt vmcnt(0)\n\t" \
    "s_branch qk_Gc43_%=\n\t" \
    "qk_Gslow44_%=:\n\t" \
    "v_max_u32 v160, v36, v37\n\t" \
    "v_max3_u32 v160, v160, v38, v39\n\t" \
    "v_max3_u32 v160, v160, v40, v41\n\t" \
    "v_max3_u32 v160, v160, v42, v43\n\t" \
    "v_max3_u32 v160, v160, v44, v45\n\t" \
    "v_max3_u32 v160, v160, v46, v47\n\t" \
    "v_max3_u32 v160, v160, v48, v49\n\t" \
    "v_max3_u32 v160, v160, v50, v51\n\t" \
    "v_max3_u32 v160, v160, v52, v53\n\t" \
    "v_max3_u32 v160, v160, v54, v55\n\t" \
    "v_max3_u32 v160, v160, v56, v57\n\t" \
    "v_max3_u32 v160, v160, v58, v59\n\t" \
    "v_max3_u32 v160, v160, v60, v61\n\t" \
    "v_max3_u32 v160, v160, v62, v63\n\t" \
    "v_max3_u32 v160, v160, v64, v65\n\t" \
    "v_max3_u32 v160, v160, v66, v67\n\t" \
    "v_max3_u32 v160, v160, v68, v69\n\t" \
    "v_max3_u32 v160, v160, v70, v71\n\t" \
    "v_cmp_eq_u32 vcc, -1, v160\n\t" \
    "s_cbranch_vccz qk_Gback45_%=\n\t" \
    "s_waitcnt lgkmcnt(0)\n\t" \
    "v_sub_u32 v160, v1, v3\n\t" \
    "s_nop 0\n\t" \
    "v_readfirstlane_b32 s69, v160\n\t" \
    "v_add_u32 v163, 20480, v0\n\t" \
    "s_nop 7\n\t" \
    "s_add_u32 s30, s46, s69\n\t" \
    "s_addc_u32 s31, s47, 0\n\t" \
    "qk_Gag54_%=:\n\t" \
    "global_load_dwordx4 v[36:39], v1, s[46:47] sc1\n\t" \
    "global_load_dwordx4 v[40:43], v23, s[46:47] sc1\n\t" \
    "global_load_dwordx4 v[44:47], v163, s[30:31] sc1\n\t" \
    "global_load_dwordx4 v[48:51], v4, s[30:31] sc1\n\t" \
    "global_load_dwordx4 v[52:55], v5, s[30:31] sc1\n\t" \
    "global_load_dwordx4 v[56:59], v6, s[30:31] sc1\n\t" \
    "global_load_dwordx4 v[60:63], v7, s[30:31] sc1\n\t" \
    "global_load_dwordx4 v[64:67], v8, s[30:31] sc1\n\t" \
    "global_load_dwordx4 v[68:71], v9, s[30:31] sc1\n\t" \
    "s_waitcnt vmcnt(0)\n\t" \
    "v_max_u32 v160, v36, v37\n\t" \
    "v_max3_u32 v160, v160, v38, v39\n\t" \
    "v_max3_u32 v160, v160, v40, v41\n\t" \
    "v_max3_u32 v160, v160, v42, v43\n\t" \
    "v_max3_u32 v160, v160, v44, v45\n\t" \
    "v_max3_u32 v160, v160, v46, v47\n\t" \
    "v_max3_u32 v160, v160, v48, v49\n\t" \
    "v_max3_u32 v160, v160, v50, v51\n\t" \
    "v_max3_u32 v160, v160, v52, v53\n\t" \
    "v_max3_u32 v160, v160, v54, v55\n\t" \
    "v_max3_u32 v160, v160, v56, v57\n\t" \
    "v_max3_u32 v160, v160, v58, v59\n\t" \
    "v_max3_u32 v160, v160, v60, v61\n\t" \
    "v_max3_u32 v160, v160, v62, v63\n\t" \
    "v_max3_u32 v160, v160, v64, v65\n\t" \
    "v_max3_u32 v160, v160, v66, v67\n\t" \
    "v_max3_u32 v160, v160, v68, v69\n\t" \
    "v_max3_u32 v160, v160, v70, v71\n\t" \
    "v_cmp_eq_u32 vcc, -1, v160\n\t" \
    "s_cbranch_vccz qk_Gredo55_%=\n\t" \
    "s_cmp_eq_u32 s68, 0\n\t" \
    "s_cbranch_scc1 qk_F56_%=\n\t" \
    "s_sub_u32 s68, s68, 1\n\t" \
    "s_sleep 1\n\t" \
    "s_branch qk_Gag54_%=\n\t" \
    "qk_F56_%=:\n\t" \
    "v_mov_b32 v160, 5767424\n\t" \
    "v_mov_b32 v161, 2\n\t" \
    "global_store_dword v160, v161, s[36:37] sc1\n\t" \
    "s_waitcnt vmcnt(0)\n\t" \
    "s_branch qk_Gredo55_%=\n\t" \
    "qk_Gredo55_%=:\n\t" \
    "v_mov_b32 v72, 0\n\t" \
    "v_mov_b32 v73, 0\n\t" \
    "v_mov_b32 v74, 0\n\t" \
    "v_mov_b32 v75, 0\n\t" \
    "v_mov_b32 v76, 0\n\t" \
    "v_mov_b32 v77, 0\n\t" \
    "v_mov_b32 v78, 0\n\t" \
    "v_mov_b32 v79, 0\n\t" \
    "v_mov_b32 v80, 0\n\t" \
    "v_mov_b32 v81, 0\n\t" \
    "v_mov_b32 v82, 0\n\t" \
    "v_mov_b32 v83, 0\n\t" \
    "v_mov_b32 v84, 0\n\t" \
    "v_mov_b32 v85, 0\n\t" \
    "v_mov_b32 v86, 0\n\t" \
    "v_mov_b32 v87, 0\n\t" \
    "v_smfmac_f32_16x16x64_bf16 v[72:75], v[36:39], a[16:23], v10\n\t" \
    "v_smfmac_f32_16x16x64_bf16 v[72:75], v[40:43], a[24:31], v10\n\t" \
    "v_smfmac_f32_16x16x64_bf16 v[72:75], v[48:51], a[32:39], v10\n\t" \
    "v_smfmac_f32_16x16x64_bf16 v[72:75], v[52:55], a[40:47], v10\n\t" \
    "v_smfmac_f32_16x16x64_bf16 v[72:75], v[56:59], a[48:55], v10\n\t" \
    "v_smfmac_f32_16x16x64_bf16 v[72:75], v[60:63], a[56:63], v10\n\t" \
    "v_smfmac_f32_16x16x64_bf16 v[72:75], v[64:67], a[0:7], v10\n\t" \
    "v_smfmac_f32_16x16x64_bf16 v[72:75], v[68:71], a[8:15], v10\n\t" \
    "v_smfmac_f32_16x16x64_bf16 v[72:75], v[44:47], a[64:71], v10\n\t" \
    "v_smfmac_f32_16x16x64_bf16 v[76:79], v[36:39], a[88:95], v10\n\t" \
    "v_smfmac_f32_16x16x64_bf16 v[76:79], v[40:43], a[96:103], v10\n\t" \
    "v_smfmac_f32_16x16x64_bf16 v[76:79], v[48:51], a[104:111], v10\n\t" \
    "v_smfmac_f32_16x16x64_bf16 v[76:79], v[52:55], a[112:119], v10\n\t" \
    "v_smfmac_f32_16x16x64_bf16 v[76:79], v[56:59], a[120:127], v10\n\t" \
    "v_smfmac_f32_16x16x64_bf16 v[76:79], v[60:63], a[128:135], v10\n\t" \
    "v_smfmac_f32_16x16x64_bf16 v[76:79], v[64:67], a[72:79], v10\n\t" \
    "v_smfmac_f32_16x16x64_bf16 v[76:79], v[68:71], a[80:87], v10\n\t" \
    "v_smfmac_f32_16x16x64_bf16 v[76:79], v[44:47], a[136:143], v10\n\t" \
    "v_smfmac_f32_16x16x64_bf16 v[80:83], v[36:39], a[160:167], v10\n\t" \
    "v_smfmac_f32_16x16x64_bf16 v[80:83], v[40:43], a[168:175], v10\n\t" \
    "v_smfmac_f32_16x16x64_bf16 v[80:83], v[48:51], a[176:183], v10\n\t" \
    "v_smfmac_f32_16x16x64_bf16 v[80:83], v[52:55], a[184:191], v10\n\t" \
    "v_smfmac_f32_16x16x64_bf16 v[80:83], v[56:59], a[192:199], v10\n\t" \
    "v_smfmac_f32_16x16x64_bf16 v[80:83], v[60:63], a[200:207], v10\n\t" \
    "v_smfmac_f32_16x16x64_bf16 v[80:83], v[64:67], a[144:151], v10\n\t" \
    "v_smfmac_f32_16x16x64_bf16 v[80:83], v[68:71], a[152:159], v10\n\t" \
    "v_smfmac_f32_16x16x64_bf16 v[80:83], v[44:47], a[208:215], v10\n\t" \
    "v_smfmac_f32_16x16x64_bf16 v[84:87], v[36:39], a[232:239], v10\n\t" \
    "v_smfmac_f32_16x16x64_bf16 v[84:87], v[40:43], a[240:247], v10\n\t" \
    "v_smfmac_f32_16x16x64_bf16 v[84:87], v[48:51], a[248:255], v10\n\t" \
    "v_smfmac_f32_16x16x64_bf16 v[84:87], v[52:55], v[224:231], v10\n\t" \
    "v_smfmac_f32_16x16x64_bf16 v[84:87], v[56:59], v[232:239], v10\n\t" \
    "v_smfmac_f32_16x16x64_bf16 v[84:87], v[60:63], v[240:247], v10\n\t" \
    "v_smfmac_f32_16x16x64_bf16 v[84:87], v[64:67], a[216:223], v10\n\t" \
    "v_smfmac_f32_16x16x64_bf16 v[84:87], v[68:71], a[224:231], v10\n\t" \
    "v_smfmac_f32_16x16x64_bf16 v[84:87], v[44:47], v[248:255], v10\n\t" \
    "v_add_f32 v72, v72, v74\n\t" \
    "v_add_f32 v73, v73, v75\n\t" \
    "v_cvt_pk_bf16_f32 v156, v72, v73\n\t" \
    "ds_write_b32 v12, v156 offset:0\n\t" \
    "v_add_f32 v76, v76, v78\n\t" \
    "v_add_f32 v77, v77, v79\n\t" \
    "v_cvt_pk_bf16_f32 v157, v76, v77\n\t" \
    "ds_write_b32 v12, v157 offset:64\n\t" \
    "v_add_f32 v80, v80, v82\n\t" \
    "v_add_f32 v81, v81, v83\n\t" \
    "v_cvt_pk_bf16_f32 v158, v80, v81\n\t" \
    "ds_write_b32 v16, v158 offset:0\n\t" \
    "v_pk_add_f32 v[84:85], v[84:85], v[86:87]\n\t" \
    "v_cvt_pk_bf16_f32 v159, v84, v85\n\t" \
    "ds_write_b32 v16, v159 offset:256\n\t" \
    "s_branch qk_Gback45_%=\n\t" \
    "qk_qs46_%=:\n\t" \
    "s_cmp_eq_u32 s91, s65\n\t" \
    "s_cbranch_scc1 qk_qb248_%=\n\t" \
    "s_mov_b32 s91, s65\n\t" \
    "s_nop 7\n\t" \
    "ds_read_b64_tr_b16 v[132:133], v13 offset:0\n\t" \
    "ds_read_b64_tr_b16 v[134:135], v13 offset:128\n\t" \
    "ds_read_b64_tr_b16 v[136:137], v13 offset:1024\n\t" \
    "ds_read_b64_tr_b16 v[138:139], v13 offset:1152\n\t" \
    "v_mov_b32 v88, 0\n\t" \
    "v_mov_b32 v89, 0\n\t" \
    "v_mov_b32 v90, 0\n\t" \
    "v_mov_b32 v91, 0\n\t" \
    "v_mov_b32 v92, 0\n\t" \
    "v_mov_b32 v93, 0\n\t" \
    "v_mov_b32 v94, 0\n\t" \
    "v_mov_b32 v95, 0\n\t" \
    "v_mov_b32 v96, 0\n\t" \
    "v_mov_b32 v97, 0\n\t" \
    "v_mov_b32 v98, 0\n\t" \
    "v_mov_b32 v99, 0\n\t" \
    "s_waitcnt lgkmcnt(0)\n\t" \
    "v_smfmac_f32_16x16x64_bf16 v[88:91], v[132:135], v[100:107], v10\n\t" \
    "v_smfmac_f32_16x16x64_bf16 v[92:95], v[132:135], v[116:123], v10\n\t" \
    "v_smfmac_f32_16x16x64_bf16 v[88:91], v[136:139], v[108:115], v10\n\t" \
    "v_smfmac_f32_16x16x64_bf16 v[92:95], v[136:139], v[124:131], v10\n\t" \
    "s_nop 7\n\t" \
    "s_branch qk_qb47_%=\n\t" \
    "qk_last36_%=:\n\t" \
    "s_endpgm\n\t" \
    "qk_loop13_%=:\n\t" \
    "v_mov_b32 v72, 0\n\t" \
    "v_mov_b32 v73, 0\n\t" \
    "v_mov_b32 v74, 0\n\t" \
    "v_mov_b32 v75, 0\n\t" \
    "v_mov_b32 v76, 0\n\t" \
    "v_mov_b32 v77, 0\n\t" \
    "v_mov_b32 v78, 0\n\t" \
    "v_mov_b32 v79, 0\n\t" \
    "v_mov_b32 v80, 0\n\t" \
    "v_mov_b32 v81, 0\n\t" \
    "v_mov_b32 v82, 0\n\t" \
    "v_mov_b32 v83, 0\n\t" \
    "v_mov_b32 v84, 0\n\t" \
    "v_mov_b32 v85, 0\n\t" \
    "v_mov_b32 v86, 0\n\t" \
    "v_mov_b32 v87, 0\n\t" \
    "s_add_u32 s69, s65, 0\n\t" \
    "s_and_b32 s69, s69, 7\n\t" \
    "s_lshl_b32 s69, s69, 18\n\t" \
    "s_add_u32 s69, s69, s62\n\t" \
    "s_add_u32 s46, s36, s69\n\t" \
    "s_addc_u32 s47, s37, 0\n\t" \
    "s_mul_i32 s70, s67, 7\n\t" \
    "s_add_u32 s69, s65, 0\n\t" \
    "s_xor_b32 s69, s69, s70\n\t" \
    "s_and_b32 s69, s69, 63\n\t" \
    "s_lshl_b32 s69, s69, 12\n\t" \
    "s_mul_i32 s70, s67, 262144\n\t" \
    "s_add_u32 s69, s69, s70\n\t" \
    "s_add_u32 s69, s69, 39845888\n\t" \
    "s_add_u32 s40, s36, s69\n\t" \
    "s_addc_u32 s41, s37, 0\n\t" \
    "s_sub_u32 s91, s65, 1\n\t" \
    "v_mov_b32 v25, s91\n\t" \
    "s_sub_u32 s70, s79, s74\n\t" \
    "s_max_i32 s70, s70, 0\n\t" \
    "s_min_i32 s70, s70, 16000\n\t" \
    "s_lshl_b32 s70, s70, 1\n\t" \
    "s_mul_hi_u32 s70, s70, s77\n\t" \
    "s_add_u32 s70, s70, s73\n\t" \
    ".p2align 6\n\t" \
    "s_nop 0\n\t" \
    "s_nop 0\n\t" \
    "s_nop 0\n\t" \
    "s_nop 0\n\t" \
    "s_nop 0\n\t" \
    "s_nop 0\n\t" \
    "qk_go58_%=:\n\t" \
    "s_getpc_b64 s[30:31]\n\t" \
    "qk_Wp62_%=:\n\t" \
    "s_add_u32 s30, s30, qk_We63_%=-qk_Wp62_%=\n\t" \
    "s_addc_u32 s31, s31, 0\n\t" \
    "s_sub_u32 s70, s70, 96\n\t" \
    "qk_W61_%=:\n\t" \
    "s_memtime s[56:57]\n\t" \
    "s_waitcnt lgkmcnt(0)\n\t" \
    "s_sub_u32 s69, s70, s56\n\t" \
    "s_cmp_gt_i32 s69, 80\n\t" \
    "s_cbranch_scc1 qk_W61_%=\n\t" \
    "s_max_i32 s69, s69, 0\n\t" \
    "s_and_b32 s69, s69, -4\n\t" \
    "s_sub_u32 s30, s30, s69\n\t" \
    "s_subb_u32 s31, s31, 0\n\t" \
    "s_setpc_b64 s[30:31]\n\t" \
    "s_nop 0\n\t" \
    "s_nop 0\n\t" \
    "s_nop 0\n\t" \
    "s_nop 0\n\t" \
    "s_nop 0\n\t" \
    "s_nop 0\n\t" \
    "s_nop 0\n\t" \
    "s_nop 0\n\t" \
    "s_nop 0\n\t" \
    "s_nop 0\n\t" \
    "s_nop 0\n\t" \
    "s_nop 0\n\t" \
    "s_nop 0\n\t" \
    "s_nop 0\n\t" \
    "s_nop 0\n\t" \
    "s_nop 0\n\t" \
    "s_nop 0\n\t" \
    "s_nop 0\n\t" \
    "s_nop 0\n\t" \
    "s_nop 0\n\t" \
    "qk_We63_%=:\n\t" \
    "qk_G64_%=:\n\t" \
    "global_load_dwordx4 v[36:39], v1, s[46:47] sc1\n\t" \
    "global_load_dword v161, v2, s[46:47] sc1\n\t" \
    "global_load_dwordx4 v[40:43], v23, s[46:47] sc1\n\t" \
    "s_waitcnt vmcnt(2)\n\t" \
    "ds_write_b128 v3, v[36:39]\n\t" \
    "v_smfmac_f32_16x16x64_bf16 v[72:75], v[36:39], a[32:39], v10\n\t" \
    "s_waitcnt vmcnt(1)\n\t" \
    "ds_write_b32 v22, v161 offset:19968\n\t" \
    "v_smfmac_f32_16x16x64_bf16 v[76:79], v[36:39], a[104:111], v10\n\t" \
    "s_waitcnt vmcnt(0)\n\t" \
    "ds_write_b128 v3, v[40:43] offset:1024\n\t" \
    "v_max3_u32 v160, v36, v40, v161\n\t" \
    "v_cmp_eq_u32 vcc, -1, v160\n\t" \
    "v_smfmac_f32_16x16x64_bf16 v[80:83], v[36:39], a[176:183], v10\n\t" \
    "v_smfmac_f32_16x16x64_bf16 v[84:87], v[36:39], a[248:255], v10\n\t" \
    "v_smfmac_f32_16x16x64_bf16 v[72:75], v[40:43], a[40:47], v10\n\t" \
    "s_cbranch_vccnz qk_Gbad65_%=\n\t" \
    "qk_Gc66_%=:\n\t" \
    "s_cmp_ge_u32 s91, 57\n\t" \
    "s_cbranch_scc1 qk_kf59_%=\n\t" \
    "qk_kb60_%=:\n\t" \
    "s_waitcnt lgkmcnt(0)\n\t" \
    "s_barrier\n\t" \
    "ds_read_b128 v[48:51], v4\n\t" \
    "ds_read_b128 v[52:55], v5\n\t" \
    "v_smfmac_f32_16x16x64_bf16 v[76:79], v[40:43], a[112:119], v10\n\t" \
    "ds_read_b128 v[56:59], v6\n\t" \
    "ds_read_b128 v[60:63], v7\n\t" \
    "v_smfmac_f32_16x16x64_bf16 v[80:83], v[40:43], a[184:191], v10\n\t" \
    "ds_read_b128 v[64:67], v8\n\t" \
    "v_smfmac_f32_16x16x64_bf16 v[84:87], v[40:43], v[224:231], v10\n\t" \
    "ds_read_b128 v[68:71], v9\n\t" \
    "s_waitcnt lgkmcnt(5)\n\t" \
    "v_smfmac_f32_16x16x64_bf16 v[72:75], v[48:51], a[48:55], v10\n\t" \
    "ds_read_b128 v[44:47], v0 offset:19456\n\t" \
    "s_waitcnt lgkmcnt(5)\n\t" \
    "v_smfmac_f32_16x16x64_bf16 v[72:75], v[52:55], a[56:63], v10\n\t" \
    "s_waitcnt lgkmcnt(4)\n\t" \
    "v_smfmac_f32_16x16x64_bf16 v[72:75], v[56:59], a[0:7], v10\n\t" \
    "s_waitcnt lgkmcnt(3)\n\t" \
    "v_smfmac_f32_16x16x64_bf16 v[72:75], v[60:63], a[8:15], v10\n\t" \
    "s_waitcnt lgkmcnt(2)\n\t" \
    "v_smfmac_f32_16x16x64_bf16 v[72:75], v[64:67], a[16:23], v10\n\t" \
    "s_waitcnt lgkmcnt(1)\n\t" \
    "v_smfmac_f32_16x16x64_bf16 v[72:75], v[68:71], a[24:31], v10\n\t" \
    "s_waitcnt lgkmcnt(0)\n\t" \
    "v_smfmac_f32_16x16x64_bf16 v[72:75], v[44:47], a[64:71], v10\n\t" \
    "s_nop 2\n\t" \
    "v_smfmac_f32_16x16x64_bf16 v[76:79], v[48:51], a[120:127], v10\n\t" \
    "s_nop 2\n\t" \
    "v_smfmac_f32_16x16x64_bf16 v[76:79], v[52:55], a[128:135], v10\n\t" \
    "v_add_f32 v72, v72, v74\n\t" \
    "v_smfmac_f32_16x16x64_bf16 v[76:79], v[56:59], a[72:79], v10\n\t" \
    "v_add_f32 v73, v73, v75\n\t" \
    "v_smfmac_f32_16x16x64_bf16 v[76:79], v[60:63], a[80:87], v10\n\t" \
    "v_cvt_pk_bf16_f32 v156, v72, v73\n\t" \
    "v_smfmac_f32_16x16x64_bf16 v[76:79], v[64:67], a[88:95], v10\n\t" \
    "ds_write_b32 v12, v156 offset:0\n\t" \
    "v_smfmac_f32_16x16x64_bf16 v[76:79], v[68:71], a[96:103], v10\n\t" \
    "v_smfmac_f32_16x16x64_bf16 v[76:79], v[44:47], a[136:143], v10\n\t" \
    "s_nop 2\n\t" \
    "v_smfmac_f32_16x16x64_bf16 v[80:83], v[48:51], a[192:199], v10\n\t" \
    "s_nop 2\n\t" \
    "v_smfmac_f32_16x16x64_bf16 v[80:83], v[52:55], a[200:207], v10\n\t" \
    "v_add_f32 v76, v76, v78\n\t" \
    "v_smfmac_f32_16x16x64_bf16 v[80:83], v[56:59], a[144:151], v10\n\t" \
    "v_add_f32 v77, v77, v79\n\t" \
    "v_smfmac_f32_16x16x64_bf16 v[80:83], v[60:63], a[152:159], v10\n\t" \
    "v_cvt_pk_bf16_f32 v157, v76, v77\n\t" \
    "v_smfmac_f32_16x16x64_bf16 v[80:83], v[64:67], a[160:167], v10\n\t" \
    "ds_write_b32 v12, v157 offset:64\n\t" \
    "v_smfmac_f32_16x16x64_bf16 v[80:83], v[68:71], a[168:175], v10\n\t" \
    "v_smfmac_f32_16x16x64_bf16 v[80:83], v[44:47], a[208:215], v10\n\t" \
    "s_nop 2\n\t" \
    "v_smfmac_f32_16x16x64_bf16 v[84:87], v[48:51], v[232:239], v10\n\t" \
    "s_nop 2\n\t" \
    "v_smfmac_f32_16x16x64_bf16 v[84:87], v[52:55], v[240:247], v10\n\t" \
    "v_add_f32 v80, v80, v82\n\t" \
    "v_smfmac_f32_16x16x64_bf16 v[84:87], v[56:59], a[216:223], v10\n\t" \
    "v_add_f32 v81, v81, v83\n\t" \
    "v_smfmac_f32_16x16x64_bf16 v[84:87], v[60:63], a[224:231], v10\n\t" \
    "v_cvt_pk_bf16_f32 v158, v80, v81\n\t" \
    "v_smfmac_f32_16x16x64_bf16 v[84:87], v[64:67], a[232:239], v10\n\t" \
    "ds_write_b32 v16, v158 offset:0\n\t" \
    "v_smfmac_f32_16x16x64_bf16 v[84:87], v[68:71], a[240:247], v10\n\t" \
    "v_smfmac_f32_16x16x64_bf16 v[84:87], v[44:47], v[248:255], v10\n\t" \
    "s_nop 6\n\t" \
    "v_cmp_u_f32 vcc, v72, v73\n\t" \
    "v_pk_add_f32 v[84:85], v[84:85], v[86:87]\n\t" \
    "v_cvt_pk_bf16_f32 v159, v84, v85\n\t" \
    "ds_write_b32 v16, v159 offset:256\n\t" \
    "s_cbranch_vccnz qk_Gslow67_%=\n\t" \
    "qk_Gback68_%=:\n\t" \
    "s_waitcnt lgkmcnt(0)\n\t" \
    "s_barrier\n\t" \
    "s_sleep 1\n\t" \
    "ds_read_b64_tr_b16 v[140:141], v17 offset:0\n\t" \
    "ds_read_b64_tr_b16 v[142:143], v17 offset:256\n\t" \
    "ds_read_b64_tr_b16 v[144:145], v17 offset:512\n\t" \
    "ds_read_b64_tr_b16 v[146:147], v17 offset:768\n\t" \
    "ds_read_b64_tr_b16 v[148:149], v17 offset:1024\n\t" \
    "ds_read_b64_tr_b16 v[150:151], v17 offset:1280\n\t" \
    "ds_read_b64_tr_b16 v[152:153], v17 offset:1536\n\t" \
    "ds_read_b64_tr_b16 v[154:155], v17 offset:1792\n\t" \
    "s_waitcnt lgkmcnt(0)\n\t" \
    "s_cmp_eq_u32 s65, 63\n\t" \
    "s_cbranch_scc1 qk_last57_%=\n\t" \
    "s_cmp_ge_u32 s65, 58\n\t" \
    "s_cbranch_scc1 qk_nsl69_%=\n\t" \
    "s_sleep 6\n\t" \
    "global_store_dword v14, v25, s[100:101] sc1\n\t" \
    "qk_nsl69_%=:\n\t" \
    "s_mov_b64 exec, s[58:59]\n\t" \
    "global_store_dwordx4 v0, v[140:143], s[40:41] offset:0 sc1\n\t" \
    "global_store_dwordx4 v0, v[144:147], s[40:41] offset:1024 sc1\n\t" \
    "global_store_dwordx4 v0, v[148:151], s[40:41] offset:2048 sc1\n\t" \
    "global_store_dwordx4 v0, v[152:155], s[40:41] offset:3072 sc1\n\t" \
    "s_mov_b64 exec, -1\n\t" \
    "s_add_u32 s65, s65, 1\n\t" \
    "v_mov_b32 v72, 0\n\t" \
    "v_mov_b32 v73, 0\n\t" \
    "v_mov_b32 v74, 0\n\t" \
    "v_mov_b32 v75, 0\n\t" \
    "v_mov_b32 v76, 0\n\t" \
    "v_mov_b32 v77, 0\n\t" \
    "v_mov_b32 v78, 0\n\t" \
    "v_mov_b32 v79, 0\n\t" \
    "v_mov_b32 v80, 0\n\t" \
    "v_mov_b32 v81, 0\n\t" \
    "v_mov_b32 v82, 0\n\t" \
    "v_mov_b32 v83, 0\n\t" \
    "v_mov_b32 v84, 0\n\t" \
    "v_mov_b32 v85, 0\n\t" \
    "v_mov_b32 v86, 0\n\t" \
    "v_mov_b32 v87, 0\n\t" \
    "s_add_u32 s69, s65, 0\n\t" \
    "s_and_b32 s69, s69, 7\n\t" \
    "s_lshl_b32 s69, s69, 18\n\t" \
    "s_add_u32 s69, s69, s62\n\t" \
    "s_add_u32 s46, s36, s69\n\t" \
    "s_addc_u32 s47, s37, 0\n\t" \
    "s_mul_i32 s70, s67, 7\n\t" \
    "s_add_u32 s69, s65, 0\n\t" \
    "s_xor_b32 s69, s69, s70\n\t" \
    "s_and_b32 s69, s69, 63\n\t" \
    "s_lshl_b32 s69, s69, 12\n\t" \
    "s_mul_i32 s70, s67, 262144\n\t" \
    "s_add_u32 s69, s69, s70\n\t" \
    "s_add_u32 s69, s69, 39845888\n\t" \
    "s_add_u32 s40, s36, s69\n\t" \
    "s_addc_u32 s41, s37, 0\n\t" \
    "s_sub_u32 s91, s65, 1\n\t" \
    "v_mov_b32 v25, s91\n\t" \
    "s_barrier\n\t" \
    "ds_read_b32 v168, v11 offset:20480\n\t" \
    "s_waitcnt lgkmcnt(0)\n\t" \
    "v_readfirstlane_b32 s70, v168\n\t" \
    "ds_write_b32 v12, v31 offset:0\n\t" \
    "ds_write_b32 v12, v31 offset:64\n\t" \
    "s_branch qk_go58_%=\n\t" \
    "qk_Gbad65_%=:\n\t" \
    "s_waitcnt lgkmcnt(0)\n\t" \
    "s_nop 7\n\t" \
    "v_mov_b32 v72, 0\n\t" \
    "v_mov_b32 v73, 0\n\t" \
    "v_mov_b32 v74, 0\n\t" \
    "v_mov_b32 v75, 0\n\t" \
    "v_mov_b32 v76, 0\n\t" \
    "v_mov_b32 v77, 0\n\t" \
    "v_mov_b32 v78, 0\n\t" \
    "v_mov_b32 v79, 0\n\t" \
    "v_mov_b32 v80, 0\n\t" \
    "v_mov_b32 v81, 0\n\t" \
    "v_mov_b32 v82, 0\n\t" \
    "v_mov_b32 v83, 0\n\t" \
    "v_mov_b32 v84, 0\n\t" \
    "v_mov_b32 v85, 0\n\t" \
    "v_mov_b32 v86, 0\n\t" \
    "v_mov_b32 v87, 0\n\t" \
    "s_cmp_eq_u32 s68, 0\n\t" \
    "s_cbranch_scc1 qk_F70_%=\n\t" \
    "s_sub_u32 s68, s68, 1\n\t" \
    "s_branch qk_G64_%=\n\t" \
    "qk_F70_%=:\n\t" \
    "v_mov_b32 v160, 5767424\n\t" \
    "v_mov_b32 v161, 1\n\t" \
    "global_store_dword v160, v161, s[36:37] sc1\n\t" \
    "s_waitcnt vmcnt(0)\n\t" \
    "s_branch qk_Gc66_%=\n\t" \
    "qk_Gslow67_%=:\n\t" \
    "v_max_u32 v160, v36, v37\n\t" \
    "v_max3_u32 v160, v160, v38, v39\n\t" \
    "v_max3_u32 v160, v160, v40, v41\n\t" \
    "v_max3_u32 v160, v160, v42, v43\n\t" \
    "v_max3_u32 v160, v160, v44, v45\n\t" \
    "v_max3_u32 v160, v160, v46, v47\n\t" \
    "v_max3_u32 v160, v160, v48, v49\n\t" \
    "v_max3_u32 v160, v160, v50, v51\n\t" \
    "v_max3_u32 v160, v160, v52, v53\n\t" \
    "v_max3_u32 v160, v160, v54, v55\n\t" \
    "v_max3_u32 v160, v160, v56, v57\n\t" \
    "v_max3_u32 v160, v160, v58, v59\n\t" \
    "v_max3_u32 v160, v160, v60, v61\n\t" \
    "v_max3_u32 v160, v160, v62, v63\n\t" \
    "v_max3_u32 v160, v160, v64, v65\n\t" \
    "v_max3_u32 v160, v160, v66, v67\n\t" \
    "v_max3_u32 v160, v160, v68, v69\n\t" \
    "v_max3_u32 v160, v160, v70, v71\n\t" \
    "v_cmp_eq_u32 vcc, -1, v160\n\t" \
    "s_cbranch_vccz qk_Gback68_%=\n\t" \
    "s_waitcnt lgkmcnt(0)\n\t" \
    "v_sub_u32 v160, v1, v3\n\t" \
    "s_nop 0\n\t" \
    "v_readfirstlane_b32 s69, v160\n\t" \
    "v_add_u32 v163, 20480, v0\n\t" \
    "s_nop 7\n\t" \
    "s_add_u32 s30, s46, s69\n\t" \
    "s_addc_u32 s31, s47, 0\n\t" \
    "qk_Gag71_%=:\n\t" \
    "global_load_dwordx4 v[36:39], v1, s[46:47] sc1\n\t" \
    "global_load_dwordx4 v[40:43], v23, s[46:47] sc1\n\t" \
    "global_load_dwordx4 v[44:47], v163, s[30:31] sc1\n\t" \
    "global_load_dwordx4 v[48:51], v4, s[30:31] sc1\n\t" \
    "global_load_dwordx4 v[52:55], v5, s[30:31] sc1\n\t" \
    "global_load_dwordx4 v[56:59], v6, s[30:31] sc1\n\t" \
    "global_load_dwordx4 v[60:63], v7, s[30:31] sc1\n\t" \
    "global_load_dwordx4 v[64:67], v8, s[30:31] sc1\n\t" \
    "global_load_dwordx4 v[68:71], v9, s[30:31] sc1\n\t" \
    "s_waitcnt vmcnt(0)\n\t" \
    "v_max_u32 v160, v36, v37\n\t" \
    "v_max3_u32 v160, v160, v38, v39\n\t" \
    "v_max3_u32 v160, v160, v40, v41\n\t" \
    "v_max3_u32 v160, v160, v42, v43\n\t" \
    "v_max3_u32 v160, v160, v44, v45\n\t" \
    "v_max3_u32 v160, v160, v46, v47\n\t" \
    "v_max3_u32 v160, v160, v48, v49\n\t" \
    "v_max3_u32 v160, v160, v50, v51\n\t" \
    "v_max3_u32 v160, v160, v52, v53\n\t" \
    "v_max3_u32 v160, v160, v54, v55\n\t" \
    "v_max3_u32 v160, v160, v56, v57\n\t" \
    "v_max3_u32 v160, v160, v58, v59\n\t" \
    "v_max3_u32 v160, v160, v60, v61\n\t" \
    "v_max3_u32 v160, v160, v62, v63\n\t" \
    "v_max3_u32 v160, v160, v64, v65\n\t" \
    "v_max3_u32 v160, v160, v66, v67\n\t" \
    "v_max3_u32 v160, v160, v68, v69\n\t" \
    "v_max3_u32 v160, v160, v70, v71\n\t" \
    "v_cmp_eq_u32 vcc, -1, v160\n\t" \
    "s_cbranch_vccz qk_Gredo72_%=\n\t" \
    "s_cmp_eq_u32 s68, 0\n\t" \
    "s_cbranch_scc1 qk_F73_%=\n\t" \
    "s_sub_u32 s68, s68, 1\n\t" \
    "s_sleep 1\n\t" \
    "s_branch qk_Gag71_%=\n\t" \
    "qk_F73_%=:\n\t" \
    "v_mov_b32 v160, 5767424\n\t" \
    "v_mov_b32 v161, 2\n\t" \
    "global_store_dword v160, v161, s[36:37] sc1\n\t" \
    "s_waitcnt vmcnt(0)\n\t" \
    "s_branch qk_Gredo72_%=\n\t" \
    "qk_Gredo72_%=:\n\t" \
    "v_mov_b32 v72, 0\n\t" \
    "v_mov_b32 v73, 0\n\t" \
    "v_mov_b32 v74, 0\n\t" \
    "v_mov_b32 v75, 0\n\t" \
    "v_mov_b32 v76, 0\n\t" \
    "v_mov_b32 v77, 0\n\t" \
    "v_mov_b32 v78, 0\n\t" \
    "v_mov_b32 v79, 0\n\t" \
    "v_mov_b32 v80, 0\n\t" \
    "v_mov_b32 v81, 0\n\t" \
    "v_mov_b32 v82, 0\n\t" \
    "v_mov_b32 v83, 0\n\t" \
    "v_mov_b32 v84, 0\n\t" \
    "v_mov_b32 v85, 0\n\t" \
    "v_mov_b32 v86, 0\n\t" \
    "v_mov_b32 v87, 0\n\t" \
    "v_smfmac_f32_16x16x64_bf16 v[72:75], v[36:39], a[32:39], v10\n\t" \
    "v_smfmac_f32_16x16x64_bf16 v[72:75], v[40:43], a[40:47], v10\n\t" \
    "v_smfmac_f32_16x16x64_bf16 v[72:75], v[48:51], a[48:55], v10\n\t" \
    "v_smfmac_f32_16x16x64_bf16 v[72:75], v[52:55], a[56:63], v10\n\t" \
    "v_smfmac_f32_16x16x64_bf16 v[72:75], v[56:59], a[0:7], v10\n\t" \
    "v_smfmac_f32_16x16x64_bf16 v[72:75], v[60:63], a[8:15], v10\n\t" \
    "v_smfmac_f32_16x16x64_bf16 v[72:75], v[64:67], a[16:23], v10\n\t" \
    "v_smfmac_f32_16x16x64_bf16 v[72:75], v[68:71], a[24:31], v10\n\t" \
    "v_smfmac_f32_16x16x64_bf16 v[72:75], v[44:47], a[64:71], v10\n\t" \
    "v_smfmac_f32_16x16x64_bf16 v[76:79], v[36:39], a[104:111], v10\n\t" \
    "v_smfmac_f32_16x16x64_bf16 v[76:79], v[40:43], a[112:119], v10\n\t" \
    "v_smfmac_f32_16x16x64_bf16 v[76:79], v[48:51], a[120:127], v10\n\t" \
    "v_smfmac_f32_16x16x64_bf16 v[76:79], v[52:55], a[128:135], v10\n\t" \
    "v_smfmac_f32_16x16x64_bf16 v[76:79], v[56:59], a[72:79], v10\n\t" \
    "v_smfmac_f32_16x16x64_bf16 v[76:79], v[60:63], a[80:87], v10\n\t" \
    "v_smfmac_f32_16x16x64_bf16 v[76:79], v[64:67], a[88:95], v10\n\t" \
    "v_smfmac_f32_16x16x64_bf16 v[76:79], v[68:71], a[96:103], v10\n\t" \
    "v_smfmac_f32_16x16x64_bf16 v[76:79], v[44:47], a[136:143], v10\n\t" \
    "v_smfmac_f32_16x16x64_bf16 v[80:83], v[36:39], a[176:183], v10\n\t" \
    "v_smfmac_f32_16x16x64_bf16 v[80:83], v[40:43], a[184:191], v10\n\t" \
    "v_smfmac_f32_16x16x64_bf16 v[80:83], v[48:51], a[192:199], v10\n\t" \
    "v_smfmac_f32_16x16x64_bf16 v[80:83], v[52:55], a[200:207], v10\n\t" \
    "v_smfmac_f32_16x16x64_bf16 v[80:83], v[56:59], a[144:151], v10\n\t" \
    "v_smfmac_f32_16x16x64_bf16 v[80:83], v[60:63], a[152:159], v10\n\t" \
    "v_smfmac_f32_16x16x64_bf16 v[80:83], v[64:67], a[160:167], v10\n\t" \
    "v_smfmac_f32_16x16x64_bf16 v[80:83], v[68:71], a[168:175], v10\n\t" \
    "v_smfmac_f32_16x16x64_bf16 v[80:83], v[44:47], a[208:215], v10\n\t" \
    "v_smfmac_f32_16x16x64_bf16 v[84:87], v[36:39], a[248:255], v10\n\t" \
    "v_smfmac_f32_16x16x64_bf16 v[84:87], v[40:43], v[224:231], v10\n\t" \
    "v_smfmac_f32_16x16x64_bf16 v[84:87], v[48:51], v[232:239], v10\n\t" \
    "v_smfmac_f32_16x16x64_bf16 v[84:87], v[52:55], v[240:247], v10\n\t" \
    "v_smfmac_f32_16x16x64_bf16 v[84:87], v[56:59], a[216:223], v10\n\t" \
    "v_smfmac_f32_16x16x64_bf16 v[84:87], v[60:63], a[224:231], v10\n\t" \
    "v_smfmac_f32_16x16x64_bf16 v[84:87], v[64:67], a[232:239], v10\n\t" \
    "v_smfmac_f32_16x16x64_bf16 v[84:87], v[68:71], a[240:247], v10\n\t" \
    "v_smfmac_f32_16x16x64_bf16 v[84:87], v[44:47], v[248:255], v10\n\t" \
    "v_add_f32 v72, v72, v74\n\t" \
    "v_add_f32 v73, v73, v75\n\t" \
    "v_cvt_pk_bf16_f32 v156, v72, v73\n\t" \
    "ds_write_b32 v12, v156 offset:0\n\t" \
    "v_add_f32 v76, v76, v78\n\t" \
    "v_add_f32 v77, v77, v79\n\t" \
    "v_cvt_pk_bf16_f32 v157, v76, v77\n\t" \
    "ds_write_b32 v12, v157 offset:64\n\t" \
    "v_add_f32 v80, v80, v82\n\t" \
    "v_add_f32 v81, v81, v83\n\t" \
    "v_cvt_pk_bf16_f32 v158, v80, v81\n\t" \
    "ds_write_b32 v16, v158 offset:0\n\t" \
    "v_pk_add_f32 v[84:85], v[84:85], v[86:87]\n\t" \
    "v_cvt_pk_bf16_f32 v159, v84, v85\n\t" \
    "ds_write_b32 v16, v159 offset:256\n\t" \
    "s_branch qk_Gback68_%=\n\t" \
    "qk_kf59_%=:\n\t" \
    "global_store_dword v14, v25, s[100:101] sc1\n\t" \
    "s_branch qk_kb60_%=\n\t" \
    "qk_last57_%=:\n\t" \
    "s_mov_b64 exec, s[58:59]\n\t" \
    "global_store_dwordx4 v0, v[140:143], s[40:41] offset:0 sc1\n\t" \
    "global_store_dwordx4 v0, v[144:147], s[40:41] offset:1024 sc1\n\t" \
    "global_store_dwordx4 v0, v[148:151], s[40:41] offset:2048 sc1\n\t" \
    "global_store_dwordx4 v0, v[152:155], s[40:41] offset:3072 sc1\n\t" \
    "s_mov_b64 exec, -1\n\t" \
    "s_endpgm\n\t" \
    "qk_loop14_%=:\n\t" \
    "s_mov_b32 s38, 0x22336600\n\t" \
    "s_mov_b32 s39, 0x1a\n\t" \
    "v_mov_b32 v72, 0\n\t" \
    "v_mov_b32 v73, 0\n\t" \
    "v_mov_b32 v74, 0\n\t" \
    "v_mov_b32 v75, 0\n\t" \
    "v_mov_b32 v76, 0\n\t" \
    "v_mov_b32 v77, 0\n\t" \
    "v_mov_b32 v78, 0\n\t" \
    "v_mov_b32 v79, 0\n\t" \
    "v_mov_b32 v80, 0\n\t" \
    "v_mov_b32 v81, 0\n\t" \
    "v_mov_b32 v82, 0\n\t" \
    "v_mov_b32 v83, 0\n\t" \
    "v_mov_b32 v84, 0\n\t" \
    "v_mov_b32 v85, 0\n\t" \
    "v_mov_b32 v86, 0\n\t" \
    "v_mov_b32 v87, 0\n\t" \
    "s_add_u32 s69, s65, 0\n\t" \
    "s_and_b32 s69, s69, 7\n\t" \
    "s_lshl_b32 s69, s69, 18\n\t" \
    "s_add_u32 s69, s69, s62\n\t" \
    "s_add_u32 s46, s36, s69\n\t" \
    "s_addc_u32 s47, s37, 0\n\t" \
    "s_lshr_b64 s[38:39], s[38:39], 4\n\t" \
    "s_and_b32 s97, s38, 15\n\t" \
    "s_lshl_b32 s97, s97, 6\n\t" \
    "s_and_b32 s69, s65, 7\n\t" \
    "s_lshl_b32 s69, s69, 18\n\t" \
    "s_add_u32 s69, s69, s63\n\t" \
    "s_add_u32 s52, s36, s69\n\t" \
    "s_addc_u32 s53, s37, 0\n\t" \
    "s_sub_u32 s70, s79, s74\n\t" \
    "s_max_i32 s70, s70, 0\n\t" \
    "s_min_i32 s70, s70, 16000\n\t" \
    "s_lshl_b32 s70, s70, 1\n\t" \
    "s_mul_hi_u32 s70, s70, s77\n\t" \
    "s_add_u32 s70, s70, s73\n\t" \
    ".p2align 6\n\t" \
    "s_nop 0\n\t" \
    "s_nop 0\n\t" \
    "s_nop 0\n\t" \
    "s_nop 0\n\t" \
    "s_nop 0\n\t" \
    "s_nop 0\n\t" \
    "qk_go75_%=:\n\t" \
    "s_getpc_b64 s[30:31]\n\t" \
    "qk_Wp77_%=:\n\t" \
    "s_add_u32 s30, s30, qk_We78_%=-qk_Wp77_%=\n\t" \
    "s_addc_u32 s31, s31, 0\n\t" \
    "s_sub_u32 s70, s70, 96\n\t" \
    "qk_W76_%=:\n\t" \
    "s_memtime s[56:57]\n\t" \
    "s_waitcnt lgkmcnt(0)\n\t" \
    "s_sub_u32 s69, s70, s56\n\t" \
    "s_cmp_gt_i32 s69, 80\n\t" \
    "s_cbranch_scc1 qk_W76_%=\n\t" \
    "s_max_i32 s69, s69, 0\n\t" \
    "s_and_b32 s69, s69, -4\n\t" \
    "s_sub_u32 s30, s30, s69\n\t" \
    "s_subb_u32 s31, s31, 0\n\t" \
    "s_setpc_b64 s[30:31]\n\t" \
    "s_nop 0\n\t" \
    "s_nop 0\n\t" \
    "s_nop 0\n\t" \
    "s_nop 0\n\t" \
    "s_nop 0\n\t" \
    "s_nop 0\n\t" \
    "s_nop 0\n\t" \
    "s_nop 0\n\t" \
    "s_nop 0\n\t" \
    "s_nop 0\n\t" \
    "s_nop 0\n\t" \
    "s_nop 0\n\t" \
    "s_nop 0\n\t" \
    "s_nop 0\n\t" \
    "s_nop 0\n\t" \
    "s_nop 0\n\t" \
    "s_nop 0\n\t" \
    "s_nop 0\n\t" \
    "s_nop 0\n\t" \
    "s_nop 0\n\t" \
    "qk_We78_%=:\n\t" \
    "qk_G79_%=:\n\t" \
    "global_load_dwordx4 v[36:39], v1, s[46:47] sc1\n\t" \
    "global_load_dword v161, v2, s[46:47] sc1\n\t" \
    "global_load_dwordx4 v[40:43], v23, s[46:47] sc1\n\t" \
    "s_mul_hi_u32 s69, s84, s78\n\t" \
    "s_add_u32 s33, s74, s69\n\t" \
    "s_add_u32 s33, s33, s97\n\t" \
    "s_lshr_b32 s69, s90, 16\n\t" \
    "s_add_u32 s34, s33, s69\n\t" \
    "s_and_b32 s69, s90, 0xffff\n\t" \
    "s_sub_u32 s35, s80, s74\n\t" \
    "s_sub_u32 s35, s35, s69\n\t" \
    "s_add_u32 s99, s35, 240\n\t" \
    "s_add_u32 s98, s33, 448\n\t" \
    "s_sub_u32 s96, s79, s74\n\t" \
    "s_cmp_le_u32 s65, 0\n\t" \
    "s_cselect_b32 s69, 112, 0\n\t" \
    "s_add_u32 s69, s69, 448\n\t" \
    "s_add_u32 s96, s96, s69\n\t" \
    "s_max_i32 s35, s35, s96\n\t" \
    "s_add_u32 s99, s35, 716\n\t" \
    "s_mul_i32 s69, s35, 10\n\t" \
    "s_ashr_i32 s69, s69, 4\n\t" \
    "s_sub_u32 s69, s33, s69\n\t" \
    "v_lshrrev_b32_e64 v163, 1, s69\n\t" \
    "s_waitcnt vmcnt(2)\n\t" \
    "ds_write_b128 v3, v[36:39]\n\t" \
    "v_smfmac_f32_16x16x64_bf16 v[72:75], v[36:39], a[48:55], v10\n\t" \
    "s_waitcnt vmcnt(1)\n\t" \
    "ds_write_b32 v22, v161 offset:20224\n\t" \
    "v_smfmac_f32_16x16x64_bf16 v[76:79], v[36:39], a[120:127], v10\n\t" \
    "s_waitcnt vmcnt(0)\n\t" \
    "ds_write_b128 v3, v[40:43] offset:1024\n\t" \
    "v_max3_u32 v160, v36, v40, v161\n\t" \
    "v_cmp_eq_u32 vcc, -1, v160\n\t" \
    "v_smfmac_f32_16x16x64_bf16 v[80:83], v[36:39], a[192:199], v10\n\t" \
    "v_smfmac_f32_16x16x64_bf16 v[84:87], v[36:39], v[232:239], v10\n\t" \
    "v_smfmac_f32_16x16x64_bf16 v[72:75], v[40:43], a[56:63], v10\n\t" \
    "s_cbranch_vccnz qk_Gbad80_%=\n\t" \
    "qk_Gc81_%=:\n\t" \
    "s_waitcnt lgkmcnt(0)\n\t" \
    "s_barrier\n\t" \
    "s_memtime s[56:57]\n\t" \
    "ds_read_b128 v[48:51], v4\n\t" \
    "ds_read_b128 v[52:55], v5\n\t" \
    "v_smfmac_f32_16x16x64_bf16 v[76:79], v[40:43], a[128:135], v10\n\t" \
    "ds_read_b128 v[56:59], v6\n\t" \
    "ds_read_b128 v[60:63], v7\n\t" \
    "v_smfmac_f32_16x16x64_bf16 v[80:83], v[40:43], a[200:207], v10\n\t" \
    "ds_read_b128 v[64:67], v8\n\t" \
    "ds_read_b128 v[68:71], v9\n\t" \
    "v_smfmac_f32_16x16x64_bf16 v[84:87], v[40:43], v[240:247], v10\n\t" \
    "ds_read_b128 v[44:47], v0 offset:19456\n\t" \
    "s_waitcnt lgkmcnt(6)\n\t" \
    "v_smfmac_f32_16x16x64_bf16 v[72:75], v[48:51], a[0:7], v10\n\t" \
    "s_sub_u32 s69, s56, s73\n\t" \
    "s_mul_hi_u32 s69, s69, s78\n\t" \
    "s_waitcnt lgkmcnt(5)\n\t" \
    "v_smfmac_f32_16x16x64_bf16 v[72:75], v[52:55], a[8:15], v10\n\t" \
    "s_max_i32 s69, s69, s96\n\t" \
    "s_max_i32 s70, s69, s35\n\t" \
    "s_waitcnt lgkmcnt(4)\n\t" \
    "v_smfmac_f32_16x16x64_bf16 v[72:75], v[56:59], a[16:23], v10\n\t" \
    "s_min_i32 s70, s70, s99\n\t" \
    "s_mul_i32 s70, s70, 10\n\t" \
    "s_waitcnt lgkmcnt(3)\n\t" \
    "v_smfmac_f32_16x16x64_bf16 v[72:75], v[60:63], a[24:31], v10\n\t" \
    "s_lshl4_add_u32 s70, s69, s70\n\t" \
    "s_ashr_i32 s70, s70, 5\n\t" \
    "v_add_lshl_u32 v162, s70, v163, 1\n\t" \
    "s_waitcnt lgkmcnt(2)\n\t" \
    "v_smfmac_f32_16x16x64_bf16 v[72:75], v[64:67], a[32:39], v10\n\t" \
    "global_store_dword v21, v162, s[52:53] sc1\n\t" \
    "s_waitcnt lgkmcnt(1)\n\t" \
    "v_smfmac_f32_16x16x64_bf16 v[72:75], v[68:71], a[40:47], v10\n\t" \
    "s_waitcnt lgkmcnt(0)\n\t" \
    "v_smfmac_f32_16x16x64_bf16 v[72:75], v[44:47], a[64:71], v10\n\t" \
    "s_nop 2\n\t" \
    "v_smfmac_f32_16x16x64_bf16 v[76:79], v[48:51], a[72:79], v10\n\t" \
    "s_nop 2\n\t" \
    "v_smfmac_f32_16x16x64_bf16 v[76:79], v[52:55], a[80:87], v10\n\t" \
    "v_add_f32 v72, v72, v74\n\t" \
    "v_smfmac_f32_16x16x64_bf16 v[76:79], v[56:59], a[88:95], v10\n\t" \
    "v_add_f32 v73, v73, v75\n\t" \
    "v_smfmac_f32_16x16x64_bf16 v[76:79], v[60:63], a[96:103], v10\n\t" \
    "v_cvt_pk_bf16_f32 v156, v72, v73\n\t" \
    "v_smfmac_f32_16x16x64_bf16 v[76:79], v[64:67], a[104:111], v10\n\t" \
    "ds_write_b32 v12, v156 offset:0\n\t" \
    "v_smfmac_f32_16x16x64_bf16 v[76:79], v[68:71], a[112:119], v10\n\t" \
    "v_smfmac_f32_16x16x64_bf16 v[76:79], v[44:47], a[136:143], v10\n\t" \
    "s_nop 2\n\t" \
    "v_smfmac_f32_16x16x64_bf16 v[80:83], v[48:51], a[144:151], v10\n\t" \
    "s_nop 2\n\t" \
    "v_smfmac_f32_16x16x64_bf16 v[80:83], v[52:55], a[152:159], v10\n\t" \
    "v_add_f32 v76, v76, v78\n\t" \
    "v_smfmac_f32_16x16x64_bf16 v[80:83], v[56:59], a[160:167], v10\n\t" \
    "v_add_f32 v77, v77, v79\n\t" \
    "v_smfmac_f32_16x16x64_bf16 v[80:83], v[60:63], a[168:175], v10\n\t" \
    "v_cvt_pk_bf16_f32 v157, v76, v77\n\t" \
    "v_smfmac_f32_16x16x64_bf16 v[80:83], v[64:67], a[176:183], v10\n\t" \
    "ds_write_b32 v12, v157 offset:64\n\t" \
    "v_smfmac_f32_16x16x64_bf16 v[80:83], v[68:71], a[184:191], v10\n\t" \
    "v_smfmac_f32_16x16x64_bf16 v[80:83], v[44:47], a[208:215], v10\n\t" \
    "s_nop 2\n\t" \
    "v_smfmac_f32_16x16x64_bf16 v[84:87], v[48:51], a[216:223], v10\n\t" \
    "s_nop 2\n\t" \
    "v_smfmac_f32_16x16x64_bf16 v[84:87], v[52:55], a[224:231], v10\n\t" \
    "v_add_f32 v80, v80, v82\n\t" \
    "v_smfmac_f32_16x16x64_bf16 v[84:87], v[56:59], a[232:239], v10\n\t" \
    "v_add_f32 v81, v81, v83\n\t" \
    "v_smfmac_f32_16x16x64_bf16 v[84:87], v[60:63], a[240:247], v10\n\t" \
    "v_cvt_pk_bf16_f32 v158, v80, v81\n\t" \
    "v_smfmac_f32_16x16x64_bf16 v[84:87], v[64:67], a[248:255], v10\n\t" \
    "ds_write_b32 v16, v158 offset:0\n\t" \
    "v_smfmac_f32_16x16x64_bf16 v[84:87], v[68:71], v[224:231], v10\n\t" \
    "v_smfmac_f32_16x16x64_bf16 v[84:87], v[44:47], v[248:255], v10\n\t" \
    "s_nop 6\n\t" \
    "v_cmp_u_f32 vcc, v72, v73\n\t" \
    "v_pk_add_f32 v[84:85], v[84:85], v[86:87]\n\t" \
    "v_cvt_pk_bf16_f32 v159, v84, v85\n\t" \
    "ds_write_b32 v16, v159 offset:256\n\t" \
    "s_cbranch_vccnz qk_Gslow82_%=\n\t" \
    "qk_Gback83_%=:\n\t" \
    "s_waitcnt lgkmcnt(0)\n\t" \
    "s_barrier\n\t" \
    "s_cmp_eq_u32 s65, 63\n\t" \
    "s_cbranch_scc1 qk_last74_%=\n\t" \
    "s_cmp_lg_u32 s65, 0\n\t" \
    "s_cbranch_scc1 qk_ng84_%=\n\t" \
    "v_mov_b32 v168, 5769216\n\t" \
    "v_mov_b32 v169, 1\n\t" \
    "s_mov_b64 exec, 1\n\t" \
    "global_atomic_add v170, v168, v169, s[36:37] sc0 sc1\n\t" \
    "s_mov_b64 exec, -1\n\t" \
    "v_mbcnt_lo_u32_b32 v168, -1, 0\n\t" \
    "v_and_b32 v169, 3, v168\n\t" \
    "v_lshlrev_b32 v169, 16, v169\n\t" \
    "v_bfe_u32 v171, v168, 2, 2\n\t" \
    "v_lshl_add_u32 v169, v171, 11, v169\n\t" \
    "v_bfe_u32 v171, v168, 4, 1\n\t" \
    "v_lshl_add_u32 v169, v171, 14, v169\n\t" \
    "v_add_u32 v169, s63, v169\n\t" \
    "s_waitcnt vmcnt(0)\n\t" \
    "v_readfirstlane_b32 s69, v170\n\t" \
    "s_nop 1\n\t" \
    "s_cmp_lg_u32 s69, 126\n\t" \
    "s_cbranch_scc1 qk_go85_%=\n\t" \
    "s_memrealtime s[56:57]\n\t" \
    "s_waitcnt lgkmcnt(0)\n\t" \
    "s_lshl_b32 s69, s56, 4\n\t" \
    "s_add_u32 s69, s69, 2800\n\t" \
    "s_add_u32 s70, s56, 24\n\t" \
    "s_lshr_b32 s70, s70, 4\n\t" \
    "v_lshrrev_b32 v171, 2, v168\n\t" \
    "v_add_u32 v171, s70, v171\n\t" \
    "v_and_b32 v171, 63, v171\n\t" \
    "v_lshlrev_b32 v171, 6, v171\n\t" \
    "v_and_b32 v170, 3, v168\n\t" \
    "v_lshl_add_u32 v171, v170, 12, v171\n\t" \
    "v_add_u32 v171, 5783552, v171\n\t" \
    "v_mov_b32 v168, s69\n\t" \
    "s_mov_b32 exec_hi, 0\n\t" \
    "global_store_dword v171, v168, s[36:37] sc1\n\t" \
    "global_store_dword v169, v168, s[36:37] sc1\n\t" \
    "s_mov_b32 exec_hi, -1\n\t" \
    "qk_go85_%=:\n\t" \
    "v_mov_b32 v21, v33\n\t" \
    "qk_ng84_%=:\n\t" \
    "s_sub_u32 s70, s80, s74\n\t" \
    "s_max_i32 s70, s70, 0\n\t" \
    "s_min_i32 s70, s70, 16000\n\t" \
    "s_lshl_b32 s70, s70, 1\n\t" \
    "s_mul_hi_u32 s70, s70, s77\n\t" \
    "s_add_u32 s70, s70, s73\n\t" \
    "qk_W86_%=:\n\t" \
    "s_memtime s[56:57]\n\t" \
    "s_waitcnt lgkmcnt(0)\n\t" \
    "s_sub_u32 s69, s70, s56\n\t" \
    "s_cmp_gt_i32 s69, 24\n\t" \
    "s_cbranch_scc1 qk_W86_%=\n\t" \
    "global_load_dword v160, v20, s[52:53] sc1\n\t" \
    "s_waitcnt lgkmcnt(0)\n\t" \
    "s_mov_b32 s70, 116\n\t" \
    "qk_E90_%=:\n\t" \
    "s_memtime s[56:57]\n\t" \
    "s_memrealtime s[2:3]\n\t" \
    "s_nop 0\n\t" \
    "s_memrealtime s[4:5]\n\t" \
    "s_nop 0\n\t" \
    "s_memrealtime s[6:7]\n\t" \
    "s_nop 0\n\t" \
    "s_memrealtime s[8:9]\n\t" \
    "s_nop 0\n\t" \
    "s_memrealtime s[10:11]\n\t" \
    "s_nop 0\n\t" \
    "s_memrealtime s[12:13]\n\t" \
    "s_nop 0\n\t" \
    "s_memrealtime s[14:15]\n\t" \
    "s_nop 0\n\t" \
    "s_memrealtime s[16:17]\n\t" \
    "s_nop 0\n\t" \
    "s_memrealtime s[18:19]\n\t" \
    "s_nop 0\n\t" \
    "s_memrealtime s[20:21]\n\t" \
    "s_nop 0\n\t" \
    "s_memrealtime s[22:23]\n\t" \
    "s_nop 0\n\t" \
    "s_memrealtime s[24:25]\n\t" \
    "s_nop 0\n\t" \
    "s_memrealtime s[26:27]\n\t" \
    "s_nop 0\n\t" \
    "s_memrealtime s[28:29]\n\t" \
    "s_memtime s[54:55]\n\t" \
    "s_add_u32 s70, s70, 8\n\t" \
    "s_waitcnt lgkmcnt(0)\n\t" \
    "s_sub_u32 s69, s54, s56\n\t" \
    "s_cmp_gt_u32 s69, s70\n\t" \
    "s_cbranch_scc1 qk_E90_%=\n\t" \
    "s_add_u32 s2, s2, s4\n\t" \
    "s_add_u32 s6, s6, s8\n\t" \
    "s_add_u32 s10, s10, s12\n\t" \
    "s_add_u32 s14, s14, s16\n\t" \
    "s_add_u32 s18, s18, s20\n\t" \
    "s_add_u32 s22, s22, s24\n\t" \
    "s_add_u32 s2, s2, s6\n\t" \
    "s_add_u32 s10, s10, s14\n\t" \
    "s_add_u32 s18, s18, s22\n\t" \
    "s_add_u32 s2, s2, s26\n\t" \
    "s_add_u32 s10, s10, s18\n\t" \
    "s_add_u32 s2, s2, s10\n\t" \
    "s_mul_i32 s70, s28, 13\n\t" \
    "s_sub_u32 s70, s70, s2\n\t" \
    "s_min_u32 s70, s70, 52\n\t" \
    "s_mul_i32 s69, s69, s70\n\t" \
    "s_mul_hi_u32 s69, s69, 76695844\n\t" \
    "s_mov_b32 s75, s73\n\t" \
    "s_mov_b32 s76, s74\n\t" \
    "s_add_u32 s73, s56, s69\n\t" \
    "s_lshl_b32 s74, s28, 4\n\t" \
    "s_sub_u32 s69, s73, s75\n\t" \
    "s_sub_u32 s70, s74, s76\n\t" \
    "s_sub_u32 s71, s70, 1600\n\t" \
    "s_cmp_lt_u32 s71, 400000\n\t" \
    "s_cbranch_scc0 qk_K91_%=\n\t" \
    "v_cvt_f32_u32 v168, s69\n\t" \
    "v_cvt_f32_u32 v169, s70\n\t" \
    "v_rcp_f32 v170, v168\n\t" \
    "v_rcp_f32 v171, v169\n\t" \
    "v_mul_f32 v170, v170, v169\n\t" \
    "v_mul_f32 v171, v171, v168\n\t" \
    "v_mul_f32 v170, 0x4f800000, v170\n\t" \
    "v_mul_f32 v171, 0x4f000000, v171\n\t" \
    "v_cvt_u32_f32 v170, v170\n\t" \
    "v_cvt_u32_f32 v171, v171\n\t" \
    "v_readfirstlane_b32 s69, v170\n\t" \
    "v_readfirstlane_b32 s70, v171\n\t" \
    "s_nop 1\n\t" \
    "s_min_u32 s71, s65, 1\n\t" \
    "s_sub_u32 s69, s69, s78\n\t" \
    "s_ashr_i32 s69, s69, s71\n\t" \
    "s_add_u32 s78, s78, s69\n\t" \
    "s_sub_u32 s70, s70, s77\n\t" \
    "s_ashr_i32 s70, s70, s71\n\t" \
    "s_add_u32 s77, s77, s70\n\t" \
    "qk_K91_%=:\n\t" \
    "s_add_u32 s65, s65, 1\n\t" \
    "v_mov_b32 v72, 0\n\t" \
    "v_mov_b32 v73, 0\n\t" \
    "v_mov_b32 v74, 0\n\t" \
    "v_mov_b32 v75, 0\n\t" \
    "v_mov_b32 v76, 0\n\t" \
    "v_mov_b32 v77, 0\n\t" \
    "v_mov_b32 v78, 0\n\t" \
    "v_mov_b32 v79, 0\n\t" \
    "v_mov_b32 v80, 0\n\t" \
    "v_mov_b32 v81, 0\n\t" \
    "v_mov_b32 v82, 0\n\t" \
    "v_mov_b32 v83, 0\n\t" \
    "v_mov_b32 v84, 0\n\t" \
    "v_mov_b32 v85, 0\n\t" \
    "v_mov_b32 v86, 0\n\t" \
    "v_mov_b32 v87, 0\n\t" \
    "s_add_u32 s69, s65, 0\n\t" \
    "s_and_b32 s69, s69, 7\n\t" \
    "s_lshl_b32 s69, s69, 18\n\t" \
    "s_add_u32 s69, s69, s62\n\t" \
    "s_add_u32 s46, s36, s69\n\t" \
    "s_addc_u32 s47, s37, 0\n\t" \
    "s_lshr_b64 s[38:39], s[38:39], 4\n\t" \
    "s_and_b32 s97, s38, 15\n\t" \
    "s_lshl_b32 s97, s97, 6\n\t" \
    "s_waitcnt vmcnt(0)\n\t" \
    "v_cmp_eq_u32 vcc, -1, v160\n\t" \
    "s_cbranch_vccz qk_Hok88_%=\n\t" \
    "qk_H87_%=:\n\t" \
    "global_load_dword v160, v20, s[52:53] sc1\n\t" \
    "s_waitcnt vmcnt(0)\n\t" \
    "v_cmp_eq_u32 vcc, -1, v160\n\t" \
    "s_cbranch_vccz qk_Hc89_%=\n\t" \
    "s_cmp_eq_u32 s68, 0\n\t" \
    "s_cbranch_scc1 qk_F92_%=\n\t" \
    "s_sub_u32 s68, s68, 1\n\t" \
    "s_sleep 2\n\t" \
    "s_branch qk_H87_%=\n\t" \
    "qk_F92_%=:\n\t" \
    "v_mov_b32 v161, 5767424\n\t" \
    "v_mov_b32 v162, 5\n\t" \
    "global_store_dword v161, v162, s[36:37] sc1\n\t" \
    "s_waitcnt vmcnt(0)\n\t" \
    "s_branch qk_Hc89_%=\n\t" \
    "qk_Hc89_%=:\n\t" \
    "s_memtime s[56:57]\n\t" \
    "s_waitcnt lgkmcnt(0)\n\t" \
    "s_sub_u32 s69, s56, s73\n\t" \
    "s_cmp_lt_u32 s69, 3000\n\t" \
    "s_cbranch_scc1 qk_St93_%=\n\t" \
    "s_waitcnt lgkmcnt(0)\n\t" \
    "s_mov_b32 s70, 116\n\t" \
    "qk_E94_%=:\n\t" \
    "s_memtime s[56:57]\n\t" \
    "s_memrealtime s[2:3]\n\t" \
    "s_nop 0\n\t" \
    "s_memrealtime s[4:5]\n\t" \
    "s_nop 0\n\t" \
    "s_memrealtime s[6:7]\n\t" \
    "s_nop 0\n\t" \
    "s_memrealtime s[8:9]\n\t" \
    "s_nop 0\n\t" \
    "s_memrealtime s[10:11]\n\t" \
    "s_nop 0\n\t" \
    "s_memrealtime s[12:13]\n\t" \
    "s_nop 0\n\t" \
    "s_memrealtime s[14:15]\n\t" \
    "s_nop 0\n\t" \
    "s_memrealtime s[16:17]\n\t" \
    "s_nop 0\n\t" \
    "s_memrealtime s[18:19]\n\t" \
    "s_nop 0\n\t" \
    "s_memrealtime s[20:21]\n\t" \
    "s_nop 0\n\t" \
    "s_memrealtime s[22:23]\n\t" \
    "s_nop 0\n\t" \
    "s_memrealtime s[24:25]\n\t" \
    "s_nop 0\n\t" \
    "s_memrealtime s[26:27]\n\t" \
    "s_nop 0\n\t" \
    "s_memrealtime s[28:29]\n\t" \
    "s_memtime s[54:55]\n\t" \
    "s_add_u32 s70, s70, 8\n\t" \
    "s_waitcnt lgkmcnt(0)\n\t" \
    "s_sub_u32 s69, s54, s56\n\t" \
    "s_cmp_gt_u32 s69, s70\n\t" \
    "s_cbranch_scc1 qk_E94_%=\n\t" \
    "s_add_u32 s2, s2, s4\n\t" \
    "s_add_u32 s6, s6, s8\n\t" \
    "s_add_u32 s10, s10, s12\n\t" \
    "s_add_u32 s14, s14, s16\n\t" \
    "s_add_u32 s18, s18, s20\n\t" \
    "s_add_u32 s22, s22, s24\n\t" \
    "s_add_u32 s2, s2, s6\n\t" \
    "s_add_u32 s10, s10, s14\n\t" \
    "s_add_u32 s18, s18, s22\n\t" \
    "s_add_u32 s2, s2, s26\n\t" \
    "s_add_u32 s10, s10, s18\n\t" \
    "s_add_u32 s2, s2, s10\n\t" \
    "s_mul_i32 s70, s28, 13\n\t" \
    "s_sub_u32 s70, s70, s2\n\t" \
    "s_min_u32 s70, s70, 52\n\t" \
    "s_mul_i32 s69, s69, s70\n\t" \
    "s_mul_hi_u32 s69, s69, 76695844\n\t" \
    "s_mov_b32 s75, s73\n\t" \
    "s_mov_b32 s76, s74\n\t" \
    "s_add_u32 s73, s56, s69\n\t" \
    "s_lshl_b32 s74, s28, 4\n\t" \
    "qk_St93_%=:\n\t" \
    "qk_Hok88_%=:\n\t" \
    "v_max_u32_dpp v160, v160, v160 row_ror:1 row_mask:0xf bank_mask:0xf\n\t" \
    "s_nop 1\n\t" \
    "v_max_u32_dpp v160, v160, v160 row_ror:2 row_mask:0xf bank_mask:0xf\n\t" \
    "s_nop 1\n\t" \
    "v_max_u32_dpp v160, v160, v160 row_ror:4 row_mask:0xf bank_mask:0xf\n\t" \
    "s_nop 1\n\t" \
    "v_max_u32_dpp v160, v160, v160 row_ror:8 row_mask:0xf bank_mask:0xf\n\t" \
    "s_nop 0\n\t" \
    "v_readlane_b32 s69, v160, 0\n\t" \
    "v_readlane_b32 s71, v160, 16\n\t" \
    "s_nop 1\n\t" \
    "s_max_u32 s81, s69, s71\n\t" \
    "s_mul_hi_u32 s69, s85, s78\n\t" \
    "s_add_u32 s79, s81, s69\n\t" \
    "s_add_u32 s79, s79, s87\n\t" \
    "s_cmp_ge_u32 s65, 53\n\t" \
    "s_cselect_b32 s69, 32, 0\n\t" \
    "s_sub_u32 s79, s79, s69\n\t" \
    "s_cmp_lt_u32 s65, 10\n\t" \
    "s_cselect_b32 s69, 48, 0\n\t" \
    "s_add_u32 s79, s79, s69\n\t" \
    "s_add_u32 s80, s79, s88\n\t" \
    "s_sub_u32 s70, s79, s74\n\t" \
    "s_max_i32 s70, s70, 0\n\t" \
    "s_min_i32 s70, s70, 16000\n\t" \
    "s_lshl_b32 s70, s70, 1\n\t" \
    "s_mul_hi_u32 s70, s70, s77\n\t" \
    "s_add_u32 s70, s70, s73\n\t" \
    "v_mov_b32 v168, s70\n\t" \
    "ds_write_b32 v11, v168 offset:20480\n\t" \
    "s_and_b32 s69, s65, 7\n\t" \
    "s_lshl_b32 s69, s69, 18\n\t" \
    "s_add_u32 s69, s69, s63\n\t" \
    "s_add_u32 s52, s36, s69\n\t" \
    "s_addc_u32 s53, s37, 0\n\t" \
    "s_waitcnt lgkmcnt(0)\n\t" \
    "s_barrier\n\t" \
    "ds_write_b32 v12, v31 offset:0\n\t" \
    "ds_write_b32 v12, v31 offset:64\n\t" \
    "s_branch qk_go75_%=\n\t" \
    "qk_Gbad80_%=:\n\t" \
    "s_waitcnt lgkmcnt(0)\n\t" \
    "s_nop 7\n\t" \
    "v_mov_b32 v72, 0\n\t" \
    "v_mov_b32 v73, 0\n\t" \
    "v_mov_b32 v74, 0\n\t" \
    "v_mov_b32 v75, 0\n\t" \
    "v_mov_b32 v76, 0\n\t" \
    "v_mov_b32 v77, 0\n\t" \
    "v_mov_b32 v78, 0\n\t" \
    "v_mov_b32 v79, 0\n\t" \
    "v_mov_b32 v80, 0\n\t" \
    "v_mov_b32 v81, 0\n\t" \
    "v_mov_b32 v82, 0\n\t" \
    "v_mov_b32 v83, 0\n\t" \
    "v_mov_b32 v84, 0\n\t" \
    "v_mov_b32 v85, 0\n\t" \
    "v_mov_b32 v86, 0\n\t" \
    "v_mov_b32 v87, 0\n\t" \
    "s_cmp_eq_u32 s68, 0\n\t" \
    "s_cbranch_scc1 qk_F95_%=\n\t" \
    "s_sub_u32 s68, s68, 1\n\t" \
    "s_branch qk_G79_%=\n\t" \
    "qk_F95_%=:\n\t" \
    "v_mov_b32 v160, 5767424\n\t" \
    "v_mov_b32 v161, 1\n\t" \
    "global_store_dword v160, v161, s[36:37] sc1\n\t" \
    "s_waitcnt vmcnt(0)\n\t" \
    "s_branch qk_Gc81_%=\n\t" \
    "qk_Gslow82_%=:\n\t" \
    "v_max_u32 v160, v36, v37\n\t" \
    "v_max3_u32 v160, v160, v38, v39\n\t" \
    "v_max3_u32 v160, v160, v40, v41\n\t" \
    "v_max3_u32 v160, v160, v42, v43\n\t" \
    "v_max3_u32 v160, v160, v44, v45\n\t" \
    "v_max3_u32 v160, v160, v46, v47\n\t" \
    "v_max3_u32 v160, v160, v48, v49\n\t" \
    "v_max3_u32 v160, v160, v50, v51\n\t" \
    "v_max3_u32 v160, v160, v52, v53\n\t" \
    "v_max3_u32 v160, v160, v54, v55\n\t" \
    "v_max3_u32 v160, v160, v56, v57\n\t" \
    "v_max3_u32 v160, v160, v58, v59\n\t" \
    "v_max3_u32 v160, v160, v60, v61\n\t" \
    "v_max3_u32 v160, v160, v62, v63\n\t" \
    "v_max3_u32 v160, v160, v64, v65\n\t" \
    "v_max3_u32 v160, v160, v66, v67\n\t" \
    "v_max3_u32 v160, v160, v68, v69\n\t" \
    "v_max3_u32 v160, v160, v70, v71\n\t" \
    "v_cmp_eq_u32 vcc, -1, v160\n\t" \
    "s_cbranch_vccz qk_Gback83_%=\n\t" \
    "s_waitcnt lgkmcnt(0)\n\t" \
    "v_sub_u32 v160, v1, v3\n\t" \
    "s_nop 0\n\t" \
    "v_readfirstlane_b32 s69, v160\n\t" \
    "v_add_u32 v163, 20480, v0\n\t" \
    "s_nop 7\n\t" \
    "s_add_u32 s30, s46, s69\n\t" \
    "s_addc_u32 s31, s47, 0\n\t" \
    "qk_Gag96_%=:\n\t" \
    "global_load_dwordx4 v[36:39], v1, s[46:47] sc1\n\t" \
    "global_load_dwordx4 v[40:43], v23, s[46:47] sc1\n\t" \
    "global_load_dwordx4 v[44:47], v163, s[30:31] sc1\n\t" \
    "global_load_dwordx4 v[48:51], v4, s[30:31] sc1\n\t" \
    "global_load_dwordx4 v[52:55], v5, s[30:31] sc1\n\t" \
    "global_load_dwordx4 v[56:59], v6, s[30:31] sc1\n\t" \
    "global_load_dwordx4 v[60:63], v7, s[30:31] sc1\n\t" \
    "global_load_dwordx4 v[64:67], v8, s[30:31] sc1\n\t" \
    "global_load_dwordx4 v[68:71], v9, s[30:31] sc1\n\t" \
    "s_waitcnt vmcnt(0)\n\t" \
    "v_max_u32 v160, v36, v37\n\t" \
    "v_max3_u32 v160, v160, v38, v39\n\t" \
    "v_max3_u32 v160, v160, v40, v41\n\t" \
    "v_max3_u32 v160, v160, v42, v43\n\t" \
    "v_max3_u32 v160, v160, v44, v45\n\t" \
    "v_max3_u32 v160, v160, v46, v47\n\t" \
    "v_max3_u32 v160, v160, v48, v49\n\t" \
    "v_max3_u32 v160, v160, v50, v51\n\t" \
    "v_max3_u32 v160, v160, v52, v53\n\t" \
    "v_max3_u32 v160, v160, v54, v55\n\t" \
    "v_max3_u32 v160, v160, v56, v57\n\t" \
    "v_max3_u32 v160, v160, v58, v59\n\t" \
    "v_max3_u32 v160, v160, v60, v61\n\t" \
    "v_max3_u32 v160, v160, v62, v63\n\t" \
    "v_max3_u32 v160, v160, v64, v65\n\t" \
    "v_max3_u32 v160, v160, v66, v67\n\t" \
    "v_max3_u32 v160, v160, v68, v69\n\t" \
    "v_max3_u32 v160, v160, v70, v71\n\t" \
    "v_cmp_eq_u32 vcc, -1, v160\n\t" \
    "s_cbranch_vccz qk_Gredo97_%=\n\t" \
    "s_cmp_eq_u32 s68, 0\n\t" \
    "s_cbranch_scc1 qk_F98_%=\n\t" \
    "s_sub_u32 s68, s68, 1\n\t" \
    "s_sleep 1\n\t" \
    "s_branch qk_Gag96_%=\n\t" \
    "qk_F98_%=:\n\t" \
    "v_mov_b32 v160, 5767424\n\t" \
    "v_mov_b32 v161, 2\n\t" \
    "global_store_dword v160, v161, s[36:37] sc1\n\t" \
    "s_waitcnt vmcnt(0)\n\t" \
    "s_branch qk_Gredo97_%=\n\t" \
    "qk_Gredo97_%=:\n\t" \
    "v_mov_b32 v72, 0\n\t" \
    "v_mov_b32 v73, 0\n\t" \
    "v_mov_b32 v74, 0\n\t" \
    "v_mov_b32 v75, 0\n\t" \
    "v_mov_b32 v76, 0\n\t" \
    "v_mov_b32 v77, 0\n\t" \
    "v_mov_b32 v78, 0\n\t" \
    "v_mov_b32 v79, 0\n\t" \
    "v_mov_b32 v80, 0\n\t" \
    "v_mov_b32 v81, 0\n\t" \
    "v_mov_b32 v82, 0\n\t" \
    "v_mov_b32 v83, 0\n\t" \
    "v_mov_b32 v84, 0\n\t" \
    "v_mov_b32 v85, 0\n\t" \
    "v_mov_b32 v86, 0\n\t" \
    "v_mov_b32 v87, 0\n\t" \
    "v_smfmac_f32_16x16x64_bf16 v[72:75], v[36:39], a[48:55], v10\n\t" \
    "v_smfmac_f32_16x16x64_bf16 v[72:75], v[40:43], a[56:63], v10\n\t" \
    "v_smfmac_f32_16x16x64_bf16 v[72:75], v[48:51], a[0:7], v10\n\t" \
    "v_smfmac_f32_16x16x64_bf16 v[72:75], v[52:55], a[8:15], v10\n\t" \
    "v_smfmac_f32_16x16x64_bf16 v[72:75], v[56:59], a[16:23], v10\n\t" \
    "v_smfmac_f32_16x16x64_bf16 v[72:75], v[60:63], a[24:31], v10\n\t" \
    "v_smfmac_f32_16x16x64_bf16 v[72:75], v[64:67], a[32:39], v10\n\t" \
    "v_smfmac_f32_16x16x64_bf16 v[72:75], v[68:71], a[40:47], v10\n\t" \
    "v_smfmac_f32_16x16x64_bf16 v[72:75], v[44:47], a[64:71], v10\n\t" \
    "v_smfmac_f32_16x16x64_bf16 v[76:79], v[36:39], a[120:127], v10\n\t" \
    "v_smfmac_f32_16x16x64_bf16 v[76:79], v[40:43], a[128:135], v10\n\t" \
    "v_smfmac_f32_16x16x64_bf16 v[76:79], v[48:51], a[72:79], v10\n\t" \
    "v_smfmac_f32_16x16x64_bf16 v[76:79], v[52:55], a[80:87], v10\n\t" \
    "v_smfmac_f32_16x16x64_bf16 v[76:79], v[56:59], a[88:95], v10\n\t" \
    "v_smfmac_f32_16x16x64_bf16 v[76:79], v[60:63], a[96:103], v10\n\t" \
    "v_smfmac_f32_16x16x64_bf16 v[76:79], v[64:67], a[104:111], v10\n\t" \
    "v_smfmac_f32_16x16x64_bf16 v[76:79], v[68:71], a[112:119], v10\n\t" \
    "v_smfmac_f32_16x16x64_bf16 v[76:79], v[44:47], a[136:143], v10\n\t" \
    "v_smfmac_f32_16x16x64_bf16 v[80:83], v[36:39], a[192:199], v10\n\t" \
    "v_smfmac_f32_16x16x64_bf16 v[80:83], v[40:43], a[200:207], v10\n\t" \
    "v_smfmac_f32_16x16x64_bf16 v[80:83], v[48:51], a[144:151], v10\n\t" \
    "v_smfmac_f32_16x16x64_bf16 v[80:83], v[52:55], a[152:159], v10\n\t" \
    "v_smfmac_f32_16x16x64_bf16 v[80:83], v[56:59], a[160:167], v10\n\t" \
    "v_smfmac_f32_16x16x64_bf16 v[80:83], v[60:63], a[168:175], v10\n\t" \
    "v_smfmac_f32_16x16x64_bf16 v[80:83], v[64:67], a[176:183], v10\n\t" \
    "v_smfmac_f32_16x16x64_bf16 v[80:83], v[68:71], a[184:191], v10\n\t" \
    "v_smfmac_f32_16x16x64_bf16 v[80:83], v[44:47], a[208:215], v10\n\t" \
    "v_smfmac_f32_16x16x64_bf16 v[84:87], v[36:39], v[232:239], v10\n\t" \
    "v_smfmac_f32_16x16x64_bf16 v[84:87], v[40:43], v[240:247], v10\n\t" \
    "v_smfmac_f32_16x16x64_bf16 v[84:87], v[48:51], a[216:223], v10\n\t" \
    "v_smfmac_f32_16x16x64_bf16 v[84:87], v[52:55], a[224:231], v10\n\t" \
    "v_smfmac_f32_16x16x64_bf16 v[84:87], v[56:59], a[232:239], v10\n\t" \
    "v_smfmac_f32_16x16x64_bf16 v[84:87], v[60:63], a[240:247], v10\n\t" \
    "v_smfmac_f32_16x16x64_bf16 v[84:87], v[64:67], a[248:255], v10\n\t" \
    "v_smfmac_f32_16x16x64_bf16 v[84:87], v[68:71], v[224:231], v10\n\t" \
    "v_smfmac_f32_16x16x64_bf16 v[84:87], v[44:47], v[248:255], v10\n\t" \
    "v_add_f32 v72, v72, v74\n\t" \
    "v_add_f32 v73, v73, v75\n\t" \
    "v_cvt_pk_bf16_f32 v156, v72, v73\n\t" \
    "ds_write_b32 v12, v156 offset:0\n\t" \
    "v_add_f32 v76, v76, v78\n\t" \
    "v_add_f32 v77, v77, v79\n\t" \
    "v_cvt_pk_bf16_f32 v157, v76, v77\n\t" \
    "ds_write_b32 v12, v157 offset:64\n\t" \
    "v_add_f32 v80, v80, v82\n\t" \
    "v_add_f32 v81, v81, v83\n\t" \
    "v_cvt_pk_bf16_f32 v158, v80, v81\n\t" \
    "ds_write_b32 v16, v158 offset:0\n\t" \
    "v_pk_add_f32 v[84:85], v[84:85], v[86:87]\n\t" \
    "v_cvt_pk_bf16_f32 v159, v84, v85\n\t" \
    "ds_write_b32 v16, v159 offset:256\n\t" \
    "s_branch qk_Gback83_%=\n\t" \
    "qk_last74_%=:\n\t" \
    "s_endpgm\n\t" \
    "qk_last_%=:\n\t" \
    ""
#define ASM_V \
    "s_load_dwordx2 s[42:43], %3, 56\n\t" \
    "s_load_dwordx2 s[44:45], %3, 64\n\t" \
    "s_load_dwordx2 s[36:37], %3, 0x58\n\t" \
    "s_load_dwordx2 s[50:51], %3, 0x48\n\t" \
    "s_load_dwordx8 s[84:91], %3, 0\n\t" \
    "s_mov_b32 s66, %4\n\t" \
    "s_mov_b32 s67, %5\n\t" \
    "s_mov_b32 s68, 262144\n\t" \
    "s_mov_b32 s72, 0\n\t" \
    "s_mov_b32 s83, -1\n\t" \
    "s_mov_b32 s65, 0\n\t" \
    "s_lshl_b32 s82, s66, 10\n\t" \
    "s_waitcnt lgkmcnt(0)\n\t" \
    "s_add_u32 s46, s42, 2097152\n\t" \
    "s_addc_u32 s47, s43, 0\n\t" \
    "s_add_u32 m0, s82, 65536\n\t" \
    "s_nop 0\n\t" \
    "global_load_lds_dwordx4 v200, s[46:47]\n\t" \
    "s_add_u32 s46, s42, 2228224\n\t" \
    "s_addc_u32 s47, s43, 0\n\t" \
    "s_add_u32 m0, s82, 69632\n\t" \
    "s_nop 0\n\t" \
    "global_load_lds_dwordx4 v200, s[46:47]\n\t" \
    "s_add_u32 s46, s42, 2359296\n\t" \
    "s_addc_u32 s47, s43, 0\n\t" \
    "s_add_u32 m0, s82, 73728\n\t" \
    "s_nop 0\n\t" \
    "global_load_lds_dwordx4 v200, s[46:47]\n\t" \
    "s_add_u32 s46, s42, 2490368\n\t" \
    "s_addc_u32 s47, s43, 0\n\t" \
    "s_add_u32 m0, s82, 77824\n\t" \
    "s_nop 0\n\t" \
    "global_load_lds_dwordx4 v200, s[46:47]\n\t" \
    "s_add_u32 s46, s42, 2621440\n\t" \
    "s_addc_u32 s47, s43, 0\n\t" \
    "s_add_u32 m0, s82, 81920\n\t" \
    "s_nop 0\n\t" \
    "global_load_lds_dwordx4 v200, s[46:47]\n\t" \
    "s_add_u32 s46, s42, 2752512\n\t" \
    "s_addc_u32 s47, s43, 0\n\t" \
    "s_add_u32 m0, s82, 86016\n\t" \
    "s_nop 0\n\t" \
    "global_load_lds_dwordx4 v200, s[46:47]\n\t" \
    "s_add_u32 s46, s42, 2883584\n\t" \
    "s_addc_u32 s47, s43, 0\n\t" \
    "s_add_u32 m0, s82, 90112\n\t" \
    "s_nop 0\n\t" \
    "global_load_lds_dwordx4 v200, s[46:47]\n\t" \
    "s_add_u32 s46, s42, 3014656\n\t" \
    "s_addc_u32 s47, s43, 0\n\t" \
    "s_add_u32 m0, s82, 94208\n\t" \
    "s_nop 0\n\t" \
    "global_load_lds_dwordx4 v200, s[46:47]\n\t" \
    "s_add_u32 s46, s42, 3145728\n\t" \
    "s_addc_u32 s47, s43, 0\n\t" \
    "s_add_u32 m0, s82, 98304\n\t" \
    "s_nop 0\n\t" \
    "global_load_lds_dwordx4 v200, s[46:47]\n\t" \
    "s_add_u32 s46, s42, 3276800\n\t" \
    "s_addc_u32 s47, s43, 0\n\t" \
    "s_add_u32 m0, s82, 102400\n\t" \
    "s_nop 0\n\t" \
    "global_load_lds_dwordx4 v200, s[46:47]\n\t" \
    "s_add_u32 s46, s42, 3407872\n\t" \
    "s_addc_u32 s47, s43, 0\n\t" \
    "s_add_u32 m0, s82, 106496\n\t" \
    "s_nop 0\n\t" \
    "global_load_lds_dwordx4 v200, s[46:47]\n\t" \
    "s_add_u32 s46, s42, 3538944\n\t" \
    "s_addc_u32 s47, s43, 0\n\t" \
    "s_add_u32 m0, s82, 110592\n\t" \
    "s_nop 0\n\t" \
    "global_load_lds_dwordx4 v200, s[46:47]\n\t" \
    "s_add_u32 s46, s42, 3670016\n\t" \
    "s_addc_u32 s47, s43, 0\n\t" \
    "s_add_u32 m0, s82, 114688\n\t" \
    "s_nop 0\n\t" \
    "global_load_lds_dwordx4 v200, s[46:47]\n\t" \
    "s_add_u32 s46, s42, 3801088\n\t" \
    "s_addc_u32 s47, s43, 0\n\t" \
    "s_add_u32 m0, s82, 118784\n\t" \
    "s_nop 0\n\t" \
    "global_load_lds_dwordx4 v200, s[46:47]\n\t" \
    "s_add_u32 s46, s42, 3932160\n\t" \
    "s_addc_u32 s47, s43, 0\n\t" \
    "s_add_u32 m0, s82, 122880\n\t" \
    "s_nop 0\n\t" \
    "global_load_lds_dwordx4 v200, s[46:47]\n\t" \
    "s_add_u32 s46, s42, 4063232\n\t" \
    "s_addc_u32 s47, s43, 0\n\t" \
    "s_add_u32 m0, s82, 126976\n\t" \
    "s_nop 0\n\t" \
    "global_load_lds_dwordx4 v200, s[46:47]\n\t" \
    "s_add_u32 s46, s42, 4194304\n\t" \
    "s_addc_u32 s47, s43, 0\n\t" \
    "s_add_u32 m0, s82, 131072\n\t" \
    "s_nop 0\n\t" \
    "global_load_lds_dwordx4 v200, s[46:47]\n\t" \
    "s_add_u32 s46, s42, 4325376\n\t" \
    "s_addc_u32 s47, s43, 0\n\t" \
    "s_add_u32 m0, s82, 135168\n\t" \
    "s_nop 0\n\t" \
    "global_load_lds_dwordx4 v200, s[46:47]\n\t" \
    "s_add_u32 s46, s42, 4456448\n\t" \
    "s_addc_u32 s47, s43, 0\n\t" \
    "s_add_u32 m0, s82, 139264\n\t" \
    "s_nop 0\n\t" \
    "global_load_lds_dwordx4 v200, s[46:47]\n\t" \
    "s_add_u32 s46, s42, 4587520\n\t" \
    "s_addc_u32 s47, s43, 0\n\t" \
    "s_add_u32 m0, s82, 143360\n\t" \
    "s_nop 0\n\t" \
    "global_load_lds_dwordx4 v200, s[46:47]\n\t" \
    "s_mov_b32 s77, 3019898880\n\t" \
    "s_mov_b32 s78, 3054198966\n\t" \
    "s_waitcnt lgkmcnt(0)\n\t" \
    "s_mov_b32 s70, 116\n\t" \
    "vv_E1_%=:\n\t" \
    "s_memtime s[56:57]\n\t" \
    "s_memrealtime s[2:3]\n\t" \
    "s_nop 0\n\t" \
    "s_memrealtime s[4:5]\n\t" \
    "s_nop 0\n\t" \
    "s_memrealtime s[6:7]\n\t" \
    "s_nop 0\n\t" \
    "s_memrealtime s[8:9]\n\t" \
    "s_nop 0\n\t" \
    "s_memrealtime s[10:11]\n\t" \
    "s_nop 0\n\t" \
    "s_memrealtime s[12:13]\n\t" \
    "s_nop 0\n\t" \
    "s_memrealtime s[14:15]\n\t" \
    "s_nop 0\n\t" \
    "s_memrealtime s[16:17]\n\t" \
    "s_nop 0\n\t" \
    "s_memrealtime s[18:19]\n\t" \
    "s_nop 0\n\t" \
    "s_memrealtime s[20:21]\n\t" \
    "s_nop 0\n\t" \
    "s_memrealtime s[22:23]\n\t" \
    "s_nop 0\n\t" \
    "s_memrealtime s[24:25]\n\t" \
    "s_nop 0\n\t" \
    "s_memrealtime s[26:27]\n\t" \
    "s_nop 0\n\t" \
    "s_memrealtime s[28:29]\n\t" \
    "s_memtime s[54:55]\n\t" \
    "s_add_u32 s70, s70, 8\n\t" \
    "s_waitcnt lgkmcnt(0)\n\t" \
    "s_sub_u32 s69, s54, s56\n\t" \
    "s_cmp_gt_u32 s69, s70\n\t" \
    "s_cbranch_scc1 vv_E1_%=\n\t" \
    "s_add_u32 s2, s2, s4\n\t" \
    "s_add_u32 s6, s6, s8\n\t" \
    "s_add_u32 s10, s10, s12\n\t" \
    "s_add_u32 s14, s14, s16\n\t" \
    "s_add_u32 s18, s18, s20\n\t" \
    "s_add_u32 s22, s22, s24\n\t" \
    "s_add_u32 s2, s2, s6\n\t" \
    "s_add_u32 s10, s10, s14\n\t" \
    "s_add_u32 s18, s18, s22\n\t" \
    "s_add_u32 s2, s2, s26\n\t" \
    "s_add_u32 s10, s10, s18\n\t" \
    "s_add_u32 s2, s2, s10\n\t" \
    "s_mul_i32 s70, s28, 13\n\t" \
    "s_sub_u32 s70, s70, s2\n\t" \
    "s_min_u32 s70, s70, 52\n\t" \
    "s_mul_i32 s69, s69, s70\n\t" \
    "s_mul_hi_u32 s69, s69, 76695844\n\t" \
    "s_mov_b32 s75, s73\n\t" \
    "s_mov_b32 s76, s74\n\t" \
    "s_add_u32 s73, s56, s69\n\t" \
    "s_lshl_b32 s74, s28, 4\n\t" \
    "s_waitcnt vmcnt(32) lgkmcnt(0)\n\t" \
    "s_barrier\n\t" \
    "ds_read_b64_tr_b16 a[0:1], v201 offset:0\n\t" \
    "ds_read_b64_tr_b16 a[2:3], v201 offset:1024\n\t" \
    "ds_read_b64_tr_b16 a[4:5], v201 offset:8192\n\t" \
    "ds_read_b64_tr_b16 a[6:7], v201 offset:9216\n\t" \
    "ds_read_b64_tr_b16 a[72:73], v202 offset:0\n\t" \
    "ds_read_b64_tr_b16 a[74:75], v202 offset:1024\n\t" \
    "ds_read_b64_tr_b16 a[76:77], v202 offset:8192\n\t" \
    "ds_read_b64_tr_b16 a[78:79], v202 offset:9216\n\t" \
    "s_waitcnt vmcnt(28) lgkmcnt(0)\n\t" \
    "s_barrier\n\t" \
    "ds_read_b64_tr_b16 a[8:9], v201 offset:16384\n\t" \
    "ds_read_b64_tr_b16 a[10:11], v201 offset:17408\n\t" \
    "ds_read_b64_tr_b16 a[12:13], v201 offset:24576\n\t" \
    "ds_read_b64_tr_b16 a[14:15], v201 offset:25600\n\t" \
    "ds_read_b64_tr_b16 a[80:81], v202 offset:16384\n\t" \
    "ds_read_b64_tr_b16 a[82:83], v202 offset:17408\n\t" \
    "ds_read_b64_tr_b16 a[84:85], v202 offset:24576\n\t" \
    "ds_read_b64_tr_b16 a[86:87], v202 offset:25600\n\t" \
    "s_waitcnt vmcnt(24) lgkmcnt(0)\n\t" \
    "s_barrier\n\t" \
    "ds_read_b64_tr_b16 a[16:17], v201 offset:32768\n\t" \
    "ds_read_b64_tr_b16 a[18:19], v201 offset:33792\n\t" \
    "ds_read_b64_tr_b16 a[20:21], v201 offset:40960\n\t" \
    "ds_read_b64_tr_b16 a[22:23], v201 offset:41984\n\t" \
    "ds_read_b64_tr_b16 a[88:89], v202 offset:32768\n\t" \
    "ds_read_b64_tr_b16 a[90:91], v202 offset:33792\n\t" \
    "ds_read_b64_tr_b16 a[92:93], v202 offset:40960\n\t" \
    "ds_read_b64_tr_b16 a[94:95], v202 offset:41984\n\t" \
    "s_waitcnt vmcnt(20) lgkmcnt(0)\n\t" \
    "s_barrier\n\t" \
    "ds_read_b64_tr_b16 a[24:25], v201 offset:49152\n\t" \
    "ds_read_b64_tr_b16 a[26:27], v201 offset:50176\n\t" \
    "ds_read_b64_tr_b16 a[28:29], v201 offset:57344\n\t" \
    "ds_read_b64_tr_b16 a[30:31], v201 offset:58368\n\t" \
    "ds_read_b64_tr_b16 a[96:97], v202 offset:49152\n\t" \
    "ds_read_b64_tr_b16 a[98:99], v202 offset:50176\n\t" \
    "ds_read_b64_tr_b16 a[100:101], v202 offset:57344\n\t" \
    "ds_read_b64_tr_b16 a[102:103], v202 offset:58368\n\t" \
    "s_add_u32 s46, s44, 0\n\t" \
    "s_addc_u32 s47, s45, 0\n\t" \
    "s_mul_i32 s69, s82, 9\n\t" \
    "s_add_u32 m0, s69, 0\n\t" \
    "s_nop 0\n\t" \
    "global_load_lds_dwordx4 v203, s[46:47]\n\t" \
    "s_add_u32 m0, s69, 1024\n\t" \
    "s_nop 0\n\t" \
    "global_load_lds_dwordx4 v204, s[46:47]\n\t" \
    "s_add_u32 m0, s69, 2048\n\t" \
    "s_nop 0\n\t" \
    "global_load_lds_dwordx4 v205, s[46:47]\n\t" \
    "s_add_u32 m0, s69, 3072\n\t" \
    "s_nop 0\n\t" \
    "global_load_lds_dwordx4 v206, s[46:47]\n\t" \
    "s_add_u32 m0, s69, 4096\n\t" \
    "s_nop 0\n\t" \
    "global_load_lds_dwordx4 v207, s[46:47]\n\t" \
    "s_add_u32 m0, s69, 5120\n\t" \
    "s_nop 0\n\t" \
    "global_load_lds_dwordx4 v208, s[46:47]\n\t" \
    "s_add_u32 m0, s69, 6144\n\t" \
    "s_nop 0\n\t" \
    "global_load_lds_dwordx4 v209, s[46:47]\n\t" \
    "s_add_u32 m0, s69, 7168\n\t" \
    "s_nop 0\n\t" \
    "global_load_lds_dwordx4 v210, s[46:47]\n\t" \
    "s_add_u32 m0, s69, 8192\n\t" \
    "s_nop 0\n\t" \
    "global_load_lds_dwordx4 v211, s[46:47]\n\t" \
    "s_waitcnt vmcnt(25) lgkmcnt(0)\n\t" \
    "s_barrier\n\t" \
    "v_add_u32 v201, 0x10000, v201\n\t" \
    "v_add_u32 v202, 0x10000, v202\n\t" \
    "ds_read_b64_tr_b16 a[32:33], v201 offset:0\n\t" \
    "ds_read_b64_tr_b16 a[34:35], v201 offset:1024\n\t" \
    "ds_read_b64_tr_b16 a[36:37], v201 offset:8192\n\t" \
    "ds_read_b64_tr_b16 a[38:39], v201 offset:9216\n\t" \
    "ds_read_b64_tr_b16 a[104:105], v202 offset:0\n\t" \
    "ds_read_b64_tr_b16 a[106:107], v202 offset:1024\n\t" \
    "ds_read_b64_tr_b16 a[108:109], v202 offset:8192\n\t" \
    "ds_read_b64_tr_b16 a[110:111], v202 offset:9216\n\t" \
    "s_waitcnt vmcnt(21) lgkmcnt(0)\n\t" \
    "s_barrier\n\t" \
    "ds_read_b64_tr_b16 a[40:41], v201 offset:16384\n\t" \
    "ds_read_b64_tr_b16 a[42:43], v201 offset:17408\n\t" \
    "ds_read_b64_tr_b16 a[44:45], v201 offset:24576\n\t" \
    "ds_read_b64_tr_b16 a[46:47], v201 offset:25600\n\t" \
    "ds_read_b64_tr_b16 a[112:113], v202 offset:16384\n\t" \
    "ds_read_b64_tr_b16 a[114:115], v202 offset:17408\n\t" \
    "ds_read_b64_tr_b16 a[116:117], v202 offset:24576\n\t" \
    "ds_read_b64_tr_b16 a[118:119], v202 offset:25600\n\t" \
    "s_add_u32 s46, s44, 36864\n\t" \
    "s_addc_u32 s47, s45, 0\n\t" \
    "s_mul_i32 s69, s82, 9\n\t" \
    "s_add_u32 m0, s69, 36864\n\t" \
    "s_nop 0\n\t" \
    "global_load_lds_dwordx4 v203, s[46:47]\n\t" \
    "s_add_u32 m0, s69, 37888\n\t" \
    "s_nop 0\n\t" \
    "global_load_lds_dwordx4 v204, s[46:47]\n\t" \
    "s_add_u32 m0, s69, 38912\n\t" \
    "s_nop 0\n\t" \
    "global_load_lds_dwordx4 v205, s[46:47]\n\t" \
    "s_add_u32 m0, s69, 39936\n\t" \
    "s_nop 0\n\t" \
    "global_load_lds_dwordx4 v206, s[46:47]\n\t" \
    "s_add_u32 m0, s69, 40960\n\t" \
    "s_nop 0\n\t" \
    "global_load_lds_dwordx4 v207, s[46:47]\n\t" \
    "s_add_u32 m0, s69, 41984\n\t" \
    "s_nop 0\n\t" \
    "global_load_lds_dwordx4 v208, s[46:47]\n\t" \
    "s_add_u32 m0, s69, 43008\n\t" \
    "s_nop 0\n\t" \
    "global_load_lds_dwordx4 v209, s[46:47]\n\t" \
    "s_add_u32 m0, s69, 44032\n\t" \
    "s_nop 0\n\t" \
    "global_load_lds_dwordx4 v210, s[46:47]\n\t" \
    "s_add_u32 m0, s69, 45056\n\t" \
    "s_nop 0\n\t" \
    "global_load_lds_dwordx4 v211, s[46:47]\n\t" \
    "s_waitcnt vmcnt(26) lgkmcnt(0)\n\t" \
    "s_barrier\n\t" \
    "ds_read_b64_tr_b16 a[48:49], v201 offset:32768\n\t" \
    "ds_read_b64_tr_b16 a[50:51], v201 offset:33792\n\t" \
    "ds_read_b64_tr_b16 a[52:53], v201 offset:40960\n\t" \
    "ds_read_b64_tr_b16 a[54:55], v201 offset:41984\n\t" \
    "ds_read_b64_tr_b16 a[120:121], v202 offset:32768\n\t" \
    "ds_read_b64_tr_b16 a[122:123], v202 offset:33792\n\t" \
    "ds_read_b64_tr_b16 a[124:125], v202 offset:40960\n\t" \
    "ds_read_b64_tr_b16 a[126:127], v202 offset:41984\n\t" \
    "s_waitcnt vmcnt(22) lgkmcnt(0)\n\t" \
    "s_barrier\n\t" \
    "ds_read_b64_tr_b16 a[56:57], v201 offset:49152\n\t" \
    "ds_read_b64_tr_b16 a[58:59], v201 offset:50176\n\t" \
    "ds_read_b64_tr_b16 a[60:61], v201 offset:57344\n\t" \
    "ds_read_b64_tr_b16 a[62:63], v201 offset:58368\n\t" \
    "ds_read_b64_tr_b16 a[128:129], v202 offset:49152\n\t" \
    "ds_read_b64_tr_b16 a[130:131], v202 offset:50176\n\t" \
    "ds_read_b64_tr_b16 a[132:133], v202 offset:57344\n\t" \
    "ds_read_b64_tr_b16 a[134:135], v202 offset:58368\n\t" \
    "s_add_u32 s46, s44, 73728\n\t" \
    "s_addc_u32 s47, s45, 0\n\t" \
    "s_mul_i32 s69, s82, 9\n\t" \
    "s_add_u32 m0, s69, 73728\n\t" \
    "s_nop 0\n\t" \
    "global_load_lds_dwordx4 v203, s[46:47]\n\t" \
    "s_add_u32 m0, s69, 74752\n\t" \
    "s_nop 0\n\t" \
    "global_load_lds_dwordx4 v204, s[46:47]\n\t" \
    "s_add_u32 m0, s69, 75776\n\t" \
    "s_nop 0\n\t" \
    "global_load_lds_dwordx4 v205, s[46:47]\n\t" \
    "s_add_u32 m0, s69, 76800\n\t" \
    "s_nop 0\n\t" \
    "global_load_lds_dwordx4 v206, s[46:47]\n\t" \
    "s_add_u32 m0, s69, 77824\n\t" \
    "s_nop 0\n\t" \
    "global_load_lds_dwordx4 v207, s[46:47]\n\t" \
    "s_add_u32 m0, s69, 78848\n\t" \
    "s_nop 0\n\t" \
    "global_load_lds_dwordx4 v208, s[46:47]\n\t" \
    "s_add_u32 m0, s69, 79872\n\t" \
    "s_nop 0\n\t" \
    "global_load_lds_dwordx4 v209, s[46:47]\n\t" \
    "s_add_u32 m0, s69, 80896\n\t" \
    "s_nop 0\n\t" \
    "global_load_lds_dwordx4 v210, s[46:47]\n\t" \
    "s_add_u32 m0, s69, 81920\n\t" \
    "s_nop 0\n\t" \
    "global_load_lds_dwordx4 v211, s[46:47]\n\t" \
    "v_mbcnt_lo_u32_b32 v140, -1, 0\n\t" \
    "v_mbcnt_hi_u32_b32 v140, -1, v140\n\t" \
    "v_lshlrev_b32 v140, 6, v140\n\t" \
    "v_add_u32 v140, 6225920, v140\n\t" \
    "v_mov_b32 v141, 6234112\n\t" \
    "global_load_dword v141, v141, s[36:37] sc1\n\t" \
    "global_load_dwordx2 v[142:143], v140, s[36:37] sc1\n\t" \
    "global_load_dword v144, v140, s[36:37] offset:2048 sc1\n\t" \
    "s_waitcnt vmcnt(30) lgkmcnt(0)\n\t" \
    "s_barrier\n\t" \
    "v_add_u32 v201, 0x10000, v201\n\t" \
    "v_add_u32 v202, 0x10000, v202\n\t" \
    "ds_read_b64_tr_b16 a[64:65], v201 offset:0\n\t" \
    "ds_read_b64_tr_b16 a[66:67], v201 offset:1024\n\t" \
    "ds_read_b64_tr_b16 a[68:69], v201 offset:8192\n\t" \
    "ds_read_b64_tr_b16 a[70:71], v201 offset:9216\n\t" \
    "ds_read_b64_tr_b16 a[136:137], v202 offset:0\n\t" \
    "ds_read_b64_tr_b16 a[138:139], v202 offset:1024\n\t" \
    "ds_read_b64_tr_b16 a[140:141], v202 offset:8192\n\t" \
    "ds_read_b64_tr_b16 a[142:143], v202 offset:9216\n\t" \
    "v_add_u32 v201, 0xfffe0000, v201\n\t" \
    "v_add_u32 v202, 0xfffe0000, v202\n\t" \
    "s_waitcnt lgkmcnt(0)\n\t" \
    "s_barrier\n\t" \
    "s_add_u32 s46, s44, 110592\n\t" \
    "s_addc_u32 s47, s45, 0\n\t" \
    "s_mul_i32 s69, s82, 9\n\t" \
    "s_add_u32 m0, s69, 110592\n\t" \
    "s_nop 0\n\t" \
    "global_load_lds_dwordx4 v203, s[46:47]\n\t" \
    "s_add_u32 m0, s69, 111616\n\t" \
    "s_nop 0\n\t" \
    "global_load_lds_dwordx4 v204, s[46:47]\n\t" \
    "s_add_u32 m0, s69, 112640\n\t" \
    "s_nop 0\n\t" \
    "global_load_lds_dwordx4 v205, s[46:47]\n\t" \
    "s_add_u32 m0, s69, 113664\n\t" \
    "s_nop 0\n\t" \
    "global_load_lds_dwordx4 v206, s[46:47]\n\t" \
    "s_add_u32 m0, s69, 114688\n\t" \
    "s_nop 0\n\t" \
    "global_load_lds_dwordx4 v207, s[46:47]\n\t" \
    "s_add_u32 m0, s69, 115712\n\t" \
    "s_nop 0\n\t" \
    "global_load_lds_dwordx4 v208, s[46:47]\n\t" \
    "s_add_u32 m0, s69, 116736\n\t" \
    "s_nop 0\n\t" \
    "global_load_lds_dwordx4 v209, s[46:47]\n\t" \
    "s_add_u32 m0, s69, 117760\n\t" \
    "s_nop 0\n\t" \
    "global_load_lds_dwordx4 v210, s[46:47]\n\t" \
    "s_add_u32 m0, s69, 118784\n\t" \
    "s_nop 0\n\t" \
    "global_load_lds_dwordx4 v211, s[46:47]\n\t" \
    "s_waitcnt vmcnt(21) lgkmcnt(0)\n\t" \
    "s_barrier\n\t" \
    "ds_read_b64_tr_b16 a[144:145], v212 offset:0\n\t" \
    "ds_read_b64_tr_b16 a[146:147], v212 offset:2304\n\t" \
    "ds_read_b64_tr_b16 a[148:149], v212 offset:18432\n\t" \
    "ds_read_b64_tr_b16 a[150:151], v212 offset:20736\n\t" \
    "s_waitcnt lgkmcnt(6)\n\t" \
    "ds_read_b64_tr_b16 a[176:177], v213 offset:0\n\t" \
    "ds_read_b64_tr_b16 a[178:179], v213 offset:2304\n\t" \
    "ds_read_b64_tr_b16 a[180:181], v213 offset:18432\n\t" \
    "ds_read_b64_tr_b16 a[182:183], v213 offset:20736\n\t" \
    "s_waitcnt lgkmcnt(6)\n\t" \
    "ds_read_b64_tr_b16 a[208:209], v212 offset:128\n\t" \
    "ds_read_b64_tr_b16 a[210:211], v212 offset:2432\n\t" \
    "ds_read_b64_tr_b16 a[212:213], v212 offset:18560\n\t" \
    "ds_read_b64_tr_b16 a[214:215], v212 offset:20864\n\t" \
    "s_waitcnt lgkmcnt(6)\n\t" \
    "ds_read_b64_tr_b16 a[240:241], v213 offset:128\n\t" \
    "ds_read_b64_tr_b16 a[242:243], v213 offset:2432\n\t" \
    "ds_read_b64_tr_b16 a[244:245], v213 offset:18560\n\t" \
    "ds_read_b64_tr_b16 a[246:247], v213 offset:20864\n\t" \
    "s_waitcnt lgkmcnt(6)\n\t" \
    "ds_read_b64_tr_b16 a[152:153], v214 offset:0\n\t" \
    "ds_read_b64_tr_b16 a[154:155], v214 offset:2304\n\t" \
    "ds_read_b64_tr_b16 a[156:157], v214 offset:18432\n\t" \
    "ds_read_b64_tr_b16 a[158:159], v214 offset:20736\n\t" \
    "s_waitcnt lgkmcnt(6)\n\t" \
    "ds_read_b64_tr_b16 a[184:185], v215 offset:0\n\t" \
    "ds_read_b64_tr_b16 a[186:187], v215 offset:2304\n\t" \
    "ds_read_b64_tr_b16 a[188:189], v215 offset:18432\n\t" \
    "ds_read_b64_tr_b16 a[190:191], v215 offset:20736\n\t" \
    "s_waitcnt lgkmcnt(6)\n\t" \
    "ds_read_b64_tr_b16 a[216:217], v214 offset:128\n\t" \
    "ds_read_b64_tr_b16 a[218:219], v214 offset:2432\n\t" \
    "ds_read_b64_tr_b16 a[220:221], v214 offset:18560\n\t" \
    "ds_read_b64_tr_b16 a[222:223], v214 offset:20864\n\t" \
    "s_waitcnt lgkmcnt(6)\n\t" \
    "ds_read_b64_tr_b16 a[248:249], v215 offset:128\n\t" \
    "ds_read_b64_tr_b16 a[250:251], v215 offset:2432\n\t" \
    "ds_read_b64_tr_b16 a[252:253], v215 offset:18560\n\t" \
    "ds_read_b64_tr_b16 a[254:255], v215 offset:20864\n\t" \
    "s_waitcnt lgkmcnt(6)\n\t" \
    "ds_read_b64_tr_b16 v[240:241], v24 offset:0\n\t" \
    "ds_read_b64_tr_b16 v[242:243], v24 offset:2304\n\t" \
    "ds_read_b64_tr_b16 v[244:245], v24 offset:18432\n\t" \
    "ds_read_b64_tr_b16 v[246:247], v24 offset:20736\n\t" \
    "v_add_u32 v212, 0x12000, v212\n\t" \
    "v_add_u32 v213, 0x12000, v213\n\t" \
    "v_add_u32 v214, 0x12000, v214\n\t" \
    "v_add_u32 v215, 0x12000, v215\n\t" \
    "v_add_u32 v24, 0x12000, v24\n\t" \
    "s_waitcnt vmcnt(9)\n\t" \
    "vv_C2_%=:\n\t" \
    "v_cmp_ne_u32 s[54:55], v142, v144\n\t" \
    "v_sub_u32 v142, v142, v141\n\t" \
    "v_cmp_ne_u32 vcc, 1, v142\n\t" \
    "s_nop 1\n\t" \
    "s_or_b64 vcc, vcc, s[54:55]\n\t" \
    "s_cbranch_scc0 vv_Cok3_%=\n\t" \
    "s_cmp_eq_u32 s68, 0\n\t" \
    "s_cbranch_scc1 vv_F5_%=\n\t" \
    "s_sub_u32 s68, s68, 1\n\t" \
    "s_sleep 6\n\t" \
    "s_branch vv_Cag4_%=\n\t" \
    "vv_F5_%=:\n\t" \
    "v_mov_b32 v144, 5767424\n\t" \
    "v_mov_b32 v145, 4\n\t" \
    "global_store_dword v144, v145, s[36:37] sc1\n\t" \
    "s_waitcnt vmcnt(0)\n\t" \
    "s_branch vv_Cok3_%=\n\t" \
    "vv_Cag4_%=:\n\t" \
    "global_load_dwordx2 v[142:143], v140, s[36:37] sc1\n\t" \
    "global_load_dword v144, v140, s[36:37] offset:2048 sc1\n\t" \
    "s_waitcnt vmcnt(0)\n\t" \
    "s_branch vv_C2_%=\n\t" \
    "vv_Cok3_%=:\n\t" \
    "v_readlane_b32 s69, v143, 2\n\t" \
    "v_readlane_b32 s70, v143, 3\n\t" \
    "s_nop 1\n\t" \
    "s_cmp_lt_u32 s70, s69\n\t" \
    "s_cselect_b32 s69, 0, 8192\n\t" \
    "s_add_u32 s100, s36, s69\n\t" \
    "s_addc_u32 s101, s37, 0\n\t" \
    "s_cmp_lg_u32 s66, 0\n\t" \
    "s_cbranch_scc1 vv_Ca6_%=\n\t" \
    "v_mov_b32 v140, 5768192\n\t" \
    "s_mov_b64 exec, 1\n\t" \
    "global_atomic_add v140, v142, s[36:37] sc1\n\t" \
    "s_mov_b64 exec, -1\n\t" \
    "vv_Ca6_%=:\n\t" \
    "v_mov_b32_dpp v140, v143 quad_perm:[1,0,3,2] row_mask:0xf bank_mask:0xf\n\t" \
    "v_cmp_lt_u32 s[44:45], v140, v143\n\t" \
    "s_nop 1\n\t" \
    "s_bitcmp1_b32 s44, 0\n\t" \
    "s_cselect_b32 s62, 8192, 0\n\t" \
    "s_bitcmp1_b32 s44, 2\n\t" \
    "s_cselect_b32 s63, 8192, 0\n\t" \
    "s_or_b32 s63, s63, 0x200000\n\t" \
    "v_min_u32 v140, v140, v143\n\t" \
    "v_cmp_lt_u32 vcc, 660, v140\n\t" \
    "s_nop 1\n\t" \
    "s_bcnt1_i32_b64 s69, vcc\n\t" \
    "s_cmp_gt_u32 s69, 16\n\t" \
    "s_cselect_b32 s69, 1, 0\n\t" \
    "s_mul_i32 s70, s69, 160\n\t" \
    "s_add_u32 s86, s86, s70\n\t" \
    "s_mul_i32 s70, s69, 536\n\t" \
    "s_add_u32 s87, s87, s70\n\t" \
    "s_mul_i32 s70, s69, 256\n\t" \
    "s_add_u32 s88, s88, s70\n\t" \
    "s_waitcnt vmcnt(0) lgkmcnt(0)\n\t" \
    "s_barrier\n\t" \
    "ds_read_b64_tr_b16 a[160:161], v212 offset:0\n\t" \
    "ds_read_b64_tr_b16 a[162:163], v212 offset:2304\n\t" \
    "ds_read_b64_tr_b16 a[164:165], v212 offset:18432\n\t" \
    "ds_read_b64_tr_b16 a[166:167], v212 offset:20736\n\t" \
    "s_waitcnt lgkmcnt(6)\n\t" \
    "ds_read_b64_tr_b16 a[192:193], v213 offset:0\n\t" \
    "ds_read_b64_tr_b16 a[194:195], v213 offset:2304\n\t" \
    "ds_read_b64_tr_b16 a[196:197], v213 offset:18432\n\t" \
    "ds_read_b64_tr_b16 a[198:199], v213 offset:20736\n\t" \
    "s_waitcnt lgkmcnt(6)\n\t" \
    "ds_read_b64_tr_b16 a[224:225], v212 offset:128\n\t" \
    "ds_read_b64_tr_b16 a[226:227], v212 offset:2432\n\t" \
    "ds_read_b64_tr_b16 a[228:229], v212 offset:18560\n\t" \
    "ds_read_b64_tr_b16 a[230:231], v212 offset:20864\n\t" \
    "s_waitcnt lgkmcnt(6)\n\t" \
    "ds_read_b64_tr_b16 v[224:225], v213 offset:128\n\t" \
    "ds_read_b64_tr_b16 v[226:227], v213 offset:2432\n\t" \
    "ds_read_b64_tr_b16 v[228:229], v213 offset:18560\n\t" \
    "ds_read_b64_tr_b16 v[230:231], v213 offset:20864\n\t" \
    "s_waitcnt lgkmcnt(6)\n\t" \
    "ds_read_b64_tr_b16 a[168:169], v214 offset:0\n\t" \
    "ds_read_b64_tr_b16 a[170:171], v214 offset:2304\n\t" \
    "ds_read_b64_tr_b16 a[172:173], v214 offset:18432\n\t" \
    "ds_read_b64_tr_b16 a[174:175], v214 offset:20736\n\t" \
    "s_waitcnt lgkmcnt(6)\n\t" \
    "ds_read_b64_tr_b16 a[200:201], v215 offset:0\n\t" \
    "ds_read_b64_tr_b16 a[202:203], v215 offset:2304\n\t" \
    "ds_read_b64_tr_b16 a[204:205], v215 offset:18432\n\t" \
    "ds_read_b64_tr_b16 a[206:207], v215 offset:20736\n\t" \
    "s_waitcnt lgkmcnt(6)\n\t" \
    "ds_read_b64_tr_b16 a[232:233], v214 offset:128\n\t" \
    "ds_read_b64_tr_b16 a[234:235], v214 offset:2432\n\t" \
    "ds_read_b64_tr_b16 a[236:237], v214 offset:18560\n\t" \
    "ds_read_b64_tr_b16 a[238:239], v214 offset:20864\n\t" \
    "s_waitcnt lgkmcnt(6)\n\t" \
    "ds_read_b64_tr_b16 v[232:233], v215 offset:128\n\t" \
    "ds_read_b64_tr_b16 v[234:235], v215 offset:2432\n\t" \
    "ds_read_b64_tr_b16 v[236:237], v215 offset:18560\n\t" \
    "ds_read_b64_tr_b16 v[238:239], v215 offset:20864\n\t" \
    "s_waitcnt lgkmcnt(6)\n\t" \
    "ds_read_b64_tr_b16 v[248:249], v24 offset:0\n\t" \
    "ds_read_b64_tr_b16 v[250:251], v24 offset:2304\n\t" \
    "ds_read_b64_tr_b16 v[252:253], v24 offset:18432\n\t" \
    "ds_read_b64_tr_b16 v[254:255], v24 offset:20736\n\t" \
    "v_add_u32 v212, 0x12000, v212\n\t" \
    "v_add_u32 v213, 0x12000, v213\n\t" \
    "v_add_u32 v214, 0x12000, v214\n\t" \
    "v_add_u32 v215, 0x12000, v215\n\t" \
    "v_add_u32 v24, 0x12000, v24\n\t" \
    "s_waitcnt lgkmcnt(0)\n\t" \
    "s_barrier\n\t" \
    "s_mov_b32 s79, s74\n\t" \
    "s_sub_u32 s80, s74, 32000\n\t" \
    "v_mov_b32 v72, 0\n\t" \
    "v_mov_b32 v73, 0\n\t" \
    "v_mov_b32 v74, 0\n\t" \
    "v_mov_b32 v75, 0\n\t" \
    "v_mov_b32 v76, 0\n\t" \
    "v_mov_b32 v77, 0\n\t" \
    "v_mov_b32 v78, 0\n\t" \
    "v_mov_b32 v79, 0\n\t" \
    "v_mov_b32 v108, 0\n\t" \
    "v_mov_b32 v109, 0\n\t" \
    "v_mov_b32 v110, 0\n\t" \
    "v_mov_b32 v111, 0\n\t" \
    "v_mov_b32 v112, 0\n\t" \
    "v_mov_b32 v113, 0\n\t" \
    "v_mov_b32 v114, 0\n\t" \
    "v_mov_b32 v115, 0\n\t" \
    "v_mov_b32 v116, 0\n\t" \
    "v_mov_b32 v117, 0\n\t" \
    "v_mov_b32 v118, 0\n\t" \
    "v_mov_b32 v119, 0\n\t" \
    "v_mov_b32 v120, 0\n\t" \
    "v_mov_b32 v121, 0\n\t" \
    "v_mov_b32 v122, 0\n\t" \
    "v_mov_b32 v123, 0\n\t" \
    "v_mov_b32 v124, 0\n\t" \
    "v_mov_b32 v125, 0\n\t" \
    "v_mov_b32 v126, 0\n\t" \
    "v_mov_b32 v127, 0\n\t" \
    "s_add_u32 s69, s65, 0\n\t" \
    "s_and_b32 s69, s69, 7\n\t" \
    "s_lshl_b32 s69, s69, 18\n\t" \
    "s_add_u32 s69, s69, s62\n\t" \
    "s_add_u32 s46, s36, s69\n\t" \
    "s_addc_u32 s47, s37, 0\n\t" \
    "s_add_u32 s70, s65, 0\n\t" \
    "s_mul_i32 s69, s70, 171\n\t" \
    "s_lshr_b32 s69, s69, 9\n\t" \
    "s_mul_i32 s70, s70, 589824\n\t" \
    "s_mul_i32 s71, s69, 327680\n\t" \
    "s_add_u32 s70, s70, s71\n\t" \
    "s_lshl1_add_u32 s69, s69, 4\n\t" \
    "s_bitcmp1_b64 s[44:45], s69\n\t" \
    "s_cselect_b32 s69, 8192, 0\n\t" \
    "s_add_u32 s69, s69, s70\n\t" \
    "s_add_u32 s69, s69, 123731968\n\t" \
    "s_add_u32 s38, s36, s69\n\t" \
    "s_addc_u32 s39, s37, 0\n\t" \
    "s_and_b32 s69, s69, 0xffe00000\n\t" \
    "s_add_u32 s69, s69, 1823744\n\t" \
    "v_mov_b32 v17, s69\n\t" \
    "global_load_dword v17, v17, s[36:37]\n\t" \
    "s_and_b32 s69, s65, 7\n\t" \
    "s_lshl_b32 s69, s69, 18\n\t" \
    "s_add_u32 s69, s69, s63\n\t" \
    "s_add_u32 s52, s36, s69\n\t" \
    "s_addc_u32 s53, s37, 0\n\t" \
    "v_mov_b32 v100, 0\n\t" \
    "v_mov_b32 v101, 0\n\t" \
    "v_mov_b32 v102, 0\n\t" \
    "v_mov_b32 v103, 0\n\t" \
    "s_sub_u32 s70, s79, s74\n\t" \
    "s_max_i32 s70, s70, 0\n\t" \
    "s_min_i32 s70, s70, 16000\n\t" \
    "s_lshl_b32 s70, s70, 1\n\t" \
    "s_mul_hi_u32 s70, s70, s77\n\t" \
    "s_add_u32 s70, s70, s73\n\t" \
    "s_cmp_eq_u32 s66, 1\n\t" \
    "s_cbranch_scc1 vv_loop10_%=\n\t" \
    "s_cmp_eq_u32 s66, 2\n\t" \
    "s_cbranch_scc1 vv_loop11_%=\n\t" \
    "s_cmp_eq_u32 s66, 3\n\t" \
    "s_cbranch_scc1 vv_loop12_%=\n\t" \
    ".p2align 6\n\t" \
    "s_nop 0\n\t" \
    "s_nop 0\n\t" \
    "s_nop 0\n\t" \
    "s_nop 0\n\t" \
    "s_nop 0\n\t" \
    "s_nop 0\n\t" \
    "vv_loop9_%=:\n\t" \
    "s_getpc_b64 s[30:31]\n\t" \
    "vv_Wp15_%=:\n\t" \
    "s_add_u32 s30, s30, vv_We16_%=-vv_Wp15_%=\n\t" \
    "s_addc_u32 s31, s31, 0\n\t" \
    "s_sub_u32 s70, s70, 96\n\t" \
    "vv_W14_%=:\n\t" \
    "s_memtime s[56:57]\n\t" \
    "s_waitcnt lgkmcnt(0)\n\t" \
    "s_sub_u32 s69, s70, s56\n\t" \
    "s_cmp_gt_i32 s69, 80\n\t" \
    "s_cbranch_scc1 vv_W14_%=\n\t" \
    "s_max_i32 s69, s69, 0\n\t" \
    "s_and_b32 s69, s69, -4\n\t" \
    "s_sub_u32 s30, s30, s69\n\t" \
    "s_subb_u32 s31, s31, 0\n\t" \
    "s_setpc_b64 s[30:31]\n\t" \
    "s_nop 0\n\t" \
    "s_nop 0\n\t" \
    "s_nop 0\n\t" \
    "s_nop 0\n\t" \
    "s_nop 0\n\t" \
    "s_nop 0\n\t" \
    "s_nop 0\n\t" \
    "s_nop 0\n\t" \
    "s_nop 0\n\t" \
    "s_nop 0\n\t" \
    "s_nop 0\n\t" \
    "s_nop 0\n\t" \
    "s_nop 0\n\t" \
    "s_nop 0\n\t" \
    "s_nop 0\n\t" \
    "s_nop 0\n\t" \
    "s_nop 0\n\t" \
    "s_nop 0\n\t" \
    "s_nop 0\n\t" \
    "s_nop 0\n\t" \
    "vv_We16_%=:\n\t" \
    "vv_G17_%=:\n\t" \
    "global_load_dwordx4 v[36:39], v1, s[46:47] sc1\n\t" \
    "global_load_dword v141, v2, s[46:47] sc1\n\t" \
    "global_load_dwordx4 v[40:43], v23, s[46:47] sc1\n\t" \
    "s_waitcnt vmcnt(2)\n\t" \
    "ds_write_b128 v3, v[36:39]\n\t" \
    "v_smfmac_f32_16x16x64_bf16 v[72:75], v[36:39], a[0:7], v10\n\t" \
    "s_waitcnt vmcnt(1)\n\t" \
    "ds_write_b32 v22, v141 offset:19456\n\t" \
    "s_waitcnt vmcnt(0)\n\t" \
    "ds_write_b128 v3, v[40:43] offset:1024\n\t" \
    "v_max3_u32 v140, v36, v40, v141\n\t" \
    "v_cmp_eq_u32 vcc, -1, v140\n\t" \
    "s_cbranch_vccnz vv_Gbad18_%=\n\t" \
    "vv_Gc19_%=:\n\t" \
    "s_waitcnt lgkmcnt(0)\n\t" \
    "s_barrier\n\t" \
    "ds_read_b128 v[48:51], v4\n\t" \
    "ds_read_b128 v[52:55], v5\n\t" \
    "v_smfmac_f32_16x16x64_bf16 v[76:79], v[36:39], a[72:79], v10\n\t" \
    "ds_read_b128 v[56:59], v6\n\t" \
    "ds_read_b128 v[60:63], v7\n\t" \
    "v_smfmac_f32_16x16x64_bf16 v[72:75], v[40:43], a[8:15], v10\n\t" \
    "ds_read_b128 v[64:67], v8\n\t" \
    "v_smfmac_f32_16x16x64_bf16 v[76:79], v[40:43], a[80:87], v10\n\t" \
    "ds_read_b128 v[68:71], v9\n\t" \
    "s_waitcnt lgkmcnt(5)\n\t" \
    "v_smfmac_f32_16x16x64_bf16 v[72:75], v[48:51], a[16:23], v10\n\t" \
    "ds_read_b128 v[44:47], v0 offset:19456\n\t" \
    "s_waitcnt lgkmcnt(5)\n\t" \
    "v_smfmac_f32_16x16x64_bf16 v[72:75], v[52:55], a[24:31], v10\n\t" \
    "s_waitcnt lgkmcnt(4)\n\t" \
    "v_smfmac_f32_16x16x64_bf16 v[72:75], v[56:59], a[32:39], v10\n\t" \
    "s_waitcnt lgkmcnt(3)\n\t" \
    "v_smfmac_f32_16x16x64_bf16 v[72:75], v[60:63], a[40:47], v10\n\t" \
    "s_waitcnt lgkmcnt(2)\n\t" \
    "v_smfmac_f32_16x16x64_bf16 v[72:75], v[64:67], a[48:55], v10\n\t" \
    "s_waitcnt lgkmcnt(1)\n\t" \
    "v_smfmac_f32_16x16x64_bf16 v[72:75], v[68:71], a[56:63], v10\n\t" \
    "s_waitcnt lgkmcnt(0)\n\t" \
    "v_smfmac_f32_16x16x64_bf16 v[72:75], v[44:47], a[64:71], v10\n\t" \
    "s_nop 2\n\t" \
    "v_smfmac_f32_16x16x64_bf16 v[76:79], v[48:51], a[88:95], v10\n\t" \
    "s_nop 2\n\t" \
    "v_smfmac_f32_16x16x64_bf16 v[76:79], v[52:55], a[96:103], v10\n\t" \
    "v_add_f32 v72, v72, v74\n\t" \
    "v_smfmac_f32_16x16x64_bf16 v[76:79], v[56:59], a[104:111], v10\n\t" \
    "v_add_f32 v73, v73, v75\n\t" \
    "v_smfmac_f32_16x16x64_bf16 v[76:79], v[60:63], a[112:119], v10\n\t" \
    "v_cvt_pk_bf16_f32 v80, v72, v73\n\t" \
    "v_smfmac_f32_16x16x64_bf16 v[76:79], v[64:67], a[120:127], v10\n\t" \
    "ds_write_b32 v12, v80 offset:0\n\t" \
    "v_smfmac_f32_16x16x64_bf16 v[76:79], v[68:71], a[128:135], v10\n\t" \
    "v_smfmac_f32_16x16x64_bf16 v[76:79], v[44:47], a[136:143], v10\n\t" \
    "s_nop 6\n\t" \
    "v_cmp_u_f32 vcc, v72, v73\n\t" \
    "v_pk_add_f32 v[76:77], v[76:77], v[78:79]\n\t" \
    "v_cvt_pk_bf16_f32 v81, v76, v77\n\t" \
    "ds_write_b32 v12, v81 offset:256\n\t" \
    "s_cbranch_vccnz vv_Gslow20_%=\n\t" \
    "vv_Gback21_%=:\n\t" \
    "s_waitcnt lgkmcnt(0)\n\t" \
    "s_barrier\n\t" \
    "ds_read_b64_tr_b16 v[84:85], v13 offset:0\n\t" \
    "v_smfmac_f32_16x16x64_bf16 v[100:103], v[36:39], a[0:7], v10\n\t" \
    "ds_read_b64_tr_b16 v[86:87], v13 offset:64\n\t" \
    "ds_read_b64_tr_b16 v[92:93], v13 offset:1152\n\t" \
    "v_smfmac_f32_16x16x64_bf16 v[100:103], v[36:39], a[0:7], v10\n\t" \
    "ds_read_b64_tr_b16 v[94:95], v13 offset:1216\n\t" \
    "ds_read_b64_tr_b16 v[88:89], v25 offset:0\n\t" \
    "v_smfmac_f32_16x16x64_bf16 v[100:103], v[36:39], a[0:7], v10\n\t" \
    "ds_read_b64_tr_b16 v[90:91], v25 offset:64\n\t" \
    "s_waitcnt lgkmcnt(4)\n\t" \
    "v_smfmac_f32_16x16x64_bf16 v[124:127], v[84:87], v[240:247], v19\n\t" \
    "ds_read_b64_tr_b16 v[96:97], v25 offset:1152\n\t" \
    "ds_read_b64_tr_b16 v[98:99], v25 offset:1216\n\t" \
    "s_waitcnt lgkmcnt(4)\n\t" \
    "v_smfmac_f32_16x16x64_bf16 v[124:127], v[92:95], v[248:255], v19\n\t" \
    "s_nop 2\n\t" \
    "v_smfmac_f32_16x16x64_bf16 v[108:111], v[84:87], a[144:151], v19\n\t" \
    "s_nop 2\n\t" \
    "v_smfmac_f32_16x16x64_bf16 v[108:111], v[92:95], a[160:167], v19\n\t" \
    "s_nop 1\n\t" \
    "v_permlane32_swap_b32 v124, v126\n\t" \
    "v_smfmac_f32_16x16x64_bf16 v[112:115], v[84:87], a[176:183], v19\n\t" \
    "s_nop 1\n\t" \
    "v_permlane32_swap_b32 v125, v127\n\t" \
    "v_smfmac_f32_16x16x64_bf16 v[112:115], v[92:95], a[192:199], v19\n\t" \
    "s_nop 0\n\t" \
    "v_add_f32 v124, v124, v126\n\t" \
    "s_waitcnt lgkmcnt(2)\n\t" \
    "v_smfmac_f32_16x16x64_bf16 v[108:111], v[88:91], a[152:159], v19\n\t" \
    "s_nop 1\n\t" \
    "v_add_f32 v125, v125, v127\n\t" \
    "v_smfmac_f32_16x16x64_bf16 v[112:115], v[88:91], a[184:191], v19\n\t" \
    "s_nop 0\n\t" \
    "v_cvt_pk_bf16_f32 v136, v124, v125\n\t" \
    "s_waitcnt lgkmcnt(0)\n\t" \
    "v_smfmac_f32_16x16x64_bf16 v[108:111], v[96:99], a[168:175], v19\n\t" \
    "s_nop 1\n\t" \
    "v_cvt_pk_bf16_f32 v137, v124, v125\n\t" \
    "v_smfmac_f32_16x16x64_bf16 v[112:115], v[96:99], a[200:207], v19\n\t" \
    "s_nop 1\n\t" \
    "v_permlane32_swap_b32 v136, v137\n\t" \
    "v_smfmac_f32_16x16x64_bf16 v[116:119], v[84:87], a[208:215], v19\n\t" \
    "s_mov_b32 exec_hi, 0\n\t" \
    "global_store_dwordx2 v16, v[136:137], s[38:39] sc1\n\t" \
    "s_mov_b32 exec_hi, -1\n\t" \
    "v_cvt_pk_bf16_f32 v128, v108, v109\n\t" \
    "v_smfmac_f32_16x16x64_bf16 v[116:119], v[88:91], a[216:223], v19\n\t" \
    "s_nop 1\n\t" \
    "v_cvt_pk_bf16_f32 v129, v110, v111\n\t" \
    "v_smfmac_f32_16x16x64_bf16 v[116:119], v[92:95], a[224:231], v19\n\t" \
    "s_nop 1\n\t" \
    "v_cvt_pk_bf16_f32 v130, v112, v113\n\t" \
    "v_smfmac_f32_16x16x64_bf16 v[116:119], v[96:99], a[232:239], v19\n\t" \
    "s_nop 1\n\t" \
    "v_cvt_pk_bf16_f32 v131, v114, v115\n\t" \
    "v_smfmac_f32_16x16x64_bf16 v[120:123], v[84:87], a[240:247], v19\n\t" \
    "s_nop 1\n\t" \
    "v_permlane16_swap_b32 v128, v130\n\t" \
    "v_smfmac_f32_16x16x64_bf16 v[120:123], v[88:91], a[248:255], v19\n\t" \
    "s_nop 1\n\t" \
    "v_permlane16_swap_b32 v129, v131\n\t" \
    "v_smfmac_f32_16x16x64_bf16 v[120:123], v[92:95], v[224:231], v19\n\t" \
    "s_nop 0\n\t" \
    "global_store_dwordx4 v14, v[128:131], s[38:39] sc1\n\t" \
    "v_cvt_pk_bf16_f32 v132, v116, v117\n\t" \
    "v_smfmac_f32_16x16x64_bf16 v[120:123], v[96:99], v[232:239], v19\n\t" \
    "s_nop 1\n\t" \
    "v_cvt_pk_bf16_f32 v133, v118, v119\n\t" \
    "s_nop 4\n\t" \
    "v_cvt_pk_bf16_f32 v134, v120, v121\n\t" \
    "v_cvt_pk_bf16_f32 v135, v122, v123\n\t" \
    "s_nop 0\n\t" \
    "v_permlane16_swap_b32 v132, v134\n\t" \
    "v_permlane16_swap_b32 v133, v135\n\t" \
    "global_store_dwordx4 v15, v[132:135], s[38:39] sc1\n\t" \
    "s_cmp_eq_u32 s65, 63\n\t" \
    "s_cbranch_scc1 vv_end13_%=\n\t" \
    "s_cmp_lg_u32 s66, 0\n\t" \
    "s_cbranch_scc1 vv_SF22_%=\n\t" \
    "s_lshl_b32 s69, s67, 7\n\t" \
    "s_add_u32 s69, s69, 3182592\n\t" \
    "s_add_u32 s70, s65, -1\n\t" \
    "v_mov_b32 v142, s69\n\t" \
    "v_mov_b32 v143, s70\n\t" \
    "s_mov_b64 exec, 1\n\t" \
    "global_store_dword v142, v143, s[100:101] sc1\n\t" \
    "s_mov_b64 exec, -1\n\t" \
    "vv_SF22_%=:\n\t" \
    "s_add_u32 s65, s65, 1\n\t" \
    "v_mov_b32 v72, 0\n\t" \
    "v_mov_b32 v73, 0\n\t" \
    "v_mov_b32 v74, 0\n\t" \
    "v_mov_b32 v75, 0\n\t" \
    "v_mov_b32 v76, 0\n\t" \
    "v_mov_b32 v77, 0\n\t" \
    "v_mov_b32 v78, 0\n\t" \
    "v_mov_b32 v79, 0\n\t" \
    "v_mov_b32 v108, 0\n\t" \
    "v_mov_b32 v109, 0\n\t" \
    "v_mov_b32 v110, 0\n\t" \
    "v_mov_b32 v111, 0\n\t" \
    "v_mov_b32 v112, 0\n\t" \
    "v_mov_b32 v113, 0\n\t" \
    "v_mov_b32 v114, 0\n\t" \
    "v_mov_b32 v115, 0\n\t" \
    "v_mov_b32 v116, 0\n\t" \
    "v_mov_b32 v117, 0\n\t" \
    "v_mov_b32 v118, 0\n\t" \
    "v_mov_b32 v119, 0\n\t" \
    "v_mov_b32 v120, 0\n\t" \
    "v_mov_b32 v121, 0\n\t" \
    "v_mov_b32 v122, 0\n\t" \
    "v_mov_b32 v123, 0\n\t" \
    "v_mov_b32 v124, 0\n\t" \
    "v_mov_b32 v125, 0\n\t" \
    "v_mov_b32 v126, 0\n\t" \
    "v_mov_b32 v127, 0\n\t" \
    "s_add_u32 s69, s65, 0\n\t" \
    "s_and_b32 s69, s69, 7\n\t" \
    "s_lshl_b32 s69, s69, 18\n\t" \
    "s_add_u32 s69, s69, s62\n\t" \
    "s_add_u32 s46, s36, s69\n\t" \
    "s_addc_u32 s47, s37, 0\n\t" \
    "s_add_u32 s70, s65, 0\n\t" \
    "s_mul_i32 s69, s70, 171\n\t" \
    "s_lshr_b32 s69, s69, 9\n\t" \
    "s_mul_i32 s70, s70, 589824\n\t" \
    "s_mul_i32 s71, s69, 327680\n\t" \
    "s_add_u32 s70, s70, s71\n\t" \
    "s_lshl1_add_u32 s69, s69, 4\n\t" \
    "s_bitcmp1_b64 s[44:45], s69\n\t" \
    "s_cselect_b32 s69, 8192, 0\n\t" \
    "s_add_u32 s69, s69, s70\n\t" \
    "s_add_u32 s69, s69, 123731968\n\t" \
    "s_add_u32 s38, s36, s69\n\t" \
    "s_addc_u32 s39, s37, 0\n\t" \
    "s_and_b32 s69, s69, 0xffe00000\n\t" \
    "s_add_u32 s69, s69, 1823744\n\t" \
    "v_mov_b32 v17, s69\n\t" \
    "global_load_dword v17, v17, s[36:37]\n\t" \
    "s_barrier\n\t" \
    "ds_read_b32 v160, v11 offset:20480\n\t" \
    "s_waitcnt lgkmcnt(0)\n\t" \
    "v_readfirstlane_b32 s70, v160\n\t" \
    "s_branch vv_loop9_%=\n\t" \
    "vv_Gbad18_%=:\n\t" \
    "s_waitcnt lgkmcnt(0)\n\t" \
    "s_nop 7\n\t" \
    "v_mov_b32 v72, 0\n\t" \
    "v_mov_b32 v73, 0\n\t" \
    "v_mov_b32 v74, 0\n\t" \
    "v_mov_b32 v75, 0\n\t" \
    "v_mov_b32 v76, 0\n\t" \
    "v_mov_b32 v77, 0\n\t" \
    "v_mov_b32 v78, 0\n\t" \
    "v_mov_b32 v79, 0\n\t" \
    "s_cmp_eq_u32 s68, 0\n\t" \
    "s_cbranch_scc1 vv_F23_%=\n\t" \
    "s_sub_u32 s68, s68, 1\n\t" \
    "s_branch vv_G17_%=\n\t" \
    "vv_F23_%=:\n\t" \
    "v_mov_b32 v140, 5767424\n\t" \
    "v_mov_b32 v141, 1\n\t" \
    "global_store_dword v140, v141, s[36:37] sc1\n\t" \
    "s_waitcnt vmcnt(0)\n\t" \
    "s_branch vv_Gc19_%=\n\t" \
    "vv_Gslow20_%=:\n\t" \
    "v_max_u32 v140, v36, v37\n\t" \
    "v_max3_u32 v140, v140, v38, v39\n\t" \
    "v_max3_u32 v140, v140, v40, v41\n\t" \
    "v_max3_u32 v140, v140, v42, v43\n\t" \
    "v_max3_u32 v140, v140, v44, v45\n\t" \
    "v_max3_u32 v140, v140, v46, v47\n\t" \
    "v_max3_u32 v140, v140, v48, v49\n\t" \
    "v_max3_u32 v140, v140, v50, v51\n\t" \
    "v_max3_u32 v140, v140, v52, v53\n\t" \
    "v_max3_u32 v140, v140, v54, v55\n\t" \
    "v_max3_u32 v140, v140, v56, v57\n\t" \
    "v_max3_u32 v140, v140, v58, v59\n\t" \
    "v_max3_u32 v140, v140, v60, v61\n\t" \
    "v_max3_u32 v140, v140, v62, v63\n\t" \
    "v_max3_u32 v140, v140, v64, v65\n\t" \
    "v_max3_u32 v140, v140, v66, v67\n\t" \
    "v_max3_u32 v140, v140, v68, v69\n\t" \
    "v_max3_u32 v140, v140, v70, v71\n\t" \
    "v_cmp_eq_u32 vcc, -1, v140\n\t" \
    "s_cbranch_vccz vv_Gback21_%=\n\t" \
    "s_waitcnt lgkmcnt(0)\n\t" \
    "v_sub_u32 v140, v1, v3\n\t" \
    "s_nop 0\n\t" \
    "v_readfirstlane_b32 s69, v140\n\t" \
    "v_add_u32 v143, 20480, v0\n\t" \
    "s_nop 7\n\t" \
    "s_add_u32 s30, s46, s69\n\t" \
    "s_addc_u32 s31, s47, 0\n\t" \
    "vv_Gag24_%=:\n\t" \
    "global_load_dwordx4 v[36:39], v1, s[46:47] sc1\n\t" \
    "global_load_dwordx4 v[40:43], v23, s[46:47] sc1\n\t" \
    "global_load_dwordx4 v[44:47], v143, s[30:31] sc1\n\t" \
    "global_load_dwordx4 v[48:51], v4, s[30:31] sc1\n\t" \
    "global_load_dwordx4 v[52:55], v5, s[30:31] sc1\n\t" \
    "global_load_dwordx4 v[56:59], v6, s[30:31] sc1\n\t" \
    "global_load_dwordx4 v[60:63], v7, s[30:31] sc1\n\t" \
    "global_load_dwordx4 v[64:67], v8, s[30:31] sc1\n\t" \
    "global_load_dwordx4 v[68:71], v9, s[30:31] sc1\n\t" \
    "s_waitcnt vmcnt(0)\n\t" \
    "v_max_u32 v140, v36, v37\n\t" \
    "v_max3_u32 v140, v140, v38, v39\n\t" \
    "v_max3_u32 v140, v140, v40, v41\n\t" \
    "v_max3_u32 v140, v140, v42, v43\n\t" \
    "v_max3_u32 v140, v140, v44, v45\n\t" \
    "v_max3_u32 v140, v140, v46, v47\n\t" \
    "v_max3_u32 v140, v140, v48, v49\n\t" \
    "v_max3_u32 v140, v140, v50, v51\n\t" \
    "v_max3_u32 v140, v140, v52, v53\n\t" \
    "v_max3_u32 v140, v140, v54, v55\n\t" \
    "v_max3_u32 v140, v140, v56, v57\n\t" \
    "v_max3_u32 v140, v140, v58, v59\n\t" \
    "v_max3_u32 v140, v140, v60, v61\n\t" \
    "v_max3_u32 v140, v140, v62, v63\n\t" \
    "v_max3_u32 v140, v140, v64, v65\n\t" \
    "v_max3_u32 v140, v140, v66, v67\n\t" \
    "v_max3_u32 v140, v140, v68, v69\n\t" \
    "v_max3_u32 v140, v140, v70, v71\n\t" \
    "v_cmp_eq_u32 vcc, -1, v140\n\t" \
    "s_cbranch_vccz vv_Gredo25_%=\n\t" \
    "s_cmp_eq_u32 s68, 0\n\t" \
    "s_cbranch_scc1 vv_F26_%=\n\t" \
    "s_sub_u32 s68, s68, 1\n\t" \
    "s_sleep 1\n\t" \
    "s_branch vv_Gag24_%=\n\t" \
    "vv_F26_%=:\n\t" \
    "v_mov_b32 v140, 5767424\n\t" \
    "v_mov_b32 v141, 2\n\t" \
    "global_store_dword v140, v141, s[36:37] sc1\n\t" \
    "s_waitcnt vmcnt(0)\n\t" \
    "s_branch vv_Gredo25_%=\n\t" \
    "vv_Gredo25_%=:\n\t" \
    "v_mov_b32 v72, 0\n\t" \
    "v_mov_b32 v73, 0\n\t" \
    "v_mov_b32 v74, 0\n\t" \
    "v_mov_b32 v75, 0\n\t" \
    "v_mov_b32 v76, 0\n\t" \
    "v_mov_b32 v77, 0\n\t" \
    "v_mov_b32 v78, 0\n\t" \
    "v_mov_b32 v79, 0\n\t" \
    "v_smfmac_f32_16x16x64_bf16 v[72:75], v[36:39], a[0:7], v10\n\t" \
    "v_smfmac_f32_16x16x64_bf16 v[72:75], v[40:43], a[8:15], v10\n\t" \
    "v_smfmac_f32_16x16x64_bf16 v[72:75], v[48:51], a[16:23], v10\n\t" \
    "v_smfmac_f32_16x16x64_bf16 v[72:75], v[52:55], a[24:31], v10\n\t" \
    "v_smfmac_f32_16x16x64_bf16 v[72:75], v[56:59], a[32:39], v10\n\t" \
    "v_smfmac_f32_16x16x64_bf16 v[72:75], v[60:63], a[40:47], v10\n\t" \
    "v_smfmac_f32_16x16x64_bf16 v[72:75], v[64:67], a[48:55], v10\n\t" \
    "v_smfmac_f32_16x16x64_bf16 v[72:75], v[68:71], a[56:63], v10\n\t" \
    "v_smfmac_f32_16x16x64_bf16 v[72:75], v[44:47], a[64:71], v10\n\t" \
    "v_smfmac_f32_16x16x64_bf16 v[76:79], v[36:39], a[72:79], v10\n\t" \
    "v_smfmac_f32_16x16x64_bf16 v[76:79], v[40:43], a[80:87], v10\n\t" \
    "v_smfmac_f32_16x16x64_bf16 v[76:79], v[48:51], a[88:95], v10\n\t" \
    "v_smfmac_f32_16x16x64_bf16 v[76:79], v[52:55], a[96:103], v10\n\t" \
    "v_smfmac_f32_16x16x64_bf16 v[76:79], v[56:59], a[104:111], v10\n\t" \
    "v_smfmac_f32_16x16x64_bf16 v[76:79], v[60:63], a[112:119], v10\n\t" \
    "v_smfmac_f32_16x16x64_bf16 v[76:79], v[64:67], a[120:127], v10\n\t" \
    "v_smfmac_f32_16x16x64_bf16 v[76:79], v[68:71], a[128:135], v10\n\t" \
    "v_smfmac_f32_16x16x64_bf16 v[76:79], v[44:47], a[136:143], v10\n\t" \
    "v_add_f32 v72, v72, v74\n\t" \
    "v_add_f32 v73, v73, v75\n\t" \
    "v_cvt_pk_bf16_f32 v80, v72, v73\n\t" \
    "ds_write_b32 v12, v80 offset:0\n\t" \
    "s_nop 3\n\t" \
    "v_pk_add_f32 v[76:77], v[76:77], v[78:79]\n\t" \
    "v_cvt_pk_bf16_f32 v81, v76, v77\n\t" \
    "ds_write_b32 v12, v81 offset:256\n\t" \
    "s_branch vv_Gback21_%=\n\t" \
    "vv_end13_%=:\n\t" \
    "s_endpgm\n\t" \
    ".p2align 6\n\t" \
    "s_nop 0\n\t" \
    "s_nop 0\n\t" \
    "s_nop 0\n\t" \
    "s_nop 0\n\t" \
    "s_nop 0\n\t" \
    "s_nop 0\n\t" \
    "vv_loop10_%=:\n\t" \
    "s_getpc_b64 s[30:31]\n\t" \
    "vv_Wp29_%=:\n\t" \
    "s_add_u32 s30, s30, vv_We30_%=-vv_Wp29_%=\n\t" \
    "s_addc_u32 s31, s31, 0\n\t" \
    "s_sub_u32 s70, s70, 96\n\t" \
    "vv_W28_%=:\n\t" \
    "s_memtime s[56:57]\n\t" \
    "s_waitcnt lgkmcnt(0)\n\t" \
    "s_sub_u32 s69, s70, s56\n\t" \
    "s_cmp_gt_i32 s69, 80\n\t" \
    "s_cbranch_scc1 vv_W28_%=\n\t" \
    "s_max_i32 s69, s69, 0\n\t" \
    "s_and_b32 s69, s69, -4\n\t" \
    "s_sub_u32 s30, s30, s69\n\t" \
    "s_subb_u32 s31, s31, 0\n\t" \
    "s_setpc_b64 s[30:31]\n\t" \
    "s_nop 0\n\t" \
    "s_nop 0\n\t" \
    "s_nop 0\n\t" \
    "s_nop 0\n\t" \
    "s_nop 0\n\t" \
    "s_nop 0\n\t" \
    "s_nop 0\n\t" \
    "s_nop 0\n\t" \
    "s_nop 0\n\t" \
    "s_nop 0\n\t" \
    "s_nop 0\n\t" \
    "s_nop 0\n\t" \
    "s_nop 0\n\t" \
    "s_nop 0\n\t" \
    "s_nop 0\n\t" \
    "s_nop 0\n\t" \
    "s_nop 0\n\t" \
    "s_nop 0\n\t" \
    "s_nop 0\n\t" \
    "s_nop 0\n\t" \
    "vv_We30_%=:\n\t" \
    "vv_G31_%=:\n\t" \
    "global_load_dwordx4 v[36:39], v1, s[46:47] sc1\n\t" \
    "global_load_dword v141, v2, s[46:47] sc1\n\t" \
    "global_load_dwordx4 v[40:43], v23, s[46:47] sc1\n\t" \
    "s_waitcnt vmcnt(2)\n\t" \
    "ds_write_b128 v3, v[36:39]\n\t" \
    "v_smfmac_f32_16x16x64_bf16 v[72:75], v[36:39], a[16:23], v10\n\t" \
    "s_waitcnt vmcnt(1)\n\t" \
    "ds_write_b32 v22, v141 offset:19712\n\t" \
    "s_waitcnt vmcnt(0)\n\t" \
    "ds_write_b128 v3, v[40:43] offset:1024\n\t" \
    "v_max3_u32 v140, v36, v40, v141\n\t" \
    "v_cmp_eq_u32 vcc, -1, v140\n\t" \
    "s_cbranch_vccnz vv_Gbad32_%=\n\t" \
    "vv_Gc33_%=:\n\t" \
    "s_waitcnt lgkmcnt(0)\n\t" \
    "s_barrier\n\t" \
    "ds_read_b128 v[48:51], v4\n\t" \
    "ds_read_b128 v[52:55], v5\n\t" \
    "v_smfmac_f32_16x16x64_bf16 v[76:79], v[36:39], a[88:95], v10\n\t" \
    "ds_read_b128 v[56:59], v6\n\t" \
    "ds_read_b128 v[60:63], v7\n\t" \
    "v_smfmac_f32_16x16x64_bf16 v[72:75], v[40:43], a[24:31], v10\n\t" \
    "ds_read_b128 v[64:67], v8\n\t" \
    "v_smfmac_f32_16x16x64_bf16 v[76:79], v[40:43], a[96:103], v10\n\t" \
    "ds_read_b128 v[68:71], v9\n\t" \
    "s_waitcnt lgkmcnt(5)\n\t" \
    "v_smfmac_f32_16x16x64_bf16 v[72:75], v[48:51], a[32:39], v10\n\t" \
    "ds_read_b128 v[44:47], v0 offset:19456\n\t" \
    "s_waitcnt lgkmcnt(5)\n\t" \
    "v_smfmac_f32_16x16x64_bf16 v[72:75], v[52:55], a[40:47], v10\n\t" \
    "s_waitcnt lgkmcnt(4)\n\t" \
    "v_smfmac_f32_16x16x64_bf16 v[72:75], v[56:59], a[48:55], v10\n\t" \
    "s_waitcnt lgkmcnt(3)\n\t" \
    "v_smfmac_f32_16x16x64_bf16 v[72:75], v[60:63], a[56:63], v10\n\t" \
    "s_waitcnt lgkmcnt(2)\n\t" \
    "v_smfmac_f32_16x16x64_bf16 v[72:75], v[64:67], a[0:7], v10\n\t" \
    "s_waitcnt lgkmcnt(1)\n\t" \
    "v_smfmac_f32_16x16x64_bf16 v[72:75], v[68:71], a[8:15], v10\n\t" \
    "s_waitcnt lgkmcnt(0)\n\t" \
    "v_smfmac_f32_16x16x64_bf16 v[72:75], v[44:47], a[64:71], v10\n\t" \
    "s_nop 2\n\t" \
    "v_smfmac_f32_16x16x64_bf16 v[76:79], v[48:51], a[104:111], v10\n\t" \
    "s_nop 2\n\t" \
    "v_smfmac_f32_16x16x64_bf16 v[76:79], v[52:55], a[112:119], v10\n\t" \
    "v_add_f32 v72, v72, v74\n\t" \
    "v_smfmac_f32_16x16x64_bf16 v[76:79], v[56:59], a[120:127], v10\n\t" \
    "v_add_f32 v73, v73, v75\n\t" \
    "v_smfmac_f32_16x16x64_bf16 v[76:79], v[60:63], a[128:135], v10\n\t" \
    "v_cvt_pk_bf16_f32 v80, v72, v73\n\t" \
    "v_smfmac_f32_16x16x64_bf16 v[76:79], v[64:67], a[72:79], v10\n\t" \
    "ds_write_b32 v12, v80 offset:0\n\t" \
    "v_smfmac_f32_16x16x64_bf16 v[76:79], v[68:71], a[80:87], v10\n\t" \
    "v_smfmac_f32_16x16x64_bf16 v[76:79], v[44:47], a[136:143], v10\n\t" \
    "s_nop 6\n\t" \
    "v_cmp_u_f32 vcc, v72, v73\n\t" \
    "v_pk_add_f32 v[76:77], v[76:77], v[78:79]\n\t" \
    "v_cvt_pk_bf16_f32 v81, v76, v77\n\t" \
    "ds_write_b32 v12, v81 offset:256\n\t" \
    "s_cbranch_vccnz vv_Gslow34_%=\n\t" \
    "vv_Gback35_%=:\n\t" \
    "s_waitcnt lgkmcnt(0)\n\t" \
    "s_barrier\n\t" \
    "ds_read_b64_tr_b16 v[84:85], v13 offset:0\n\t" \
    "v_smfmac_f32_16x16x64_bf16 v[100:103], v[36:39], a[0:7], v10\n\t" \
    "ds_read_b64_tr_b16 v[86:87], v13 offset:64\n\t" \
    "ds_read_b64_tr_b16 v[92:93], v13 offset:1152\n\t" \
    "v_smfmac_f32_16x16x64_bf16 v[100:103], v[36:39], a[0:7], v10\n\t" \
    "ds_read_b64_tr_b16 v[94:95], v13 offset:1216\n\t" \
    "ds_read_b64_tr_b16 v[88:89], v25 offset:0\n\t" \
    "v_smfmac_f32_16x16x64_bf16 v[100:103], v[36:39], a[0:7], v10\n\t" \
    "ds_read_b64_tr_b16 v[90:91], v25 offset:64\n\t" \
    "s_waitcnt lgkmcnt(4)\n\t" \
    "v_smfmac_f32_16x16x64_bf16 v[124:127], v[84:87], v[240:247], v19\n\t" \
    "ds_read_b64_tr_b16 v[96:97], v25 offset:1152\n\t" \
    "ds_read_b64_tr_b16 v[98:99], v25 offset:1216\n\t" \
    "s_waitcnt lgkmcnt(4)\n\t" \
    "v_smfmac_f32_16x16x64_bf16 v[124:127], v[92:95], v[248:255], v19\n\t" \
    "s_nop 2\n\t" \
    "v_smfmac_f32_16x16x64_bf16 v[108:111], v[84:87], a[144:151], v19\n\t" \
    "s_nop 2\n\t" \
    "v_smfmac_f32_16x16x64_bf16 v[108:111], v[92:95], a[160:167], v19\n\t" \
    "s_nop 1\n\t" \
    "v_permlane32_swap_b32 v124, v126\n\t" \
    "v_smfmac_f32_16x16x64_bf16 v[112:115], v[84:87], a[176:183], v19\n\t" \
    "s_nop 1\n\t" \
    "v_permlane32_swap_b32 v125, v127\n\t" \
    "v_smfmac_f32_16x16x64_bf16 v[112:115], v[92:95], a[192:199], v19\n\t" \
    "s_nop 0\n\t" \
    "v_add_f32 v124, v124, v126\n\t" \
    "s_waitcnt lgkmcnt(2)\n\t" \
    "v_smfmac_f32_16x16x64_bf16 v[108:111], v[88:91], a[152:159], v19\n\t" \
    "s_nop 1\n\t" \
    "v_add_f32 v125, v125, v127\n\t" \
    "v_smfmac_f32_16x16x64_bf16 v[112:115], v[88:91], a[184:191], v19\n\t" \
    "s_nop 0\n\t" \
    "v_cvt_pk_bf16_f32 v136, v124, v125\n\t" \
    "s_waitcnt lgkmcnt(0)\n\t" \
    "v_smfmac_f32_16x16x64_bf16 v[108:111], v[96:99], a[168:175], v19\n\t" \
    "s_nop 1\n\t" \
    "v_cvt_pk_bf16_f32 v137, v124, v125\n\t" \
    "v_smfmac_f32_16x16x64_bf16 v[112:115], v[96:99], a[200:207], v19\n\t" \
    "s_nop 1\n\t" \
    "v_permlane32_swap_b32 v136, v137\n\t" \
    "v_smfmac_f32_16x16x64_bf16 v[116:119], v[84:87], a[208:215], v19\n\t" \
    "s_mov_b32 exec_hi, 0\n\t" \
    "global_store_dwordx2 v16, v[136:137], s[38:39] sc1\n\t" \
    "s_mov_b32 exec_hi, -1\n\t" \
    "v_cvt_pk_bf16_f32 v128, v108, v109\n\t" \
    "v_smfmac_f32_16x16x64_bf16 v[116:119], v[88:91], a[216:223], v19\n\t" \
    "s_nop 1\n\t" \
    "v_cvt_pk_bf16_f32 v129, v110, v111\n\t" \
    "v_smfmac_f32_16x16x64_bf16 v[116:119], v[92:95], a[224:231], v19\n\t" \
    "s_nop 1\n\t" \
    "v_cvt_pk_bf16_f32 v130, v112, v113\n\t" \
    "v_smfmac_f32_16x16x64_bf16 v[116:119], v[96:99], a[232:239], v19\n\t" \
    "s_nop 1\n\t" \
    "v_cvt_pk_bf16_f32 v131, v114, v115\n\t" \
    "v_smfmac_f32_16x16x64_bf16 v[120:123], v[84:87], a[240:247], v19\n\t" \
    "s_nop 1\n\t" \
    "v_permlane16_swap_b32 v128, v130\n\t" \
    "v_smfmac_f32_16x16x64_bf16 v[120:123], v[88:91], a[248:255], v19\n\t" \
    "s_nop 1\n\t" \
    "v_permlane16_swap_b32 v129, v131\n\t" \
    "v_smfmac_f32_16x16x64_bf16 v[120:123], v[92:95], v[224:231], v19\n\t" \
    "s_nop 0\n\t" \
    "global_store_dwordx4 v14, v[128:131], s[38:39] sc1\n\t" \
    "v_cvt_pk_bf16_f32 v132, v116, v117\n\t" \
    "v_smfmac_f32_16x16x64_bf16 v[120:123], v[96:99], v[232:239], v19\n\t" \
    "s_nop 1\n\t" \
    "v_cvt_pk_bf16_f32 v133, v118, v119\n\t" \
    "s_nop 4\n\t" \
    "v_cvt_pk_bf16_f32 v134, v120, v121\n\t" \
    "v_cvt_pk_bf16_f32 v135, v122, v123\n\t" \
    "s_nop 0\n\t" \
    "v_permlane16_swap_b32 v132, v134\n\t" \
    "v_permlane16_swap_b32 v133, v135\n\t" \
    "global_store_dwordx4 v15, v[132:135], s[38:39] sc1\n\t" \
    "s_cmp_eq_u32 s65, 63\n\t" \
    "s_cbranch_scc1 vv_end27_%=\n\t" \
    "s_add_u32 s65, s65, 1\n\t" \
    "v_mov_b32 v72, 0\n\t" \
    "v_mov_b32 v73, 0\n\t" \
    "v_mov_b32 v74, 0\n\t" \
    "v_mov_b32 v75, 0\n\t" \
    "v_mov_b32 v76, 0\n\t" \
    "v_mov_b32 v77, 0\n\t" \
    "v_mov_b32 v78, 0\n\t" \
    "v_mov_b32 v79, 0\n\t" \
    "v_mov_b32 v108, 0\n\t" \
    "v_mov_b32 v109, 0\n\t" \
    "v_mov_b32 v110, 0\n\t" \
    "v_mov_b32 v111, 0\n\t" \
    "v_mov_b32 v112, 0\n\t" \
    "v_mov_b32 v113, 0\n\t" \
    "v_mov_b32 v114, 0\n\t" \
    "v_mov_b32 v115, 0\n\t" \
    "v_mov_b32 v116, 0\n\t" \
    "v_mov_b32 v117, 0\n\t" \
    "v_mov_b32 v118, 0\n\t" \
    "v_mov_b32 v119, 0\n\t" \
    "v_mov_b32 v120, 0\n\t" \
    "v_mov_b32 v121, 0\n\t" \
    "v_mov_b32 v122, 0\n\t" \
    "v_mov_b32 v123, 0\n\t" \
    "v_mov_b32 v124, 0\n\t" \
    "v_mov_b32 v125, 0\n\t" \
    "v_mov_b32 v126, 0\n\t" \
    "v_mov_b32 v127, 0\n\t" \
    "s_add_u32 s69, s65, 0\n\t" \
    "s_and_b32 s69, s69, 7\n\t" \
    "s_lshl_b32 s69, s69, 18\n\t" \
    "s_add_u32 s69, s69, s62\n\t" \
    "s_add_u32 s46, s36, s69\n\t" \
    "s_addc_u32 s47, s37, 0\n\t" \
    "s_add_u32 s70, s65, 0\n\t" \
    "s_mul_i32 s69, s70, 171\n\t" \
    "s_lshr_b32 s69, s69, 9\n\t" \
    "s_mul_i32 s70, s70, 589824\n\t" \
    "s_mul_i32 s71, s69, 327680\n\t" \
    "s_add_u32 s70, s70, s71\n\t" \
    "s_lshl1_add_u32 s69, s69, 4\n\t" \
    "s_bitcmp1_b64 s[44:45], s69\n\t" \
    "s_cselect_b32 s69, 8192, 0\n\t" \
    "s_add_u32 s69, s69, s70\n\t" \
    "s_add_u32 s69, s69, 123731968\n\t" \
    "s_add_u32 s38, s36, s69\n\t" \
    "s_addc_u32 s39, s37, 0\n\t" \
    "s_and_b32 s69, s69, 0xffe00000\n\t" \
    "s_add_u32 s69, s69, 1823744\n\t" \
    "v_mov_b32 v17, s69\n\t" \
    "global_load_dword v17, v17, s[36:37]\n\t" \
    "s_barrier\n\t" \
    "ds_read_b32 v160, v11 offset:20480\n\t" \
    "s_waitcnt lgkmcnt(0)\n\t" \
    "v_readfirstlane_b32 s70, v160\n\t" \
    "s_branch vv_loop10_%=\n\t" \
    "vv_Gbad32_%=:\n\t" \
    "s_waitcnt lgkmcnt(0)\n\t" \
    "s_nop 7\n\t" \
    "v_mov_b32 v72, 0\n\t" \
    "v_mov_b32 v73, 0\n\t" \
    "v_mov_b32 v74, 0\n\t" \
    "v_mov_b32 v75, 0\n\t" \
    "v_mov_b32 v76, 0\n\t" \
    "v_mov_b32 v77, 0\n\t" \
    "v_mov_b32 v78, 0\n\t" \
    "v_mov_b32 v79, 0\n\t" \
    "s_cmp_eq_u32 s68, 0\n\t" \
    "s_cbranch_scc1 vv_F36_%=\n\t" \
    "s_sub_u32 s68, s68, 1\n\t" \
    "s_branch vv_G31_%=\n\t" \
    "vv_F36_%=:\n\t" \
    "v_mov_b32 v140, 5767424\n\t" \
    "v_mov_b32 v141, 1\n\t" \
    "global_store_dword v140, v141, s[36:37] sc1\n\t" \
    "s_waitcnt vmcnt(0)\n\t" \
    "s_branch vv_Gc33_%=\n\t" \
    "vv_Gslow34_%=:\n\t" \
    "v_max_u32 v140, v36, v37\n\t" \
    "v_max3_u32 v140, v140, v38, v39\n\t" \
    "v_max3_u32 v140, v140, v40, v41\n\t" \
    "v_max3_u32 v140, v140, v42, v43\n\t" \
    "v_max3_u32 v140, v140, v44, v45\n\t" \
    "v_max3_u32 v140, v140, v46, v47\n\t" \
    "v_max3_u32 v140, v140, v48, v49\n\t" \
    "v_max3_u32 v140, v140, v50, v51\n\t" \
    "v_max3_u32 v140, v140, v52, v53\n\t" \
    "v_max3_u32 v140, v140, v54, v55\n\t" \
    "v_max3_u32 v140, v140, v56, v57\n\t" \
    "v_max3_u32 v140, v140, v58, v59\n\t" \
    "v_max3_u32 v140, v140, v60, v61\n\t" \
    "v_max3_u32 v140, v140, v62, v63\n\t" \
    "v_max3_u32 v140, v140, v64, v65\n\t" \
    "v_max3_u32 v140, v140, v66, v67\n\t" \
    "v_max3_u32 v140, v140, v68, v69\n\t" \
    "v_max3_u32 v140, v140, v70, v71\n\t" \
    "v_cmp_eq_u32 vcc, -1, v140\n\t" \
    "s_cbranch_vccz vv_Gback35_%=\n\t" \
    "s_waitcnt lgkmcnt(0)\n\t" \
    "v_sub_u32 v140, v1, v3\n\t" \
    "s_nop 0\n\t" \
    "v_readfirstlane_b32 s69, v140\n\t" \
    "v_add_u32 v143, 20480, v0\n\t" \
    "s_nop 7\n\t" \
    "s_add_u32 s30, s46, s69\n\t" \
    "s_addc_u32 s31, s47, 0\n\t" \
    "vv_Gag37_%=:\n\t" \
    "global_load_dwordx4 v[36:39], v1, s[46:47] sc1\n\t" \
    "global_load_dwordx4 v[40:43], v23, s[46:47] sc1\n\t" \
    "global_load_dwordx4 v[44:47], v143, s[30:31] sc1\n\t" \
    "global_load_dwordx4 v[48:51], v4, s[30:31] sc1\n\t" \
    "global_load_dwordx4 v[52:55], v5, s[30:31] sc1\n\t" \
    "global_load_dwordx4 v[56:59], v6, s[30:31] sc1\n\t" \
    "global_load_dwordx4 v[60:63], v7, s[30:31] sc1\n\t" \
    "global_load_dwordx4 v[64:67], v8, s[30:31] sc1\n\t" \
    "global_load_dwordx4 v[68:71], v9, s[30:31] sc1\n\t" \
    "s_waitcnt vmcnt(0)\n\t" \
    "v_max_u32 v140, v36, v37\n\t" \
    "v_max3_u32 v140, v140, v38, v39\n\t" \
    "v_max3_u32 v140, v140, v40, v41\n\t" \
    "v_max3_u32 v140, v140, v42, v43\n\t" \
    "v_max3_u32 v140, v140, v44, v45\n\t" \
    "v_max3_u32 v140, v140, v46, v47\n\t" \
    "v_max3_u32 v140, v140, v48, v49\n\t" \
    "v_max3_u32 v140, v140, v50, v51\n\t" \
    "v_max3_u32 v140, v140, v52, v53\n\t" \
    "v_max3_u32 v140, v140, v54, v55\n\t" \
    "v_max3_u32 v140, v140, v56, v57\n\t" \
    "v_max3_u32 v140, v140, v58, v59\n\t" \
    "v_max3_u32 v140, v140, v60, v61\n\t" \
    "v_max3_u32 v140, v140, v62, v63\n\t" \
    "v_max3_u32 v140, v140, v64, v65\n\t" \
    "v_max3_u32 v140, v140, v66, v67\n\t" \
    "v_max3_u32 v140, v140, v68, v69\n\t" \
    "v_max3_u32 v140, v140, v70, v71\n\t" \
    "v_cmp_eq_u32 vcc, -1, v140\n\t" \
    "s_cbranch_vccz vv_Gredo38_%=\n\t" \
    "s_cmp_eq_u32 s68, 0\n\t" \
    "s_cbranch_scc1 vv_F39_%=\n\t" \
    "s_sub_u32 s68, s68, 1\n\t" \
    "s_sleep 1\n\t" \
    "s_branch vv_Gag37_%=\n\t" \
    "vv_F39_%=:\n\t" \
    "v_mov_b32 v140, 5767424\n\t" \
    "v_mov_b32 v141, 2\n\t" \
    "global_store_dword v140, v141, s[36:37] sc1\n\t" \
    "s_waitcnt vmcnt(0)\n\t" \
    "s_branch vv_Gredo38_%=\n\t" \
    "vv_Gredo38_%=:\n\t" \
    "v_mov_b32 v72, 0\n\t" \
    "v_mov_b32 v73, 0\n\t" \
    "v_mov_b32 v74, 0\n\t" \
    "v_mov_b32 v75, 0\n\t" \
    "v_mov_b32 v76, 0\n\t" \
    "v_mov_b32 v77, 0\n\t" \
    "v_mov_b32 v78, 0\n\t" \
    "v_mov_b32 v79, 0\n\t" \
    "v_smfmac_f32_16x16x64_bf16 v[72:75], v[36:39], a[16:23], v10\n\t" \
    "v_smfmac_f32_16x16x64_bf16 v[72:75], v[40:43], a[24:31], v10\n\t" \
    "v_smfmac_f32_16x16x64_bf16 v[72:75], v[48:51], a[32:39], v10\n\t" \
    "v_smfmac_f32_16x16x64_bf16 v[72:75], v[52:55], a[40:47], v10\n\t" \
    "v_smfmac_f32_16x16x64_bf16 v[72:75], v[56:59], a[48:55], v10\n\t" \
    "v_smfmac_f32_16x16x64_bf16 v[72:75], v[60:63], a[56:63], v10\n\t" \
    "v_smfmac_f32_16x16x64_bf16 v[72:75], v[64:67], a[0:7], v10\n\t" \
    "v_smfmac_f32_16x16x64_bf16 v[72:75], v[68:71], a[8:15], v10\n\t" \
    "v_smfmac_f32_16x16x64_bf16 v[72:75], v[44:47], a[64:71], v10\n\t" \
    "v_smfmac_f32_16x16x64_bf16 v[76:79], v[36:39], a[88:95], v10\n\t" \
    "v_smfmac_f32_16x16x64_bf16 v[76:79], v[40:43], a[96:103], v10\n\t" \
    "v_smfmac_f32_16x16x64_bf16 v[76:79], v[48:51], a[104:111], v10\n\t" \
    "v_smfmac_f32_16x16x64_bf16 v[76:79], v[52:55], a[112:119], v10\n\t" \
    "v_smfmac_f32_16x16x64_bf16 v[76:79], v[56:59], a[120:127], v10\n\t" \
    "v_smfmac_f32_16x16x64_bf16 v[76:79], v[60:63], a[128:135], v10\n\t" \
    "v_smfmac_f32_16x16x64_bf16 v[76:79], v[64:67], a[72:79], v10\n\t" \
    "v_smfmac_f32_16x16x64_bf16 v[76:79], v[68:71], a[80:87], v10\n\t" \
    "v_smfmac_f32_16x16x64_bf16 v[76:79], v[44:47], a[136:143], v10\n\t" \
    "v_add_f32 v72, v72, v74\n\t" \
    "v_add_f32 v73, v73, v75\n\t" \
    "v_cvt_pk_bf16_f32 v80, v72, v73\n\t" \
    "ds_write_b32 v12, v80 offset:0\n\t" \
    "s_nop 3\n\t" \
    "v_pk_add_f32 v[76:77], v[76:77], v[78:79]\n\t" \
    "v_cvt_pk_bf16_f32 v81, v76, v77\n\t" \
    "ds_write_b32 v12, v81 offset:256\n\t" \
    "s_branch vv_Gback35_%=\n\t" \
    "vv_end27_%=:\n\t" \
    "s_endpgm\n\t" \
    ".p2align 6\n\t" \
    "s_nop 0\n\t" \
    "s_nop 0\n\t" \
    "s_nop 0\n\t" \
    "s_nop 0\n\t" \
    "s_nop 0\n\t" \
    "s_nop 0\n\t" \
    "vv_loop11_%=:\n\t" \
    "s_getpc_b64 s[30:31]\n\t" \
    "vv_Wp42_%=:\n\t" \
    "s_add_u32 s30, s30, vv_We43_%=-vv_Wp42_%=\n\t" \
    "s_addc_u32 s31, s31, 0\n\t" \
    "s_sub_u32 s70, s70, 96\n\t" \
    "vv_W41_%=:\n\t" \
    "s_memtime s[56:57]\n\t" \
    "s_waitcnt lgkmcnt(0)\n\t" \
    "s_sub_u32 s69, s70, s56\n\t" \
    "s_cmp_gt_i32 s69, 80\n\t" \
    "s_cbranch_scc1 vv_W41_%=\n\t" \
    "s_max_i32 s69, s69, 0\n\t" \
    "s_and_b32 s69, s69, -4\n\t" \
    "s_sub_u32 s30, s30, s69\n\t" \
    "s_subb_u32 s31, s31, 0\n\t" \
    "s_setpc_b64 s[30:31]\n\t" \
    "s_nop 0\n\t" \
    "s_nop 0\n\t" \
    "s_nop 0\n\t" \
    "s_nop 0\n\t" \
    "s_nop 0\n\t" \
    "s_nop 0\n\t" \
    "s_nop 0\n\t" \
    "s_nop 0\n\t" \
    "s_nop 0\n\t" \
    "s_nop 0\n\t" \
    "s_nop 0\n\t" \
    "s_nop 0\n\t" \
    "s_nop 0\n\t" \
    "s_nop 0\n\t" \
    "s_nop 0\n\t" \
    "s_nop 0\n\t" \
    "s_nop 0\n\t" \
    "s_nop 0\n\t" \
    "s_nop 0\n\t" \
    "s_nop 0\n\t" \
    "vv_We43_%=:\n\t" \
    "vv_G44_%=:\n\t" \
    "global_load_dwordx4 v[36:39], v1, s[46:47] sc1\n\t" \
    "global_load_dword v141, v2, s[46:47] sc1\n\t" \
    "global_load_dwordx4 v[40:43], v23, s[46:47] sc1\n\t" \
    "s_waitcnt vmcnt(2)\n\t" \
    "ds_write_b128 v3, v[36:39]\n\t" \
    "v_smfmac_f32_16x16x64_bf16 v[72:75], v[36:39], a[32:39], v10\n\t" \
    "s_waitcnt vmcnt(1)\n\t" \
    "ds_write_b32 v22, v141 offset:19968\n\t" \
    "s_waitcnt vmcnt(0)\n\t" \
    "ds_write_b128 v3, v[40:43] offset:1024\n\t" \
    "v_max3_u32 v140, v36, v40, v141\n\t" \
    "v_cmp_eq_u32 vcc, -1, v140\n\t" \
    "s_cbranch_vccnz vv_Gbad45_%=\n\t" \
    "vv_Gc46_%=:\n\t" \
    "s_waitcnt lgkmcnt(0)\n\t" \
    "s_barrier\n\t" \
    "ds_read_b128 v[48:51], v4\n\t" \
    "ds_read_b128 v[52:55], v5\n\t" \
    "v_smfmac_f32_16x16x64_bf16 v[76:79], v[36:39], a[104:111], v10\n\t" \
    "ds_read_b128 v[56:59], v6\n\t" \
    "ds_read_b128 v[60:63], v7\n\t" \
    "v_smfmac_f32_16x16x64_bf16 v[72:75], v[40:43], a[40:47], v10\n\t" \
    "ds_read_b128 v[64:67], v8\n\t" \
    "v_smfmac_f32_16x16x64_bf16 v[76:79], v[40:43], a[112:119], v10\n\t" \
    "ds_read_b128 v[68:71], v9\n\t" \
    "s_waitcnt lgkmcnt(5)\n\t" \
    "v_smfmac_f32_16x16x64_bf16 v[72:75], v[48:51], a[48:55], v10\n\t" \
    "ds_read_b128 v[44:47], v0 offset:19456\n\t" \
    "s_waitcnt lgkmcnt(5)\n\t" \
    "v_smfmac_f32_16x16x64_bf16 v[72:75], v[52:55], a[56:63], v10\n\t" \
    "s_waitcnt lgkmcnt(4)\n\t" \
    "v_smfmac_f32_16x16x64_bf16 v[72:75], v[56:59], a[0:7], v10\n\t" \
    "s_waitcnt lgkmcnt(3)\n\t" \
    "v_smfmac_f32_16x16x64_bf16 v[72:75], v[60:63], a[8:15], v10\n\t" \
    "s_waitcnt lgkmcnt(2)\n\t" \
    "v_smfmac_f32_16x16x64_bf16 v[72:75], v[64:67], a[16:23], v10\n\t" \
    "s_waitcnt lgkmcnt(1)\n\t" \
    "v_smfmac_f32_16x16x64_bf16 v[72:75], v[68:71], a[24:31], v10\n\t" \
    "s_waitcnt lgkmcnt(0)\n\t" \
    "v_smfmac_f32_16x16x64_bf16 v[72:75], v[44:47], a[64:71], v10\n\t" \
    "s_nop 2\n\t" \
    "v_smfmac_f32_16x16x64_bf16 v[76:79], v[48:51], a[120:127], v10\n\t" \
    "s_nop 2\n\t" \
    "v_smfmac_f32_16x16x64_bf16 v[76:79], v[52:55], a[128:135], v10\n\t" \
    "v_add_f32 v72, v72, v74\n\t" \
    "v_smfmac_f32_16x16x64_bf16 v[76:79], v[56:59], a[72:79], v10\n\t" \
    "v_add_f32 v73, v73, v75\n\t" \
    "v_smfmac_f32_16x16x64_bf16 v[76:79], v[60:63], a[80:87], v10\n\t" \
    "v_cvt_pk_bf16_f32 v80, v72, v73\n\t" \
    "v_smfmac_f32_16x16x64_bf16 v[76:79], v[64:67], a[88:95], v10\n\t" \
    "ds_write_b32 v12, v80 offset:0\n\t" \
    "v_smfmac_f32_16x16x64_bf16 v[76:79], v[68:71], a[96:103], v10\n\t" \
    "v_smfmac_f32_16x16x64_bf16 v[76:79], v[44:47], a[136:143], v10\n\t" \
    "s_nop 6\n\t" \
    "v_cmp_u_f32 vcc, v72, v73\n\t" \
    "v_pk_add_f32 v[76:77], v[76:77], v[78:79]\n\t" \
    "v_cvt_pk_bf16_f32 v81, v76, v77\n\t" \
    "ds_write_b32 v12, v81 offset:256\n\t" \
    "s_cbranch_vccnz vv_Gslow47_%=\n\t" \
    "vv_Gback48_%=:\n\t" \
    "s_waitcnt lgkmcnt(0)\n\t" \
    "s_barrier\n\t" \
    "ds_read_b64_tr_b16 v[84:85], v13 offset:0\n\t" \
    "v_smfmac_f32_16x16x64_bf16 v[100:103], v[36:39], a[0:7], v10\n\t" \
    "ds_read_b64_tr_b16 v[86:87], v13 offset:64\n\t" \
    "ds_read_b64_tr_b16 v[92:93], v13 offset:1152\n\t" \
    "v_smfmac_f32_16x16x64_bf16 v[100:103], v[36:39], a[0:7], v10\n\t" \
    "ds_read_b64_tr_b16 v[94:95], v13 offset:1216\n\t" \
    "ds_read_b64_tr_b16 v[88:89], v25 offset:0\n\t" \
    "v_smfmac_f32_16x16x64_bf16 v[100:103], v[36:39], a[0:7], v10\n\t" \
    "ds_read_b64_tr_b16 v[90:91], v25 offset:64\n\t" \
    "s_waitcnt lgkmcnt(4)\n\t" \
    "v_smfmac_f32_16x16x64_bf16 v[124:127], v[84:87], v[240:247], v19\n\t" \
    "ds_read_b64_tr_b16 v[96:97], v25 offset:1152\n\t" \
    "ds_read_b64_tr_b16 v[98:99], v25 offset:1216\n\t" \
    "s_waitcnt lgkmcnt(4)\n\t" \
    "v_smfmac_f32_16x16x64_bf16 v[124:127], v[92:95], v[248:255], v19\n\t" \
    "s_nop 2\n\t" \
    "v_smfmac_f32_16x16x64_bf16 v[108:111], v[84:87], a[144:151], v19\n\t" \
    "s_nop 2\n\t" \
    "v_smfmac_f32_16x16x64_bf16 v[108:111], v[92:95], a[160:167], v19\n\t" \
    "s_nop 1\n\t" \
    "v_permlane32_swap_b32 v124, v126\n\t" \
    "v_smfmac_f32_16x16x64_bf16 v[112:115], v[84:87], a[176:183], v19\n\t" \
    "s_nop 1\n\t" \
    "v_permlane32_swap_b32 v125, v127\n\t" \
    "v_smfmac_f32_16x16x64_bf16 v[112:115], v[92:95], a[192:199], v19\n\t" \
    "s_nop 0\n\t" \
    "v_add_f32 v124, v124, v126\n\t" \
    "s_waitcnt lgkmcnt(2)\n\t" \
    "v_smfmac_f32_16x16x64_bf16 v[108:111], v[88:91], a[152:159], v19\n\t" \
    "s_nop 1\n\t" \
    "v_add_f32 v125, v125, v127\n\t" \
    "v_smfmac_f32_16x16x64_bf16 v[112:115], v[88:91], a[184:191], v19\n\t" \
    "s_nop 0\n\t" \
    "v_cvt_pk_bf16_f32 v136, v124, v125\n\t" \
    "s_waitcnt lgkmcnt(0)\n\t" \
    "v_smfmac_f32_16x16x64_bf16 v[108:111], v[96:99], a[168:175], v19\n\t" \
    "s_nop 1\n\t" \
    "v_cvt_pk_bf16_f32 v137, v124, v125\n\t" \
    "v_smfmac_f32_16x16x64_bf16 v[112:115], v[96:99], a[200:207], v19\n\t" \
    "s_nop 1\n\t" \
    "v_permlane32_swap_b32 v136, v137\n\t" \
    "v_smfmac_f32_16x16x64_bf16 v[116:119], v[84:87], a[208:215], v19\n\t" \
    "s_mov_b32 exec_hi, 0\n\t" \
    "global_store_dwordx2 v16, v[136:137], s[38:39] sc1\n\t" \
    "s_mov_b32 exec_hi, -1\n\t" \
    "v_cvt_pk_bf16_f32 v128, v108, v109\n\t" \
    "v_smfmac_f32_16x16x64_bf16 v[116:119], v[88:91], a[216:223], v19\n\t" \
    "s_nop 1\n\t" \
    "v_cvt_pk_bf16_f32 v129, v110, v111\n\t" \
    "v_smfmac_f32_16x16x64_bf16 v[116:119], v[92:95], a[224:231], v19\n\t" \
    "s_nop 1\n\t" \
    "v_cvt_pk_bf16_f32 v130, v112, v113\n\t" \
    "v_smfmac_f32_16x16x64_bf16 v[116:119], v[96:99], a[232:239], v19\n\t" \
    "s_nop 1\n\t" \
    "v_cvt_pk_bf16_f32 v131, v114, v115\n\t" \
    "v_smfmac_f32_16x16x64_bf16 v[120:123], v[84:87], a[240:247], v19\n\t" \
    "s_nop 1\n\t" \
    "v_permlane16_swap_b32 v128, v130\n\t" \
    "v_smfmac_f32_16x16x64_bf16 v[120:123], v[88:91], a[248:255], v19\n\t" \
    "s_nop 1\n\t" \
    "v_permlane16_swap_b32 v129, v131\n\t" \
    "v_smfmac_f32_16x16x64_bf16 v[120:123], v[92:95], v[224:231], v19\n\t" \
    "s_nop 0\n\t" \
    "global_store_dwordx4 v14, v[128:131], s[38:39] sc1\n\t" \
    "v_cvt_pk_bf16_f32 v132, v116, v117\n\t" \
    "v_smfmac_f32_16x16x64_bf16 v[120:123], v[96:99], v[232:239], v19\n\t" \
    "s_nop 1\n\t" \
    "v_cvt_pk_bf16_f32 v133, v118, v119\n\t" \
    "s_nop 4\n\t" \
    "v_cvt_pk_bf16_f32 v134, v120, v121\n\t" \
    "v_cvt_pk_bf16_f32 v135, v122, v123\n\t" \
    "s_nop 0\n\t" \
    "v_permlane16_swap_b32 v132, v134\n\t" \
    "v_permlane16_swap_b32 v133, v135\n\t" \
    "global_store_dwordx4 v15, v[132:135], s[38:39] sc1\n\t" \
    "s_cmp_eq_u32 s65, 63\n\t" \
    "s_cbranch_scc1 vv_end40_%=\n\t" \
    "s_add_u32 s65, s65, 1\n\t" \
    "v_mov_b32 v72, 0\n\t" \
    "v_mov_b32 v73, 0\n\t" \
    "v_mov_b32 v74, 0\n\t" \
    "v_mov_b32 v75, 0\n\t" \
    "v_mov_b32 v76, 0\n\t" \
    "v_mov_b32 v77, 0\n\t" \
    "v_mov_b32 v78, 0\n\t" \
    "v_mov_b32 v79, 0\n\t" \
    "v_mov_b32 v108, 0\n\t" \
    "v_mov_b32 v109, 0\n\t" \
    "v_mov_b32 v110, 0\n\t" \
    "v_mov_b32 v111, 0\n\t" \
    "v_mov_b32 v112, 0\n\t" \
    "v_mov_b32 v113, 0\n\t" \
    "v_mov_b32 v114, 0\n\t" \
    "v_mov_b32 v115, 0\n\t" \
    "v_mov_b32 v116, 0\n\t" \
    "v_mov_b32 v117, 0\n\t" \
    "v_mov_b32 v118, 0\n\t" \
    "v_mov_b32 v119, 0\n\t" \
    "v_mov_b32 v120, 0\n\t" \
    "v_mov_b32 v121, 0\n\t" \
    "v_mov_b32 v122, 0\n\t" \
    "v_mov_b32 v123, 0\n\t" \
    "v_mov_b32 v124, 0\n\t" \
    "v_mov_b32 v125, 0\n\t" \
    "v_mov_b32 v126, 0\n\t" \
    "v_mov_b32 v127, 0\n\t" \
    "s_add_u32 s69, s65, 0\n\t" \
    "s_and_b32 s69, s69, 7\n\t" \
    "s_lshl_b32 s69, s69, 18\n\t" \
    "s_add_u32 s69, s69, s62\n\t" \
    "s_add_u32 s46, s36, s69\n\t" \
    "s_addc_u32 s47, s37, 0\n\t" \
    "s_add_u32 s70, s65, 0\n\t" \
    "s_mul_i32 s69, s70, 171\n\t" \
    "s_lshr_b32 s69, s69, 9\n\t" \
    "s_mul_i32 s70, s70, 589824\n\t" \
    "s_mul_i32 s71, s69, 327680\n\t" \
    "s_add_u32 s70, s70, s71\n\t" \
    "s_lshl1_add_u32 s69, s69, 4\n\t" \
    "s_bitcmp1_b64 s[44:45], s69\n\t" \
    "s_cselect_b32 s69, 8192, 0\n\t" \
    "s_add_u32 s69, s69, s70\n\t" \
    "s_add_u32 s69, s69, 123731968\n\t" \
    "s_add_u32 s38, s36, s69\n\t" \
    "s_addc_u32 s39, s37, 0\n\t" \
    "s_and_b32 s69, s69, 0xffe00000\n\t" \
    "s_add_u32 s69, s69, 1823744\n\t" \
    "v_mov_b32 v17, s69\n\t" \
    "global_load_dword v17, v17, s[36:37]\n\t" \
    "s_barrier\n\t" \
    "ds_read_b32 v160, v11 offset:20480\n\t" \
    "s_waitcnt lgkmcnt(0)\n\t" \
    "v_readfirstlane_b32 s70, v160\n\t" \
    "s_branch vv_loop11_%=\n\t" \
    "vv_Gbad45_%=:\n\t" \
    "s_waitcnt lgkmcnt(0)\n\t" \
    "s_nop 7\n\t" \
    "v_mov_b32 v72, 0\n\t" \
    "v_mov_b32 v73, 0\n\t" \
    "v_mov_b32 v74, 0\n\t" \
    "v_mov_b32 v75, 0\n\t" \
    "v_mov_b32 v76, 0\n\t" \
    "v_mov_b32 v77, 0\n\t" \
    "v_mov_b32 v78, 0\n\t" \
    "v_mov_b32 v79, 0\n\t" \
    "s_cmp_eq_u32 s68, 0\n\t" \
    "s_cbranch_scc1 vv_F49_%=\n\t" \
    "s_sub_u32 s68, s68, 1\n\t" \
    "s_branch vv_G44_%=\n\t" \
    "vv_F49_%=:\n\t" \
    "v_mov_b32 v140, 5767424\n\t" \
    "v_mov_b32 v141, 1\n\t" \
    "global_store_dword v140, v141, s[36:37] sc1\n\t" \
    "s_waitcnt vmcnt(0)\n\t" \
    "s_branch vv_Gc46_%=\n\t" \
    "vv_Gslow47_%=:\n\t" \
    "v_max_u32 v140, v36, v37\n\t" \
    "v_max3_u32 v140, v140, v38, v39\n\t" \
    "v_max3_u32 v140, v140, v40, v41\n\t" \
    "v_max3_u32 v140, v140, v42, v43\n\t" \
    "v_max3_u32 v140, v140, v44, v45\n\t" \
    "v_max3_u32 v140, v140, v46, v47\n\t" \
    "v_max3_u32 v140, v140, v48, v49\n\t" \
    "v_max3_u32 v140, v140, v50, v51\n\t" \
    "v_max3_u32 v140, v140, v52, v53\n\t" \
    "v_max3_u32 v140, v140, v54, v55\n\t" \
    "v_max3_u32 v140, v140, v56, v57\n\t" \
    "v_max3_u32 v140, v140, v58, v59\n\t" \
    "v_max3_u32 v140, v140, v60, v61\n\t" \
    "v_max3_u32 v140, v140, v62, v63\n\t" \
    "v_max3_u32 v140, v140, v64, v65\n\t" \
    "v_max3_u32 v140, v140, v66, v67\n\t" \
    "v_max3_u32 v140, v140, v68, v69\n\t" \
    "v_max3_u32 v140, v140, v70, v71\n\t" \
    "v_cmp_eq_u32 vcc, -1, v140\n\t" \
    "s_cbranch_vccz vv_Gback48_%=\n\t" \
    "s_waitcnt lgkmcnt(0)\n\t" \
    "v_sub_u32 v140, v1, v3\n\t" \
    "s_nop 0\n\t" \
    "v_readfirstlane_b32 s69, v140\n\t" \
    "v_add_u32 v143, 20480, v0\n\t" \
    "s_nop 7\n\t" \
    "s_add_u32 s30, s46, s69\n\t" \
    "s_addc_u32 s31, s47, 0\n\t" \
    "vv_Gag50_%=:\n\t" \
    "global_load_dwordx4 v[36:39], v1, s[46:47] sc1\n\t" \
    "global_load_dwordx4 v[40:43], v23, s[46:47] sc1\n\t" \
    "global_load_dwordx4 v[44:47], v143, s[30:31] sc1\n\t" \
    "global_load_dwordx4 v[48:51], v4, s[30:31] sc1\n\t" \
    "global_load_dwordx4 v[52:55], v5, s[30:31] sc1\n\t" \
    "global_load_dwordx4 v[56:59], v6, s[30:31] sc1\n\t" \
    "global_load_dwordx4 v[60:63], v7, s[30:31] sc1\n\t" \
    "global_load_dwordx4 v[64:67], v8, s[30:31] sc1\n\t" \
    "global_load_dwordx4 v[68:71], v9, s[30:31] sc1\n\t" \
    "s_waitcnt vmcnt(0)\n\t" \
    "v_max_u32 v140, v36, v37\n\t" \
    "v_max3_u32 v140, v140, v38, v39\n\t" \
    "v_max3_u32 v140, v140, v40, v41\n\t" \
    "v_max3_u32 v140, v140, v42, v43\n\t" \
    "v_max3_u32 v140, v140, v44, v45\n\t" \
    "v_max3_u32 v140, v140, v46, v47\n\t" \
    "v_max3_u32 v140, v140, v48, v49\n\t" \
    "v_max3_u32 v140, v140, v50, v51\n\t" \
    "v_max3_u32 v140, v140, v52, v53\n\t" \
    "v_max3_u32 v140, v140, v54, v55\n\t" \
    "v_max3_u32 v140, v140, v56, v57\n\t" \
    "v_max3_u32 v140, v140, v58, v59\n\t" \
    "v_max3_u32 v140, v140, v60, v61\n\t" \
    "v_max3_u32 v140, v140, v62, v63\n\t" \
    "v_max3_u32 v140, v140, v64, v65\n\t" \
    "v_max3_u32 v140, v140, v66, v67\n\t" \
    "v_max3_u32 v140, v140, v68, v69\n\t" \
    "v_max3_u32 v140, v140, v70, v71\n\t" \
    "v_cmp_eq_u32 vcc, -1, v140\n\t" \
    "s_cbranch_vccz vv_Gredo51_%=\n\t" \
    "s_cmp_eq_u32 s68, 0\n\t" \
    "s_cbranch_scc1 vv_F52_%=\n\t" \
    "s_sub_u32 s68, s68, 1\n\t" \
    "s_sleep 1\n\t" \
    "s_branch vv_Gag50_%=\n\t" \
    "vv_F52_%=:\n\t" \
    "v_mov_b32 v140, 5767424\n\t" \
    "v_mov_b32 v141, 2\n\t" \
    "global_store_dword v140, v141, s[36:37] sc1\n\t" \
    "s_waitcnt vmcnt(0)\n\t" \
    "s_branch vv_Gredo51_%=\n\t" \
    "vv_Gredo51_%=:\n\t" \
    "v_mov_b32 v72, 0\n\t" \
    "v_mov_b32 v73, 0\n\t" \
    "v_mov_b32 v74, 0\n\t" \
    "v_mov_b32 v75, 0\n\t" \
    "v_mov_b32 v76, 0\n\t" \
    "v_mov_b32 v77, 0\n\t" \
    "v_mov_b32 v78, 0\n\t" \
    "v_mov_b32 v79, 0\n\t" \
    "v_smfmac_f32_16x16x64_bf16 v[72:75], v[36:39], a[32:39], v10\n\t" \
    "v_smfmac_f32_16x16x64_bf16 v[72:75], v[40:43], a[40:47], v10\n\t" \
    "v_smfmac_f32_16x16x64_bf16 v[72:75], v[48:51], a[48:55], v10\n\t" \
    "v_smfmac_f32_16x16x64_bf16 v[72:75], v[52:55], a[56:63], v10\n\t" \
    "v_smfmac_f32_16x16x64_bf16 v[72:75], v[56:59], a[0:7], v10\n\t" \
    "v_smfmac_f32_16x16x64_bf16 v[72:75], v[60:63], a[8:15], v10\n\t" \
    "v_smfmac_f32_16x16x64_bf16 v[72:75], v[64:67], a[16:23], v10\n\t" \
    "v_smfmac_f32_16x16x64_bf16 v[72:75], v[68:71], a[24:31], v10\n\t" \
    "v_smfmac_f32_16x16x64_bf16 v[72:75], v[44:47], a[64:71], v10\n\t" \
    "v_smfmac_f32_16x16x64_bf16 v[76:79], v[36:39], a[104:111], v10\n\t" \
    "v_smfmac_f32_16x16x64_bf16 v[76:79], v[40:43], a[112:119], v10\n\t" \
    "v_smfmac_f32_16x16x64_bf16 v[76:79], v[48:51], a[120:127], v10\n\t" \
    "v_smfmac_f32_16x16x64_bf16 v[76:79], v[52:55], a[128:135], v10\n\t" \
    "v_smfmac_f32_16x16x64_bf16 v[76:79], v[56:59], a[72:79], v10\n\t" \
    "v_smfmac_f32_16x16x64_bf16 v[76:79], v[60:63], a[80:87], v10\n\t" \
    "v_smfmac_f32_16x16x64_bf16 v[76:79], v[64:67], a[88:95], v10\n\t" \
    "v_smfmac_f32_16x16x64_bf16 v[76:79], v[68:71], a[96:103], v10\n\t" \
    "v_smfmac_f32_16x16x64_bf16 v[76:79], v[44:47], a[136:143], v10\n\t" \
    "v_add_f32 v72, v72, v74\n\t" \
    "v_add_f32 v73, v73, v75\n\t" \
    "v_cvt_pk_bf16_f32 v80, v72, v73\n\t" \
    "ds_write_b32 v12, v80 offset:0\n\t" \
    "s_nop 3\n\t" \
    "v_pk_add_f32 v[76:77], v[76:77], v[78:79]\n\t" \
    "v_cvt_pk_bf16_f32 v81, v76, v77\n\t" \
    "ds_write_b32 v12, v81 offset:256\n\t" \
    "s_branch vv_Gback48_%=\n\t" \
    "vv_end40_%=:\n\t" \
    "s_endpgm\n\t" \
    ".p2align 6\n\t" \
    "s_nop 0\n\t" \
    "s_nop 0\n\t" \
    "s_nop 0\n\t" \
    "s_nop 0\n\t" \
    "s_nop 0\n\t" \
    "s_nop 0\n\t" \
    "vv_loop12_%=:\n\t" \
    "s_getpc_b64 s[30:31]\n\t" \
    "vv_Wp55_%=:\n\t" \
    "s_add_u32 s30, s30, vv_We56_%=-vv_Wp55_%=\n\t" \
    "s_addc_u32 s31, s31, 0\n\t" \
    "s_sub_u32 s70, s70, 96\n\t" \
    "vv_W54_%=:\n\t" \
    "s_memtime s[56:57]\n\t" \
    "s_waitcnt lgkmcnt(0)\n\t" \
    "s_sub_u32 s69, s70, s56\n\t" \
    "s_cmp_gt_i32 s69, 80\n\t" \
    "s_cbranch_scc1 vv_W54_%=\n\t" \
    "s_max_i32 s69, s69, 0\n\t" \
    "s_and_b32 s69, s69, -4\n\t" \
    "s_sub_u32 s30, s30, s69\n\t" \
    "s_subb_u32 s31, s31, 0\n\t" \
    "s_setpc_b64 s[30:31]\n\t" \
    "s_nop 0\n\t" \
    "s_nop 0\n\t" \
    "s_nop 0\n\t" \
    "s_nop 0\n\t" \
    "s_nop 0\n\t" \
    "s_nop 0\n\t" \
    "s_nop 0\n\t" \
    "s_nop 0\n\t" \
    "s_nop 0\n\t" \
    "s_nop 0\n\t" \
    "s_nop 0\n\t" \
    "s_nop 0\n\t" \
    "s_nop 0\n\t" \
    "s_nop 0\n\t" \
    "s_nop 0\n\t" \
    "s_nop 0\n\t" \
    "s_nop 0\n\t" \
    "s_nop 0\n\t" \
    "s_nop 0\n\t" \
    "s_nop 0\n\t" \
    "vv_We56_%=:\n\t" \
    "vv_G57_%=:\n\t" \
    "global_load_dwordx4 v[36:39], v1, s[46:47] sc1\n\t" \
    "global_load_dword v141, v2, s[46:47] sc1\n\t" \
    "global_load_dwordx4 v[40:43], v23, s[46:47] sc1\n\t" \
    "s_waitcnt vmcnt(2)\n\t" \
    "ds_write_b128 v3, v[36:39]\n\t" \
    "v_smfmac_f32_16x16x64_bf16 v[72:75], v[36:39], a[48:55], v10\n\t" \
    "s_waitcnt vmcnt(1)\n\t" \
    "ds_write_b32 v22, v141 offset:20224\n\t" \
    "s_waitcnt vmcnt(0)\n\t" \
    "ds_write_b128 v3, v[40:43] offset:1024\n\t" \
    "v_max3_u32 v140, v36, v40, v141\n\t" \
    "v_cmp_eq_u32 vcc, -1, v140\n\t" \
    "s_cbranch_vccnz vv_Gbad58_%=\n\t" \
    "vv_Gc59_%=:\n\t" \
    "s_waitcnt lgkmcnt(0)\n\t" \
    "s_barrier\n\t" \
    "ds_read_b128 v[48:51], v4\n\t" \
    "ds_read_b128 v[52:55], v5\n\t" \
    "v_smfmac_f32_16x16x64_bf16 v[76:79], v[36:39], a[120:127], v10\n\t" \
    "ds_read_b128 v[56:59], v6\n\t" \
    "ds_read_b128 v[60:63], v7\n\t" \
    "v_smfmac_f32_16x16x64_bf16 v[72:75], v[40:43], a[56:63], v10\n\t" \
    "ds_read_b128 v[64:67], v8\n\t" \
    "v_smfmac_f32_16x16x64_bf16 v[76:79], v[40:43], a[128:135], v10\n\t" \
    "ds_read_b128 v[68:71], v9\n\t" \
    "s_waitcnt lgkmcnt(5)\n\t" \
    "v_smfmac_f32_16x16x64_bf16 v[72:75], v[48:51], a[0:7], v10\n\t" \
    "ds_read_b128 v[44:47], v0 offset:19456\n\t" \
    "s_waitcnt lgkmcnt(5)\n\t" \
    "v_smfmac_f32_16x16x64_bf16 v[72:75], v[52:55], a[8:15], v10\n\t" \
    "s_waitcnt lgkmcnt(4)\n\t" \
    "v_smfmac_f32_16x16x64_bf16 v[72:75], v[56:59], a[16:23], v10\n\t" \
    "s_waitcnt lgkmcnt(3)\n\t" \
    "v_smfmac_f32_16x16x64_bf16 v[72:75], v[60:63], a[24:31], v10\n\t" \
    "s_waitcnt lgkmcnt(2)\n\t" \
    "v_smfmac_f32_16x16x64_bf16 v[72:75], v[64:67], a[32:39], v10\n\t" \
    "s_waitcnt lgkmcnt(1)\n\t" \
    "v_smfmac_f32_16x16x64_bf16 v[72:75], v[68:71], a[40:47], v10\n\t" \
    "s_waitcnt lgkmcnt(0)\n\t" \
    "v_smfmac_f32_16x16x64_bf16 v[72:75], v[44:47], a[64:71], v10\n\t" \
    "s_nop 2\n\t" \
    "v_smfmac_f32_16x16x64_bf16 v[76:79], v[48:51], a[72:79], v10\n\t" \
    "s_nop 2\n\t" \
    "v_smfmac_f32_16x16x64_bf16 v[76:79], v[52:55], a[80:87], v10\n\t" \
    "v_add_f32 v72, v72, v74\n\t" \
    "v_smfmac_f32_16x16x64_bf16 v[76:79], v[56:59], a[88:95], v10\n\t" \
    "v_add_f32 v73, v73, v75\n\t" \
    "v_smfmac_f32_16x16x64_bf16 v[76:79], v[60:63], a[96:103], v10\n\t" \
    "v_cvt_pk_bf16_f32 v80, v72, v73\n\t" \
    "v_smfmac_f32_16x16x64_bf16 v[76:79], v[64:67], a[104:111], v10\n\t" \
    "ds_write_b32 v12, v80 offset:0\n\t" \
    "v_smfmac_f32_16x16x64_bf16 v[76:79], v[68:71], a[112:119], v10\n\t" \
    "v_smfmac_f32_16x16x64_bf16 v[76:79], v[44:47], a[136:143], v10\n\t" \
    "s_nop 6\n\t" \
    "v_cmp_u_f32 vcc, v72, v73\n\t" \
    "v_pk_add_f32 v[76:77], v[76:77], v[78:79]\n\t" \
    "v_cvt_pk_bf16_f32 v81, v76, v77\n\t" \
    "ds_write_b32 v12, v81 offset:256\n\t" \
    "s_cbranch_vccnz vv_Gslow60_%=\n\t" \
    "vv_Gback61_%=:\n\t" \
    "s_waitcnt lgkmcnt(0)\n\t" \
    "s_barrier\n\t" \
    "ds_read_b64_tr_b16 v[84:85], v13 offset:0\n\t" \
    "v_smfmac_f32_16x16x64_bf16 v[100:103], v[36:39], a[0:7], v10\n\t" \
    "ds_read_b64_tr_b16 v[86:87], v13 offset:64\n\t" \
    "ds_read_b64_tr_b16 v[92:93], v13 offset:1152\n\t" \
    "v_smfmac_f32_16x16x64_bf16 v[100:103], v[36:39], a[0:7], v10\n\t" \
    "ds_read_b64_tr_b16 v[94:95], v13 offset:1216\n\t" \
    "ds_read_b64_tr_b16 v[88:89], v25 offset:0\n\t" \
    "v_smfmac_f32_16x16x64_bf16 v[100:103], v[36:39], a[0:7], v10\n\t" \
    "ds_read_b64_tr_b16 v[90:91], v25 offset:64\n\t" \
    "s_waitcnt lgkmcnt(4)\n\t" \
    "v_smfmac_f32_16x16x64_bf16 v[124:127], v[84:87], v[240:247], v19\n\t" \
    "ds_read_b64_tr_b16 v[96:97], v25 offset:1152\n\t" \
    "ds_read_b64_tr_b16 v[98:99], v25 offset:1216\n\t" \
    "s_waitcnt lgkmcnt(4)\n\t" \
    "v_smfmac_f32_16x16x64_bf16 v[124:127], v[92:95], v[248:255], v19\n\t" \
    "s_nop 2\n\t" \
    "v_smfmac_f32_16x16x64_bf16 v[108:111], v[84:87], a[144:151], v19\n\t" \
    "s_nop 2\n\t" \
    "v_smfmac_f32_16x16x64_bf16 v[108:111], v[92:95], a[160:167], v19\n\t" \
    "s_nop 1\n\t" \
    "v_permlane32_swap_b32 v124, v126\n\t" \
    "v_smfmac_f32_16x16x64_bf16 v[112:115], v[84:87], a[176:183], v19\n\t" \
    "s_nop 1\n\t" \
    "v_permlane32_swap_b32 v125, v127\n\t" \
    "v_smfmac_f32_16x16x64_bf16 v[112:115], v[92:95], a[192:199], v19\n\t" \
    "s_nop 0\n\t" \
    "v_add_f32 v124, v124, v126\n\t" \
    "s_waitcnt lgkmcnt(2)\n\t" \
    "v_smfmac_f32_16x16x64_bf16 v[108:111], v[88:91], a[152:159], v19\n\t" \
    "s_nop 1\n\t" \
    "v_add_f32 v125, v125, v127\n\t" \
    "v_smfmac_f32_16x16x64_bf16 v[112:115], v[88:91], a[184:191], v19\n\t" \
    "s_nop 0\n\t" \
    "v_cvt_pk_bf16_f32 v136, v124, v125\n\t" \
    "s_waitcnt lgkmcnt(0)\n\t" \
    "v_smfmac_f32_16x16x64_bf16 v[108:111], v[96:99], a[168:175], v19\n\t" \
    "s_nop 1\n\t" \
    "v_cvt_pk_bf16_f32 v137, v124, v125\n\t" \
    "v_smfmac_f32_16x16x64_bf16 v[112:115], v[96:99], a[200:207], v19\n\t" \
    "s_nop 1\n\t" \
    "v_permlane32_swap_b32 v136, v137\n\t" \
    "v_smfmac_f32_16x16x64_bf16 v[116:119], v[84:87], a[208:215], v19\n\t" \
    "s_mov_b32 exec_hi, 0\n\t" \
    "global_store_dwordx2 v16, v[136:137], s[38:39] sc1\n\t" \
    "s_mov_b32 exec_hi, -1\n\t" \
    "v_cvt_pk_bf16_f32 v128, v108, v109\n\t" \
    "v_smfmac_f32_16x16x64_bf16 v[116:119], v[88:91], a[216:223], v19\n\t" \
    "s_nop 1\n\t" \
    "v_cvt_pk_bf16_f32 v129, v110, v111\n\t" \
    "v_smfmac_f32_16x16x64_bf16 v[116:119], v[92:95], a[224:231], v19\n\t" \
    "s_nop 1\n\t" \
    "v_cvt_pk_bf16_f32 v130, v112, v113\n\t" \
    "v_smfmac_f32_16x16x64_bf16 v[116:119], v[96:99], a[232:239], v19\n\t" \
    "s_nop 1\n\t" \
    "v_cvt_pk_bf16_f32 v131, v114, v115\n\t" \
    "v_smfmac_f32_16x16x64_bf16 v[120:123], v[84:87], a[240:247], v19\n\t" \
    "s_nop 1\n\t" \
    "v_permlane16_swap_b32 v128, v130\n\t" \
    "v_smfmac_f32_16x16x64_bf16 v[120:123], v[88:91], a[248:255], v19\n\t" \
    "s_nop 1\n\t" \
    "v_permlane16_swap_b32 v129, v131\n\t" \
    "v_smfmac_f32_16x16x64_bf16 v[120:123], v[92:95], v[224:231], v19\n\t" \
    "s_nop 0\n\t" \
    "global_store_dwordx4 v14, v[128:131], s[38:39] sc1\n\t" \
    "v_cvt_pk_bf16_f32 v132, v116, v117\n\t" \
    "v_smfmac_f32_16x16x64_bf16 v[120:123], v[96:99], v[232:239], v19\n\t" \
    "s_nop 1\n\t" \
    "v_cvt_pk_bf16_f32 v133, v118, v119\n\t" \
    "s_nop 4\n\t" \
    "v_cvt_pk_bf16_f32 v134, v120, v121\n\t" \
    "v_cvt_pk_bf16_f32 v135, v122, v123\n\t" \
    "s_nop 0\n\t" \
    "v_permlane16_swap_b32 v132, v134\n\t" \
    "v_permlane16_swap_b32 v133, v135\n\t" \
    "global_store_dwordx4 v15, v[132:135], s[38:39] sc1\n\t" \
    "global_load_dword v140, v20, s[52:53] sc1\n\t" \
    "s_cmp_eq_u32 s65, 63\n\t" \
    "s_cbranch_scc1 vv_end53_%=\n\t" \
    "s_cmp_lg_u32 s65, 0\n\t" \
    "s_cbranch_scc1 vv_ng62_%=\n\t" \
    "v_mov_b32 v160, 5769216\n\t" \
    "v_mov_b32 v161, 1\n\t" \
    "s_mov_b64 exec, 1\n\t" \
    "global_atomic_add v162, v160, v161, s[36:37] sc0 sc1\n\t" \
    "s_mov_b64 exec, -1\n\t" \
    "v_mbcnt_lo_u32_b32 v160, -1, 0\n\t" \
    "v_and_b32 v161, 3, v160\n\t" \
    "v_lshlrev_b32 v161, 16, v161\n\t" \
    "v_bfe_u32 v163, v160, 2, 2\n\t" \
    "v_lshl_add_u32 v161, v163, 11, v161\n\t" \
    "v_bfe_u32 v163, v160, 4, 1\n\t" \
    "v_lshl_add_u32 v161, v163, 14, v161\n\t" \
    "v_add_u32 v161, s63, v161\n\t" \
    "s_waitcnt vmcnt(0)\n\t" \
    "v_readfirstlane_b32 s69, v162\n\t" \
    "s_nop 1\n\t" \
    "s_cmp_lg_u32 s69, 126\n\t" \
    "s_cbranch_scc1 vv_go63_%=\n\t" \
    "s_memrealtime s[56:57]\n\t" \
    "s_waitcnt lgkmcnt(0)\n\t" \
    "s_lshl_b32 s69, s56, 4\n\t" \
    "s_add_u32 s69, s69, 2800\n\t" \
    "s_add_u32 s70, s56, 24\n\t" \
    "s_lshr_b32 s70, s70, 4\n\t" \
    "v_lshrrev_b32 v163, 2, v160\n\t" \
    "v_add_u32 v163, s70, v163\n\t" \
    "v_and_b32 v163, 63, v163\n\t" \
    "v_lshlrev_b32 v163, 6, v163\n\t" \
    "v_and_b32 v162, 3, v160\n\t" \
    "v_lshl_add_u32 v163, v162, 12, v163\n\t" \
    "v_add_u32 v163, 5783552, v163\n\t" \
    "v_mov_b32 v160, s69\n\t" \
    "s_mov_b32 exec_hi, 0\n\t" \
    "global_store_dword v163, v160, s[36:37] sc1\n\t" \
    "global_store_dword v161, v160, s[36:37] sc1\n\t" \
    "s_mov_b32 exec_hi, -1\n\t" \
    "vv_go63_%=:\n\t" \
    "vv_ng62_%=:\n\t" \
    "s_waitcnt lgkmcnt(0)\n\t" \
    "s_mov_b32 s70, 116\n\t" \
    "vv_E67_%=:\n\t" \
    "s_memtime s[56:57]\n\t" \
    "s_memrealtime s[2:3]\n\t" \
    "s_nop 0\n\t" \
    "s_memrealtime s[4:5]\n\t" \
    "s_nop 0\n\t" \
    "s_memrealtime s[6:7]\n\t" \
    "s_nop 0\n\t" \
    "s_memrealtime s[8:9]\n\t" \
    "s_nop 0\n\t" \
    "s_memrealtime s[10:11]\n\t" \
    "s_nop 0\n\t" \
    "s_memrealtime s[12:13]\n\t" \
    "s_nop 0\n\t" \
    "s_memrealtime s[14:15]\n\t" \
    "s_nop 0\n\t" \
    "s_memrealtime s[16:17]\n\t" \
    "s_nop 0\n\t" \
    "s_memrealtime s[18:19]\n\t" \
    "s_nop 0\n\t" \
    "s_memrealtime s[20:21]\n\t" \
    "s_nop 0\n\t" \
    "s_memrealtime s[22:23]\n\t" \
    "s_nop 0\n\t" \
    "s_memrealtime s[24:25]\n\t" \
    "s_nop 0\n\t" \
    "s_memrealtime s[26:27]\n\t" \
    "s_nop 0\n\t" \
    "s_memrealtime s[28:29]\n\t" \
    "s_memtime s[54:55]\n\t" \
    "s_add_u32 s70, s70, 8\n\t" \
    "s_waitcnt lgkmcnt(0)\n\t" \
    "s_sub_u32 s69, s54, s56\n\t" \
    "s_cmp_gt_u32 s69, s70\n\t" \
    "s_cbranch_scc1 vv_E67_%=\n\t" \
    "s_add_u32 s2, s2, s4\n\t" \
    "s_add_u32 s6, s6, s8\n\t" \
    "s_add_u32 s10, s10, s12\n\t" \
    "s_add_u32 s14, s14, s16\n\t" \
    "s_add_u32 s18, s18, s20\n\t" \
    "s_add_u32 s22, s22, s24\n\t" \
    "s_add_u32 s2, s2, s6\n\t" \
    "s_add_u32 s10, s10, s14\n\t" \
    "s_add_u32 s18, s18, s22\n\t" \
    "s_add_u32 s2, s2, s26\n\t" \
    "s_add_u32 s10, s10, s18\n\t" \
    "s_add_u32 s2, s2, s10\n\t" \
    "s_mul_i32 s70, s28, 13\n\t" \
    "s_sub_u32 s70, s70, s2\n\t" \
    "s_min_u32 s70, s70, 52\n\t" \
    "s_mul_i32 s69, s69, s70\n\t" \
    "s_mul_hi_u32 s69, s69, 76695844\n\t" \
    "s_mov_b32 s75, s73\n\t" \
    "s_mov_b32 s76, s74\n\t" \
    "s_add_u32 s73, s56, s69\n\t" \
    "s_lshl_b32 s74, s28, 4\n\t" \
    "s_sub_u32 s69, s73, s75\n\t" \
    "s_sub_u32 s70, s74, s76\n\t" \
    "s_sub_u32 s71, s70, 1600\n\t" \
    "s_cmp_lt_u32 s71, 400000\n\t" \
    "s_cbranch_scc0 vv_K68_%=\n\t" \
    "v_cvt_f32_u32 v160, s69\n\t" \
    "v_cvt_f32_u32 v161, s70\n\t" \
    "v_rcp_f32 v162, v160\n\t" \
    "v_rcp_f32 v163, v161\n\t" \
    "v_mul_f32 v162, v162, v161\n\t" \
    "v_mul_f32 v163, v163, v160\n\t" \
    "v_mul_f32 v162, 0x4f800000, v162\n\t" \
    "v_mul_f32 v163, 0x4f000000, v163\n\t" \
    "v_cvt_u32_f32 v162, v162\n\t" \
    "v_cvt_u32_f32 v163, v163\n\t" \
    "v_readfirstlane_b32 s69, v162\n\t" \
    "v_readfirstlane_b32 s70, v163\n\t" \
    "s_nop 1\n\t" \
    "s_min_u32 s71, s65, 1\n\t" \
    "s_sub_u32 s69, s69, s78\n\t" \
    "s_ashr_i32 s69, s69, s71\n\t" \
    "s_add_u32 s78, s78, s69\n\t" \
    "s_sub_u32 s70, s70, s77\n\t" \
    "s_ashr_i32 s70, s70, s71\n\t" \
    "s_add_u32 s77, s77, s70\n\t" \
    "vv_K68_%=:\n\t" \
    "s_add_u32 s65, s65, 1\n\t" \
    "v_mov_b32 v72, 0\n\t" \
    "v_mov_b32 v73, 0\n\t" \
    "v_mov_b32 v74, 0\n\t" \
    "v_mov_b32 v75, 0\n\t" \
    "v_mov_b32 v76, 0\n\t" \
    "v_mov_b32 v77, 0\n\t" \
    "v_mov_b32 v78, 0\n\t" \
    "v_mov_b32 v79, 0\n\t" \
    "v_mov_b32 v108, 0\n\t" \
    "v_mov_b32 v109, 0\n\t" \
    "v_mov_b32 v110, 0\n\t" \
    "v_mov_b32 v111, 0\n\t" \
    "v_mov_b32 v112, 0\n\t" \
    "v_mov_b32 v113, 0\n\t" \
    "v_mov_b32 v114, 0\n\t" \
    "v_mov_b32 v115, 0\n\t" \
    "v_mov_b32 v116, 0\n\t" \
    "v_mov_b32 v117, 0\n\t" \
    "v_mov_b32 v118, 0\n\t" \
    "v_mov_b32 v119, 0\n\t" \
    "v_mov_b32 v120, 0\n\t" \
    "v_mov_b32 v121, 0\n\t" \
    "v_mov_b32 v122, 0\n\t" \
    "v_mov_b32 v123, 0\n\t" \
    "v_mov_b32 v124, 0\n\t" \
    "v_mov_b32 v125, 0\n\t" \
    "v_mov_b32 v126, 0\n\t" \
    "v_mov_b32 v127, 0\n\t" \
    "s_add_u32 s69, s65, 0\n\t" \
    "s_and_b32 s69, s69, 7\n\t" \
    "s_lshl_b32 s69, s69, 18\n\t" \
    "s_add_u32 s69, s69, s62\n\t" \
    "s_add_u32 s46, s36, s69\n\t" \
    "s_addc_u32 s47, s37, 0\n\t" \
    "s_add_u32 s70, s65, 0\n\t" \
    "s_mul_i32 s69, s70, 171\n\t" \
    "s_lshr_b32 s69, s69, 9\n\t" \
    "s_mul_i32 s70, s70, 589824\n\t" \
    "s_mul_i32 s71, s69, 327680\n\t" \
    "s_add_u32 s70, s70, s71\n\t" \
    "s_lshl1_add_u32 s69, s69, 4\n\t" \
    "s_bitcmp1_b64 s[44:45], s69\n\t" \
    "s_cselect_b32 s69, 8192, 0\n\t" \
    "s_add_u32 s69, s69, s70\n\t" \
    "s_add_u32 s69, s69, 123731968\n\t" \
    "s_add_u32 s38, s36, s69\n\t" \
    "s_addc_u32 s39, s37, 0\n\t" \
    "s_and_b32 s69, s69, 0xffe00000\n\t" \
    "s_add_u32 s69, s69, 1823744\n\t" \
    "v_mov_b32 v17, s69\n\t" \
    "global_load_dword v17, v17, s[36:37]\n\t" \
    "s_waitcnt vmcnt(0)\n\t" \
    "v_cmp_eq_u32 vcc, -1, v140\n\t" \
    "s_cbranch_vccz vv_Hok65_%=\n\t" \
    "vv_H64_%=:\n\t" \
    "global_load_dword v140, v20, s[52:53] sc1\n\t" \
    "s_waitcnt vmcnt(0)\n\t" \
    "v_cmp_eq_u32 vcc, -1, v140\n\t" \
    "s_cbranch_vccz vv_Hc66_%=\n\t" \
    "s_cmp_eq_u32 s68, 0\n\t" \
    "s_cbranch_scc1 vv_F69_%=\n\t" \
    "s_sub_u32 s68, s68, 1\n\t" \
    "s_sleep 2\n\t" \
    "s_branch vv_H64_%=\n\t" \
    "vv_F69_%=:\n\t" \
    "v_mov_b32 v141, 5767424\n\t" \
    "v_mov_b32 v142, 6\n\t" \
    "global_store_dword v141, v142, s[36:37] sc1\n\t" \
    "s_waitcnt vmcnt(0)\n\t" \
    "s_branch vv_Hc66_%=\n\t" \
    "vv_Hc66_%=:\n\t" \
    "s_memtime s[56:57]\n\t" \
    "s_waitcnt lgkmcnt(0)\n\t" \
    "s_sub_u32 s69, s56, s73\n\t" \
    "s_cmp_lt_u32 s69, 3000\n\t" \
    "s_cbranch_scc1 vv_St70_%=\n\t" \
    "s_waitcnt lgkmcnt(0)\n\t" \
    "s_mov_b32 s70, 116\n\t" \
    "vv_E71_%=:\n\t" \
    "s_memtime s[56:57]\n\t" \
    "s_memrealtime s[2:3]\n\t" \
    "s_nop 0\n\t" \
    "s_memrealtime s[4:5]\n\t" \
    "s_nop 0\n\t" \
    "s_memrealtime s[6:7]\n\t" \
    "s_nop 0\n\t" \
    "s_memrealtime s[8:9]\n\t" \
    "s_nop 0\n\t" \
    "s_memrealtime s[10:11]\n\t" \
    "s_nop 0\n\t" \
    "s_memrealtime s[12:13]\n\t" \
    "s_nop 0\n\t" \
    "s_memrealtime s[14:15]\n\t" \
    "s_nop 0\n\t" \
    "s_memrealtime s[16:17]\n\t" \
    "s_nop 0\n\t" \
    "s_memrealtime s[18:19]\n\t" \
    "s_nop 0\n\t" \
    "s_memrealtime s[20:21]\n\t" \
    "s_nop 0\n\t" \
    "s_memrealtime s[22:23]\n\t" \
    "s_nop 0\n\t" \
    "s_memrealtime s[24:25]\n\t" \
    "s_nop 0\n\t" \
    "s_memrealtime s[26:27]\n\t" \
    "s_nop 0\n\t" \
    "s_memrealtime s[28:29]\n\t" \
    "s_memtime s[54:55]\n\t" \
    "s_add_u32 s70, s70, 8\n\t" \
    "s_waitcnt lgkmcnt(0)\n\t" \
    "s_sub_u32 s69, s54, s56\n\t" \
    "s_cmp_gt_u32 s69, s70\n\t" \
    "s_cbranch_scc1 vv_E71_%=\n\t" \
    "s_add_u32 s2, s2, s4\n\t" \
    "s_add_u32 s6, s6, s8\n\t" \
    "s_add_u32 s10, s10, s12\n\t" \
    "s_add_u32 s14, s14, s16\n\t" \
    "s_add_u32 s18, s18, s20\n\t" \
    "s_add_u32 s22, s22, s24\n\t" \
    "s_add_u32 s2, s2, s6\n\t" \
    "s_add_u32 s10, s10, s14\n\t" \
    "s_add_u32 s18, s18, s22\n\t" \
    "s_add_u32 s2, s2, s26\n\t" \
    "s_add_u32 s10, s10, s18\n\t" \
    "s_add_u32 s2, s2, s10\n\t" \
    "s_mul_i32 s70, s28, 13\n\t" \
    "s_sub_u32 s70, s70, s2\n\t" \
    "s_min_u32 s70, s70, 52\n\t" \
    "s_mul_i32 s69, s69, s70\n\t" \
    "s_mul_hi_u32 s69, s69, 76695844\n\t" \
    "s_mov_b32 s75, s73\n\t" \
    "s_mov_b32 s76, s74\n\t" \
    "s_add_u32 s73, s56, s69\n\t" \
    "s_lshl_b32 s74, s28, 4\n\t" \
    "vv_St70_%=:\n\t" \
    "vv_Hok65_%=:\n\t" \
    "v_max_u32_dpp v140, v140, v140 row_ror:1 row_mask:0xf bank_mask:0xf\n\t" \
    "s_nop 1\n\t" \
    "v_max_u32_dpp v140, v140, v140 row_ror:2 row_mask:0xf bank_mask:0xf\n\t" \
    "s_nop 1\n\t" \
    "v_max_u32_dpp v140, v140, v140 row_ror:4 row_mask:0xf bank_mask:0xf\n\t" \
    "s_nop 1\n\t" \
    "v_max_u32_dpp v140, v140, v140 row_ror:8 row_mask:0xf bank_mask:0xf\n\t" \
    "s_nop 0\n\t" \
    "v_readlane_b32 s69, v140, 0\n\t" \
    "v_readlane_b32 s71, v140, 16\n\t" \
    "s_nop 1\n\t" \
    "s_max_u32 s81, s69, s71\n\t" \
    "s_mul_hi_u32 s69, s85, s78\n\t" \
    "s_add_u32 s79, s81, s69\n\t" \
    "s_add_u32 s79, s79, s87\n\t" \
    "s_cmp_ge_u32 s65, 53\n\t" \
    "s_cselect_b32 s69, 32, 0\n\t" \
    "s_sub_u32 s79, s79, s69\n\t" \
    "s_cmp_lt_u32 s65, 10\n\t" \
    "s_cselect_b32 s69, 48, 0\n\t" \
    "s_add_u32 s79, s79, s69\n\t" \
    "s_add_u32 s80, s79, s88\n\t" \
    "s_sub_u32 s70, s79, s74\n\t" \
    "s_max_i32 s70, s70, 0\n\t" \
    "s_min_i32 s70, s70, 16000\n\t" \
    "s_lshl_b32 s70, s70, 1\n\t" \
    "s_mul_hi_u32 s70, s70, s77\n\t" \
    "s_add_u32 s70, s70, s73\n\t" \
    "v_mov_b32 v160, s70\n\t" \
    "ds_write_b32 v11, v160 offset:20480\n\t" \
    "s_and_b32 s69, s65, 7\n\t" \
    "s_lshl_b32 s69, s69, 18\n\t" \
    "s_add_u32 s69, s69, s63\n\t" \
    "s_add_u32 s52, s36, s69\n\t" \
    "s_addc_u32 s53, s37, 0\n\t" \
    "s_waitcnt lgkmcnt(0)\n\t" \
    "s_barrier\n\t" \
    "s_branch vv_loop12_%=\n\t" \
    "vv_Gbad58_%=:\n\t" \
    "s_waitcnt lgkmcnt(0)\n\t" \
    "s_nop 7\n\t" \
    "v_mov_b32 v72, 0\n\t" \
    "v_mov_b32 v73, 0\n\t" \
    "v_mov_b32 v74, 0\n\t" \
    "v_mov_b32 v75, 0\n\t" \
    "v_mov_b32 v76, 0\n\t" \
    "v_mov_b32 v77, 0\n\t" \
    "v_mov_b32 v78, 0\n\t" \
    "v_mov_b32 v79, 0\n\t" \
    "s_cmp_eq_u32 s68, 0\n\t" \
    "s_cbranch_scc1 vv_F72_%=\n\t" \
    "s_sub_u32 s68, s68, 1\n\t" \
    "s_branch vv_G57_%=\n\t" \
    "vv_F72_%=:\n\t" \
    "v_mov_b32 v140, 5767424\n\t" \
    "v_mov_b32 v141, 1\n\t" \
    "global_store_dword v140, v141, s[36:37] sc1\n\t" \
    "s_waitcnt vmcnt(0)\n\t" \
    "s_branch vv_Gc59_%=\n\t" \
    "vv_Gslow60_%=:\n\t" \
    "v_max_u32 v140, v36, v37\n\t" \
    "v_max3_u32 v140, v140, v38, v39\n\t" \
    "v_max3_u32 v140, v140, v40, v41\n\t" \
    "v_max3_u32 v140, v140, v42, v43\n\t" \
    "v_max3_u32 v140, v140, v44, v45\n\t" \
    "v_max3_u32 v140, v140, v46, v47\n\t" \
    "v_max3_u32 v140, v140, v48, v49\n\t" \
    "v_max3_u32 v140, v140, v50, v51\n\t" \
    "v_max3_u32 v140, v140, v52, v53\n\t" \
    "v_max3_u32 v140, v140, v54, v55\n\t" \
    "v_max3_u32 v140, v140, v56, v57\n\t" \
    "v_max3_u32 v140, v140, v58, v59\n\t" \
    "v_max3_u32 v140, v140, v60, v61\n\t" \
    "v_max3_u32 v140, v140, v62, v63\n\t" \
    "v_max3_u32 v140, v140, v64, v65\n\t" \
    "v_max3_u32 v140, v140, v66, v67\n\t" \
    "v_max3_u32 v140, v140, v68, v69\n\t" \
    "v_max3_u32 v140, v140, v70, v71\n\t" \
    "v_cmp_eq_u32 vcc, -1, v140\n\t" \
    "s_cbranch_vccz vv_Gback61_%=\n\t" \
    "s_waitcnt lgkmcnt(0)\n\t" \
    "v_sub_u32 v140, v1, v3\n\t" \
    "s_nop 0\n\t" \
    "v_readfirstlane_b32 s69, v140\n\t" \
    "v_add_u32 v143, 20480, v0\n\t" \
    "s_nop 7\n\t" \
    "s_add_u32 s30, s46, s69\n\t" \
    "s_addc_u32 s31, s47, 0\n\t" \
    "vv_Gag73_%=:\n\t" \
    "global_load_dwordx4 v[36:39], v1, s[46:47] sc1\n\t" \
    "global_load_dwordx4 v[40:43], v23, s[46:47] sc1\n\t" \
    "global_load_dwordx4 v[44:47], v143, s[30:31] sc1\n\t" \
    "global_load_dwordx4 v[48:51], v4, s[30:31] sc1\n\t" \
    "global_load_dwordx4 v[52:55], v5, s[30:31] sc1\n\t" \
    "global_load_dwordx4 v[56:59], v6, s[30:31] sc1\n\t" \
    "global_load_dwordx4 v[60:63], v7, s[30:31] sc1\n\t" \
    "global_load_dwordx4 v[64:67], v8, s[30:31] sc1\n\t" \
    "global_load_dwordx4 v[68:71], v9, s[30:31] sc1\n\t" \
    "s_waitcnt vmcnt(0)\n\t" \
    "v_max_u32 v140, v36, v37\n\t" \
    "v_max3_u32 v140, v140, v38, v39\n\t" \
    "v_max3_u32 v140, v140, v40, v41\n\t" \
    "v_max3_u32 v140, v140, v42, v43\n\t" \
    "v_max3_u32 v140, v140, v44, v45\n\t" \
    "v_max3_u32 v140, v140, v46, v47\n\t" \
    "v_max3_u32 v140, v140, v48, v49\n\t" \
    "v_max3_u32 v140, v140, v50, v51\n\t" \
    "v_max3_u32 v140, v140, v52, v53\n\t" \
    "v_max3_u32 v140, v140, v54, v55\n\t" \
    "v_max3_u32 v140, v140, v56, v57\n\t" \
    "v_max3_u32 v140, v140, v58, v59\n\t" \
    "v_max3_u32 v140, v140, v60, v61\n\t" \
    "v_max3_u32 v140, v140, v62, v63\n\t" \
    "v_max3_u32 v140, v140, v64, v65\n\t" \
    "v_max3_u32 v140, v140, v66, v67\n\t" \
    "v_max3_u32 v140, v140, v68, v69\n\t" \
    "v_max3_u32 v140, v140, v70, v71\n\t" \
    "v_cmp_eq_u32 vcc, -1, v140\n\t" \
    "s_cbranch_vccz vv_Gredo74_%=\n\t" \
    "s_cmp_eq_u32 s68, 0\n\t" \
    "s_cbranch_scc1 vv_F75_%=\n\t" \
    "s_sub_u32 s68, s68, 1\n\t" \
    "s_sleep 1\n\t" \
    "s_branch vv_Gag73_%=\n\t" \
    "vv_F75_%=:\n\t" \
    "v_mov_b32 v140, 5767424\n\t" \
    "v_mov_b32 v141, 2\n\t" \
    "global_store_dword v140, v141, s[36:37] sc1\n\t" \
    "s_waitcnt vmcnt(0)\n\t" \
    "s_branch vv_Gredo74_%=\n\t" \
    "vv_Gredo74_%=:\n\t" \
    "v_mov_b32 v72, 0\n\t" \
    "v_mov_b32 v73, 0\n\t" \
    "v_mov_b32 v74, 0\n\t" \
    "v_mov_b32 v75, 0\n\t" \
    "v_mov_b32 v76, 0\n\t" \
    "v_mov_b32 v77, 0\n\t" \
    "v_mov_b32 v78, 0\n\t" \
    "v_mov_b32 v79, 0\n\t" \
    "v_smfmac_f32_16x16x64_bf16 v[72:75], v[36:39], a[48:55], v10\n\t" \
    "v_smfmac_f32_16x16x64_bf16 v[72:75], v[40:43], a[56:63], v10\n\t" \
    "v_smfmac_f32_16x16x64_bf16 v[72:75], v[48:51], a[0:7], v10\n\t" \
    "v_smfmac_f32_16x16x64_bf16 v[72:75], v[52:55], a[8:15], v10\n\t" \
    "v_smfmac_f32_16x16x64_bf16 v[72:75], v[56:59], a[16:23], v10\n\t" \
    "v_smfmac_f32_16x16x64_bf16 v[72:75], v[60:63], a[24:31], v10\n\t" \
    "v_smfmac_f32_16x16x64_bf16 v[72:75], v[64:67], a[32:39], v10\n\t" \
    "v_smfmac_f32_16x16x64_bf16 v[72:75], v[68:71], a[40:47], v10\n\t" \
    "v_smfmac_f32_16x16x64_bf16 v[72:75], v[44:47], a[64:71], v10\n\t" \
    "v_smfmac_f32_16x16x64_bf16 v[76:79], v[36:39], a[120:127], v10\n\t" \
    "v_smfmac_f32_16x16x64_bf16 v[76:79], v[40:43], a[128:135], v10\n\t" \
    "v_smfmac_f32_16x16x64_bf16 v[76:79], v[48:51], a[72:79], v10\n\t" \
    "v_smfmac_f32_16x16x64_bf16 v[76:79], v[52:55], a[80:87], v10\n\t" \
    "v_smfmac_f32_16x16x64_bf16 v[76:79], v[56:59], a[88:95], v10\n\t" \
    "v_smfmac_f32_16x16x64_bf16 v[76:79], v[60:63], a[96:103], v10\n\t" \
    "v_smfmac_f32_16x16x64_bf16 v[76:79], v[64:67], a[104:111], v10\n\t" \
    "v_smfmac_f32_16x16x64_bf16 v[76:79], v[68:71], a[112:119], v10\n\t" \
    "v_smfmac_f32_16x16x64_bf16 v[76:79], v[44:47], a[136:143], v10\n\t" \
    "v_add_f32 v72, v72, v74\n\t" \
    "v_add_f32 v73, v73, v75\n\t" \
    "v_cvt_pk_bf16_f32 v80, v72, v73\n\t" \
    "ds_write_b32 v12, v80 offset:0\n\t" \
    "s_nop 3\n\t" \
    "v_pk_add_f32 v[76:77], v[76:77], v[78:79]\n\t" \
    "v_cvt_pk_bf16_f32 v81, v76, v77\n\t" \
    "ds_write_b32 v12, v81 offset:256\n\t" \
    "s_branch vv_Gback61_%=\n\t" \
    "vv_end53_%=:\n\t" \
    "s_endpgm\n\t" \
    "vv_last_%=:\n\t" \
    ""
#define ASM_R \
    "s_mov_b64 s[36:37], %0\n\t" \
    "s_mov_b64 s[42:43], %1\n\t" \
    "s_mov_b64 s[44:45], %2\n\t" \
    "s_mov_b32 s66, %3\n\t" \
    "s_mov_b32 s67, %4\n\t" \
    "s_mov_b64 s[50:51], %5\n\t" \
    "s_mov_b32 s82, %7\n\t" \
    "s_load_dwordx8 s[84:91], %6, 0\n\t" \
    "v_mbcnt_lo_u32_b32 v223, -1, 0\n\t" \
    "v_mbcnt_hi_u32_b32 v223, -1, v223\n\t" \
    "s_getpc_b64 s[46:47]\n\t" \
    "rr_pc_%=:\n\t" \
    "s_lshl_b32 s69, s66, 13\n\t" \
    "v_lshl_add_u32 v222, v223, 7, s69\n\t" \
    "v_min_u32 v222, rr_last_%=-rr_pc_%=-4, v222\n\t" \
    "global_load_dword v222, v222, s[46:47]\n\t" \
    "v_lshlrev_b32 v223, 2, v223\n\t" \
    "v_add_u32 v223, s82, v223\n\t" \
    "ds_read_b32 v0, v223 offset:0\n\t" \
    "ds_read_b32 v1, v223 offset:256\n\t" \
    "ds_read_b32 v2, v223 offset:512\n\t" \
    "ds_read_b32 v3, v223 offset:768\n\t" \
    "ds_read_b32 v4, v223 offset:1024\n\t" \
    "ds_read_b32 v5, v223 offset:1280\n\t" \
    "ds_read_b32 v6, v223 offset:1536\n\t" \
    "ds_read_b32 v7, v223 offset:1792\n\t" \
    "ds_read_b32 v8, v223 offset:2048\n\t" \
    "ds_read_b32 v9, v223 offset:2304\n\t" \
    "ds_read_b32 v10, v223 offset:2560\n\t" \
    "ds_read_b32 v11, v223 offset:2816\n\t" \
    "s_waitcnt lgkmcnt(0)\n\t" \
    "ds_read_b32 v12, v223 offset:3072\n\t" \
    "ds_read_b32 v13, v223 offset:3328\n\t" \
    "ds_read_b32 v14, v223 offset:3584\n\t" \
    "ds_read_b32 v15, v223 offset:3840\n\t" \
    "ds_read_b32 v16, v223 offset:4096\n\t" \
    "ds_read_b32 v17, v223 offset:4352\n\t" \
    "ds_read_b32 v18, v223 offset:4608\n\t" \
    "ds_read_b32 v19, v223 offset:4864\n\t" \
    "ds_read_b32 v20, v223 offset:5120\n\t" \
    "ds_read_b32 v21, v223 offset:5376\n\t" \
    "ds_read_b32 v22, v223 offset:5632\n\t" \
    "ds_read_b32 v23, v223 offset:5888\n\t" \
    "s_waitcnt lgkmcnt(0)\n\t" \
    "ds_read_b32 v24, v223 offset:6144\n\t" \
    "ds_read_b32 v25, v223 offset:6400\n\t" \
    "ds_read_b32 v26, v223 offset:6656\n\t" \
    "ds_read_b32 v27, v223 offset:6912\n\t" \
    "ds_read_b32 v28, v223 offset:7168\n\t" \
    "ds_read_b32 v29, v223 offset:7424\n\t" \
    "ds_read_b32 v30, v223 offset:7680\n\t" \
    "ds_read_b32 v31, v223 offset:7936\n\t" \
    "ds_read_b32 v32, v223 offset:8192\n\t" \
    "ds_read_b32 v33, v223 offset:8448\n\t" \
    "ds_read_b32 v34, v223 offset:8704\n\t" \
    "ds_read_b32 v35, v223 offset:8960\n\t" \
    "s_waitcnt lgkmcnt(0)\n\t" \
    "ds_read_b32 v36, v223 offset:9216\n\t" \
    "ds_read_b32 v37, v223 offset:9472\n\t" \
    "ds_read_b32 v38, v223 offset:9728\n\t" \
    "ds_read_b32 v39, v223 offset:9984\n\t" \
    "ds_read_b32 v40, v223 offset:10240\n\t" \
    "ds_read_b32 v41, v223 offset:10496\n\t" \
    "ds_read_b32 v42, v223 offset:10752\n\t" \
    "ds_read_b32 v43, v223 offset:11008\n\t" \
    "ds_read_b32 v44, v223 offset:11264\n\t" \
    "ds_read_b32 v45, v223 offset:11520\n\t" \
    "ds_read_b32 v184, v223 offset:11776\n\t" \
    "ds_read_b32 v185, v223 offset:12032\n\t" \
    "s_waitcnt lgkmcnt(0)\n\t" \
    "ds_read_b32 v186, v223 offset:12288\n\t" \
    "ds_read_b32 v187, v223 offset:12544\n\t" \
    "ds_read_b32 v188, v223 offset:12800\n\t" \
    "s_waitcnt lgkmcnt(0)\n\t" \
    "s_mov_b32 s68, 262144\n\t" \
    "s_mov_b32 s72, 0\n\t" \
    "s_mov_b32 s83, -1\n\t" \
    "s_mov_b32 s65, 0\n\t" \
    "v_xor_b32 v180, 65536, v2\n\t" \
    "v_xor_b32 v181, 131072, v2\n\t" \
    "v_xor_b32 v182, 196608, v2\n\t" \
    "v_add_u32 v189, 4096, v7\n\t" \
    "v_add_u32 v190, 16384, v7\n\t" \
    "v_add_u32 v191, 20480, v7\n\t" \
    "s_mul_i32 s69, s67, 57\n\t" \
    "s_lshr_b32 s69, s69, 9\n\t" \
    "s_mul_i32 s69, s69, 9\n\t" \
    "s_sub_u32 s69, s67, s69\n\t" \
    "s_cmp_lt_u32 s69, 4\n\t" \
    "s_cselect_b32 s70, 21, 16\n\t" \
    "s_cmp_eq_u32 s69, 8\n\t" \
    "s_cselect_b32 s70, 26, s70\n\t" \
    "s_lshr_b32 s97, s91, s70\n\t" \
    "s_and_b32 s97, s97, 31\n\t" \
    "s_min_u32 s97, s97, 0\n\t" \
    "s_lshl_b32 s97, s97, 4\n\t" \
    "s_mov_b32 s77, 3019898880\n\t" \
    "s_mov_b32 s78, 3054198966\n\t" \
    "s_waitcnt lgkmcnt(0)\n\t" \
    "s_mov_b32 s70, 116\n\t" \
    "rr_E1_%=:\n\t" \
    "s_memtime s[56:57]\n\t" \
    "s_memrealtime s[2:3]\n\t" \
    "s_nop 0\n\t" \
    "s_memrealtime s[4:5]\n\t" \
    "s_nop 0\n\t" \
    "s_memrealtime s[6:7]\n\t" \
    "s_nop 0\n\t" \
    "s_memrealtime s[8:9]\n\t" \
    "s_nop 0\n\t" \
    "s_memrealtime s[10:11]\n\t" \
    "s_nop 0\n\t" \
    "s_memrealtime s[12:13]\n\t" \
    "s_nop 0\n\t" \
    "s_memrealtime s[14:15]\n\t" \
    "s_nop 0\n\t" \
    "s_memrealtime s[16:17]\n\t" \
    "s_nop 0\n\t" \
    "s_memrealtime s[18:19]\n\t" \
    "s_nop 0\n\t" \
    "s_memrealtime s[20:21]\n\t" \
    "s_nop 0\n\t" \
    "s_memrealtime s[22:23]\n\t" \
    "s_nop 0\n\t" \
    "s_memrealtime s[24:25]\n\t" \
    "s_nop 0\n\t" \
    "s_memrealtime s[26:27]\n\t" \
    "s_nop 0\n\t" \
    "s_memrealtime s[28:29]\n\t" \
    "s_memtime s[54:55]\n\t" \
    "s_add_u32 s70, s70, 8\n\t" \
    "s_waitcnt lgkmcnt(0)\n\t" \
    "s_sub_u32 s69, s54, s56\n\t" \
    "s_cmp_gt_u32 s69, s70\n\t" \
    "s_cbranch_scc1 rr_E1_%=\n\t" \
    "s_add_u32 s2, s2, s4\n\t" \
    "s_add_u32 s6, s6, s8\n\t" \
    "s_add_u32 s10, s10, s12\n\t" \
    "s_add_u32 s14, s14, s16\n\t" \
    "s_add_u32 s18, s18, s20\n\t" \
    "s_add_u32 s22, s22, s24\n\t" \
    "s_add_u32 s2, s2, s6\n\t" \
    "s_add_u32 s10, s10, s14\n\t" \
    "s_add_u32 s18, s18, s22\n\t" \
    "s_add_u32 s2, s2, s26\n\t" \
    "s_add_u32 s10, s10, s18\n\t" \
    "s_add_u32 s2, s2, s10\n\t" \
    "s_mul_i32 s70, s28, 13\n\t" \
    "s_sub_u32 s70, s70, s2\n\t" \
    "s_min_u32 s70, s70, 52\n\t" \
    "s_mul_i32 s69, s69, s70\n\t" \
    "s_mul_hi_u32 s69, s69, 76695844\n\t" \
    "s_mov_b32 s75, s73\n\t" \
    "s_mov_b32 s76, s74\n\t" \
    "s_add_u32 s73, s56, s69\n\t" \
    "s_lshl_b32 s74, s28, 4\n\t" \
    "v_mov_b32 v172, 0xd800\n\t" \
    "v_mov_b32 v173, 0x20d800\n\t" \
    "global_load_dword v172, v172, s[36:37]\n\t" \
    "global_load_dword v173, v173, s[36:37]\n\t" \
    "s_mov_b32 s62, 0\n\t" \
    "s_mov_b32 s63, 0x200000\n\t" \
    "s_mov_b64 s[44:45], 0\n\t" \
    "v_mov_b32 v170, 0\n\t" \
    "s_mov_b32 s65, s66\n\t" \
    "s_lshl_b32 s58, s67, 14\n\t" \
    "s_sub_u32 s80, s74, 32000\n\t" \
    "s_mov_b32 s64, 1\n\t" \
    "s_mov_b32 s65, 5\n\t" \
    "s_mov_b32 s83, 99\n\t" \
    "s_add_u32 s70, s65, 59\n\t" \
    "s_mul_i32 s69, s70, 171\n\t" \
    "s_lshr_b32 s69, s69, 9\n\t" \
    "s_mul_i32 s70, s70, 589824\n\t" \
    "s_mul_i32 s71, s69, 327680\n\t" \
    "s_add_u32 s70, s70, s71\n\t" \
    "s_lshl1_add_u32 s69, s69, 4\n\t" \
    "s_bitcmp1_b64 s[44:45], s69\n\t" \
    "s_cselect_b32 s69, 8192, 0\n\t" \
    "s_add_u32 s69, s69, s70\n\t" \
    "s_add_u32 s69, s69, 123731968\n\t" \
    "s_add_u32 s69, s69, s58\n\t" \
    "s_add_u32 s40, s36, s69\n\t" \
    "s_addc_u32 s41, s37, 0\n\t" \
    "v_mov_b32 v192, 0\n\t" \
    "s_mov_b64 exec, 1\n\t" \
    "s_branch rr_act3_%=\n\t" \
    "rr_g214_%=:\n\t" \
    "s_mov_b64 exec, -1\n\t" \
    "s_waitcnt vmcnt(0)\n\t" \
    "v_mbcnt_lo_u32_b32 v164, -1, 0\n\t" \
    "v_mbcnt_hi_u32_b32 v164, -1, v164\n\t" \
    "v_lshlrev_b32 v164, 6, v164\n\t" \
    "v_add_u32 v164, 6225920, v164\n\t" \
    "v_mov_b32 v165, 6234112\n\t" \
    "global_load_dword v165, v165, s[36:37] sc1\n\t" \
    "global_load_dwordx2 v[166:167], v164, s[36:37] sc1\n\t" \
    "global_load_dword v168, v164, s[36:37] offset:2048 sc1\n\t" \
    "s_waitcnt vmcnt(0)\n\t" \
    "rr_C15_%=:\n\t" \
    "v_cmp_ne_u32 s[54:55], v166, v168\n\t" \
    "v_sub_u32 v166, v166, v165\n\t" \
    "v_cmp_ne_u32 vcc, 1, v166\n\t" \
    "s_nop 1\n\t" \
    "s_or_b64 vcc, vcc, s[54:55]\n\t" \
    "s_cbranch_scc0 rr_Cok16_%=\n\t" \
    "s_cmp_eq_u32 s68, 0\n\t" \
    "s_cbranch_scc1 rr_F18_%=\n\t" \
    "s_sub_u32 s68, s68, 1\n\t" \
    "s_sleep 6\n\t" \
    "s_branch rr_Cag17_%=\n\t" \
    "rr_F18_%=:\n\t" \
    "v_mov_b32 v168, 5767424\n\t" \
    "v_mov_b32 v169, 4\n\t" \
    "global_store_dword v168, v169, s[36:37] sc1\n\t" \
    "s_waitcnt vmcnt(0)\n\t" \
    "s_branch rr_Cok16_%=\n\t" \
    "rr_Cag17_%=:\n\t" \
    "global_load_dwordx2 v[166:167], v164, s[36:37] sc1\n\t" \
    "global_load_dword v168, v164, s[36:37] offset:2048 sc1\n\t" \
    "s_waitcnt vmcnt(0)\n\t" \
    "s_branch rr_C15_%=\n\t" \
    "rr_Cok16_%=:\n\t" \
    "v_readlane_b32 s69, v167, 2\n\t" \
    "v_readlane_b32 s70, v167, 3\n\t" \
    "s_nop 1\n\t" \
    "s_cmp_lt_u32 s70, s69\n\t" \
    "s_cselect_b32 s69, 0, 8192\n\t" \
    "s_add_u32 s100, s36, s69\n\t" \
    "s_addc_u32 s101, s37, 0\n\t" \
    "s_cmp_lg_u32 s66, 0\n\t" \
    "s_cbranch_scc1 rr_Ca19_%=\n\t" \
    "v_mov_b32 v164, 5768192\n\t" \
    "s_mov_b64 exec, 1\n\t" \
    "global_atomic_add v164, v166, s[36:37] sc1\n\t" \
    "s_mov_b64 exec, -1\n\t" \
    "rr_Ca19_%=:\n\t" \
    "v_mov_b32_dpp v164, v167 quad_perm:[1,0,3,2] row_mask:0xf bank_mask:0xf\n\t" \
    "v_cmp_lt_u32 s[44:45], v164, v167\n\t" \
    "s_nop 1\n\t" \
    "s_bitcmp1_b32 s44, 0\n\t" \
    "s_cselect_b32 s62, 8192, 0\n\t" \
    "s_bitcmp1_b32 s44, 2\n\t" \
    "s_cselect_b32 s63, 8192, 0\n\t" \
    "s_or_b32 s63, s63, 0x200000\n\t" \
    "v_min_u32 v164, v164, v167\n\t" \
    "v_cmp_lt_u32 vcc, 660, v164\n\t" \
    "s_nop 1\n\t" \
    "s_bcnt1_i32_b64 s69, vcc\n\t" \
    "s_cmp_gt_u32 s69, 16\n\t" \
    "s_cselect_b32 s69, 1, 0\n\t" \
    "s_mul_i32 s70, s69, 160\n\t" \
    "s_add_u32 s86, s86, s70\n\t" \
    "s_mul_i32 s70, s69, 536\n\t" \
    "s_add_u32 s87, s87, s70\n\t" \
    "s_mul_i32 s70, s69, 256\n\t" \
    "s_add_u32 s88, s88, s70\n\t" \
    "v_mov_b32 v172, -1\n\t" \
    "v_mov_b32 v173, -1\n\t" \
    "v_mov_b32 v174, -1\n\t" \
    "v_mov_b32 v175, -1\n\t" \
    "v_mov_b32 v176, 32000\n\t" \
    "s_sub_u32 s69, s63, s62\n\t" \
    "s_sub_u32 s69, s69, 0x200000\n\t" \
    "v_bfe_u32 v164, v187, 21, 1\n\t" \
    "v_mad_i32_i24 v187, v164, s69, v187\n\t" \
    "v_bfe_u32 v164, v188, 21, 1\n\t" \
    "v_mad_i32_i24 v188, v164, s69, v188\n\t" \
    "s_mov_b32 s64, 0\n\t" \
    "s_mov_b32 s65, s66\n\t" \
    "s_mov_b32 s83, 1\n\t" \
    "s_mov_b32 s77, 3154116608\n\t" \
    "rr_pf9_%=:\n\t" \
    "s_cmp_gt_u32 s65, 7\n\t" \
    "s_cselect_b32 s83, 99, s83\n\t" \
    "rr_WF20_%=:\n\t" \
    "s_cmp_ge_i32 s83, s65\n\t" \
    "s_cbranch_scc1 rr_WFok21_%=\n\t" \
    "global_load_dword v164, v6, s[100:101] sc1\n\t" \
    "s_waitcnt vmcnt(0)\n\t" \
    "v_min_i32_dpp v164, v164, v164 row_ror:1 row_mask:0xf bank_mask:0xf\n\t" \
    "s_nop 1\n\t" \
    "v_min_i32_dpp v164, v164, v164 row_ror:2 row_mask:0xf bank_mask:0xf\n\t" \
    "s_nop 1\n\t" \
    "v_min_i32_dpp v164, v164, v164 row_ror:4 row_mask:0xf bank_mask:0xf\n\t" \
    "s_nop 1\n\t" \
    "v_min_i32_dpp v164, v164, v164 row_ror:8 row_mask:0xf bank_mask:0xf\n\t" \
    "s_nop 0\n\t" \
    "v_readlane_b32 s69, v164, 0\n\t" \
    "v_readlane_b32 s70, v164, 16\n\t" \
    "s_nop 1\n\t" \
    "s_min_i32 s83, s69, s70\n\t" \
    "s_cmp_ge_i32 s83, s65\n\t" \
    "s_cbranch_scc1 rr_WFok21_%=\n\t" \
    "s_cmp_eq_u32 s68, 0\n\t" \
    "s_cbranch_scc1 rr_F22_%=\n\t" \
    "s_sub_u32 s68, s68, 1\n\t" \
    "s_sleep 2\n\t" \
    "s_branch rr_WF20_%=\n\t" \
    "rr_F22_%=:\n\t" \
    "v_mov_b32 v164, 5767424\n\t" \
    "v_mov_b32 v165, 10\n\t" \
    "global_store_dword v164, v165, s[36:37] sc1\n\t" \
    "s_waitcnt vmcnt(0)\n\t" \
    "s_branch rr_WFok21_%=\n\t" \
    "rr_WFok21_%=:\n\t" \
    "s_mul_i32 s70, s67, 7\n\t" \
    "s_add_u32 s69, s65, 0\n\t" \
    "s_xor_b32 s69, s69, s70\n\t" \
    "s_and_b32 s69, s69, 63\n\t" \
    "s_lshl_b32 s69, s69, 15\n\t" \
    "s_mul_i32 s70, s67, 2097152\n\t" \
    "s_add_u32 s69, s69, s70\n\t" \
    "s_add_u32 s69, s69, 48234496\n\t" \
    "s_add_u32 s38, s36, s69\n\t" \
    "s_addc_u32 s39, s37, 0\n\t" \
    "s_add_u32 s70, s65, 0\n\t" \
    "s_mul_i32 s69, s70, 171\n\t" \
    "s_lshr_b32 s69, s69, 9\n\t" \
    "s_mul_i32 s70, s70, 589824\n\t" \
    "s_mul_i32 s71, s69, 327680\n\t" \
    "s_add_u32 s70, s70, s71\n\t" \
    "s_lshl1_add_u32 s69, s69, 4\n\t" \
    "s_bitcmp1_b64 s[44:45], s69\n\t" \
    "s_cselect_b32 s69, 8192, 0\n\t" \
    "s_add_u32 s69, s69, s70\n\t" \
    "s_add_u32 s69, s69, 123731968\n\t" \
    "s_add_u32 s69, s69, s58\n\t" \
    "s_add_u32 s40, s36, s69\n\t" \
    "s_addc_u32 s41, s37, 0\n\t" \
    "s_and_b32 s69, s69, 0xffe00000\n\t" \
    "s_add_u32 s69, s69, 1823744\n\t" \
    "v_mov_b32 v168, s69\n\t" \
    "global_load_dword v168, v168, s[36:37]\n\t" \
    "v_mov_b32 v164, s83\n\t" \
    "s_add_u32 s69, s65, 2\n\t" \
    "s_cmp_ge_i32 s83, s69\n\t" \
    "s_cbranch_scc1 rr_nq23_%=\n\t" \
    "global_load_dword v164, v6, s[100:101] sc1\n\t" \
    "rr_nq23_%=:\n\t" \
    "s_cmp_gt_u32 s65, 61\n\t" \
    "s_cbranch_scc1 rr_idle2_%=\n\t" \
    "s_add_u32 s69, s65, 2\n\t" \
    "s_and_b32 s69, s69, 7\n\t" \
    "s_lshl_b32 s69, s69, 18\n\t" \
    "s_add_u32 s69, s69, s62\n\t" \
    "s_add_u32 s46, s36, s69\n\t" \
    "s_addc_u32 s47, s37, 0\n\t" \
    "global_store_dwordx4 v184, v[172:175], s[46:47] sc1\n\t" \
    "global_store_dwordx4 v185, v[172:175], s[46:47] sc1\n\t" \
    "global_store_dwordx4 v186, v[172:175], s[46:47] sc1\n\t" \
    "global_store_dwordx4 v187, v[172:175], s[46:47] sc1\n\t" \
    "global_store_dwordx4 v188, v[172:175], s[46:47] sc1\n\t" \
    "rr_idle2_%=:\n\t" \
    "v_mov_b32 v200, 0\n\t" \
    "s_cmp_lg_u32 s67, 35\n\t" \
    "s_cbranch_scc1 rr_cke24_%=\n\t" \
    "s_cmp_lt_u32 s65, 6\n\t" \
    "s_cbranch_scc1 rr_cke24_%=\n\t" \
    "s_lshl_b32 s69, s65, 7\n\t" \
    "s_add_u32 s69, s69, 5775360\n\t" \
    "v_mov_b32 v201, s69\n\t" \
    "global_load_dword v200, v201, s[36:37] sc1\n\t" \
    "rr_cke24_%=:\n\t" \
    "s_cmp_gt_u32 s65, 62\n\t" \
    "s_cbranch_scc1 rr_n225_%=\n\t" \
    "s_cmp_lt_u32 s65, 2\n\t" \
    "s_cbranch_scc1 rr_nw226_%=\n\t" \
    "s_mul_hi_u32 s69, s85, s78\n\t" \
    "s_add_u32 s79, s81, s69\n\t" \
    "s_add_u32 s79, s79, s87\n\t" \
    "s_cmp_ge_u32 s65, 54\n\t" \
    "s_cselect_b32 s69, 32, 0\n\t" \
    "s_sub_u32 s79, s79, s69\n\t" \
    "s_cmp_lt_u32 s65, 11\n\t" \
    "s_cselect_b32 s69, 48, 0\n\t" \
    "s_add_u32 s79, s79, s69\n\t" \
    "s_add_u32 s80, s79, s88\n\t" \
    "s_cmp_gt_u32 s65, 8\n\t" \
    "s_cselect_b32 s71, 240, 64\n\t" \
    "s_add_u32 s71, s71, s79\n\t" \
    "s_sub_u32 s70, s71, s74\n\t" \
    "s_max_i32 s70, s70, 0\n\t" \
    "s_min_i32 s70, s70, 16000\n\t" \
    "s_lshl_b32 s70, s70, 1\n\t" \
    "s_mul_hi_u32 s70, s70, s77\n\t" \
    "s_add_u32 s70, s70, s73\n\t" \
    "rr_W27_%=:\n\t" \
    "s_memtime s[56:57]\n\t" \
    "s_waitcnt lgkmcnt(0)\n\t" \
    "s_sub_u32 s69, s70, s56\n\t" \
    "s_cmp_gt_i32 s69, 24\n\t" \
    "s_cbranch_scc1 rr_W27_%=\n\t" \
    "s_branch rr_g113_%=\n\t" \
    "rr_nw226_%=:\n\t" \
    "s_mov_b32 s96, 0xaaaaaaaa\n\t" \
    "v_mov_b32 v166, -1\n\t" \
    "v_readfirstlane_b32 s71, v7\n\t" \
    "s_nop 1\n\t" \
    "s_lshr_b32 s71, s71, 4\n\t" \
    "s_add_u32 s71, s71, 5783552\n\t" \
    "rr_g012_%=:\n\t" \
    "s_memrealtime s[54:55]\n\t" \
    "s_waitcnt lgkmcnt(0)\n\t" \
    "s_bfe_u32 s69, s54, 0x60004\n\t" \
    "s_lshl_b32 s69, s69, 6\n\t" \
    "s_add_u32 s69, s69, s71\n\t" \
    "v_mov_b32 v165, s69\n\t" \
    "global_load_dword v169, v165, s[36:37] sc1\n\t" \
    "s_and_b32 s69, s68, 63\n\t" \
    "s_cbranch_scc1 rr_gf28_%=\n\t" \
    "global_load_dword v166, v6, s[100:101] sc1\n\t" \
    "rr_gf28_%=:\n\t" \
    "s_waitcnt vmcnt(0)\n\t" \
    "v_cmp_ne_u32 s[30:31], -1, v169\n\t" \
    "v_cmp_le_i32 vcc, s65, v166\n\t" \
    "s_nop 1\n\t" \
    "s_lshr_b32 s69, vcc_lo, 1\n\t" \
    "s_and_b32 s69, s69, vcc_lo\n\t" \
    "s_or_b32 s69, s69, s30\n\t" \
    "s_andn2_b32 s69, s69, s96\n\t" \
    "s_cbranch_scc0 rr_gs29_%=\n\t" \
    "s_or_b32 s96, s96, s69\n\t" \
    "s_cmp_eq_u32 s66, 0\n\t" \
    "s_cbranch_scc1 rr_w030_%=\n\t" \
    "s_sleep 8\n\t" \
    "rr_w030_%=:\n\t" \
    "s_bitcmp0_b32 s69, 0\n\t" \
    "s_cbranch_scc1 rr_gk31_%=\n\t" \
    "global_load_dwordx4 a[0:3], v26, s[38:39] offset:0 sc1\n\t" \
    "global_load_dwordx4 a[4:7], v26, s[38:39] offset:1024 sc1\n\t" \
    "rr_gk31_%=:\n\t" \
    "s_bitcmp0_b32 s69, 2\n\t" \
    "s_cbranch_scc1 rr_gk32_%=\n\t" \
    "global_load_dwordx4 a[8:11], v27, s[38:39] offset:0 sc1\n\t" \
    "global_load_dwordx4 a[12:15], v27, s[38:39] offset:1024 sc1\n\t" \
    "rr_gk32_%=:\n\t" \
    "s_bitcmp0_b32 s69, 4\n\t" \
    "s_cbranch_scc1 rr_gk33_%=\n\t" \
    "global_load_dwordx4 a[16:19], v28, s[38:39] offset:0 sc1\n\t" \
    "global_load_dwordx4 a[20:23], v28, s[38:39] offset:1024 sc1\n\t" \
    "rr_gk33_%=:\n\t" \
    "s_bitcmp0_b32 s69, 6\n\t" \
    "s_cbranch_scc1 rr_gk34_%=\n\t" \
    "global_load_dwordx4 a[24:27], v29, s[38:39] offset:0 sc1\n\t" \
    "global_load_dwordx4 a[28:31], v29, s[38:39] offset:1024 sc1\n\t" \
    "rr_gk34_%=:\n\t" \
    "s_bitcmp0_b32 s69, 8\n\t" \
    "s_cbranch_scc1 rr_gk35_%=\n\t" \
    "global_load_dwordx4 a[32:35], v30, s[38:39] offset:0 sc1\n\t" \
    "global_load_dwordx4 a[36:39], v30, s[38:39] offset:1024 sc1\n\t" \
    "rr_gk35_%=:\n\t" \
    "s_bitcmp0_b32 s69, 10\n\t" \
    "s_cbranch_scc1 rr_gk36_%=\n\t" \
    "global_load_dwordx4 a[40:43], v31, s[38:39] offset:0 sc1\n\t" \
    "global_load_dwordx4 a[44:47], v31, s[38:39] offset:1024 sc1\n\t" \
    "rr_gk36_%=:\n\t" \
    "s_bitcmp0_b32 s69, 12\n\t" \
    "s_cbranch_scc1 rr_gk37_%=\n\t" \
    "global_load_dwordx4 a[48:51], v32, s[38:39] offset:0 sc1\n\t" \
    "global_load_dwordx4 a[52:55], v32, s[38:39] offset:1024 sc1\n\t" \
    "rr_gk37_%=:\n\t" \
    "s_bitcmp0_b32 s69, 14\n\t" \
    "s_cbranch_scc1 rr_gk38_%=\n\t" \
    "global_load_dwordx4 a[56:59], v33, s[38:39] offset:0 sc1\n\t" \
    "global_load_dwordx4 a[60:63], v33, s[38:39] offset:1024 sc1\n\t" \
    "rr_gk38_%=:\n\t" \
    "s_bitcmp0_b32 s69, 16\n\t" \
    "s_cbranch_scc1 rr_gk39_%=\n\t" \
    "global_load_dwordx4 a[64:67], v34, s[38:39] offset:0 sc1\n\t" \
    "global_load_dwordx4 a[68:71], v34, s[38:39] offset:1024 sc1\n\t" \
    "rr_gk39_%=:\n\t" \
    "s_bitcmp0_b32 s69, 18\n\t" \
    "s_cbranch_scc1 rr_gk40_%=\n\t" \
    "global_load_dwordx4 a[72:75], v35, s[38:39] offset:0 sc1\n\t" \
    "global_load_dwordx4 a[76:79], v35, s[38:39] offset:1024 sc1\n\t" \
    "rr_gk40_%=:\n\t" \
    "s_bitcmp0_b32 s69, 20\n\t" \
    "s_cbranch_scc1 rr_gk41_%=\n\t" \
    "global_load_dwordx4 a[80:83], v36, s[38:39] offset:0 sc1\n\t" \
    "global_load_dwordx4 a[84:87], v36, s[38:39] offset:1024 sc1\n\t" \
    "rr_gk41_%=:\n\t" \
    "s_bitcmp0_b32 s69, 22\n\t" \
    "s_cbranch_scc1 rr_gk42_%=\n\t" \
    "global_load_dwordx4 a[88:91], v37, s[38:39] offset:0 sc1\n\t" \
    "global_load_dwordx4 a[92:95], v37, s[38:39] offset:1024 sc1\n\t" \
    "rr_gk42_%=:\n\t" \
    "s_bitcmp0_b32 s69, 24\n\t" \
    "s_cbranch_scc1 rr_gk43_%=\n\t" \
    "global_load_dwordx4 a[96:99], v38, s[38:39] offset:0 sc1\n\t" \
    "global_load_dwordx4 a[100:103], v38, s[38:39] offset:1024 sc1\n\t" \
    "rr_gk43_%=:\n\t" \
    "s_bitcmp0_b32 s69, 26\n\t" \
    "s_cbranch_scc1 rr_gk44_%=\n\t" \
    "global_load_dwordx4 a[104:107], v39, s[38:39] offset:0 sc1\n\t" \
    "global_load_dwordx4 a[108:111], v39, s[38:39] offset:1024 sc1\n\t" \
    "rr_gk44_%=:\n\t" \
    "s_bitcmp0_b32 s69, 28\n\t" \
    "s_cbranch_scc1 rr_gk45_%=\n\t" \
    "global_load_dwordx4 a[112:115], v40, s[38:39] offset:0 sc1\n\t" \
    "global_load_dwordx4 a[116:119], v40, s[38:39] offset:1024 sc1\n\t" \
    "rr_gk45_%=:\n\t" \
    "s_bitcmp0_b32 s69, 30\n\t" \
    "s_cbranch_scc1 rr_gk46_%=\n\t" \
    "global_load_dwordx4 a[120:123], v41, s[38:39] offset:0 sc1\n\t" \
    "global_load_dwordx4 a[124:127], v41, s[38:39] offset:1024 sc1\n\t" \
    "rr_gk46_%=:\n\t" \
    "s_cmp_eq_u32 s96, -1\n\t" \
    "s_cbranch_scc1 rr_n225_%=\n\t" \
    "rr_gs29_%=:\n\t" \
    "s_cmp_eq_u32 s68, 0\n\t" \
    "s_cbranch_scc1 rr_F47_%=\n\t" \
    "s_sub_u32 s68, s68, 1\n\t" \
    "s_sleep 1\n\t" \
    "s_branch rr_g012_%=\n\t" \
    "rr_F47_%=:\n\t" \
    "v_mov_b32 v167, 5767424\n\t" \
    "v_mov_b32 v168, 16\n\t" \
    "global_store_dword v167, v168, s[36:37] sc1\n\t" \
    "s_waitcnt vmcnt(0)\n\t" \
    "s_branch rr_n225_%=\n\t" \
    "rr_g113_%=:\n\t" \
    "global_load_dwordx4 a[0:3], v26, s[38:39] offset:0 sc1\n\t" \
    "global_load_dwordx4 a[4:7], v26, s[38:39] offset:1024 sc1\n\t" \
    "global_load_dwordx4 a[8:11], v27, s[38:39] offset:0 sc1\n\t" \
    "global_load_dwordx4 a[12:15], v27, s[38:39] offset:1024 sc1\n\t" \
    "global_load_dwordx4 a[16:19], v28, s[38:39] offset:0 sc1\n\t" \
    "global_load_dwordx4 a[20:23], v28, s[38:39] offset:1024 sc1\n\t" \
    "global_load_dwordx4 a[24:27], v29, s[38:39] offset:0 sc1\n\t" \
    "global_load_dwordx4 a[28:31], v29, s[38:39] offset:1024 sc1\n\t" \
    "global_load_dwordx4 a[32:35], v30, s[38:39] offset:0 sc1\n\t" \
    "global_load_dwordx4 a[36:39], v30, s[38:39] offset:1024 sc1\n\t" \
    "global_load_dwordx4 a[40:43], v31, s[38:39] offset:0 sc1\n\t" \
    "global_load_dwordx4 a[44:47], v31, s[38:39] offset:1024 sc1\n\t" \
    "global_load_dwordx4 a[48:51], v32, s[38:39] offset:0 sc1\n\t" \
    "global_load_dwordx4 a[52:55], v32, s[38:39] offset:1024 sc1\n\t" \
    "global_load_dwordx4 a[56:59], v33, s[38:39] offset:0 sc1\n\t" \
    "global_load_dwordx4 a[60:63], v33, s[38:39] offset:1024 sc1\n\t" \
    "global_load_dwordx4 a[64:67], v34, s[38:39] offset:0 sc1\n\t" \
    "global_load_dwordx4 a[68:71], v34, s[38:39] offset:1024 sc1\n\t" \
    "global_load_dwordx4 a[72:75], v35, s[38:39] offset:0 sc1\n\t" \
    "global_load_dwordx4 a[76:79], v35, s[38:39] offset:1024 sc1\n\t" \
    "global_load_dwordx4 a[80:83], v36, s[38:39] offset:0 sc1\n\t" \
    "global_load_dwordx4 a[84:87], v36, s[38:39] offset:1024 sc1\n\t" \
    "global_load_dwordx4 a[88:91], v37, s[38:39] offset:0 sc1\n\t" \
    "global_load_dwordx4 a[92:95], v37, s[38:39] offset:1024 sc1\n\t" \
    "global_load_dwordx4 a[96:99], v38, s[38:39] offset:0 sc1\n\t" \
    "global_load_dwordx4 a[100:103], v38, s[38:39] offset:1024 sc1\n\t" \
    "global_load_dwordx4 a[104:107], v39, s[38:39] offset:0 sc1\n\t" \
    "global_load_dwordx4 a[108:111], v39, s[38:39] offset:1024 sc1\n\t" \
    "global_load_dwordx4 a[112:115], v40, s[38:39] offset:0 sc1\n\t" \
    "global_load_dwordx4 a[116:119], v40, s[38:39] offset:1024 sc1\n\t" \
    "global_load_dwordx4 a[120:123], v41, s[38:39] offset:0 sc1\n\t" \
    "global_load_dwordx4 a[124:127], v41, s[38:39] offset:1024 sc1\n\t" \
    "rr_n225_%=:\n\t" \
    "s_cmp_eq_u32 s65, 0\n\t" \
    "s_cbranch_scc1 rr_act3_%=\n\t" \
    "s_waitcnt vmcnt(0)\n\t" \
    "rr_ckl49_%=:\n\t" \
    "v_cmp_eq_u32 vcc, -1, v200\n\t" \
    "s_cbranch_vccz rr_ck48_%=\n\t" \
    "global_load_dword v200, v201, s[36:37] sc1\n\t" \
    "s_waitcnt vmcnt(0)\n\t" \
    "s_cmp_eq_u32 s68, 0\n\t" \
    "s_cbranch_scc1 rr_F50_%=\n\t" \
    "s_sub_u32 s68, s68, 1\n\t" \
    "s_sleep 1\n\t" \
    "s_branch rr_ckl49_%=\n\t" \
    "rr_F50_%=:\n\t" \
    "v_mov_b32 v166, 5767424\n\t" \
    "v_mov_b32 v167, 15\n\t" \
    "global_store_dword v166, v167, s[36:37] sc1\n\t" \
    "s_waitcnt vmcnt(0)\n\t" \
    "s_branch rr_ck48_%=\n\t" \
    "rr_ck48_%=:\n\t" \
    "v_min_i32_dpp v164, v164, v164 row_ror:1 row_mask:0xf bank_mask:0xf\n\t" \
    "s_nop 1\n\t" \
    "v_min_i32_dpp v164, v164, v164 row_ror:2 row_mask:0xf bank_mask:0xf\n\t" \
    "s_nop 1\n\t" \
    "v_min_i32_dpp v164, v164, v164 row_ror:4 row_mask:0xf bank_mask:0xf\n\t" \
    "s_nop 1\n\t" \
    "v_min_i32_dpp v164, v164, v164 row_ror:8 row_mask:0xf bank_mask:0xf\n\t" \
    "s_nop 0\n\t" \
    "v_readlane_b32 s69, v164, 0\n\t" \
    "v_readlane_b32 s70, v164, 16\n\t" \
    "s_nop 1\n\t" \
    "s_min_i32 s69, s69, s70\n\t" \
    "s_max_i32 s83, s83, s69\n\t" \
    "s_cmp_eq_u32 s65, 0\n\t" \
    "s_cbranch_scc1 rr_act3_%=\n\t" \
    "s_barrier\n\t" \
    "s_cmp_gt_u32 s65, 62\n\t" \
    "s_cbranch_scc1 rr_end4_%=\n\t" \
    "ds_read_b32 v164, v9\n\t" \
    "s_waitcnt lgkmcnt(0)\n\t" \
    "v_readfirstlane_b32 s81, v164\n\t" \
    "s_nop 1\n\t" \
    "s_cmp_lg_u32 s65, 1\n\t" \
    "s_cbranch_scc1 rr_n151_%=\n\t" \
    "s_waitcnt lgkmcnt(0)\n\t" \
    "s_mov_b32 s70, 116\n\t" \
    "rr_E52_%=:\n\t" \
    "s_memtime s[56:57]\n\t" \
    "s_memrealtime s[2:3]\n\t" \
    "s_nop 0\n\t" \
    "s_memrealtime s[4:5]\n\t" \
    "s_nop 0\n\t" \
    "s_memrealtime s[6:7]\n\t" \
    "s_nop 0\n\t" \
    "s_memrealtime s[8:9]\n\t" \
    "s_nop 0\n\t" \
    "s_memrealtime s[10:11]\n\t" \
    "s_nop 0\n\t" \
    "s_memrealtime s[12:13]\n\t" \
    "s_nop 0\n\t" \
    "s_memrealtime s[14:15]\n\t" \
    "s_nop 0\n\t" \
    "s_memrealtime s[16:17]\n\t" \
    "s_nop 0\n\t" \
    "s_memrealtime s[18:19]\n\t" \
    "s_nop 0\n\t" \
    "s_memrealtime s[20:21]\n\t" \
    "s_nop 0\n\t" \
    "s_memrealtime s[22:23]\n\t" \
    "s_nop 0\n\t" \
    "s_memrealtime s[24:25]\n\t" \
    "s_nop 0\n\t" \
    "s_memrealtime s[26:27]\n\t" \
    "s_nop 0\n\t" \
    "s_memrealtime s[28:29]\n\t" \
    "s_memtime s[54:55]\n\t" \
    "s_add_u32 s70, s70, 8\n\t" \
    "s_waitcnt lgkmcnt(0)\n\t" \
    "s_sub_u32 s69, s54, s56\n\t" \
    "s_cmp_gt_u32 s69, s70\n\t" \
    "s_cbranch_scc1 rr_E52_%=\n\t" \
    "s_add_u32 s2, s2, s4\n\t" \
    "s_add_u32 s6, s6, s8\n\t" \
    "s_add_u32 s10, s10, s12\n\t" \
    "s_add_u32 s14, s14, s16\n\t" \
    "s_add_u32 s18, s18, s20\n\t" \
    "s_add_u32 s22, s22, s24\n\t" \
    "s_add_u32 s2, s2, s6\n\t" \
    "s_add_u32 s10, s10, s14\n\t" \
    "s_add_u32 s18, s18, s22\n\t" \
    "s_add_u32 s2, s2, s26\n\t" \
    "s_add_u32 s10, s10, s18\n\t" \
    "s_add_u32 s2, s2, s10\n\t" \
    "s_mul_i32 s70, s28, 13\n\t" \
    "s_sub_u32 s70, s70, s2\n\t" \
    "s_min_u32 s70, s70, 52\n\t" \
    "s_mul_i32 s69, s69, s70\n\t" \
    "s_mul_hi_u32 s69, s69, 76695844\n\t" \
    "s_mov_b32 s75, s73\n\t" \
    "s_mov_b32 s76, s74\n\t" \
    "s_add_u32 s73, s56, s69\n\t" \
    "s_lshl_b32 s74, s28, 4\n\t" \
    "rr_n151_%=:\n\t" \
    "s_mul_hi_u32 s69, s85, s78\n\t" \
    "s_add_u32 s79, s81, s69\n\t" \
    "s_add_u32 s79, s79, s87\n\t" \
    "s_cmp_ge_u32 s65, 53\n\t" \
    "s_cselect_b32 s69, 32, 0\n\t" \
    "s_sub_u32 s79, s79, s69\n\t" \
    "s_cmp_lt_u32 s65, 10\n\t" \
    "s_cselect_b32 s69, 48, 0\n\t" \
    "s_add_u32 s79, s79, s69\n\t" \
    "s_add_u32 s80, s79, s88\n\t" \
    "s_sub_u32 s70, s79, s74\n\t" \
    "s_max_i32 s70, s70, 0\n\t" \
    "s_min_i32 s70, s70, 16000\n\t" \
    "s_lshl_b32 s70, s70, 1\n\t" \
    "s_mul_hi_u32 s70, s70, s77\n\t" \
    "s_add_u32 s70, s70, s73\n\t" \
    "rr_W53_%=:\n\t" \
    "s_memtime s[56:57]\n\t" \
    "s_waitcnt lgkmcnt(0)\n\t" \
    "s_sub_u32 s69, s70, s56\n\t" \
    "s_cmp_gt_i32 s69, 24\n\t" \
    "s_cbranch_scc1 rr_W53_%=\n\t" \
    "rr_act3_%=:\n\t" \
    "s_add_u32 s69, s65, 0\n\t" \
    "s_and_b32 s69, s69, 7\n\t" \
    "s_lshl_b32 s69, s69, 18\n\t" \
    "s_add_u32 s69, s69, s62\n\t" \
    "s_add_u32 s46, s36, s69\n\t" \
    "s_addc_u32 s47, s37, 0\n\t" \
    "s_add_u32 s69, s65, 1\n\t" \
    "s_and_b32 s69, s69, 7\n\t" \
    "s_lshl_b32 s69, s69, 18\n\t" \
    "s_add_u32 s69, s69, s62\n\t" \
    "s_add_u32 s48, s36, s69\n\t" \
    "s_addc_u32 s49, s37, 0\n\t" \
    "s_and_b32 s69, s65, 7\n\t" \
    "s_lshl_b32 s69, s69, 18\n\t" \
    "s_add_u32 s69, s69, s63\n\t" \
    "s_add_u32 s52, s36, s69\n\t" \
    "s_addc_u32 s53, s37, 0\n\t" \
    "s_cmp_eq_u32 s65, 0\n\t" \
    "s_cbranch_scc1 rr_nf54_%=\n\t" \
    "s_add_u32 s69, s65, 2\n\t" \
    "s_cmp_ge_i32 s83, s69\n\t" \
    "s_cbranch_scc1 rr_nf54_%=\n\t" \
    "global_load_dword v164, v6, s[100:101] sc1\n\t" \
    "s_waitcnt vmcnt(0)\n\t" \
    "v_min_i32_dpp v164, v164, v164 row_ror:1 row_mask:0xf bank_mask:0xf\n\t" \
    "s_nop 1\n\t" \
    "v_min_i32_dpp v164, v164, v164 row_ror:2 row_mask:0xf bank_mask:0xf\n\t" \
    "s_nop 1\n\t" \
    "v_min_i32_dpp v164, v164, v164 row_ror:4 row_mask:0xf bank_mask:0xf\n\t" \
    "s_nop 1\n\t" \
    "v_min_i32_dpp v164, v164, v164 row_ror:8 row_mask:0xf bank_mask:0xf\n\t" \
    "s_nop 0\n\t" \
    "v_readlane_b32 s69, v164, 0\n\t" \
    "v_readlane_b32 s70, v164, 16\n\t" \
    "s_nop 1\n\t" \
    "s_min_i32 s83, s69, s70\n\t" \
    "rr_nf54_%=:\n\t" \
    "s_waitcnt lgkmcnt(0)\n\t" \
    "s_mov_b32 s70, 116\n\t" \
    "rr_E55_%=:\n\t" \
    "s_memtime s[56:57]\n\t" \
    "s_memrealtime s[2:3]\n\t" \
    "s_nop 0\n\t" \
    "s_memrealtime s[4:5]\n\t" \
    "s_nop 0\n\t" \
    "s_memrealtime s[6:7]\n\t" \
    "s_nop 0\n\t" \
    "s_memrealtime s[8:9]\n\t" \
    "s_nop 0\n\t" \
    "s_memrealtime s[10:11]\n\t" \
    "s_nop 0\n\t" \
    "s_memrealtime s[12:13]\n\t" \
    "s_nop 0\n\t" \
    "s_memrealtime s[14:15]\n\t" \
    "s_nop 0\n\t" \
    "s_memrealtime s[16:17]\n\t" \
    "s_nop 0\n\t" \
    "s_memrealtime s[18:19]\n\t" \
    "s_nop 0\n\t" \
    "s_memrealtime s[20:21]\n\t" \
    "s_nop 0\n\t" \
    "s_memrealtime s[22:23]\n\t" \
    "s_nop 0\n\t" \
    "s_memrealtime s[24:25]\n\t" \
    "s_nop 0\n\t" \
    "s_memrealtime s[26:27]\n\t" \
    "s_nop 0\n\t" \
    "s_memrealtime s[28:29]\n\t" \
    "s_memtime s[54:55]\n\t" \
    "s_add_u32 s70, s70, 8\n\t" \
    "s_waitcnt lgkmcnt(0)\n\t" \
    "s_sub_u32 s69, s54, s56\n\t" \
    "s_cmp_gt_u32 s69, s70\n\t" \
    "s_cbranch_scc1 rr_E55_%=\n\t" \
    "s_add_u32 s2, s2, s4\n\t" \
    "s_add_u32 s6, s6, s8\n\t" \
    "s_add_u32 s10, s10, s12\n\t" \
    "s_add_u32 s14, s14, s16\n\t" \
    "s_add_u32 s18, s18, s20\n\t" \
    "s_add_u32 s22, s22, s24\n\t" \
    "s_add_u32 s2, s2, s6\n\t" \
    "s_add_u32 s10, s10, s14\n\t" \
    "s_add_u32 s18, s18, s22\n\t" \
    "s_add_u32 s2, s2, s26\n\t" \
    "s_add_u32 s10, s10, s18\n\t" \
    "s_add_u32 s2, s2, s10\n\t" \
    "s_mul_i32 s70, s28, 13\n\t" \
    "s_sub_u32 s70, s70, s2\n\t" \
    "s_min_u32 s70, s70, 52\n\t" \
    "s_mul_i32 s69, s69, s70\n\t" \
    "s_mul_hi_u32 s69, s69, 76695844\n\t" \
    "s_mov_b32 s75, s73\n\t" \
    "s_mov_b32 s76, s74\n\t" \
    "s_add_u32 s73, s56, s69\n\t" \
    "s_lshl_b32 s74, s28, 4\n\t" \
    "s_sub_u32 s69, s73, s75\n\t" \
    "s_sub_u32 s70, s74, s76\n\t" \
    "s_sub_u32 s71, s70, 1600\n\t" \
    "s_cmp_lt_u32 s71, 400000\n\t" \
    "s_cbranch_scc0 rr_K56_%=\n\t" \
    "v_cvt_f32_u32 v164, s69\n\t" \
    "v_cvt_f32_u32 v165, s70\n\t" \
    "v_rcp_f32 v166, v164\n\t" \
    "v_rcp_f32 v167, v165\n\t" \
    "v_mul_f32 v166, v166, v165\n\t" \
    "v_mul_f32 v167, v167, v164\n\t" \
    "v_mul_f32 v166, 0x4f800000, v166\n\t" \
    "v_mul_f32 v167, 0x4f000000, v167\n\t" \
    "v_cvt_u32_f32 v166, v166\n\t" \
    "v_cvt_u32_f32 v167, v167\n\t" \
    "v_readfirstlane_b32 s69, v166\n\t" \
    "v_readfirstlane_b32 s70, v167\n\t" \
    "s_nop 1\n\t" \
    "s_lshr_b32 s71, s65, 1\n\t" \
    "s_min_u32 s71, s71, 1\n\t" \
    "s_sub_u32 s69, s69, s78\n\t" \
    "s_ashr_i32 s69, s69, s71\n\t" \
    "s_add_u32 s78, s78, s69\n\t" \
    "s_sub_u32 s70, s70, s77\n\t" \
    "s_ashr_i32 s70, s70, s71\n\t" \
    "s_add_u32 s77, s77, s70\n\t" \
    "rr_K56_%=:\n\t" \
    "s_mov_b32 s91, s64\n\t" \
    "s_cmp_lg_u32 s64, 0\n\t" \
    "s_cselect_b32 s77, 0, s77\n\t" \
    "v_mov_b32 v160, 0\n\t" \
    "v_mov_b32 v161, 0\n\t" \
    "v_mov_b32 v162, 0\n\t" \
    "v_mov_b32 v163, 0\n\t" \
    ".p2align 6\n\t" \
    "s_nop 0\n\t" \
    "s_nop 0\n\t" \
    "s_sub_u32 s70, s80, s74\n\t" \
    "s_max_i32 s70, s70, 0\n\t" \
    "s_min_i32 s70, s70, 16000\n\t" \
    "s_lshl_b32 s70, s70, 1\n\t" \
    "s_mul_hi_u32 s70, s70, s77\n\t" \
    "s_add_u32 s70, s70, s73\n\t" \
    "s_getpc_b64 s[30:31]\n\t" \
    "rr_Wp59_%=:\n\t" \
    "s_add_u32 s30, s30, rr_We60_%=-rr_Wp59_%=\n\t" \
    "s_addc_u32 s31, s31, 0\n\t" \
    "s_sub_u32 s70, s70, 96\n\t" \
    "rr_W58_%=:\n\t" \
    "s_memtime s[56:57]\n\t" \
    "s_waitcnt lgkmcnt(0)\n\t" \
    "s_sub_u32 s69, s70, s56\n\t" \
    "s_cmp_gt_i32 s69, 80\n\t" \
    "s_cbranch_scc1 rr_W58_%=\n\t" \
    "s_max_i32 s69, s69, 0\n\t" \
    "s_and_b32 s69, s69, -4\n\t" \
    "s_sub_u32 s30, s30, s69\n\t" \
    "s_subb_u32 s31, s31, 0\n\t" \
    "s_setpc_b64 s[30:31]\n\t" \
    "s_nop 0\n\t" \
    "s_nop 0\n\t" \
    "s_nop 0\n\t" \
    "s_nop 0\n\t" \
    "s_nop 0\n\t" \
    "s_nop 0\n\t" \
    "s_nop 0\n\t" \
    "s_nop 0\n\t" \
    "s_nop 0\n\t" \
    "s_nop 0\n\t" \
    "s_nop 0\n\t" \
    "s_nop 0\n\t" \
    "s_nop 0\n\t" \
    "s_nop 0\n\t" \
    "s_nop 0\n\t" \
    "s_nop 0\n\t" \
    "s_nop 0\n\t" \
    "s_nop 0\n\t" \
    "s_nop 0\n\t" \
    "s_nop 0\n\t" \
    "rr_We60_%=:\n\t" \
    "global_load_dword v192, v7, s[52:53] offset:0 sc1\n\t" \
    "s_sub_u32 s71, s86, s74\n\t" \
    "s_lshl_b32 s71, s71, 2\n\t" \
    "s_lshr_b32 s33, s77, 1\n\t" \
    "s_sub_u32 s34, s73, 176\n\t" \
    "s_mov_b32 s96, 0\n\t" \
    "s_getpc_b64 s[30:31]\n\t" \
    "rr_rpc64_%=:\n\t" \
    "s_add_u32 s50, s30, rr_pgo67_%=-rr_rpc64_%=\n\t" \
    "s_addc_u32 s51, s31, 0\n\t" \
    "s_add_u32 s30, s30, rr_again5_%=-rr_rpc64_%=\n\t" \
    "s_addc_u32 s31, s31, 0\n\t" \
    "s_nop 7\n\t" \
    "s_nop 7\n\t" \
    "s_nop 7\n\t" \
    "s_nop 7\n\t" \
    "s_nop 3\n\t" \
    "global_load_dword v193, v7, s[52:53] offset:2048 sc1\n\t" \
    "s_nop 7\n\t" \
    "s_nop 7\n\t" \
    "s_nop 7\n\t" \
    "s_nop 7\n\t" \
    "s_nop 7\n\t" \
    "s_nop 7\n\t" \
    "global_load_dword v194, v189, s[52:53] offset:0 sc1\n\t" \
    "s_waitcnt vmcnt(2)\n\t" \
    "rr_hok61_%=:\n\t" \
    "s_memtime s[54:55]\n\t" \
    "v_max_u32_dpp v192, v192, v192 row_ror:1 row_mask:0xf bank_mask:0xf\n\t" \
    "s_nop 1\n\t" \
    "v_max_u32_dpp v192, v192, v192 row_ror:2 row_mask:0xf bank_mask:0xf\n\t" \
    "s_nop 1\n\t" \
    "v_max_u32_dpp v192, v192, v192 row_ror:4 row_mask:0xf bank_mask:0xf\n\t" \
    "s_nop 1\n\t" \
    "v_max_u32_dpp v192, v192, v192 row_ror:8 row_mask:0xf bank_mask:0xf\n\t" \
    "s_nop 0\n\t" \
    "v_readlane_b32 s69, v192, 0\n\t" \
    "v_readlane_b32 s70, v192, 16\n\t" \
    "s_waitcnt lgkmcnt(0)\n\t" \
    "rr_hmax70_%=:\n\t" \
    "s_sub_u32 s56, s34, s54\n\t" \
    "s_max_u32 s81, s69, s70\n\t" \
    "s_cmp_eq_u32 s81, -1\n\t" \
    "s_cbranch_scc1 rr_hlate62_%=\n\t" \
    "s_lshl2_add_u32 s70, s81, s71\n\t" \
    "s_mul_hi_i32 s70, s70, s33\n\t" \
    "s_add_u32 s69, s70, s56\n\t" \
    "s_max_i32 s69, s69, 0\n\t" \
    "s_cmp_gt_i32 s69, 80\n\t" \
    "s_cbranch_scc1 rr_rpoll65_%=\n\t" \
    "rr_rback66_%=:\n\t" \
    "s_and_b32 s69, s69, -4\n\t" \
    "s_sub_u32 s30, s30, s69\n\t" \
    "s_subb_u32 s31, s31, 0\n\t" \
    "s_setpc_b64 s[30:31]\n\t" \
    "rr_rpoll65_%=:\n\t" \
    "s_min_i32 s70, s70, 24000\n\t" \
    "s_add_u32 s70, s70, s73\n\t" \
    "s_sub_u32 s70, s70, 124\n\t" \
    "s_sub_u32 s70, s70, s96\n\t" \
    "rr_wl63_%=:\n\t" \
    "s_memtime s[56:57]\n\t" \
    "s_waitcnt lgkmcnt(0)\n\t" \
    "s_sub_u32 s69, s70, s56\n\t" \
    "s_cmp_gt_i32 s69, 80\n\t" \
    "s_cbranch_scc1 rr_wl63_%=\n\t" \
    "s_max_i32 s69, s69, 0\n\t" \
    "s_branch rr_rback66_%=\n\t" \
    "s_nop 0\n\t" \
    "s_nop 0\n\t" \
    "s_nop 0\n\t" \
    "s_nop 0\n\t" \
    "s_nop 0\n\t" \
    "s_nop 0\n\t" \
    "s_nop 0\n\t" \
    "s_nop 0\n\t" \
    "s_nop 0\n\t" \
    "s_nop 0\n\t" \
    "s_nop 0\n\t" \
    "s_nop 0\n\t" \
    "s_nop 0\n\t" \
    "s_nop 0\n\t" \
    "s_nop 0\n\t" \
    "s_nop 0\n\t" \
    "s_nop 0\n\t" \
    "s_nop 0\n\t" \
    "s_nop 0\n\t" \
    "s_nop 0\n\t" \
    "rr_again5_%=:\n\t" \
    "rr_pgo67_%=:\n\t" \
    "global_load_dwordx4 v[48:51], v10, s[46:47] sc1\n\t" \
    "global_load_dwordx4 v[52:55], v11, s[46:47] sc1\n\t" \
    "global_load_dwordx4 v[56:59], v12, s[46:47] sc1\n\t" \
    "global_load_dwordx4 v[60:63], v13, s[46:47] sc1\n\t" \
    "global_load_dwordx4 v[64:67], v14, s[46:47] sc1\n\t" \
    "global_load_dwordx4 v[68:71], v15, s[46:47] sc1\n\t" \
    "global_load_dwordx4 v[72:75], v16, s[46:47] sc1\n\t" \
    "global_load_dwordx4 v[76:79], v17, s[46:47] sc1\n\t" \
    "global_load_dwordx4 v[80:83], v18, s[46:47] sc1\n\t" \
    "global_load_dwordx4 v[84:87], v19, s[46:47] sc1\n\t" \
    "global_load_dwordx4 v[88:91], v20, s[46:47] sc1\n\t" \
    "global_load_dwordx4 v[92:95], v21, s[46:47] sc1\n\t" \
    "global_load_dwordx4 v[96:99], v22, s[46:47] sc1\n\t" \
    "global_load_dwordx4 v[100:103], v23, s[46:47] sc1\n\t" \
    "global_load_dwordx4 v[104:107], v24, s[46:47] sc1\n\t" \
    "global_load_dwordx4 v[108:111], v25, s[46:47] sc1\n\t" \
    "global_load_dwordx4 v[112:115], v42, s[46:47] sc1\n\t" \
    "global_load_dwordx4 v[116:119], v43, s[46:47] sc1\n\t" \
    "global_load_dwordx4 v[120:123], v44, s[46:47] sc1\n\t" \
    "global_load_dwordx4 v[124:127], v45, s[46:47] sc1\n\t" \
    "global_load_dwordx4 v[128:131], v0, s[40:41] offset:0 sc1\n\t" \
    "global_load_dwordx4 v[132:135], v0, s[40:41] offset:1024 sc1\n\t" \
    "global_load_dwordx4 v[136:139], v0, s[40:41] offset:2048 sc1\n\t" \
    "global_load_dwordx4 v[140:143], v0, s[40:41] offset:3072 sc1\n\t" \
    "global_load_dwordx4 v[144:147], v1, s[40:41] offset:0 sc1\n\t" \
    "global_load_dwordx4 v[148:151], v1, s[40:41] offset:1024 sc1\n\t" \
    "global_load_dwordx4 v[152:155], v1, s[40:41] offset:2048 sc1\n\t" \
    "global_load_dwordx4 v[156:159], v1, s[40:41] offset:3072 sc1\n\t" \
    "s_cmp_lg_u32 s91, 0\n\t" \
    "s_cbranch_scc1 rr_nob8_%=\n\t" \
    "rr_hbk71_%=:\n\t" \
    "v_mov_b32 v164, s81\n\t" \
    "ds_write_b32 v9, v164\n\t" \
    "s_waitcnt lgkmcnt(0)\n\t" \
    "s_barrier\n\t" \
    "rr_nob8_%=:\n\t" \
    "s_waitcnt vmcnt(27)\n\t" \
    "v_smfmac_f32_16x16x64_bf16 v[160:163], v[48:51], a[0:7], v4\n\t" \
    "s_waitcnt vmcnt(26)\n\t" \
    "v_smfmac_f32_16x16x64_bf16 v[160:163], v[52:55], a[8:15], v4\n\t" \
    "s_waitcnt vmcnt(25)\n\t" \
    "v_smfmac_f32_16x16x64_bf16 v[160:163], v[56:59], a[16:23], v4\n\t" \
    "s_waitcnt vmcnt(24)\n\t" \
    "v_smfmac_f32_16x16x64_bf16 v[160:163], v[60:63], a[24:31], v4\n\t" \
    "s_waitcnt vmcnt(23)\n\t" \
    "v_smfmac_f32_16x16x64_bf16 v[160:163], v[64:67], a[32:39], v4\n\t" \
    "s_waitcnt vmcnt(22)\n\t" \
    "v_smfmac_f32_16x16x64_bf16 v[160:163], v[68:71], a[40:47], v4\n\t" \
    "s_waitcnt vmcnt(21)\n\t" \
    "v_smfmac_f32_16x16x64_bf16 v[160:163], v[72:75], a[48:55], v4\n\t" \
    "s_waitcnt vmcnt(20)\n\t" \
    "v_smfmac_f32_16x16x64_bf16 v[160:163], v[76:79], a[56:63], v4\n\t" \
    "s_waitcnt vmcnt(19)\n\t" \
    "v_smfmac_f32_16x16x64_bf16 v[160:163], v[80:83], a[64:71], v4\n\t" \
    "s_waitcnt vmcnt(18)\n\t" \
    "v_smfmac_f32_16x16x64_bf16 v[160:163], v[84:87], a[72:79], v4\n\t" \
    "s_waitcnt vmcnt(17)\n\t" \
    "v_smfmac_f32_16x16x64_bf16 v[160:163], v[88:91], a[80:87], v4\n\t" \
    "s_waitcnt vmcnt(16)\n\t" \
    "v_smfmac_f32_16x16x64_bf16 v[160:163], v[92:95], a[88:95], v4\n\t" \
    "s_waitcnt vmcnt(15)\n\t" \
    "v_smfmac_f32_16x16x64_bf16 v[160:163], v[96:99], a[96:103], v4\n\t" \
    "s_waitcnt vmcnt(14)\n\t" \
    "v_smfmac_f32_16x16x64_bf16 v[160:163], v[100:103], a[104:111], v4\n\t" \
    "s_waitcnt vmcnt(13)\n\t" \
    "v_smfmac_f32_16x16x64_bf16 v[160:163], v[104:107], a[112:119], v4\n\t" \
    "s_waitcnt vmcnt(12)\n\t" \
    "v_smfmac_f32_16x16x64_bf16 v[160:163], v[108:111], a[120:127], v4\n\t" \
    "s_waitcnt vmcnt(6)\n\t" \
    "v_smfmac_f32_16x16x64_bf16 v[160:163], v[112:115], v[128:135], v4\n\t" \
    "s_waitcnt vmcnt(4)\n\t" \
    "v_smfmac_f32_16x16x64_bf16 v[160:163], v[116:119], v[136:143], v4\n\t" \
    "s_waitcnt vmcnt(2)\n\t" \
    "v_smfmac_f32_16x16x64_bf16 v[160:163], v[120:123], v[144:151], v4\n\t" \
    "s_waitcnt vmcnt(0)\n\t" \
    "v_smfmac_f32_16x16x64_bf16 v[160:163], v[124:127], v[152:159], v4\n\t" \
    "s_nop 7\n\t" \
    "v_pk_add_f32 v[160:161], v[160:161], v[162:163]\n\t" \
    "v_add_f32 v166, v160, v161\n\t" \
    "v_cvt_pk_bf16_f32 v164, v160, v161\n\t" \
    "s_nop 0\n\t" \
    "v_add_f32_dpp v166, v166, v166 quad_perm:[1,0,3,2] row_mask:0xf bank_mask:0xf\n\t" \
    "v_mov_b32_dpp v165, v164 quad_perm:[1,0,3,2] row_mask:0xf bank_mask:0xf\n\t" \
    "v_cmp_u_f32 vcc, v166, v166\n\t" \
    "v_perm_b32 v167, v165, v164, v5\n\t" \
    "s_nop 0\n\t" \
    "v_cndmask_b32 v164, v167, v172, vcc\n\t" \
    "rr_cont7_%=:\n\t" \
    "global_store_dword v180, v164, s[48:49] sc1\n\t" \
    "global_store_dword v181, v164, s[48:49] sc1\n\t" \
    "global_store_dword v182, v164, s[48:49] sc1\n\t" \
    "global_store_dword v2, v164, s[48:49] sc1\n\t" \
    "s_cmp_lg_u32 s64, 0\n\t" \
    "s_cbranch_scc1 rr_g214_%=\n\t" \
    "s_cbranch_vccnz rr_bad6_%=\n\t" \
    "s_cmp_eq_u32 s65, 62\n\t" \
    "s_cbranch_scc1 rr_end4_%=\n\t" \
    "s_add_u32 s65, s65, 2\n\t" \
    "s_branch rr_pf9_%=\n\t" \
    "rr_bad6_%=:\n\t" \
    "s_cmp_lg_u32 s64, 0\n\t" \
    "s_cbranch_scc1 rr_cont7_%=\n\t" \
    "s_waitcnt lgkmcnt(0)\n\t" \
    "v_max_u32 v165, v48, v49\n\t" \
    "v_max3_u32 v165, v165, v50, v51\n\t" \
    "v_max3_u32 v165, v165, v52, v53\n\t" \
    "v_max3_u32 v165, v165, v54, v55\n\t" \
    "v_max3_u32 v165, v165, v56, v57\n\t" \
    "v_max3_u32 v165, v165, v58, v59\n\t" \
    "v_max3_u32 v165, v165, v60, v61\n\t" \
    "v_max3_u32 v165, v165, v62, v63\n\t" \
    "v_max3_u32 v165, v165, v64, v65\n\t" \
    "v_max3_u32 v165, v165, v66, v67\n\t" \
    "v_max3_u32 v165, v165, v68, v69\n\t" \
    "v_max3_u32 v165, v165, v70, v71\n\t" \
    "v_max3_u32 v165, v165, v72, v73\n\t" \
    "v_max3_u32 v165, v165, v74, v75\n\t" \
    "v_max3_u32 v165, v165, v76, v77\n\t" \
    "v_max3_u32 v165, v165, v78, v79\n\t" \
    "v_max3_u32 v165, v165, v80, v81\n\t" \
    "v_max3_u32 v165, v165, v82, v83\n\t" \
    "v_max3_u32 v165, v165, v84, v85\n\t" \
    "v_max3_u32 v165, v165, v86, v87\n\t" \
    "v_max3_u32 v165, v165, v88, v89\n\t" \
    "v_max3_u32 v165, v165, v90, v91\n\t" \
    "v_max3_u32 v165, v165, v92, v93\n\t" \
    "v_max3_u32 v165, v165, v94, v95\n\t" \
    "v_max3_u32 v165, v165, v96, v97\n\t" \
    "v_max3_u32 v165, v165, v98, v99\n\t" \
    "v_max3_u32 v165, v165, v100, v101\n\t" \
    "v_max3_u32 v165, v165, v102, v103\n\t" \
    "v_max3_u32 v165, v165, v104, v105\n\t" \
    "v_max3_u32 v165, v165, v106, v107\n\t" \
    "v_max3_u32 v165, v165, v108, v109\n\t" \
    "v_max3_u32 v165, v165, v110, v111\n\t" \
    "v_max3_u32 v165, v165, v112, v113\n\t" \
    "v_max3_u32 v165, v165, v114, v115\n\t" \
    "v_max3_u32 v165, v165, v116, v117\n\t" \
    "v_max3_u32 v165, v165, v118, v119\n\t" \
    "v_max3_u32 v165, v165, v120, v121\n\t" \
    "v_max3_u32 v165, v165, v122, v123\n\t" \
    "v_max3_u32 v165, v165, v124, v125\n\t" \
    "v_max3_u32 v165, v165, v126, v127\n\t" \
    "v_max_u32 v166, v128, v129\n\t" \
    "v_max3_u32 v166, v166, v130, v131\n\t" \
    "v_max3_u32 v166, v166, v132, v133\n\t" \
    "v_max3_u32 v166, v166, v134, v135\n\t" \
    "v_max3_u32 v166, v166, v136, v137\n\t" \
    "v_max3_u32 v166, v166, v138, v139\n\t" \
    "v_max3_u32 v166, v166, v140, v141\n\t" \
    "v_max3_u32 v166, v166, v142, v143\n\t" \
    "v_max3_u32 v166, v166, v144, v145\n\t" \
    "v_max3_u32 v166, v166, v146, v147\n\t" \
    "v_max3_u32 v166, v166, v148, v149\n\t" \
    "v_max3_u32 v166, v166, v150, v151\n\t" \
    "v_max3_u32 v166, v166, v152, v153\n\t" \
    "v_max3_u32 v166, v166, v154, v155\n\t" \
    "v_max3_u32 v166, v166, v156, v157\n\t" \
    "v_max3_u32 v166, v166, v158, v159\n\t" \
    "v_max_u32 v165, v165, v166\n\t" \
    "v_mov_b32 v164, v167\n\t" \
    "v_cmp_eq_u32 vcc, -1, v165\n\t" \
    "s_cbranch_vccz rr_cont7_%=\n\t" \
    "s_mov_b64 vcc, 0\n\t" \
    "s_mov_b32 s91, 1\n\t" \
    "s_mov_b32 s96, 0\n\t" \
    "v_mov_b32 v160, 0\n\t" \
    "v_mov_b32 v161, 0\n\t" \
    "v_mov_b32 v162, 0\n\t" \
    "v_mov_b32 v163, 0\n\t" \
    "s_cmp_eq_u32 s68, 0\n\t" \
    "s_cbranch_scc1 rr_F74_%=\n\t" \
    "s_sub_u32 s68, s68, 1\n\t" \
    "s_branch rr_again5_%=\n\t" \
    "rr_F74_%=:\n\t" \
    "v_mov_b32 v166, 5767424\n\t" \
    "v_mov_b32 v167, 3\n\t" \
    "global_store_dword v166, v167, s[36:37] sc1\n\t" \
    "s_waitcnt vmcnt(0)\n\t" \
    "s_branch rr_cont7_%=\n\t" \
    "rr_hlate62_%=:\n\t" \
    "s_cmp_lg_u32 s64, 0\n\t" \
    "s_cbranch_scc1 rr_hpd73_%=\n\t" \
    "global_load_dword v195, v189, s[52:53] offset:2048 sc1\n\t" \
    "s_waitcnt vmcnt(2)\n\t" \
    "v_mov_b32 v192, v193\n\t" \
    "v_cmp_eq_u32 vcc, -1, v193\n\t" \
    "s_cbranch_vccz rr_hok61_%=\n\t" \
    "global_load_dword v196, v190, s[52:53] offset:0 sc1\n\t" \
    "s_waitcnt vmcnt(2)\n\t" \
    "v_mov_b32 v192, v194\n\t" \
    "v_cmp_eq_u32 vcc, -1, v194\n\t" \
    "s_cbranch_vccz rr_hok61_%=\n\t" \
    "global_load_dword v197, v190, s[52:53] offset:2048 sc1\n\t" \
    "s_waitcnt vmcnt(2)\n\t" \
    "v_mov_b32 v192, v195\n\t" \
    "v_cmp_eq_u32 vcc, -1, v195\n\t" \
    "s_cbranch_vccz rr_hok61_%=\n\t" \
    "s_waitcnt vmcnt(1)\n\t" \
    "v_mov_b32 v192, v196\n\t" \
    "v_cmp_eq_u32 vcc, -1, v196\n\t" \
    "s_cbranch_vccz rr_hok61_%=\n\t" \
    "s_waitcnt vmcnt(0)\n\t" \
    "v_mov_b32 v192, v197\n\t" \
    "v_cmp_eq_u32 vcc, -1, v197\n\t" \
    "s_cbranch_vccz rr_hok61_%=\n\t" \
    "rr_hp72_%=:\n\t" \
    "global_load_dword v192, v191, s[52:53] offset:0 sc1\n\t" \
    "s_waitcnt vmcnt(0)\n\t" \
    "v_cmp_eq_u32 vcc, -1, v192\n\t" \
    "s_cbranch_vccz rr_hpd73_%=\n\t" \
    "s_cmp_eq_u32 s68, 0\n\t" \
    "s_cbranch_scc1 rr_F75_%=\n\t" \
    "s_sub_u32 s68, s68, 1\n\t" \
    "s_sleep 1\n\t" \
    "s_branch rr_hp72_%=\n\t" \
    "rr_F75_%=:\n\t" \
    "v_mov_b32 v166, 5767424\n\t" \
    "v_mov_b32 v167, 7\n\t" \
    "global_store_dword v166, v167, s[36:37] sc1\n\t" \
    "s_waitcnt vmcnt(0)\n\t" \
    "s_branch rr_hpd73_%=\n\t" \
    "rr_hpd73_%=:\n\t" \
    "v_and_b32 v192, -2, v192\n\t" \
    "s_waitcnt lgkmcnt(0)\n\t" \
    "s_mov_b32 s70, 116\n\t" \
    "rr_E76_%=:\n\t" \
    "s_memtime s[56:57]\n\t" \
    "s_memrealtime s[2:3]\n\t" \
    "s_nop 0\n\t" \
    "s_memrealtime s[4:5]\n\t" \
    "s_nop 0\n\t" \
    "s_memrealtime s[6:7]\n\t" \
    "s_nop 0\n\t" \
    "s_memrealtime s[8:9]\n\t" \
    "s_nop 0\n\t" \
    "s_memrealtime s[10:11]\n\t" \
    "s_nop 0\n\t" \
    "s_memrealtime s[12:13]\n\t" \
    "s_nop 0\n\t" \
    "s_memrealtime s[14:15]\n\t" \
    "s_nop 0\n\t" \
    "s_memrealtime s[16:17]\n\t" \
    "s_nop 0\n\t" \
    "s_memrealtime s[18:19]\n\t" \
    "s_nop 0\n\t" \
    "s_memrealtime s[20:21]\n\t" \
    "s_nop 0\n\t" \
    "s_memrealtime s[22:23]\n\t" \
    "s_nop 0\n\t" \
    "s_memrealtime s[24:25]\n\t" \
    "s_nop 0\n\t" \
    "s_memrealtime s[26:27]\n\t" \
    "s_nop 0\n\t" \
    "s_memrealtime s[28:29]\n\t" \
    "s_memtime s[54:55]\n\t" \
    "s_add_u32 s70, s70, 8\n\t" \
    "s_waitcnt lgkmcnt(0)\n\t" \
    "s_sub_u32 s69, s54, s56\n\t" \
    "s_cmp_gt_u32 s69, s70\n\t" \
    "s_cbranch_scc1 rr_E76_%=\n\t" \
    "s_add_u32 s2, s2, s4\n\t" \
    "s_add_u32 s6, s6, s8\n\t" \
    "s_add_u32 s10, s10, s12\n\t" \
    "s_add_u32 s14, s14, s16\n\t" \
    "s_add_u32 s18, s18, s20\n\t" \
    "s_add_u32 s22, s22, s24\n\t" \
    "s_add_u32 s2, s2, s6\n\t" \
    "s_add_u32 s10, s10, s14\n\t" \
    "s_add_u32 s18, s18, s22\n\t" \
    "s_add_u32 s2, s2, s26\n\t" \
    "s_add_u32 s10, s10, s18\n\t" \
    "s_add_u32 s2, s2, s10\n\t" \
    "s_mul_i32 s70, s28, 13\n\t" \
    "s_sub_u32 s70, s70, s2\n\t" \
    "s_min_u32 s70, s70, 52\n\t" \
    "s_mul_i32 s69, s69, s70\n\t" \
    "s_mul_hi_u32 s69, s69, 76695844\n\t" \
    "s_mov_b32 s75, s73\n\t" \
    "s_mov_b32 s76, s74\n\t" \
    "s_add_u32 s73, s56, s69\n\t" \
    "s_lshl_b32 s74, s28, 4\n\t" \
    "s_sub_u32 s71, s86, s74\n\t" \
    "s_lshl_b32 s71, s71, 2\n\t" \
    "s_lshr_b32 s33, s77, 1\n\t" \
    "s_sub_u32 s34, s73, 176\n\t" \
    "s_branch rr_hok61_%=\n\t" \
    "rr_end4_%=:\n\t" \
    "s_endpgm\n\t" \
    "rr_last_%=:\n\t" \
    ""
#define ASM_H \
    "s_mov_b64 s[36:37], %0\n\t" \
    "s_mov_b64 s[42:43], %1\n\t" \
    "s_mov_b64 s[44:45], %2\n\t" \
    "s_mov_b32 s66, %3\n\t" \
    "s_mov_b32 s67, %4\n\t" \
    "s_mov_b64 s[50:51], %5\n\t" \
    "s_mov_b32 s82, %7\n\t" \
    "s_load_dwordx8 s[84:91], %6, 0\n\t" \
    "v_mbcnt_lo_u32_b32 v223, -1, 0\n\t" \
    "v_mbcnt_hi_u32_b32 v223, -1, v223\n\t" \
    "s_getpc_b64 s[46:47]\n\t" \
    "hh_pc_%=:\n\t" \
    "s_lshl_b32 s69, s66, 13\n\t" \
    "v_lshl_add_u32 v222, v223, 7, s69\n\t" \
    "v_min_u32 v222, hh_last_%=-hh_pc_%=-4, v222\n\t" \
    "global_load_dword v222, v222, s[46:47]\n\t" \
    "v_lshlrev_b32 v223, 2, v223\n\t" \
    "v_add_u32 v223, s82, v223\n\t" \
    "ds_read_b32 v0, v223 offset:0\n\t" \
    "ds_read_b32 v1, v223 offset:256\n\t" \
    "ds_read_b32 v2, v223 offset:512\n\t" \
    "ds_read_b32 v3, v223 offset:768\n\t" \
    "ds_read_b32 v4, v223 offset:1024\n\t" \
    "ds_read_b32 v5, v223 offset:1280\n\t" \
    "ds_read_b32 v6, v223 offset:1536\n\t" \
    "ds_read_b32 v7, v223 offset:1792\n\t" \
    "ds_read_b32 v8, v223 offset:2048\n\t" \
    "ds_read_b32 v9, v223 offset:2304\n\t" \
    "ds_read_b32 v10, v223 offset:2560\n\t" \
    "ds_read_b32 v11, v223 offset:2816\n\t" \
    "s_waitcnt lgkmcnt(0)\n\t" \
    "ds_read_b32 v12, v223 offset:3072\n\t" \
    "ds_read_b32 v13, v223 offset:3328\n\t" \
    "ds_read_b32 v14, v223 offset:3584\n\t" \
    "ds_read_b32 v15, v223 offset:3840\n\t" \
    "ds_read_b32 v16, v223 offset:4096\n\t" \
    "ds_read_b32 v17, v223 offset:4352\n\t" \
    "s_waitcnt lgkmcnt(0)\n\t" \
    "s_mov_b32 s68, 262144\n\t" \
    "s_mov_b32 s72, 0\n\t" \
    "s_mov_b32 s83, -1\n\t" \
    "s_mov_b32 s65, 0\n\t" \
    "s_mov_b32 s77, 3019898880\n\t" \
    "s_mov_b32 s78, 3054198966\n\t" \
    "s_waitcnt lgkmcnt(0)\n\t" \
    "s_mov_b32 s70, 116\n\t" \
    "hh_E1_%=:\n\t" \
    "s_memtime s[56:57]\n\t" \
    "s_memrealtime s[2:3]\n\t" \
    "s_nop 0\n\t" \
    "s_memrealtime s[4:5]\n\t" \
    "s_nop 0\n\t" \
    "s_memrealtime s[6:7]\n\t" \
    "s_nop 0\n\t" \
    "s_memrealtime s[8:9]\n\t" \
    "s_nop 0\n\t" \
    "s_memrealtime s[10:11]\n\t" \
    "s_nop 0\n\t" \
    "s_memrealtime s[12:13]\n\t" \
    "s_nop 0\n\t" \
    "s_memrealtime s[14:15]\n\t" \
    "s_nop 0\n\t" \
    "s_memrealtime s[16:17]\n\t" \
    "s_nop 0\n\t" \
    "s_memrealtime s[18:19]\n\t" \
    "s_nop 0\n\t" \
    "s_memrealtime s[20:21]\n\t" \
    "s_nop 0\n\t" \
    "s_memrealtime s[22:23]\n\t" \
    "s_nop 0\n\t" \
    "s_memrealtime s[24:25]\n\t" \
    "s_nop 0\n\t" \
    "s_memrealtime s[26:27]\n\t" \
    "s_nop 0\n\t" \
    "s_memrealtime s[28:29]\n\t" \
    "s_memtime s[54:55]\n\t" \
    "s_add_u32 s70, s70, 8\n\t" \
    "s_waitcnt lgkmcnt(0)\n\t" \
    "s_sub_u32 s69, s54, s56\n\t" \
    "s_cmp_gt_u32 s69, s70\n\t" \
    "s_cbranch_scc1 hh_E1_%=\n\t" \
    "s_add_u32 s2, s2, s4\n\t" \
    "s_add_u32 s6, s6, s8\n\t" \
    "s_add_u32 s10, s10, s12\n\t" \
    "s_add_u32 s14, s14, s16\n\t" \
    "s_add_u32 s18, s18, s20\n\t" \
    "s_add_u32 s22, s22, s24\n\t" \
    "s_add_u32 s2, s2, s6\n\t" \
    "s_add_u32 s10, s10, s14\n\t" \
    "s_add_u32 s18, s18, s22\n\t" \
    "s_add_u32 s2, s2, s26\n\t" \
    "s_add_u32 s10, s10, s18\n\t" \
    "s_add_u32 s2, s2, s10\n\t" \
    "s_mul_i32 s70, s28, 13\n\t" \
    "s_sub_u32 s70, s70, s2\n\t" \
    "s_min_u32 s70, s70, 52\n\t" \
    "s_mul_i32 s69, s69, s70\n\t" \
    "s_mul_hi_u32 s69, s69, 76695844\n\t" \
    "s_mov_b32 s75, s73\n\t" \
    "s_mov_b32 s76, s74\n\t" \
    "s_add_u32 s73, s56, s69\n\t" \
    "s_lshl_b32 s74, s28, 4\n\t" \
    "v_mbcnt_lo_u32_b32 v30, -1, 0\n\t" \
    "v_mbcnt_hi_u32_b32 v30, -1, v30\n\t" \
    "v_lshlrev_b32 v30, 6, v30\n\t" \
    "v_add_u32 v30, 6225920, v30\n\t" \
    "v_mov_b32 v31, 6234112\n\t" \
    "global_load_dword v31, v31, s[36:37] sc1\n\t" \
    "global_load_dwordx2 v[32:33], v30, s[36:37] sc1\n\t" \
    "global_load_dword v34, v30, s[36:37] offset:2048 sc1\n\t" \
    "s_waitcnt vmcnt(0)\n\t" \
    "hh_C2_%=:\n\t" \
    "v_cmp_ne_u32 s[54:55], v32, v34\n\t" \
    "v_sub_u32 v32, v32, v31\n\t" \
    "v_cmp_ne_u32 vcc, 1, v32\n\t" \
    "s_nop 1\n\t" \
    "s_or_b64 vcc, vcc, s[54:55]\n\t" \
    "s_cbranch_scc0 hh_Cok3_%=\n\t" \
    "s_cmp_eq_u32 s68, 0\n\t" \
    "s_cbranch_scc1 hh_F5_%=\n\t" \
    "s_sub_u32 s68, s68, 1\n\t" \
    "s_sleep 6\n\t" \
    "s_branch hh_Cag4_%=\n\t" \
    "hh_F5_%=:\n\t" \
    "v_mov_b32 v34, 5767424\n\t" \
    "v_mov_b32 v35, 4\n\t" \
    "global_store_dword v34, v35, s[36:37] sc1\n\t" \
    "s_waitcnt vmcnt(0)\n\t" \
    "s_branch hh_Cok3_%=\n\t" \
    "hh_Cag4_%=:\n\t" \
    "global_load_dwordx2 v[32:33], v30, s[36:37] sc1\n\t" \
    "global_load_dword v34, v30, s[36:37] offset:2048 sc1\n\t" \
    "s_waitcnt vmcnt(0)\n\t" \
    "s_branch hh_C2_%=\n\t" \
    "hh_Cok3_%=:\n\t" \
    "v_readlane_b32 s69, v33, 2\n\t" \
    "v_readlane_b32 s70, v33, 3\n\t" \
    "s_nop 1\n\t" \
    "s_cmp_lt_u32 s70, s69\n\t" \
    "s_cselect_b32 s69, 0, 8192\n\t" \
    "s_add_u32 s100, s36, s69\n\t" \
    "s_addc_u32 s101, s37, 0\n\t" \
    "v_mov_b32_dpp v30, v33 quad_perm:[1,0,3,2] row_mask:0xf bank_mask:0xf\n\t" \
    "v_cmp_lt_u32 s[44:45], v30, v33\n\t" \
    "s_nop 1\n\t" \
    "s_bitcmp1_b32 s44, 0\n\t" \
    "s_cselect_b32 s62, 8192, 0\n\t" \
    "s_bitcmp1_b32 s44, 2\n\t" \
    "s_cselect_b32 s63, 8192, 0\n\t" \
    "s_or_b32 s63, s63, 0x200000\n\t" \
    "v_min_u32 v30, v30, v33\n\t" \
    "v_cmp_lt_u32 vcc, 660, v30\n\t" \
    "s_nop 1\n\t" \
    "s_bcnt1_i32_b64 s69, vcc\n\t" \
    "s_cmp_gt_u32 s69, 16\n\t" \
    "s_cselect_b32 s69, 1, 0\n\t" \
    "s_mul_i32 s70, s69, 160\n\t" \
    "s_add_u32 s86, s86, s70\n\t" \
    "s_mul_i32 s70, s69, 536\n\t" \
    "s_add_u32 s87, s87, s70\n\t" \
    "s_mul_i32 s70, s69, 256\n\t" \
    "s_add_u32 s88, s88, s70\n\t" \
    "s_waitcnt lgkmcnt(0)\n\t" \
    "s_mov_b32 s70, 116\n\t" \
    "hh_E6_%=:\n\t" \
    "s_memtime s[56:57]\n\t" \
    "s_memrealtime s[2:3]\n\t" \
    "s_nop 0\n\t" \
    "s_memrealtime s[4:5]\n\t" \
    "s_nop 0\n\t" \
    "s_memrealtime s[6:7]\n\t" \
    "s_nop 0\n\t" \
    "s_memrealtime s[8:9]\n\t" \
    "s_nop 0\n\t" \
    "s_memrealtime s[10:11]\n\t" \
    "s_nop 0\n\t" \
    "s_memrealtime s[12:13]\n\t" \
    "s_nop 0\n\t" \
    "s_memrealtime s[14:15]\n\t" \
    "s_nop 0\n\t" \
    "s_memrealtime s[16:17]\n\t" \
    "s_nop 0\n\t" \
    "s_memrealtime s[18:19]\n\t" \
    "s_nop 0\n\t" \
    "s_memrealtime s[20:21]\n\t" \
    "s_nop 0\n\t" \
    "s_memrealtime s[22:23]\n\t" \
    "s_nop 0\n\t" \
    "s_memrealtime s[24:25]\n\t" \
    "s_nop 0\n\t" \
    "s_memrealtime s[26:27]\n\t" \
    "s_nop 0\n\t" \
    "s_memrealtime s[28:29]\n\t" \
    "s_memtime s[54:55]\n\t" \
    "s_add_u32 s70, s70, 8\n\t" \
    "s_waitcnt lgkmcnt(0)\n\t" \
    "s_sub_u32 s69, s54, s56\n\t" \
    "s_cmp_gt_u32 s69, s70\n\t" \
    "s_cbranch_scc1 hh_E6_%=\n\t" \
    "s_add_u32 s2, s2, s4\n\t" \
    "s_add_u32 s6, s6, s8\n\t" \
    "s_add_u32 s10, s10, s12\n\t" \
    "s_add_u32 s14, s14, s16\n\t" \
    "s_add_u32 s18, s18, s20\n\t" \
    "s_add_u32 s22, s22, s24\n\t" \
    "s_add_u32 s2, s2, s6\n\t" \
    "s_add_u32 s10, s10, s14\n\t" \
    "s_add_u32 s18, s18, s22\n\t" \
    "s_add_u32 s2, s2, s26\n\t" \
    "s_add_u32 s10, s10, s18\n\t" \
    "s_add_u32 s2, s2, s10\n\t" \
    "s_mul_i32 s70, s28, 13\n\t" \
    "s_sub_u32 s70, s70, s2\n\t" \
    "s_min_u32 s70, s70, 52\n\t" \
    "s_mul_i32 s69, s69, s70\n\t" \
    "s_mul_hi_u32 s69, s69, 76695844\n\t" \
    "s_mov_b32 s75, s73\n\t" \
    "s_mov_b32 s76, s74\n\t" \
    "s_add_u32 s73, s56, s69\n\t" \
    "s_lshl_b32 s74, s28, 4\n\t" \
    "s_sub_u32 s69, s73, s75\n\t" \
    "s_sub_u32 s70, s74, s76\n\t" \
    "s_sub_u32 s71, s70, 1600\n\t" \
    "s_cmp_lt_u32 s71, 400000\n\t" \
    "s_cbranch_scc0 hh_K7_%=\n\t" \
    "v_cvt_f32_u32 v30, s69\n\t" \
    "v_cvt_f32_u32 v31, s70\n\t" \
    "v_rcp_f32 v32, v30\n\t" \
    "v_rcp_f32 v33, v31\n\t" \
    "v_mul_f32 v32, v32, v31\n\t" \
    "v_mul_f32 v33, v33, v30\n\t" \
    "v_mul_f32 v32, 0x4f800000, v32\n\t" \
    "v_mul_f32 v33, 0x4f000000, v33\n\t" \
    "v_cvt_u32_f32 v32, v32\n\t" \
    "v_cvt_u32_f32 v33, v33\n\t" \
    "v_readfirstlane_b32 s69, v32\n\t" \
    "v_readfirstlane_b32 s70, v33\n\t" \
    "s_nop 1\n\t" \
    "s_mov_b32 s78, s69\n\t" \
    "s_mov_b32 s77, s70\n\t" \
    "hh_K7_%=:\n\t" \
    "s_sub_u32 s80, s74, 32000\n\t" \
    "v_mov_b32 v38, -1\n\t" \
    "v_mov_b32 v39, -1\n\t" \
    "s_mov_b32 m0, 155648\n\t" \
    "s_mul_i32 s96, s67, 16\n\t" \
    "s_add_u32 s96, s96, 64\n\t" \
    "hh_loop8_%=:\n\t" \
    "s_and_b32 s69, s65, 7\n\t" \
    "s_lshl_b32 s69, s69, 18\n\t" \
    "s_add_u32 s69, s69, s63\n\t" \
    "s_add_u32 s52, s36, s69\n\t" \
    "s_addc_u32 s53, s37, 0\n\t" \
    "s_add_u32 s71, s80, 608\n\t" \
    "s_sub_u32 s70, s71, s74\n\t" \
    "s_max_i32 s70, s70, 0\n\t" \
    "s_min_i32 s70, s70, 16000\n\t" \
    "s_lshl_b32 s70, s70, 1\n\t" \
    "s_mul_hi_u32 s70, s70, s77\n\t" \
    "s_add_u32 s70, s70, s73\n\t" \
    "hh_W11_%=:\n\t" \
    "s_memtime s[56:57]\n\t" \
    "s_waitcnt lgkmcnt(0)\n\t" \
    "s_sub_u32 s69, s70, s56\n\t" \
    "s_cmp_gt_i32 s69, 24\n\t" \
    "s_cbranch_scc1 hh_W11_%=\n\t" \
    "global_load_dword v30, v1, s[52:53] sc1\n\t" \
    "s_waitcnt lgkmcnt(0)\n\t" \
    "s_mov_b32 s70, 116\n\t" \
    "hh_E15_%=:\n\t" \
    "s_memtime s[56:57]\n\t" \
    "s_memrealtime s[2:3]\n\t" \
    "s_nop 0\n\t" \
    "s_memrealtime s[4:5]\n\t" \
    "s_nop 0\n\t" \
    "s_memrealtime s[6:7]\n\t" \
    "s_nop 0\n\t" \
    "s_memrealtime s[8:9]\n\t" \
    "s_nop 0\n\t" \
    "s_memrealtime s[10:11]\n\t" \
    "s_nop 0\n\t" \
    "s_memrealtime s[12:13]\n\t" \
    "s_nop 0\n\t" \
    "s_memrealtime s[14:15]\n\t" \
    "s_nop 0\n\t" \
    "s_memrealtime s[16:17]\n\t" \
    "s_nop 0\n\t" \
    "s_memrealtime s[18:19]\n\t" \
    "s_nop 0\n\t" \
    "s_memrealtime s[20:21]\n\t" \
    "s_nop 0\n\t" \
    "s_memrealtime s[22:23]\n\t" \
    "s_nop 0\n\t" \
    "s_memrealtime s[24:25]\n\t" \
    "s_nop 0\n\t" \
    "s_memrealtime s[26:27]\n\t" \
    "s_nop 0\n\t" \
    "s_memrealtime s[28:29]\n\t" \
    "s_memtime s[54:55]\n\t" \
    "s_add_u32 s70, s70, 8\n\t" \
    "s_waitcnt lgkmcnt(0)\n\t" \
    "s_sub_u32 s69, s54, s56\n\t" \
    "s_cmp_gt_u32 s69, s70\n\t" \
    "s_cbranch_scc1 hh_E15_%=\n\t" \
    "s_add_u32 s2, s2, s4\n\t" \
    "s_add_u32 s6, s6, s8\n\t" \
    "s_add_u32 s10, s10, s12\n\t" \
    "s_add_u32 s14, s14, s16\n\t" \
    "s_add_u32 s18, s18, s20\n\t" \
    "s_add_u32 s22, s22, s24\n\t" \
    "s_add_u32 s2, s2, s6\n\t" \
    "s_add_u32 s10, s10, s14\n\t" \
    "s_add_u32 s18, s18, s22\n\t" \
    "s_add_u32 s2, s2, s26\n\t" \
    "s_add_u32 s10, s10, s18\n\t" \
    "s_add_u32 s2, s2, s10\n\t" \
    "s_mul_i32 s70, s28, 13\n\t" \
    "s_sub_u32 s70, s70, s2\n\t" \
    "s_min_u32 s70, s70, 52\n\t" \
    "s_mul_i32 s69, s69, s70\n\t" \
    "s_mul_hi_u32 s69, s69, 76695844\n\t" \
    "s_mov_b32 s75, s73\n\t" \
    "s_mov_b32 s76, s74\n\t" \
    "s_add_u32 s73, s56, s69\n\t" \
    "s_lshl_b32 s74, s28, 4\n\t" \
    "s_sub_u32 s69, s73, s75\n\t" \
    "s_sub_u32 s70, s74, s76\n\t" \
    "s_sub_u32 s71, s70, 1600\n\t" \
    "s_cmp_lt_u32 s71, 400000\n\t" \
    "s_cbranch_scc0 hh_K16_%=\n\t" \
    "v_cvt_f32_u32 v34, s69\n\t" \
    "v_cvt_f32_u32 v35, s70\n\t" \
    "v_rcp_f32 v36, v34\n\t" \
    "v_rcp_f32 v37, v35\n\t" \
    "v_mul_f32 v36, v36, v35\n\t" \
    "v_mul_f32 v37, v37, v34\n\t" \
    "v_mul_f32 v36, 0x4f800000, v36\n\t" \
    "v_mul_f32 v37, 0x4f000000, v37\n\t" \
    "v_cvt_u32_f32 v36, v36\n\t" \
    "v_cvt_u32_f32 v37, v37\n\t" \
    "v_readfirstlane_b32 s69, v36\n\t" \
    "v_readfirstlane_b32 s70, v37\n\t" \
    "s_nop 1\n\t" \
    "s_sub_u32 s69, s69, s78\n\t" \
    "s_ashr_i32 s69, s69, 1\n\t" \
    "s_add_u32 s78, s78, s69\n\t" \
    "s_sub_u32 s70, s70, s77\n\t" \
    "s_ashr_i32 s70, s70, 1\n\t" \
    "s_add_u32 s77, s77, s70\n\t" \
    "hh_K16_%=:\n\t" \
    "s_waitcnt vmcnt(0)\n\t" \
    "v_cmp_eq_u32 vcc, -1, v30\n\t" \
    "s_cbranch_vccz hh_Hok13_%=\n\t" \
    "hh_H12_%=:\n\t" \
    "global_load_dword v30, v1, s[52:53] sc1\n\t" \
    "s_waitcnt vmcnt(0)\n\t" \
    "v_cmp_eq_u32 vcc, -1, v30\n\t" \
    "s_cbranch_vccz hh_Hc14_%=\n\t" \
    "s_cmp_eq_u32 s68, 0\n\t" \
    "s_cbranch_scc1 hh_F17_%=\n\t" \
    "s_sub_u32 s68, s68, 1\n\t" \
    "s_sleep 2\n\t" \
    "s_branch hh_H12_%=\n\t" \
    "hh_F17_%=:\n\t" \
    "v_mov_b32 v31, 5767424\n\t" \
    "v_mov_b32 v32, 14\n\t" \
    "global_store_dword v31, v32, s[36:37] sc1\n\t" \
    "s_waitcnt vmcnt(0)\n\t" \
    "s_branch hh_Hc14_%=\n\t" \
    "hh_Hc14_%=:\n\t" \
    "s_waitcnt lgkmcnt(0)\n\t" \
    "s_mov_b32 s70, 116\n\t" \
    "hh_E18_%=:\n\t" \
    "s_memtime s[56:57]\n\t" \
    "s_memrealtime s[2:3]\n\t" \
    "s_nop 0\n\t" \
    "s_memrealtime s[4:5]\n\t" \
    "s_nop 0\n\t" \
    "s_memrealtime s[6:7]\n\t" \
    "s_nop 0\n\t" \
    "s_memrealtime s[8:9]\n\t" \
    "s_nop 0\n\t" \
    "s_memrealtime s[10:11]\n\t" \
    "s_nop 0\n\t" \
    "s_memrealtime s[12:13]\n\t" \
    "s_nop 0\n\t" \
    "s_memrealtime s[14:15]\n\t" \
    "s_nop 0\n\t" \
    "s_memrealtime s[16:17]\n\t" \
    "s_nop 0\n\t" \
    "s_memrealtime s[18:19]\n\t" \
    "s_nop 0\n\t" \
    "s_memrealtime s[20:21]\n\t" \
    "s_nop 0\n\t" \
    "s_memrealtime s[22:23]\n\t" \
    "s_nop 0\n\t" \
    "s_memrealtime s[24:25]\n\t" \
    "s_nop 0\n\t" \
    "s_memrealtime s[26:27]\n\t" \
    "s_nop 0\n\t" \
    "s_memrealtime s[28:29]\n\t" \
    "s_memtime s[54:55]\n\t" \
    "s_add_u32 s70, s70, 8\n\t" \
    "s_waitcnt lgkmcnt(0)\n\t" \
    "s_sub_u32 s69, s54, s56\n\t" \
    "s_cmp_gt_u32 s69, s70\n\t" \
    "s_cbranch_scc1 hh_E18_%=\n\t" \
    "s_add_u32 s2, s2, s4\n\t" \
    "s_add_u32 s6, s6, s8\n\t" \
    "s_add_u32 s10, s10, s12\n\t" \
    "s_add_u32 s14, s14, s16\n\t" \
    "s_add_u32 s18, s18, s20\n\t" \
    "s_add_u32 s22, s22, s24\n\t" \
    "s_add_u32 s2, s2, s6\n\t" \
    "s_add_u32 s10, s10, s14\n\t" \
    "s_add_u32 s18, s18, s22\n\t" \
    "s_add_u32 s2, s2, s26\n\t" \
    "s_add_u32 s10, s10, s18\n\t" \
    "s_add_u32 s2, s2, s10\n\t" \
    "s_mul_i32 s70, s28, 13\n\t" \
    "s_sub_u32 s70, s70, s2\n\t" \
    "s_min_u32 s70, s70, 52\n\t" \
    "s_mul_i32 s69, s69, s70\n\t" \
    "s_mul_hi_u32 s69, s69, 76695844\n\t" \
    "s_mov_b32 s75, s73\n\t" \
    "s_mov_b32 s76, s74\n\t" \
    "s_add_u32 s73, s56, s69\n\t" \
    "s_lshl_b32 s74, s28, 4\n\t" \
    "s_sub_u32 s69, s73, s75\n\t" \
    "s_sub_u32 s70, s74, s76\n\t" \
    "s_sub_u32 s71, s70, 1600\n\t" \
    "s_cmp_lt_u32 s71, 400000\n\t" \
    "s_cbranch_scc0 hh_K19_%=\n\t" \
    "v_cvt_f32_u32 v34, s69\n\t" \
    "v_cvt_f32_u32 v35, s70\n\t" \
    "v_rcp_f32 v36, v34\n\t" \
    "v_rcp_f32 v37, v35\n\t" \
    "v_mul_f32 v36, v36, v35\n\t" \
    "v_mul_f32 v37, v37, v34\n\t" \
    "v_mul_f32 v36, 0x4f800000, v36\n\t" \
    "v_mul_f32 v37, 0x4f000000, v37\n\t" \
    "v_cvt_u32_f32 v36, v36\n\t" \
    "v_cvt_u32_f32 v37, v37\n\t" \
    "v_readfirstlane_b32 s69, v36\n\t" \
    "v_readfirstlane_b32 s70, v37\n\t" \
    "s_nop 1\n\t" \
    "s_sub_u32 s69, s69, s78\n\t" \
    "s_ashr_i32 s69, s69, 1\n\t" \
    "s_add_u32 s78, s78, s69\n\t" \
    "s_sub_u32 s70, s70, s77\n\t" \
    "s_ashr_i32 s70, s70, 1\n\t" \
    "s_add_u32 s77, s77, s70\n\t" \
    "hh_K19_%=:\n\t" \
    "hh_Hok13_%=:\n\t" \
    "v_max_u32_dpp v30, v30, v30 row_ror:1 row_mask:0xf bank_mask:0xf\n\t" \
    "s_nop 1\n\t" \
    "v_max_u32_dpp v30, v30, v30 row_ror:2 row_mask:0xf bank_mask:0xf\n\t" \
    "s_nop 1\n\t" \
    "v_max_u32_dpp v30, v30, v30 row_ror:4 row_mask:0xf bank_mask:0xf\n\t" \
    "s_nop 1\n\t" \
    "v_max_u32_dpp v30, v30, v30 row_ror:8 row_mask:0xf bank_mask:0xf\n\t" \
    "s_nop 0\n\t" \
    "v_readlane_b32 s69, v30, 0\n\t" \
    "v_readlane_b32 s71, v30, 16\n\t" \
    "s_nop 1\n\t" \
    "s_max_u32 s81, s69, s71\n\t" \
    "s_mul_hi_u32 s69, s85, s78\n\t" \
    "s_add_u32 s79, s81, s69\n\t" \
    "s_add_u32 s79, s79, s87\n\t" \
    "s_cmp_ge_u32 s65, 52\n\t" \
    "s_cselect_b32 s69, 32, 0\n\t" \
    "s_sub_u32 s79, s79, s69\n\t" \
    "s_cmp_lt_u32 s65, 9\n\t" \
    "s_cselect_b32 s69, 48, 0\n\t" \
    "s_add_u32 s79, s79, s69\n\t" \
    "s_add_u32 s80, s79, s88\n\t" \
    "s_add_u32 s65, s65, 1\n\t" \
    "s_cmp_gt_u32 s65, 62\n\t" \
    "s_cbranch_scc1 hh_end10_%=\n\t" \
    "s_cmp_le_u32 s65, 6\n\t" \
    "s_cbranch_scc1 hh_loop8_%=\n\t" \
    "v_lshrrev_b32 v20, 15, v11\n\t" \
    "v_add3_u32 v20, v20, s65, 1\n\t" \
    "v_min_u32 v20, 63, v20\n\t" \
    "v_mul_u32_u24 v20, v20, v11\n\t" \
    "v_xor_b32 v20, v20, v4\n\t" \
    "v_lshrrev_b32 v21, 15, v12\n\t" \
    "v_add3_u32 v21, v21, s65, 1\n\t" \
    "v_min_u32 v21, 63, v21\n\t" \
    "v_mul_u32_u24 v21, v21, v12\n\t" \
    "v_xor_b32 v21, v21, v5\n\t" \
    "v_lshrrev_b32 v22, 15, v13\n\t" \
    "v_add3_u32 v22, v22, s65, 1\n\t" \
    "v_min_u32 v22, 63, v22\n\t" \
    "v_mul_u32_u24 v22, v22, v13\n\t" \
    "v_xor_b32 v22, v22, v6\n\t" \
    "v_lshrrev_b32 v23, 15, v14\n\t" \
    "v_add3_u32 v23, v23, s65, 1\n\t" \
    "v_min_u32 v23, 63, v23\n\t" \
    "v_mul_u32_u24 v23, v23, v14\n\t" \
    "v_xor_b32 v23, v23, v7\n\t" \
    "v_lshrrev_b32 v24, 15, v15\n\t" \
    "v_add3_u32 v24, v24, s65, 1\n\t" \
    "v_min_u32 v24, 63, v24\n\t" \
    "v_mul_u32_u24 v24, v24, v15\n\t" \
    "v_xor_b32 v24, v24, v8\n\t" \
    "v_lshrrev_b32 v25, 15, v16\n\t" \
    "v_add3_u32 v25, v25, s65, 1\n\t" \
    "v_min_u32 v25, 63, v25\n\t" \
    "v_mul_u32_u24 v25, v25, v16\n\t" \
    "v_xor_b32 v25, v25, v9\n\t" \
    "v_lshrrev_b32 v26, 15, v17\n\t" \
    "v_add3_u32 v26, v26, s65, 1\n\t" \
    "v_min_u32 v26, 63, v26\n\t" \
    "v_mul_u32_u24 v26, v26, v17\n\t" \
    "v_xor_b32 v26, v26, v10\n\t" \
    "s_add_u32 s71, s79, s96\n\t" \
    "s_sub_u32 s70, s71, s74\n\t" \
    "s_max_i32 s70, s70, 0\n\t" \
    "s_min_i32 s70, s70, 16000\n\t" \
    "s_lshl_b32 s70, s70, 1\n\t" \
    "s_mul_hi_u32 s70, s70, s77\n\t" \
    "s_add_u32 s70, s70, s73\n\t" \
    "s_getpc_b64 s[30:31]\n\t" \
    "hh_Wp22_%=:\n\t" \
    "s_add_u32 s30, s30, hh_We23_%=-hh_Wp22_%=\n\t" \
    "s_addc_u32 s31, s31, 0\n\t" \
    "s_sub_u32 s70, s70, 96\n\t" \
    "hh_W21_%=:\n\t" \
    "s_memtime s[56:57]\n\t" \
    "s_waitcnt lgkmcnt(0)\n\t" \
    "s_sub_u32 s69, s70, s56\n\t" \
    "s_cmp_gt_i32 s69, 80\n\t" \
    "s_cbranch_scc1 hh_W21_%=\n\t" \
    "s_max_i32 s69, s69, 0\n\t" \
    "s_and_b32 s69, s69, -4\n\t" \
    "s_sub_u32 s30, s30, s69\n\t" \
    "s_subb_u32 s31, s31, 0\n\t" \
    "s_setpc_b64 s[30:31]\n\t" \
    "s_nop 0\n\t" \
    "s_nop 0\n\t" \
    "s_nop 0\n\t" \
    "s_nop 0\n\t" \
    "s_nop 0\n\t" \
    "s_nop 0\n\t" \
    "s_nop 0\n\t" \
    "s_nop 0\n\t" \
    "s_nop 0\n\t" \
    "s_nop 0\n\t" \
    "s_nop 0\n\t" \
    "s_nop 0\n\t" \
    "s_nop 0\n\t" \
    "s_nop 0\n\t" \
    "s_nop 0\n\t" \
    "s_nop 0\n\t" \
    "s_nop 0\n\t" \
    "s_nop 0\n\t" \
    "s_nop 0\n\t" \
    "s_nop 0\n\t" \
    "hh_We23_%=:\n\t" \
    "global_load_lds_dword v26, s[36:37] sc1\n\t" \
    "s_sleep 2\n\t" \
    "s_nop 5\n\t" \
    "global_load_lds_dword v25, s[36:37] sc1\n\t" \
    "s_sleep 2\n\t" \
    "s_nop 5\n\t" \
    "global_load_lds_dword v24, s[36:37] sc1\n\t" \
    "s_sleep 2\n\t" \
    "s_nop 5\n\t" \
    "global_load_lds_dword v23, s[36:37] sc1\n\t" \
    "s_sleep 2\n\t" \
    "s_nop 5\n\t" \
    "global_load_lds_dword v22, s[36:37] sc1\n\t" \
    "s_sleep 2\n\t" \
    "s_nop 5\n\t" \
    "global_load_lds_dword v21, s[36:37] sc1\n\t" \
    "s_sleep 2\n\t" \
    "s_nop 5\n\t" \
    "global_load_lds_dword v20, s[36:37] sc1\n\t" \
    "s_branch hh_loop8_%=\n\t" \
    "hh_end10_%=:\n\t" \
    "s_waitcnt vmcnt(0)\n\t" \
    "s_endpgm\n\t" \
    "hh_last_%=:\n\t" \
    ""
#define ASM_GX \
    "s_load_dwordx2 s[42:43], %3, 48\n\t" \
    "s_load_dwordx2 s[44:45], %3, 40\n\t" \
    "s_load_dwordx2 s[36:37], %3, 0x58\n\t" \
    "s_load_dwordx2 s[50:51], %3, 0x48\n\t" \
    "s_mov_b32 s66, %4\n\t" \
    "s_mov_b32 s67, %5\n\t" \
    "s_mov_b32 s68, 262144\n\t" \
    "s_mov_b32 s72, 0\n\t" \
    "s_mov_b32 s83, -1\n\t" \
    "s_mov_b32 s65, 0\n\t" \
    "s_lshl_b32 s82, s66, 10\n\t" \
    "s_waitcnt lgkmcnt(0)\n\t" \
    "s_mul_i32 s89, s66, 9216\n\t" \
    "s_mov_b32 s90, 0\n\t" \
    "s_add_u32 s46, s42, 2097152\n\t" \
    "s_addc_u32 s47, s43, 0\n\t" \
    "s_add_u32 m0, s82, 65536\n\t" \
    "s_nop 0\n\t" \
    "global_load_lds_dwordx4 v200, s[46:47]\n\t" \
    "s_add_u32 s46, s42, 2228224\n\t" \
    "s_addc_u32 s47, s43, 0\n\t" \
    "s_add_u32 m0, s82, 69632\n\t" \
    "s_nop 0\n\t" \
    "global_load_lds_dwordx4 v200, s[46:47]\n\t" \
    "s_add_u32 s46, s42, 2359296\n\t" \
    "s_addc_u32 s47, s43, 0\n\t" \
    "s_add_u32 m0, s82, 73728\n\t" \
    "s_nop 0\n\t" \
    "global_load_lds_dwordx4 v200, s[46:47]\n\t" \
    "s_add_u32 s46, s42, 2490368\n\t" \
    "s_addc_u32 s47, s43, 0\n\t" \
    "s_add_u32 m0, s82, 77824\n\t" \
    "s_nop 0\n\t" \
    "global_load_lds_dwordx4 v200, s[46:47]\n\t" \
    "s_add_u32 s46, s42, 2621440\n\t" \
    "s_addc_u32 s47, s43, 0\n\t" \
    "s_add_u32 m0, s82, 81920\n\t" \
    "s_nop 0\n\t" \
    "global_load_lds_dwordx4 v200, s[46:47]\n\t" \
    "s_add_u32 s46, s42, 2752512\n\t" \
    "s_addc_u32 s47, s43, 0\n\t" \
    "s_add_u32 m0, s82, 86016\n\t" \
    "s_nop 0\n\t" \
    "global_load_lds_dwordx4 v200, s[46:47]\n\t" \
    "s_add_u32 s46, s42, 2883584\n\t" \
    "s_addc_u32 s47, s43, 0\n\t" \
    "s_add_u32 m0, s82, 90112\n\t" \
    "s_nop 0\n\t" \
    "global_load_lds_dwordx4 v200, s[46:47]\n\t" \
    "s_add_u32 s46, s42, 3014656\n\t" \
    "s_addc_u32 s47, s43, 0\n\t" \
    "s_add_u32 m0, s82, 94208\n\t" \
    "s_nop 0\n\t" \
    "global_load_lds_dwordx4 v200, s[46:47]\n\t" \
    "s_add_u32 s46, s42, 3145728\n\t" \
    "s_addc_u32 s47, s43, 0\n\t" \
    "s_add_u32 m0, s82, 98304\n\t" \
    "s_nop 0\n\t" \
    "global_load_lds_dwordx4 v200, s[46:47]\n\t" \
    "s_add_u32 s46, s42, 3276800\n\t" \
    "s_addc_u32 s47, s43, 0\n\t" \
    "s_add_u32 m0, s82, 102400\n\t" \
    "s_nop 0\n\t" \
    "global_load_lds_dwordx4 v200, s[46:47]\n\t" \
    "s_add_u32 s46, s42, 3407872\n\t" \
    "s_addc_u32 s47, s43, 0\n\t" \
    "s_add_u32 m0, s82, 106496\n\t" \
    "s_nop 0\n\t" \
    "global_load_lds_dwordx4 v200, s[46:47]\n\t" \
    "s_add_u32 s46, s42, 3538944\n\t" \
    "s_addc_u32 s47, s43, 0\n\t" \
    "s_add_u32 m0, s82, 110592\n\t" \
    "s_nop 0\n\t" \
    "global_load_lds_dwordx4 v200, s[46:47]\n\t" \
    "s_add_u32 s46, s42, 3670016\n\t" \
    "s_addc_u32 s47, s43, 0\n\t" \
    "s_add_u32 m0, s82, 114688\n\t" \
    "s_nop 0\n\t" \
    "global_load_lds_dwordx4 v200, s[46:47]\n\t" \
    "s_add_u32 s46, s42, 3801088\n\t" \
    "s_addc_u32 s47, s43, 0\n\t" \
    "s_add_u32 m0, s82, 118784\n\t" \
    "s_nop 0\n\t" \
    "global_load_lds_dwordx4 v200, s[46:47]\n\t" \
    "s_add_u32 s46, s42, 3932160\n\t" \
    "s_addc_u32 s47, s43, 0\n\t" \
    "s_add_u32 m0, s82, 122880\n\t" \
    "s_nop 0\n\t" \
    "global_load_lds_dwordx4 v200, s[46:47]\n\t" \
    "s_add_u32 s46, s42, 4063232\n\t" \
    "s_addc_u32 s47, s43, 0\n\t" \
    "s_add_u32 m0, s82, 126976\n\t" \
    "s_nop 0\n\t" \
    "global_load_lds_dwordx4 v200, s[46:47]\n\t" \
    "s_add_u32 s46, s42, 4194304\n\t" \
    "s_addc_u32 s47, s43, 0\n\t" \
    "s_add_u32 m0, s82, 131072\n\t" \
    "s_nop 0\n\t" \
    "global_load_lds_dwordx4 v200, s[46:47]\n\t" \
    "s_add_u32 s46, s42, 4325376\n\t" \
    "s_addc_u32 s47, s43, 0\n\t" \
    "s_add_u32 m0, s82, 135168\n\t" \
    "s_nop 0\n\t" \
    "global_load_lds_dwordx4 v200, s[46:47]\n\t" \
    "s_add_u32 s46, s42, 4456448\n\t" \
    "s_addc_u32 s47, s43, 0\n\t" \
    "s_add_u32 m0, s82, 139264\n\t" \
    "s_nop 0\n\t" \
    "global_load_lds_dwordx4 v200, s[46:47]\n\t" \
    "s_add_u32 s46, s42, 4587520\n\t" \
    "s_addc_u32 s47, s43, 0\n\t" \
    "s_add_u32 m0, s82, 143360\n\t" \
    "s_nop 0\n\t" \
    "global_load_lds_dwordx4 v200, s[46:47]\n\t" \
    "global_load_dwordx4 v[120:123], v203, s[50:51]\n\t" \
    "global_load_dwordx4 v[124:127], v204, s[50:51]\n\t" \
    "global_load_dwordx4 v[128:131], v205, s[50:51]\n\t" \
    "global_load_dwordx4 v[132:135], v206, s[50:51]\n\t" \
    "global_load_dwordx4 v[136:139], v207, s[50:51]\n\t" \
    "global_load_dwordx4 v[140:143], v208, s[50:51]\n\t" \
    "global_load_dwordx4 v[144:147], v209, s[50:51]\n\t" \
    "global_load_dwordx4 v[148:151], v210, s[50:51]\n\t" \
    "global_load_dwordx4 v[152:155], v211, s[50:51]\n\t" \
    "s_add_u32 s52, s50, 36864\n\t" \
    "s_addc_u32 s53, s51, 0\n\t" \
    "global_load_dwordx4 v[72:75], v203, s[52:53]\n\t" \
    "global_load_dwordx4 v[76:79], v204, s[52:53]\n\t" \
    "global_load_dwordx4 v[80:83], v205, s[52:53]\n\t" \
    "global_load_dwordx4 v[84:87], v206, s[52:53]\n\t" \
    "global_load_dwordx4 v[88:91], v207, s[52:53]\n\t" \
    "global_load_dwordx4 v[92:95], v208, s[52:53]\n\t" \
    "global_load_dwordx4 v[96:99], v209, s[52:53]\n\t" \
    "global_load_dwordx4 v[100:103], v210, s[52:53]\n\t" \
    "global_load_dwordx4 v[104:107], v211, s[52:53]\n\t" \
    "s_waitcnt vmcnt(50) lgkmcnt(0)\n\t" \
    "s_barrier\n\t" \
    "ds_read_b64_tr_b16 a[144:145], v201 offset:0\n\t" \
    "ds_read_b64_tr_b16 a[146:147], v201 offset:1024\n\t" \
    "ds_read_b64_tr_b16 a[148:149], v201 offset:8192\n\t" \
    "ds_read_b64_tr_b16 a[150:151], v201 offset:9216\n\t" \
    "ds_read_b64_tr_b16 a[216:217], v202 offset:0\n\t" \
    "ds_read_b64_tr_b16 a[218:219], v202 offset:1024\n\t" \
    "ds_read_b64_tr_b16 a[220:221], v202 offset:8192\n\t" \
    "ds_read_b64_tr_b16 a[222:223], v202 offset:9216\n\t" \
    "s_waitcnt vmcnt(46) lgkmcnt(0)\n\t" \
    "s_barrier\n\t" \
    "ds_read_b64_tr_b16 a[152:153], v201 offset:16384\n\t" \
    "ds_read_b64_tr_b16 a[154:155], v201 offset:17408\n\t" \
    "ds_read_b64_tr_b16 a[156:157], v201 offset:24576\n\t" \
    "ds_read_b64_tr_b16 a[158:159], v201 offset:25600\n\t" \
    "ds_read_b64_tr_b16 a[224:225], v202 offset:16384\n\t" \
    "ds_read_b64_tr_b16 a[226:227], v202 offset:17408\n\t" \
    "ds_read_b64_tr_b16 a[228:229], v202 offset:24576\n\t" \
    "ds_read_b64_tr_b16 a[230:231], v202 offset:25600\n\t" \
    "s_waitcnt vmcnt(42) lgkmcnt(0)\n\t" \
    "s_barrier\n\t" \
    "ds_read_b64_tr_b16 a[160:161], v201 offset:32768\n\t" \
    "ds_read_b64_tr_b16 a[162:163], v201 offset:33792\n\t" \
    "ds_read_b64_tr_b16 a[164:165], v201 offset:40960\n\t" \
    "ds_read_b64_tr_b16 a[166:167], v201 offset:41984\n\t" \
    "ds_read_b64_tr_b16 a[232:233], v202 offset:32768\n\t" \
    "ds_read_b64_tr_b16 a[234:235], v202 offset:33792\n\t" \
    "ds_read_b64_tr_b16 a[236:237], v202 offset:40960\n\t" \
    "ds_read_b64_tr_b16 a[238:239], v202 offset:41984\n\t" \
    "s_waitcnt vmcnt(38) lgkmcnt(0)\n\t" \
    "s_barrier\n\t" \
    "ds_read_b64_tr_b16 a[168:169], v201 offset:49152\n\t" \
    "ds_read_b64_tr_b16 a[170:171], v201 offset:50176\n\t" \
    "ds_read_b64_tr_b16 a[172:173], v201 offset:57344\n\t" \
    "ds_read_b64_tr_b16 a[174:175], v201 offset:58368\n\t" \
    "ds_read_b64_tr_b16 a[240:241], v202 offset:49152\n\t" \
    "ds_read_b64_tr_b16 a[242:243], v202 offset:50176\n\t" \
    "ds_read_b64_tr_b16 a[244:245], v202 offset:57344\n\t" \
    "ds_read_b64_tr_b16 a[246:247], v202 offset:58368\n\t" \
    "v_mbcnt_lo_u32_b32 v164, -1, 0\n\t" \
    "v_mbcnt_hi_u32_b32 v164, -1, v164\n\t" \
    "v_lshlrev_b32 v164, 6, v164\n\t" \
    "v_add_u32 v164, 6225920, v164\n\t" \
    "v_mov_b32 v165, 6234112\n\t" \
    "global_load_dword v165, v165, s[36:37] sc1\n\t" \
    "global_load_dwordx2 v[166:167], v164, s[36:37] sc1\n\t" \
    "global_load_dword v168, v164, s[36:37] offset:2048 sc1\n\t" \
    "s_waitcnt vmcnt(37) lgkmcnt(0)\n\t" \
    "s_barrier\n\t" \
    "v_add_u32 v201, 0x10000, v201\n\t" \
    "v_add_u32 v202, 0x10000, v202\n\t" \
    "ds_read_b64_tr_b16 a[176:177], v201 offset:0\n\t" \
    "ds_read_b64_tr_b16 a[178:179], v201 offset:1024\n\t" \
    "ds_read_b64_tr_b16 a[180:181], v201 offset:8192\n\t" \
    "ds_read_b64_tr_b16 a[182:183], v201 offset:9216\n\t" \
    "ds_read_b64_tr_b16 a[248:249], v202 offset:0\n\t" \
    "ds_read_b64_tr_b16 a[250:251], v202 offset:1024\n\t" \
    "ds_read_b64_tr_b16 a[252:253], v202 offset:8192\n\t" \
    "ds_read_b64_tr_b16 a[254:255], v202 offset:9216\n\t" \
    "s_waitcnt vmcnt(33) lgkmcnt(0)\n\t" \
    "s_barrier\n\t" \
    "ds_read_b64_tr_b16 a[184:185], v201 offset:16384\n\t" \
    "ds_read_b64_tr_b16 a[186:187], v201 offset:17408\n\t" \
    "ds_read_b64_tr_b16 a[188:189], v201 offset:24576\n\t" \
    "ds_read_b64_tr_b16 a[190:191], v201 offset:25600\n\t" \
    "ds_read_b64_tr_b16 v[224:225], v202 offset:16384\n\t" \
    "ds_read_b64_tr_b16 v[226:227], v202 offset:17408\n\t" \
    "ds_read_b64_tr_b16 v[228:229], v202 offset:24576\n\t" \
    "ds_read_b64_tr_b16 v[230:231], v202 offset:25600\n\t" \
    "s_waitcnt vmcnt(29) lgkmcnt(0)\n\t" \
    "s_barrier\n\t" \
    "ds_read_b64_tr_b16 a[192:193], v201 offset:32768\n\t" \
    "ds_read_b64_tr_b16 a[194:195], v201 offset:33792\n\t" \
    "ds_read_b64_tr_b16 a[196:197], v201 offset:40960\n\t" \
    "ds_read_b64_tr_b16 a[198:199], v201 offset:41984\n\t" \
    "ds_read_b64_tr_b16 v[232:233], v202 offset:32768\n\t" \
    "ds_read_b64_tr_b16 v[234:235], v202 offset:33792\n\t" \
    "ds_read_b64_tr_b16 v[236:237], v202 offset:40960\n\t" \
    "ds_read_b64_tr_b16 v[238:239], v202 offset:41984\n\t" \
    "s_waitcnt vmcnt(25) lgkmcnt(0)\n\t" \
    "s_barrier\n\t" \
    "ds_read_b64_tr_b16 a[200:201], v201 offset:49152\n\t" \
    "ds_read_b64_tr_b16 a[202:203], v201 offset:50176\n\t" \
    "ds_read_b64_tr_b16 a[204:205], v201 offset:57344\n\t" \
    "ds_read_b64_tr_b16 a[206:207], v201 offset:58368\n\t" \
    "ds_read_b64_tr_b16 v[240:241], v202 offset:49152\n\t" \
    "ds_read_b64_tr_b16 v[242:243], v202 offset:50176\n\t" \
    "ds_read_b64_tr_b16 v[244:245], v202 offset:57344\n\t" \
    "ds_read_b64_tr_b16 v[246:247], v202 offset:58368\n\t" \
    "s_waitcnt vmcnt(21) lgkmcnt(0)\n\t" \
    "s_barrier\n\t" \
    "v_add_u32 v201, 0x10000, v201\n\t" \
    "v_add_u32 v202, 0x10000, v202\n\t" \
    "ds_read_b64_tr_b16 a[208:209], v201 offset:0\n\t" \
    "ds_read_b64_tr_b16 a[210:211], v201 offset:1024\n\t" \
    "ds_read_b64_tr_b16 a[212:213], v201 offset:8192\n\t" \
    "ds_read_b64_tr_b16 a[214:215], v201 offset:9216\n\t" \
    "ds_read_b64_tr_b16 v[248:249], v202 offset:0\n\t" \
    "ds_read_b64_tr_b16 v[250:251], v202 offset:1024\n\t" \
    "ds_read_b64_tr_b16 v[252:253], v202 offset:8192\n\t" \
    "ds_read_b64_tr_b16 v[254:255], v202 offset:9216\n\t" \
    "v_add_u32 v201, 0xfffe0000, v201\n\t" \
    "v_add_u32 v202, 0xfffe0000, v202\n\t" \
    "s_waitcnt lgkmcnt(0)\n\t" \
    "s_barrier\n\t" \
    "s_waitcnt vmcnt(0)\n\t" \
    "ds_write_b128 v3, v[120:123] offset:0\n\t" \
    "ds_write_b128 v3, v[124:127] offset:1024\n\t" \
    "ds_write_b128 v3, v[128:131] offset:2048\n\t" \
    "ds_write_b128 v3, v[132:135] offset:3072\n\t" \
    "ds_write_b128 v3, v[136:139] offset:4096\n\t" \
    "ds_write_b128 v3, v[140:143] offset:5120\n\t" \
    "ds_write_b128 v3, v[144:147] offset:6144\n\t" \
    "ds_write_b128 v3, v[148:151] offset:7168\n\t" \
    "ds_write_b128 v3, v[152:155] offset:8192\n\t" \
    "ds_write_b128 v3, v[72:75] offset:36864\n\t" \
    "ds_write_b128 v3, v[76:79] offset:37888\n\t" \
    "ds_write_b128 v3, v[80:83] offset:38912\n\t" \
    "ds_write_b128 v3, v[84:87] offset:39936\n\t" \
    "ds_write_b128 v3, v[88:91] offset:40960\n\t" \
    "ds_write_b128 v3, v[92:95] offset:41984\n\t" \
    "ds_write_b128 v3, v[96:99] offset:43008\n\t" \
    "ds_write_b128 v3, v[100:103] offset:44032\n\t" \
    "ds_write_b128 v3, v[104:107] offset:45056\n\t" \
    "gx_C1_%=:\n\t" \
    "v_cmp_ne_u32 s[54:55], v166, v168\n\t" \
    "v_sub_u32 v166, v166, v165\n\t" \
    "v_cmp_ne_u32 vcc, 1, v166\n\t" \
    "s_nop 1\n\t" \
    "s_or_b64 vcc, vcc, s[54:55]\n\t" \
    "s_cbranch_scc0 gx_Cok2_%=\n\t" \
    "s_cmp_eq_u32 s68, 0\n\t" \
    "s_cbranch_scc1 gx_F4_%=\n\t" \
    "s_sub_u32 s68, s68, 1\n\t" \
    "s_sleep 6\n\t" \
    "s_branch gx_Cag3_%=\n\t" \
    "gx_F4_%=:\n\t" \
    "v_mov_b32 v168, 5767424\n\t" \
    "v_mov_b32 v169, 4\n\t" \
    "global_store_dword v168, v169, s[36:37] sc1\n\t" \
    "s_waitcnt vmcnt(0)\n\t" \
    "s_branch gx_Cok2_%=\n\t" \
    "gx_Cag3_%=:\n\t" \
    "global_load_dwordx2 v[166:167], v164, s[36:37] sc1\n\t" \
    "global_load_dword v168, v164, s[36:37] offset:2048 sc1\n\t" \
    "s_waitcnt vmcnt(0)\n\t" \
    "s_branch gx_C1_%=\n\t" \
    "gx_Cok2_%=:\n\t" \
    "v_readlane_b32 s69, v167, 2\n\t" \
    "v_readlane_b32 s70, v167, 3\n\t" \
    "s_nop 1\n\t" \
    "s_cmp_lt_u32 s70, s69\n\t" \
    "s_cselect_b32 s69, 0, 8192\n\t" \
    "s_add_u32 s100, s36, s69\n\t" \
    "s_addc_u32 s101, s37, 0\n\t" \
    "s_cmp_lg_u32 s66, 0\n\t" \
    "s_cbranch_scc1 gx_Ca5_%=\n\t" \
    "v_mov_b32 v164, 5768192\n\t" \
    "s_mov_b64 exec, 1\n\t" \
    "global_atomic_add v164, v166, s[36:37] sc1\n\t" \
    "s_mov_b64 exec, -1\n\t" \
    "gx_Ca5_%=:\n\t" \
    "s_waitcnt lgkmcnt(0)\n\t" \
    "s_barrier\n\t" \
    "ds_read_b128 v[24:27], v1 offset:0\n\t" \
    "ds_read_b128 v[28:31], v1 offset:18432\n\t" \
    "ds_read_b128 v[32:35], v2 offset:0\n\t" \
    "ds_read_b128 v[36:39], v2 offset:18432\n\t" \
    "ds_read_b128 v[40:43], v1 offset:128\n\t" \
    "ds_read_b128 v[44:47], v1 offset:18560\n\t" \
    "gx_l16_%=:\n\t" \
    "s_add_u32 s84, s65, 2\n\t" \
    "s_min_u32 s69, s84, 63\n\t" \
    "s_mul_i32 s69, s69, 36864\n\t" \
    "s_add_u32 s52, s50, s69\n\t" \
    "s_addc_u32 s53, s51, 0\n\t" \
    "s_add_u32 s90, s90, 36864\n\t" \
    "s_cmp_eq_u32 s90, 147456\n\t" \
    "s_cselect_b32 s90, 0, s90\n\t" \
    "s_add_u32 s91, s90, 36864\n\t" \
    "s_cmp_eq_u32 s91, 147456\n\t" \
    "s_cselect_b32 s91, 0, s91\n\t" \
    "s_add_u32 s91, s91, s89\n\t" \
    "v_add_u32 v22, s90, v0\n\t" \
    "v_xor_b32 v23, 64, v22\n\t" \
    "s_mul_i32 s70, s67, 7\n\t" \
    "s_add_u32 s69, s65, 0\n\t" \
    "s_xor_b32 s69, s69, s70\n\t" \
    "s_and_b32 s69, s69, 63\n\t" \
    "s_lshl_b32 s69, s69, 13\n\t" \
    "s_mul_i32 s70, s67, 524288\n\t" \
    "s_add_u32 s69, s69, s70\n\t" \
    "s_add_u32 s69, s69, 23068672\n\t" \
    "s_add_u32 s40, s36, s69\n\t" \
    "s_addc_u32 s41, s37, 0\n\t" \
    "s_waitcnt lgkmcnt(5)\n\t" \
    "v_mfma_f32_16x16x32_bf16 v[72:75], a[144:147], v[24:27], 0\n\t" \
    "ds_read_b128 v[48:51], v2 offset:128\n\t" \
    "s_waitcnt lgkmcnt(5)\n\t" \
    "v_mfma_f32_16x16x32_bf16 v[76:79], a[144:147], v[28:31], 0\n\t" \
    "ds_read_b128 v[52:55], v2 offset:18560\n\t" \
    "v_mfma_f32_16x16x32_bf16 v[80:83], a[216:219], v[24:27], 0\n\t" \
    "s_add_u32 m0, s91, 0\n\t" \
    "v_mfma_f32_16x16x32_bf16 v[84:87], a[216:219], v[28:31], 0\n\t" \
    "global_load_lds_dwordx4 v203, s[52:53]\n\t" \
    "s_waitcnt lgkmcnt(5)\n\t" \
    "v_mfma_f32_16x16x32_bf16 v[72:75], a[148:151], v[32:35], v[72:75]\n\t" \
    "ds_read_b128 v[56:59], v1 offset:256\n\t" \
    "s_waitcnt lgkmcnt(5)\n\t" \
    "v_mfma_f32_16x16x32_bf16 v[76:79], a[148:151], v[36:39], v[76:79]\n\t" \
    "ds_read_b128 v[60:63], v1 offset:18688\n\t" \
    "v_mfma_f32_16x16x32_bf16 v[80:83], a[220:223], v[32:35], v[80:83]\n\t" \
    "s_add_u32 m0, s91, 1024\n\t" \
    "v_mfma_f32_16x16x32_bf16 v[84:87], a[220:223], v[36:39], v[84:87]\n\t" \
    "global_load_lds_dwordx4 v204, s[52:53]\n\t" \
    "s_waitcnt lgkmcnt(5)\n\t" \
    "v_mfma_f32_16x16x32_bf16 v[72:75], a[152:155], v[40:43], v[72:75]\n\t" \
    "ds_read_b128 v[64:67], v2 offset:256\n\t" \
    "s_waitcnt lgkmcnt(5)\n\t" \
    "v_mfma_f32_16x16x32_bf16 v[76:79], a[152:155], v[44:47], v[76:79]\n\t" \
    "ds_read_b128 v[68:71], v2 offset:18688\n\t" \
    "v_mfma_f32_16x16x32_bf16 v[80:83], a[224:227], v[40:43], v[80:83]\n\t" \
    "s_add_u32 m0, s91, 2048\n\t" \
    "v_mfma_f32_16x16x32_bf16 v[84:87], a[224:227], v[44:47], v[84:87]\n\t" \
    "global_load_lds_dwordx4 v205, s[52:53]\n\t" \
    "s_waitcnt lgkmcnt(5)\n\t" \
    "v_mfma_f32_16x16x32_bf16 v[72:75], a[156:159], v[48:51], v[72:75]\n\t" \
    "ds_read_b128 v[24:27], v1 offset:384\n\t" \
    "s_waitcnt lgkmcnt(5)\n\t" \
    "v_mfma_f32_16x16x32_bf16 v[76:79], a[156:159], v[52:55], v[76:79]\n\t" \
    "ds_read_b128 v[28:31], v1 offset:18816\n\t" \
    "v_mfma_f32_16x16x32_bf16 v[80:83], a[228:231], v[48:51], v[80:83]\n\t" \
    "s_add_u32 m0, s91, 3072\n\t" \
    "v_mfma_f32_16x16x32_bf16 v[84:87], a[228:231], v[52:55], v[84:87]\n\t" \
    "global_load_lds_dwordx4 v206, s[52:53]\n\t" \
    "s_waitcnt lgkmcnt(5)\n\t" \
    "v_mfma_f32_16x16x32_bf16 v[72:75], a[160:163], v[56:59], v[72:75]\n\t" \
    "ds_read_b128 v[32:35], v2 offset:384\n\t" \
    "s_waitcnt lgkmcnt(5)\n\t" \
    "v_mfma_f32_16x16x32_bf16 v[76:79], a[160:163], v[60:63], v[76:79]\n\t" \
    "ds_read_b128 v[36:39], v2 offset:18816\n\t" \
    "v_mfma_f32_16x16x32_bf16 v[80:83], a[232:235], v[56:59], v[80:83]\n\t" \
    "s_add_u32 m0, s91, 4096\n\t" \
    "v_mfma_f32_16x16x32_bf16 v[84:87], a[232:235], v[60:63], v[84:87]\n\t" \
    "global_load_lds_dwordx4 v207, s[52:53]\n\t" \
    "s_waitcnt lgkmcnt(5)\n\t" \
    "v_mfma_f32_16x16x32_bf16 v[72:75], a[164:167], v[64:67], v[72:75]\n\t" \
    "ds_read_b128 v[40:43], v1 offset:512\n\t" \
    "s_waitcnt lgkmcnt(5)\n\t" \
    "v_mfma_f32_16x16x32_bf16 v[76:79], a[164:167], v[68:71], v[76:79]\n\t" \
    "ds_read_b128 v[44:47], v1 offset:18944\n\t" \
    "v_mfma_f32_16x16x32_bf16 v[80:83], a[236:239], v[64:67], v[80:83]\n\t" \
    "s_add_u32 m0, s91, 5120\n\t" \
    "v_mfma_f32_16x16x32_bf16 v[84:87], a[236:239], v[68:71], v[84:87]\n\t" \
    "global_load_lds_dwordx4 v208, s[52:53]\n\t" \
    "s_waitcnt lgkmcnt(5)\n\t" \
    "v_mfma_f32_16x16x32_bf16 v[72:75], a[168:171], v[24:27], v[72:75]\n\t" \
    "ds_read_b128 v[48:51], v2 offset:512\n\t" \
    "s_waitcnt lgkmcnt(5)\n\t" \
    "v_mfma_f32_16x16x32_bf16 v[76:79], a[168:171], v[28:31], v[76:79]\n\t" \
    "ds_read_b128 v[52:55], v2 offset:18944\n\t" \
    "v_mfma_f32_16x16x32_bf16 v[80:83], a[240:243], v[24:27], v[80:83]\n\t" \
    "s_add_u32 m0, s91, 6144\n\t" \
    "v_mfma_f32_16x16x32_bf16 v[84:87], a[240:243], v[28:31], v[84:87]\n\t" \
    "global_load_lds_dwordx4 v209, s[52:53]\n\t" \
    "s_waitcnt lgkmcnt(5)\n\t" \
    "v_mfma_f32_16x16x32_bf16 v[72:75], a[172:175], v[32:35], v[72:75]\n\t" \
    "ds_read_b128 v[56:59], v1 offset:640\n\t" \
    "s_waitcnt lgkmcnt(5)\n\t" \
    "v_mfma_f32_16x16x32_bf16 v[76:79], a[172:175], v[36:39], v[76:79]\n\t" \
    "ds_read_b128 v[60:63], v1 offset:19072\n\t" \
    "v_mfma_f32_16x16x32_bf16 v[80:83], a[244:247], v[32:35], v[80:83]\n\t" \
    "s_add_u32 m0, s91, 7168\n\t" \
    "v_mfma_f32_16x16x32_bf16 v[84:87], a[244:247], v[36:39], v[84:87]\n\t" \
    "global_load_lds_dwordx4 v210, s[52:53]\n\t" \
    "s_waitcnt lgkmcnt(5)\n\t" \
    "v_mfma_f32_16x16x32_bf16 v[72:75], a[176:179], v[40:43], v[72:75]\n\t" \
    "ds_read_b128 v[64:67], v2 offset:640\n\t" \
    "s_waitcnt lgkmcnt(5)\n\t" \
    "v_mfma_f32_16x16x32_bf16 v[76:79], a[176:179], v[44:47], v[76:79]\n\t" \
    "ds_read_b128 v[68:71], v2 offset:19072\n\t" \
    "v_mfma_f32_16x16x32_bf16 v[80:83], a[248:251], v[40:43], v[80:83]\n\t" \
    "s_add_u32 m0, s91, 8192\n\t" \
    "v_mfma_f32_16x16x32_bf16 v[84:87], a[248:251], v[44:47], v[84:87]\n\t" \
    "global_load_lds_dwordx4 v211, s[52:53]\n\t" \
    "s_waitcnt lgkmcnt(5)\n\t" \
    "v_mfma_f32_16x16x32_bf16 v[72:75], a[180:183], v[48:51], v[72:75]\n\t" \
    "ds_read_b128 v[24:27], v1 offset:768\n\t" \
    "s_waitcnt lgkmcnt(5)\n\t" \
    "v_mfma_f32_16x16x32_bf16 v[76:79], a[180:183], v[52:55], v[76:79]\n\t" \
    "ds_read_b128 v[28:31], v1 offset:19200\n\t" \
    "v_mfma_f32_16x16x32_bf16 v[80:83], a[252:255], v[48:51], v[80:83]\n\t" \
    "v_mfma_f32_16x16x32_bf16 v[84:87], a[252:255], v[52:55], v[84:87]\n\t" \
    "s_waitcnt lgkmcnt(5)\n\t" \
    "v_mfma_f32_16x16x32_bf16 v[72:75], a[184:187], v[56:59], v[72:75]\n\t" \
    "ds_read_b128 v[32:35], v2 offset:768\n\t" \
    "s_waitcnt lgkmcnt(5)\n\t" \
    "v_mfma_f32_16x16x32_bf16 v[76:79], a[184:187], v[60:63], v[76:79]\n\t" \
    "ds_read_b128 v[36:39], v2 offset:19200\n\t" \
    "v_mfma_f32_16x16x32_bf16 v[80:83], v[224:227], v[56:59], v[80:83]\n\t" \
    "s_waitcnt vmcnt(11)\n\t" \
    "s_barrier\n\t" \
    "v_mfma_f32_16x16x32_bf16 v[84:87], v[224:227], v[60:63], v[84:87]\n\t" \
    "s_sub_u32 s71, s65, 1\n\t" \
    "s_min_i32 s71, s71, 0\n\t" \
    "s_sub_u32 s69, s65, 2\n\t" \
    "s_max_i32 s71, s71, s69\n\t" \
    "s_cmp_lg_u32 s66, 0\n\t" \
    "s_cbranch_scc1 gx_SF9_%=\n\t" \
    "s_lshl_b32 s69, s67, 7\n\t" \
    "s_add_u32 s69, s69, 3149824\n\t" \
    "s_add_u32 s70, s71, 0\n\t" \
    "v_mov_b32 v156, s69\n\t" \
    "v_mov_b32 v157, s70\n\t" \
    "s_mov_b64 exec, 1\n\t" \
    "global_store_dword v156, v157, s[100:101] sc1\n\t" \
    "s_mov_b64 exec, -1\n\t" \
    "gx_SF9_%=:\n\t" \
    "s_waitcnt lgkmcnt(5)\n\t" \
    "v_mfma_f32_16x16x32_bf16 v[72:75], a[188:191], v[64:67], v[72:75]\n\t" \
    "ds_read_b128 v[40:43], v1 offset:896\n\t" \
    "s_waitcnt lgkmcnt(5)\n\t" \
    "v_mfma_f32_16x16x32_bf16 v[76:79], a[188:191], v[68:71], v[76:79]\n\t" \
    "ds_read_b128 v[44:47], v1 offset:19328\n\t" \
    "v_mfma_f32_16x16x32_bf16 v[80:83], v[228:231], v[64:67], v[80:83]\n\t" \
    "v_mfma_f32_16x16x32_bf16 v[84:87], v[228:231], v[68:71], v[84:87]\n\t" \
    "s_waitcnt lgkmcnt(5)\n\t" \
    "v_mfma_f32_16x16x32_bf16 v[72:75], a[192:195], v[24:27], v[72:75]\n\t" \
    "ds_read_b128 v[48:51], v2 offset:896\n\t" \
    "s_waitcnt lgkmcnt(5)\n\t" \
    "v_mfma_f32_16x16x32_bf16 v[76:79], a[192:195], v[28:31], v[76:79]\n\t" \
    "ds_read_b128 v[52:55], v2 offset:19328\n\t" \
    "v_mfma_f32_16x16x32_bf16 v[80:83], v[232:235], v[24:27], v[80:83]\n\t" \
    "v_mfma_f32_16x16x32_bf16 v[84:87], v[232:235], v[28:31], v[84:87]\n\t" \
    "s_waitcnt lgkmcnt(5)\n\t" \
    "v_mfma_f32_16x16x32_bf16 v[72:75], a[196:199], v[32:35], v[72:75]\n\t" \
    "ds_read_b128 v[56:59], v1 offset:1024\n\t" \
    "s_waitcnt lgkmcnt(5)\n\t" \
    "v_mfma_f32_16x16x32_bf16 v[76:79], a[196:199], v[36:39], v[76:79]\n\t" \
    "ds_read_b128 v[60:63], v1 offset:19456\n\t" \
    "v_mfma_f32_16x16x32_bf16 v[80:83], v[236:239], v[32:35], v[80:83]\n\t" \
    "v_mfma_f32_16x16x32_bf16 v[84:87], v[236:239], v[36:39], v[84:87]\n\t" \
    "s_waitcnt lgkmcnt(5)\n\t" \
    "v_mfma_f32_16x16x32_bf16 v[72:75], a[200:203], v[40:43], v[72:75]\n\t" \
    "ds_read_b128 v[64:67], v2 offset:1024\n\t" \
    "s_waitcnt lgkmcnt(5)\n\t" \
    "v_mfma_f32_16x16x32_bf16 v[76:79], a[200:203], v[44:47], v[76:79]\n\t" \
    "ds_read_b128 v[68:71], v2 offset:19456\n\t" \
    "v_mfma_f32_16x16x32_bf16 v[80:83], v[240:243], v[40:43], v[80:83]\n\t" \
    "v_mfma_f32_16x16x32_bf16 v[84:87], v[240:243], v[44:47], v[84:87]\n\t" \
    "s_waitcnt lgkmcnt(5)\n\t" \
    "v_mfma_f32_16x16x32_bf16 v[72:75], a[204:207], v[48:51], v[72:75]\n\t" \
    "ds_read_b128 v[24:27], v22 offset:0\n\t" \
    "s_waitcnt lgkmcnt(5)\n\t" \
    "v_mfma_f32_16x16x32_bf16 v[76:79], a[204:207], v[52:55], v[76:79]\n\t" \
    "ds_read_b128 v[28:31], v22 offset:18432\n\t" \
    "v_mfma_f32_16x16x32_bf16 v[80:83], v[244:247], v[48:51], v[80:83]\n\t" \
    "v_mfma_f32_16x16x32_bf16 v[84:87], v[244:247], v[52:55], v[84:87]\n\t" \
    "s_waitcnt lgkmcnt(5)\n\t" \
    "v_mfma_f32_16x16x32_bf16 v[72:75], a[208:211], v[56:59], v[72:75]\n\t" \
    "ds_read_b128 v[32:35], v23 offset:0\n\t" \
    "s_waitcnt lgkmcnt(5)\n\t" \
    "v_mfma_f32_16x16x32_bf16 v[76:79], a[208:211], v[60:63], v[76:79]\n\t" \
    "ds_read_b128 v[36:39], v23 offset:18432\n\t" \
    "v_mfma_f32_16x16x32_bf16 v[80:83], v[248:251], v[56:59], v[80:83]\n\t" \
    "v_mfma_f32_16x16x32_bf16 v[84:87], v[248:251], v[60:63], v[84:87]\n\t" \
    "s_waitcnt lgkmcnt(5)\n\t" \
    "v_mfma_f32_16x16x32_bf16 v[72:75], a[212:215], v[64:67], v[72:75]\n\t" \
    "ds_read_b128 v[40:43], v22 offset:128\n\t" \
    "s_waitcnt lgkmcnt(5)\n\t" \
    "v_mfma_f32_16x16x32_bf16 v[76:79], a[212:215], v[68:71], v[76:79]\n\t" \
    "ds_read_b128 v[44:47], v22 offset:18560\n\t" \
    "v_mfma_f32_16x16x32_bf16 v[80:83], v[252:255], v[64:67], v[80:83]\n\t" \
    "v_mfma_f32_16x16x32_bf16 v[84:87], v[252:255], v[68:71], v[84:87]\n\t" \
    "v_mov_b32 v1, v22\n\t" \
    "v_mov_b32 v2, v23\n\t" \
    "v_cvt_pk_bf16_f32 v104, v72, v73\n\t" \
    "v_cvt_pk_bf16_f32 v105, v74, v75\n\t" \
    "s_nop 2\n\t" \
    "v_cvt_pk_bf16_f32 v106, v80, v81\n\t" \
    "v_cvt_pk_bf16_f32 v107, v82, v83\n\t" \
    "global_store_dwordx4 v8, v[104:107], s[40:41] offset:0 sc1\n\t" \
    "v_cvt_pk_bf16_f32 v108, v76, v77\n\t" \
    "v_cvt_pk_bf16_f32 v109, v78, v79\n\t" \
    "v_cvt_pk_bf16_f32 v110, v84, v85\n\t" \
    "v_cvt_pk_bf16_f32 v111, v86, v87\n\t" \
    "global_store_dwordx4 v8, v[108:111], s[40:41] offset:1024 sc1\n\t" \
    "s_cmp_ge_u32 s65, 1\n\t" \
    "s_cbranch_scc1 gx_nofast7_%=\n\t" \
    "s_waitcnt vmcnt(0)\n\t" \
    "s_barrier\n\t" \
    "s_cmp_lg_u32 s66, 0\n\t" \
    "s_cbranch_scc1 gx_SF10_%=\n\t" \
    "s_lshl_b32 s69, s67, 7\n\t" \
    "s_add_u32 s69, s69, 3149824\n\t" \
    "s_add_u32 s70, s65, 0\n\t" \
    "v_mov_b32 v156, s69\n\t" \
    "v_mov_b32 v157, s70\n\t" \
    "s_mov_b64 exec, 1\n\t" \
    "global_store_dword v156, v157, s[100:101] sc1\n\t" \
    "s_mov_b64 exec, -1\n\t" \
    "gx_SF10_%=:\n\t" \
    "gx_nofast7_%=:\n\t" \
    "s_sub_u32 s69, s65, 4\n\t" \
    "s_cmp_gt_u32 s69, 4\n\t" \
    "s_cbranch_scc1 gx_nt11_%=\n\t" \
    "s_lshl_b32 s69, s69, 7\n\t" \
    "s_lshl_b32 s70, s66, 5\n\t" \
    "s_add_u32 s69, s69, s70\n\t" \
    "v_lshrrev_b32 v156, 3, v6\n\t" \
    "v_add_u32 v156, s69, v156\n\t" \
    "v_min_u32 v156, 575, v156\n\t" \
    "v_and_b32 v157, 4, v6\n\t" \
    "v_lshlrev_b32 v157, 5, v157\n\t" \
    "v_lshl_add_u32 v156, v156, 13, v157\n\t" \
    "s_lshl_b32 s69, s67, 8\n\t" \
    "v_add_u32 v156, s69, v156\n\t" \
    "s_mov_b32 m0, 155648\n\t" \
    "s_nop 0\n\t" \
    "global_load_lds_dword v156, s[44:45]\n\t" \
    "gx_nt11_%=:\n\t" \
    "s_add_u32 s65, s65, 1\n\t" \
    "s_cmp_lt_u32 s65, 9\n\t" \
    "s_cbranch_scc1 gx_l16_%=\n\t" \
    "s_waitcnt vmcnt(0) lgkmcnt(0)\n\t" \
    "s_barrier\n\t" \
    "s_cmp_lg_u32 s66, 0\n\t" \
    "s_cbranch_scc1 gx_SF12_%=\n\t" \
    "s_lshl_b32 s69, s67, 7\n\t" \
    "s_add_u32 s69, s69, 3149824\n\t" \
    "s_add_u32 s70, s65, -1\n\t" \
    "v_mov_b32 v156, s69\n\t" \
    "v_mov_b32 v157, s70\n\t" \
    "s_mov_b64 exec, 1\n\t" \
    "global_store_dword v156, v157, s[100:101] sc1\n\t" \
    "s_mov_b64 exec, -1\n\t" \
    "gx_SF12_%=:\n\t" \
    "s_add_u32 s46, s44, 0\n\t" \
    "s_addc_u32 s47, s45, 0\n\t" \
    "s_add_u32 m0, s82, 0\n\t" \
    "s_nop 0\n\t" \
    "global_load_lds_dwordx4 v200, s[46:47]\n\t" \
    "s_add_u32 s46, s44, 131072\n\t" \
    "s_addc_u32 s47, s45, 0\n\t" \
    "s_add_u32 m0, s82, 4096\n\t" \
    "s_nop 0\n\t" \
    "global_load_lds_dwordx4 v200, s[46:47]\n\t" \
    "s_add_u32 s46, s44, 262144\n\t" \
    "s_addc_u32 s47, s45, 0\n\t" \
    "s_add_u32 m0, s82, 8192\n\t" \
    "s_nop 0\n\t" \
    "global_load_lds_dwordx4 v200, s[46:47]\n\t" \
    "s_add_u32 s46, s44, 393216\n\t" \
    "s_addc_u32 s47, s45, 0\n\t" \
    "s_add_u32 m0, s82, 12288\n\t" \
    "s_nop 0\n\t" \
    "global_load_lds_dwordx4 v200, s[46:47]\n\t" \
    "s_add_u32 s46, s44, 524288\n\t" \
    "s_addc_u32 s47, s45, 0\n\t" \
    "s_add_u32 m0, s82, 16384\n\t" \
    "s_nop 0\n\t" \
    "global_load_lds_dwordx4 v200, s[46:47]\n\t" \
    "s_add_u32 s46, s44, 655360\n\t" \
    "s_addc_u32 s47, s45, 0\n\t" \
    "s_add_u32 m0, s82, 20480\n\t" \
    "s_nop 0\n\t" \
    "global_load_lds_dwordx4 v200, s[46:47]\n\t" \
    "s_add_u32 s46, s44, 786432\n\t" \
    "s_addc_u32 s47, s45, 0\n\t" \
    "s_add_u32 m0, s82, 24576\n\t" \
    "s_nop 0\n\t" \
    "global_load_lds_dwordx4 v200, s[46:47]\n\t" \
    "s_add_u32 s46, s44, 917504\n\t" \
    "s_addc_u32 s47, s45, 0\n\t" \
    "s_add_u32 m0, s82, 28672\n\t" \
    "s_nop 0\n\t" \
    "global_load_lds_dwordx4 v200, s[46:47]\n\t" \
    "s_add_u32 s46, s44, 1048576\n\t" \
    "s_addc_u32 s47, s45, 0\n\t" \
    "s_add_u32 m0, s82, 32768\n\t" \
    "s_nop 0\n\t" \
    "global_load_lds_dwordx4 v200, s[46:47]\n\t" \
    "s_add_u32 s46, s44, 1179648\n\t" \
    "s_addc_u32 s47, s45, 0\n\t" \
    "s_add_u32 m0, s82, 36864\n\t" \
    "s_nop 0\n\t" \
    "global_load_lds_dwordx4 v200, s[46:47]\n\t" \
    "s_add_u32 s46, s44, 1310720\n\t" \
    "s_addc_u32 s47, s45, 0\n\t" \
    "s_add_u32 m0, s82, 40960\n\t" \
    "s_nop 0\n\t" \
    "global_load_lds_dwordx4 v200, s[46:47]\n\t" \
    "s_add_u32 s46, s44, 1441792\n\t" \
    "s_addc_u32 s47, s45, 0\n\t" \
    "s_add_u32 m0, s82, 45056\n\t" \
    "s_nop 0\n\t" \
    "global_load_lds_dwordx4 v200, s[46:47]\n\t" \
    "s_add_u32 s46, s44, 1572864\n\t" \
    "s_addc_u32 s47, s45, 0\n\t" \
    "s_add_u32 m0, s82, 49152\n\t" \
    "s_nop 0\n\t" \
    "global_load_lds_dwordx4 v200, s[46:47]\n\t" \
    "s_add_u32 s46, s44, 1703936\n\t" \
    "s_addc_u32 s47, s45, 0\n\t" \
    "s_add_u32 m0, s82, 53248\n\t" \
    "s_nop 0\n\t" \
    "global_load_lds_dwordx4 v200, s[46:47]\n\t" \
    "s_add_u32 s46, s44, 1835008\n\t" \
    "s_addc_u32 s47, s45, 0\n\t" \
    "s_add_u32 m0, s82, 57344\n\t" \
    "s_nop 0\n\t" \
    "global_load_lds_dwordx4 v200, s[46:47]\n\t" \
    "s_add_u32 s46, s44, 1966080\n\t" \
    "s_addc_u32 s47, s45, 0\n\t" \
    "s_add_u32 m0, s82, 61440\n\t" \
    "s_nop 0\n\t" \
    "global_load_lds_dwordx4 v200, s[46:47]\n\t" \
    "s_add_u32 s46, s44, 2097152\n\t" \
    "s_addc_u32 s47, s45, 0\n\t" \
    "s_add_u32 m0, s82, 65536\n\t" \
    "s_nop 0\n\t" \
    "global_load_lds_dwordx4 v200, s[46:47]\n\t" \
    "s_add_u32 s46, s44, 2228224\n\t" \
    "s_addc_u32 s47, s45, 0\n\t" \
    "s_add_u32 m0, s82, 69632\n\t" \
    "s_nop 0\n\t" \
    "global_load_lds_dwordx4 v200, s[46:47]\n\t" \
    "s_add_u32 s46, s44, 2359296\n\t" \
    "s_addc_u32 s47, s45, 0\n\t" \
    "s_add_u32 m0, s82, 73728\n\t" \
    "s_nop 0\n\t" \
    "global_load_lds_dwordx4 v200, s[46:47]\n\t" \
    "s_add_u32 s46, s44, 2490368\n\t" \
    "s_addc_u32 s47, s45, 0\n\t" \
    "s_add_u32 m0, s82, 77824\n\t" \
    "s_nop 0\n\t" \
    "global_load_lds_dwordx4 v200, s[46:47]\n\t" \
    "s_add_u32 s46, s44, 2621440\n\t" \
    "s_addc_u32 s47, s45, 0\n\t" \
    "s_add_u32 m0, s82, 81920\n\t" \
    "s_nop 0\n\t" \
    "global_load_lds_dwordx4 v200, s[46:47]\n\t" \
    "s_add_u32 s46, s44, 2752512\n\t" \
    "s_addc_u32 s47, s45, 0\n\t" \
    "s_add_u32 m0, s82, 86016\n\t" \
    "s_nop 0\n\t" \
    "global_load_lds_dwordx4 v200, s[46:47]\n\t" \
    "s_add_u32 s46, s44, 2883584\n\t" \
    "s_addc_u32 s47, s45, 0\n\t" \
    "s_add_u32 m0, s82, 90112\n\t" \
    "s_nop 0\n\t" \
    "global_load_lds_dwordx4 v200, s[46:47]\n\t" \
    "s_add_u32 s46, s44, 3014656\n\t" \
    "s_addc_u32 s47, s45, 0\n\t" \
    "s_add_u32 m0, s82, 94208\n\t" \
    "s_nop 0\n\t" \
    "global_load_lds_dwordx4 v200, s[46:47]\n\t" \
    "s_add_u32 s46, s44, 3145728\n\t" \
    "s_addc_u32 s47, s45, 0\n\t" \
    "s_add_u32 m0, s82, 98304\n\t" \
    "s_nop 0\n\t" \
    "global_load_lds_dwordx4 v200, s[46:47]\n\t" \
    "s_add_u32 s46, s44, 3276800\n\t" \
    "s_addc_u32 s47, s45, 0\n\t" \
    "s_add_u32 m0, s82, 102400\n\t" \
    "s_nop 0\n\t" \
    "global_load_lds_dwordx4 v200, s[46:47]\n\t" \
    "s_add_u32 s46, s44, 3407872\n\t" \
    "s_addc_u32 s47, s45, 0\n\t" \
    "s_add_u32 m0, s82, 106496\n\t" \
    "s_nop 0\n\t" \
    "global_load_lds_dwordx4 v200, s[46:47]\n\t" \
    "s_add_u32 s46, s44, 3538944\n\t" \
    "s_addc_u32 s47, s45, 0\n\t" \
    "s_add_u32 m0, s82, 110592\n\t" \
    "s_nop 0\n\t" \
    "global_load_lds_dwordx4 v200, s[46:47]\n\t" \
    "s_add_u32 s46, s44, 3670016\n\t" \
    "s_addc_u32 s47, s45, 0\n\t" \
    "s_add_u32 m0, s82, 114688\n\t" \
    "s_nop 0\n\t" \
    "global_load_lds_dwordx4 v200, s[46:47]\n\t" \
    "s_add_u32 s46, s44, 3801088\n\t" \
    "s_addc_u32 s47, s45, 0\n\t" \
    "s_add_u32 m0, s82, 118784\n\t" \
    "s_nop 0\n\t" \
    "global_load_lds_dwordx4 v200, s[46:47]\n\t" \
    "s_add_u32 s46, s44, 3932160\n\t" \
    "s_addc_u32 s47, s45, 0\n\t" \
    "s_add_u32 m0, s82, 122880\n\t" \
    "s_nop 0\n\t" \
    "global_load_lds_dwordx4 v200, s[46:47]\n\t" \
    "s_add_u32 s46, s44, 4063232\n\t" \
    "s_addc_u32 s47, s45, 0\n\t" \
    "s_add_u32 m0, s82, 126976\n\t" \
    "s_nop 0\n\t" \
    "global_load_lds_dwordx4 v200, s[46:47]\n\t" \
    "s_add_u32 s46, s44, 4194304\n\t" \
    "s_addc_u32 s47, s45, 0\n\t" \
    "s_add_u32 m0, s82, 131072\n\t" \
    "s_nop 0\n\t" \
    "global_load_lds_dwordx4 v200, s[46:47]\n\t" \
    "s_add_u32 s46, s44, 4325376\n\t" \
    "s_addc_u32 s47, s45, 0\n\t" \
    "s_add_u32 m0, s82, 135168\n\t" \
    "s_nop 0\n\t" \
    "global_load_lds_dwordx4 v200, s[46:47]\n\t" \
    "s_add_u32 s46, s44, 4456448\n\t" \
    "s_addc_u32 s47, s45, 0\n\t" \
    "s_add_u32 m0, s82, 139264\n\t" \
    "s_nop 0\n\t" \
    "global_load_lds_dwordx4 v200, s[46:47]\n\t" \
    "s_add_u32 s46, s44, 4587520\n\t" \
    "s_addc_u32 s47, s45, 0\n\t" \
    "s_add_u32 m0, s82, 143360\n\t" \
    "s_nop 0\n\t" \
    "global_load_lds_dwordx4 v200, s[46:47]\n\t" \
    "global_load_dwordx4 v[120:123], v203, s[50:51]\n\t" \
    "global_load_dwordx4 v[124:127], v204, s[50:51]\n\t" \
    "global_load_dwordx4 v[128:131], v205, s[50:51]\n\t" \
    "global_load_dwordx4 v[132:135], v206, s[50:51]\n\t" \
    "global_load_dwordx4 v[136:139], v207, s[50:51]\n\t" \
    "global_load_dwordx4 v[140:143], v208, s[50:51]\n\t" \
    "global_load_dwordx4 v[144:147], v209, s[50:51]\n\t" \
    "global_load_dwordx4 v[148:151], v210, s[50:51]\n\t" \
    "global_load_dwordx4 v[152:155], v211, s[50:51]\n\t" \
    "s_add_u32 s52, s50, 331776\n\t" \
    "s_addc_u32 s53, s51, 0\n\t" \
    "global_load_dwordx4 v[72:75], v203, s[52:53]\n\t" \
    "global_load_dwordx4 v[76:79], v204, s[52:53]\n\t" \
    "global_load_dwordx4 v[80:83], v205, s[52:53]\n\t" \
    "global_load_dwordx4 v[84:87], v206, s[52:53]\n\t" \
    "global_load_dwordx4 v[88:91], v207, s[52:53]\n\t" \
    "global_load_dwordx4 v[92:95], v208, s[52:53]\n\t" \
    "global_load_dwordx4 v[96:99], v209, s[52:53]\n\t" \
    "global_load_dwordx4 v[100:103], v210, s[52:53]\n\t" \
    "global_load_dwordx4 v[104:107], v211, s[52:53]\n\t" \
    "s_waitcnt vmcnt(50) lgkmcnt(0)\n\t" \
    "s_barrier\n\t" \
    "ds_read_b64_tr_b16 a[0:1], v201 offset:0\n\t" \
    "ds_read_b64_tr_b16 a[2:3], v201 offset:1024\n\t" \
    "ds_read_b64_tr_b16 a[4:5], v201 offset:8192\n\t" \
    "ds_read_b64_tr_b16 a[6:7], v201 offset:9216\n\t" \
    "ds_read_b64_tr_b16 a[72:73], v202 offset:0\n\t" \
    "ds_read_b64_tr_b16 a[74:75], v202 offset:1024\n\t" \
    "ds_read_b64_tr_b16 a[76:77], v202 offset:8192\n\t" \
    "ds_read_b64_tr_b16 a[78:79], v202 offset:9216\n\t" \
    "s_waitcnt vmcnt(46) lgkmcnt(0)\n\t" \
    "s_barrier\n\t" \
    "ds_read_b64_tr_b16 a[8:9], v201 offset:16384\n\t" \
    "ds_read_b64_tr_b16 a[10:11], v201 offset:17408\n\t" \
    "ds_read_b64_tr_b16 a[12:13], v201 offset:24576\n\t" \
    "ds_read_b64_tr_b16 a[14:15], v201 offset:25600\n\t" \
    "ds_read_b64_tr_b16 a[80:81], v202 offset:16384\n\t" \
    "ds_read_b64_tr_b16 a[82:83], v202 offset:17408\n\t" \
    "ds_read_b64_tr_b16 a[84:85], v202 offset:24576\n\t" \
    "ds_read_b64_tr_b16 a[86:87], v202 offset:25600\n\t" \
    "s_waitcnt vmcnt(42) lgkmcnt(0)\n\t" \
    "s_barrier\n\t" \
    "ds_read_b64_tr_b16 a[16:17], v201 offset:32768\n\t" \
    "ds_read_b64_tr_b16 a[18:19], v201 offset:33792\n\t" \
    "ds_read_b64_tr_b16 a[20:21], v201 offset:40960\n\t" \
    "ds_read_b64_tr_b16 a[22:23], v201 offset:41984\n\t" \
    "ds_read_b64_tr_b16 a[88:89], v202 offset:32768\n\t" \
    "ds_read_b64_tr_b16 a[90:91], v202 offset:33792\n\t" \
    "ds_read_b64_tr_b16 a[92:93], v202 offset:40960\n\t" \
    "ds_read_b64_tr_b16 a[94:95], v202 offset:41984\n\t" \
    "s_waitcnt vmcnt(38) lgkmcnt(0)\n\t" \
    "s_barrier\n\t" \
    "ds_read_b64_tr_b16 a[24:25], v201 offset:49152\n\t" \
    "ds_read_b64_tr_b16 a[26:27], v201 offset:50176\n\t" \
    "ds_read_b64_tr_b16 a[28:29], v201 offset:57344\n\t" \
    "ds_read_b64_tr_b16 a[30:31], v201 offset:58368\n\t" \
    "ds_read_b64_tr_b16 a[96:97], v202 offset:49152\n\t" \
    "ds_read_b64_tr_b16 a[98:99], v202 offset:50176\n\t" \
    "ds_read_b64_tr_b16 a[100:101], v202 offset:57344\n\t" \
    "ds_read_b64_tr_b16 a[102:103], v202 offset:58368\n\t" \
    "s_waitcnt vmcnt(34) lgkmcnt(0)\n\t" \
    "s_barrier\n\t" \
    "v_add_u32 v201, 0x10000, v201\n\t" \
    "v_add_u32 v202, 0x10000, v202\n\t" \
    "ds_read_b64_tr_b16 a[32:33], v201 offset:0\n\t" \
    "ds_read_b64_tr_b16 a[34:35], v201 offset:1024\n\t" \
    "ds_read_b64_tr_b16 a[36:37], v201 offset:8192\n\t" \
    "ds_read_b64_tr_b16 a[38:39], v201 offset:9216\n\t" \
    "ds_read_b64_tr_b16 a[104:105], v202 offset:0\n\t" \
    "ds_read_b64_tr_b16 a[106:107], v202 offset:1024\n\t" \
    "ds_read_b64_tr_b16 a[108:109], v202 offset:8192\n\t" \
    "ds_read_b64_tr_b16 a[110:111], v202 offset:9216\n\t" \
    "s_waitcnt vmcnt(30) lgkmcnt(0)\n\t" \
    "s_barrier\n\t" \
    "ds_read_b64_tr_b16 a[40:41], v201 offset:16384\n\t" \
    "ds_read_b64_tr_b16 a[42:43], v201 offset:17408\n\t" \
    "ds_read_b64_tr_b16 a[44:45], v201 offset:24576\n\t" \
    "ds_read_b64_tr_b16 a[46:47], v201 offset:25600\n\t" \
    "ds_read_b64_tr_b16 a[112:113], v202 offset:16384\n\t" \
    "ds_read_b64_tr_b16 a[114:115], v202 offset:17408\n\t" \
    "ds_read_b64_tr_b16 a[116:117], v202 offset:24576\n\t" \
    "ds_read_b64_tr_b16 a[118:119], v202 offset:25600\n\t" \
    "s_waitcnt vmcnt(26) lgkmcnt(0)\n\t" \
    "s_barrier\n\t" \
    "ds_read_b64_tr_b16 a[48:49], v201 offset:32768\n\t" \
    "ds_read_b64_tr_b16 a[50:51], v201 offset:33792\n\t" \
    "ds_read_b64_tr_b16 a[52:53], v201 offset:40960\n\t" \
    "ds_read_b64_tr_b16 a[54:55], v201 offset:41984\n\t" \
    "ds_read_b64_tr_b16 a[120:121], v202 offset:32768\n\t" \
    "ds_read_b64_tr_b16 a[122:123], v202 offset:33792\n\t" \
    "ds_read_b64_tr_b16 a[124:125], v202 offset:40960\n\t" \
    "ds_read_b64_tr_b16 a[126:127], v202 offset:41984\n\t" \
    "s_waitcnt vmcnt(22) lgkmcnt(0)\n\t" \
    "s_barrier\n\t" \
    "ds_read_b64_tr_b16 a[56:57], v201 offset:49152\n\t" \
    "ds_read_b64_tr_b16 a[58:59], v201 offset:50176\n\t" \
    "ds_read_b64_tr_b16 a[60:61], v201 offset:57344\n\t" \
    "ds_read_b64_tr_b16 a[62:63], v201 offset:58368\n\t" \
    "ds_read_b64_tr_b16 a[128:129], v202 offset:49152\n\t" \
    "ds_read_b64_tr_b16 a[130:131], v202 offset:50176\n\t" \
    "ds_read_b64_tr_b16 a[132:133], v202 offset:57344\n\t" \
    "ds_read_b64_tr_b16 a[134:135], v202 offset:58368\n\t" \
    "s_waitcnt vmcnt(18) lgkmcnt(0)\n\t" \
    "s_barrier\n\t" \
    "v_add_u32 v201, 0x10000, v201\n\t" \
    "v_add_u32 v202, 0x10000, v202\n\t" \
    "ds_read_b64_tr_b16 a[64:65], v201 offset:0\n\t" \
    "ds_read_b64_tr_b16 a[66:67], v201 offset:1024\n\t" \
    "ds_read_b64_tr_b16 a[68:69], v201 offset:8192\n\t" \
    "ds_read_b64_tr_b16 a[70:71], v201 offset:9216\n\t" \
    "ds_read_b64_tr_b16 a[136:137], v202 offset:0\n\t" \
    "ds_read_b64_tr_b16 a[138:139], v202 offset:1024\n\t" \
    "ds_read_b64_tr_b16 a[140:141], v202 offset:8192\n\t" \
    "ds_read_b64_tr_b16 a[142:143], v202 offset:9216\n\t" \
    "v_add_u32 v201, 0xfffe0000, v201\n\t" \
    "v_add_u32 v202, 0xfffe0000, v202\n\t" \
    "s_waitcnt lgkmcnt(0)\n\t" \
    "s_barrier\n\t" \
    "s_waitcnt vmcnt(0)\n\t" \
    "ds_write_b128 v3, v[120:123] offset:0\n\t" \
    "ds_write_b128 v3, v[124:127] offset:1024\n\t" \
    "ds_write_b128 v3, v[128:131] offset:2048\n\t" \
    "ds_write_b128 v3, v[132:135] offset:3072\n\t" \
    "ds_write_b128 v3, v[136:139] offset:4096\n\t" \
    "ds_write_b128 v3, v[140:143] offset:5120\n\t" \
    "ds_write_b128 v3, v[144:147] offset:6144\n\t" \
    "ds_write_b128 v3, v[148:151] offset:7168\n\t" \
    "ds_write_b128 v3, v[152:155] offset:8192\n\t" \
    "ds_write_b128 v3, v[72:75] offset:36864\n\t" \
    "ds_write_b128 v3, v[76:79] offset:37888\n\t" \
    "ds_write_b128 v3, v[80:83] offset:38912\n\t" \
    "ds_write_b128 v3, v[84:87] offset:39936\n\t" \
    "ds_write_b128 v3, v[88:91] offset:40960\n\t" \
    "ds_write_b128 v3, v[92:95] offset:41984\n\t" \
    "ds_write_b128 v3, v[96:99] offset:43008\n\t" \
    "ds_write_b128 v3, v[100:103] offset:44032\n\t" \
    "ds_write_b128 v3, v[104:107] offset:45056\n\t" \
    "s_mov_b32 s65, 0\n\t" \
    "v_mov_b32 v1, v0\n\t" \
    "v_xor_b32 v2, 64, v0\n\t" \
    "s_mov_b32 s90, 0\n\t" \
    "s_add_u32 s91, s89, 0x12000\n\t" \
    "s_add_u32 s85, s91, s89\n\t" \
    "s_add_u32 s52, s50, 36864\n\t" \
    "s_addc_u32 s53, s51, 0\n\t" \
    "s_mul_i32 s69, s67, 8192\n\t" \
    "s_add_u32 s69, s69, 175112192\n\t" \
    "s_add_u32 s42, s36, s69\n\t" \
    "s_addc_u32 s43, s37, 0\n\t" \
    "s_mov_b64 s[40:41], s[42:43]\n\t" \
    "s_waitcnt lgkmcnt(0)\n\t" \
    "s_barrier\n\t" \
    "ds_read_b128 v[24:27], v1 offset:0\n\t" \
    "ds_read_b128 v[28:31], v1 offset:18432\n\t" \
    "ds_read_b128 v[32:35], v2 offset:0\n\t" \
    "ds_read_b128 v[36:39], v2 offset:18432\n\t" \
    "ds_read_b128 v[40:43], v1 offset:128\n\t" \
    "ds_read_b128 v[44:47], v1 offset:18560\n\t" \
    ".p2align 6\n\t" \
    "s_nop 0\n\t" \
    "s_nop 0\n\t" \
    "gx_l28_%=:\n\t" \
    "s_waitcnt lgkmcnt(5)\n\t" \
    "v_mfma_f32_16x16x32_bf16 v[88:91], a[0:3], v[24:27], 0\n\t" \
    "ds_read_b128 v[48:51], v2 offset:128\n\t" \
    "s_waitcnt lgkmcnt(5)\n\t" \
    "v_mfma_f32_16x16x32_bf16 v[92:95], a[0:3], v[28:31], 0\n\t" \
    "ds_read_b128 v[52:55], v2 offset:18560\n\t" \
    "v_mfma_f32_16x16x32_bf16 v[96:99], a[72:75], v[24:27], 0\n\t" \
    "s_add_u32 m0, s91, 0\n\t" \
    "v_mfma_f32_16x16x32_bf16 v[100:103], a[72:75], v[28:31], 0\n\t" \
    "global_load_lds_dwordx4 v203, s[52:53]\n\t" \
    "s_waitcnt lgkmcnt(5)\n\t" \
    "v_mfma_f32_16x16x32_bf16 v[88:91], a[4:7], v[32:35], v[88:91]\n\t" \
    "ds_read_b128 v[56:59], v1 offset:256\n\t" \
    "s_waitcnt lgkmcnt(5)\n\t" \
    "v_mfma_f32_16x16x32_bf16 v[92:95], a[4:7], v[36:39], v[92:95]\n\t" \
    "ds_read_b128 v[60:63], v1 offset:18688\n\t" \
    "v_mfma_f32_16x16x32_bf16 v[96:99], a[76:79], v[32:35], v[96:99]\n\t" \
    "s_add_u32 m0, s91, 1024\n\t" \
    "v_mfma_f32_16x16x32_bf16 v[100:103], a[76:79], v[36:39], v[100:103]\n\t" \
    "global_load_lds_dwordx4 v204, s[52:53]\n\t" \
    "s_waitcnt lgkmcnt(5)\n\t" \
    "v_mfma_f32_16x16x32_bf16 v[88:91], a[8:11], v[40:43], v[88:91]\n\t" \
    "ds_read_b128 v[64:67], v2 offset:256\n\t" \
    "s_waitcnt lgkmcnt(5)\n\t" \
    "v_mfma_f32_16x16x32_bf16 v[92:95], a[8:11], v[44:47], v[92:95]\n\t" \
    "ds_read_b128 v[68:71], v2 offset:18688\n\t" \
    "v_cvt_pk_bf16_f32 v104, v72, v73\n\t" \
    "v_mfma_f32_16x16x32_bf16 v[96:99], a[80:83], v[40:43], v[96:99]\n\t" \
    "s_add_u32 m0, s91, 2048\n\t" \
    "v_cvt_pk_bf16_f32 v105, v74, v75\n\t" \
    "v_mfma_f32_16x16x32_bf16 v[100:103], a[80:83], v[44:47], v[100:103]\n\t" \
    "global_load_lds_dwordx4 v205, s[52:53]\n\t" \
    "s_waitcnt lgkmcnt(5)\n\t" \
    "v_mfma_f32_16x16x32_bf16 v[88:91], a[12:15], v[48:51], v[88:91]\n\t" \
    "ds_read_b128 v[24:27], v1 offset:384\n\t" \
    "s_waitcnt lgkmcnt(5)\n\t" \
    "v_mfma_f32_16x16x32_bf16 v[92:95], a[12:15], v[52:55], v[92:95]\n\t" \
    "ds_read_b128 v[28:31], v1 offset:18816\n\t" \
    "v_cvt_pk_bf16_f32 v106, v80, v81\n\t" \
    "v_mfma_f32_16x16x32_bf16 v[96:99], a[84:87], v[48:51], v[96:99]\n\t" \
    "s_add_u32 m0, s91, 3072\n\t" \
    "v_cvt_pk_bf16_f32 v107, v82, v83\n\t" \
    "v_mfma_f32_16x16x32_bf16 v[100:103], a[84:87], v[52:55], v[100:103]\n\t" \
    "global_load_lds_dwordx4 v206, s[52:53]\n\t" \
    "s_waitcnt lgkmcnt(5)\n\t" \
    "v_mfma_f32_16x16x32_bf16 v[88:91], a[16:19], v[56:59], v[88:91]\n\t" \
    "ds_read_b128 v[32:35], v2 offset:384\n\t" \
    "s_waitcnt lgkmcnt(5)\n\t" \
    "v_mfma_f32_16x16x32_bf16 v[92:95], a[16:19], v[60:63], v[92:95]\n\t" \
    "ds_read_b128 v[36:39], v2 offset:18816\n\t" \
    "global_store_dwordx4 v8, v[104:107], s[40:41] offset:0 sc1\n\t" \
    "v_mfma_f32_16x16x32_bf16 v[96:99], a[88:91], v[56:59], v[96:99]\n\t" \
    "s_add_u32 m0, s91, 4096\n\t" \
    "v_cvt_pk_bf16_f32 v108, v76, v77\n\t" \
    "v_mfma_f32_16x16x32_bf16 v[100:103], a[88:91], v[60:63], v[100:103]\n\t" \
    "global_load_lds_dwordx4 v207, s[52:53]\n\t" \
    "s_waitcnt lgkmcnt(5)\n\t" \
    "v_mfma_f32_16x16x32_bf16 v[88:91], a[20:23], v[64:67], v[88:91]\n\t" \
    "ds_read_b128 v[40:43], v1 offset:512\n\t" \
    "s_waitcnt lgkmcnt(5)\n\t" \
    "v_mfma_f32_16x16x32_bf16 v[92:95], a[20:23], v[68:71], v[92:95]\n\t" \
    "ds_read_b128 v[44:47], v1 offset:18944\n\t" \
    "v_cvt_pk_bf16_f32 v109, v78, v79\n\t" \
    "v_mfma_f32_16x16x32_bf16 v[96:99], a[92:95], v[64:67], v[96:99]\n\t" \
    "s_add_u32 m0, s91, 5120\n\t" \
    "v_cvt_pk_bf16_f32 v110, v84, v85\n\t" \
    "v_mfma_f32_16x16x32_bf16 v[100:103], a[92:95], v[68:71], v[100:103]\n\t" \
    "global_load_lds_dwordx4 v208, s[52:53]\n\t" \
    "s_waitcnt lgkmcnt(5)\n\t" \
    "v_mfma_f32_16x16x32_bf16 v[88:91], a[24:27], v[24:27], v[88:91]\n\t" \
    "ds_read_b128 v[48:51], v2 offset:512\n\t" \
    "s_waitcnt lgkmcnt(5)\n\t" \
    "v_mfma_f32_16x16x32_bf16 v[92:95], a[24:27], v[28:31], v[92:95]\n\t" \
    "ds_read_b128 v[52:55], v2 offset:18944\n\t" \
    "v_cvt_pk_bf16_f32 v111, v86, v87\n\t" \
    "v_mfma_f32_16x16x32_bf16 v[96:99], a[96:99], v[24:27], v[96:99]\n\t" \
    "s_add_u32 m0, s91, 6144\n\t" \
    "global_store_dwordx4 v8, v[108:111], s[40:41] offset:1024 sc1\n\t" \
    "v_mfma_f32_16x16x32_bf16 v[100:103], a[96:99], v[28:31], v[100:103]\n\t" \
    "global_load_lds_dwordx4 v209, s[52:53]\n\t" \
    "s_waitcnt lgkmcnt(5)\n\t" \
    "v_mfma_f32_16x16x32_bf16 v[88:91], a[28:31], v[32:35], v[88:91]\n\t" \
    "ds_read_b128 v[56:59], v1 offset:640\n\t" \
    "s_waitcnt lgkmcnt(5)\n\t" \
    "v_mfma_f32_16x16x32_bf16 v[92:95], a[28:31], v[36:39], v[92:95]\n\t" \
    "ds_read_b128 v[60:63], v1 offset:19072\n\t" \
    "v_mfma_f32_16x16x32_bf16 v[96:99], a[100:103], v[32:35], v[96:99]\n\t" \
    "s_add_u32 m0, s91, 7168\n\t" \
    "v_mfma_f32_16x16x32_bf16 v[100:103], a[100:103], v[36:39], v[100:103]\n\t" \
    "global_load_lds_dwordx4 v210, s[52:53]\n\t" \
    "s_waitcnt lgkmcnt(5)\n\t" \
    "v_mfma_f32_16x16x32_bf16 v[88:91], a[32:35], v[40:43], v[88:91]\n\t" \
    "ds_read_b128 v[64:67], v2 offset:640\n\t" \
    "s_waitcnt lgkmcnt(5)\n\t" \
    "v_mfma_f32_16x16x32_bf16 v[92:95], a[32:35], v[44:47], v[92:95]\n\t" \
    "ds_read_b128 v[68:71], v2 offset:19072\n\t" \
    "v_mfma_f32_16x16x32_bf16 v[96:99], a[104:107], v[40:43], v[96:99]\n\t" \
    "s_add_u32 m0, s91, 8192\n\t" \
    "v_mfma_f32_16x16x32_bf16 v[100:103], a[104:107], v[44:47], v[100:103]\n\t" \
    "global_load_lds_dwordx4 v211, s[52:53]\n\t" \
    "s_waitcnt lgkmcnt(5)\n\t" \
    "v_mfma_f32_16x16x32_bf16 v[88:91], a[36:39], v[48:51], v[88:91]\n\t" \
    "ds_read_b128 v[24:27], v1 offset:768\n\t" \
    "s_waitcnt lgkmcnt(5)\n\t" \
    "v_mfma_f32_16x16x32_bf16 v[92:95], a[36:39], v[52:55], v[92:95]\n\t" \
    "ds_read_b128 v[28:31], v1 offset:19200\n\t" \
    "v_mfma_f32_16x16x32_bf16 v[96:99], a[108:111], v[48:51], v[96:99]\n\t" \
    "v_mfma_f32_16x16x32_bf16 v[100:103], a[108:111], v[52:55], v[100:103]\n\t" \
    "s_waitcnt lgkmcnt(5)\n\t" \
    "v_mfma_f32_16x16x32_bf16 v[88:91], a[40:43], v[56:59], v[88:91]\n\t" \
    "ds_read_b128 v[32:35], v2 offset:768\n\t" \
    "s_waitcnt lgkmcnt(5)\n\t" \
    "v_mfma_f32_16x16x32_bf16 v[92:95], a[40:43], v[60:63], v[92:95]\n\t" \
    "ds_read_b128 v[36:39], v2 offset:19200\n\t" \
    "v_mfma_f32_16x16x32_bf16 v[96:99], a[112:115], v[56:59], v[96:99]\n\t" \
    "s_waitcnt vmcnt(11)\n\t" \
    "s_barrier\n\t" \
    "v_mfma_f32_16x16x32_bf16 v[100:103], a[112:115], v[60:63], v[100:103]\n\t" \
    "s_cmp_lg_u32 s66, 0\n\t" \
    "s_cbranch_scc1 gx_SF13_%=\n\t" \
    "s_lshl_b32 s69, s67, 7\n\t" \
    "s_add_u32 s69, s69, 3211264\n\t" \
    "s_add_u32 s70, s65, -1\n\t" \
    "v_mov_b32 v156, s69\n\t" \
    "v_mov_b32 v157, s70\n\t" \
    "s_mov_b64 exec, 1\n\t" \
    "global_store_dword v156, v157, s[100:101] sc1\n\t" \
    "s_mov_b64 exec, -1\n\t" \
    "gx_SF13_%=:\n\t" \
    "s_waitcnt lgkmcnt(5)\n\t" \
    "v_mfma_f32_16x16x32_bf16 v[88:91], a[44:47], v[64:67], v[88:91]\n\t" \
    "ds_read_b128 v[40:43], v1 offset:896\n\t" \
    "s_waitcnt lgkmcnt(5)\n\t" \
    "v_mfma_f32_16x16x32_bf16 v[92:95], a[44:47], v[68:71], v[92:95]\n\t" \
    "ds_read_b128 v[44:47], v1 offset:19328\n\t" \
    "v_mfma_f32_16x16x32_bf16 v[96:99], a[116:119], v[64:67], v[96:99]\n\t" \
    "v_add_u32 v22, 0x9000, v1\n\t" \
    "v_add_u32 v23, 0x9000, v2\n\t" \
    "v_mfma_f32_16x16x32_bf16 v[100:103], a[116:119], v[68:71], v[100:103]\n\t" \
    "s_cmp_lt_u32 s65, 54\n\t" \
    "s_cselect_b32 s84, 10, 2\n\t" \
    "s_waitcnt lgkmcnt(5)\n\t" \
    "v_mfma_f32_16x16x32_bf16 v[88:91], a[48:51], v[24:27], v[88:91]\n\t" \
    "ds_read_b128 v[48:51], v2 offset:896\n\t" \
    "s_waitcnt lgkmcnt(5)\n\t" \
    "v_mfma_f32_16x16x32_bf16 v[92:95], a[48:51], v[28:31], v[92:95]\n\t" \
    "ds_read_b128 v[52:55], v2 offset:19328\n\t" \
    "v_mfma_f32_16x16x32_bf16 v[96:99], a[120:123], v[24:27], v[96:99]\n\t" \
    "s_add_u32 s84, s65, s84\n\t" \
    "s_min_u32 s69, s84, 63\n\t" \
    "v_mfma_f32_16x16x32_bf16 v[100:103], a[120:123], v[28:31], v[100:103]\n\t" \
    "s_mul_i32 s69, s69, 36864\n\t" \
    "s_add_u32 s48, s50, s69\n\t" \
    "s_waitcnt lgkmcnt(5)\n\t" \
    "v_mfma_f32_16x16x32_bf16 v[88:91], a[52:55], v[32:35], v[88:91]\n\t" \
    "ds_read_b128 v[56:59], v1 offset:1024\n\t" \
    "s_waitcnt lgkmcnt(5)\n\t" \
    "v_mfma_f32_16x16x32_bf16 v[92:95], a[52:55], v[36:39], v[92:95]\n\t" \
    "ds_read_b128 v[60:63], v1 offset:19456\n\t" \
    "v_mfma_f32_16x16x32_bf16 v[96:99], a[124:127], v[32:35], v[96:99]\n\t" \
    "s_addc_u32 s49, s51, 0\n\t" \
    "s_mul_i32 s70, s67, 7\n\t" \
    "v_mfma_f32_16x16x32_bf16 v[100:103], a[124:127], v[36:39], v[100:103]\n\t" \
    "s_add_u32 s69, s65, 0\n\t" \
    "s_xor_b32 s69, s69, s70\n\t" \
    "s_waitcnt lgkmcnt(5)\n\t" \
    "v_mfma_f32_16x16x32_bf16 v[88:91], a[56:59], v[40:43], v[88:91]\n\t" \
    "ds_read_b128 v[64:67], v2 offset:1024\n\t" \
    "s_waitcnt lgkmcnt(5)\n\t" \
    "v_mfma_f32_16x16x32_bf16 v[92:95], a[56:59], v[44:47], v[92:95]\n\t" \
    "ds_read_b128 v[68:71], v2 offset:19456\n\t" \
    "v_mfma_f32_16x16x32_bf16 v[96:99], a[128:131], v[40:43], v[96:99]\n\t" \
    "s_and_b32 s69, s69, 63\n\t" \
    "s_lshl_b32 s69, s69, 13\n\t" \
    "v_mfma_f32_16x16x32_bf16 v[100:103], a[128:131], v[44:47], v[100:103]\n\t" \
    "s_mul_i32 s70, s67, 524288\n\t" \
    "s_add_u32 s69, s69, s70\n\t" \
    "s_waitcnt lgkmcnt(5)\n\t" \
    "v_mfma_f32_16x16x32_bf16 v[88:91], a[60:63], v[48:51], v[88:91]\n\t" \
    "ds_read_b128 v[24:27], v22 offset:0\n\t" \
    "s_waitcnt lgkmcnt(5)\n\t" \
    "v_mfma_f32_16x16x32_bf16 v[92:95], a[60:63], v[52:55], v[92:95]\n\t" \
    "ds_read_b128 v[28:31], v22 offset:18432\n\t" \
    "v_mfma_f32_16x16x32_bf16 v[96:99], a[132:135], v[48:51], v[96:99]\n\t" \
    "s_add_u32 s69, s69, 6291456\n\t" \
    "s_add_u32 s38, s36, s69\n\t" \
    "v_mfma_f32_16x16x32_bf16 v[100:103], a[132:135], v[52:55], v[100:103]\n\t" \
    "s_addc_u32 s39, s37, 0\n\t" \
    "s_waitcnt lgkmcnt(5)\n\t" \
    "v_mfma_f32_16x16x32_bf16 v[88:91], a[64:67], v[56:59], v[88:91]\n\t" \
    "ds_read_b128 v[32:35], v23 offset:0\n\t" \
    "s_waitcnt lgkmcnt(5)\n\t" \
    "v_mfma_f32_16x16x32_bf16 v[92:95], a[64:67], v[60:63], v[92:95]\n\t" \
    "ds_read_b128 v[36:39], v23 offset:18432\n\t" \
    "v_mfma_f32_16x16x32_bf16 v[96:99], a[136:139], v[56:59], v[96:99]\n\t" \
    "v_mfma_f32_16x16x32_bf16 v[100:103], a[136:139], v[60:63], v[100:103]\n\t" \
    "s_waitcnt lgkmcnt(5)\n\t" \
    "v_mfma_f32_16x16x32_bf16 v[88:91], a[68:71], v[64:67], v[88:91]\n\t" \
    "ds_read_b128 v[40:43], v22 offset:128\n\t" \
    "s_waitcnt lgkmcnt(5)\n\t" \
    "v_mfma_f32_16x16x32_bf16 v[92:95], a[68:71], v[68:71], v[92:95]\n\t" \
    "ds_read_b128 v[44:47], v22 offset:18560\n\t" \
    "v_mfma_f32_16x16x32_bf16 v[96:99], a[140:143], v[64:67], v[96:99]\n\t" \
    "v_mfma_f32_16x16x32_bf16 v[100:103], a[140:143], v[68:71], v[100:103]\n\t" \
    "s_waitcnt lgkmcnt(5)\n\t" \
    "v_mfma_f32_16x16x32_bf16 v[72:75], a[144:147], v[24:27], 0\n\t" \
    "ds_read_b128 v[48:51], v23 offset:128\n\t" \
    "s_waitcnt lgkmcnt(5)\n\t" \
    "v_mfma_f32_16x16x32_bf16 v[76:79], a[144:147], v[28:31], 0\n\t" \
    "ds_read_b128 v[52:55], v23 offset:18560\n\t" \
    "v_mfma_f32_16x16x32_bf16 v[80:83], a[216:219], v[24:27], 0\n\t" \
    "s_add_u32 m0, s91, 36864\n\t" \
    "v_mfma_f32_16x16x32_bf16 v[84:87], a[216:219], v[28:31], 0\n\t" \
    "global_load_lds_dwordx4 v203, s[48:49]\n\t" \
    "s_waitcnt lgkmcnt(5)\n\t" \
    "v_mfma_f32_16x16x32_bf16 v[72:75], a[148:151], v[32:35], v[72:75]\n\t" \
    "ds_read_b128 v[56:59], v22 offset:256\n\t" \
    "s_waitcnt lgkmcnt(5)\n\t" \
    "v_mfma_f32_16x16x32_bf16 v[76:79], a[148:151], v[36:39], v[76:79]\n\t" \
    "ds_read_b128 v[60:63], v22 offset:18688\n\t" \
    "v_mfma_f32_16x16x32_bf16 v[80:83], a[220:223], v[32:35], v[80:83]\n\t" \
    "s_add_u32 m0, s91, 37888\n\t" \
    "v_mfma_f32_16x16x32_bf16 v[84:87], a[220:223], v[36:39], v[84:87]\n\t" \
    "global_load_lds_dwordx4 v204, s[48:49]\n\t" \
    "s_waitcnt lgkmcnt(5)\n\t" \
    "v_mfma_f32_16x16x32_bf16 v[72:75], a[152:155], v[40:43], v[72:75]\n\t" \
    "ds_read_b128 v[64:67], v23 offset:256\n\t" \
    "s_waitcnt lgkmcnt(5)\n\t" \
    "v_mfma_f32_16x16x32_bf16 v[76:79], a[152:155], v[44:47], v[76:79]\n\t" \
    "ds_read_b128 v[68:71], v23 offset:18688\n\t" \
    "v_cvt_pk_bf16_f32 v104, v88, v89\n\t" \
    "v_mfma_f32_16x16x32_bf16 v[80:83], a[224:227], v[40:43], v[80:83]\n\t" \
    "s_add_u32 m0, s91, 38912\n\t" \
    "v_cvt_pk_bf16_f32 v105, v90, v91\n\t" \
    "v_mfma_f32_16x16x32_bf16 v[84:87], a[224:227], v[44:47], v[84:87]\n\t" \
    "global_load_lds_dwordx4 v205, s[48:49]\n\t" \
    "s_waitcnt lgkmcnt(5)\n\t" \
    "v_mfma_f32_16x16x32_bf16 v[72:75], a[156:159], v[48:51], v[72:75]\n\t" \
    "ds_read_b128 v[24:27], v22 offset:384\n\t" \
    "s_waitcnt lgkmcnt(5)\n\t" \
    "v_mfma_f32_16x16x32_bf16 v[76:79], a[156:159], v[52:55], v[76:79]\n\t" \
    "ds_read_b128 v[28:31], v22 offset:18816\n\t" \
    "v_cvt_pk_bf16_f32 v106, v96, v97\n\t" \
    "v_mfma_f32_16x16x32_bf16 v[80:83], a[228:231], v[48:51], v[80:83]\n\t" \
    "s_add_u32 m0, s91, 39936\n\t" \
    "v_cvt_pk_bf16_f32 v107, v98, v99\n\t" \
    "v_mfma_f32_16x16x32_bf16 v[84:87], a[228:231], v[52:55], v[84:87]\n\t" \
    "global_load_lds_dwordx4 v206, s[48:49]\n\t" \
    "s_waitcnt lgkmcnt(5)\n\t" \
    "v_mfma_f32_16x16x32_bf16 v[72:75], a[160:163], v[56:59], v[72:75]\n\t" \
    "ds_read_b128 v[32:35], v23 offset:384\n\t" \
    "s_waitcnt lgkmcnt(5)\n\t" \
    "v_mfma_f32_16x16x32_bf16 v[76:79], a[160:163], v[60:63], v[76:79]\n\t" \
    "ds_read_b128 v[36:39], v23 offset:18816\n\t" \
    "global_store_dwordx4 v8, v[104:107], s[38:39] offset:0 sc1\n\t" \
    "v_mfma_f32_16x16x32_bf16 v[80:83], a[232:235], v[56:59], v[80:83]\n\t" \
    "s_add_u32 m0, s91, 40960\n\t" \
    "v_cvt_pk_bf16_f32 v108, v92, v93\n\t" \
    "v_mfma_f32_16x16x32_bf16 v[84:87], a[232:235], v[60:63], v[84:87]\n\t" \
    "global_load_lds_dwordx4 v207, s[48:49]\n\t" \
    "s_waitcnt lgkmcnt(5)\n\t" \
    "v_mfma_f32_16x16x32_bf16 v[72:75], a[164:167], v[64:67], v[72:75]\n\t" \
    "ds_read_b128 v[40:43], v22 offset:512\n\t" \
    "s_waitcnt lgkmcnt(5)\n\t" \
    "v_mfma_f32_16x16x32_bf16 v[76:79], a[164:167], v[68:71], v[76:79]\n\t" \
    "ds_read_b128 v[44:47], v22 offset:18944\n\t" \
    "v_cvt_pk_bf16_f32 v109, v94, v95\n\t" \
    "v_mfma_f32_16x16x32_bf16 v[80:83], a[236:239], v[64:67], v[80:83]\n\t" \
    "s_add_u32 m0, s91, 41984\n\t" \
    "v_cvt_pk_bf16_f32 v110, v100, v101\n\t" \
    "v_mfma_f32_16x16x32_bf16 v[84:87], a[236:239], v[68:71], v[84:87]\n\t" \
    "global_load_lds_dwordx4 v208, s[48:49]\n\t" \
    "s_waitcnt lgkmcnt(5)\n\t" \
    "v_mfma_f32_16x16x32_bf16 v[72:75], a[168:171], v[24:27], v[72:75]\n\t" \
    "ds_read_b128 v[48:51], v23 offset:512\n\t" \
    "s_waitcnt lgkmcnt(5)\n\t" \
    "v_mfma_f32_16x16x32_bf16 v[76:79], a[168:171], v[28:31], v[76:79]\n\t" \
    "ds_read_b128 v[52:55], v23 offset:18944\n\t" \
    "v_cvt_pk_bf16_f32 v111, v102, v103\n\t" \
    "v_mfma_f32_16x16x32_bf16 v[80:83], a[240:243], v[24:27], v[80:83]\n\t" \
    "s_add_u32 m0, s91, 43008\n\t" \
    "global_store_dwordx4 v8, v[108:111], s[38:39] offset:1024 sc1\n\t" \
    "v_mfma_f32_16x16x32_bf16 v[84:87], a[240:243], v[28:31], v[84:87]\n\t" \
    "global_load_lds_dwordx4 v209, s[48:49]\n\t" \
    "s_waitcnt lgkmcnt(5)\n\t" \
    "v_mfma_f32_16x16x32_bf16 v[72:75], a[172:175], v[32:35], v[72:75]\n\t" \
    "ds_read_b128 v[56:59], v22 offset:640\n\t" \
    "s_waitcnt lgkmcnt(5)\n\t" \
    "v_mfma_f32_16x16x32_bf16 v[76:79], a[172:175], v[36:39], v[76:79]\n\t" \
    "ds_read_b128 v[60:63], v22 offset:19072\n\t" \
    "v_mfma_f32_16x16x32_bf16 v[80:83], a[244:247], v[32:35], v[80:83]\n\t" \
    "s_add_u32 m0, s91, 44032\n\t" \
    "v_mfma_f32_16x16x32_bf16 v[84:87], a[244:247], v[36:39], v[84:87]\n\t" \
    "global_load_lds_dwordx4 v210, s[48:49]\n\t" \
    "s_waitcnt lgkmcnt(5)\n\t" \
    "v_mfma_f32_16x16x32_bf16 v[72:75], a[176:179], v[40:43], v[72:75]\n\t" \
    "ds_read_b128 v[64:67], v23 offset:640\n\t" \
    "s_waitcnt lgkmcnt(5)\n\t" \
    "v_mfma_f32_16x16x32_bf16 v[76:79], a[176:179], v[44:47], v[76:79]\n\t" \
    "ds_read_b128 v[68:71], v23 offset:19072\n\t" \
    "v_mfma_f32_16x16x32_bf16 v[80:83], a[248:251], v[40:43], v[80:83]\n\t" \
    "s_add_u32 m0, s91, 45056\n\t" \
    "v_mfma_f32_16x16x32_bf16 v[84:87], a[248:251], v[44:47], v[84:87]\n\t" \
    "global_load_lds_dwordx4 v211, s[48:49]\n\t" \
    "s_waitcnt lgkmcnt(5)\n\t" \
    "v_mfma_f32_16x16x32_bf16 v[72:75], a[180:183], v[48:51], v[72:75]\n\t" \
    "ds_read_b128 v[24:27], v22 offset:768\n\t" \
    "s_waitcnt lgkmcnt(5)\n\t" \
    "v_mfma_f32_16x16x32_bf16 v[76:79], a[180:183], v[52:55], v[76:79]\n\t" \
    "ds_read_b128 v[28:31], v22 offset:19200\n\t" \
    "v_mfma_f32_16x16x32_bf16 v[80:83], a[252:255], v[48:51], v[80:83]\n\t" \
    "v_mfma_f32_16x16x32_bf16 v[84:87], a[252:255], v[52:55], v[84:87]\n\t" \
    "s_waitcnt lgkmcnt(5)\n\t" \
    "v_mfma_f32_16x16x32_bf16 v[72:75], a[184:187], v[56:59], v[72:75]\n\t" \
    "ds_read_b128 v[32:35], v23 offset:768\n\t" \
    "s_waitcnt lgkmcnt(5)\n\t" \
    "v_mfma_f32_16x16x32_bf16 v[76:79], a[184:187], v[60:63], v[76:79]\n\t" \
    "ds_read_b128 v[36:39], v23 offset:19200\n\t" \
    "v_mfma_f32_16x16x32_bf16 v[80:83], v[224:227], v[56:59], v[80:83]\n\t" \
    "s_waitcnt vmcnt(11)\n\t" \
    "s_barrier\n\t" \
    "v_mfma_f32_16x16x32_bf16 v[84:87], v[224:227], v[60:63], v[84:87]\n\t" \
    "s_add_u32 s71, s65, 8\n\t" \
    "s_cmp_lg_u32 s66, 0\n\t" \
    "s_cbranch_scc1 gx_SF14_%=\n\t" \
    "s_lshl_b32 s69, s67, 7\n\t" \
    "s_add_u32 s69, s69, 3149824\n\t" \
    "s_add_u32 s70, s71, 0\n\t" \
    "v_mov_b32 v156, s69\n\t" \
    "v_mov_b32 v157, s70\n\t" \
    "s_mov_b64 exec, 1\n\t" \
    "global_store_dword v156, v157, s[100:101] sc1\n\t" \
    "s_mov_b64 exec, -1\n\t" \
    "gx_SF14_%=:\n\t" \
    "s_waitcnt lgkmcnt(5)\n\t" \
    "v_mfma_f32_16x16x32_bf16 v[72:75], a[188:191], v[64:67], v[72:75]\n\t" \
    "ds_read_b128 v[40:43], v22 offset:896\n\t" \
    "s_waitcnt lgkmcnt(5)\n\t" \
    "v_mfma_f32_16x16x32_bf16 v[76:79], a[188:191], v[68:71], v[76:79]\n\t" \
    "ds_read_b128 v[44:47], v22 offset:19328\n\t" \
    "v_mfma_f32_16x16x32_bf16 v[80:83], v[228:231], v[64:67], v[80:83]\n\t" \
    "s_xor_b32 s90, s90, 0x12000\n\t" \
    "v_add_u32 v1, s90, v0\n\t" \
    "v_mfma_f32_16x16x32_bf16 v[84:87], v[228:231], v[68:71], v[84:87]\n\t" \
    "v_xor_b32 v2, 64, v1\n\t" \
    "s_add_u32 s84, s65, 2\n\t" \
    "s_waitcnt lgkmcnt(5)\n\t" \
    "v_mfma_f32_16x16x32_bf16 v[72:75], a[192:195], v[24:27], v[72:75]\n\t" \
    "ds_read_b128 v[48:51], v23 offset:896\n\t" \
    "s_waitcnt lgkmcnt(5)\n\t" \
    "v_mfma_f32_16x16x32_bf16 v[76:79], a[192:195], v[28:31], v[76:79]\n\t" \
    "ds_read_b128 v[52:55], v23 offset:19328\n\t" \
    "v_mfma_f32_16x16x32_bf16 v[80:83], v[232:235], v[24:27], v[80:83]\n\t" \
    "s_min_u32 s69, s84, 63\n\t" \
    "s_mul_i32 s69, s69, 36864\n\t" \
    "v_mfma_f32_16x16x32_bf16 v[84:87], v[232:235], v[28:31], v[84:87]\n\t" \
    "s_add_u32 s52, s50, s69\n\t" \
    "s_addc_u32 s53, s51, 0\n\t" \
    "s_waitcnt lgkmcnt(5)\n\t" \
    "v_mfma_f32_16x16x32_bf16 v[72:75], a[196:199], v[32:35], v[72:75]\n\t" \
    "ds_read_b128 v[56:59], v22 offset:1024\n\t" \
    "s_waitcnt lgkmcnt(5)\n\t" \
    "v_mfma_f32_16x16x32_bf16 v[76:79], a[196:199], v[36:39], v[76:79]\n\t" \
    "ds_read_b128 v[60:63], v22 offset:19456\n\t" \
    "v_mfma_f32_16x16x32_bf16 v[80:83], v[236:239], v[32:35], v[80:83]\n\t" \
    "s_mul_i32 s70, s67, 7\n\t" \
    "s_add_u32 s69, s65, 9\n\t" \
    "v_mfma_f32_16x16x32_bf16 v[84:87], v[236:239], v[36:39], v[84:87]\n\t" \
    "s_xor_b32 s69, s69, s70\n\t" \
    "s_and_b32 s69, s69, 63\n\t" \
    "s_waitcnt lgkmcnt(5)\n\t" \
    "v_mfma_f32_16x16x32_bf16 v[72:75], a[200:203], v[40:43], v[72:75]\n\t" \
    "ds_read_b128 v[64:67], v23 offset:1024\n\t" \
    "s_waitcnt lgkmcnt(5)\n\t" \
    "v_mfma_f32_16x16x32_bf16 v[76:79], a[200:203], v[44:47], v[76:79]\n\t" \
    "ds_read_b128 v[68:71], v23 offset:19456\n\t" \
    "v_mfma_f32_16x16x32_bf16 v[80:83], v[240:243], v[40:43], v[80:83]\n\t" \
    "s_lshl_b32 s69, s69, 13\n\t" \
    "s_mul_i32 s70, s67, 524288\n\t" \
    "v_mfma_f32_16x16x32_bf16 v[84:87], v[240:243], v[44:47], v[84:87]\n\t" \
    "s_add_u32 s69, s69, s70\n\t" \
    "s_add_u32 s69, s69, 23068672\n\t" \
    "s_waitcnt lgkmcnt(5)\n\t" \
    "v_mfma_f32_16x16x32_bf16 v[72:75], a[204:207], v[48:51], v[72:75]\n\t" \
    "ds_read_b128 v[24:27], v1 offset:0\n\t" \
    "s_waitcnt lgkmcnt(5)\n\t" \
    "v_mfma_f32_16x16x32_bf16 v[76:79], a[204:207], v[52:55], v[76:79]\n\t" \
    "ds_read_b128 v[28:31], v1 offset:18432\n\t" \
    "v_mfma_f32_16x16x32_bf16 v[80:83], v[244:247], v[48:51], v[80:83]\n\t" \
    "s_add_u32 s40, s36, s69\n\t" \
    "s_addc_u32 s41, s37, 0\n\t" \
    "v_mfma_f32_16x16x32_bf16 v[84:87], v[244:247], v[52:55], v[84:87]\n\t" \
    "s_sub_u32 s91, s85, s91\n\t" \
    "s_add_u32 s65, s65, 1\n\t" \
    "s_waitcnt lgkmcnt(5)\n\t" \
    "v_mfma_f32_16x16x32_bf16 v[72:75], a[208:211], v[56:59], v[72:75]\n\t" \
    "ds_read_b128 v[32:35], v2 offset:0\n\t" \
    "s_waitcnt lgkmcnt(5)\n\t" \
    "v_mfma_f32_16x16x32_bf16 v[76:79], a[208:211], v[60:63], v[76:79]\n\t" \
    "ds_read_b128 v[36:39], v2 offset:18432\n\t" \
    "v_mfma_f32_16x16x32_bf16 v[80:83], v[248:251], v[56:59], v[80:83]\n\t" \
    "v_mfma_f32_16x16x32_bf16 v[84:87], v[248:251], v[60:63], v[84:87]\n\t" \
    "s_waitcnt lgkmcnt(5)\n\t" \
    "v_mfma_f32_16x16x32_bf16 v[72:75], a[212:215], v[64:67], v[72:75]\n\t" \
    "ds_read_b128 v[40:43], v1 offset:128\n\t" \
    "s_waitcnt lgkmcnt(5)\n\t" \
    "v_mfma_f32_16x16x32_bf16 v[76:79], a[212:215], v[68:71], v[76:79]\n\t" \
    "ds_read_b128 v[44:47], v1 offset:18560\n\t" \
    "v_mfma_f32_16x16x32_bf16 v[80:83], v[252:255], v[64:67], v[80:83]\n\t" \
    "v_mfma_f32_16x16x32_bf16 v[84:87], v[252:255], v[68:71], v[84:87]\n\t" \
    "s_cmp_lt_u32 s65, 55\n\t" \
    "s_cbranch_scc1 gx_l28_%=\n\t" \
    "v_cvt_pk_bf16_f32 v104, v72, v73\n\t" \
    "v_cvt_pk_bf16_f32 v105, v74, v75\n\t" \
    "s_nop 2\n\t" \
    "v_cvt_pk_bf16_f32 v106, v80, v81\n\t" \
    "v_cvt_pk_bf16_f32 v107, v82, v83\n\t" \
    "global_store_dwordx4 v8, v[104:107], s[40:41] offset:0 sc1\n\t" \
    "v_cvt_pk_bf16_f32 v108, v76, v77\n\t" \
    "v_cvt_pk_bf16_f32 v109, v78, v79\n\t" \
    "v_cvt_pk_bf16_f32 v110, v84, v85\n\t" \
    "v_cvt_pk_bf16_f32 v111, v86, v87\n\t" \
    "global_store_dwordx4 v8, v[108:111], s[40:41] offset:1024 sc1\n\t" \
    "gx_l315_%=:\n\t" \
    "s_add_u32 s84, s65, 2\n\t" \
    "s_min_u32 s69, s84, 63\n\t" \
    "s_mul_i32 s69, s69, 36864\n\t" \
    "s_add_u32 s52, s50, s69\n\t" \
    "s_addc_u32 s53, s51, 0\n\t" \
    "s_add_u32 s90, s90, 36864\n\t" \
    "s_cmp_eq_u32 s90, 147456\n\t" \
    "s_cselect_b32 s90, 0, s90\n\t" \
    "s_add_u32 s91, s90, 36864\n\t" \
    "s_cmp_eq_u32 s91, 147456\n\t" \
    "s_cselect_b32 s91, 0, s91\n\t" \
    "s_add_u32 s91, s91, s89\n\t" \
    "v_add_u32 v22, s90, v0\n\t" \
    "v_xor_b32 v23, 64, v22\n\t" \
    "s_mul_i32 s70, s67, 7\n\t" \
    "s_add_u32 s69, s65, 0\n\t" \
    "s_xor_b32 s69, s69, s70\n\t" \
    "s_and_b32 s69, s69, 63\n\t" \
    "s_lshl_b32 s69, s69, 13\n\t" \
    "s_mul_i32 s70, s67, 524288\n\t" \
    "s_add_u32 s69, s69, s70\n\t" \
    "s_add_u32 s69, s69, 6291456\n\t" \
    "s_add_u32 s38, s36, s69\n\t" \
    "s_addc_u32 s39, s37, 0\n\t" \
    "s_waitcnt lgkmcnt(5)\n\t" \
    "v_mfma_f32_16x16x32_bf16 v[88:91], a[0:3], v[24:27], 0\n\t" \
    "ds_read_b128 v[48:51], v2 offset:128\n\t" \
    "s_waitcnt lgkmcnt(5)\n\t" \
    "v_mfma_f32_16x16x32_bf16 v[92:95], a[0:3], v[28:31], 0\n\t" \
    "ds_read_b128 v[52:55], v2 offset:18560\n\t" \
    "v_mfma_f32_16x16x32_bf16 v[96:99], a[72:75], v[24:27], 0\n\t" \
    "s_add_u32 m0, s91, 0\n\t" \
    "v_mfma_f32_16x16x32_bf16 v[100:103], a[72:75], v[28:31], 0\n\t" \
    "global_load_lds_dwordx4 v203, s[52:53]\n\t" \
    "s_waitcnt lgkmcnt(5)\n\t" \
    "v_mfma_f32_16x16x32_bf16 v[88:91], a[4:7], v[32:35], v[88:91]\n\t" \
    "ds_read_b128 v[56:59], v1 offset:256\n\t" \
    "s_waitcnt lgkmcnt(5)\n\t" \
    "v_mfma_f32_16x16x32_bf16 v[92:95], a[4:7], v[36:39], v[92:95]\n\t" \
    "ds_read_b128 v[60:63], v1 offset:18688\n\t" \
    "v_mfma_f32_16x16x32_bf16 v[96:99], a[76:79], v[32:35], v[96:99]\n\t" \
    "s_add_u32 m0, s91, 1024\n\t" \
    "v_mfma_f32_16x16x32_bf16 v[100:103], a[76:79], v[36:39], v[100:103]\n\t" \
    "global_load_lds_dwordx4 v204, s[52:53]\n\t" \
    "s_waitcnt lgkmcnt(5)\n\t" \
    "v_mfma_f32_16x16x32_bf16 v[88:91], a[8:11], v[40:43], v[88:91]\n\t" \
    "ds_read_b128 v[64:67], v2 offset:256\n\t" \
    "s_waitcnt lgkmcnt(5)\n\t" \
    "v_mfma_f32_16x16x32_bf16 v[92:95], a[8:11], v[44:47], v[92:95]\n\t" \
    "ds_read_b128 v[68:71], v2 offset:18688\n\t" \
    "v_mfma_f32_16x16x32_bf16 v[96:99], a[80:83], v[40:43], v[96:99]\n\t" \
    "s_add_u32 m0, s91, 2048\n\t" \
    "v_mfma_f32_16x16x32_bf16 v[100:103], a[80:83], v[44:47], v[100:103]\n\t" \
    "global_load_lds_dwordx4 v205, s[52:53]\n\t" \
    "s_waitcnt lgkmcnt(5)\n\t" \
    "v_mfma_f32_16x16x32_bf16 v[88:91], a[12:15], v[48:51], v[88:91]\n\t" \
    "ds_read_b128 v[24:27], v1 offset:384\n\t" \
    "s_waitcnt lgkmcnt(5)\n\t" \
    "v_mfma_f32_16x16x32_bf16 v[92:95], a[12:15], v[52:55], v[92:95]\n\t" \
    "ds_read_b128 v[28:31], v1 offset:18816\n\t" \
    "v_mfma_f32_16x16x32_bf16 v[96:99], a[84:87], v[48:51], v[96:99]\n\t" \
    "s_add_u32 m0, s91, 3072\n\t" \
    "v_mfma_f32_16x16x32_bf16 v[100:103], a[84:87], v[52:55], v[100:103]\n\t" \
    "global_load_lds_dwordx4 v206, s[52:53]\n\t" \
    "s_waitcnt lgkmcnt(5)\n\t" \
    "v_mfma_f32_16x16x32_bf16 v[88:91], a[16:19], v[56:59], v[88:91]\n\t" \
    "ds_read_b128 v[32:35], v2 offset:384\n\t" \
    "s_waitcnt lgkmcnt(5)\n\t" \
    "v_mfma_f32_16x16x32_bf16 v[92:95], a[16:19], v[60:63], v[92:95]\n\t" \
    "ds_read_b128 v[36:39], v2 offset:18816\n\t" \
    "v_mfma_f32_16x16x32_bf16 v[96:99], a[88:91], v[56:59], v[96:99]\n\t" \
    "s_add_u32 m0, s91, 4096\n\t" \
    "v_mfma_f32_16x16x32_bf16 v[100:103], a[88:91], v[60:63], v[100:103]\n\t" \
    "global_load_lds_dwordx4 v207, s[52:53]\n\t" \
    "s_waitcnt lgkmcnt(5)\n\t" \
    "v_mfma_f32_16x16x32_bf16 v[88:91], a[20:23], v[64:67], v[88:91]\n\t" \
    "ds_read_b128 v[40:43], v1 offset:512\n\t" \
    "s_waitcnt lgkmcnt(5)\n\t" \
    "v_mfma_f32_16x16x32_bf16 v[92:95], a[20:23], v[68:71], v[92:95]\n\t" \
    "ds_read_b128 v[44:47], v1 offset:18944\n\t" \
    "v_mfma_f32_16x16x32_bf16 v[96:99], a[92:95], v[64:67], v[96:99]\n\t" \
    "s_add_u32 m0, s91, 5120\n\t" \
    "v_mfma_f32_16x16x32_bf16 v[100:103], a[92:95], v[68:71], v[100:103]\n\t" \
    "global_load_lds_dwordx4 v208, s[52:53]\n\t" \
    "s_waitcnt lgkmcnt(5)\n\t" \
    "v_mfma_f32_16x16x32_bf16 v[88:91], a[24:27], v[24:27], v[88:91]\n\t" \
    "ds_read_b128 v[48:51], v2 offset:512\n\t" \
    "s_waitcnt lgkmcnt(5)\n\t" \
    "v_mfma_f32_16x16x32_bf16 v[92:95], a[24:27], v[28:31], v[92:95]\n\t" \
    "ds_read_b128 v[52:55], v2 offset:18944\n\t" \
    "v_mfma_f32_16x16x32_bf16 v[96:99], a[96:99], v[24:27], v[96:99]\n\t" \
    "s_add_u32 m0, s91, 6144\n\t" \
    "v_mfma_f32_16x16x32_bf16 v[100:103], a[96:99], v[28:31], v[100:103]\n\t" \
    "global_load_lds_dwordx4 v209, s[52:53]\n\t" \
    "s_waitcnt lgkmcnt(5)\n\t" \
    "v_mfma_f32_16x16x32_bf16 v[88:91], a[28:31], v[32:35], v[88:91]\n\t" \
    "ds_read_b128 v[56:59], v1 offset:640\n\t" \
    "s_waitcnt lgkmcnt(5)\n\t" \
    "v_mfma_f32_16x16x32_bf16 v[92:95], a[28:31], v[36:39], v[92:95]\n\t" \
    "ds_read_b128 v[60:63], v1 offset:19072\n\t" \
    "v_mfma_f32_16x16x32_bf16 v[96:99], a[100:103], v[32:35], v[96:99]\n\t" \
    "s_add_u32 m0, s91, 7168\n\t" \
    "v_mfma_f32_16x16x32_bf16 v[100:103], a[100:103], v[36:39], v[100:103]\n\t" \
    "global_load_lds_dwordx4 v210, s[52:53]\n\t" \
    "s_waitcnt lgkmcnt(5)\n\t" \
    "v_mfma_f32_16x16x32_bf16 v[88:91], a[32:35], v[40:43], v[88:91]\n\t" \
    "ds_read_b128 v[64:67], v2 offset:640\n\t" \
    "s_waitcnt lgkmcnt(5)\n\t" \
    "v_mfma_f32_16x16x32_bf16 v[92:95], a[32:35], v[44:47], v[92:95]\n\t" \
    "ds_read_b128 v[68:71], v2 offset:19072\n\t" \
    "v_mfma_f32_16x16x32_bf16 v[96:99], a[104:107], v[40:43], v[96:99]\n\t" \
    "s_add_u32 m0, s91, 8192\n\t" \
    "v_mfma_f32_16x16x32_bf16 v[100:103], a[104:107], v[44:47], v[100:103]\n\t" \
    "global_load_lds_dwordx4 v211, s[52:53]\n\t" \
    "s_waitcnt lgkmcnt(5)\n\t" \
    "v_mfma_f32_16x16x32_bf16 v[88:91], a[36:39], v[48:51], v[88:91]\n\t" \
    "ds_read_b128 v[24:27], v1 offset:768\n\t" \
    "s_waitcnt lgkmcnt(5)\n\t" \
    "v_mfma_f32_16x16x32_bf16 v[92:95], a[36:39], v[52:55], v[92:95]\n\t" \
    "ds_read_b128 v[28:31], v1 offset:19200\n\t" \
    "v_mfma_f32_16x16x32_bf16 v[96:99], a[108:111], v[48:51], v[96:99]\n\t" \
    "v_mfma_f32_16x16x32_bf16 v[100:103], a[108:111], v[52:55], v[100:103]\n\t" \
    "s_waitcnt lgkmcnt(5)\n\t" \
    "v_mfma_f32_16x16x32_bf16 v[88:91], a[40:43], v[56:59], v[88:91]\n\t" \
    "ds_read_b128 v[32:35], v2 offset:768\n\t" \
    "s_waitcnt lgkmcnt(5)\n\t" \
    "v_mfma_f32_16x16x32_bf16 v[92:95], a[40:43], v[60:63], v[92:95]\n\t" \
    "ds_read_b128 v[36:39], v2 offset:19200\n\t" \
    "v_mfma_f32_16x16x32_bf16 v[96:99], a[112:115], v[56:59], v[96:99]\n\t" \
    "s_waitcnt vmcnt(9)\n\t" \
    "s_barrier\n\t" \
    "v_mfma_f32_16x16x32_bf16 v[100:103], a[112:115], v[60:63], v[100:103]\n\t" \
    "s_cmp_lg_u32 s66, 0\n\t" \
    "s_cbranch_scc1 gx_SF16_%=\n\t" \
    "s_lshl_b32 s69, s67, 7\n\t" \
    "s_add_u32 s69, s69, 3211264\n\t" \
    "s_add_u32 s70, s65, -1\n\t" \
    "v_mov_b32 v156, s69\n\t" \
    "v_mov_b32 v157, s70\n\t" \
    "s_mov_b64 exec, 1\n\t" \
    "global_store_dword v156, v157, s[100:101] sc1\n\t" \
    "s_mov_b64 exec, -1\n\t" \
    "gx_SF16_%=:\n\t" \
    "s_cmp_lg_u32 s66, 1\n\t" \
    "s_cbranch_scc1 gx_SF17_%=\n\t" \
    "s_lshl_b32 s69, s67, 7\n\t" \
    "s_add_u32 s69, s69, 3149824\n\t" \
    "s_add_u32 s70, s65, 40\n\t" \
    "v_mov_b32 v156, s69\n\t" \
    "v_mov_b32 v157, s70\n\t" \
    "s_mov_b64 exec, 1\n\t" \
    "global_store_dword v156, v157, s[100:101] sc1\n\t" \
    "s_mov_b64 exec, -1\n\t" \
    "gx_SF17_%=:\n\t" \
    "s_waitcnt lgkmcnt(5)\n\t" \
    "v_mfma_f32_16x16x32_bf16 v[88:91], a[44:47], v[64:67], v[88:91]\n\t" \
    "ds_read_b128 v[40:43], v1 offset:896\n\t" \
    "s_waitcnt lgkmcnt(5)\n\t" \
    "v_mfma_f32_16x16x32_bf16 v[92:95], a[44:47], v[68:71], v[92:95]\n\t" \
    "ds_read_b128 v[44:47], v1 offset:19328\n\t" \
    "v_mfma_f32_16x16x32_bf16 v[96:99], a[116:119], v[64:67], v[96:99]\n\t" \
    "v_mfma_f32_16x16x32_bf16 v[100:103], a[116:119], v[68:71], v[100:103]\n\t" \
    "s_waitcnt lgkmcnt(5)\n\t" \
    "v_mfma_f32_16x16x32_bf16 v[88:91], a[48:51], v[24:27], v[88:91]\n\t" \
    "ds_read_b128 v[48:51], v2 offset:896\n\t" \
    "s_waitcnt lgkmcnt(5)\n\t" \
    "v_mfma_f32_16x16x32_bf16 v[92:95], a[48:51], v[28:31], v[92:95]\n\t" \
    "ds_read_b128 v[52:55], v2 offset:19328\n\t" \
    "v_mfma_f32_16x16x32_bf16 v[96:99], a[120:123], v[24:27], v[96:99]\n\t" \
    "v_mfma_f32_16x16x32_bf16 v[100:103], a[120:123], v[28:31], v[100:103]\n\t" \
    "s_waitcnt lgkmcnt(5)\n\t" \
    "v_mfma_f32_16x16x32_bf16 v[88:91], a[52:55], v[32:35], v[88:91]\n\t" \
    "ds_read_b128 v[56:59], v1 offset:1024\n\t" \
    "s_waitcnt lgkmcnt(5)\n\t" \
    "v_mfma_f32_16x16x32_bf16 v[92:95], a[52:55], v[36:39], v[92:95]\n\t" \
    "ds_read_b128 v[60:63], v1 offset:19456\n\t" \
    "v_mfma_f32_16x16x32_bf16 v[96:99], a[124:127], v[32:35], v[96:99]\n\t" \
    "v_mfma_f32_16x16x32_bf16 v[100:103], a[124:127], v[36:39], v[100:103]\n\t" \
    "s_waitcnt lgkmcnt(5)\n\t" \
    "v_mfma_f32_16x16x32_bf16 v[88:91], a[56:59], v[40:43], v[88:91]\n\t" \
    "ds_read_b128 v[64:67], v2 offset:1024\n\t" \
    "s_waitcnt lgkmcnt(5)\n\t" \
    "v_mfma_f32_16x16x32_bf16 v[92:95], a[56:59], v[44:47], v[92:95]\n\t" \
    "ds_read_b128 v[68:71], v2 offset:19456\n\t" \
    "v_mfma_f32_16x16x32_bf16 v[96:99], a[128:131], v[40:43], v[96:99]\n\t" \
    "v_mfma_f32_16x16x32_bf16 v[100:103], a[128:131], v[44:47], v[100:103]\n\t" \
    "s_waitcnt lgkmcnt(5)\n\t" \
    "v_mfma_f32_16x16x32_bf16 v[88:91], a[60:63], v[48:51], v[88:91]\n\t" \
    "ds_read_b128 v[24:27], v22 offset:0\n\t" \
    "s_waitcnt lgkmcnt(5)\n\t" \
    "v_mfma_f32_16x16x32_bf16 v[92:95], a[60:63], v[52:55], v[92:95]\n\t" \
    "ds_read_b128 v[28:31], v22 offset:18432\n\t" \
    "v_mfma_f32_16x16x32_bf16 v[96:99], a[132:135], v[48:51], v[96:99]\n\t" \
    "v_mfma_f32_16x16x32_bf16 v[100:103], a[132:135], v[52:55], v[100:103]\n\t" \
    "s_waitcnt lgkmcnt(5)\n\t" \
    "v_mfma_f32_16x16x32_bf16 v[88:91], a[64:67], v[56:59], v[88:91]\n\t" \
    "ds_read_b128 v[32:35], v23 offset:0\n\t" \
    "s_waitcnt lgkmcnt(5)\n\t" \
    "v_mfma_f32_16x16x32_bf16 v[92:95], a[64:67], v[60:63], v[92:95]\n\t" \
    "ds_read_b128 v[36:39], v23 offset:18432\n\t" \
    "v_mfma_f32_16x16x32_bf16 v[96:99], a[136:139], v[56:59], v[96:99]\n\t" \
    "v_mfma_f32_16x16x32_bf16 v[100:103], a[136:139], v[60:63], v[100:103]\n\t" \
    "s_waitcnt lgkmcnt(5)\n\t" \
    "v_mfma_f32_16x16x32_bf16 v[88:91], a[68:71], v[64:67], v[88:91]\n\t" \
    "ds_read_b128 v[40:43], v22 offset:128\n\t" \
    "s_waitcnt lgkmcnt(5)\n\t" \
    "v_mfma_f32_16x16x32_bf16 v[92:95], a[68:71], v[68:71], v[92:95]\n\t" \
    "ds_read_b128 v[44:47], v22 offset:18560\n\t" \
    "v_mfma_f32_16x16x32_bf16 v[96:99], a[140:143], v[64:67], v[96:99]\n\t" \
    "v_mfma_f32_16x16x32_bf16 v[100:103], a[140:143], v[68:71], v[100:103]\n\t" \
    "v_mov_b32 v1, v22\n\t" \
    "v_mov_b32 v2, v23\n\t" \
    "v_cvt_pk_bf16_f32 v104, v88, v89\n\t" \
    "v_cvt_pk_bf16_f32 v105, v90, v91\n\t" \
    "s_nop 2\n\t" \
    "v_cvt_pk_bf16_f32 v106, v96, v97\n\t" \
    "v_cvt_pk_bf16_f32 v107, v98, v99\n\t" \
    "global_store_dwordx4 v8, v[104:107], s[38:39] offset:0 sc1\n\t" \
    "v_cvt_pk_bf16_f32 v108, v92, v93\n\t" \
    "v_cvt_pk_bf16_f32 v109, v94, v95\n\t" \
    "v_cvt_pk_bf16_f32 v110, v100, v101\n\t" \
    "v_cvt_pk_bf16_f32 v111, v102, v103\n\t" \
    "global_store_dwordx4 v8, v[108:111], s[38:39] offset:1024 sc1\n\t" \
    "s_add_u32 s65, s65, 1\n\t" \
    "s_cmp_lt_u32 s65, 64\n\t" \
    "s_cbranch_scc1 gx_l315_%=\n\t" \
    "s_waitcnt vmcnt(0) lgkmcnt(0)\n\t" \
    "s_barrier\n\t" \
    "s_cmp_lg_u32 s66, 0\n\t" \
    "s_cbranch_scc1 gx_SF18_%=\n\t" \
    "s_lshl_b32 s69, s67, 7\n\t" \
    "s_add_u32 s69, s69, 3211264\n\t" \
    "s_add_u32 s70, s65, -1\n\t" \
    "v_mov_b32 v156, s69\n\t" \
    "v_mov_b32 v157, s70\n\t" \
    "s_mov_b64 exec, 1\n\t" \
    "global_store_dword v156, v157, s[100:101] sc1\n\t" \
    "s_mov_b64 exec, -1\n\t" \
    "gx_SF18_%=:\n\t" \
    "gx_last_%=:\n\t" \
    ""
#define ASM_GV \
    "s_load_dwordx2 s[42:43], %3, 56\n\t" \
    "s_load_dwordx2 s[44:45], %3, 64\n\t" \
    "s_load_dwordx2 s[36:37], %3, 0x58\n\t" \
    "s_load_dwordx2 s[50:51], %3, 0x48\n\t" \
    "s_mov_b32 s66, %4\n\t" \
    "s_mov_b32 s67, %5\n\t" \
    "s_mov_b32 s68, 262144\n\t" \
    "s_mov_b32 s72, 0\n\t" \
    "s_mov_b32 s83, -1\n\t" \
    "s_mov_b32 s65, 0\n\t" \
    "s_lshl_b32 s82, s66, 10\n\t" \
    "s_waitcnt lgkmcnt(0)\n\t" \
    "s_lshr_b32 s65, s67, 5\n\t" \
    "s_cselect_b32 s88, 24, 64\n\t" \
    "s_cmp_lt_u32 s65, 24\n\t" \
    "s_addc_u32 s84, s65, 1\n\t" \
    "s_cmp_lt_u32 s84, 24\n\t" \
    "s_addc_u32 s85, s84, 1\n\t" \
    "s_mov_b32 s86, -1\n\t" \
    "s_mov_b32 s87, -1\n\t" \
    "s_add_u32 s38, s36, 48234496\n\t" \
    "s_addc_u32 s39, s37, 0\n\t" \
    "s_and_b32 s69, s67, 31\n\t" \
    "s_mul_i32 s69, s69, 147456\n\t" \
    "s_add_u32 s44, s44, s69\n\t" \
    "s_addc_u32 s45, s45, 0\n\t" \
    "s_mul_i32 s89, s66, 9216\n\t" \
    "s_mov_b32 s90, 0\n\t" \
    "v_mov_b32 v196, -1\n\t" \
    "v_mov_b32 v197, -1\n\t" \
    "v_mov_b32 v198, -1\n\t" \
    "v_mov_b32 v199, -1\n\t" \
    "s_add_u32 s46, s42, 2097152\n\t" \
    "s_addc_u32 s47, s43, 0\n\t" \
    "s_add_u32 m0, s82, 65536\n\t" \
    "s_nop 0\n\t" \
    "global_load_lds_dwordx4 v200, s[46:47]\n\t" \
    "s_add_u32 s46, s42, 2228224\n\t" \
    "s_addc_u32 s47, s43, 0\n\t" \
    "s_add_u32 m0, s82, 69632\n\t" \
    "s_nop 0\n\t" \
    "global_load_lds_dwordx4 v200, s[46:47]\n\t" \
    "s_add_u32 s46, s42, 2359296\n\t" \
    "s_addc_u32 s47, s43, 0\n\t" \
    "s_add_u32 m0, s82, 73728\n\t" \
    "s_nop 0\n\t" \
    "global_load_lds_dwordx4 v200, s[46:47]\n\t" \
    "s_add_u32 s46, s42, 2490368\n\t" \
    "s_addc_u32 s47, s43, 0\n\t" \
    "s_add_u32 m0, s82, 77824\n\t" \
    "s_nop 0\n\t" \
    "global_load_lds_dwordx4 v200, s[46:47]\n\t" \
    "s_add_u32 s46, s42, 2621440\n\t" \
    "s_addc_u32 s47, s43, 0\n\t" \
    "s_add_u32 m0, s82, 81920\n\t" \
    "s_nop 0\n\t" \
    "global_load_lds_dwordx4 v200, s[46:47]\n\t" \
    "s_add_u32 s46, s42, 2752512\n\t" \
    "s_addc_u32 s47, s43, 0\n\t" \
    "s_add_u32 m0, s82, 86016\n\t" \
    "s_nop 0\n\t" \
    "global_load_lds_dwordx4 v200, s[46:47]\n\t" \
    "s_add_u32 s46, s42, 2883584\n\t" \
    "s_addc_u32 s47, s43, 0\n\t" \
    "s_add_u32 m0, s82, 90112\n\t" \
    "s_nop 0\n\t" \
    "global_load_lds_dwordx4 v200, s[46:47]\n\t" \
    "s_add_u32 s46, s42, 3014656\n\t" \
    "s_addc_u32 s47, s43, 0\n\t" \
    "s_add_u32 m0, s82, 94208\n\t" \
    "s_nop 0\n\t" \
    "global_load_lds_dwordx4 v200, s[46:47]\n\t" \
    "s_add_u32 s46, s42, 3145728\n\t" \
    "s_addc_u32 s47, s43, 0\n\t" \
    "s_add_u32 m0, s82, 98304\n\t" \
    "s_nop 0\n\t" \
    "global_load_lds_dwordx4 v200, s[46:47]\n\t" \
    "s_add_u32 s46, s42, 3276800\n\t" \
    "s_addc_u32 s47, s43, 0\n\t" \
    "s_add_u32 m0, s82, 102400\n\t" \
    "s_nop 0\n\t" \
    "global_load_lds_dwordx4 v200, s[46:47]\n\t" \
    "s_add_u32 s46, s42, 3407872\n\t" \
    "s_addc_u32 s47, s43, 0\n\t" \
    "s_add_u32 m0, s82, 106496\n\t" \
    "s_nop 0\n\t" \
    "global_load_lds_dwordx4 v200, s[46:47]\n\t" \
    "s_add_u32 s46, s42, 3538944\n\t" \
    "s_addc_u32 s47, s43, 0\n\t" \
    "s_add_u32 m0, s82, 110592\n\t" \
    "s_nop 0\n\t" \
    "global_load_lds_dwordx4 v200, s[46:47]\n\t" \
    "s_add_u32 s46, s42, 3670016\n\t" \
    "s_addc_u32 s47, s43, 0\n\t" \
    "s_add_u32 m0, s82, 114688\n\t" \
    "s_nop 0\n\t" \
    "global_load_lds_dwordx4 v200, s[46:47]\n\t" \
    "s_add_u32 s46, s42, 3801088\n\t" \
    "s_addc_u32 s47, s43, 0\n\t" \
    "s_add_u32 m0, s82, 118784\n\t" \
    "s_nop 0\n\t" \
    "global_load_lds_dwordx4 v200, s[46:47]\n\t" \
    "s_add_u32 s46, s42, 3932160\n\t" \
    "s_addc_u32 s47, s43, 0\n\t" \
    "s_add_u32 m0, s82, 122880\n\t" \
    "s_nop 0\n\t" \
    "global_load_lds_dwordx4 v200, s[46:47]\n\t" \
    "s_add_u32 s46, s42, 4063232\n\t" \
    "s_addc_u32 s47, s43, 0\n\t" \
    "s_add_u32 m0, s82, 126976\n\t" \
    "s_nop 0\n\t" \
    "global_load_lds_dwordx4 v200, s[46:47]\n\t" \
    "s_add_u32 s46, s42, 4194304\n\t" \
    "s_addc_u32 s47, s43, 0\n\t" \
    "s_add_u32 m0, s82, 131072\n\t" \
    "s_nop 0\n\t" \
    "global_load_lds_dwordx4 v200, s[46:47]\n\t" \
    "s_add_u32 s46, s42, 4325376\n\t" \
    "s_addc_u32 s47, s43, 0\n\t" \
    "s_add_u32 m0, s82, 135168\n\t" \
    "s_nop 0\n\t" \
    "global_load_lds_dwordx4 v200, s[46:47]\n\t" \
    "s_add_u32 s46, s42, 4456448\n\t" \
    "s_addc_u32 s47, s43, 0\n\t" \
    "s_add_u32 m0, s82, 139264\n\t" \
    "s_nop 0\n\t" \
    "global_load_lds_dwordx4 v200, s[46:47]\n\t" \
    "s_add_u32 s46, s42, 4587520\n\t" \
    "s_addc_u32 s47, s43, 0\n\t" \
    "s_add_u32 m0, s82, 143360\n\t" \
    "s_nop 0\n\t" \
    "global_load_lds_dwordx4 v200, s[46:47]\n\t" \
    "s_waitcnt vmcnt(32) lgkmcnt(0)\n\t" \
    "s_barrier\n\t" \
    "ds_read_b64_tr_b16 a[0:1], v201 offset:0\n\t" \
    "ds_read_b64_tr_b16 a[2:3], v201 offset:1024\n\t" \
    "ds_read_b64_tr_b16 a[4:5], v201 offset:8192\n\t" \
    "ds_read_b64_tr_b16 a[6:7], v201 offset:9216\n\t" \
    "ds_read_b64_tr_b16 a[72:73], v202 offset:0\n\t" \
    "ds_read_b64_tr_b16 a[74:75], v202 offset:1024\n\t" \
    "ds_read_b64_tr_b16 a[76:77], v202 offset:8192\n\t" \
    "ds_read_b64_tr_b16 a[78:79], v202 offset:9216\n\t" \
    "s_waitcnt vmcnt(28) lgkmcnt(0)\n\t" \
    "s_barrier\n\t" \
    "ds_read_b64_tr_b16 a[8:9], v201 offset:16384\n\t" \
    "ds_read_b64_tr_b16 a[10:11], v201 offset:17408\n\t" \
    "ds_read_b64_tr_b16 a[12:13], v201 offset:24576\n\t" \
    "ds_read_b64_tr_b16 a[14:15], v201 offset:25600\n\t" \
    "ds_read_b64_tr_b16 a[80:81], v202 offset:16384\n\t" \
    "ds_read_b64_tr_b16 a[82:83], v202 offset:17408\n\t" \
    "ds_read_b64_tr_b16 a[84:85], v202 offset:24576\n\t" \
    "ds_read_b64_tr_b16 a[86:87], v202 offset:25600\n\t" \
    "s_waitcnt vmcnt(24) lgkmcnt(0)\n\t" \
    "s_barrier\n\t" \
    "ds_read_b64_tr_b16 a[16:17], v201 offset:32768\n\t" \
    "ds_read_b64_tr_b16 a[18:19], v201 offset:33792\n\t" \
    "ds_read_b64_tr_b16 a[20:21], v201 offset:40960\n\t" \
    "ds_read_b64_tr_b16 a[22:23], v201 offset:41984\n\t" \
    "ds_read_b64_tr_b16 a[88:89], v202 offset:32768\n\t" \
    "ds_read_b64_tr_b16 a[90:91], v202 offset:33792\n\t" \
    "ds_read_b64_tr_b16 a[92:93], v202 offset:40960\n\t" \
    "ds_read_b64_tr_b16 a[94:95], v202 offset:41984\n\t" \
    "s_waitcnt vmcnt(20) lgkmcnt(0)\n\t" \
    "s_barrier\n\t" \
    "ds_read_b64_tr_b16 a[24:25], v201 offset:49152\n\t" \
    "ds_read_b64_tr_b16 a[26:27], v201 offset:50176\n\t" \
    "ds_read_b64_tr_b16 a[28:29], v201 offset:57344\n\t" \
    "ds_read_b64_tr_b16 a[30:31], v201 offset:58368\n\t" \
    "ds_read_b64_tr_b16 a[96:97], v202 offset:49152\n\t" \
    "ds_read_b64_tr_b16 a[98:99], v202 offset:50176\n\t" \
    "ds_read_b64_tr_b16 a[100:101], v202 offset:57344\n\t" \
    "ds_read_b64_tr_b16 a[102:103], v202 offset:58368\n\t" \
    "s_add_u32 s46, s44, 0\n\t" \
    "s_addc_u32 s47, s45, 0\n\t" \
    "s_mul_i32 s69, s82, 9\n\t" \
    "s_add_u32 m0, s69, 0\n\t" \
    "s_nop 0\n\t" \
    "global_load_lds_dwordx4 v203, s[46:47]\n\t" \
    "s_add_u32 m0, s69, 1024\n\t" \
    "s_nop 0\n\t" \
    "global_load_lds_dwordx4 v204, s[46:47]\n\t" \
    "s_add_u32 m0, s69, 2048\n\t" \
    "s_nop 0\n\t" \
    "global_load_lds_dwordx4 v205, s[46:47]\n\t" \
    "s_add_u32 m0, s69, 3072\n\t" \
    "s_nop 0\n\t" \
    "global_load_lds_dwordx4 v206, s[46:47]\n\t" \
    "s_add_u32 m0, s69, 4096\n\t" \
    "s_nop 0\n\t" \
    "global_load_lds_dwordx4 v207, s[46:47]\n\t" \
    "s_add_u32 m0, s69, 5120\n\t" \
    "s_nop 0\n\t" \
    "global_load_lds_dwordx4 v208, s[46:47]\n\t" \
    "s_add_u32 m0, s69, 6144\n\t" \
    "s_nop 0\n\t" \
    "global_load_lds_dwordx4 v209, s[46:47]\n\t" \
    "s_add_u32 m0, s69, 7168\n\t" \
    "s_nop 0\n\t" \
    "global_load_lds_dwordx4 v210, s[46:47]\n\t" \
    "s_add_u32 m0, s69, 8192\n\t" \
    "s_nop 0\n\t" \
    "global_load_lds_dwordx4 v211, s[46:47]\n\t" \
    "s_min_u32 s69, s65, 63\n\t" \
    "s_mul_i32 s69, s69, 36864\n\t" \
    "s_add_u32 s52, s50, s69\n\t" \
    "s_addc_u32 s53, s51, 0\n\t" \
    "global_load_dwordx4 v[120:123], v203, s[52:53]\n\t" \
    "global_load_dwordx4 v[124:127], v204, s[52:53]\n\t" \
    "global_load_dwordx4 v[128:131], v205, s[52:53]\n\t" \
    "global_load_dwordx4 v[132:135], v206, s[52:53]\n\t" \
    "global_load_dwordx4 v[136:139], v207, s[52:53]\n\t" \
    "global_load_dwordx4 v[140:143], v208, s[52:53]\n\t" \
    "global_load_dwordx4 v[144:147], v209, s[52:53]\n\t" \
    "global_load_dwordx4 v[148:151], v210, s[52:53]\n\t" \
    "global_load_dwordx4 v[152:155], v211, s[52:53]\n\t" \
    "s_waitcnt vmcnt(34) lgkmcnt(0)\n\t" \
    "s_barrier\n\t" \
    "v_add_u32 v201, 0x10000, v201\n\t" \
    "v_add_u32 v202, 0x10000, v202\n\t" \
    "ds_read_b64_tr_b16 a[32:33], v201 offset:0\n\t" \
    "ds_read_b64_tr_b16 a[34:35], v201 offset:1024\n\t" \
    "ds_read_b64_tr_b16 a[36:37], v201 offset:8192\n\t" \
    "ds_read_b64_tr_b16 a[38:39], v201 offset:9216\n\t" \
    "ds_read_b64_tr_b16 a[104:105], v202 offset:0\n\t" \
    "ds_read_b64_tr_b16 a[106:107], v202 offset:1024\n\t" \
    "ds_read_b64_tr_b16 a[108:109], v202 offset:8192\n\t" \
    "ds_read_b64_tr_b16 a[110:111], v202 offset:9216\n\t" \
    "s_waitcnt vmcnt(30) lgkmcnt(0)\n\t" \
    "s_barrier\n\t" \
    "ds_read_b64_tr_b16 a[40:41], v201 offset:16384\n\t" \
    "ds_read_b64_tr_b16 a[42:43], v201 offset:17408\n\t" \
    "ds_read_b64_tr_b16 a[44:45], v201 offset:24576\n\t" \
    "ds_read_b64_tr_b16 a[46:47], v201 offset:25600\n\t" \
    "ds_read_b64_tr_b16 a[112:113], v202 offset:16384\n\t" \
    "ds_read_b64_tr_b16 a[114:115], v202 offset:17408\n\t" \
    "ds_read_b64_tr_b16 a[116:117], v202 offset:24576\n\t" \
    "ds_read_b64_tr_b16 a[118:119], v202 offset:25600\n\t" \
    "s_add_u32 s46, s44, 36864\n\t" \
    "s_addc_u32 s47, s45, 0\n\t" \
    "s_mul_i32 s69, s82, 9\n\t" \
    "s_add_u32 m0, s69, 36864\n\t" \
    "s_nop 0\n\t" \
    "global_load_lds_dwordx4 v203, s[46:47]\n\t" \
    "s_add_u32 m0, s69, 37888\n\t" \
    "s_nop 0\n\t" \
    "global_load_lds_dwordx4 v204, s[46:47]\n\t" \
    "s_add_u32 m0, s69, 38912\n\t" \
    "s_nop 0\n\t" \
    "global_load_lds_dwordx4 v205, s[46:47]\n\t" \
    "s_add_u32 m0, s69, 39936\n\t" \
    "s_nop 0\n\t" \
    "global_load_lds_dwordx4 v206, s[46:47]\n\t" \
    "s_add_u32 m0, s69, 40960\n\t" \
    "s_nop 0\n\t" \
    "global_load_lds_dwordx4 v207, s[46:47]\n\t" \
    "s_add_u32 m0, s69, 41984\n\t" \
    "s_nop 0\n\t" \
    "global_load_lds_dwordx4 v208, s[46:47]\n\t" \
    "s_add_u32 m0, s69, 43008\n\t" \
    "s_nop 0\n\t" \
    "global_load_lds_dwordx4 v209, s[46:47]\n\t" \
    "s_add_u32 m0, s69, 44032\n\t" \
    "s_nop 0\n\t" \
    "global_load_lds_dwordx4 v210, s[46:47]\n\t" \
    "s_add_u32 m0, s69, 45056\n\t" \
    "s_nop 0\n\t" \
    "global_load_lds_dwordx4 v211, s[46:47]\n\t" \
    "s_waitcnt vmcnt(35) lgkmcnt(0)\n\t" \
    "s_barrier\n\t" \
    "ds_read_b64_tr_b16 a[48:49], v201 offset:32768\n\t" \
    "ds_read_b64_tr_b16 a[50:51], v201 offset:33792\n\t" \
    "ds_read_b64_tr_b16 a[52:53], v201 offset:40960\n\t" \
    "ds_read_b64_tr_b16 a[54:55], v201 offset:41984\n\t" \
    "ds_read_b64_tr_b16 a[120:121], v202 offset:32768\n\t" \
    "ds_read_b64_tr_b16 a[122:123], v202 offset:33792\n\t" \
    "ds_read_b64_tr_b16 a[124:125], v202 offset:40960\n\t" \
    "ds_read_b64_tr_b16 a[126:127], v202 offset:41984\n\t" \
    "s_waitcnt vmcnt(31) lgkmcnt(0)\n\t" \
    "s_barrier\n\t" \
    "ds_read_b64_tr_b16 a[56:57], v201 offset:49152\n\t" \
    "ds_read_b64_tr_b16 a[58:59], v201 offset:50176\n\t" \
    "ds_read_b64_tr_b16 a[60:61], v201 offset:57344\n\t" \
    "ds_read_b64_tr_b16 a[62:63], v201 offset:58368\n\t" \
    "ds_read_b64_tr_b16 a[128:129], v202 offset:49152\n\t" \
    "ds_read_b64_tr_b16 a[130:131], v202 offset:50176\n\t" \
    "ds_read_b64_tr_b16 a[132:133], v202 offset:57344\n\t" \
    "ds_read_b64_tr_b16 a[134:135], v202 offset:58368\n\t" \
    "s_add_u32 s46, s44, 73728\n\t" \
    "s_addc_u32 s47, s45, 0\n\t" \
    "s_mul_i32 s69, s82, 9\n\t" \
    "s_add_u32 m0, s69, 73728\n\t" \
    "s_nop 0\n\t" \
    "global_load_lds_dwordx4 v203, s[46:47]\n\t" \
    "s_add_u32 m0, s69, 74752\n\t" \
    "s_nop 0\n\t" \
    "global_load_lds_dwordx4 v204, s[46:47]\n\t" \
    "s_add_u32 m0, s69, 75776\n\t" \
    "s_nop 0\n\t" \
    "global_load_lds_dwordx4 v205, s[46:47]\n\t" \
    "s_add_u32 m0, s69, 76800\n\t" \
    "s_nop 0\n\t" \
    "global_load_lds_dwordx4 v206, s[46:47]\n\t" \
    "s_add_u32 m0, s69, 77824\n\t" \
    "s_nop 0\n\t" \
    "global_load_lds_dwordx4 v207, s[46:47]\n\t" \
    "s_add_u32 m0, s69, 78848\n\t" \
    "s_nop 0\n\t" \
    "global_load_lds_dwordx4 v208, s[46:47]\n\t" \
    "s_add_u32 m0, s69, 79872\n\t" \
    "s_nop 0\n\t" \
    "global_load_lds_dwordx4 v209, s[46:47]\n\t" \
    "s_add_u32 m0, s69, 80896\n\t" \
    "s_nop 0\n\t" \
    "global_load_lds_dwordx4 v210, s[46:47]\n\t" \
    "s_add_u32 m0, s69, 81920\n\t" \
    "s_nop 0\n\t" \
    "global_load_lds_dwordx4 v211, s[46:47]\n\t" \
    "v_mbcnt_lo_u32_b32 v164, -1, 0\n\t" \
    "v_mbcnt_hi_u32_b32 v164, -1, v164\n\t" \
    "v_lshlrev_b32 v164, 6, v164\n\t" \
    "v_add_u32 v164, 6225920, v164\n\t" \
    "v_mov_b32 v165, 6234112\n\t" \
    "global_load_dword v165, v165, s[36:37] sc1\n\t" \
    "global_load_dwordx2 v[166:167], v164, s[36:37] sc1\n\t" \
    "global_load_dword v168, v164, s[36:37] offset:2048 sc1\n\t" \
    "s_waitcnt vmcnt(39) lgkmcnt(0)\n\t" \
    "s_barrier\n\t" \
    "v_add_u32 v201, 0x10000, v201\n\t" \
    "v_add_u32 v202, 0x10000, v202\n\t" \
    "ds_read_b64_tr_b16 a[64:65], v201 offset:0\n\t" \
    "ds_read_b64_tr_b16 a[66:67], v201 offset:1024\n\t" \
    "ds_read_b64_tr_b16 a[68:69], v201 offset:8192\n\t" \
    "ds_read_b64_tr_b16 a[70:71], v201 offset:9216\n\t" \
    "ds_read_b64_tr_b16 a[136:137], v202 offset:0\n\t" \
    "ds_read_b64_tr_b16 a[138:139], v202 offset:1024\n\t" \
    "ds_read_b64_tr_b16 a[140:141], v202 offset:8192\n\t" \
    "ds_read_b64_tr_b16 a[142:143], v202 offset:9216\n\t" \
    "v_add_u32 v201, 0xfffe0000, v201\n\t" \
    "v_add_u32 v202, 0xfffe0000, v202\n\t" \
    "gv_jn2_%=:\n\t" \
    "s_waitcnt lgkmcnt(0)\n\t" \
    "s_barrier\n\t" \
    "s_add_u32 s46, s44, 110592\n\t" \
    "s_addc_u32 s47, s45, 0\n\t" \
    "s_mul_i32 s69, s82, 9\n\t" \
    "s_add_u32 m0, s69, 110592\n\t" \
    "s_nop 0\n\t" \
    "global_load_lds_dwordx4 v203, s[46:47]\n\t" \
    "s_add_u32 m0, s69, 111616\n\t" \
    "s_nop 0\n\t" \
    "global_load_lds_dwordx4 v204, s[46:47]\n\t" \
    "s_add_u32 m0, s69, 112640\n\t" \
    "s_nop 0\n\t" \
    "global_load_lds_dwordx4 v205, s[46:47]\n\t" \
    "s_add_u32 m0, s69, 113664\n\t" \
    "s_nop 0\n\t" \
    "global_load_lds_dwordx4 v206, s[46:47]\n\t" \
    "s_add_u32 m0, s69, 114688\n\t" \
    "s_nop 0\n\t" \
    "global_load_lds_dwordx4 v207, s[46:47]\n\t" \
    "s_add_u32 m0, s69, 115712\n\t" \
    "s_nop 0\n\t" \
    "global_load_lds_dwordx4 v208, s[46:47]\n\t" \
    "s_add_u32 m0, s69, 116736\n\t" \
    "s_nop 0\n\t" \
    "global_load_lds_dwordx4 v209, s[46:47]\n\t" \
    "s_add_u32 m0, s69, 117760\n\t" \
    "s_nop 0\n\t" \
    "global_load_lds_dwordx4 v210, s[46:47]\n\t" \
    "s_add_u32 m0, s69, 118784\n\t" \
    "s_nop 0\n\t" \
    "global_load_lds_dwordx4 v211, s[46:47]\n\t" \
    "s_waitcnt vmcnt(39) lgkmcnt(0)\n\t" \
    "s_barrier\n\t" \
    "ds_read_b64_tr_b16 a[144:145], v212 offset:0\n\t" \
    "ds_read_b64_tr_b16 a[146:147], v212 offset:18432\n\t" \
    "ds_read_b64_tr_b16 a[160:161], v213 offset:0\n\t" \
    "ds_read_b64_tr_b16 a[162:163], v213 offset:18432\n\t" \
    "ds_read_b64_tr_b16 a[176:177], v214 offset:0\n\t" \
    "ds_read_b64_tr_b16 a[178:179], v214 offset:18432\n\t" \
    "s_waitcnt lgkmcnt(6)\n\t" \
    "ds_read_b64_tr_b16 a[192:193], v215 offset:0\n\t" \
    "ds_read_b64_tr_b16 a[194:195], v215 offset:18432\n\t" \
    "ds_read_b64_tr_b16 a[208:209], v212 offset:128\n\t" \
    "ds_read_b64_tr_b16 a[210:211], v212 offset:18560\n\t" \
    "ds_read_b64_tr_b16 a[224:225], v213 offset:128\n\t" \
    "ds_read_b64_tr_b16 a[226:227], v213 offset:18560\n\t" \
    "s_waitcnt lgkmcnt(6)\n\t" \
    "ds_read_b64_tr_b16 a[240:241], v214 offset:128\n\t" \
    "ds_read_b64_tr_b16 a[242:243], v214 offset:18560\n\t" \
    "ds_read_b64_tr_b16 v[224:225], v215 offset:128\n\t" \
    "ds_read_b64_tr_b16 v[226:227], v215 offset:18560\n\t" \
    "ds_read_b64_tr_b16 v[240:241], v212 offset:256\n\t" \
    "ds_read_b64_tr_b16 v[242:243], v212 offset:18688\n\t" \
    "s_waitcnt lgkmcnt(6)\n\t" \
    "v_add_u32 v212, 0x9000, v212\n\t" \
    "v_add_u32 v213, 0x9000, v213\n\t" \
    "v_add_u32 v214, 0x9000, v214\n\t" \
    "v_add_u32 v215, 0x9000, v215\n\t" \
    "s_waitcnt vmcnt(21) lgkmcnt(0)\n\t" \
    "s_barrier\n\t" \
    "ds_read_b64_tr_b16 a[148:149], v212 offset:0\n\t" \
    "ds_read_b64_tr_b16 a[150:151], v212 offset:18432\n\t" \
    "ds_read_b64_tr_b16 a[164:165], v213 offset:0\n\t" \
    "ds_read_b64_tr_b16 a[166:167], v213 offset:18432\n\t" \
    "ds_read_b64_tr_b16 a[180:181], v214 offset:0\n\t" \
    "ds_read_b64_tr_b16 a[182:183], v214 offset:18432\n\t" \
    "s_waitcnt lgkmcnt(6)\n\t" \
    "ds_read_b64_tr_b16 a[196:197], v215 offset:0\n\t" \
    "ds_read_b64_tr_b16 a[198:199], v215 offset:18432\n\t" \
    "ds_read_b64_tr_b16 a[212:213], v212 offset:128\n\t" \
    "ds_read_b64_tr_b16 a[214:215], v212 offset:18560\n\t" \
    "ds_read_b64_tr_b16 a[228:229], v213 offset:128\n\t" \
    "ds_read_b64_tr_b16 a[230:231], v213 offset:18560\n\t" \
    "s_waitcnt lgkmcnt(6)\n\t" \
    "ds_read_b64_tr_b16 a[244:245], v214 offset:128\n\t" \
    "ds_read_b64_tr_b16 a[246:247], v214 offset:18560\n\t" \
    "ds_read_b64_tr_b16 v[228:229], v215 offset:128\n\t" \
    "ds_read_b64_tr_b16 v[230:231], v215 offset:18560\n\t" \
    "ds_read_b64_tr_b16 v[244:245], v212 offset:256\n\t" \
    "ds_read_b64_tr_b16 v[246:247], v212 offset:18688\n\t" \
    "s_waitcnt lgkmcnt(6)\n\t" \
    "v_add_u32 v212, 0x9000, v212\n\t" \
    "v_add_u32 v213, 0x9000, v213\n\t" \
    "v_add_u32 v214, 0x9000, v214\n\t" \
    "v_add_u32 v215, 0x9000, v215\n\t" \
    "ds_write_b128 v3, v[120:123] offset:0\n\t" \
    "ds_write_b128 v3, v[124:127] offset:1024\n\t" \
    "ds_write_b128 v3, v[128:131] offset:2048\n\t" \
    "ds_write_b128 v3, v[132:135] offset:3072\n\t" \
    "ds_write_b128 v3, v[136:139] offset:4096\n\t" \
    "ds_write_b128 v3, v[140:143] offset:5120\n\t" \
    "ds_write_b128 v3, v[144:147] offset:6144\n\t" \
    "ds_write_b128 v3, v[148:151] offset:7168\n\t" \
    "ds_write_b128 v3, v[152:155] offset:8192\n\t" \
    "s_waitcnt vmcnt(12) lgkmcnt(0)\n\t" \
    "s_barrier\n\t" \
    "ds_read_b64_tr_b16 a[152:153], v212 offset:0\n\t" \
    "ds_read_b64_tr_b16 a[154:155], v212 offset:18432\n\t" \
    "ds_read_b64_tr_b16 a[168:169], v213 offset:0\n\t" \
    "ds_read_b64_tr_b16 a[170:171], v213 offset:18432\n\t" \
    "ds_read_b64_tr_b16 a[184:185], v214 offset:0\n\t" \
    "ds_read_b64_tr_b16 a[186:187], v214 offset:18432\n\t" \
    "s_waitcnt lgkmcnt(6)\n\t" \
    "ds_read_b64_tr_b16 a[200:201], v215 offset:0\n\t" \
    "ds_read_b64_tr_b16 a[202:203], v215 offset:18432\n\t" \
    "ds_read_b64_tr_b16 a[216:217], v212 offset:128\n\t" \
    "ds_read_b64_tr_b16 a[218:219], v212 offset:18560\n\t" \
    "ds_read_b64_tr_b16 a[232:233], v213 offset:128\n\t" \
    "ds_read_b64_tr_b16 a[234:235], v213 offset:18560\n\t" \
    "s_waitcnt lgkmcnt(6)\n\t" \
    "ds_read_b64_tr_b16 a[248:249], v214 offset:128\n\t" \
    "ds_read_b64_tr_b16 a[250:251], v214 offset:18560\n\t" \
    "ds_read_b64_tr_b16 v[232:233], v215 offset:128\n\t" \
    "ds_read_b64_tr_b16 v[234:235], v215 offset:18560\n\t" \
    "ds_read_b64_tr_b16 v[248:249], v212 offset:256\n\t" \
    "ds_read_b64_tr_b16 v[250:251], v212 offset:18688\n\t" \
    "s_waitcnt lgkmcnt(6)\n\t" \
    "v_add_u32 v212, 0x9000, v212\n\t" \
    "v_add_u32 v213, 0x9000, v213\n\t" \
    "v_add_u32 v214, 0x9000, v214\n\t" \
    "v_add_u32 v215, 0x9000, v215\n\t" \
    "s_min_u32 s69, s84, 63\n\t" \
    "s_mul_i32 s69, s69, 36864\n\t" \
    "s_add_u32 s52, s50, s69\n\t" \
    "s_addc_u32 s53, s51, 0\n\t" \
    "s_add_u32 m0, s89, 36864\n\t" \
    "s_nop 0\n\t" \
    "global_load_lds_dwordx4 v203, s[52:53]\n\t" \
    "s_add_u32 m0, s89, 37888\n\t" \
    "s_nop 0\n\t" \
    "global_load_lds_dwordx4 v204, s[52:53]\n\t" \
    "s_add_u32 m0, s89, 38912\n\t" \
    "s_nop 0\n\t" \
    "global_load_lds_dwordx4 v205, s[52:53]\n\t" \
    "s_add_u32 m0, s89, 39936\n\t" \
    "s_nop 0\n\t" \
    "global_load_lds_dwordx4 v206, s[52:53]\n\t" \
    "s_add_u32 m0, s89, 40960\n\t" \
    "s_nop 0\n\t" \
    "global_load_lds_dwordx4 v207, s[52:53]\n\t" \
    "s_add_u32 m0, s89, 41984\n\t" \
    "s_nop 0\n\t" \
    "global_load_lds_dwordx4 v208, s[52:53]\n\t" \
    "s_add_u32 m0, s89, 43008\n\t" \
    "s_nop 0\n\t" \
    "global_load_lds_dwordx4 v209, s[52:53]\n\t" \
    "s_add_u32 m0, s89, 44032\n\t" \
    "s_nop 0\n\t" \
    "global_load_lds_dwordx4 v210, s[52:53]\n\t" \
    "s_add_u32 m0, s89, 45056\n\t" \
    "s_nop 0\n\t" \
    "global_load_lds_dwordx4 v211, s[52:53]\n\t" \
    "s_waitcnt vmcnt(18)\n\t" \
    "gv_C3_%=:\n\t" \
    "v_cmp_ne_u32 s[54:55], v166, v168\n\t" \
    "v_sub_u32 v166, v166, v165\n\t" \
    "v_cmp_ne_u32 vcc, 1, v166\n\t" \
    "s_nop 1\n\t" \
    "s_or_b64 vcc, vcc, s[54:55]\n\t" \
    "s_cbranch_scc0 gv_Cok4_%=\n\t" \
    "s_cmp_eq_u32 s68, 0\n\t" \
    "s_cbranch_scc1 gv_F6_%=\n\t" \
    "s_sub_u32 s68, s68, 1\n\t" \
    "s_sleep 6\n\t" \
    "s_branch gv_Cag5_%=\n\t" \
    "gv_F6_%=:\n\t" \
    "v_mov_b32 v168, 5767424\n\t" \
    "v_mov_b32 v169, 4\n\t" \
    "global_store_dword v168, v169, s[36:37] sc1\n\t" \
    "s_waitcnt vmcnt(0)\n\t" \
    "s_branch gv_Cok4_%=\n\t" \
    "gv_Cag5_%=:\n\t" \
    "global_load_dwordx2 v[166:167], v164, s[36:37] sc1\n\t" \
    "global_load_dword v168, v164, s[36:37] offset:2048 sc1\n\t" \
    "s_waitcnt vmcnt(0)\n\t" \
    "s_branch gv_C3_%=\n\t" \
    "gv_Cok4_%=:\n\t" \
    "v_readlane_b32 s69, v167, 2\n\t" \
    "v_readlane_b32 s70, v167, 3\n\t" \
    "s_nop 1\n\t" \
    "s_cmp_lt_u32 s70, s69\n\t" \
    "s_cselect_b32 s69, 0, 8192\n\t" \
    "s_add_u32 s100, s36, s69\n\t" \
    "s_addc_u32 s101, s37, 0\n\t" \
    "s_cmp_lg_u32 s66, 0\n\t" \
    "s_cbranch_scc1 gv_Ca7_%=\n\t" \
    "v_mov_b32 v164, 5768192\n\t" \
    "s_mov_b64 exec, 1\n\t" \
    "global_atomic_add v164, v166, s[36:37] sc1\n\t" \
    "s_mov_b64 exec, -1\n\t" \
    "gv_Ca7_%=:\n\t" \
    "v_mov_b32_dpp v164, v167 quad_perm:[1,0,3,2] row_mask:0xf bank_mask:0xf\n\t" \
    "v_cmp_lt_u32 s[44:45], v164, v167\n\t" \
    "s_nop 1\n\t" \
    "s_waitcnt lgkmcnt(0)\n\t" \
    "s_barrier\n\t" \
    "ds_read_b128 v[24:27], v1 offset:0\n\t" \
    "ds_read_b128 v[28:31], v1 offset:18432\n\t" \
    "ds_read_b128 v[32:35], v2 offset:0\n\t" \
    "ds_read_b128 v[36:39], v2 offset:18432\n\t" \
    "ds_read_b128 v[40:43], v1 offset:128\n\t" \
    "ds_read_b128 v[44:47], v1 offset:18560\n\t" \
    "s_mov_b64 vcc, 0\n\t" \
    ".p2align 6\n\t" \
    "s_nop 0\n\t" \
    "s_nop 0\n\t" \
    "gv_loop8_%=:\n\t" \
    "s_min_u32 s69, s85, 63\n\t" \
    "s_mul_i32 s69, s69, 36864\n\t" \
    "s_add_u32 s52, s50, s69\n\t" \
    "s_addc_u32 s53, s51, 0\n\t" \
    "s_add_u32 s90, s90, 36864\n\t" \
    "s_cmp_eq_u32 s90, 110592\n\t" \
    "s_cselect_b32 s90, 0, s90\n\t" \
    "s_add_u32 s91, s90, 36864\n\t" \
    "s_cmp_eq_u32 s91, 110592\n\t" \
    "s_cselect_b32 s91, 0, s91\n\t" \
    "s_add_u32 s91, s91, s89\n\t" \
    "v_add_u32 v22, s90, v0\n\t" \
    "v_xor_b32 v23, 64, v22\n\t" \
    "s_lshl_b32 s40, s65, 15\n\t" \
    "s_cselect_b32 s70, s65, 64\n\t" \
    "s_mul_i32 s69, s70, 171\n\t" \
    "s_lshr_b32 s69, s69, 9\n\t" \
    "s_mul_i32 s70, s70, 589824\n\t" \
    "s_mul_i32 s71, s69, 327680\n\t" \
    "s_add_u32 s70, s70, s71\n\t" \
    "s_lshl1_add_u32 s69, s69, 4\n\t" \
    "s_bitcmp1_b64 s[44:45], s69\n\t" \
    "s_cselect_b32 s69, 8192, 0\n\t" \
    "s_add_u32 s69, s69, s70\n\t" \
    "s_add_u32 s69, s69, 123731968\n\t" \
    "s_add_u32 s48, s36, s69\n\t" \
    "s_addc_u32 s49, s37, 0\n\t" \
    "s_waitcnt lgkmcnt(5)\n\t" \
    "v_mfma_f32_16x16x32_bf16 v[72:75], a[0:3], v[24:27], 0\n\t" \
    "ds_read_b128 v[48:51], v2 offset:128\n\t" \
    "s_waitcnt lgkmcnt(5)\n\t" \
    "v_mfma_f32_16x16x32_bf16 v[76:79], a[0:3], v[28:31], 0\n\t" \
    "ds_read_b128 v[52:55], v2 offset:18560\n\t" \
    "v_mfma_f32_16x16x32_bf16 v[80:83], a[72:75], v[24:27], 0\n\t" \
    "s_add_u32 m0, s91, 0\n\t" \
    "v_mfma_f32_16x16x32_bf16 v[84:87], a[72:75], v[28:31], 0\n\t" \
    "global_load_lds_dwordx4 v203, s[52:53]\n\t" \
    "s_waitcnt lgkmcnt(5)\n\t" \
    "v_mfma_f32_16x16x32_bf16 v[72:75], a[4:7], v[32:35], v[72:75]\n\t" \
    "ds_read_b128 v[56:59], v1 offset:256\n\t" \
    "s_waitcnt lgkmcnt(5)\n\t" \
    "v_mfma_f32_16x16x32_bf16 v[76:79], a[4:7], v[36:39], v[76:79]\n\t" \
    "ds_read_b128 v[60:63], v1 offset:18688\n\t" \
    "v_mfma_f32_16x16x32_bf16 v[80:83], a[76:79], v[32:35], v[80:83]\n\t" \
    "s_add_u32 m0, s91, 1024\n\t" \
    "v_mfma_f32_16x16x32_bf16 v[84:87], a[76:79], v[36:39], v[84:87]\n\t" \
    "global_load_lds_dwordx4 v204, s[52:53]\n\t" \
    "s_waitcnt lgkmcnt(5)\n\t" \
    "v_mfma_f32_16x16x32_bf16 v[72:75], a[8:11], v[40:43], v[72:75]\n\t" \
    "ds_read_b128 v[64:67], v2 offset:256\n\t" \
    "s_waitcnt lgkmcnt(5)\n\t" \
    "v_mfma_f32_16x16x32_bf16 v[76:79], a[8:11], v[44:47], v[76:79]\n\t" \
    "ds_read_b128 v[68:71], v2 offset:18688\n\t" \
    "v_mfma_f32_16x16x32_bf16 v[80:83], a[80:83], v[40:43], v[80:83]\n\t" \
    "s_add_u32 m0, s91, 2048\n\t" \
    "v_mfma_f32_16x16x32_bf16 v[84:87], a[80:83], v[44:47], v[84:87]\n\t" \
    "global_load_lds_dwordx4 v205, s[52:53]\n\t" \
    "s_waitcnt lgkmcnt(5)\n\t" \
    "v_mfma_f32_16x16x32_bf16 v[72:75], a[12:15], v[48:51], v[72:75]\n\t" \
    "ds_read_b128 v[24:27], v1 offset:384\n\t" \
    "s_waitcnt lgkmcnt(5)\n\t" \
    "v_mfma_f32_16x16x32_bf16 v[76:79], a[12:15], v[52:55], v[76:79]\n\t" \
    "ds_read_b128 v[28:31], v1 offset:18816\n\t" \
    "v_mfma_f32_16x16x32_bf16 v[80:83], a[84:87], v[48:51], v[80:83]\n\t" \
    "s_add_u32 m0, s91, 3072\n\t" \
    "v_mfma_f32_16x16x32_bf16 v[84:87], a[84:87], v[52:55], v[84:87]\n\t" \
    "global_load_lds_dwordx4 v206, s[52:53]\n\t" \
    "s_waitcnt lgkmcnt(5)\n\t" \
    "v_mfma_f32_16x16x32_bf16 v[72:75], a[16:19], v[56:59], v[72:75]\n\t" \
    "ds_read_b128 v[32:35], v2 offset:384\n\t" \
    "s_waitcnt lgkmcnt(5)\n\t" \
    "v_mfma_f32_16x16x32_bf16 v[76:79], a[16:19], v[60:63], v[76:79]\n\t" \
    "ds_read_b128 v[36:39], v2 offset:18816\n\t" \
    "v_mfma_f32_16x16x32_bf16 v[80:83], a[88:91], v[56:59], v[80:83]\n\t" \
    "s_add_u32 m0, s91, 4096\n\t" \
    "v_mfma_f32_16x16x32_bf16 v[84:87], a[88:91], v[60:63], v[84:87]\n\t" \
    "global_load_lds_dwordx4 v207, s[52:53]\n\t" \
    "s_waitcnt lgkmcnt(5)\n\t" \
    "v_mfma_f32_16x16x32_bf16 v[72:75], a[20:23], v[64:67], v[72:75]\n\t" \
    "ds_read_b128 v[40:43], v1 offset:512\n\t" \
    "s_waitcnt lgkmcnt(5)\n\t" \
    "v_mfma_f32_16x16x32_bf16 v[76:79], a[20:23], v[68:71], v[76:79]\n\t" \
    "ds_read_b128 v[44:47], v1 offset:18944\n\t" \
    "v_mfma_f32_16x16x32_bf16 v[80:83], a[92:95], v[64:67], v[80:83]\n\t" \
    "s_add_u32 m0, s91, 5120\n\t" \
    "v_mfma_f32_16x16x32_bf16 v[84:87], a[92:95], v[68:71], v[84:87]\n\t" \
    "global_load_lds_dwordx4 v208, s[52:53]\n\t" \
    "s_waitcnt lgkmcnt(5)\n\t" \
    "v_mfma_f32_16x16x32_bf16 v[72:75], a[24:27], v[24:27], v[72:75]\n\t" \
    "ds_read_b128 v[48:51], v2 offset:512\n\t" \
    "s_waitcnt lgkmcnt(5)\n\t" \
    "v_mfma_f32_16x16x32_bf16 v[76:79], a[24:27], v[28:31], v[76:79]\n\t" \
    "ds_read_b128 v[52:55], v2 offset:18944\n\t" \
    "v_mfma_f32_16x16x32_bf16 v[80:83], a[96:99], v[24:27], v[80:83]\n\t" \
    "s_add_u32 m0, s91, 6144\n\t" \
    "v_mfma_f32_16x16x32_bf16 v[84:87], a[96:99], v[28:31], v[84:87]\n\t" \
    "global_load_lds_dwordx4 v209, s[52:53]\n\t" \
    "s_waitcnt lgkmcnt(5)\n\t" \
    "v_mfma_f32_16x16x32_bf16 v[72:75], a[28:31], v[32:35], v[72:75]\n\t" \
    "ds_read_b128 v[56:59], v1 offset:640\n\t" \
    "s_waitcnt lgkmcnt(5)\n\t" \
    "v_mfma_f32_16x16x32_bf16 v[76:79], a[28:31], v[36:39], v[76:79]\n\t" \
    "ds_read_b128 v[60:63], v1 offset:19072\n\t" \
    "v_mfma_f32_16x16x32_bf16 v[80:83], a[100:103], v[32:35], v[80:83]\n\t" \
    "s_add_u32 m0, s91, 7168\n\t" \
    "v_mfma_f32_16x16x32_bf16 v[84:87], a[100:103], v[36:39], v[84:87]\n\t" \
    "global_load_lds_dwordx4 v210, s[52:53]\n\t" \
    "s_waitcnt lgkmcnt(5)\n\t" \
    "v_mfma_f32_16x16x32_bf16 v[72:75], a[32:35], v[40:43], v[72:75]\n\t" \
    "ds_read_b128 v[64:67], v2 offset:640\n\t" \
    "s_waitcnt lgkmcnt(5)\n\t" \
    "v_mfma_f32_16x16x32_bf16 v[76:79], a[32:35], v[44:47], v[76:79]\n\t" \
    "ds_read_b128 v[68:71], v2 offset:19072\n\t" \
    "v_mfma_f32_16x16x32_bf16 v[80:83], a[104:107], v[40:43], v[80:83]\n\t" \
    "s_add_u32 m0, s91, 8192\n\t" \
    "v_mfma_f32_16x16x32_bf16 v[84:87], a[104:107], v[44:47], v[84:87]\n\t" \
    "global_load_lds_dwordx4 v211, s[52:53]\n\t" \
    "s_waitcnt lgkmcnt(5)\n\t" \
    "v_mfma_f32_16x16x32_bf16 v[72:75], a[36:39], v[48:51], v[72:75]\n\t" \
    "ds_read_b128 v[24:27], v1 offset:768\n\t" \
    "s_waitcnt lgkmcnt(5)\n\t" \
    "v_mfma_f32_16x16x32_bf16 v[76:79], a[36:39], v[52:55], v[76:79]\n\t" \
    "ds_read_b128 v[28:31], v1 offset:19200\n\t" \
    "v_mfma_f32_16x16x32_bf16 v[80:83], a[108:111], v[48:51], v[80:83]\n\t" \
    "s_cbranch_vccnz gv_gol12_%=\n\t" \
    "gv_gob13_%=:\n\t" \
    "v_mfma_f32_16x16x32_bf16 v[84:87], a[108:111], v[52:55], v[84:87]\n\t" \
    "s_waitcnt lgkmcnt(5)\n\t" \
    "v_mfma_f32_16x16x32_bf16 v[72:75], a[40:43], v[56:59], v[72:75]\n\t" \
    "ds_read_b128 v[32:35], v2 offset:768\n\t" \
    "s_waitcnt lgkmcnt(5)\n\t" \
    "v_mfma_f32_16x16x32_bf16 v[76:79], a[40:43], v[60:63], v[76:79]\n\t" \
    "ds_read_b128 v[36:39], v2 offset:19200\n\t" \
    "v_mfma_f32_16x16x32_bf16 v[80:83], a[112:115], v[56:59], v[80:83]\n\t" \
    "v_mfma_f32_16x16x32_bf16 v[84:87], a[112:115], v[60:63], v[84:87]\n\t" \
    "global_store_dwordx4 v19, v[196:199], s[48:49] sc1\n\t" \
    "s_waitcnt lgkmcnt(5)\n\t" \
    "v_mfma_f32_16x16x32_bf16 v[72:75], a[44:47], v[64:67], v[72:75]\n\t" \
    "ds_read_b128 v[40:43], v1 offset:896\n\t" \
    "s_waitcnt lgkmcnt(5)\n\t" \
    "v_mfma_f32_16x16x32_bf16 v[76:79], a[44:47], v[68:71], v[76:79]\n\t" \
    "ds_read_b128 v[44:47], v1 offset:19328\n\t" \
    "v_mfma_f32_16x16x32_bf16 v[80:83], a[116:119], v[64:67], v[80:83]\n\t" \
    "v_mfma_f32_16x16x32_bf16 v[84:87], a[116:119], v[68:71], v[84:87]\n\t" \
    "s_waitcnt lgkmcnt(5)\n\t" \
    "v_mfma_f32_16x16x32_bf16 v[72:75], a[48:51], v[24:27], v[72:75]\n\t" \
    "ds_read_b128 v[48:51], v2 offset:896\n\t" \
    "s_waitcnt lgkmcnt(5)\n\t" \
    "v_mfma_f32_16x16x32_bf16 v[76:79], a[48:51], v[28:31], v[76:79]\n\t" \
    "ds_read_b128 v[52:55], v2 offset:19328\n\t" \
    "v_mfma_f32_16x16x32_bf16 v[80:83], a[120:123], v[24:27], v[80:83]\n\t" \
    "v_mfma_f32_16x16x32_bf16 v[84:87], a[120:123], v[28:31], v[84:87]\n\t" \
    "s_waitcnt lgkmcnt(5)\n\t" \
    "v_mfma_f32_16x16x32_bf16 v[72:75], a[52:55], v[32:35], v[72:75]\n\t" \
    "ds_read_b128 v[56:59], v1 offset:1024\n\t" \
    "s_waitcnt lgkmcnt(5)\n\t" \
    "v_mfma_f32_16x16x32_bf16 v[76:79], a[52:55], v[36:39], v[76:79]\n\t" \
    "ds_read_b128 v[60:63], v1 offset:19456\n\t" \
    "v_mfma_f32_16x16x32_bf16 v[80:83], a[124:127], v[32:35], v[80:83]\n\t" \
    "v_mfma_f32_16x16x32_bf16 v[84:87], a[124:127], v[36:39], v[84:87]\n\t" \
    "global_store_dwordx4 v20, v[196:199], s[48:49] sc1\n\t" \
    "s_waitcnt lgkmcnt(5)\n\t" \
    "v_mfma_f32_16x16x32_bf16 v[72:75], a[56:59], v[40:43], v[72:75]\n\t" \
    "ds_read_b128 v[64:67], v2 offset:1024\n\t" \
    "s_waitcnt lgkmcnt(5)\n\t" \
    "v_mfma_f32_16x16x32_bf16 v[76:79], a[56:59], v[44:47], v[76:79]\n\t" \
    "ds_read_b128 v[68:71], v2 offset:19456\n\t" \
    "v_mfma_f32_16x16x32_bf16 v[80:83], a[128:131], v[40:43], v[80:83]\n\t" \
    "v_mfma_f32_16x16x32_bf16 v[84:87], a[128:131], v[44:47], v[84:87]\n\t" \
    "s_waitcnt lgkmcnt(5)\n\t" \
    "v_mfma_f32_16x16x32_bf16 v[72:75], a[60:63], v[48:51], v[72:75]\n\t" \
    "s_waitcnt lgkmcnt(4)\n\t" \
    "v_mfma_f32_16x16x32_bf16 v[76:79], a[60:63], v[52:55], v[76:79]\n\t" \
    "v_mfma_f32_16x16x32_bf16 v[80:83], a[132:135], v[48:51], v[80:83]\n\t" \
    "v_mfma_f32_16x16x32_bf16 v[84:87], a[132:135], v[52:55], v[84:87]\n\t" \
    "s_waitcnt lgkmcnt(3)\n\t" \
    "v_mfma_f32_16x16x32_bf16 v[72:75], a[64:67], v[56:59], v[72:75]\n\t" \
    "s_waitcnt lgkmcnt(2)\n\t" \
    "v_mfma_f32_16x16x32_bf16 v[76:79], a[64:67], v[60:63], v[76:79]\n\t" \
    "v_mfma_f32_16x16x32_bf16 v[80:83], a[136:139], v[56:59], v[80:83]\n\t" \
    "v_mfma_f32_16x16x32_bf16 v[84:87], a[136:139], v[60:63], v[84:87]\n\t" \
    "global_store_dword v21, v196, s[48:49] sc1\n\t" \
    "s_waitcnt lgkmcnt(1)\n\t" \
    "v_mfma_f32_16x16x32_bf16 v[72:75], a[68:71], v[64:67], v[72:75]\n\t" \
    "s_waitcnt lgkmcnt(0)\n\t" \
    "v_mfma_f32_16x16x32_bf16 v[76:79], a[68:71], v[68:71], v[76:79]\n\t" \
    "v_mfma_f32_16x16x32_bf16 v[80:83], a[140:143], v[64:67], v[80:83]\n\t" \
    "v_mfma_f32_16x16x32_bf16 v[84:87], a[140:143], v[68:71], v[84:87]\n\t" \
    "v_mov_b32 v1, v22\n\t" \
    "v_mov_b32 v2, v23\n\t" \
    "s_nop 1\n\t" \
    "v_cvt_pk_bf16_f32 v104, v72, v73\n\t" \
    "v_cvt_pk_bf16_f32 v105, v74, v75\n\t" \
    "s_nop 0\n\t" \
    "v_cvt_pk_bf16_f32 v106, v80, v81\n\t" \
    "v_cvt_pk_bf16_f32 v107, v82, v83\n\t" \
    "ds_write_b128 v4, v[104:107] offset:0\n\t" \
    "v_cvt_pk_bf16_f32 v108, v76, v77\n\t" \
    "v_cvt_pk_bf16_f32 v109, v78, v79\n\t" \
    "v_cvt_pk_bf16_f32 v110, v84, v85\n\t" \
    "v_cvt_pk_bf16_f32 v111, v86, v87\n\t" \
    "ds_write_b128 v4, v[108:111] offset:1024\n\t" \
    "s_cmp_lt_i32 s86, 16\n\t" \
    "s_cbranch_scc1 gv_rest10_%=\n\t" \
    "gv_back11_%=:\n\t" \
    "s_waitcnt vmcnt(21) lgkmcnt(0)\n\t" \
    "s_barrier\n\t" \
    "ds_read_b128 v[164:167], v5 offset:0\n\t" \
    "ds_read_b128 v[168:171], v5 offset:1024\n\t" \
    "ds_read_b128 v[172:175], v5 offset:2048\n\t" \
    "ds_read_b128 v[176:179], v5 offset:3072\n\t" \
    "ds_read_b128 v[180:183], v5 offset:4096\n\t" \
    "ds_read_b128 v[184:187], v5 offset:5120\n\t" \
    "ds_read_b128 v[188:191], v5 offset:6144\n\t" \
    "ds_read_b128 v[192:195], v5 offset:7168\n\t" \
    "s_cmp_lt_i32 s86, 16\n\t" \
    "s_cselect_b32 s71, s86, s87\n\t" \
    "s_cmp_lg_u32 s66, 0\n\t" \
    "s_cbranch_scc1 gv_SF17_%=\n\t" \
    "s_lshl_b32 s69, s67, 7\n\t" \
    "s_add_u32 s69, s69, 3162112\n\t" \
    "s_add_u32 s70, s71, 0\n\t" \
    "v_mov_b32 v156, s69\n\t" \
    "v_mov_b32 v157, s70\n\t" \
    "s_mov_b64 exec, 1\n\t" \
    "global_store_dword v156, v157, s[100:101] sc1\n\t" \
    "s_mov_b64 exec, -1\n\t" \
    "gv_SF17_%=:\n\t" \
    "v_xor_b32 v4, 0x2000, v4\n\t" \
    "v_xor_b32 v5, 0x2000, v5\n\t" \
    "s_waitcnt lgkmcnt(7)\n\t" \
    "v_mfma_f32_16x16x32_bf16 v[72:75], v[164:167], a[144:147], 0\n\t" \
    "s_waitcnt lgkmcnt(6)\n\t" \
    "v_mfma_f32_16x16x32_bf16 v[76:79], v[168:171], a[144:147], 0\n\t" \
    "s_waitcnt lgkmcnt(5)\n\t" \
    "v_mfma_f32_16x16x32_bf16 v[72:75], v[172:175], a[148:151], v[72:75]\n\t" \
    "s_waitcnt lgkmcnt(4)\n\t" \
    "v_mfma_f32_16x16x32_bf16 v[76:79], v[176:179], a[148:151], v[76:79]\n\t" \
    "s_waitcnt lgkmcnt(3)\n\t" \
    "v_mfma_f32_16x16x32_bf16 v[72:75], v[180:183], a[152:155], v[72:75]\n\t" \
    "s_waitcnt lgkmcnt(2)\n\t" \
    "v_mfma_f32_16x16x32_bf16 v[76:79], v[184:187], a[152:155], v[76:79]\n\t" \
    "s_waitcnt lgkmcnt(1)\n\t" \
    "v_mfma_f32_16x16x32_bf16 v[72:75], v[188:191], a[156:159], v[72:75]\n\t" \
    "s_waitcnt lgkmcnt(0)\n\t" \
    "v_mfma_f32_16x16x32_bf16 v[76:79], v[192:195], a[156:159], v[76:79]\n\t" \
    "v_mfma_f32_16x16x32_bf16 v[80:83], v[164:167], a[160:163], 0\n\t" \
    "v_mfma_f32_16x16x32_bf16 v[84:87], v[168:171], a[160:163], 0\n\t" \
    "v_mfma_f32_16x16x32_bf16 v[80:83], v[172:175], a[164:167], v[80:83]\n\t" \
    "v_mfma_f32_16x16x32_bf16 v[84:87], v[176:179], a[164:167], v[84:87]\n\t" \
    "v_mfma_f32_16x16x32_bf16 v[80:83], v[180:183], a[168:171], v[80:83]\n\t" \
    "v_mfma_f32_16x16x32_bf16 v[84:87], v[184:187], a[168:171], v[84:87]\n\t" \
    "v_cvt_pk_bf16_f32 v104, v72, v73\n\t" \
    "v_mfma_f32_16x16x32_bf16 v[80:83], v[188:191], a[172:175], v[80:83]\n\t" \
    "v_cvt_pk_bf16_f32 v105, v74, v75\n\t" \
    "v_mfma_f32_16x16x32_bf16 v[84:87], v[192:195], a[172:175], v[84:87]\n\t" \
    "v_cvt_pk_bf16_f32 v106, v76, v77\n\t" \
    "v_mfma_f32_16x16x32_bf16 v[88:91], v[164:167], a[176:179], 0\n\t" \
    "v_cvt_pk_bf16_f32 v107, v78, v79\n\t" \
    "v_mfma_f32_16x16x32_bf16 v[92:95], v[168:171], a[176:179], 0\n\t" \
    "v_xor_b32 v18, s40, v8\n\t" \
    "v_mfma_f32_16x16x32_bf16 v[88:91], v[172:175], a[180:183], v[88:91]\n\t" \
    "global_store_dwordx4 v18, v[104:107], s[38:39] sc1\n\t" \
    "v_mfma_f32_16x16x32_bf16 v[92:95], v[176:179], a[180:183], v[92:95]\n\t" \
    "v_mfma_f32_16x16x32_bf16 v[88:91], v[180:183], a[184:187], v[88:91]\n\t" \
    "v_mfma_f32_16x16x32_bf16 v[92:95], v[184:187], a[184:187], v[92:95]\n\t" \
    "v_cvt_pk_bf16_f32 v108, v80, v81\n\t" \
    "v_mfma_f32_16x16x32_bf16 v[88:91], v[188:191], a[188:191], v[88:91]\n\t" \
    "v_cvt_pk_bf16_f32 v109, v82, v83\n\t" \
    "v_mfma_f32_16x16x32_bf16 v[92:95], v[192:195], a[188:191], v[92:95]\n\t" \
    "v_cvt_pk_bf16_f32 v110, v84, v85\n\t" \
    "v_mfma_f32_16x16x32_bf16 v[72:75], v[164:167], a[192:195], 0\n\t" \
    "v_cvt_pk_bf16_f32 v111, v86, v87\n\t" \
    "v_mfma_f32_16x16x32_bf16 v[76:79], v[168:171], a[192:195], 0\n\t" \
    "v_xor_b32 v18, s40, v9\n\t" \
    "v_mfma_f32_16x16x32_bf16 v[72:75], v[172:175], a[196:199], v[72:75]\n\t" \
    "global_store_dwordx4 v18, v[108:111], s[38:39] sc1\n\t" \
    "v_mfma_f32_16x16x32_bf16 v[76:79], v[176:179], a[196:199], v[76:79]\n\t" \
    "v_mfma_f32_16x16x32_bf16 v[72:75], v[180:183], a[200:203], v[72:75]\n\t" \
    "v_mfma_f32_16x16x32_bf16 v[76:79], v[184:187], a[200:203], v[76:79]\n\t" \
    "v_cvt_pk_bf16_f32 v104, v88, v89\n\t" \
    "v_mfma_f32_16x16x32_bf16 v[72:75], v[188:191], a[204:207], v[72:75]\n\t" \
    "v_cvt_pk_bf16_f32 v105, v90, v91\n\t" \
    "v_mfma_f32_16x16x32_bf16 v[76:79], v[192:195], a[204:207], v[76:79]\n\t" \
    "v_cvt_pk_bf16_f32 v106, v92, v93\n\t" \
    "v_mfma_f32_16x16x32_bf16 v[80:83], v[164:167], a[208:211], 0\n\t" \
    "v_cvt_pk_bf16_f32 v107, v94, v95\n\t" \
    "v_mfma_f32_16x16x32_bf16 v[84:87], v[168:171], a[208:211], 0\n\t" \
    "v_xor_b32 v18, s40, v10\n\t" \
    "v_mfma_f32_16x16x32_bf16 v[80:83], v[172:175], a[212:215], v[80:83]\n\t" \
    "global_store_dwordx4 v18, v[104:107], s[38:39] sc1\n\t" \
    "v_mfma_f32_16x16x32_bf16 v[84:87], v[176:179], a[212:215], v[84:87]\n\t" \
    "v_mfma_f32_16x16x32_bf16 v[80:83], v[180:183], a[216:219], v[80:83]\n\t" \
    "v_mfma_f32_16x16x32_bf16 v[84:87], v[184:187], a[216:219], v[84:87]\n\t" \
    "v_cvt_pk_bf16_f32 v108, v72, v73\n\t" \
    "v_mfma_f32_16x16x32_bf16 v[80:83], v[188:191], a[220:223], v[80:83]\n\t" \
    "v_cvt_pk_bf16_f32 v109, v74, v75\n\t" \
    "v_mfma_f32_16x16x32_bf16 v[84:87], v[192:195], a[220:223], v[84:87]\n\t" \
    "v_cvt_pk_bf16_f32 v110, v76, v77\n\t" \
    "v_mfma_f32_16x16x32_bf16 v[88:91], v[164:167], a[224:227], 0\n\t" \
    "v_cvt_pk_bf16_f32 v111, v78, v79\n\t" \
    "v_mfma_f32_16x16x32_bf16 v[92:95], v[168:171], a[224:227], 0\n\t" \
    "v_xor_b32 v18, s40, v11\n\t" \
    "v_mfma_f32_16x16x32_bf16 v[88:91], v[172:175], a[228:231], v[88:91]\n\t" \
    "global_store_dwordx4 v18, v[108:111], s[38:39] sc1\n\t" \
    "v_mfma_f32_16x16x32_bf16 v[92:95], v[176:179], a[228:231], v[92:95]\n\t" \
    "v_mfma_f32_16x16x32_bf16 v[88:91], v[180:183], a[232:235], v[88:91]\n\t" \
    "v_mfma_f32_16x16x32_bf16 v[92:95], v[184:187], a[232:235], v[92:95]\n\t" \
    "v_cvt_pk_bf16_f32 v104, v80, v81\n\t" \
    "v_mfma_f32_16x16x32_bf16 v[88:91], v[188:191], a[236:239], v[88:91]\n\t" \
    "v_cvt_pk_bf16_f32 v105, v82, v83\n\t" \
    "v_mfma_f32_16x16x32_bf16 v[92:95], v[192:195], a[236:239], v[92:95]\n\t" \
    "v_cvt_pk_bf16_f32 v106, v84, v85\n\t" \
    "v_mfma_f32_16x16x32_bf16 v[72:75], v[164:167], a[240:243], 0\n\t" \
    "v_cvt_pk_bf16_f32 v107, v86, v87\n\t" \
    "v_mfma_f32_16x16x32_bf16 v[76:79], v[168:171], a[240:243], 0\n\t" \
    "v_xor_b32 v18, s40, v12\n\t" \
    "v_mfma_f32_16x16x32_bf16 v[72:75], v[172:175], a[244:247], v[72:75]\n\t" \
    "global_store_dwordx4 v18, v[104:107], s[38:39] sc1\n\t" \
    "v_mfma_f32_16x16x32_bf16 v[76:79], v[176:179], a[244:247], v[76:79]\n\t" \
    "v_mfma_f32_16x16x32_bf16 v[72:75], v[180:183], a[248:251], v[72:75]\n\t" \
    "v_mfma_f32_16x16x32_bf16 v[76:79], v[184:187], a[248:251], v[76:79]\n\t" \
    "v_cvt_pk_bf16_f32 v108, v88, v89\n\t" \
    "v_mfma_f32_16x16x32_bf16 v[72:75], v[188:191], a[252:255], v[72:75]\n\t" \
    "v_cvt_pk_bf16_f32 v109, v90, v91\n\t" \
    "v_mfma_f32_16x16x32_bf16 v[76:79], v[192:195], a[252:255], v[76:79]\n\t" \
    "v_cvt_pk_bf16_f32 v110, v92, v93\n\t" \
    "v_mfma_f32_16x16x32_bf16 v[80:83], v[164:167], v[224:227], 0\n\t" \
    "v_cvt_pk_bf16_f32 v111, v94, v95\n\t" \
    "v_mfma_f32_16x16x32_bf16 v[84:87], v[168:171], v[224:227], 0\n\t" \
    "v_xor_b32 v18, s40, v13\n\t" \
    "v_mfma_f32_16x16x32_bf16 v[80:83], v[172:175], v[228:231], v[80:83]\n\t" \
    "global_store_dwordx4 v18, v[108:111], s[38:39] sc1\n\t" \
    "v_mfma_f32_16x16x32_bf16 v[84:87], v[176:179], v[228:231], v[84:87]\n\t" \
    "v_mfma_f32_16x16x32_bf16 v[80:83], v[180:183], v[232:235], v[80:83]\n\t" \
    "v_mfma_f32_16x16x32_bf16 v[84:87], v[184:187], v[232:235], v[84:87]\n\t" \
    "v_cvt_pk_bf16_f32 v104, v72, v73\n\t" \
    "v_mfma_f32_16x16x32_bf16 v[80:83], v[188:191], v[236:239], v[80:83]\n\t" \
    "v_cvt_pk_bf16_f32 v105, v74, v75\n\t" \
    "v_mfma_f32_16x16x32_bf16 v[84:87], v[192:195], v[236:239], v[84:87]\n\t" \
    "v_cvt_pk_bf16_f32 v106, v76, v77\n\t" \
    "v_mfma_f32_16x16x32_bf16 v[88:91], v[164:167], v[240:243], 0\n\t" \
    "v_cvt_pk_bf16_f32 v107, v78, v79\n\t" \
    "ds_read_b128 v[24:27], v22 offset:0\n\t" \
    "v_mfma_f32_16x16x32_bf16 v[92:95], v[168:171], v[240:243], 0\n\t" \
    "v_xor_b32 v18, s40, v14\n\t" \
    "ds_read_b128 v[28:31], v22 offset:18432\n\t" \
    "v_mfma_f32_16x16x32_bf16 v[88:91], v[172:175], v[244:247], v[88:91]\n\t" \
    "global_store_dwordx4 v18, v[104:107], s[38:39] sc1\n\t" \
    "ds_read_b128 v[32:35], v23 offset:0\n\t" \
    "v_mfma_f32_16x16x32_bf16 v[92:95], v[176:179], v[244:247], v[92:95]\n\t" \
    "ds_read_b128 v[36:39], v23 offset:18432\n\t" \
    "v_mfma_f32_16x16x32_bf16 v[88:91], v[180:183], v[248:251], v[88:91]\n\t" \
    "ds_read_b128 v[40:43], v22 offset:128\n\t" \
    "v_mfma_f32_16x16x32_bf16 v[92:95], v[184:187], v[248:251], v[92:95]\n\t" \
    "v_cvt_pk_bf16_f32 v108, v80, v81\n\t" \
    "ds_read_b128 v[44:47], v22 offset:18560\n\t" \
    "v_mfma_f32_16x16x32_bf16 v[88:91], v[188:191], v[252:255], v[88:91]\n\t" \
    "v_cvt_pk_bf16_f32 v109, v82, v83\n\t" \
    "v_mfma_f32_16x16x32_bf16 v[92:95], v[192:195], v[252:255], v[92:95]\n\t" \
    "v_cvt_pk_bf16_f32 v110, v84, v85\n\t" \
    "v_cvt_pk_bf16_f32 v111, v86, v87\n\t" \
    "v_xor_b32 v18, s40, v15\n\t" \
    "global_store_dwordx4 v18, v[108:111], s[38:39] sc1\n\t" \
    "s_nop 1\n\t" \
    "v_cvt_pk_bf16_f32 v104, v88, v89\n\t" \
    "v_cvt_pk_bf16_f32 v105, v90, v91\n\t" \
    "v_cvt_pk_bf16_f32 v106, v92, v93\n\t" \
    "v_cvt_pk_bf16_f32 v107, v94, v95\n\t" \
    "v_xor_b32 v18, s40, v16\n\t" \
    "global_store_dwordx4 v18, v[104:107], s[38:39] sc1\n\t" \
    "s_cmp_ge_u32 s65, 2\n\t" \
    "s_cbranch_scc1 gv_nofast9_%=\n\t" \
    "s_waitcnt vmcnt(0)\n\t" \
    "s_barrier\n\t" \
    "s_cmp_lg_u32 s66, 0\n\t" \
    "s_cbranch_scc1 gv_SF18_%=\n\t" \
    "s_lshl_b32 s69, s67, 7\n\t" \
    "s_add_u32 s69, s69, 3162112\n\t" \
    "s_add_u32 s70, s65, 0\n\t" \
    "v_mov_b32 v156, s69\n\t" \
    "v_mov_b32 v157, s70\n\t" \
    "s_mov_b64 exec, 1\n\t" \
    "global_store_dword v156, v157, s[100:101] sc1\n\t" \
    "s_mov_b64 exec, -1\n\t" \
    "gv_SF18_%=:\n\t" \
    "s_cmp_lg_u32 s66, 1\n\t" \
    "s_cbranch_scc1 gv_nofast9_%=\n\t" \
    "v_mov_b32 v120, 5769216\n\t" \
    "v_mov_b32 v121, 1\n\t" \
    "s_mov_b64 exec, 1\n\t" \
    "global_atomic_add v122, v120, v121, s[36:37] sc0 sc1\n\t" \
    "s_mov_b64 exec, -1\n\t" \
    "s_mov_b64 vcc, -1\n\t" \
    "gv_nofast9_%=:\n\t" \
    "s_mov_b32 s87, s86\n\t" \
    "s_mov_b32 s86, s65\n\t" \
    "s_mov_b32 s65, s84\n\t" \
    "s_mov_b32 s84, s85\n\t" \
    "s_cmp_lt_u32 s84, 24\n\t" \
    "s_addc_u32 s85, s84, 1\n\t" \
    "s_cmp_lt_u32 s65, s88\n\t" \
    "s_cbranch_scc1 gv_loop8_%=\n\t" \
    "s_waitcnt vmcnt(0) lgkmcnt(0)\n\t" \
    "s_barrier\n\t" \
    "s_mov_b32 s65, 99\n\t" \
    "s_cmp_lg_u32 s66, 0\n\t" \
    "s_cbranch_scc1 gv_SF20_%=\n\t" \
    "s_lshl_b32 s69, s67, 7\n\t" \
    "s_add_u32 s69, s69, 3162112\n\t" \
    "s_add_u32 s70, s65, 0\n\t" \
    "v_mov_b32 v156, s69\n\t" \
    "v_mov_b32 v157, s70\n\t" \
    "s_mov_b64 exec, 1\n\t" \
    "global_store_dword v156, v157, s[100:101] sc1\n\t" \
    "s_mov_b64 exec, -1\n\t" \
    "gv_SF20_%=:\n\t" \
    "s_branch gv_end21_%=\n\t" \
    "gv_gol12_%=:\n\t" \
    "s_mov_b64 vcc, 0\n\t" \
    "v_mbcnt_lo_u32_b32 v120, -1, 0\n\t" \
    "v_and_b32 v121, 3, v120\n\t" \
    "v_lshlrev_b32 v121, 16, v121\n\t" \
    "v_bfe_u32 v123, v120, 2, 2\n\t" \
    "v_lshl_add_u32 v121, v123, 11, v121\n\t" \
    "v_bfe_u32 v123, v120, 4, 1\n\t" \
    "v_lshl_add_u32 v121, v123, 14, v121\n\t" \
    "s_bitcmp1_b32 s44, 2\n\t" \
    "s_cselect_b32 s63, 8192, 0\n\t" \
    "s_or_b32 s63, s63, 0x200000\n\t" \
    "v_add_u32 v121, s63, v121\n\t" \
    "s_waitcnt vmcnt(9)\n\t" \
    "v_readfirstlane_b32 s69, v122\n\t" \
    "s_nop 1\n\t" \
    "s_cmp_lg_u32 s69, 126\n\t" \
    "s_cbranch_scc1 gv_go22_%=\n\t" \
    "s_memrealtime s[56:57]\n\t" \
    "s_waitcnt lgkmcnt(0)\n\t" \
    "s_lshl_b32 s69, s56, 4\n\t" \
    "s_add_u32 s69, s69, 2800\n\t" \
    "s_add_u32 s70, s56, 24\n\t" \
    "s_lshr_b32 s70, s70, 4\n\t" \
    "v_lshrrev_b32 v123, 2, v120\n\t" \
    "v_add_u32 v123, s70, v123\n\t" \
    "v_and_b32 v123, 63, v123\n\t" \
    "v_lshlrev_b32 v123, 6, v123\n\t" \
    "v_and_b32 v122, 3, v120\n\t" \
    "v_lshl_add_u32 v123, v122, 12, v123\n\t" \
    "v_add_u32 v123, 5783552, v123\n\t" \
    "v_mov_b32 v120, s69\n\t" \
    "s_mov_b32 exec_hi, 0\n\t" \
    "global_store_dword v123, v120, s[36:37] sc1\n\t" \
    "global_store_dword v121, v120, s[36:37] sc1\n\t" \
    "s_mov_b32 exec_hi, -1\n\t" \
    "gv_go22_%=:\n\t" \
    "s_branch gv_gob13_%=\n\t" \
    "gv_rest10_%=:\n\t" \
    "s_waitcnt vmcnt(12) lgkmcnt(0)\n\t" \
    "s_cmp_ge_i32 s86, 0\n\t" \
    "s_cbranch_scc1 gv_back11_%=\n\t" \
    "s_barrier\n\t" \
    "ds_read_b64_tr_b16 a[156:157], v212 offset:0\n\t" \
    "ds_read_b64_tr_b16 a[158:159], v212 offset:18432\n\t" \
    "ds_read_b64_tr_b16 a[172:173], v213 offset:0\n\t" \
    "ds_read_b64_tr_b16 a[174:175], v213 offset:18432\n\t" \
    "ds_read_b64_tr_b16 a[188:189], v214 offset:0\n\t" \
    "ds_read_b64_tr_b16 a[190:191], v214 offset:18432\n\t" \
    "s_waitcnt lgkmcnt(6)\n\t" \
    "ds_read_b64_tr_b16 a[204:205], v215 offset:0\n\t" \
    "ds_read_b64_tr_b16 a[206:207], v215 offset:18432\n\t" \
    "ds_read_b64_tr_b16 a[220:221], v212 offset:128\n\t" \
    "ds_read_b64_tr_b16 a[222:223], v212 offset:18560\n\t" \
    "ds_read_b64_tr_b16 a[236:237], v213 offset:128\n\t" \
    "ds_read_b64_tr_b16 a[238:239], v213 offset:18560\n\t" \
    "s_waitcnt lgkmcnt(6)\n\t" \
    "ds_read_b64_tr_b16 a[252:253], v214 offset:128\n\t" \
    "ds_read_b64_tr_b16 a[254:255], v214 offset:18560\n\t" \
    "ds_read_b64_tr_b16 v[236:237], v215 offset:128\n\t" \
    "ds_read_b64_tr_b16 v[238:239], v215 offset:18560\n\t" \
    "ds_read_b64_tr_b16 v[252:253], v212 offset:256\n\t" \
    "ds_read_b64_tr_b16 v[254:255], v212 offset:18688\n\t" \
    "s_waitcnt lgkmcnt(6)\n\t" \
    "v_add_u32 v212, 0x9000, v212\n\t" \
    "v_add_u32 v213, 0x9000, v213\n\t" \
    "v_add_u32 v214, 0x9000, v214\n\t" \
    "v_add_u32 v215, 0x9000, v215\n\t" \
    "s_branch gv_back11_%=\n\t" \
    "gv_end21_%=:\n\t" \
    "gv_last_%=:\n\t" \
    ""
#define ASM_CV \
    "v_mov_b32 v0, %0\n\t" \
    "v_mov_b32 v2, %2\n\t" \
    "v_subrev_u32 v1, 768, %1\n\t" \
    "v_mov_b32 v4, 0\n\t" \
    "v_mov_b32 v5, 0\n\t" \
    "v_mov_b32 v6, 0\n\t" \
    "v_mov_b32 v7, 0\n\t" \
    "v_mov_b32 v8, 0\n\t" \
    "v_mov_b32 v9, 0\n\t" \
    "v_mov_b32 v10, 0\n\t" \
    "v_mov_b32 v11, 0\n\t" \
    "v_mov_b32 v12, 0\n\t" \
    "v_mov_b32 v13, 0\n\t" \
    "v_mov_b32 v14, 0\n\t" \
    "v_mov_b32 v15, 0\n\t" \
    "v_mov_b32 v16, 0\n\t" \
    "v_mov_b32 v17, 0\n\t" \
    "v_mov_b32 v18, 0\n\t" \
    "v_mov_b32 v19, 0\n\t" \
    "v_mov_b32 v20, 0\n\t" \
    "v_mov_b32 v21, 0\n\t" \
    "v_mov_b32 v22, 0\n\t" \
    "v_mov_b32 v23, 0\n\t" \
    "v_mov_b32 v24, 0\n\t" \
    "v_mov_b32 v25, 0\n\t" \
    "v_mov_b32 v26, 0\n\t" \
    "v_mov_b32 v27, 0\n\t" \
    "v_mov_b32 v28, 0\n\t" \
    "v_mov_b32 v29, 0\n\t" \
    "v_mov_b32 v30, 0\n\t" \
    "v_mov_b32 v31, 0\n\t" \
    "v_mov_b32 v32, 0\n\t" \
    "v_mov_b32 v33, 0\n\t" \
    "v_mov_b32 v34, 0\n\t" \
    "v_mov_b32 v35, 0\n\t" \
    "s_waitcnt lgkmcnt(0)\n\t" \
    "ds_read_b64 v[36:37], v0 offset:0\n\t" \
    "ds_read_b64 v[38:39], v0 offset:8\n\t" \
    "ds_read_b64 v[40:41], v0 offset:32\n\t" \
    "ds_read_b64 v[42:43], v0 offset:40\n\t" \
    "ds_read_b128 v[44:47], v1 offset:768\n\t" \
    "ds_read_b128 v[48:51], v1 offset:2304\n\t" \
    "ds_read_b128 v[52:55], v1 offset:3840\n\t" \
    "ds_read_b128 v[56:59], v1 offset:5376\n\t" \
    "s_mov_b32 s69, 8\n\t" \
    "cv_c1_%=:\n\t" \
    "s_waitcnt lgkmcnt(3)\n\t" \
    "v_mfma_f32_16x16x32_bf16 v[4:7], v[36:39], v[44:47], v[4:7]\n\t" \
    "ds_read_b64 v[76:77], v0 offset:64\n\t" \
    "ds_read_b64 v[78:79], v0 offset:72\n\t" \
    "ds_read_b64 v[80:81], v0 offset:96\n\t" \
    "v_mfma_f32_16x16x32_bf16 v[20:23], v[40:43], v[44:47], v[20:23]\n\t" \
    "ds_read_b64 v[82:83], v0 offset:104\n\t" \
    "ds_read_b128 v[84:87], v1 offset:672\n\t" \
    "ds_read_b128 v[88:91], v1 offset:2208\n\t" \
    "s_waitcnt lgkmcnt(8)\n\t" \
    "v_mfma_f32_16x16x32_bf16 v[8:11], v[36:39], v[48:51], v[8:11]\n\t" \
    "ds_read_b128 v[92:95], v1 offset:3744\n\t" \
    "ds_read_b128 v[96:99], v1 offset:5280\n\t" \
    "v_mfma_f32_16x16x32_bf16 v[24:27], v[40:43], v[48:51], v[24:27]\n\t" \
    "s_waitcnt lgkmcnt(9)\n\t" \
    "v_mfma_f32_16x16x32_bf16 v[12:15], v[36:39], v[52:55], v[12:15]\n\t" \
    "v_mfma_f32_16x16x32_bf16 v[28:31], v[40:43], v[52:55], v[28:31]\n\t" \
    "s_waitcnt lgkmcnt(8)\n\t" \
    "v_mfma_f32_16x16x32_bf16 v[16:19], v[36:39], v[56:59], v[16:19]\n\t" \
    "v_mfma_f32_16x16x32_bf16 v[32:35], v[40:43], v[56:59], v[32:35]\n\t" \
    "s_waitcnt lgkmcnt(3)\n\t" \
    "v_mfma_f32_16x16x32_bf16 v[4:7], v[76:79], v[84:87], v[4:7]\n\t" \
    "ds_read_b64 v[36:37], v0 offset:128\n\t" \
    "ds_read_b64 v[38:39], v0 offset:136\n\t" \
    "ds_read_b64 v[40:41], v0 offset:160\n\t" \
    "v_mfma_f32_16x16x32_bf16 v[20:23], v[80:83], v[84:87], v[20:23]\n\t" \
    "ds_read_b64 v[42:43], v0 offset:168\n\t" \
    "ds_read_b128 v[44:47], v1 offset:576\n\t" \
    "ds_read_b128 v[48:51], v1 offset:2112\n\t" \
    "s_waitcnt lgkmcnt(8)\n\t" \
    "v_mfma_f32_16x16x32_bf16 v[8:11], v[76:79], v[88:91], v[8:11]\n\t" \
    "ds_read_b128 v[52:55], v1 offset:3648\n\t" \
    "ds_read_b128 v[56:59], v1 offset:5184\n\t" \
    "v_mfma_f32_16x16x32_bf16 v[24:27], v[80:83], v[88:91], v[24:27]\n\t" \
    "s_waitcnt lgkmcnt(9)\n\t" \
    "v_mfma_f32_16x16x32_bf16 v[12:15], v[76:79], v[92:95], v[12:15]\n\t" \
    "v_mfma_f32_16x16x32_bf16 v[28:31], v[80:83], v[92:95], v[28:31]\n\t" \
    "s_waitcnt lgkmcnt(8)\n\t" \
    "v_mfma_f32_16x16x32_bf16 v[16:19], v[76:79], v[96:99], v[16:19]\n\t" \
    "v_mfma_f32_16x16x32_bf16 v[32:35], v[80:83], v[96:99], v[32:35]\n\t" \
    "v_add_u32 v0, 128, v0\n\t" \
    "v_subrev_u32 v1, 192, v1\n\t" \
    "s_sub_u32 s69, s69, 1\n\t" \
    "s_cmp_lg_u32 s69, 0\n\t" \
    "s_cbranch_scc1 cv_c1_%=\n\t" \
    "s_waitcnt lgkmcnt(0)\n\t" \
    "ds_read_b64 v[36:37], v0 offset:0\n\t" \
    "ds_read_b64 v[38:39], v0 offset:8\n\t" \
    "ds_read_b64 v[40:41], v0 offset:32\n\t" \
    "ds_read_b64 v[42:43], v0 offset:40\n\t" \
    "ds_read_b128 v[48:51], v1 offset:2304\n\t" \
    "ds_read_b128 v[52:55], v1 offset:3840\n\t" \
    "ds_read_b128 v[56:59], v1 offset:5376\n\t" \
    "ds_read_b64 v[76:77], v0 offset:64\n\t" \
    "ds_read_b64 v[78:79], v0 offset:72\n\t" \
    "ds_read_b64 v[80:81], v0 offset:96\n\t" \
    "ds_read_b64 v[82:83], v0 offset:104\n\t" \
    "ds_read_b128 v[88:91], v1 offset:2208\n\t" \
    "ds_read_b128 v[92:95], v1 offset:3744\n\t" \
    "ds_read_b128 v[96:99], v1 offset:5280\n\t" \
    "s_mov_b32 s69, 4\n\t" \
    "cv_c2_%=:\n\t" \
    "s_waitcnt lgkmcnt(9)\n\t" \
    "v_mfma_f32_16x16x32_bf16 v[8:11], v[36:39], v[48:51], v[8:11]\n\t" \
    "ds_read_b64 v[116:117], v0 offset:128\n\t" \
    "ds_read_b64 v[118:119], v0 offset:136\n\t" \
    "ds_read_b64 v[120:121], v0 offset:160\n\t" \
    "v_mfma_f32_16x16x32_bf16 v[24:27], v[40:43], v[48:51], v[24:27]\n\t" \
    "ds_read_b64 v[122:123], v0 offset:168\n\t" \
    "ds_read_b128 v[128:131], v1 offset:2112\n\t" \
    "ds_read_b128 v[132:135], v1 offset:3648\n\t" \
    "s_waitcnt lgkmcnt(14)\n\t" \
    "v_mfma_f32_16x16x32_bf16 v[12:15], v[36:39], v[52:55], v[12:15]\n\t" \
    "ds_read_b128 v[136:139], v1 offset:5184\n\t" \
    "v_mfma_f32_16x16x32_bf16 v[28:31], v[40:43], v[52:55], v[28:31]\n\t" \
    "s_waitcnt lgkmcnt(14)\n\t" \
    "v_mfma_f32_16x16x32_bf16 v[16:19], v[36:39], v[56:59], v[16:19]\n\t" \
    "ds_read_b64 v[156:157], v0 offset:192\n\t" \
    "v_mfma_f32_16x16x32_bf16 v[32:35], v[40:43], v[56:59], v[32:35]\n\t" \
    "s_waitcnt lgkmcnt(10)\n\t" \
    "v_mfma_f32_16x16x32_bf16 v[8:11], v[76:79], v[88:91], v[8:11]\n\t" \
    "ds_read_b64 v[158:159], v0 offset:200\n\t" \
    "ds_read_b64 v[160:161], v0 offset:224\n\t" \
    "ds_read_b64 v[162:163], v0 offset:232\n\t" \
    "v_mfma_f32_16x16x32_bf16 v[24:27], v[80:83], v[88:91], v[24:27]\n\t" \
    "ds_read_b128 v[168:171], v1 offset:2016\n\t" \
    "ds_read_b128 v[172:175], v1 offset:3552\n\t" \
    "s_waitcnt lgkmcnt(14)\n\t" \
    "v_mfma_f32_16x16x32_bf16 v[12:15], v[76:79], v[92:95], v[12:15]\n\t" \
    "ds_read_b128 v[176:179], v1 offset:5088\n\t" \
    "v_mfma_f32_16x16x32_bf16 v[28:31], v[80:83], v[92:95], v[28:31]\n\t" \
    "s_waitcnt lgkmcnt(14)\n\t" \
    "v_mfma_f32_16x16x32_bf16 v[16:19], v[76:79], v[96:99], v[16:19]\n\t" \
    "v_mfma_f32_16x16x32_bf16 v[32:35], v[80:83], v[96:99], v[32:35]\n\t" \
    "s_waitcnt lgkmcnt(9)\n\t" \
    "v_mfma_f32_16x16x32_bf16 v[8:11], v[116:119], v[128:131], v[8:11]\n\t" \
    "ds_read_b64 v[36:37], v0 offset:256\n\t" \
    "ds_read_b64 v[38:39], v0 offset:264\n\t" \
    "ds_read_b64 v[40:41], v0 offset:288\n\t" \
    "v_mfma_f32_16x16x32_bf16 v[24:27], v[120:123], v[128:131], v[24:27]\n\t" \
    "ds_read_b64 v[42:43], v0 offset:296\n\t" \
    "ds_read_b128 v[48:51], v1 offset:1920\n\t" \
    "ds_read_b128 v[52:55], v1 offset:3456\n\t" \
    "s_waitcnt lgkmcnt(14)\n\t" \
    "v_mfma_f32_16x16x32_bf16 v[12:15], v[116:119], v[132:135], v[12:15]\n\t" \
    "ds_read_b128 v[56:59], v1 offset:4992\n\t" \
    "v_mfma_f32_16x16x32_bf16 v[28:31], v[120:123], v[132:135], v[28:31]\n\t" \
    "s_waitcnt lgkmcnt(14)\n\t" \
    "v_mfma_f32_16x16x32_bf16 v[16:19], v[116:119], v[136:139], v[16:19]\n\t" \
    "ds_read_b64 v[76:77], v0 offset:320\n\t" \
    "v_mfma_f32_16x16x32_bf16 v[32:35], v[120:123], v[136:139], v[32:35]\n\t" \
    "s_waitcnt lgkmcnt(10)\n\t" \
    "v_mfma_f32_16x16x32_bf16 v[8:11], v[156:159], v[168:171], v[8:11]\n\t" \
    "ds_read_b64 v[78:79], v0 offset:328\n\t" \
    "ds_read_b64 v[80:81], v0 offset:352\n\t" \
    "ds_read_b64 v[82:83], v0 offset:360\n\t" \
    "v_mfma_f32_16x16x32_bf16 v[24:27], v[160:163], v[168:171], v[24:27]\n\t" \
    "ds_read_b128 v[88:91], v1 offset:1824\n\t" \
    "ds_read_b128 v[92:95], v1 offset:3360\n\t" \
    "s_waitcnt lgkmcnt(14)\n\t" \
    "v_mfma_f32_16x16x32_bf16 v[12:15], v[156:159], v[172:175], v[12:15]\n\t" \
    "ds_read_b128 v[96:99], v1 offset:4896\n\t" \
    "v_mfma_f32_16x16x32_bf16 v[28:31], v[160:163], v[172:175], v[28:31]\n\t" \
    "s_waitcnt lgkmcnt(14)\n\t" \
    "v_mfma_f32_16x16x32_bf16 v[16:19], v[156:159], v[176:179], v[16:19]\n\t" \
    "v_mfma_f32_16x16x32_bf16 v[32:35], v[160:163], v[176:179], v[32:35]\n\t" \
    "v_add_u32 v0, 256, v0\n\t" \
    "v_subrev_u32 v1, 384, v1\n\t" \
    "s_sub_u32 s69, s69, 1\n\t" \
    "s_cmp_lg_u32 s69, 0\n\t" \
    "s_cbranch_scc1 cv_c2_%=\n\t" \
    "s_waitcnt lgkmcnt(0)\n\t" \
    "s_mov_b32 s69, 4\n\t" \
    "cv_c3_%=:\n\t" \
    "s_waitcnt lgkmcnt(7)\n\t" \
    "v_mfma_f32_16x16x32_bf16 v[12:15], v[36:39], v[52:55], v[12:15]\n\t" \
    "ds_read_b64 v[116:117], v0 offset:128\n\t" \
    "ds_read_b64 v[118:119], v0 offset:136\n\t" \
    "ds_read_b64 v[120:121], v0 offset:160\n\t" \
    "v_mfma_f32_16x16x32_bf16 v[28:31], v[40:43], v[52:55], v[28:31]\n\t" \
    "ds_read_b64 v[122:123], v0 offset:168\n\t" \
    "ds_read_b128 v[132:135], v1 offset:3648\n\t" \
    "ds_read_b128 v[136:139], v1 offset:5184\n\t" \
    "s_waitcnt lgkmcnt(12)\n\t" \
    "v_mfma_f32_16x16x32_bf16 v[16:19], v[36:39], v[56:59], v[16:19]\n\t" \
    "ds_read_b64 v[156:157], v0 offset:192\n\t" \
    "ds_read_b64 v[158:159], v0 offset:200\n\t" \
    "ds_read_b64 v[160:161], v0 offset:224\n\t" \
    "v_mfma_f32_16x16x32_bf16 v[32:35], v[40:43], v[56:59], v[32:35]\n\t" \
    "s_waitcnt lgkmcnt(10)\n\t" \
    "v_mfma_f32_16x16x32_bf16 v[12:15], v[76:79], v[92:95], v[12:15]\n\t" \
    "ds_read_b64 v[162:163], v0 offset:232\n\t" \
    "ds_read_b128 v[172:175], v1 offset:3552\n\t" \
    "ds_read_b128 v[176:179], v1 offset:5088\n\t" \
    "v_mfma_f32_16x16x32_bf16 v[28:31], v[80:83], v[92:95], v[28:31]\n\t" \
    "s_waitcnt lgkmcnt(12)\n\t" \
    "v_mfma_f32_16x16x32_bf16 v[16:19], v[76:79], v[96:99], v[16:19]\n\t" \
    "v_mfma_f32_16x16x32_bf16 v[32:35], v[80:83], v[96:99], v[32:35]\n\t" \
    "s_waitcnt lgkmcnt(7)\n\t" \
    "v_mfma_f32_16x16x32_bf16 v[12:15], v[116:119], v[132:135], v[12:15]\n\t" \
    "ds_read_b64 v[36:37], v0 offset:256\n\t" \
    "ds_read_b64 v[38:39], v0 offset:264\n\t" \
    "ds_read_b64 v[40:41], v0 offset:288\n\t" \
    "v_mfma_f32_16x16x32_bf16 v[28:31], v[120:123], v[132:135], v[28:31]\n\t" \
    "ds_read_b64 v[42:43], v0 offset:296\n\t" \
    "ds_read_b128 v[52:55], v1 offset:3456\n\t" \
    "ds_read_b128 v[56:59], v1 offset:4992\n\t" \
    "s_waitcnt lgkmcnt(12)\n\t" \
    "v_mfma_f32_16x16x32_bf16 v[16:19], v[116:119], v[136:139], v[16:19]\n\t" \
    "ds_read_b64 v[76:77], v0 offset:320\n\t" \
    "ds_read_b64 v[78:79], v0 offset:328\n\t" \
    "ds_read_b64 v[80:81], v0 offset:352\n\t" \
    "v_mfma_f32_16x16x32_bf16 v[32:35], v[120:123], v[136:139], v[32:35]\n\t" \
    "s_waitcnt lgkmcnt(10)\n\t" \
    "v_mfma_f32_16x16x32_bf16 v[12:15], v[156:159], v[172:175], v[12:15]\n\t" \
    "ds_read_b64 v[82:83], v0 offset:360\n\t" \
    "ds_read_b128 v[92:95], v1 offset:3360\n\t" \
    "ds_read_b128 v[96:99], v1 offset:4896\n\t" \
    "v_mfma_f32_16x16x32_bf16 v[28:31], v[160:163], v[172:175], v[28:31]\n\t" \
    "s_waitcnt lgkmcnt(12)\n\t" \
    "v_mfma_f32_16x16x32_bf16 v[16:19], v[156:159], v[176:179], v[16:19]\n\t" \
    "v_mfma_f32_16x16x32_bf16 v[32:35], v[160:163], v[176:179], v[32:35]\n\t" \
    "v_add_u32 v0, 256, v0\n\t" \
    "v_subrev_u32 v1, 384, v1\n\t" \
    "s_sub_u32 s69, s69, 1\n\t" \
    "s_cmp_lg_u32 s69, 0\n\t" \
    "s_cbranch_scc1 cv_c3_%=\n\t" \
    "s_waitcnt lgkmcnt(0)\n\t" \
    "s_mov_b32 s69, 4\n\t" \
    "cv_c4_%=:\n\t" \
    "s_waitcnt lgkmcnt(5)\n\t" \
    "v_mfma_f32_16x16x32_bf16 v[16:19], v[36:39], v[56:59], v[16:19]\n\t" \
    "ds_read_b64 v[116:117], v0 offset:128\n\t" \
    "ds_read_b64 v[118:119], v0 offset:136\n\t" \
    "ds_read_b64 v[120:121], v0 offset:160\n\t" \
    "v_mfma_f32_16x16x32_bf16 v[32:35], v[40:43], v[56:59], v[32:35]\n\t" \
    "ds_read_b64 v[122:123], v0 offset:168\n\t" \
    "ds_read_b128 v[136:139], v1 offset:5184\n\t" \
    "ds_read_b64 v[156:157], v0 offset:192\n\t" \
    "s_waitcnt lgkmcnt(6)\n\t" \
    "v_mfma_f32_16x16x32_bf16 v[16:19], v[76:79], v[96:99], v[16:19]\n\t" \
    "ds_read_b64 v[158:159], v0 offset:200\n\t" \
    "ds_read_b64 v[160:161], v0 offset:224\n\t" \
    "ds_read_b64 v[162:163], v0 offset:232\n\t" \
    "v_mfma_f32_16x16x32_bf16 v[32:35], v[80:83], v[96:99], v[32:35]\n\t" \
    "ds_read_b128 v[176:179], v1 offset:5088\n\t" \
    "s_waitcnt lgkmcnt(5)\n\t" \
    "v_mfma_f32_16x16x32_bf16 v[16:19], v[116:119], v[136:139], v[16:19]\n\t" \
    "ds_read_b64 v[36:37], v0 offset:256\n\t" \
    "ds_read_b64 v[38:39], v0 offset:264\n\t" \
    "ds_read_b64 v[40:41], v0 offset:288\n\t" \
    "v_mfma_f32_16x16x32_bf16 v[32:35], v[120:123], v[136:139], v[32:35]\n\t" \
    "ds_read_b64 v[42:43], v0 offset:296\n\t" \
    "ds_read_b128 v[56:59], v1 offset:4992\n\t" \
    "ds_read_b64 v[76:77], v0 offset:320\n\t" \
    "s_waitcnt lgkmcnt(6)\n\t" \
    "v_mfma_f32_16x16x32_bf16 v[16:19], v[156:159], v[176:179], v[16:19]\n\t" \
    "ds_read_b64 v[78:79], v0 offset:328\n\t" \
    "ds_read_b64 v[80:81], v0 offset:352\n\t" \
    "ds_read_b64 v[82:83], v0 offset:360\n\t" \
    "v_mfma_f32_16x16x32_bf16 v[32:35], v[160:163], v[176:179], v[32:35]\n\t" \
    "ds_read_b128 v[96:99], v1 offset:4896\n\t" \
    "v_add_u32 v0, 256, v0\n\t" \
    "v_subrev_u32 v1, 384, v1\n\t" \
    "s_sub_u32 s69, s69, 1\n\t" \
    "s_cmp_lg_u32 s69, 0\n\t" \
    "s_cbranch_scc1 cv_c4_%=\n\t" \
    "s_waitcnt lgkmcnt(0)\n\t" \
    "s_nop 7\n\t" \
    "ds_write_b128 v2, v[4:7] offset:0\n\t" \
    "ds_write_b128 v2, v[8:11] offset:1024\n\t" \
    "ds_write_b128 v2, v[12:15] offset:2048\n\t" \
    "ds_write_b128 v2, v[16:19] offset:3072\n\t" \
    "ds_write_b128 v2, v[20:23] offset:4096\n\t" \
    "ds_write_b128 v2, v[24:27] offset:5120\n\t" \
    "ds_write_b128 v2, v[28:31] offset:6144\n\t" \
    "ds_write_b128 v2, v[32:35] offset:7168\n\t" \
    "s_waitcnt lgkmcnt(0)\n\t" \
    "cv_last_%=:\n\t" \
    ""
#define ASM_EA \
    "s_bitcmp1_b32 %1, 2\n\t" \
    "s_cselect_b32 s40, 0x30, 0x28\n\t" \
    "s_cmp_lt_u32 %1, 64\n\t" \
    "s_cselect_b32 s40, s40, 0x38\n\t" \
    "s_load_dwordx2 s[42:43], %0, s40\n\t" \
    "s_load_dword s48, %0, 0x40\n\t" \
    "s_getpc_b64 s[44:45]\n\t" \
    "ea_pc_%=:\n\t" \
    "s_bfe_u32 s40, %1, 0x20003\n\t" \
    "s_lshl_b32 s40, s40, 15\n\t" \
    "v_lshl_add_u32 v1, %2, 7, s40\n\t" \
    "v_min_u32 v1, code_end_marker-ea_pc_%=, v1\n\t" \
    "s_mov_b32 m0, 155648\n\t" \
    "v_readfirstlane_b32 s41, %2\n\t" \
    "global_load_lds_dword v1, s[44:45]\n\t" \
    "s_bitcmp1_b32 %1, 2\n\t" \
    "s_cselect_b32 s40, 24, 16\n\t" \
    "s_lshr_b32 s47, %1, 3\n\t" \
    "s_cmp_ge_u32 s47, s40\n\t" \
    "s_cbranch_scc1 ea_done1_%=\n\t" \
    "s_mov_b32 s46, pad_qk-ea_pc_%=\n\t" \
    "s_mov_b32 s40, pad_vv-ea_pc_%=\n\t" \
    "s_bitcmp1_b32 %1, 2\n\t" \
    "s_cselect_b32 s46, pad_gx-ea_pc_%=, s46\n\t" \
    "s_cselect_b32 s40, pad_gv-ea_pc_%=, s40\n\t" \
    "s_cmp_lt_u32 %1, 64\n\t" \
    "s_cselect_b32 s40, s46, s40\n\t" \
    "s_add_u32 s44, s44, s40\n\t" \
    "s_addc_u32 s45, s45, 0\n\t" \
    "s_swappc_b64 s[46:47], s[44:45]\n\t" \
    "s_lshl_b32 m0, s41, 4\n\t" \
    "s_and_b32 s40, %1, 3\n\t" \
    "s_lshl_b32 s40, s40, 11\n\t" \
    "s_bfe_u32 s46, %1, 0x30003\n\t" \
    "s_lshl_b32 s46, s46, 8\n\t" \
    "s_add_u32 s40, s40, s46\n\t" \
    "s_and_b32 s41, s41, 0x80\n\t" \
    "s_xor_b32 s40, s40, s41\n\t" \
    "v_lshrrev_b32 v1, 3, %2\n\t" \
    "v_and_b32 v1, 6, v1\n\t" \
    "v_xor_b32 v1, v1, %2\n\t" \
    "v_and_b32 v1, 15, v1\n\t" \
    "v_lshlrev_b32 v1, 4, v1\n\t" \
    "v_xor_b32 v1, s40, v1\n\t" \
    "v_lshrrev_b32 v2, 4, %2\n\t" \
    "v_lshl_add_u32 v1, v2, 13, v1\n\t" \
    "s_mov_b32 s40, 16\n\t" \
    "s_waitcnt lgkmcnt(0)\n\t" \
    "ea_lp2_%=:\n\t" \
    "global_load_lds_dwordx4 v1, s[42:43]\n\t" \
    "v_add_u32 v1, 0x20000, v1\n\t" \
    "s_add_u32 m0, m0, 4096\n\t" \
    "s_sub_u32 s40, s40, 1\n\t" \
    "s_cmp_lg_u32 s40, 0\n\t" \
    "s_cbranch_scc1 ea_lp2_%=\n\t" \
    "ea_done1_%=:\n\t" \
    "s_waitcnt lgkmcnt(0)\n\t" \
    "ea_last_%=:\n\t" \
    ""
#define ASM_CLOB "v0", "v1", "v2", "v3", "v4", "v5", "v6", "v7", "v8", "v9", "v10", "v11", "v12", "v13", "v14", "v15", "v16", "v17", "v18", "v19", "v20", "v21", "v22", "v23", "v24", "v25", "v26", "v27", "v28", "v29", "v30", "v31", "v32", "v33", "v34", "v35", "v36", "v37", "v38", "v39", "v40", "v41", "v42", "v43", "v44", "v45", "v46", "v47", "v48", "v49", "v50", "v51", "v52", "v53", "v54", "v55", "v56", "v57", "v58", "v59", "v60", "v61", "v62", "v63", "v64", "v65", "v66", "v67", "v68", "v69", "v70", "v71", "v72", "v73", "v74", "v75", "v76", "v77", "v78", "v79", "v80", "v81", "v82", "v83", "v84", "v85", "v86", "v87", "v88", "v89", "v90", "v91", "v92", "v93", "v94", "v95", "v96", "v97", "v98", "v99", "v100", "v101", "v102", "v103", "v104", "v105", "v106", "v107", "v108", "v109", "v110", "v111", "v112", "v113", "v114", "v115", "v116", "v117", "v118", "v119", "v120", "v121", "v122", "v123", "v124", "v125", "v126", "v127", "v128", "v129", "v130", "v131", "v132", "v133", "v134", "v135", "v136", "v137", "v138", "v139", "v140", "v141", "v142", "v143", "v144", "v145", "v146", "v147", "v148", "v149", "v150", "v151", "v152", "v153", "v154", "v155", "v156", "v157", "v158", "v159", "v160", "v161", "v162", "v163", "v164", "v165", "v166", "v167", "v168", "v169", "v170", "v171", "v172", "v173", "v174", "v175", "v176", "v177", "v178", "v179", "v180", "v181", "v182", "v183", "v184", "v185", "v186", "v187", "v188", "v189", "v190", "v191", "v192", "v193", "v194", "v195", "v196", "v197", "v198", "v199", "v200", "v201", "v202", "v203", "v204", "v205", "v206", "v207", "v208", "v209", "v210", "v211", "v212", "v213", "v214", "v215", "v222", "v223", "v224", "v225", "v226", "v227", "v228", "v229", "v230", "v231", "v232", "v233", "v234", "v235", "v236", "v237", "v238", "v239", "v240", "v241", "v242", "v243", "v244", "v245", "v246", "v247", "v248", "v249", "v250", "v251", "v252", "v253", "v254", "v255", "a0", "a1", "a2", "a3", "a4", "a5", "a6", "a7", "a8", "a9", "a10", "a11", "a12", "a13", "a14", "a15", "a16", "a17", "a18", "a19", "a20", "a21", "a22", "a23", "a24", "a25", "a26", "a27", "a28", "a29", "a30", "a31", "a32", "a33", "a34", "a35", "a36", "a37", "a38", "a39", "a40", "a41", "a42", "a43", "a44", "a45", "a46", "a47", "a48", "a49", "a50", "a51", "a52", "a53", "a54", "a55", "a56", "a57", "a58", "a59", "a60", "a61", "a62", "a63", "a64", "a65", "a66", "a67", "a68", "a69", "a70", "a71", "a72", "a73", "a74", "a75", "a76", "a77", "a78", "a79", "a80", "a81", "a82", "a83", "a84", "a85", "a86", "a87", "a88", "a89", "a90", "a91", "a92", "a93", "a94", "a95", "a96", "a97", "a98", "a99", "a100", "a101", "a102", "a103", "a104", "a105", "a106", "a107", "a108", "a109", "a110", "a111", "a112", "a113", "a114", "a115", "a116", "a117", "a118", "a119", "a120", "a121", "a122", "a123", "a124", "a125", "a126", "a127", "a128", "a129", "a130", "a131", "a132", "a133", "a134", "a135", "a136", "a137", "a138", "a139", "a140", "a141", "a142", "a143", "a144", "a145", "a146", "a147", "a148", "a149", "a150", "a151", "a152", "a153", "a154", "a155", "a156", "a157", "a158", "a159", "a160", "a161", "a162", "a163", "a164", "a165", "a166", "a167", "a168", "a169", "a170", "a171", "a172", "a173", "a174", "a175", "a176", "a177", "a178", "a179", "a180", "a181", "a182", "a183", "a184", "a185", "a186", "a187", "a188", "a189", "a190", "a191", "a192", "a193", "a194", "a195", "a196", "a197", "a198", "a199", "a200", "a201", "a202", "a203", "a204", "a205", "a206", "a207", "a208", "a209", "a210", "a211", "a212", "a213", "a214", "a215", "a216", "a217", "a218", "a219", "a220", "a221", "a222", "a223", "a224", "a225", "a226", "a227", "a228", "a229", "a230", "a231", "a232", "a233", "a234", "a235", "a236", "a237", "a238", "a239", "a240", "a241", "a242", "a243", "a244", "a245", "a246", "a247", "a248", "a249", "a250", "a251", "a252", "a253", "a254", "a255", "s33", "s34", "s35", "s36", "s37", "s38", "s39", "s40", "s41", "s42", "s43", "s44", "s45", "s46", "s47", "s48", "s49", "s50", "s51", "s52", "s53", "s54", "s55", "s56", "s57", "s58", "s59", "s60", "s61", "s62", "s63", "s64", "s65", "s66", "s67", "s68", "s69", "s70", "s71", "s72", "s73", "s74", "s75", "s76", "s77", "s78", "s79", "s80", "s81", "s82", "s83", "s84", "s85", "s86", "s87", "s88", "s89", "s90", "s91", "s92", "s93", "s94", "s95", "s96", "s97", "s98", "s99", "s100", "s101", "vcc", "scc", "memory"
#define ASM_CLOBW "v32", "v33", "v34", "v35", "v36", "v37", "v38", "v39", "v40", "v41", "v42", "v43", "v44", "v45", "v46", "v47", "v48", "v49", "v50", "v51", "v52", "v53", "v54", "v55", "v56", "v57", "v58", "v59", "v60", "v61", "v62", "v63", "v64", "v65", "v66", "v67", "v68", "v69", "v70", "v71", "v72", "v73", "v74", "v75", "v76", "v77", "v78", "v79", "v80", "v81", "v82", "v83", "v84", "v85", "v86", "v87", "v88", "v89", "v90", "v91", "v92", "v93", "v94", "v95", "v96", "v97", "v98", "v99", "v100", "v101", "v102", "v103", "v104", "v105", "v106", "v107", "v108", "v109", "v110", "v111", "v112", "v113", "v114", "v115", "v116", "v117", "v118", "v119", "v120", "v121", "v122", "v123", "v124", "v125", "v126", "v127", "v128", "v129", "v130", "v131", "v132", "v133", "v134", "v135", "v136", "v137", "v138", "v139", "v140", "v141", "v142", "v143", "v144", "v145", "v146", "v147", "v148", "v149", "v150", "v151", "v152", "v153", "v154", "v155", "v156", "v157", "v158", "v159", "v160", "v161", "v162", "v163", "v164", "v165", "v166", "v167", "v168", "v169", "v170", "v171", "v172", "v173", "v174", "v175", "v176", "v177", "v178", "v179", "v180", "v181", "v182", "v183", "v184", "v185", "v186", "v187", "v188", "v189", "v190", "v191", "v192", "v193", "v194", "v195", "v196", "v197", "v198", "v199", "v222", "v223", "v224", "v225", "v226", "v227", "v228", "v229", "v230", "v231", "v232", "v233", "v234", "v235", "v236", "v237", "v238", "v239", "v240", "v241", "v242", "v243", "v244", "v245", "v246", "v247", "v248", "v249", "v250", "v251", "v252", "v253", "v254", "v255", "a0", "a1", "a2", "a3", "a4", "a5", "a6", "a7", "a8", "a9", "a10", "a11", "a12", "a13", "a14", "a15", "a16", "a17", "a18", "a19", "a20", "a21", "a22", "a23", "a24", "a25", "a26", "a27", "a28", "a29", "a30", "a31", "a32", "a33", "a34", "a35", "a36", "a37", "a38", "a39", "a40", "a41", "a42", "a43", "a44", "a45", "a46", "a47", "a48", "a49", "a50", "a51", "a52", "a53", "a54", "a55", "a56", "a57", "a58", "a59", "a60", "a61", "a62", "a63", "a64", "a65", "a66", "a67", "a68", "a69", "a70", "a71", "a72", "a73", "a74", "a75", "a76", "a77", "a78", "a79", "a80", "a81", "a82", "a83", "a84", "a85", "a86", "a87", "a88", "a89", "a90", "a91", "a92", "a93", "a94", "a95", "a96", "a97", "a98", "a99", "a100", "a101", "a102", "a103", "a104", "a105", "a106", "a107", "a108", "a109", "a110", "a111", "a112", "a113", "a114", "a115", "a116", "a117", "a118", "a119", "a120", "a121", "a122", "a123", "a124", "a125", "a126", "a127", "a128", "a129", "a130", "a131", "a132", "a133", "a134", "a135", "a136", "a137", "a138", "a139", "a140", "a141", "a142", "a143", "a144", "a145", "a146", "a147", "a148", "a149", "a150", "a151", "a152", "a153", "a154", "a155", "a156", "a157", "a158", "a159", "a160", "a161", "a162", "a163", "a164", "a165", "a166", "a167", "a168", "a169", "a170", "a171", "a172", "a173", "a174", "a175", "a176", "a177", "a178", "a179", "a180", "a181", "a182", "a183", "a184", "a185", "a186", "a187", "a188", "a189", "a190", "a191", "a192", "a193", "a194", "a195", "a196", "a197", "a198", "a199", "a200", "a201", "a202", "a203", "a204", "a205", "a206", "a207", "a208", "a209", "a210", "a211", "a212", "a213", "a214", "a215", "a216", "a217", "a218", "a219", "a220", "a221", "a222", "a223", "a224", "a225", "a226", "a227", "a228", "a229", "a230", "a231", "a232", "a233", "a234", "a235", "a236", "a237", "a238", "a239", "a240", "a241", "a242", "a243", "a244", "a245", "a246", "a247", "a248", "a249", "a250", "a251", "a252", "a253", "a254", "a255", "s33", "s34", "s35", "s36", "s37", "s38", "s39", "s40", "s41", "s42", "s43", "s44", "s45", "s46", "s47", "s48", "s49", "s50", "s51", "s52", "s53", "s54", "s55", "s56", "s57", "s58", "s59", "s60", "s61", "s62", "s63", "s64", "s65", "s66", "s67", "s68", "s69", "s70", "s71", "s72", "s73", "s74", "s75", "s76", "s77", "s78", "s79", "s80", "s81", "s82", "s83", "s84", "s85", "s86", "s87", "s88", "s89", "s90", "s91", "s92", "s93", "s94", "s95", "s96", "s97", "s98", "s99", "s100", "s101", "vcc", "scc", "memory"
#define CV_CLOB "v0", "v1", "v2", "v3", "v4", "v5", "v6", "v7", "v8", "v9", "v10", "v11", "v12", "v13", "v14", "v15", "v16", "v17", "v18", "v19", "v20", "v21", "v22", "v23", "v24", "v25", "v26", "v27", "v28", "v29", "v30", "v31", "v32", "v33", "v34", "v35", "v36", "v37", "v38", "v39", "v40", "v41", "v42", "v43", "v44", "v45", "v46", "v47", "v48", "v49", "v50", "v51", "v52", "v53", "v54", "v55", "v56", "v57", "v58", "v59", "v60", "v61", "v62", "v63", "v64", "v65", "v66", "v67", "v68", "v69", "v70", "v71", "v72", "v73", "v74", "v75", "v76", "v77", "v78", "v79", "v80", "v81", "v82", "v83", "v84", "v85", "v86", "v87", "v88", "v89", "v90", "v91", "v92", "v93", "v94", "v95", "v96", "v97", "v98", "v99", "v100", "v101", "v102", "v103", "v104", "v105", "v106", "v107", "v108", "v109", "v110", "v111", "v112", "v113", "v114", "v115", "v116", "v117", "v118", "v119", "v120", "v121", "v122", "v123", "v124", "v125", "v126", "v127", "v128", "v129", "v130", "v131", "v132", "v133", "v134", "v135", "v136", "v137", "v138", "v139", "v140", "v141", "v142", "v143", "v144", "v145", "v146", "v147", "v148", "v149", "v150", "v151", "v152", "v153", "v154", "v155", "v156", "v157", "v158", "v159", "v160", "v161", "v162", "v163", "v164", "v165", "v166", "v167", "v168", "v169", "v170", "v171", "v172", "v173", "v174", "v175", "v176", "v177", "v178", "v179", "v180", "v181", "v182", "v183", "v184", "v185", "v186", "v187", "v188", "v189", "v190", "v191", "v192", "v193", "v194", "v195", "s69", "scc", "memory"
static constexpr unsigned G_STEP = 0x10000;
static constexpr unsigned G_WS_CTL = 0x300000;
static constexpr unsigned G_WS_FIX = 0x580000;
static constexpr unsigned G_WS_DONE = 0x5f0000;
static constexpr unsigned G_WS_EP = 0x5f2000;
static constexpr unsigned G_WS_STAMPS = 0xc000000;
static constexpr unsigned G_C_TASK = 0x200;
static constexpr unsigned G_C_GX = 0x1000;
static constexpr unsigned G_C_GV = 0x4000;
static constexpr unsigned G_C_KM = 0x8000;
static constexpr unsigned G_C_UM = 0x9000;
static constexpr unsigned G_C_PC = 0xc000;
static constexpr unsigned G_C_YC = 0xd000;
static constexpr unsigned G_C_GQ = 0x10000;
static constexpr unsigned G_C_FX = 0x280000;
static constexpr unsigned G_C_ERR = 0x280100;
static constexpr unsigned G_C_ARR = 0x280400;
static constexpr unsigned G_C_GO = 0x280800;
static constexpr unsigned G_C_UT = 0x281000;
static constexpr unsigned G_C_RDY = 0x282000;
static constexpr unsigned G_C_GT = 0x284000;
static constexpr unsigned G_C_KG = 0x288000;
static constexpr unsigned G_C_RW = 0x280000;
static constexpr unsigned G_WS_H = 0x0;
static constexpr unsigned G_HS8 = 0x80000;
static constexpr unsigned G_HS4 = 0x40000;
static constexpr unsigned G_UXS = 0x200000;
static constexpr unsigned G_UMS = 0x90000;
static constexpr unsigned G_PROBE = 0x1bd400;
static constexpr unsigned G_D_QX = 0x600000;
static constexpr unsigned G_D_KX = 0x1600000;
static constexpr unsigned G_D_KM = 0x2600000;
static constexpr unsigned G_D_UX = 0x2e00000;
static constexpr unsigned G_D_UM = 0x7600000;
static constexpr unsigned G_D_PX = 0xa200000;
static constexpr unsigned G_D_PM = 0xa600000;
static constexpr unsigned G_D_KT = 0xa700000;
static constexpr unsigned G_D_UT = 0xac00000;
static constexpr unsigned G_D_YB = 0xaf00000;
static constexpr unsigned G_D_END = 0xb200000;
static constexpr unsigned G_SKM = 0x7;
static constexpr unsigned G_S1 = 0x18;
static constexpr unsigned G_S0 = 0x6;
static constexpr unsigned G_GVN = 0x2;
static constexpr unsigned G_STG = 0x9000;
static constexpr unsigned G_KROW = 0x30;
static constexpr unsigned G_NHL = 0x1;
#define FLG(x_) (kDefault[5])
#define FLH(x_) (kDefault[7])
static constexpr int kDefault[8] = {PRM_DEFAULT};
#define MFMA32(a_, b_, c_) __builtin_amdgcn_mfma_f32_16x16x32_bf16(__builtin_bit_cast(bf16x8, a_), __builtin_bit_cast(bf16x8, b_), c_, 0, 0, 0)
#define MFMA16(a_, b_, c_) __builtin_amdgcn_mfma_f32_16x16x16bf16_1k(__builtin_bit_cast(s16x4, a_), __builtin_bit_cast(s16x4, b_), c_, 0, 0, 0)
#define WLD8(d_, voff_, base_, off_) asm volatile("s_nop 4\n\tglobal_load_dwordx2 %0, %1, %2 offset:%3 sc1" : "=v"(d_) : "v"(voff_), "s"(base_), "i"(off_) : "memory")
#define WLD16(d_, voff_, base_, off_) asm volatile("s_nop 4\n\tglobal_load_dwordx4 %0, %1, %2 offset:%3 sc1" : "=v"(d_) : "v"(voff_), "s"(base_), "i"(off_) : "memory")
#define WST16(voff_, d_, base_, off_) asm volatile("s_nop 4\n\tglobal_store_dwordx4 %0, %1, %2 offset:%3 sc1\n\ts_nop 1" :: "v"(voff_), "v"(d_), "s"(base_), "i"(off_) : "memory")
#define OMOD ""
#define OST8(p_, d_) asm volatile("global_store_dwordx2 %0, %1, off " OMOD :: "v"(p_), "v"(d_) : "memory")
#define OST16(p_, d_) asm volatile("global_store_dwordx4 %0, %1, off " OMOD "\n\ts_nop 1" :: "v"(p_), "v"(d_) : "memory")
#define OWM "sc1"
#define OST16W(p_, d_) asm volatile("global_store_dwordx4 %0, %1, off " OWM "\n\ts_nop 1" :: "v"(p_), "v"(d_) : "memory")
#define WLD16X4(d0, d1, d2, d3, voff_, base_) asm volatile("s_nop 4\n\tglobal_load_dwordx4 %0, %4, %5 sc1\n\tglobal_load_dwordx4 %1, %4, %5 offset:1024 sc1\n\t" \
    "global_load_dwordx4 %2, %4, %5 offset:2048 sc1\n\tglobal_load_dwordx4 %3, %4, %5 offset:3072 sc1" \
    : "=&v"(d0), "=&v"(d1), "=&v"(d2), "=&v"(d3) : "v"(voff_), "s"(base_) : "memory")

__device__ __forceinline__ u32x4 pack8(const f32x4& a, const f32x4& b) {
    return u32x4{pk2bf(a[0], a[1]), pk2bf(a[2], a[3]), pk2bf(b[0], b[1]), pk2bf(b[2], b[3])};
}
template <class T> __device__ __forceinline__ T* uni(T* q) {
    const u64 v = (u64)q;
    unsigned lo = __builtin_amdgcn_readfirstlane((unsigned)v), hi = __builtin_amdgcn_readfirstlane((unsigned)(v >> 32));
    asm volatile("s_nop 4" : "+s"(lo), "+s"(hi));
    return (T*)(((u64)hi << 32) | lo);
}
#define WC(p_) ((char*)(p_).X)
template <bool FAST = false>
__device__ __forceinline__ void wait_word(const MainP& p, const char* f, int want, unsigned code) {
    unsigned tries = 0;
#pragma unroll 1
    while (__any((int)__hip_atomic_load((const gu32*)f, RLX_AGENT) < want)) {
        if (++tries > kWaitCap) { __hip_atomic_store((gu32*)(p.ws + G_WS_CTL + G_C_ERR), code, RLX_AGENT); break; }
        if ((tries & 1023u) == 0u && __hip_atomic_load((const gu32*)(p.ws + G_WS_CTL + G_C_ERR), RLX_AGENT) != 0xffffffffu) break;
        if (FAST) __builtin_amdgcn_s_sleep(1); else __builtin_amdgcn_s_sleep(10);
    }
}

__device__ __forceinline__ int skew(int s, int id) { return (s ^ (G_SKM * id)) & 63; }
__device__ __forceinline__ unsigned um_base(int s, unsigned nm) { return G_D_UM + (s / 3) * 0x200000 + (s % 3) * G_UMS + ((nm >> (2 + s / 3)) & 1) * 8192; }
typedef GAS unsigned long long gu64;
#define IST(k_, slot_)
__device__ __forceinline__ void init_done(char* ws, int slot, unsigned val) {
    const unsigned e = __hip_atomic_load((const gu32*)(ws + G_WS_EP), RLX_AGENT);
    __hip_atomic_store((gu64*)(ws + G_WS_DONE + slot * 64), ((u64)val << 32) | (e + 1u), RLX_AGENT);
}
__device__ __forceinline__ void init_wait(char* ws, int w, int lane) {
    if (w == 0) {
        const unsigned e = __hip_atomic_load((const gu32*)(ws + G_WS_EP), RLX_AGENT);
        for (unsigned tries = 0; tries < kWaitCap; ++tries) {
            const unsigned d0 = __hip_atomic_load((const gu32*)(ws + G_WS_DONE + lane * 64), RLX_AGENT), d1 = __hip_atomic_load((const gu32*)(ws + G_WS_DONE + 2048 + lane * 64), RLX_AGENT);
            if (!__any((d0 != e + 1u) | (d1 != e + 1u))) break;
            __builtin_amdgcn_s_sleep(8);
        }
    }
    __syncthreads();
    if (w == 0 && lane == 0) __hip_atomic_fetch_add((gu32*)(ws + G_WS_CTL + G_C_ARR), 1u, RLX_AGENT);
}
__device__ __forceinline__ void fill_block(char* ws, int b, int tid, const u32x4& v0, const u32x4& v1) {
    const unsigned o = b * 8192 + tid * 16, o2 = o + 4096;
    asm volatile("global_store_dwordx4 %0, %1, %2 sc1\n\ts_nop 1" :: "v"(o), "v"(v0), "s"(ws) : "memory");
    if (b >= 256 || (b & 6) != 6 || tid < 64)
        asm volatile("global_store_dwordx4 %0, %1, %2 sc1\n\ts_nop 1" :: "v"(o2), "v"(v1), "s"(ws) : "memory");
}
__device__ __forceinline__ void init_fill(char* ws, const u16* mem0, int f, int tid) {
    const u32x4 sent = {0xffffffffu, 0xffffffffu, 0xffffffffu, 0xffffffffu};
    u32x4 v0 = sent, v1 = sent;
    if (f < 32 && (f & 4)) {
#pragma unroll
        for (int k = 0; k < 2; ++k) {
            const int q = tid + 256 * k + ((f & 2) ? 256 : 0), m = q & 15;
            if (!(f & 2) || (q >= 512 && q < 576)) {
                const unsigned* src = (const unsigned*)(mem0 + (2 * (m >> 2) + (m & 1)) * DM + 16 * (q >> 4) + (m & 2));
                const u32x4 o = {min(src[0], 0xfffffffeu), min(src[2], 0xfffffffeu), min(src[4], 0xfffffffeu), min(src[6], 0xfffffffeu)};
                if (k) v1 = o; else v0 = o;
            }
        }
    }
#pragma unroll 1
    for (int q = f; q < 184; q += 48) {
        if (q >= 136 && q < 152 && (tid & 127)) { v0 = u32x4{0u, 0u, 0u, 0u}; v1 = v0; }
        fill_block(ws, q < 64 ? q : q < 136 ? q - 64 + G_D_UM / 8192 : q < 168 ? 256 + ((q - 136) >> 2) * 8 + (q & 3) : q < 178 ? q - 168 + G_WS_CTL / 8192 : q - 178 + G_WS_FIX / 8192, tid, v0, v1);
        v0 = sent; v1 = sent;
    }
    const unsigned e = __hip_atomic_load((const gu32*)(ws + G_WS_EP), RLX_AGENT);
    asm volatile("s_waitcnt vmcnt(0)" :: "v"(e) : "memory");
    __syncthreads();
    if (tid == 0) __hip_atomic_store((gu64*)(ws + G_WS_DONE + (48 + f) * 64), (u64)(e + 1u), RLX_AGENT);
    if (tid == 0) IST(0, 48 + f);
}
__device__ __forceinline__ void init_cal(char* ws, int id, int tid, int n) {
    if (tid < 64) {
        const int pg = id >> 1;
        char* a = ws + (pg < 2 ? pg * 0x200000u : G_D_UM + (pg - 2) * 0x200000u) + G_PROBE + (id & 1) * 8192;
        const u32x4 z = {0u, 0u, 0u, 0u};
        asm volatile("global_store_dwordx4 %0, %1, off sc1\n\tglobal_store_dwordx4 %0, %1, off offset:1024 sc1\n\ts_waitcnt vmcnt(0)" :: "v"(a + tid * 16), "v"(z) : "memory");
        if (tid == 0) {
            unsigned best = 0xffffu;
            const unsigned ts1 = (unsigned)__builtin_amdgcn_s_memrealtime() << 4;
#pragma unroll 1
            for (int it = 0; it < n; ++it) {
                const unsigned c0 = (unsigned)__builtin_amdgcn_s_memtime();
                const unsigned v = __hip_atomic_load((const gu32*)(a + it * 128), RLX_AGENT);
                asm volatile("s_waitcnt vmcnt(0)" :: "v"(v) : "memory");
                best = min(best, (unsigned)__builtin_amdgcn_s_memtime() - c0);
            }
            init_done(ws, id, best);
            IST(0, id);
        }
    }
    __syncthreads();
}
__device__ __forceinline__ unsigned near_mask(char* ws, int lane) {
    const gu32* q = (const gu32*)(ws + G_WS_DONE + (lane & 31) * 128 + 4);
    return (unsigned)__ballot(__hip_atomic_load(q + 16, RLX_AGENT) < __hip_atomic_load(q, RLX_AGENT));
}

#define TST(slot_, i_)
#define PIN8(a) asm volatile("" : "+v"(a[0]), "+v"(a[1]), "+v"(a[2]), "+v"(a[3]), "+v"(a[4]), "+v"(a[5]), "+v"(a[6]), "+v"(a[7]))
struct AOps { u32x4 q[4], k[8]; int fl; };
struct APtr { const char* q; const char* k; const char* m; unsigned voff, vkm, voffm, vfl; };
__device__ __forceinline__ void attn_batch(const MainP& p, AOps& o, const APtr& a) {
    const char* q2 = a.q + 4096; const char* k2 = a.k + 4096;
    WLD16(o.q[0], a.voff, a.q, 0); WLD16(o.q[1], a.voff, a.q, 2048); WLD16(o.q[2], a.voff, q2, 0); WLD16(o.q[3], a.voff, q2, 2048);
    WLD16X4(o.k[0], o.k[1], o.k[2], o.k[3], a.voff, a.k); WLD16X4(o.k[4], o.k[5], o.k[6], o.k[7], a.voff, k2);
    asm volatile("global_load_dword %0, %1, %2 sc1" : "=v"(o.fl) : "v"(a.vfl), "s"(WC(p)) : "memory");
}
__device__ __forceinline__ void attn_pin(AOps& o) {
    asm volatile("" : "+v"(o.fl), "+v"(o.q[0]), "+v"(o.q[1]), "+v"(o.q[2]), "+v"(o.q[3])); PIN8(o.k);
}
static constexpr int NFINE = 6;
static constexpr int NG1 = 64 - NFINE;
__device__ __forceinline__ void score_round4(float* r, const f32x4& x) {
    asm volatile("v_cvt_pk_bf16_f32 %0, 0, %4\n\tv_cvt_pk_bf16_f32 %1, 0, %5\n\tv_cvt_pk_bf16_f32 %2, 0, %6\n\tv_cvt_pk_bf16_f32 %3, 0, %7\n\t"
                 "v_mul_f32 %0, 0x3db504f3, %0\n\tv_mul_f32 %1, 0x3db504f3, %1\n\tv_mul_f32 %2, 0x3db504f3, %2\n\tv_mul_f32 %3, 0x3db504f3, %3\n\t"
                 "v_cvt_pk_bf16_f32 %0, 0, %0\n\tv_cvt_pk_bf16_f32 %1, 0, %1\n\tv_cvt_pk_bf16_f32 %2, 0, %2\n\tv_cvt_pk_bf16_f32 %3, 0, %3"
                 : "=&v"(r[0]), "=&v"(r[1]), "=&v"(r[2]), "=&v"(r[3]) : "v"(x[0]), "v"(x[1]), "v"(x[2]), "v"(x[3]));
}
__device__ __forceinline__ float vmax3(float a, float b, float c) { float r; asm("v_max3_f32 %0, %1, %2, %3" : "=v"(r) : "v"(a), "v"(b), "v"(c)); return r; }
template <int N>
__device__ __forceinline__ void attn_soft(float (&q)[12], u32x2& Pm, u32x4& Px) {
    float m = vmax3(q[0], q[1], q[2]);
#pragma unroll
    for (int i = 3; i < N; i += 2) m = vmax3(m, q[i], q[i + 1 < N ? i + 1 : i]);
    { auto a = __builtin_amdgcn_permlane16_swap(__float_as_uint(m), __float_as_uint(m), false, false); m = vmax3(m, __uint_as_float(a[0]), __uint_as_float(a[1])); }
    { auto a = __builtin_amdgcn_permlane32_swap(__float_as_uint(m), __float_as_uint(m), false, false); m = vmax3(m, __uint_as_float(a[0]), __uint_as_float(a[1])); }
    const float ml = -m * 1.4426950408889634f;
#pragma unroll
    for (int i = 0; i < N; ++i) q[i] = __builtin_amdgcn_exp2f(fmaf(q[i], 1.4426950408889634f, ml));
    float sum = 0.5f * ((q[0] + q[1]) + (q[2] + q[3])) + ((q[4] + q[5]) + (q[6] + q[7]));
    if (N > 8) sum += (q[8] + q[9]) + (q[10] + q[11]);
    sum = rowsum4(sum);
    const float inv = __builtin_amdgcn_rcpf(sum);
    Pm = u32x2{pk2bf(q[0] * inv, q[1] * inv), pk2bf(q[2] * inv, q[3] * inv)};
    Px = u32x4{pk2bf(q[4] * inv, q[5] * inv), pk2bf(q[6] * inv, q[7] * inv), N > 8 ? pk2bf(q[8] * inv, q[9] * inv) : 0u, N > 8 ? pk2bf(q[10] * inv, q[11] * inv) : 0u};
}
#define NASPLIT 12
#define HSL 1
#define NAG 2
static constexpr int NA = NASPLIT;
__device__ __forceinline__ int attn_next(int s) { return s < NA ? s + 2 : s + NAG; }
__device__ __forceinline__ unsigned attn_flag(int s, int hq) { return G_WS_CTL + G_C_PC + (s < NA ? s & 1 : (s - NA) % NAG) * 1024 + hq * 128; }
struct BOps { u32x4 q[8], k[8]; };
#define WLD16X4A(d0, d1, d2, d3, voff_, base_) asm volatile("s_nop 4\n\tglobal_load_dwordx4 %0, %4, %5 sc1\n\tglobal_load_dwordx4 %1, %4, %5 offset:1024 sc1\n\t" \
    "global_load_dwordx4 %2, %4, %5 offset:2048 sc1\n\tglobal_load_dwordx4 %3, %4, %5 offset:3072 sc1" \
    : "=&a"(d0), "=&a"(d1), "=&a"(d2), "=&a"(d3) : "v"(voff_), "s"(base_) : "memory")
#define PIN8A(a) asm volatile("" : "+a"(a[0]), "+a"(a[1]), "+a"(a[2]), "+a"(a[3]), "+a"(a[4]), "+a"(a[5]), "+a"(a[6]), "+a"(a[7]))
#define MFMA_AV0(acc, a_, b_) asm volatile("v_mfma_f32_16x16x32_bf16 %0, %1, %2, 0" : "=&v"(acc) : "a"(a_), "v"(b_))
#define MFMA_AV(acc, a_, b_) asm volatile("v_mfma_f32_16x16x32_bf16 %0, %1, %2, %0" : "+v"(acc) : "a"(a_), "v"(b_))
__device__ __forceinline__ void attn_batch8(const MainP& p, BOps& o, const APtr& a) {
    const char* q2 = a.q + 4096; const char* k2 = a.k + 4096;
    WLD16X4(o.q[0], o.q[1], o.q[2], o.q[3], a.voff, a.q); WLD16X4(o.q[4], o.q[5], o.q[6], o.q[7], a.voff, q2);
    WLD16X4A(o.k[0], o.k[1], o.k[2], o.k[3], a.voff, a.k); WLD16X4A(o.k[4], o.k[5], o.k[6], o.k[7], a.voff, k2);
}
__device__ __forceinline__ void attn_step(const MainP& p, BOps& o, BOps& n1, BOps& n2, APtr& a, int& kg, int& kk, int s, int h, int w, int lane) {
    const int g = lane >> 4, c = lane & 15;
    const float scale = 0.08838834764831845f;
    TST(s * 64 + h * 2, 0);
    const int s2 = min(attn_next(attn_next(s)), NSEG - 1);
    if (kg < s2 || kk < s) {
        VMC0();
        if (s < 50) wait_word(p, WC(p) + a.vfl, (lane & 1) ? s : s2, 13u); else wait_word<true>(p, WC(p) + a.vfl, (lane & 1) ? s : s2, 13u);
    }
    TST(s * 64 + h * 2, 1);
    a.q = p.ws + G_D_QX + (size_t)h * G_HS8 + skew(s2, h) * 8192; a.k = a.q + (G_D_KX - G_D_QX); a.m = p.ws + G_D_KM + (size_t)h * G_HS4 + skew(s, h) * 4096;
    __builtin_amdgcn_sched_barrier(0);
    u32x4 km[4];
    int fl;
    WLD16X4A(km[0], km[1], km[2], km[3], a.vkm, a.m);
    asm volatile("global_load_dword %0, %1, %2 sc1" : "=v"(fl) : "v"(a.vfl), "s"(WC(p)) : "memory");
    attn_batch8(p, n2, a);
    f32x4 y[5];
#pragma unroll
    for (int wc = 0; wc < 4; ++wc)
#pragma unroll
        for (int i = 0; i < 3; ++i) { if (wc == 0) MFMA_AV0(y[i], o.k[i >> 1], o.q[(i + 1) >> 1]); else MFMA_AV(y[i], o.k[wc * 2 + (i >> 1)], o.q[wc * 2 + ((i + 1) >> 1)]); }
    asm volatile("s_nop 7" : "+v"(y[0]), "+v"(y[1]), "+v"(y[2]));
    float q0[12], q1[12];
    score_round4(q0 + 4, y[0]); score_round4(q1 + 4, y[1]); score_round4(q1 + 8, y[2]);
#pragma unroll
    for (int i = 0; i < 4; ++i) {
        const bool vis = 4 * g + i <= c;
        q0[4 + i] = vis ? q0[4 + i] : -INFINITY; q1[8 + i] = vis ? q1[8 + i] : -INFINITY;
    }
    __builtin_amdgcn_sched_barrier(0);
    asm volatile("s_waitcnt vmcnt(16)\n\ts_barrier" : "+a"(km[0]), "+a"(km[1]), "+a"(km[2]), "+a"(km[3]), "+v"(fl) :: "memory");
    PIN8(n1.q); PIN8A(n1.k);
    { const unsigned fo = attn_flag(s, h >> 2); const int fv = s - 1;
      asm volatile("s_cmp_lg_u32 %3, 0\n\ts_cbranch_scc1 1f\n\tglobal_store_dword %0, %1, %2 sc1\n1:" :: "v"(fo), "v"(fv), "s"(WC(p)), "s"(w) : "scc", "memory"); }
#pragma unroll
    for (int wc = 0; wc < 4; ++wc)
#pragma unroll
        for (int i = 0; i < 2; ++i) { if (wc == 0) MFMA_AV0(y[3 + i], km[0], o.q[i]); else MFMA_AV(y[3 + i], km[wc], o.q[wc * 2 + i]); }
    asm volatile("s_nop 7" : "+v"(y[3]), "+v"(y[4]));
    kg = __builtin_amdgcn_readlane(fl, 0); kk = __builtin_amdgcn_readlane(fl, 1);
    score_round4(q0, y[3]); score_round4(q1, y[4]);
    u32x2 Pm0, Pm1; u32x4 Px0, Px1;
    attn_soft<8>(q0, Pm0, Px0); attn_soft<12>(q1, Pm1, Px1);
    char* pxb = p.ws + G_D_PX + (size_t)(s * 32 + h) * 2048;
    char* pmb = p.ws + G_D_PM + (size_t)s * 16384 + h * 256; char* pm2 = pmb + 8192;
    WST16(a.voff, Px0, pxb, 0); WST16(a.voff, Px1, pxb, 1024);
    asm volatile("global_store_dwordx2 %0, %1, %3 sc1\n\tglobal_store_dwordx2 %0, %2, %4 sc1" :: "v"(a.voffm), "v"(Pm0), "v"(Pm1), "s"(pmb), "s"(pm2) : "memory");
    { const unsigned fo = attn_flag(s, h >> 2);
      asm volatile("s_cmp_lt_u32 %4, 46\n\ts_cbranch_scc1 1f\n\ts_waitcnt vmcnt(0)\n\ts_barrier\n\ts_cmp_lg_u32 %3, 0\n\ts_cbranch_scc1 1f\n\tglobal_store_dword %0, %1, %2 sc1\n1:"
                   :: "v"(fo), "v"(s), "s"(WC(p)), "s"(w), "s"(s) : "scc", "memory"); }
    TST(s * 64 + h * 2, 2);
}
__device__ __forceinline__ void attn_role(const MainP& p, int s, int h, int w, int lane) {
    APtr a;
    a.q = p.ws + G_D_QX + (size_t)h * G_HS8 + skew(s, h) * 8192; a.k = a.q + (G_D_KX - G_D_QX);
    a.voff = lane * 16; a.vkm = (lane & 55) * 16; a.voffm = (lane & 15) * 16 + ((lane >> 4) & 1) * 8;
    a.vfl = G_WS_CTL + ((lane & 1) ? G_C_KM : G_C_GQ) + h * 128;
    BOps o0, o1, o2;
    wait_word(p, WC(p) + G_WS_CTL + G_C_GQ + h * 128, attn_next(s), 10u);
    attn_batch8(p, o0, a);
    a.q = p.ws + G_D_QX + (size_t)h * G_HS8 + skew(attn_next(s), h) * 8192; a.k = a.q + (G_D_KX - G_D_QX);
    attn_batch8(p, o1, a);
    VMC0();
    PIN8(o0.q); PIN8A(o0.k); PIN8(o1.q); PIN8A(o1.k);
    int kg = -1, kk = -1;
    static_assert(NA % 2 == 0, "ranges");
#pragma unroll 1
    for (;;) {
        attn_step(p, o0, o1, o2, a, kg, kk, s, h, w, lane);
        if ((s = attn_next(s)) >= NG1) break;
        attn_step(p, o1, o2, o0, a, kg, kk, s, h, w, lane);
        if ((s = attn_next(s)) >= NG1) break;
        attn_step(p, o2, o0, o1, a, kg, kk, s, h, w, lane);
        if ((s = attn_next(s)) >= NG1) break;
    }
    VMC0();
}
__device__ __forceinline__ unsigned umax4(const u32x4& v) { return max(max(v[0], v[1]), max(v[2], v[3])); }
__device__ __forceinline__ bool give_up(const MainP& p, unsigned& tries, unsigned code) {
    if (++tries <= kWaitCap / 4) return false;
    __hip_atomic_store((gu32*)(p.ws + G_WS_CTL + G_C_ERR), code, RLX_AGENT);
    return true;
}
__device__ __forceinline__ void task_attn_tail(const MainP& p, int s, int h, int w, int lane) {
    const int g = lane >> 4, c = lane & 15, qt = w & 1;
    const float scale = 0.08838834764831845f;
    APtr a;
    a.q = p.ws + G_D_QX + (size_t)h * G_HS8 + skew(s, h) * 8192 + qt * 1024; a.k = p.ws + G_D_KX + (size_t)h * G_HS8 + skew(s, h) * 8192; a.m = p.ws + G_D_KM + (size_t)h * G_HS4 + skew(s, h) * 4096;
    a.voff = lane * 16; a.vkm = (lane & 55) * 16; a.voffm = c * 16 + (g & 1) * 8; a.vfl = G_WS_CTL;
    AOps o;
    wait_word(p, WC(p) + G_WS_CTL + G_C_GQ + h * 128, s, 14u);
    attn_batch(p, o, a);
    VMC0();
    attn_pin(o);
    float q[12];
    u32x4 km[4];
#pragma unroll
    for (int kt = 1; kt >= -1; --kt) {
        if (kt < 0) {
            TST(s * 64 + h * 2 + qt, 0);
            wait_word(p, WC(p) + G_WS_CTL + G_C_KM + h * 128, s - 2, 15u);
            if (FLG(p.prm[5]) & 2) wait_word<true>(p, WC(p) + G_WS_CTL + G_C_KM + h * 128, s - 1, 15u);
            for (int i = 0; i < ((FLH(p.prm[7]) >> 14) & 15); ++i) __builtin_amdgcn_s_sleep(1);
            for (unsigned tries = 0;;) {
                WLD16X4(km[0], km[1], km[2], km[3], a.vkm, a.m);
                asm volatile("s_waitcnt vmcnt(0)" : "+v"(km[0]), "+v"(km[1]), "+v"(km[2]), "+v"(km[3]) :: "memory");
                if (!__any(max(max(umax4(km[0]), umax4(km[1])), max(umax4(km[2]), umax4(km[3]))) == 0xffffffffu) || give_up(p, tries, 16u)) break;
            }
            TST(s * 64 + h * 2 + qt, 1);
        }
        f32x4 y = {0.f, 0.f, 0.f, 0.f};
#pragma unroll
        for (int wc = 0; wc < 4; ++wc) y = MFMA32(kt < 0 ? km[wc] : o.k[wc * 2 + (kt < 0 ? 0 : kt)], o.q[wc], y);
#pragma unroll
        for (int i = 0; i < 4; ++i) q[4 + 4 * kt + i] = (16 * kt + 4 * g + i <= 16 * qt + c) ? bfr(bfr(y[i]) * scale) : -INFINITY;
    }
    u32x2 Pm; u32x4 Px;
    attn_soft<12>(q, Pm, Px);
    char* pxb = p.ws + G_D_PX + (size_t)(s * 32 + h) * 2048 + qt * 1024;
    char* pmb = p.ws + G_D_PM + (size_t)s * 16384 + h * 256 + qt * 8192;
    WST16(a.voff, Px, pxb, 0);
    asm volatile("global_store_dwordx2 %0, %1, %2 sc1" :: "v"(a.voffm), "v"(Pm), "s"(pmb) : "memory");
    TST(s * 64 + h * 2 + qt, 2);
}

#define RLD(d_, voff_, base_, off_) asm volatile("global_load_dwordx4 %0, %1, %2 offset:%3 sc1" : "=v"(d_) : "v"(voff_), "s"(base_), "i"(off_) : "memory")
#define RLDA(d_, voff_, base_, off_) asm volatile("global_load_dwordx4 %0, %1, %2 offset:%3 sc1" : "=a"(d_) : "v"(voff_), "s"(base_), "i"(off_) : "memory")
#define MFMA_ACC_A(acc, a_, b_) asm volatile("v_mfma_f32_16x16x32_bf16 %0, %1, %2, %0" : "+v"(acc) : "a"(a_), "v"(b_))
#define VMW(n_) asm volatile("s_waitcnt vmcnt(%0)" :: "i"(n_) : "memory")
template <int NT, int DEPTH, bool TAIL, int NM = 2>
__device__ __forceinline__ void task_out(const MainP& p, char* smem, int s, int ct0, int w, int lane, int m0 = 0) {
    const int g = lane >> 4, c = lane & 15;
    const unsigned voff = lane * 16;
    f32x4 acc[NT][2];
#pragma unroll
    for (int t = 0; t < NT; ++t) { acc[t][0] = f32x4{0.f, 0.f, 0.f, 0.f}; acc[t][1] = acc[t][0]; asm volatile("" : "+v"(acc[t][0]), "+v"(acc[t][1])); }
    const char* ub[NT];
#pragma unroll
    for (int t = 0; t < NT; ++t) ub[t] = uni(p.ws + G_D_UX + (size_t)(ct0 + t) * G_UXS + skew(s, ct0 + t) * 32768 + w * 8192);
    const char* pb = uni(p.ws + G_D_PX + (size_t)(s * 32 + w * 8) * 2048 + m0 * 1024);
    const char* um = uni(p.ws + um_base(s, p.prm[0]) + ct0 * 16384);
    unsigned vu[NT];
#pragma unroll
    for (int t = 0; t < NT; ++t) { vu[t] = t * 16384 + w * 2048 + voff; asm volatile("" : "+v"(vu[t])); }
    const char* pm = uni(p.ws + G_D_PM + (size_t)s * 16384 + w * 2048 + m0 * 8192);
    constexpr int NR = DEPTH + 1 > 10 ? 10 : DEPTH + 1;
    u32x4 a[NR][NT], b[10][2];
    unsigned vb = voff, va = voff;
    TST(4096 + (s * 36 + ct0) * 4 + w, 0);
    const int f0 = (int)__hip_atomic_load((const gu32*)(WC(p) + G_WS_CTL + (lane < 8 ? G_C_UM + (w * 8 + lane) * 128 : lane < 16 ? G_C_GV + ((s < (int)G_S1 ? s & 1 : 0) * 32 + w * 8 + lane - 8) * 128
                                                                                                                                : attn_flag(s, w * 2 + (lane & 1)) - G_WS_CTL)), RLX_AGENT);
    const bool early = !TAIL && !__any(f0 < s);
    if (!early) wait_word(p, WC(p) + G_WS_CTL + (lane < 8 ? G_C_GV + ((s < (int)G_S1 ? s & 1 : 0) * 32 + w * 8 + lane) * 128 : G_C_UM + w * 8 * 128), lane < 8 ? s : s - 3, 21u);
#define ALOAD(q) do { if ((q) < 8) { _Pragma("unroll") for (int t = 0; t < NT; ++t) RLDA(a[(q) % NR][t], va, ub[t], 0);  \
                                     asm volatile("v_add_u32 %0, 0x400, %0" : "+v"(va)); }                                            \
                      else { _Pragma("unroll") for (int t = 0; t < NT; ++t) RLDA(a[(q) % NR][t], vu[t], um, ((q) & 1) * 1024); } } while (0)
    constexpr int D0 = DEPTH < 8 ? DEPTH : 8;
    asm volatile("s_nop 4");
#pragma unroll
    for (int q = 0; q < D0; ++q) ALOAD(q);
    asm volatile("s_cmp_lg_u32 %0, 0\n\ts_cbranch_scc1 1f\n\ts_waitcnt vmcnt(0)\n1:\n\ts_waitcnt vmcnt(30)" :: "s"(__builtin_amdgcn_readfirstlane((int)early)) : "scc", "memory");
    unsigned tries = 0;
    if (TAIL) {
        const char* sp = pm + (lane & 7) * 256 + ((lane >> 3) & 1) * 8192;
        const int dl = (FLG(p.prm[5]) >> 26) & 31;
        if (dl) {
            wait_word<true>(p, WC(p) + G_WS_CTL + G_C_KM + ((ct0 >> 1) + w) * 128, s - 1, 25u);
            for (int i = 0; i < dl + (s < 63 ? 5 : 0); ++i) __builtin_amdgcn_s_sleep(2);
        } else
        while (__any(__hip_atomic_load((const gu32*)sp, RLX_AGENT) == 0xffffffffu) && !give_up(p, tries, 23u)) __builtin_amdgcn_s_sleep(1);
    } else if (!early) wait_word(p, WC(p) + G_WS_CTL + (lane < 8 ? G_C_UM + (w * 8 + lane) * 128 : attn_flag(s, w * 2 + (lane & 1)) - G_WS_CTL), s, 22u);
    if (!early) TST(4096 + (s * 36 + ct0) * 4 + w, 1);
    for (;;) {
    asm volatile("s_nop 4");
#pragma unroll
    for (int q = 0; q < 8; ++q) { RLD(b[q][0], vb, pb, 0); if (NM == 2) RLD(b[q][1], vb, pb, 1024); asm volatile("v_add_u32 %0, 0x800, %0" : "+v"(vb)); }
#pragma unroll
    for (int q = 8; q < 10; ++q) { RLD(b[q][0], voff, pm, (q & 1) * 1024); if (NM == 2) RLD(b[q][1], voff, pm + 8192, (q & 1) * 1024); }
#pragma unroll
    for (int q = D0; q < NR; ++q) ALOAD(q);
#pragma unroll
    for (int q = 0; q < 10; ++q) {
        const int issuedA = (NR + q < 10 ? NR + q : 10) - D0;
        if (q < D0) VMW(NM * (9 - q) + issuedA * NT); else VMW((issuedA - (q - D0 + 1)) * NT);
#pragma unroll
        for (int t = 0; t < NT; ++t) { MFMA_ACC_A(acc[t][0], a[q % NR][t], b[q][0]); if (NM == 2) MFMA_ACC_A(acc[t][1], a[q % NR][t], b[q][1]); }
        if (NR + q < 10) ALOAD(NR + q);
    }
    asm volatile("s_nop 7" ::: "memory");
#pragma unroll
    for (int t = 0; t < NT; ++t) asm volatile("" : "+v"(acc[t][0]), "+v"(acc[t][1]));
    if (!TAIL) break;
    f32x4 chk = acc[0][0];
#pragma unroll
    for (int t = 0; t < NT; ++t) chk += acc[t][1] + acc[t][0];
    const float ck = (chk[0] + chk[1]) + (chk[2] + chk[3]);
    if (!__any(ck != ck)) break;
    unsigned mx = 0;
#pragma unroll
    for (int q = 0; q < 10; ++q) { asm volatile("" : "+v"(b[q][0])); mx = max(mx, umax4(b[q][0])); if (NM == 2) { asm volatile("" : "+v"(b[q][1])); mx = max(mx, umax4(b[q][1])); } }
#pragma unroll
    for (int t = 0; t < NT; ++t) { asm volatile("" : "+a"(a[8 % NR][t]), "+a"(a[9 % NR][t])); mx = max(mx, max(umax4(a[8 % NR][t]), umax4(a[9 % NR][t]))); }
    if (!__any(mx == 0xffffffffu) || give_up(p, tries, 24u)) break;
    vb = voff;
#pragma unroll
    for (int t = 0; t < NT; ++t) { acc[t][0] = f32x4{0.f, 0.f, 0.f, 0.f}; acc[t][1] = acc[t][0]; asm volatile("" : "+v"(acc[t][0]), "+v"(acc[t][1])); }
    }
    TST(4096 + (s * 36 + ct0) * 4 + w, 2);
    f32x4* red = (f32x4*)smem;
#pragma unroll
    for (int t = 0; t < NT; ++t) { red[((w * NT + t) * 2 + 0) * 64 + lane] = acc[t][0]; if (NM == 2) red[((w * NT + t) * 2 + 1) * 64 + lane] = acc[t][1]; }
    __syncthreads();
    const int row = lane >> 2, pc = lane & 3;
#pragma unroll
    for (int it = 0; it < NT / 2 * NM; ++it)
        if ((it & 3) == w) {
            const int m = it % NM, t = 2 * (it / NM) + (pc >> 1);
            const f32x4* r0 = red + (t * 2 + m) * 64 + (pc & 1) * 32 + row;
            const f32x4 v0 = (r0[0] + r0[NT * 128]) + (r0[NT * 256] + r0[NT * 384]), v1 = (r0[16] + r0[NT * 128 + 16]) + (r0[NT * 256 + 16] + r0[NT * 384 + 16]);
            u16* o = p.out + (size_t)(32 * s + 16 * (m + m0) + row) * OUTW + 16 * ct0 + 32 * (it / NM) + 8 * pc;
            if (TAIL || s >= OWT) OST16W(o, pack8(v0, v1)); else OST16(o, pack8(v0, v1));
        }
}

__device__ __forceinline__ int index_of_pos(int pv) { return min(min(pv, 128 + (pv >> 1)), min(256 + (pv >> 2), 384 + (pv >> 3))); }
static constexpr int KROW = G_KROW;
static constexpr int KARR = 80 * KROW;
static constexpr int UCS = 2208;
struct BCh {
    u16 uc[4][UCS];
    f32x4 cst[NST];
    unsigned Kd[648];
    u16 Kh[KARR];
    u16 Kl[KARR];
};
static_assert(4 * sizeof(BCh) <= LDS_TOTAL - 32, "LDS");
__device__ __forceinline__ unsigned prm(unsigned hi, unsigned lo, unsigned sel) { return __builtin_amdgcn_perm(hi, lo, sel); }
#define BST(i) asm volatile("" ::: "memory")
#define UT_LOAD(p_) __builtin_nontemporal_load(p_)
template <class F>
__device__ __forceinline__ void task_ut(const MainP& p, char* smem, int rb, int w, int lane, F more) {
    const int tid = w * 64 + lane, g = lane >> 4, c = lane & 15;
    { char* ws = p.ws; if (tid == 0) IST(4, rb); }
    const u32x4* src = (const u32x4*)(p.U + (size_t)rb * 64 * DM);
    u32x4 v[18];
#pragma unroll
    for (int i = 0; i < 18; ++i) v[i] = UT_LOAD(src + tid + 256 * i);
#pragma unroll
    for (int i = 0; i < 18; ++i) { const int ch = tid + 256 * i, row = ch / 72; *(u32x4*)(smem + row * 1184 + (ch - 72 * row) * 16) = v[i]; }
    __syncthreads();
    { char* ws = p.ws; if (tid == 0) IST(3, rb); }
    const unsigned ad = (unsigned)(uintptr_t)smem + (16 * g + (c >> 2)) * 1184 + (w * 144 + 4 * (c & 3)) * 2;
    char* tw = smem + 77824 + w * 20736;
#pragma unroll
    for (int i = 0; i < 9; ++i) {
        u32x2 x0, x1, x2, x3;
        asm volatile("ds_read_b64_tr_b16 %0, %4 offset:%5\n\tds_read_b64_tr_b16 %1, %4 offset:%6\n\tds_read_b64_tr_b16 %2, %4 offset:%7\n\tds_read_b64_tr_b16 %3, %4 offset:%8\n\ts_waitcnt lgkmcnt(0)"
                     : "=&v"(x0), "=&v"(x1), "=&v"(x2), "=&v"(x3) : "v"(ad), "i"(i * 32), "i"(4 * 1184 + i * 32), "i"(8 * 1184 + i * 32), "i"(12 * 1184 + i * 32) : "memory");
        u32x4* o = (u32x4*)(tw + (16 * i + c) * 144 + g * 32);
        o[0] = u32x4{x0[0], x0[1], x1[0], x1[1]}; o[1] = u32x4{x2[0], x2[1], x3[0], x3[1]};
    }
    asm volatile("" ::: "memory");
    const unsigned vo = (w * 144 + (lane >> 3)) * (SL * 2) + rb * 128 + (lane & 7) * 16;
    const char* dst = p.ws + G_D_UT;
#pragma unroll
    for (int i = 0; i < 18; ++i) {
        const u32x4 a = *(const u32x4*)(tw + (8 * i + (lane >> 3)) * 144 + (lane & 7) * 16);
        const unsigned vi = vo + i * 8 * (SL * 2);
        WST16(vi, a, dst, 0);
    }
    VMC0();
    init_wait(p.ws, w, lane);
    if (tid == 0) __hip_atomic_store((gu32*)(p.ws + G_WS_CTL + G_C_UT + rb * 128), 1u, RLX_AGENT);
    { char* ws = p.ws; if (tid == 0) IST(2, rb); }
    more();
    VMC0();
    __syncthreads();
    if (tid == 0) __hip_atomic_store((gu32*)(p.ws + G_WS_CTL + G_C_UT + rb * 128), 2u, RLX_AGENT);
}
struct KPar { u16 ldt, dsk, pa[2][4]; };
__device__ __forceinline__ KPar kgen_par(const MainP& p, int d, int lane) {
    KPar k;
    k.ldt = p.logdt[d]; k.dsk = p.Dskip[d];
#pragma unroll
    for (int q = 0; q < 2; ++q) { const int i = d * NST + lane + 64 * q; k.pa[q][0] = p.logA[i]; k.pa[q][1] = p.Aim[i]; k.pa[q][2] = p.Cre[i]; k.pa[q][3] = p.Cim[i]; }
    return k;
}
__device__ __forceinline__ void task_s4d(const MainP& p, BCh& L, int d, int lane, bool first, int& nb, KPar& kp, int& pend) {
    const int g = lane >> 4, c = lane & 15;
    BST(0);
    const float dsk = bf2f(kp.dsk);
    const auto& pa = kp.pa;
    const float dt = bfr(expf(bf2f(kp.ldt)));
#pragma unroll
    for (int q = 0; q < 2; ++q) {
        const float ar = -bfr(expf(bf2f(pa[q][0])));
        const float cr = bf2f(pa[q][2]) * dt, ci = bf2f(pa[q][3]) * dt;
        L.cst[lane + 64 * q] = f32x4{ar * dt * 1.4426950408889634f, bf2f(pa[q][1]) * dt * 0.15915494309189535f,
                                     __log2f(hypotf(cr, ci)), atan2f(ci, cr) * 0.15915494309189535f};
    }
    const char* ut = uni(p.ws + G_D_UT + (size_t)d * 4096);
    const unsigned voff = lane * 16;
    u32x4 uu[4];
    unsigned tk = nb >= 0 ? 1u : 0u;
    __builtin_amdgcn_sched_barrier(0);
    asm volatile("s_mov_b64 exec, 1\n\tglobal_atomic_add %0, %1, %0, %2 sc0\n\ts_mov_b64 exec, -1" : "+v"(tk) : "v"(G_WS_CTL + G_C_TASK + 128), "s"(WC(p)) : "memory");
    const char* ue = uni(first ? (const char*)p.logA : ut);
    WLD16X4A(uu[0], uu[1], uu[2], uu[3], voff, ue);
    BST(1);
    {
        f32x4 acc[3];
#pragma unroll
        for (int v = 0; v < 3; ++v) acc[v] = f32x4{0.f, 0.f, 0.f, 0.f};
        auto base_of = [](int v, int r) { return v == 0 ? (r & 7) + 128 * (r >> 3) : v == 1 ? (r < 8 ? 256 + 2 * (r & 3) + 128 * (r >> 2) : 512 + 4 * (r & 1) + 128 * ((r - 8) >> 1))
                                                                                           : 1024 + 128 * min(r, 8); };
        const float b0 = (float)base_of(0, c), b1 = (float)base_of(1, c), b2 = (float)base_of(2, c), off = (float)(8 * c);
#define KG_POLAR(h_, l_, lg_, f_, a_, b_) do { const float m_ = __builtin_amdgcn_exp2f(fmaf(cc[0], b_, lg_)), p_ = __builtin_amdgcn_fractf(fmaf(a_, b_, f_)); \
            const float re_ = m_ * __builtin_amdgcn_cosf(p_), im_ = m_ * __builtin_amdgcn_sinf(p_);                                                            \
            h_ = pk2bf(re_, im_); l_ = pk2bf(re_ - lo2f(h_), im_ - hi2f(h_)); } while (0)
#pragma unroll 1
        for (int t = 0; t < NST / 16; ++t) {
            u32x4 fh, fl, ah[3], al[3];
#pragma unroll
            for (int j = 0; j < 4; ++j) {
                const f32x4 cc = L.cst[16 * t + 4 * g + j];
                KG_POLAR(fh[j], fl[j], 0.f, 0.f, -cc[1], off);
                KG_POLAR(ah[0][j], al[0][j], cc[2], cc[3], cc[1], b0); KG_POLAR(ah[1][j], al[1][j], cc[2], cc[3], cc[1], b1); KG_POLAR(ah[2][j], al[2][j], cc[2], cc[3], cc[1], b2);
            }
#pragma unroll
            for (int v = 0; v < 3; ++v) { acc[v] = MFMA32(al[v], fh, acc[v]); acc[v] = MFMA32(ah[v], fl, acc[v]); acc[v] = MFMA32(ah[v], fh, acc[v]); }
        }
        BST(2);
        auto split = [](float k) { const unsigned hi = pk2bf(k, 0.f) & 0xffffu; return hi | (pk2bf(0.f, k - __uint_as_float(hi << 16)) & 0xffff0000u); };
#pragma unroll
        for (int v = 0; v < 3; ++v)
#pragma unroll
            for (int i = 0; i < 4; ++i) {
                const int pos = base_of(v, 4 * g + i) + 8 * c;
                if (pos <= 2048) L.Kd[index_of_pos(pos)] = split(2.0f * acc[v][i]);
            }
    }
    __builtin_amdgcn_sched_barrier(0);
    asm volatile("s_waitcnt vmcnt(0)" : "+v"(tk), "+a"(uu[0]), "+a"(uu[1]), "+a"(uu[2]), "+a"(uu[3]) :: "memory");
    if (first) {
        wait_word(p, p.ws + G_WS_CTL + G_C_UT + (lane & 31) * 128, 1, 30u);
        WLD16X4A(uu[0], uu[1], uu[2], uu[3], voff, ut);
        asm volatile("s_waitcnt vmcnt(0)" : "+a"(uu[0]), "+a"(uu[1]), "+a"(uu[2]), "+a"(uu[3]) :: "memory");
    }
    if (pend >= 0 && lane == 0) __hip_atomic_fetch_add((gu32*)(WC(p) + G_WS_CTL + G_C_YC + pend * 128), 1u, RLX_AGENT);
    nb = __builtin_amdgcn_readfirstlane(tk) + 1;
    const u32x4 z4 = {0u, 0u, 0u, 0u};
    *(u32x4*)&L.Kh[8 * lane] = z4; *(u32x4*)&L.Kl[8 * lane] = z4;
    *(u32x4*)&L.Kh[512 + 8 * (lane & 31)] = z4; *(u32x4*)&L.Kl[512 + 8 * (lane & 31)] = z4;
    *(u32x4*)&L.uc[0][64 + SL] = z4;
    *(u32x4*)&L.uc[(lane >> 3) & 3][8 * (lane & 7)] = z4;
    asm volatile("" ::: "memory");
#pragma unroll
    for (int q = 0; q < 4; ++q) {
        const int e = lane + 64 * q, at = (16 + (e >> 2)) * KROW + 24 - 8 * (e & 3);
        unsigned k[8];
        const unsigned* kd = L.Kd + index_of_pos(8 * e);
        if (q == 0) {
            const bool two = lane >= 32;
#pragma unroll
            for (int r = 0; r < 8; ++r) k[7 - r] = kd[two ? (0x43222100 >> (4 * r)) & 7 : r];
        } else if (q == 1) {
            const unsigned k0 = kd[0], k1 = kd[1], k2 = kd[2];
            k[7] = k[6] = k[5] = k0; k[4] = k[3] = k[2] = k1; k[1] = k[0] = k2;
        } else {
            const unsigned k0 = kd[0], k1 = kd[1];
            k[7] = k[6] = k[5] = k[4] = k0; k[3] = (e & 1) ? k1 : k0; k[2] = k[1] = k[0] = k1;
        }
        u32x4 kh, kl;
#pragma unroll
        for (int j = 0; j < 4; ++j) { kh[j] = prm(k[2 * j + 1], k[2 * j], 0x05040100u); kl[j] = prm(k[2 * j + 1], k[2 * j], 0x07060302u); }
        *(u32x4*)&L.Kh[at] = kh;
        if (G_NHL == 2) *(u32x4*)&L.Kl[at] = kl;
    }
    BST(3);
#pragma unroll
    for (int q = 0; q < 4; ++q) *(u32x4*)&L.uc[0][64 + 8 * (lane + 64 * q)] = uu[q];
    asm volatile("" ::: "memory");
#pragma unroll
    for (int q = 0; q < 4; ++q) {
        const int e = lane + 64 * q;
        const int ue = 56 + 8 * e;
        const u32x4 a = *(const u32x4*)&L.uc[0][ue];
        const u32x2 b = *(const u32x2*)&L.uc[0][ue + 8];
        const unsigned s0 = __builtin_amdgcn_alignbit(a[1], a[0], 16), s1 = __builtin_amdgcn_alignbit(a[2], a[1], 16), s2 = __builtin_amdgcn_alignbit(a[3], a[2], 16),
                       s3 = __builtin_amdgcn_alignbit(b[0], a[3], 16), s4 = __builtin_amdgcn_alignbit(b[1], b[0], 16);
        *(u32x4*)&L.uc[1][ue] = u32x4{s0, s1, s2, s3};
        *(u32x4*)&L.uc[2][ue] = u32x4{a[1], a[2], a[3], b[0]};
        *(u32x4*)&L.uc[3][ue] = u32x4{s1, s2, s3, s4};
    }
    if (lane == 0) {
        const u32x4 a = *(const u32x4*)&L.uc[0][56 + SL];
        const unsigned s0 = __builtin_amdgcn_alignbit(a[1], a[0], 16), s1 = __builtin_amdgcn_alignbit(a[2], a[1], 16), s2 = __builtin_amdgcn_alignbit(a[3], a[2], 16), s3 = a[3] >> 16;
        *(u32x4*)&L.uc[1][56 + SL] = u32x4{s0, s1, s2, s3};
        *(u32x4*)&L.uc[2][56 + SL] = u32x4{a[1], a[2], a[3], 0u};
        *(u32x4*)&L.uc[3][56 + SL] = u32x4{s1, s2, s3, 0u};
    }
    kp = kgen_par(p, min(nb, DM - 1), lane);
    BST(4);
    {
        const int o = 33 + c + 8 * g;
        const unsigned ua = (unsigned)(uintptr_t)&L.uc[o & 3][o & ~3], ka = (unsigned)(uintptr_t)&L.Kh[(16 + c) * KROW + 8 * g];
        const f32x4* res = (const f32x4*)L.Kh + lane;
        asm volatile(ASM_CV :: "v"(ua), "v"(ka), "v"((unsigned)(uintptr_t)res) : CV_CLOB);
        asm volatile("" :: "v"(kp.ldt), "v"(kp.dsk), "v"(kp.pa[0][0]), "v"(kp.pa[0][1]), "v"(kp.pa[0][2]), "v"(kp.pa[0][3]), "v"(kp.pa[1][0]), "v"(kp.pa[1][1]), "v"(kp.pa[1][2]), "v"(kp.pa[1][3]));
        BST(5);
#pragma unroll
        for (int par = 0; par < 2; ++par)
#pragma unroll
            for (int t = 0; t < 4; ++t) {
                const int n = 16 * (par + 2 * (16 * t + c)) + 4 * g;
                const u32x2 uu = *(const u32x2*)&L.uc[0][64 + n];
                const f32x4 y4 = res[(par * 4 + t) * 64];
                const float r0 = bfr(y4[0]) + bfr(lo2f(uu[0]) * dsk), r1 = bfr(y4[1]) + bfr(hi2f(uu[0]) * dsk);
                const float r2 = bfr(y4[2]) + bfr(lo2f(uu[1]) * dsk), r3 = bfr(y4[3]) + bfr(hi2f(uu[1]) * dsk);
                *(u32x2*)&L.uc[1][n] = u32x2{pk2bf(r0, r1), pk2bf(r2, r3)};
            }
    }
    BST(6);
    char* yb = uni(p.ws + G_D_YB + (size_t)d * 4096);
#pragma unroll
    for (int q = 0; q < 4; ++q) { const u32x4 o = *(const u32x4*)&L.uc[1][8 * (lane + 64 * q)]; WST16(voff, o, yb, q * 1024); }
    pend = d >> 6;
    BST(7);
}
__device__ __forceinline__ void s4d_wave(const MainP& p, char* smem, int w, int lane, int quota) {
    int nb = 0, pend = -1;
    if (lane == 0) nb = (int)(__hip_atomic_fetch_add((gu32*)(WC(p) + G_WS_CTL + G_C_TASK + 128), 1u, RLX_AGENT) + 1u);
    nb = __builtin_amdgcn_readfirstlane(nb);
    KPar kp = kgen_par(p, min(nb, DM - 1), lane);
#pragma unroll 1
    for (int n = 0; n < quota; ++n) {
        const int d = nb;
        if (d >= DM) break;
        nb = n + 1 < quota ? 0 : -1;
        task_s4d(p, reinterpret_cast<BCh*>(smem)[w], d, lane, n == 0, nb, kp, pend);
    }
    VMC0();
    if (pend >= 0 && lane == 0) __hip_atomic_fetch_add((gu32*)(WC(p) + G_WS_CTL + G_C_YC + pend * 128), 1u, RLX_AGENT);
}
__device__ __forceinline__ void watch_role(const MainP& p, int lane) {
    const char* c = WC(p) + G_WS_CTL;
#pragma unroll 1
    for (int s = G_S0; s < NSEG; ++s) {
        const int n = min(s + 3, NSEG - 1);
        wait_word(p, c + G_C_GV + lane * 128, n - (n < (int)G_S1 ? (n ^ (lane >> 5)) & 1 : 0), 60u);
        wait_word(p, (lane < 32 ? c + G_C_GX : p.ws + G_WS_CTL + G_C_UT - 4096) + lane * 128, lane < 32 ? min(s + 2, NSEG - 1) : s < 40 ? -1 : 2, 61u);
        if (lane == 0) __hip_atomic_store((gu32*)(p.ws + G_WS_CTL + G_C_RDY + s * 128), 1u, RLX_AGENT);
    }
}
__device__ __forceinline__ void task_merge(const MainP& p, char* smem, int gr, int i, int w, int lane) {
    const int g = lane >> 4, c = lane & 15;
    wait_word(p, WC(p) + G_WS_CTL + G_C_YC + gr * 128, 63, 31u);
    const char* sb = uni(p.ws + G_D_YB + (size_t)gr * 262144 + i * 1024 + w * 256);
    const unsigned vt = g * 4096 + c * 16;
    u32x4 z[16];
#pragma unroll
    for (int j = 0; j < 16; ++j) { const char* sj = sb + j * 16384; WLD16(z[j], vt, sj, 0); }
    asm volatile("s_waitcnt vmcnt(0)" : "+v"(z[0]), "+v"(z[1]), "+v"(z[2]), "+v"(z[3]), "+v"(z[4]), "+v"(z[5]), "+v"(z[6]), "+v"(z[7]),
                 "+v"(z[8]), "+v"(z[9]), "+v"(z[10]), "+v"(z[11]), "+v"(z[12]), "+v"(z[13]), "+v"(z[14]), "+v"(z[15]) :: "memory");
    char* tw = smem + w * 17408;
#pragma unroll
    for (int j = 0; j < 16; ++j) *(u32x4*)(tw + (4 * j + g) * 272 + c * 16) = z[j];
    const unsigned ad = (unsigned)(uintptr_t)tw + (8 * g + (c >> 2)) * 272 + (c & 3) * 8;
    u16* orow = p.out + (size_t)(512 * i + 128 * w + c) * OUTW + DM + 64 * gr + 8 * g;
#pragma unroll
    for (int k = 0; k < 8; ++k) {
        u32x2 x0, x1, x2, x3;
        asm volatile("ds_read_b64_tr_b16 %0, %4 offset:%5\n\tds_read_b64_tr_b16 %1, %4 offset:%6\n\tds_read_b64_tr_b16 %2, %4 offset:%7\n\tds_read_b64_tr_b16 %3, %4 offset:%8\n\ts_waitcnt lgkmcnt(0)"
                     : "=&v"(x0), "=&v"(x1), "=&v"(x2), "=&v"(x3) : "v"(ad), "i"(k * 32), "i"(4 * 272 + k * 32), "i"(32 * 272 + k * 32), "i"(36 * 272 + k * 32) : "memory");
        OST16(orow + (size_t)16 * k * OUTW, (u32x4{x0[0], x0[1], x1[0], x1[1]}));
        OST16(orow + (size_t)16 * k * OUTW + 32, (u32x4{x2[0], x2[1], x3[0], x3[1]}));
    }
}

static constexpr int Q1 = NG1 * 6 + 36, Q2 = Q1 + (NFINE - 2) * 25;
static constexpr int NTASK = Q2 + 34 + 52;
__host__ __device__ __forceinline__ void task_of(int u, int m0, int& sg, int& r) {
    if (u < 6 * m0) { sg = u / 6; r = u % 6; }
    else if (u < 6 * m0 + 108) { sg = m0 + (u - 6 * m0) / 9; r = (u - 6 * m0) % 9; }
    else if (u < Q1) { sg = (u - 36) / 6; r = (u - 36) % 6; }
    else if (u < Q2) { sg = NG1 + (u - Q1) / 25; r = (u - Q1) % 25; }
    else { sg = u < Q2 + 34 ? 62 : 63; r = u < Q2 + 34 ? u - Q2 : u - Q2 - 34; }
}
__device__ __forceinline__ void worker_body(const MainP& p, char* smem, const int w, const int lane) {
    const int tid = w * 64 + lane;
    int* slot = (int*)(smem + LDS_TOTAL - 16);
    int nst = 0, nxt = -1;
    gu32* qa = (gu32*)(WC(p) + G_WS_CTL + G_C_TASK);
    const int m0 = (FLH(p.prm[7]) >> 4) & 63;
    for (;;) {
        if (tid == 0) {
            *slot = nxt >= 0 ? nxt : (int)(__hip_atomic_fetch_add(qa, 1u, RLX_AGENT) + 1u);
            nxt = -1;
        }
        __syncthreads();
        const int u = __builtin_amdgcn_readfirstlane(*slot);
        if (u >= NTASK) break;
        if (tid == 0 && u < Q1 - 140) nxt = (int)(__hip_atomic_fetch_add(qa, 1u, RLX_AGENT) + 1u);
        const bool last2 = u >= Q2;
        int sg, r;
        task_of(u, m0, sg, r);
        if (u < Q1) {
            if (r < 6) task_out<6, 6, false>(p, smem, sg, 6 * r, w, lane);
            else { const int m = 3 * (sg - m0) + r - 6; task_merge(p, smem, m >> 2, m & 3, w, lane); }
        }
        else if (r < 16) task_attn_tail(p, sg, 2 * r + (w >> 1), w, lane);
        else if (sg == 63) task_out<2, 9, true, 1>(p, smem, sg, (r - 16) & ~1, w, lane, r & 1);
        else if (last2) task_out<2, 9, true>(p, smem, sg, 2 * (r - 16), w, lane);
        else task_out<4, 9, true>(p, smem, sg, 4 * (r - 16), w, lane);
        if (last2) break;
        __syncthreads();
    }
}

__host__ __device__ constexpr unsigned phys(unsigned x) { return (x >> 13) * 16384 + (x & 8191); }
__host__ __device__ constexpr unsigned aMB(int f, int b) { return phys(f < 8 ? 16384 + f * 1024 + b : 28672 + b); }
__host__ __device__ constexpr unsigned aPX(int pr, int b) { return phys(pr * 1024 + b); }
__host__ __device__ constexpr unsigned aPM(int u, int b) { return phys(24576 + u * 1024 + b); }
static constexpr int L_KI = 8192, L_QI = 16384, L_VI = 147456;

__device__ __forceinline__ int lane_id() { int l; asm volatile("v_mbcnt_lo_u32_b32 %0, -1, 0\n\tv_mbcnt_hi_u32_b32 %0, -1, %0" : "=v"(l)); return l; }
typedef const __attribute__((address_space(4))) MainP* KArg;

__global__ __launch_bounds__(256, 1) void main_kernel(MainP p_) {
    __shared__ __attribute__((aligned(16))) char smem[LDS_TOTAL];
    KArg ka = (KArg)__builtin_amdgcn_kernarg_segment_ptr();
    static_assert(offsetof(MainP, W) == 0x28, "argument layout");
    asm volatile(ASM_EA :: "s"(ka), "s"(blockIdx.x), "v"(threadIdx.x) : "v1", "v2", "s40", "s41", "s42", "s43", "s44", "s45", "s46", "s47", "s48", "s49", "scc", "memory");
    asm volatile("" : "+s"(ka));
    const int b = blockIdx.x, x = b & 7, ix = b >> 3;
    const int w = __builtin_amdgcn_readfirstlane(threadIdx.x >> 6);
    const bool isr = x < 4 && ix >= 16;
    constexpr int NRF = 8 + 8 * G_GVN;
    if (__builtin_expect(ix < (x < 4 ? 16 : NRF), 1)) {
        const int h = (x & 3) * 8 + (ix & 7), id = h + (ix >> 4) * 32;
        const unsigned stb = 0;
#define UNR _Pragma("unroll")
#define CW(k, val) do { if ((k) < 16) ca[(k) & 15] = (unsigned)(val); else if ((k) < 32) cb[(k) & 15] = (unsigned)(val); else cl[((k) - 40) & 15] = (unsigned)(val); } while (0)
#define ROLEW(A_) asm volatile(A_ : "+{v[0:15]}"(ca), "+{v[16:31]}"(cb), "+{v[200:215]}"(cl) : "s"(ka), "s"(w), "s"(id), "s"(stb) : ASM_CLOBW)
#define PAD(n_) asm volatile("s_branch skip_" n_ "\npad_" n_ ":\n\ts_setpc_b64 s[46:47]\nskip_" n_ ":")
#define CW_ALL const int lane = lane_id(), g = lane >> 4, c = lane & 15, q = c >> 2; u32x16 ca = {}, cb = {}, cl = {};                                      \
        CW(0, lane * 16); CW(40, (4 * w + g) * 8192 + h * 256 + (c ^ (2 * g + 8 * (w >> 1))) * 16);                                                          \
        UNR for (int k = 0; k < 2; ++k) CW(41 + k, (8 * g + q) * 256 + ((4 * w + 2 * k + ((c >> 1) & 1)) ^ (2 * q + 8 * (g & 1))) * 16 + (c & 1) * 8)
#define CW_SRC(b_, key_) UNR for (int k = 0; k < 9; ++k) { const int pp = 64 * k + lane, r = pp / 72, pc = pp - 72 * r; CW(43 + k, (b_) + (8 * w + r) * 1152 + (pc ^ (key_)) * 16); }
#define UNIT(u_, key_) (((u_) >> 2) * 128 + (((u_) & 3) ^ (key_)) * 32 + (c & 3) * 8)
#define CW_NEAR CW(1, x * 65536 + aMB(2 * w, lane * 16)); CW(2, x * 65536 + aMB(8, w * 256 + lane * 4)); CW(3, 2 * w * 1024 + lane * 16);                            \
        UNR for (int i = 0; i < 6; ++i) CW(4 + i, ((2 * w + 2 + i) & 7) * 1024 + lane * 16);                                                                 \
        CW(10, (c & 2) ? 0xEEEE : 0x4444);                                                                                                                   \
        CW(20, x * 65536 + 22528 + (lane & 31) * 64); CW(21, lane < 32 ? (lane & 3) * 65536 + phys((lane >> 2) * 2048) + h * 64 : 32768 + h * 128);          \
        CW(22, lane * 4); CW(23, x * 65536 + aMB(2 * w + 1, lane * 16))
#define CW_FAR CW(0, c * 1152 + (g ^ (c >> 1)) * 16); CW(1, ca[0]); CW(2, ca[0] ^ 64); CW(3, w * 9216 + lane * 16); CW_SRC(0, (r >> 1) + 4 * (w & 1))                                   \
        CW(4, L_VI + w * 2048 + lane * 16); CW(5, L_VI + lane * 16); CW(6, lane * 4)
        if (x >= 4 && ix >= 8) {
            PAD("gv"); CW_ALL; CW_FAR;
            UNR for (int t = 0; t < 9; ++t) CW(8 + t, (9 * w + t) * G_UXS + skew(0, 9 * w + t) * 32768 + h * 1024 + lane * 16);
            UNR for (int k = 0; k < 2; ++k) CW(19 + k, (9 * w + 4 * k + g) * 16384 + h * 256 + c * 16);
            CW(21, (9 * w + 8) * 16384 + h * 256 + lane * 4);
            UNR for (int k = 0; k < 4; ++k) CW(52 + k, (4 * g + q) * 1152 + ((9 * w + k) >> 2) * 128 + ((2 * ((9 * w + k) & 3) + ((c >> 1) & 1)) ^ (2 * g + (q >> 1))) * 16 + (c & 1) * 8);
            ROLEW(ASM_GV);
        } else if (x >= 4) {
            PAD("gx"); CW_ALL; CW_FAR;
            CW(8, w * 2048 + lane * 16);
            ROLEW(ASM_GX);
        } else if (ix < 8) {
            PAD("qk"); CW_ALL; CW_NEAR;
            CW(12, L_QI + (16 * w + (c & 13)) * 32 + 8 * g + 4 * ((c >> 1) & 1));
            CW(13, L_QI + (8 * g + q) * 32 + (c & 3) * 8);
            CW(14, G_WS_CTL + G_C_KM + h * 128); CW(15, lane * 16 + 4096);
            CW(16, L_KI + (32 * w + c) * 16 + 4 * g);
            CW(17, L_KI + (4 * g + q) * 16 + (c & 1) * 8);
            const int mm = 8 * (w & 1) + 4 * (g >> 1) + (g & 1);
            CW(18, (x ^ 3) * 65536 + aPX(h >> 1, (h & 1) * 512 + (w & 1) * 256 + g * 64 + (c >> 3) * 32 + ((c >> 1) & 1) * 16 + (2 * (q & 1) + (c & 1)) * 4));
            CW(19, ((c & 1) + 2 * (c >> 3)) * 65536 + aPM(h >> 3, ((h >> 1) & 3) * 256 + (mm + 2 * ((c >> 1) & 1)) * 16 + (h & 1) * 8 + (q & 1) * 4));
            CW(24, G_WS_CTL + G_C_GX + h * 128);
            ROLEW(ASM_QK);
        } else {
            PAD("vv"); CW_ALL; CW_NEAR; CW_SRC(h * 147456, r & 4);
            const int tq = 9 * w + 2 * (g & 1) + (g >> 1);
            CW(14, tq * 16384 + h * 256 + c * 16);
            CW(15, (tq + 4) * 16384 + h * 256 + c * 16);
            CW(16, (9 * w + 8) * 16384 + h * 256 + c * 16 + (g & 1) * 8);
            CW(19, (c & 8) ? 0xEEEE : 0x4444);
            CW(12, L_QI + w * 576 + c * 16 + 4 * g);
            CW(13, L_QI + (8 * g + q) * 16 + (c & 1) * 8 + ((c >> 1) & 1) * 576);
            CW(25, L_QI + (8 * g + q) * 16 + (c & 1) * 8 + (1 - ((c >> 1) & 1)) * 576);
            UNR for (int k = 0; k < 4; ++k) CW(52 + k, ((k >> 1) ^ (q >> 1)) * 36864 + (4 * g + (q & 1)) * 1152 + UNIT(9 * w + 2 * (k & 1) + (q >> 1), 2 * (g & 1)));
            CW(24, (q >> 1) * 36864 + (4 * g + (q & 1)) * 1152 + UNIT(9 * w + 8, 2 * (g & 1)));
            ROLEW(ASM_V);
        }
    } else {
    if (isr && (ix < 25 || ix >= 29)) {
        const int id = x * 12 + (ix < 25 ? ix - 13 : ix - 29);
        init_cal(ka->ws, id, threadIdx.x, (FLG(ka->prm[5]) >> 16) & 15);
        if (FLG(ka->prm[5]) & 4) {
            const unsigned o = (id * 384 + threadIdx.x) * 128, o2 = o + (threadIdx.x & 128 ? 16384 : 32768);
            const char* a = (const char*)ka->X + o; const char* b2 = (const char*)ka->U + o;
            const char* c2 = (const char*)(w < 2 ? ka->X : ka->U) + o2;
            const char* ps = (const char*)(w == 0 ? ka->logA : w == 1 ? ka->Aim : w == 2 ? ka->Cre : ka->Cim) + id * 3072 + min((int)(threadIdx.x & 63), 23) * 128;
            asm volatile("s_mov_b32 m0, %4\n\ts_nop 0\n\tglobal_load_lds_dword %0, off\n\tglobal_load_lds_dword %1, off\n\tglobal_load_lds_dword %2, off\n\tglobal_load_lds_dword %3, off"
                         :: "v"(a), "v"(c2), "v"(b2), "v"(ps), "s"(163072) : "memory");
        }
    }
    if (x >= 4 ? ix >= 24 : (ix >= 25 && ix < 29)) init_fill(ka->ws, ka->mem0, x >= 4 ? (ix - 24) * 4 + (x & 3) : 32 + (ix - 25) * 4 + x, threadIdx.x);
    if (x >= 4 && !(FLG(ka->prm[5]) & 1)) {
        const char* src = (const char*)ka->W[3] + (size_t)((x & 3) * 8 + (ix & 7)) * 147456 + threadIdx.x * 128;
#pragma unroll
        for (int i = 0; i < 5; ++i)
            if (i < 4 || w < 2) asm volatile("s_mov_b32 m0, %1\n\ts_nop 0\n\tglobal_load_lds_dword %0, off" :: "v"(src + i * 32768), "s"(155648) : "memory");
    }
    if (isr && ix >= 25) {
        for (int i = 0; i < (ix < 29 ? 3 : 0) + HSL; ++i) __builtin_amdgcn_s_sleep(40);
        init_wait(ka->ws, w, threadIdx.x & 63);
    }
    asm volatile("" : "+s"(ka));
    if (x < 4 && (ix < 25 || w == 0)) {
        const int lane = threadIdx.x & 63, g = lane >> 4, c = lane & 15;
        const unsigned lb = w * G_STG;
        unsigned* ct = (unsigned*)(smem + lb) + lane;
        asm volatile("" :: "v"(ct) : "memory");
#define CTW(k, val) ct[(k) * 64] = (unsigned)(val)
        const u64 ws = (u64)ka->ws, prm = (u64)ka;
        const u64 stb = 0;
        CTW(0, lane * 16);
        const int j = x * 9 + ix - 16;
        if (ix >= 25) {
            CTW(1, x * 65536 + 22528 + (lane & 31) * 64);
            for (int k = 0; k < 7; ++k) {
                const int q = min(ix - 25 + 7 * k, 43), id = q < 36 ? 9 * x + (q >> 2) : 8 * x + q - 36;
                CTW(4 + k, (q < 36 ? G_D_UX + id * G_UXS + skew(0, id) * 32768 + (q & 3) * 8192 : G_D_KX + id * G_HS8 + skew(0, id) * 8192) + lane * 128);
                CTW(11 + k, q < 36 ? 32768 : 8192);
            }
        } else {
            const int rot = (FLG(ka->prm[5]) & 16) ? 0 : (x ^ 3) * 4;
            if (w >= 2) {
                return;
            }
            CTW(1, lane * 16 + 4096);
            CTW(2, x * 65536 + aMB(j >> 2, (j & 3) * 256 + (4 * g + 2 * ((c >> 1) & 1) + (c & 1)) * 16 + (c >> 2) * 4));
            for (int k = 0; k < 5; ++k) { const int q = 9 * k + ix - 16; CTW(46 + k, x * 65536 + lane * 16 + (q < 29 ? phys(q * 1024) : 0x200000 + phys((q - 29) * 1024))); }
            CTW(4, (c & 2) ? 0xEEEE : 0x4444); CTW(5, (c & 1) ? 0x03020706u : 0x05040100u);
            CTW(6, G_WS_CTL + G_C_GV + ((w & (G_GVN - 1)) * 32 + ((lane & 31) ^ (2 * rot))) * 128); CTW(7, x * 65536 + (lane & 31) * 64); CTW(8, lane * 4); CTW(9, 131072);
            for (int k = 0; k < 16; ++k) { const int i = k ^ rot; CTW(10 + k, x * 65536 + aPX(i, (g >> 1) * 512 + (2 * (c >> 2) + (c & 1)) * 64 + (g & 1) * 32 + ((c >> 1) & 1) * 16)); CTW(26 + k, i * 2048 + lane * 16); }
            for (int k = 0; k < 4; ++k) CTW(42 + k, x * 65536 + aPM(k, lane * 16));
        }
        const int id = ix >= 25 ? ix - 25 : j;
#define ROLE(A_) asm volatile(A_ :: "s"(ws), "s"(ws), "s"(ws), "s"(w), "s"(id), "s"(ws), "s"(prm), "s"(lb), "s"(stb) : ASM_CLOB)
        if (ix >= 25) ROLE(ASM_H);
        else ROLE(ASM_R);
    }
    else if (x >= 4 && ix >= 24) {
        if (ix >= 25) for (int i = 0; i < ((FLG(ka->prm[5]) >> 5) & 7); ++i) __builtin_amdgcn_s_sleep(36);
        const char* xs = (const char*)ka->X + (size_t)((ix - 24) * 4 + (x & 3)) * 73728 + threadIdx.x * 128;
        const int f = (ix - 24) * 4 + (x & 3);
        const u16* ps = (w == 0 ? ka->logA : w == 1 ? ka->Aim : w == 2 ? ka->Cre : ka->Cim) + f * 2304 + min((int)(threadIdx.x & 63), 35) * 64;
        if (ix < 25 || !(FLG(ka->prm[5]) & 4))
        asm volatile("s_mov_b32 m0, %4\n\ts_nop 0\n\tglobal_load_lds_dword %0, off\n\tglobal_load_lds_dword %1, off\n\tglobal_load_lds_dword %2, off\n\tglobal_load_lds_dword %3, off"
                     :: "v"(xs), "v"(xs + 32768), "v"(xs + 65536 - w * 8192), "v"(ps), "s"(163072) : "memory");
        if (ix < 25) for (int i = 0; i < ((FLG(ka->prm[5]) >> 5) & 7); ++i) __builtin_amdgcn_s_sleep(36);
    }
    }
    asm volatile("" : "+s"(ka));
    MainP p;
    p.prm[5] = ka->prm[5]; p.prm[7] = ka->prm[7];
    p.U = ka->U; p.ws = ka->ws; p.logA = ka->logA; p.Aim = ka->Aim; p.Cre = ka->Cre; p.Cim = ka->Cim; p.logdt = ka->logdt; p.Dskip = ka->Dskip; p.out = ka->out;
    if (x >= 4 && ix >= 24) {
        const u32x4 sent = {0xffffffffu, 0xffffffffu, 0xffffffffu, 0xffffffffu};
        const int tid = w * 64 + lane_id(), h = (ix - 24) * 4 + (x & 3);
        task_ut(p, smem, h, w, lane_id(), [&] {
#pragma unroll 1
            for (int sg = NG1; sg < 64; ++sg) {
                const unsigned ok = G_D_KM + h * G_HS4 + skew(sg, h) * 4096 + tid * 16;
                const unsigned op = tid < 128 ? G_D_PX + (sg * 32 + h) * 2048 + tid * 16 : G_D_PM + sg * 16384 + ((tid >> 4) & 1) * 8192 + h * 256 + (tid & 15) * 16;
                asm volatile("global_store_dwordx4 %0, %2, %3 sc1\n\tglobal_store_dwordx4 %1, %2, %3 sc1\n\ts_nop 1" :: "v"(ok), "v"(op), "v"(sent), "s"(p.ws) : "memory");
            }
        });
    }
    p.prm[0] = near_mask(p.ws, lane_id());
    p.X = (const u16*)(p.ws + ((p.prm[0] & 2) ? 0 : 8192));
    {
        const int nq = x < 4 ? (w + ((FLH(p.prm[7]) >> 1) & 3) > 3 ? 0 : 1000) : ix < 8 ? (FLG(p.prm[5]) >> 8) & 3 : ix < 16 ? (FLG(p.prm[5]) >> 10) & 3 : (ix & 2) && ((31 - ix) >> 2) < NAG ? 0 : ix < 24 ? (ix < 16 + ((FLH(p.prm[7]) >> 10) & 15) ? (FLG(p.prm[5]) >> 12) & 3 : 0) : (FLG(p.prm[5]) >> 14) & 3;
        if (b == 239 && w == 0) watch_role(p, lane_id());
        else if (nq || b == 239) {
            for (int i = 0; i < (int)(((ix & 7) * 4 + w) * ((FLG(p.prm[5]) >> 23) & 7)); ++i) __builtin_amdgcn_s_sleep(3);
            s4d_wave(p, smem, w, lane_id(), nq == 3 || b == 239 ? 1000 : nq);
        }
        if (x < 4) return;
        __syncthreads();
    }
    if ((ix & 18) == 18 && ((31 - ix) >> 2) < NAG) { attn_role(p, ((31 - ix) >> 2) + (ix < 24 ? NA : 0), (x & 3) * 8 + (ix & 1) * 4 + w, w, lane_id());
        if (b == 255) {
            wait_word(p, p.ws + G_WS_CTL + G_C_ARR, 255, 50u);
            if (w == 0 && lane_id() == 0) __hip_atomic_fetch_add((gu32*)(p.ws + G_WS_EP), 1u, RLX_AGENT);
        }
        __syncthreads();
    }
    worker_body(p, smem, w, lane_id());
}

extern "C" __global__ void code_end_marker() {}

static constexpr int g_prm[8] = {PRM_DEFAULT};

extern "C" void kernel_launch(void* const* d_in, const int* in_sizes, int n_in, void* d_out, int out_size,
                              void* d_ws, size_t ws_size, hipStream_t stream) {
    char* ws = (char*)(((uintptr_t)d_ws + 0x1FFFFF) & ~(uintptr_t)0x1FFFFF);
    MainP p;
    for (int i = 0; i < 8; ++i) p.prm[i] = g_prm[i];
    p.mem0 = (const u16*)d_in[1];
    for (int i = 0; i < 4; ++i) p.W[i] = (const u16*)d_in[2 + i];
    p.X = (const u16*)d_in[0]; p.U = (const u16*)d_in[7]; p.ws = ws;
    p.logA = (const u16*)d_in[8]; p.Aim = (const u16*)d_in[9]; p.Cre = (const u16*)d_in[10]; p.Cim = (const u16*)d_in[11];
    p.logdt = (const u16*)d_in[12]; p.Dskip = (const u16*)d_in[13]; p.out = (u16*)d_out;
    main_kernel<<<256, 256, 0, stream>>>(p);
}
```

```cpp
#include <hip/hip_runtime.h>
#include <hip/hip_bf16.h>
#include <stdint.h>
#include <stddef.h>

typedef __attribute__((ext_vector_type(8))) __bf16 bf16x8;
typedef __attribute__((ext_vector_type(4))) short s16x4;
typedef __attribute__((ext_vector_type(4))) float f32x4;
typedef __attribute__((ext_vector_type(4))) unsigned u32x4;
typedef __attribute__((ext_vector_type(2))) unsigned u32x2;
typedef __attribute__((ext_vector_type(2))) float f32x2;
typedef __attribute__((ext_vector_type(16))) unsigned u32x16;
typedef unsigned short u16;
typedef unsigned long long u64;

#define GAS __attribute__((address_space(1)))
typedef GAS unsigned gu32;
#define RLX_AGENT __ATOMIC_RELAXED, __HIP_MEMORY_SCOPE_AGENT

static constexpr int DM = 576, NH = 32, DH = 128, MM = 8, SEG = 32, NSEG = 64, SL = 2048;
static constexpr int HD = NH * DH;
static constexpr int OUTW = 2 * DM;
static constexpr int NST = 128;

__device__ __forceinline__ float bf2f(u16 h) { return __uint_as_float(((unsigned)h) << 16); }
typedef __attribute__((ext_vector_type(2))) __bf16 bf16x2_t;
__device__ __forceinline__ unsigned pk2bf(float a, float b) {
    bf16x2_t v; v[0] = (__bf16)a; v[1] = (__bf16)b;
    return __builtin_bit_cast(unsigned, v);
}
__device__ __forceinline__ float bfr(float f) { return __uint_as_float(pk2bf(f, f) << 16); }
__device__ __forceinline__ float lo2f(unsigned u) { return __uint_as_float(u << 16); }
__device__ __forceinline__ float hi2f(unsigned u) { return __uint_as_float(u & 0xffff0000u); }
__device__ __forceinline__ float rowmax4(float x) {
    auto a = __builtin_amdgcn_permlane16_swap(__float_as_uint(x), __float_as_uint(x), false, false);
    x = fmaxf(__uint_as_float(a[0]), __uint_as_float(a[1]));
    auto b = __builtin_amdgcn_permlane32_swap(__float_as_uint(x), __float_as_uint(x), false, false);
    return fmaxf(__uint_as_float(b[0]), __uint_as_float(b[1]));
}
__device__ __forceinline__ float rowsum4(float x) {
    auto a = __builtin_amdgcn_permlane16_swap(__float_as_uint(x), __float_as_uint(x), false, false);
    x = __uint_as_float(a[0]) + __uint_as_float(a[1]);
    auto b = __builtin_amdgcn_permlane32_swap(__float_as_uint(x), __float_as_uint(x), false, false);
    return __uint_as_float(b[0]) + __uint_as_float(b[1]);
}

struct MainP {
    int prm[8];
    const u16* mem0; const u16* W[4]; const u16* X; const u16* U;
    char* ws;
    const u16* logA; const u16* Aim; const u16* Cre; const u16* Cim; const u16* logdt; const u16* Dskip;
    u16* out;
};
static constexpr int LDS_TOTAL = 163840;
static constexpr unsigned kWaitCap = 1u << 18;
#define VMC0() asm volatile("s_waitcnt vmcnt(0)" ::: "memory")
#define PRM_DEFAULT 1020, 600, 120, 656, 760, 1434873899, 3145976, 52931
#define ASM_QK \
    "s_load_dwordx2 s[42:43], %3, 40\n\t" \
    "s_load_dwordx2 s[44:45], %3, 48\n\t" \
    "s_load_dwordx2 s[36:37], %3, 0x58\n\t" \
    "s_load_dwordx2 s[50:51], %3, 0x48\n\t" \
    "s_load_dwordx8 s[84:91], %3, 0\n\t" \
    "s_mov_b32 s66, %4\n\t" \
    "s_mov_b32 s67, %5\n\t" \
    "s_mov_b32 s68, 262144\n\t" \
    "s_mov_b32 s72, 0\n\t" \
    "s_mov_b32 s83, -1\n\t" \
    "s_mov_b32 s65, 0\n\t" \
    "s_lshl_b32 s82, s66, 10\n\t" \
    "s_waitcnt lgkmcnt(0)\n\t" \
    "s_add_u32 s46, s42, 2097152\n\t" \
    "s_addc_u32 s47, s43, 0\n\t" \
    "s_add_u32 m0, s82, 65536\n\t" \
    "s_nop 0\n\t" \
    "global_load_lds_dwordx4 v200, s[46:47]\n\t" \
    "s_add_u32 s46, s42, 2228224\n\t" \
    "s_addc_u32 s47, s43, 0\n\t" \
    "s_add_u32 m0, s82, 69632\n\t" \
    "s_nop 0\n\t" \
    "global_load_lds_dwordx4 v200, s[46:47]\n\t" \
    "s_add_u32 s46, s42, 2359296\n\t" \
    "s_addc_u32 s47, s43, 0\n\t" \
    "s_add_u32 m0, s82, 73728\n\t" \
    "s_nop 0\n\t" \
    "global_load_lds_dwordx4 v200, s[46:47]\n\t" \
    "s_add_u32 s46, s42, 2490368\n\t" \
    "s_addc_u32 s47, s43, 0\n\t" \
    "s_add_u32 m0, s82, 77824\n\t" \
    "s_nop 0\n\t" \
    "global_load_lds_dwordx4 v200, s[46:47]\n\t" \
    "s_add_u32 s46, s42, 2621440\n\t" \
    "s_addc_u32 s47, s43, 0\n\t" \
    "s_add_u32 m0, s82, 81920\n\t" \
    "s_nop 0\n\t" \
    "global_load_lds_dwordx4 v200, s[46:47]\n\t" \
    "s_add_u32 s46, s42, 2752512\n\t" \
    "s_addc_u32 s47, s43, 0\n\t" \
    "s_add_u32 m0, s82, 86016\n\t" \
    "s_nop 0\n\t" \
    "global_load_lds_dwordx4 v200, s[46:47]\n\t" \
    "s_add_u32 s46, s42, 2883584\n\t" \
    "s_addc_u32 s47, s43, 0\n\t" \
    "s_add_u32 m0, s82, 90112\n\t" \
    "s_nop 0\n\t" \
    "global_load_lds_dwordx4 v200, s[46:47]\n\t" \
    "s_add_u32 s46, s42, 3014656\n\t" \
    "s_addc_u32 s47, s43, 0\n\t" \
    "s_add_u32 m0, s82, 94208\n\t" \
    "s_nop 0\n\t" \
    "global_load_lds_dwordx4 v200, s[46:47]\n\t" \
    "s_add_u32 s46, s42, 3145728\n\t" \
    "s_addc_u32 s47, s43, 0\n\t" \
    "s_add_u32 m0, s82, 98304\n\t" \
    "s_nop 0\n\t" \
    "global_load_lds_dwordx4 v200, s[46:47]\n\t" \
    "s_add_u32 s46, s42, 3276800\n\t" \
    "s_addc_u32 s47, s43, 0\n\t" \
    "s_add_u32 m0, s82, 102400\n\t" \
    "s_nop 0\n\t" \
    "global_load_lds_dwordx4 v200, s[46:47]\n\t" \
    "s_add_u32 s46, s42, 3407872\n\t" \
    "s_addc_u32 s47, s43, 0\n\t" \
    "s_add_u32 m0, s82, 106496\n\t" \
    "s_nop 0\n\t" \
    "global_load_lds_dwordx4 v200, s[46:47]\n\t" \
    "s_add_u32 s46, s42, 3538944\n\t" \
    "s_addc_u32 s47, s43, 0\n\t" \
    "s_add_u32 m0, s82, 110592\n\t" \
    "s_nop 0\n\t" \
    "global_load_lds_dwordx4 v200, s[46:47]\n\t" \
    "s_add_u32 s46, s42, 3670016\n\t" \
    "s_addc_u32 s47, s43, 0\n\t" \
    "s_add_u32 m0, s82, 114688\n\t" \
    "s_nop 0\n\t" \
    "global_load_lds_dwordx4 v200, s[46:47]\n\t" \
    "s_add_u32 s46, s42, 3801088\n\t" \
    "s_addc_u32 s47, s43, 0\n\t" \
    "s_add_u32 m0, s82, 118784\n\t" \
    "s_nop 0\n\t" \
    "global_load_lds_dwordx4 v200, s[46:47]\n\t" \
    "s_add_u32 s46, s42, 3932160\n\t" \
    "s_addc_u32 s47, s43, 0\n\t" \
    "s_add_u32 m0, s82, 122880\n\t" \
    "s_nop 0\n\t" \
    "global_load_lds_dwordx4 v200, s[46:47]\n\t" \
    "s_add_u32 s46, s42, 4063232\n\t" \
    "s_addc_u32 s47, s43, 0\n\t" \
    "s_add_u32 m0, s82, 126976\n\t" \
    "s_nop 0\n\t" \
    "global_load_lds_dwordx4 v200, s[46:47]\n\t" \
    "s_add_u32 s46, s42, 4194304\n\t" \
    "s_addc_u32 s47, s43, 0\n\t" \
    "s_add_u32 m0, s82, 131072\n\t" \
    "s_nop 0\n\t" \
    "global_load_lds_dwordx4 v200, s[46:47]\n\t" \
    "s_add_u32 s46, s42, 4325376\n\t" \
    "s_addc_u32 s47, s43, 0\n\t" \
    "s_add_u32 m0, s82, 135168\n\t" \
    "s_nop 0\n\t" \
    "global_load_lds_dwordx4 v200, s[46:47]\n\t" \
    "s_add_u32 s46, s42, 4456448\n\t" \
    "s_addc_u32 s47, s43, 0\n\t" \
    "s_add_u32 m0, s82, 139264\n\t" \
    "s_nop 0\n\t" \
    "global_load_lds_dwordx4 v200, s[46:47]\n\t" \
    "s_add_u32 s46, s42, 4587520\n\t" \
    "s_addc_u32 s47, s43, 0\n\t" \
    "s_add_u32 m0, s82, 143360\n\t" \
    "s_nop 0\n\t" \
    "global_load_lds_dwordx4 v200, s[46:47]\n\t" \
    "s_mov_b32 s77, 3019898880\n\t" \
    "s_mov_b32 s78, 3054198966\n\t" \
    "s_waitcnt lgkmcnt(0)\n\t" \
    "s_mov_b32 s70, 116\n\t" \
    "qk_E1_%=:\n\t" \
    "s_memtime s[56:57]\n\t" \
    "s_memrealtime s[2:3]\n\t" \
    "s_nop 0\n\t" \
    "s_memrealtime s[4:5]\n\t" \
    "s_nop 0\n\t" \
    "s_memrealtime s[6:7]\n\t" \
    "s_nop 0\n\t" \
    "s_memrealtime s[8:9]\n\t" \
    "s_nop 0\n\t" \
    "s_memrealtime s[10:11]\n\t" \
    "s_nop 0\n\t" \
    "s_memrealtime s[12:13]\n\t" \
    "s_nop 0\n\t" \
    "s_memrealtime s[14:15]\n\t" \
    "s_nop 0\n\t" \
    "s_memrealtime s[16:17]\n\t" \
    "s_nop 0\n\t" \
    "s_memrealtime s[18:19]\n\t" \
    "s_nop 0\n\t" \
    "s_memrealtime s[20:21]\n\t" \
    "s_nop 0\n\t" \
    "s_memrealtime s[22:23]\n\t" \
    "s_nop 0\n\t" \
    "s_memrealtime s[24:25]\n\t" \
    "s_nop 0\n\t" \
    "s_memrealtime s[26:27]\n\t" \
    "s_nop 0\n\t" \
    "s_memrealtime s[28:29]\n\t" \
    "s_memtime s[54:55]\n\t" \
    "s_add_u32 s70, s70, 8\n\t" \
    "s_waitcnt lgkmcnt(0)\n\t" \
    "s_sub_u32 s69, s54, s56\n\t" \
    "s_cmp_gt_u32 s69, s70\n\t" \
    "s_cbranch_scc1 qk_E1_%=\n\t" \
    "s_add_u32 s2, s2, s4\n\t" \
    "s_add_u32 s6, s6, s8\n\t" \
    "s_add_u32 s10, s10, s12\n\t" \
    "s_add_u32 s14, s14, s16\n\t" \
    "s_add_u32 s18, s18, s20\n\t" \
    "s_add_u32 s22, s22, s24\n\t" \
    "s_add_u32 s2, s2, s6\n\t" \
    "s_add_u32 s10, s10, s14\n\t" \
    "s_add_u32 s18, s18, s22\n\t" \
    "s_add_u32 s2, s2, s26\n\t" \
    "s_add_u32 s10, s10, s18\n\t" \
    "s_add_u32 s2, s2, s10\n\t" \
    "s_mul_i32 s70, s28, 13\n\t" \
    "s_sub_u32 s70, s70, s2\n\t" \
    "s_min_u32 s70, s70, 52\n\t" \
    "s_mul_i32 s69, s69, s70\n\t" \
    "s_mul_hi_u32 s69, s69, 76695844\n\t" \
    "s_mov_b32 s75, s73\n\t" \
    "s_mov_b32 s76, s74\n\t" \
    "s_add_u32 s73, s56, s69\n\t" \
    "s_lshl_b32 s74, s28, 4\n\t" \
    "s_waitcnt vmcnt(32) lgkmcnt(0)\n\t" \
    "s_barrier\n\t" \
    "ds_read_b64_tr_b16 a[0:1], v201 offset:0\n\t" \
    "ds_read_b64_tr_b16 a[2:3], v201 offset:1024\n\t" \
    "ds_read_b64_tr_b16 a[4:5], v201 offset:8192\n\t" \
    "ds_read_b64_tr_b16 a[6:7], v201 offset:9216\n\t" \
    "ds_read_b64_tr_b16 a[72:73], v202 offset:0\n\t" \
    "ds_read_b64_tr_b16 a[74:75], v202 offset:1024\n\t" \
    "ds_read_b64_tr_b16 a[76:77], v202 offset:8192\n\t" \
    "ds_read_b64_tr_b16 a[78:79], v202 offset:9216\n\t" \
    "s_waitcnt vmcnt(28) lgkmcnt(0)\n\t" \
    "s_barrier\n\t" \
    "ds_read_b64_tr_b16 a[8:9], v201 offset:16384\n\t" \
    "ds_read_b64_tr_b16 a[10:11], v201 offset:17408\n\t" \
    "ds_read_b64_tr_b16 a[12:13], v201 offset:24576\n\t" \
    "ds_read_b64_tr_b16 a[14:15], v201 offset:25600\n\t" \
    "ds_read_b64_tr_b16 a[80:81], v202 offset:16384\n\t" \
    "ds_read_b64_tr_b16 a[82:83], v202 offset:17408\n\t" \
    "ds_read_b64_tr_b16 a[84:85], v202 offset:24576\n\t" \
    "ds_read_b64_tr_b16 a[86:87], v202 offset:25600\n\t" \
    "s_add_u32 s46, s44, 0\n\t" \
    "s_addc_u32 s47, s45, 0\n\t" \
    "s_add_u32 m0, s82, 0\n\t" \
    "s_nop 0\n\t" \
    "global_load_lds_dwordx4 v200, s[46:47]\n\t" \
    "s_add_u32 s46, s44, 131072\n\t" \
    "s_addc_u32 s47, s45, 0\n\t" \
    "s_add_u32 m0, s82, 4096\n\t" \
    "s_nop 0\n\t" \
    "global_load_lds_dwordx4 v200, s[46:47]\n\t" \
    "s_add_u32 s46, s44, 262144\n\t" \
    "s_addc_u32 s47, s45, 0\n\t" \
    "s_add_u32 m0, s82, 8192\n\t" \
    "s_nop 0\n\t" \
    "global_load_lds_dwordx4 v200, s[46:47]\n\t" \
    "s_add_u32 s46, s44, 393216\n\t" \
    "s_addc_u32 s47, s45, 0\n\t" \
    "s_add_u32 m0, s82, 12288\n\t" \
    "s_nop 0\n\t" \
    "global_load_lds_dwordx4 v200, s[46:47]\n\t" \
    "s_waitcnt vmcnt(28) lgkmcnt(0)\n\t" \
    "s_barrier\n\t" \
    "ds_read_b64_tr_b16 a[16:17], v201 offset:32768\n\t" \
    "ds_read_b64_tr_b16 a[18:19], v201 offset:33792\n\t" \
    "ds_read_b64_tr_b16 a[20:21], v201 offset:40960\n\t" \
    "ds_read_b64_tr_b16 a[22:23], v201 offset:41984\n\t" \
    "ds_read_b64_tr_b16 a[88:89], v202 offset:32768\n\t" \
    "ds_read_b64_tr_b16 a[90:91], v202 offset:33792\n\t" \
    "ds_read_b64_tr_b16 a[92:93], v202 offset:40960\n\t" \
    "ds_read_b64_tr_b16 a[94:95], v202 offset:41984\n\t" \
    "s_add_u32 s46, s44, 524288\n\t" \
    "s_addc_u32 s47, s45, 0\n\t" \
    "s_add_u32 m0, s82, 16384\n\t" \
    "s_nop 0\n\t" \
    "global_load_lds_dwordx4 v200, s[46:47]\n\t" \
    "s_add_u32 s46, s44, 655360\n\t" \
    "s_addc_u32 s47, s45, 0\n\t" \
    "s_add_u32 m0, s82, 20480\n\t" \
    "s_nop 0\n\t" \
    "global_load_lds_dwordx4 v200, s[46:47]\n\t" \
    "s_add_u32 s46, s44, 786432\n\t" \
    "s_addc_u32 s47, s45, 0\n\t" \
    "s_add_u32 m0, s82, 24576\n\t" \
    "s_nop 0\n\t" \
    "global_load_lds_dwordx4 v200, s[46:47]\n\t" \
    "s_add_u32 s46, s44, 917504\n\t" \
    "s_addc_u32 s47, s45, 0\n\t" \
    "s_add_u32 m0, s82, 28672\n\t" \
    "s_nop 0\n\t" \
    "global_load_lds_dwordx4 v200, s[46:47]\n\t" \
    "s_waitcnt vmcnt(28) lgkmcnt(0)\n\t" \
    "s_barrier\n\t" \
    "ds_read_b64_tr_b16 a[24:25], v201 offset:49152\n\t" \
    "ds_read_b64_tr_b16 a[26:27], v201 offset:50176\n\t" \
    "ds_read_b64_tr_b16 a[28:29], v201 offset:57344\n\t" \
    "ds_read_b64_tr_b16 a[30:31], v201 offset:58368\n\t" \
    "ds_read_b64_tr_b16 a[96:97], v202 offset:49152\n\t" \
    "ds_read_b64_tr_b16 a[98:99], v202 offset:50176\n\t" \
    "ds_read_b64_tr_b16 a[100:101], v202 offset:57344\n\t" \
    "ds_read_b64_tr_b16 a[102:103], v202 offset:58368\n\t" \
    "s_add_u32 s46, s44, 1048576\n\t" \
    "s_addc_u32 s47, s45, 0\n\t" \
    "s_add_u32 m0, s82, 32768\n\t" \
    "s_nop 0\n\t" \
    "global_load_lds_dwordx4 v200, s[46:47]\n\t" \
    "s_add_u32 s46, s44, 1179648\n\t" \
    "s_addc_u32 s47, s45, 0\n\t" \
    "s_add_u32 m0, s82, 36864\n\t" \
    "s_nop 0\n\t" \
    "global_load_lds_dwordx4 v200, s[46:47]\n\t" \
    "s_add_u32 s46, s44, 1310720\n\t" \
    "s_addc_u32 s47, s45, 0\n\t" \
    "s_add_u32 m0, s82, 40960\n\t" \
    "s_nop 0\n\t" \
    "global_load_lds_dwordx4 v200, s[46:47]\n\t" \
    "s_add_u32 s46, s44, 1441792\n\t" \
    "s_addc_u32 s47, s45, 0\n\t" \
    "s_add_u32 m0, s82, 45056\n\t" \
    "s_nop 0\n\t" \
    "global_load_lds_dwordx4 v200, s[46:47]\n\t" \
    "s_waitcnt vmcnt(28) lgkmcnt(0)\n\t" \
    "s_barrier\n\t" \
    "v_add_u32 v201, 0x10000, v201\n\t" \
    "v_add_u32 v202, 0x10000, v202\n\t" \
    "ds_read_b64_tr_b16 a[32:33], v201 offset:0\n\t" \
    "ds_read_b64_tr_b16 a[34:35], v201 offset:1024\n\t" \
    "ds_read_b64_tr_b16 a[36:37], v201 offset:8192\n\t" \
    "ds_read_b64_tr_b16 a[38:39], v201 offset:9216\n\t" \
    "ds_read_b64_tr_b16 a[104:105], v202 offset:0\n\t" \
    "ds_read_b64_tr_b16 a[106:107], v202 offset:1024\n\t" \
    "ds_read_b64_tr_b16 a[108:109], v202 offset:8192\n\t" \
    "ds_read_b64_tr_b16 a[110:111], v202 offset:9216\n\t" \
    "s_add_u32 s46, s44, 1572864\n\t" \
    "s_addc_u32 s47, s45, 0\n\t" \
    "s_add_u32 m0, s82, 49152\n\t" \
    "s_nop 0\n\t" \
    "global_load_lds_dwordx4 v200, s[46:47]\n\t" \
    "s_add_u32 s46, s44, 1703936\n\t" \
    "s_addc_u32 s47, s45, 0\n\t" \
    "s_add_u32 m0, s82, 53248\n\t" \
    "s_nop 0\n\t" \
    "global_load_lds_dwordx4 v200, s[46:47]\n\t" \
    "s_add_u32 s46, s44, 1835008\n\t" \
    "s_addc_u32 s47, s45, 0\n\t" \
    "s_add_u32 m0, s82, 57344\n\t" \
    "s_nop 0\n\t" \
    "global_load_lds_dwordx4 v200, s[46:47]\n\t" \
    "s_add_u32 s46, s44, 1966080\n\t" \
    "s_addc_u32 s47, s45, 0\n\t" \
    "s_add_u32 m0, s82, 61440\n\t" \
    "s_nop 0\n\t" \
    "global_load_lds_dwordx4 v200, s[46:47]\n\t" \
    "s_waitcnt vmcnt(28) lgkmcnt(0)\n\t" \
    "s_barrier\n\t" \
    "ds_read_b64_tr_b16 a[40:41], v201 offset:16384\n\t" \
    "ds_read_b64_tr_b16 a[42:43], v201 offset:17408\n\t" \
    "ds_read_b64_tr_b16 a[44:45], v201 offset:24576\n\t" \
    "ds_read_b64_tr_b16 a[46:47], v201 offset:25600\n\t" \
    "ds_read_b64_tr_b16 a[112:113], v202 offset:16384\n\t" \
    "ds_read_b64_tr_b16 a[114:115], v202 offset:17408\n\t" \
    "ds_read_b64_tr_b16 a[116:117], v202 offset:24576\n\t" \
    "ds_read_b64_tr_b16 a[118:119], v202 offset:25600\n\t" \
    "s_add_u32 s46, s44, 2097152\n\t" \
    "s_addc_u32 s47, s45, 0\n\t" \
    "s_add_u32 m0, s82, 65536\n\t" \
    "s_nop 0\n\t" \
    "global_load_lds_dwordx4 v200, s[46:47]\n\t" \
    "s_add_u32 s46, s44, 2228224\n\t" \
    "s_addc_u32 s47, s45, 0\n\t" \
    "s_add_u32 m0, s82, 69632\n\t" \
    "s_nop 0\n\t" \
    "global_load_lds_dwordx4 v200, s[46:47]\n\t" \
    "s_add_u32 s46, s44, 2359296\n\t" \
    "s_addc_u32 s47, s45, 0\n\t" \
    "s_add_u32 m0, s82, 73728\n\t" \
    "s_nop 0\n\t" \
    "global_load_lds_dwordx4 v200, s[46:47]\n\t" \
    "s_add_u32 s46, s44, 2490368\n\t" \
    "s_addc_u32 s47, s45, 0\n\t" \
    "s_add_u32 m0, s82, 77824\n\t" \
    "s_nop 0\n\t" \
    "global_load_lds_dwordx4 v200, s[46:47]\n\t" \
    "s_waitcnt vmcnt(28) lgkmcnt(0)\n\t" \
    "s_barrier\n\t" \
    "ds_read_b64_tr_b16 a[48:49], v201 offset:32768\n\t" \
    "ds_read_b64_tr_b16 a[50:51], v201 offset:33792\n\t" \
    "ds_read_b64_tr_b16 a[52:53], v201 offset:40960\n\t" \
    "ds_read_b64_tr_b16 a[54:55], v201 offset:41984\n\t" \
    "ds_read_b64_tr_b16 a[120:121], v202 offset:32768\n\t" \
    "ds_read_b64_tr_b16 a[122:123], v202 offset:33792\n\t" \
    "ds_read_b64_tr_b16 a[124:125], v202 offset:40960\n\t" \
    "ds_read_b64_tr_b16 a[126:127], v202 offset:41984\n\t" \
    "s_add_u32 s46, s44, 2621440\n\t" \
    "s_addc_u32 s47, s45, 0\n\t" \
    "s_add_u32 m0, s82, 81920\n\t" \
    "s_nop 0\n\t" \
    "global_load_lds_dwordx4 v200, s[46:47]\n\t" \
    "s_add_u32 s46, s44, 2752512\n\t" \
    "s_addc_u32 s47, s45, 0\n\t" \
    "s_add_u32 m0, s82, 86016\n\t" \
    "s_nop 0\n\t" \
    "global_load_lds_dwordx4 v200, s[46:47]\n\t" \
    "s_add_u32 s46, s44, 2883584\n\t" \
    "s_addc_u32 s47, s45, 0\n\t" \
    "s_add_u32 m0, s82, 90112\n\t" \
    "s_nop 0\n\t" \
    "global_load_lds_dwordx4 v200, s[46:47]\n\t" \
    "s_add_u32 s46, s44, 3014656\n\t" \
    "s_addc_u32 s47, s45, 0\n\t" \
    "s_add_u32 m0, s82, 94208\n\t" \
    "s_nop 0\n\t" \
    "global_load_lds_dwordx4 v200, s[46:47]\n\t" \
    "s_waitcnt vmcnt(28) lgkmcnt(0)\n\t" \
    "s_barrier\n\t" \
    "ds_read_b64_tr_b16 a[56:57], v201 offset:49152\n\t" \
    "ds_read_b64_tr_b16 a[58:59], v201 offset:50176\n\t" \
    "ds_read_b64_tr_b16 a[60:61], v201 offset:57344\n\t" \
    "ds_read_b64_tr_b16 a[62:63], v201 offset:58368\n\t" \
    "ds_read_b64_tr_b16 a[128:129], v202 offset:49152\n\t" \
    "ds_read_b64_tr_b16 a[130:131], v202 offset:50176\n\t" \
    "ds_read_b64_tr_b16 a[132:133], v202 offset:57344\n\t" \
    "ds_read_b64_tr_b16 a[134:135], v202 offset:58368\n\t" \
    "s_add_u32 s46, s44, 3145728\n\t" \
    "s_addc_u32 s47, s45, 0\n\t" \
    "s_add_u32 m0, s82, 98304\n\t" \
    "s_nop 0\n\t" \
    "global_load_lds_dwordx4 v200, s[46:47]\n\t" \
    "s_add_u32 s46, s44, 3276800\n\t" \
    "s_addc_u32 s47, s45, 0\n\t" \
    "s_add_u32 m0, s82, 102400\n\t" \
    "s_nop 0\n\t" \
    "global_load_lds_dwordx4 v200, s[46:47]\n\t" \
    "s_add_u32 s46, s44, 3407872\n\t" \
    "s_addc_u32 s47, s45, 0\n\t" \
    "s_add_u32 m0, s82, 106496\n\t" \
    "s_nop 0\n\t" \
    "global_load_lds_dwordx4 v200, s[46:47]\n\t" \
    "s_add_u32 s46, s44, 3538944\n\t" \
    "s_addc_u32 s47, s45, 0\n\t" \
    "s_add_u32 m0, s82, 110592\n\t" \
    "s_nop 0\n\t" \
    "global_load_lds_dwordx4 v200, s[46:47]\n\t" \
    "v_mbcnt_lo_u32_b32 v160, -1, 0\n\t" \
    "v_mbcnt_hi_u32_b32 v160, -1, v160\n\t" \
    "v_lshlrev_b32 v160, 6, v160\n\t" \
    "v_add_u32 v160, 6225920, v160\n\t" \
    "v_mov_b32 v161, 6234112\n\t" \
    "global_load_dword v161, v161, s[36:37] sc1\n\t" \
    "global_load_dwordx2 v[162:163], v160, s[36:37] sc1\n\t" \
    "global_load_dword v164, v160, s[36:37] offset:2048 sc1\n\t" \
    "s_waitcnt vmcnt(31) lgkmcnt(0)\n\t" \
    "s_barrier\n\t" \
    "v_add_u32 v201, 0x10000, v201\n\t" \
    "v_add_u32 v202, 0x10000, v202\n\t" \
    "ds_read_b64_tr_b16 a[64:65], v201 offset:0\n\t" \
    "ds_read_b64_tr_b16 a[66:67], v201 offset:1024\n\t" \
    "ds_read_b64_tr_b16 a[68:69], v201 offset:8192\n\t" \
    "ds_read_b64_tr_b16 a[70:71], v201 offset:9216\n\t" \
    "ds_read_b64_tr_b16 a[136:137], v202 offset:0\n\t" \
    "ds_read_b64_tr_b16 a[138:139], v202 offset:1024\n\t" \
    "ds_read_b64_tr_b16 a[140:141], v202 offset:8192\n\t" \
    "ds_read_b64_tr_b16 a[142:143], v202 offset:9216\n\t" \
    "v_add_u32 v201, 0xfffe0000, v201\n\t" \
    "v_add_u32 v202, 0xfffe0000, v202\n\t" \
    "s_add_u32 s46, s44, 3670016\n\t" \
    "s_addc_u32 s47, s45, 0\n\t" \
    "s_add_u32 m0, s82, 114688\n\t" \
    "s_nop 0\n\t" \
    "global_load_lds_dwordx4 v200, s[46:47]\n\t" \
    "s_add_u32 s46, s44, 3801088\n\t" \
    "s_addc_u32 s47, s45, 0\n\t" \
    "s_add_u32 m0, s82, 118784\n\t" \
    "s_nop 0\n\t" \
    "global_load_lds_dwordx4 v200, s[46:47]\n\t" \
    "s_add_u32 s46, s44, 3932160\n\t" \
    "s_addc_u32 s47, s45, 0\n\t" \
    "s_add_u32 m0, s82, 122880\n\t" \
    "s_nop 0\n\t" \
    "global_load_lds_dwordx4 v200, s[46:47]\n\t" \
    "s_add_u32 s46, s44, 4063232\n\t" \
    "s_addc_u32 s47, s45, 0\n\t" \
    "s_add_u32 m0, s82, 126976\n\t" \
    "s_nop 0\n\t" \
    "global_load_lds_dwordx4 v200, s[46:47]\n\t" \
    "s_waitcnt lgkmcnt(0)\n\t" \
    "s_barrier\n\t" \
    "s_add_u32 s46, s44, 4194304\n\t" \
    "s_addc_u32 s47, s45, 0\n\t" \
    "s_add_u32 m0, s82, 131072\n\t" \
    "s_nop 0\n\t" \
    "global_load_lds_dwordx4 v200, s[46:47]\n\t" \
    "s_add_u32 s46, s44, 4325376\n\t" \
    "s_addc_u32 s47, s45, 0\n\t" \
    "s_add_u32 m0, s82, 135168\n\t" \
    "s_nop 0\n\t" \
    "global_load_lds_dwordx4 v200, s[46:47]\n\t" \
    "s_add_u32 s46, s44, 4456448\n\t" \
    "s_addc_u32 s47, s45, 0\n\t" \
    "s_add_u32 m0, s82, 139264\n\t" \
    "s_nop 0\n\t" \
    "global_load_lds_dwordx4 v200, s[46:47]\n\t" \
    "s_add_u32 s46, s44, 4587520\n\t" \
    "s_addc_u32 s47, s45, 0\n\t" \
    "s_add_u32 m0, s82, 143360\n\t" \
    "s_nop 0\n\t" \
    "global_load_lds_dwordx4 v200, s[46:47]\n\t" \
    "s_waitcnt vmcnt(35) lgkmcnt(0)\n\t" \
    "s_barrier\n\t" \
    "ds_read_b64_tr_b16 a[144:145], v201 offset:0\n\t" \
    "ds_read_b64_tr_b16 a[146:147], v201 offset:1024\n\t" \
    "ds_read_b64_tr_b16 a[148:149], v201 offset:8192\n\t" \
    "ds_read_b64_tr_b16 a[150:151], v201 offset:9216\n\t" \
    "ds_read_b64_tr_b16 a[216:217], v202 offset:0\n\t" \
    "ds_read_b64_tr_b16 a[218:219], v202 offset:1024\n\t" \
    "ds_read_b64_tr_b16 a[220:221], v202 offset:8192\n\t" \
    "ds_read_b64_tr_b16 a[222:223], v202 offset:9216\n\t" \
    "s_waitcnt vmcnt(31) lgkmcnt(0)\n\t" \
    "s_barrier\n\t" \
    "ds_read_b64_tr_b16 a[152:153], v201 offset:16384\n\t" \
    "ds_read_b64_tr_b16 a[154:155], v201 offset:17408\n\t" \
    "ds_read_b64_tr_b16 a[156:157], v201 offset:24576\n\t" \
    "ds_read_b64_tr_b16 a[158:159], v201 offset:25600\n\t" \
    "ds_read_b64_tr_b16 a[224:225], v202 offset:16384\n\t" \
    "ds_read_b64_tr_b16 a[226:227], v202 offset:17408\n\t" \
    "ds_read_b64_tr_b16 a[228:229], v202 offset:24576\n\t" \
    "ds_read_b64_tr_b16 a[230:231], v202 offset:25600\n\t" \
    "s_waitcnt vmcnt(27) lgkmcnt(0)\n\t" \
    "s_barrier\n\t" \
    "ds_read_b64_tr_b16 a[160:161], v201 offset:32768\n\t" \
    "ds_read_b64_tr_b16 a[162:163], v201 offset:33792\n\t" \
    "ds_read_b64_tr_b16 a[164:165], v201 offset:40960\n\t" \
    "ds_read_b64_tr_b16 a[166:167], v201 offset:41984\n\t" \
    "ds_read_b64_tr_b16 a[232:233], v202 offset:32768\n\t" \
    "ds_read_b64_tr_b16 a[234:235], v202 offset:33792\n\t" \
    "ds_read_b64_tr_b16 a[236:237], v202 offset:40960\n\t" \
    "ds_read_b64_tr_b16 a[238:239], v202 offset:41984\n\t" \
    "s_waitcnt vmcnt(23) lgkmcnt(0)\n\t" \
    "s_barrier\n\t" \
    "ds_read_b64_tr_b16 a[168:169], v201 offset:49152\n\t" \
    "ds_read_b64_tr_b16 a[170:171], v201 offset:50176\n\t" \
    "ds_read_b64_tr_b16 a[172:173], v201 offset:57344\n\t" \
    "ds_read_b64_tr_b16 a[174:175], v201 offset:58368\n\t" \
    "ds_read_b64_tr_b16 a[240:241], v202 offset:49152\n\t" \
    "ds_read_b64_tr_b16 a[242:243], v202 offset:50176\n\t" \
    "ds_read_b64_tr_b16 a[244:245], v202 offset:57344\n\t" \
    "ds_read_b64_tr_b16 a[246:247], v202 offset:58368\n\t" \
    "s_waitcnt vmcnt(19) lgkmcnt(0)\n\t" \
    "s_barrier\n\t" \
    "v_add_u32 v201, 0x10000, v201\n\t" \
    "v_add_u32 v202, 0x10000, v202\n\t" \
    "ds_read_b64_tr_b16 a[176:177], v201 offset:0\n\t" \
    "ds_read_b64_tr_b16 a[178:179], v201 offset:1024\n\t" \
    "ds_read_b64_tr_b16 a[180:181], v201 offset:8192\n\t" \
    "ds_read_b64_tr_b16 a[182:183], v201 offset:9216\n\t" \
    "ds_read_b64_tr_b16 a[248:249], v202 offset:0\n\t" \
    "ds_read_b64_tr_b16 a[250:251], v202 offset:1024\n\t" \
    "ds_read_b64_tr_b16 a[252:253], v202 offset:8192\n\t" \
    "ds_read_b64_tr_b16 a[254:255], v202 offset:9216\n\t" \
    "s_waitcnt vmcnt(15) lgkmcnt(0)\n\t" \
    "s_barrier\n\t" \
    "ds_read_b64_tr_b16 a[184:185], v201 offset:16384\n\t" \
    "ds_read_b64_tr_b16 a[186:187], v201 offset:17408\n\t" \
    "ds_read_b64_tr_b16 a[188:189], v201 offset:24576\n\t" \
    "ds_read_b64_tr_b16 a[190:191], v201 offset:25600\n\t" \
    "ds_read_b64_tr_b16 v[224:225], v202 offset:16384\n\t" \
    "ds_read_b64_tr_b16 v[226:227], v202 offset:17408\n\t" \
    "ds_read_b64_tr_b16 v[228:229], v202 offset:24576\n\t" \
    "ds_read_b64_tr_b16 v[230:231], v202 offset:25600\n\t" \
    "s_waitcnt vmcnt(11) lgkmcnt(0)\n\t" \
    "s_barrier\n\t" \
    "ds_read_b64_tr_b16 a[192:193], v201 offset:32768\n\t" \
    "ds_read_b64_tr_b16 a[194:195], v201 offset:33792\n\t" \
    "ds_read_b64_tr_b16 a[196:197], v201 offset:40960\n\t" \
    "ds_read_b64_tr_b16 a[198:199], v201 offset:41984\n\t" \
    "ds_read_b64_tr_b16 v[232:233], v202 offset:32768\n\t" \
    "ds_read_b64_tr_b16 v[234:235], v202 offset:33792\n\t" \
    "ds_read_b64_tr_b16 v[236:237], v202 offset:40960\n\t" \
    "ds_read_b64_tr_b16 v[238:239], v202 offset:41984\n\t" \
    "s_waitcnt vmcnt(8)\n\t" \
    "qk_C2_%=:\n\t" \
    "v_cmp_ne_u32 s[54:55], v162, v164\n\t" \
    "v_sub_u32 v162, v162, v161\n\t" \
    "v_cmp_ne_u32 vcc, 1, v162\n\t" \
    "s_nop 1\n\t" \
    "s_or_b64 vcc, vcc, s[54:55]\n\t" \
    "s_cbranch_scc0 qk_Cok3_%=\n\t" \
    "s_cmp_eq_u32 s68, 0\n\t" \
    "s_cbranch_scc1 qk_F5_%=\n\t" \
    "s_sub_u32 s68, s68, 1\n\t" \
    "s_sleep 6\n\t" \
    "s_branch qk_Cag4_%=\n\t" \
    "qk_F5_%=:\n\t" \
    "v_mov_b32 v164, 5767424\n\t" \
    "v_mov_b32 v165, 4\n\t" \
    "global_store_dword v164, v165, s[36:37] sc1\n\t" \
    "s_waitcnt vmcnt(0)\n\t" \
    "s_branch qk_Cok3_%=\n\t" \
    "qk_Cag4_%=:\n\t" \
    "global_load_dwordx2 v[162:163], v160, s[36:37] sc1\n\t" \
    "global_load_dword v164, v160, s[36:37] offset:2048 sc1\n\t" \
    "s_waitcnt vmcnt(0)\n\t" \
    "s_branch qk_C2_%=\n\t" \
    "qk_Cok3_%=:\n\t" \
    "v_readlane_b32 s69, v163, 2\n\t" \
    "v_readlane_b32 s70, v163, 3\n\t" \
    "s_nop 1\n\t" \
    "s_cmp_lt_u32 s70, s69\n\t" \
    "s_cselect_b32 s69, 0, 8192\n\t" \
    "s_add_u32 s100, s36, s69\n\t" \
    "s_addc_u32 s101, s37, 0\n\t" \
    "s_cmp_lg_u32 s66, 0\n\t" \
    "s_cbranch_scc1 qk_Ca6_%=\n\t" \
    "v_mov_b32 v160, 5768192\n\t" \
    "s_mov_b64 exec, 1\n\t" \
    "global_atomic_add v160, v162, s[36:37] sc1\n\t" \
    "s_mov_b64 exec, -1\n\t" \
    "qk_Ca6_%=:\n\t" \
    "v_mov_b32_dpp v160, v163 quad_perm:[1,0,3,2] row_mask:0xf bank_mask:0xf\n\t" \
    "v_cmp_lt_u32 s[44:45], v160, v163\n\t" \
    "s_nop 1\n\t" \
    "s_bitcmp1_b32 s44, 0\n\t" \
    "s_cselect_b32 s62, 8192, 0\n\t" \
    "s_bitcmp1_b32 s44, 2\n\t" \
    "s_cselect_b32 s63, 8192, 0\n\t" \
    "s_or_b32 s63, s63, 0x200000\n\t" \
    "v_min_u32 v160, v160, v163\n\t" \
    "v_cmp_lt_u32 vcc, 660, v160\n\t" \
    "s_nop 1\n\t" \
    "s_bcnt1_i32_b64 s69, vcc\n\t" \
    "s_cmp_gt_u32 s69, 16\n\t" \
    "s_cselect_b32 s69, 1, 0\n\t" \
    "s_mul_i32 s70, s69, 160\n\t" \
    "s_add_u32 s86, s86, s70\n\t" \
    "s_mul_i32 s70, s69, 536\n\t" \
    "s_add_u32 s87, s87, s70\n\t" \
    "s_mul_i32 s70, s69, 256\n\t" \
    "s_add_u32 s88, s88, s70\n\t" \
    "global_load_dword v25, v24, s[100:101] sc1\n\t" \
    "s_waitcnt vmcnt(5) lgkmcnt(0)\n\t" \
    "s_barrier\n\t" \
    "ds_read_b64_tr_b16 a[200:201], v201 offset:49152\n\t" \
    "ds_read_b64_tr_b16 a[202:203], v201 offset:50176\n\t" \
    "ds_read_b64_tr_b16 a[204:205], v201 offset:57344\n\t" \
    "ds_read_b64_tr_b16 a[206:207], v201 offset:58368\n\t" \
    "ds_read_b64_tr_b16 v[240:241], v202 offset:49152\n\t" \
    "ds_read_b64_tr_b16 v[242:243], v202 offset:50176\n\t" \
    "ds_read_b64_tr_b16 v[244:245], v202 offset:57344\n\t" \
    "ds_read_b64_tr_b16 v[246:247], v202 offset:58368\n\t" \
    "s_waitcnt vmcnt(1) lgkmcnt(0)\n\t" \
    "s_barrier\n\t" \
    "v_add_u32 v201, 0x10000, v201\n\t" \
    "v_add_u32 v202, 0x10000, v202\n\t" \
    "ds_read_b64_tr_b16 a[208:209], v201 offset:0\n\t" \
    "ds_read_b64_tr_b16 a[210:211], v201 offset:1024\n\t" \
    "ds_read_b64_tr_b16 a[212:213], v201 offset:8192\n\t" \
    "ds_read_b64_tr_b16 a[214:215], v201 offset:9216\n\t" \
    "ds_read_b64_tr_b16 v[248:249], v202 offset:0\n\t" \
    "ds_read_b64_tr_b16 v[250:251], v202 offset:1024\n\t" \
    "ds_read_b64_tr_b16 v[252:253], v202 offset:8192\n\t" \
    "ds_read_b64_tr_b16 v[254:255], v202 offset:9216\n\t" \
    "v_add_u32 v201, 0xfffe0000, v201\n\t" \
    "v_add_u32 v202, 0xfffe0000, v202\n\t" \
    "s_waitcnt lgkmcnt(0)\n\t" \
    "s_barrier\n\t" \
    "s_mul_i32 s70, s67, 7\n\t" \
    "s_add_u32 s69, s65, 0\n\t" \
    "s_xor_b32 s69, s69, s70\n\t" \
    "s_and_b32 s69, s69, 63\n\t" \
    "s_lshl_b32 s69, s69, 13\n\t" \
    "s_mul_i32 s70, s67, 524288\n\t" \
    "s_add_u32 s69, s69, s70\n\t" \
    "s_add_u32 s69, s69, 23068672\n\t" \
    "s_add_u32 s38, s36, s69\n\t" \
    "s_addc_u32 s39, s37, 0\n\t" \
    "v_mov_b32 v26, 0x3db504f3\n\t" \
    "v_mov_b32 v28, 0x3fb8aa3b\n\t" \
    "v_mov_b32 v27, 0x3db504f3\n\t" \
    "v_mov_b32 v29, 0x3fb8aa3b\n\t" \
    "v_xor_b32 v180, 65536, v18\n\t" \
    "v_xor_b32 v181, 131072, v18\n\t" \
    "v_xor_b32 v182, 196608, v18\n\t" \
    "v_add_u32 v34, 16384, v1\n\t" \
    "v_subrev_u32 v35, 16384, v2\n\t" \
    "s_mov_b32 s48, 0xaaaaaaaa\n\t" \
    "s_mov_b32 s49, 0xaaaaaaaa\n\t" \
    "s_mov_b32 s58, 0x00ff00ff\n\t" \
    "s_mov_b32 s59, 0x00ff00ff\n\t" \
    "v_mov_b32 v31, -1\n\t" \
    "s_mov_b32 s91, -1\n\t" \
    "ds_write_b32 v12, v31 offset:0\n\t" \
    "ds_write_b32 v12, v31 offset:64\n\t" \
    "s_waitcnt vmcnt(0)\n\t" \
    "s_cmp_gt_u32 s66, 1\n\t" \
    "s_cbranch_scc1 qk_nk7_%=\n\t" \
    "v_readfirstlane_b32 s83, v25\n\t" \
    "s_nop 1\n\t" \
    "qk_WF8_%=:\n\t" \
    "s_cmp_ge_i32 s83, s65\n\t" \
    "s_cbranch_scc1 qk_WFok9_%=\n\t" \
    "global_load_dword v160, v24, s[100:101] sc1\n\t" \
    "s_waitcnt vmcnt(0)\n\t" \
    "v_readfirstlane_b32 s83, v160\n\t" \
    "s_nop 1\n\t" \
    "s_cmp_ge_i32 s83, s65\n\t" \
    "s_cbranch_scc1 qk_WFok9_%=\n\t" \
    "s_cmp_eq_u32 s68, 0\n\t" \
    "s_cbranch_scc1 qk_F10_%=\n\t" \
    "s_sub_u32 s68, s68, 1\n\t" \
    "s_sleep 2\n\t" \
    "s_branch qk_WF8_%=\n\t" \
    "qk_F10_%=:\n\t" \
    "v_mov_b32 v160, 5767424\n\t" \
    "v_mov_b32 v161, 8\n\t" \
    "global_store_dword v160, v161, s[36:37] sc1\n\t" \
    "s_waitcnt vmcnt(0)\n\t" \
    "s_branch qk_WFok9_%=\n\t" \
    "qk_WFok9_%=:\n\t" \
    "global_load_dwordx4 v[100:103], v0, s[38:39] offset:0 sc1\n\t" \
    "global_load_dwordx4 v[104:107], v0, s[38:39] offset:2048 sc1\n\t" \
    "global_load_dwordx4 v[108:111], v15, s[38:39] offset:0 sc1\n\t" \
    "global_load_dwordx4 v[112:115], v15, s[38:39] offset:2048 sc1\n\t" \
    "global_load_dwordx4 v[116:119], v0, s[38:39] offset:1024 sc1\n\t" \
    "global_load_dwordx4 v[120:123], v0, s[38:39] offset:3072 sc1\n\t" \
    "global_load_dwordx4 v[124:127], v15, s[38:39] offset:1024 sc1\n\t" \
    "global_load_dwordx4 v[128:131], v15, s[38:39] offset:3072 sc1\n\t" \
    "qk_nk7_%=:\n\t" \
    "v_mov_b32 v33, v21\n\t" \
    "v_readlane_b32 s69, v21, 63\n\t" \
    "s_nop 1\n\t" \
    "v_mov_b32 v21, s69\n\t" \
    "s_mov_b32 s79, s74\n\t" \
    "s_mov_b32 s80, s74\n\t" \
    "s_cmp_eq_u32 s66, 1\n\t" \
    "s_cbranch_scc1 qk_loop12_%=\n\t" \
    "s_cmp_eq_u32 s66, 2\n\t" \
    "s_cbranch_scc1 qk_loop13_%=\n\t" \
    "s_cmp_eq_u32 s66, 3\n\t" \
    "s_cbranch_scc1 qk_loop14_%=\n\t" \
    "qk_loop11_%=:\n\t" \
    "v_mov_b32 v72, 0\n\t" \
    "v_mov_b32 v73, 0\n\t" \
    "v_mov_b32 v74, 0\n\t" \
    "v_mov_b32 v75, 0\n\t" \
    "v_mov_b32 v76, 0\n\t" \
    "v_mov_b32 v77, 0\n\t" \
    "v_mov_b32 v78, 0\n\t" \
    "v_mov_b32 v79, 0\n\t" \
    "v_mov_b32 v80, 0\n\t" \
    "v_mov_b32 v81, 0\n\t" \
    "v_mov_b32 v82, 0\n\t" \
    "v_mov_b32 v83, 0\n\t" \
    "v_mov_b32 v84, 0\n\t" \
    "v_mov_b32 v85, 0\n\t" \
    "v_mov_b32 v86, 0\n\t" \
    "v_mov_b32 v87, 0\n\t" \
    "v_mov_b32 v88, 0\n\t" \
    "v_mov_b32 v89, 0\n\t" \
    "v_mov_b32 v90, 0\n\t" \
    "v_mov_b32 v91, 0\n\t" \
    "v_mov_b32 v92, 0\n\t" \
    "v_mov_b32 v93, 0\n\t" \
    "v_mov_b32 v94, 0\n\t" \
    "v_mov_b32 v95, 0\n\t" \
    "v_mov_b32 v96, 0\n\t" \
    "v_mov_b32 v97, 0\n\t" \
    "v_mov_b32 v98, 0\n\t" \
    "v_mov_b32 v99, 0\n\t" \
    "s_add_u32 s69, s65, 0\n\t" \
    "s_and_b32 s69, s69, 7\n\t" \
    "s_lshl_b32 s69, s69, 18\n\t" \
    "s_add_u32 s69, s69, s62\n\t" \
    "s_add_u32 s46, s36, s69\n\t" \
    "s_addc_u32 s47, s37, 0\n\t" \
    "s_sub_u32 s70, s79, s74\n\t" \
    "s_max_i32 s70, s70, 0\n\t" \
    "s_min_i32 s70, s70, 16000\n\t" \
    "s_lshl_b32 s70, s70, 1\n\t" \
    "s_mul_hi_u32 s70, s70, s77\n\t" \
    "s_add_u32 s70, s70, s73\n\t" \
    ".p2align 6\n\t" \
    "s_nop 0\n\t" \
    "s_nop 0\n\t" \
    "s_nop 0\n\t" \
    "s_nop 0\n\t" \
    "s_nop 0\n\t" \
    "s_nop 0\n\t" \
    "qk_go16_%=:\n\t" \
    "s_getpc_b64 s[30:31]\n\t" \
    "qk_Wp18_%=:\n\t" \
    "s_add_u32 s30, s30, qk_We19_%=-qk_Wp18_%=\n\t" \
    "s_addc_u32 s31, s31, 0\n\t" \
    "s_sub_u32 s70, s70, 96\n\t" \
    "qk_W17_%=:\n\t" \
    "s_memtime s[56:57]\n\t" \
    "s_waitcnt lgkmcnt(0)\n\t" \
    "s_sub_u32 s69, s70, s56\n\t" \
    "s_cmp_gt_i32 s69, 80\n\t" \
    "s_cbranch_scc1 qk_W17_%=\n\t" \
    "s_max_i32 s69, s69, 0\n\t" \
    "s_and_b32 s69, s69, -4\n\t" \
    "s_sub_u32 s30, s30, s69\n\t" \
    "s_subb_u32 s31, s31, 0\n\t" \
    "s_setpc_b64 s[30:31]\n\t" \
    "s_nop 0\n\t" \
    "s_nop 0\n\t" \
    "s_nop 0\n\t" \
    "s_nop 0\n\t" \
    "s_nop 0\n\t" \
    "s_nop 0\n\t" \
    "s_nop 0\n\t" \
    "s_nop 0\n\t" \
    "s_nop 0\n\t" \
    "s_nop 0\n\t" \
    "s_nop 0\n\t" \
    "s_nop 0\n\t" \
    "s_nop 0\n\t" \
    "s_nop 0\n\t" \
    "s_nop 0\n\t" \
    "s_nop 0\n\t" \
    "s_nop 0\n\t" \
    "s_nop 0\n\t" \
    "s_nop 0\n\t" \
    "s_nop 0\n\t" \
    "qk_We19_%=:\n\t" \
    "qk_G20_%=:\n\t" \
    "global_load_dwordx4 v[36:39], v1, s[46:47] sc1\n\t" \
    "global_load_dword v161, v2, s[46:47] sc1\n\t" \
    "global_load_dwordx4 v[40:43], v23, s[46:47] sc1\n\t" \
    "s_waitcnt vmcnt(2)\n\t" \
    "qk_Garr25_%=:\n\t" \
    "ds_write_b128 v3, v[36:39]\n\t" \
    "v_smfmac_f32_16x16x64_bf16 v[72:75], v[36:39], a[0:7], v10\n\t" \
    "s_waitcnt vmcnt(1)\n\t" \
    "ds_write_b32 v22, v161 offset:19456\n\t" \
    "v_smfmac_f32_16x16x64_bf16 v[76:79], v[36:39], a[72:79], v10\n\t" \
    "s_waitcnt vmcnt(0)\n\t" \
    "ds_write_b128 v3, v[40:43] offset:1024\n\t" \
    "v_max3_u32 v160, v36, v40, v161\n\t" \
    "v_cmp_eq_u32 vcc, -1, v160\n\t" \
    "v_smfmac_f32_16x16x64_bf16 v[80:83], v[36:39], a[144:151], v10\n\t" \
    "v_smfmac_f32_16x16x64_bf16 v[84:87], v[36:39], a[216:223], v10\n\t" \
    "v_smfmac_f32_16x16x64_bf16 v[72:75], v[40:43], a[8:15], v10\n\t" \
    "s_cbranch_vccnz qk_Gbad21_%=\n\t" \
    "qk_Gc22_%=:\n\t" \
    "s_waitcnt lgkmcnt(0)\n\t" \
    "s_barrier\n\t" \
    "ds_read_b128 v[48:51], v4\n\t" \
    "ds_read_b128 v[52:55], v5\n\t" \
    "v_smfmac_f32_16x16x64_bf16 v[76:79], v[40:43], a[80:87], v10\n\t" \
    "ds_read_b128 v[56:59], v6\n\t" \
    "ds_read_b128 v[60:63], v7\n\t" \
    "v_smfmac_f32_16x16x64_bf16 v[80:83], v[40:43], a[152:159], v10\n\t" \
    "ds_read_b128 v[64:67], v8\n\t" \
    "v_smfmac_f32_16x16x64_bf16 v[84:87], v[40:43], a[224:231], v10\n\t" \
    "ds_read_b128 v[68:71], v9\n\t" \
    "s_waitcnt lgkmcnt(5)\n\t" \
    "v_smfmac_f32_16x16x64_bf16 v[72:75], v[48:51], a[16:23], v10\n\t" \
    "ds_read_b128 v[44:47], v0 offset:19456\n\t" \
    "s_waitcnt lgkmcnt(5)\n\t" \
    "v_smfmac_f32_16x16x64_bf16 v[72:75], v[52:55], a[24:31], v10\n\t" \
    "s_waitcnt lgkmcnt(4)\n\t" \
    "v_smfmac_f32_16x16x64_bf16 v[72:75], v[56:59], a[32:39], v10\n\t" \
    "s_waitcnt lgkmcnt(3)\n\t" \
    "v_smfmac_f32_16x16x64_bf16 v[72:75], v[60:63], a[40:47], v10\n\t" \
    "s_waitcnt lgkmcnt(2)\n\t" \
    "v_smfmac_f32_16x16x64_bf16 v[72:75], v[64:67], a[48:55], v10\n\t" \
    "s_waitcnt lgkmcnt(1)\n\t" \
    "v_smfmac_f32_16x16x64_bf16 v[72:75], v[68:71], a[56:63], v10\n\t" \
    "s_waitcnt lgkmcnt(0)\n\t" \
    "v_smfmac_f32_16x16x64_bf16 v[72:75], v[44:47], a[64:71], v10\n\t" \
    "s_nop 2\n\t" \
    "v_smfmac_f32_16x16x64_bf16 v[76:79], v[48:51], a[88:95], v10\n\t" \
    "s_nop 2\n\t" \
    "v_smfmac_f32_16x16x64_bf16 v[76:79], v[52:55], a[96:103], v10\n\t" \
    "v_add_f32 v72, v72, v74\n\t" \
    "v_smfmac_f32_16x16x64_bf16 v[76:79], v[56:59], a[104:111], v10\n\t" \
    "v_add_f32 v73, v73, v75\n\t" \
    "v_smfmac_f32_16x16x64_bf16 v[76:79], v[60:63], a[112:119], v10\n\t" \
    "v_cvt_pk_bf16_f32 v156, v72, v73\n\t" \
    "v_smfmac_f32_16x16x64_bf16 v[76:79], v[64:67], a[120:127], v10\n\t" \
    "ds_write_b32 v12, v156 offset:0\n\t" \
    "v_smfmac_f32_16x16x64_bf16 v[76:79], v[68:71], a[128:135], v10\n\t" \
    "v_smfmac_f32_16x16x64_bf16 v[76:79], v[44:47], a[136:143], v10\n\t" \
    "s_nop 2\n\t" \
    "v_smfmac_f32_16x16x64_bf16 v[80:83], v[48:51], a[160:167], v10\n\t" \
    "s_nop 2\n\t" \
    "v_smfmac_f32_16x16x64_bf16 v[80:83], v[52:55], a[168:175], v10\n\t" \
    "v_add_f32 v76, v76, v78\n\t" \
    "v_smfmac_f32_16x16x64_bf16 v[80:83], v[56:59], a[176:183], v10\n\t" \
    "v_add_f32 v77, v77, v79\n\t" \
    "v_smfmac_f32_16x16x64_bf16 v[80:83], v[60:63], a[184:191], v10\n\t" \
    "v_cvt_pk_bf16_f32 v157, v76, v77\n\t" \
    "v_smfmac_f32_16x16x64_bf16 v[80:83], v[64:67], a[192:199], v10\n\t" \
    "ds_write_b32 v12, v157 offset:64\n\t" \
    "v_smfmac_f32_16x16x64_bf16 v[80:83], v[68:71], a[200:207], v10\n\t" \
    "v_smfmac_f32_16x16x64_bf16 v[80:83], v[44:47], a[208:215], v10\n\t" \
    "s_nop 2\n\t" \
    "v_smfmac_f32_16x16x64_bf16 v[84:87], v[48:51], a[232:239], v10\n\t" \
    "s_nop 2\n\t" \
    "v_smfmac_f32_16x16x64_bf16 v[84:87], v[52:55], a[240:247], v10\n\t" \
    "v_add_f32 v80, v80, v82\n\t" \
    "v_smfmac_f32_16x16x64_bf16 v[84:87], v[56:59], a[248:255], v10\n\t" \
    "ds_read_b64_tr_b16 v[132:133], v13 offset:0\n\t" \
    "v_add_f32 v81, v81, v83\n\t" \
    "v_smfmac_f32_16x16x64_bf16 v[84:87], v[60:63], v[224:231], v10\n\t" \
    "ds_read_b64_tr_b16 v[134:135], v13 offset:128\n\t" \
    "v_cvt_pk_bf16_f32 v158, v80, v81\n\t" \
    "v_smfmac_f32_16x16x64_bf16 v[84:87], v[64:67], v[232:239], v10\n\t" \
    "ds_write_b32 v16, v158 offset:0\n\t" \
    "v_smfmac_f32_16x16x64_bf16 v[84:87], v[68:71], v[240:247], v10\n\t" \
    "ds_read_b64_tr_b16 v[136:137], v13 offset:1024\n\t" \
    "ds_read_b64_tr_b16 v[138:139], v13 offset:1152\n\t" \
    "v_smfmac_f32_16x16x64_bf16 v[84:87], v[44:47], v[248:255], v10\n\t" \
    "s_waitcnt lgkmcnt(3)\n\t" \
    "v_smfmac_f32_16x16x64_bf16 v[88:91], v[132:135], v[100:107], v10\n\t" \
    "s_nop 4\n\t" \
    "v_cmp_u_f32 vcc, v72, v73\n\t" \
    "v_pk_add_f32 v[84:85], v[84:85], v[86:87]\n\t" \
    "v_cvt_pk_bf16_f32 v159, v84, v85\n\t" \
    "ds_write_b32 v16, v159 offset:256\n\t" \
    "v_smfmac_f32_16x16x64_bf16 v[92:95], v[132:135], v[116:123], v10\n\t" \
    "s_waitcnt lgkmcnt(1)\n\t" \
    "v_smfmac_f32_16x16x64_bf16 v[88:91], v[136:139], v[108:115], v10\n\t" \
    "s_cbranch_vccnz qk_Gslow23_%=\n\t" \
    "qk_Gback24_%=:\n\t" \
    "v_smfmac_f32_16x16x64_bf16 v[92:95], v[136:139], v[124:131], v10\n\t" \
    "s_waitcnt lgkmcnt(0)\n\t" \
    "s_barrier\n\t" \
    "ds_read_b64_tr_b16 v[140:141], v17 offset:0\n\t" \
    "ds_read_b64_tr_b16 v[142:143], v17 offset:256\n\t" \
    "ds_read_b64_tr_b16 v[144:145], v17 offset:512\n\t" \
    "ds_read_b64_tr_b16 v[146:147], v17 offset:768\n\t" \
    "ds_read_b64_tr_b16 v[148:149], v17 offset:1024\n\t" \
    "ds_read_b64_tr_b16 v[150:151], v17 offset:1280\n\t" \
    "ds_read_b64_tr_b16 v[152:153], v17 offset:1536\n\t" \
    "ds_read_b64_tr_b16 v[154:155], v17 offset:1792\n\t" \
    "qk_qb27_%=:\n\t" \
    "v_add_f32 v168, v88, v90\n\t" \
    "v_add_f32 v169, v92, v94\n\t" \
    "v_cmp_u_f32 vcc, v168, v169\n\t" \
    "v_cvt_pk_bf16_f32 v168, 0, v168\n\t" \
    "v_cvt_pk_bf16_f32 v169, 0, v169\n\t" \
    "s_waitcnt lgkmcnt(4)\n\t" \
    "v_smfmac_f32_16x16x64_bf16 v[96:99], v[132:135], v[140:147], v10\n\t" \
    "s_waitcnt lgkmcnt(0)\n\t" \
    "v_smfmac_f32_16x16x64_bf16 v[96:99], v[136:139], v[148:155], v10\n\t" \
    "s_cbranch_vccnz qk_qs26_%=\n\t" \
    "qk_qb228_%=:\n\t" \
    "v_mul_f32 v168, v168, v26\n\t" \
    "v_mul_f32 v169, v169, v26\n\t" \
    "v_cvt_pk_bf16_f32 v168, 0, v168\n\t" \
    "v_cvt_pk_bf16_f32 v169, 0, v169\n\t" \
    "v_max_f32 v178, v168, v169\n\t" \
    "v_mul_f32 v168, v168, v28\n\t" \
    "v_mul_f32 v169, v169, v28\n\t" \
    "v_max_f32_dpp v178, v178, v178 row_mirror row_mask:0xf bank_mask:0xf\n\t" \
    "v_add_f32 v172, v96, v98\n\t" \
    "v_cvt_pk_bf16_f32 v172, 0, v172\n\t" \
    "v_mul_f32 v172, v172, v26\n\t" \
    "v_cvt_pk_bf16_f32 v172, 0, v172\n\t" \
    "v_max_f32 v173, v178, v172\n\t" \
    "v_mul_f32 v172, v172, v28\n\t" \
    "s_nop 0\n\t" \
    "v_max_f32_dpp v173, v173, v173 quad_perm:[1,0,3,2] row_mask:0xf bank_mask:0xf\n\t" \
    "s_nop 1\n\t" \
    "v_max_f32_dpp v173, v173, v173 quad_perm:[2,3,0,1] row_mask:0xf bank_mask:0xf\n\t" \
    "s_nop 1\n\t" \
    "v_max_f32_dpp v173, v173, v173 row_half_mirror row_mask:0xf bank_mask:0xf\n\t" \
    "v_fma_f32 v172, -v173, v28, v172\n\t" \
    "v_fma_f32 v168, -v173, v28, v168\n\t" \
    "v_fma_f32 v169, -v173, v28, v169\n\t" \
    "v_exp_f32 v172, v172\n\t" \
    "v_exp_f32 v168, v168\n\t" \
    "v_exp_f32 v169, v169\n\t" \
    "v_fma_f32 v173, v172, 0.5, v168\n\t" \
    "v_add_f32 v173, v173, v169\n\t" \
    "v_mov_b32_dpp v174, v172 quad_perm:[0,0,2,2] row_mask:0xf bank_mask:0xf\n\t" \
    "v_mov_b32_dpp v175, v172 quad_perm:[1,1,3,3] row_mask:0xf bank_mask:0xf\n\t" \
    "v_add_f32_dpp v173, v173, v173 quad_perm:[1,0,3,2] row_mask:0xf bank_mask:0xf\n\t" \
    "v_mov_b32_dpp v176, v168 quad_perm:[1,0,3,2] row_mask:0xf bank_mask:0xf\n\t" \
    "v_mov_b32_dpp v177, v169 quad_perm:[1,0,3,2] row_mask:0xf bank_mask:0xf\n\t" \
    "v_add_f32_dpp v173, v173, v173 quad_perm:[2,3,0,1] row_mask:0xf bank_mask:0xf\n\t" \
    "v_cndmask_b32 v168, v168, v177, s[48:49]\n\t" \
    "v_cndmask_b32 v169, v176, v169, s[48:49]\n\t" \
    "v_add_f32_dpp v173, v173, v173 row_half_mirror row_mask:0xf bank_mask:0xf\n\t" \
    "s_nop 1\n\t" \
    "v_add_f32_dpp v173, v173, v173 row_mirror row_mask:0xf bank_mask:0xf\n\t" \
    "v_rcp_f32 v173, v173\n\t" \
    "s_nop 0\n\t" \
    "v_pk_mul_f32 v[168:169], v[168:169], v[172:173] op_sel:[0,1] op_sel_hi:[1,1]\n\t" \
    "v_cvt_pk_bf16_f32 v176, v168, v169\n\t" \
    "global_store_dword v18, v176, s[46:47] sc1\n\t" \
    "global_store_dword v180, v176, s[46:47] sc1\n\t" \
    "global_store_dword v181, v176, s[46:47] sc1\n\t" \
    "global_store_dword v182, v176, s[46:47] sc1\n\t" \
    "v_pk_mul_f32 v[174:175], v[174:175], v[172:173] op_sel:[0,1] op_sel_hi:[1,1]\n\t" \
    "v_cvt_pk_bf16_f32 v177, v174, v175\n\t" \
    "global_store_dword v19, v177, s[46:47] sc1\n\t" \
    "s_cmp_eq_u32 s65, 63\n\t" \
    "s_cbranch_scc1 qk_last15_%=\n\t" \
    "v_readfirstlane_b32 s69, v25\n\t" \
    "s_add_u32 s65, s65, 1\n\t" \
    "s_max_i32 s83, s83, s69\n\t" \
    "s_cmp_gt_u32 s65, 7\n\t" \
    "s_cselect_b32 s83, 99, s83\n\t" \
    "v_mov_b32 v72, 0\n\t" \
    "v_mov_b32 v73, 0\n\t" \
    "v_mov_b32 v74, 0\n\t" \
    "v_mov_b32 v75, 0\n\t" \
    "v_mov_b32 v76, 0\n\t" \
    "v_mov_b32 v77, 0\n\t" \
    "v_mov_b32 v78, 0\n\t" \
    "v_mov_b32 v79, 0\n\t" \
    "v_mov_b32 v80, 0\n\t" \
    "v_mov_b32 v81, 0\n\t" \
    "v_mov_b32 v82, 0\n\t" \
    "v_mov_b32 v83, 0\n\t" \
    "v_mov_b32 v84, 0\n\t" \
    "v_mov_b32 v85, 0\n\t" \
    "v_mov_b32 v86, 0\n\t" \
    "v_mov_b32 v87, 0\n\t" \
    "v_mov_b32 v88, 0\n\t" \
    "v_mov_b32 v89, 0\n\t" \
    "v_mov_b32 v90, 0\n\t" \
    "v_mov_b32 v91, 0\n\t" \
    "v_mov_b32 v92, 0\n\t" \
    "v_mov_b32 v93, 0\n\t" \
    "v_mov_b32 v94, 0\n\t" \
    "v_mov_b32 v95, 0\n\t" \
    "v_mov_b32 v96, 0\n\t" \
    "v_mov_b32 v97, 0\n\t" \
    "v_mov_b32 v98, 0\n\t" \
    "v_mov_b32 v99, 0\n\t" \
    "s_add_u32 s69, s65, 0\n\t" \
    "s_and_b32 s69, s69, 7\n\t" \
    "s_lshl_b32 s69, s69, 18\n\t" \
    "s_add_u32 s69, s69, s62\n\t" \
    "s_add_u32 s46, s36, s69\n\t" \
    "s_addc_u32 s47, s37, 0\n\t" \
    "s_mul_i32 s70, s67, 7\n\t" \
    "s_add_u32 s69, s65, 0\n\t" \
    "s_xor_b32 s69, s69, s70\n\t" \
    "s_and_b32 s69, s69, 63\n\t" \
    "s_lshl_b32 s69, s69, 13\n\t" \
    "s_mul_i32 s70, s67, 524288\n\t" \
    "s_add_u32 s69, s69, s70\n\t" \
    "s_add_u32 s69, s69, 23068672\n\t" \
    "s_add_u32 s38, s36, s69\n\t" \
    "s_addc_u32 s39, s37, 0\n\t" \
    "qk_WF29_%=:\n\t" \
    "s_cmp_ge_i32 s83, s65\n\t" \
    "s_cbranch_scc1 qk_WFok30_%=\n\t" \
    "global_load_dword v160, v24, s[100:101] sc1\n\t" \
    "s_waitcnt vmcnt(0)\n\t" \
    "v_readfirstlane_b32 s83, v160\n\t" \
    "s_nop 1\n\t" \
    "s_cmp_ge_i32 s83, s65\n\t" \
    "s_cbranch_scc1 qk_WFok30_%=\n\t" \
    "s_cmp_eq_u32 s68, 0\n\t" \
    "s_cbranch_scc1 qk_F31_%=\n\t" \
    "s_sub_u32 s68, s68, 1\n\t" \
    "s_sleep 2\n\t" \
    "s_branch qk_WF29_%=\n\t" \
    "qk_F31_%=:\n\t" \
    "v_mov_b32 v160, 5767424\n\t" \
    "v_mov_b32 v161, 9\n\t" \
    "global_store_dword v160, v161, s[36:37] sc1\n\t" \
    "s_waitcnt vmcnt(0)\n\t" \
    "s_branch qk_WFok30_%=\n\t" \
    "qk_WFok30_%=:\n\t" \
    "global_load_dwordx4 v[100:103], v0, s[38:39] offset:0 sc1\n\t" \
    "global_load_dwordx4 v[104:107], v0, s[38:39] offset:2048 sc1\n\t" \
    "global_load_dwordx4 v[108:111], v15, s[38:39] offset:0 sc1\n\t" \
    "global_load_dwordx4 v[112:115], v15, s[38:39] offset:2048 sc1\n\t" \
    "global_load_dwordx4 v[116:119], v0, s[38:39] offset:1024 sc1\n\t" \
    "global_load_dwordx4 v[120:123], v0, s[38:39] offset:3072 sc1\n\t" \
    "global_load_dwordx4 v[124:127], v15, s[38:39] offset:1024 sc1\n\t" \
    "global_load_dwordx4 v[128:131], v15, s[38:39] offset:3072 sc1\n\t" \
    "s_cmp_gt_i32 s83, s65\n\t" \
    "s_cbranch_scc1 qk_nf32_%=\n\t" \
    "global_load_dword v25, v24, s[100:101] sc1\n\t" \
    "qk_nf32_%=:\n\t" \
    "s_barrier\n\t" \
    "ds_read_b32 v168, v11 offset:20480\n\t" \
    "s_waitcnt lgkmcnt(0)\n\t" \
    "v_readfirstlane_b32 s70, v168\n\t" \
    "ds_write_b32 v12, v31 offset:0\n\t" \
    "ds_write_b32 v12, v31 offset:64\n\t" \
    "s_branch qk_go16_%=\n\t" \
    "qk_Gbad21_%=:\n\t" \
    "s_waitcnt vmcnt(0) lgkmcnt(0)\n\t" \
    "s_nop 7\n\t" \
    "v_mov_b32 v72, 0\n\t" \
    "v_mov_b32 v73, 0\n\t" \
    "v_mov_b32 v74, 0\n\t" \
    "v_mov_b32 v75, 0\n\t" \
    "v_mov_b32 v76, 0\n\t" \
    "v_mov_b32 v77, 0\n\t" \
    "v_mov_b32 v78, 0\n\t" \
    "v_mov_b32 v79, 0\n\t" \
    "v_mov_b32 v80, 0\n\t" \
    "v_mov_b32 v81, 0\n\t" \
    "v_mov_b32 v82, 0\n\t" \
    "v_mov_b32 v83, 0\n\t" \
    "v_mov_b32 v84, 0\n\t" \
    "v_mov_b32 v85, 0\n\t" \
    "v_mov_b32 v86, 0\n\t" \
    "v_mov_b32 v87, 0\n\t" \
    "s_cmp_eq_u32 s68, 0\n\t" \
    "s_cbranch_scc1 qk_F33_%=\n\t" \
    "s_sub_u32 s68, s68, 1\n\t" \
    "s_branch qk_G20_%=\n\t" \
    "qk_F33_%=:\n\t" \
    "v_mov_b32 v160, 5767424\n\t" \
    "v_mov_b32 v161, 1\n\t" \
    "global_store_dword v160, v161, s[36:37] sc1\n\t" \
    "s_waitcnt vmcnt(0)\n\t" \
    "s_branch qk_Gc22_%=\n\t" \
    "qk_Gslow23_%=:\n\t" \
    "v_max_u32 v160, v36, v37\n\t" \
    "v_max3_u32 v160, v160, v38, v39\n\t" \
    "v_max3_u32 v160, v160, v40, v41\n\t" \
    "v_max3_u32 v160, v160, v42, v43\n\t" \
    "v_max3_u32 v160, v160, v44, v45\n\t" \
    "v_max3_u32 v160, v160, v46, v47\n\t" \
    "v_max3_u32 v160, v160, v48, v49\n\t" \
    "v_max3_u32 v160, v160, v50, v51\n\t" \
    "v_max3_u32 v160, v160, v52, v53\n\t" \
    "v_max3_u32 v160, v160, v54, v55\n\t" \
    "v_max3_u32 v160, v160, v56, v57\n\t" \
    "v_max3_u32 v160, v160, v58, v59\n\t" \
    "v_max3_u32 v160, v160, v60, v61\n\t" \
    "v_max3_u32 v160, v160, v62, v63\n\t" \
    "v_max3_u32 v160, v160, v64, v65\n\t" \
    "v_max3_u32 v160, v160, v66, v67\n\t" \
    "v_max3_u32 v160, v160, v68, v69\n\t" \
    "v_max3_u32 v160, v160, v70, v71\n\t" \
    "v_cmp_eq_u32 vcc, -1, v160\n\t" \
    "s_cbranch_vccz qk_Gback24_%=\n\t" \
    "s_waitcnt lgkmcnt(0)\n\t" \
    "v_sub_u32 v160, v1, v3\n\t" \
    "s_nop 0\n\t" \
    "v_readfirstlane_b32 s69, v160\n\t" \
    "v_add_u32 v163, 20480, v0\n\t" \
    "s_nop 7\n\t" \
    "s_add_u32 s30, s46, s69\n\t" \
    "s_addc_u32 s31, s47, 0\n\t" \
    "qk_Gag34_%=:\n\t" \
    "global_load_dwordx4 v[36:39], v1, s[46:47] sc1\n\t" \
    "global_load_dwordx4 v[40:43], v23, s[46:47] sc1\n\t" \
    "global_load_dwordx4 v[44:47], v163, s[30:31] sc1\n\t" \
    "global_load_dwordx4 v[48:51], v4, s[30:31] sc1\n\t" \
    "global_load_dwordx4 v[52:55], v5, s[30:31] sc1\n\t" \
    "global_load_dwordx4 v[56:59], v6, s[30:31] sc1\n\t" \
    "global_load_dwordx4 v[60:63], v7, s[30:31] sc1\n\t" \
    "global_load_dwordx4 v[64:67], v8, s[30:31] sc1\n\t" \
    "global_load_dwordx4 v[68:71], v9, s[30:31] sc1\n\t" \
    "s_waitcnt vmcnt(0)\n\t" \
    "v_max_u32 v160, v36, v37\n\t" \
    "v_max3_u32 v160, v160, v38, v39\n\t" \
    "v_max3_u32 v160, v160, v40, v41\n\t" \
    "v_max3_u32 v160, v160, v42, v43\n\t" \
    "v_max3_u32 v160, v160, v44, v45\n\t" \
    "v_max3_u32 v160, v160, v46, v47\n\t" \
    "v_max3_u32 v160, v160, v48, v49\n\t" \
    "v_max3_u32 v160, v160, v50, v51\n\t" \
    "v_max3_u32 v160, v160, v52, v53\n\t" \
    "v_max3_u32 v160, v160, v54, v55\n\t" \
    "v_max3_u32 v160, v160, v56, v57\n\t" \
    "v_max3_u32 v160, v160, v58, v59\n\t" \
    "v_max3_u32 v160, v160, v60, v61\n\t" \
    "v_max3_u32 v160, v160, v62, v63\n\t" \
    "v_max3_u32 v160, v160, v64, v65\n\t" \
    "v_max3_u32 v160, v160, v66, v67\n\t" \
    "v_max3_u32 v160, v160, v68, v69\n\t" \
    "v_max3_u32 v160, v160, v70, v71\n\t" \
    "v_cmp_eq_u32 vcc, -1, v160\n\t" \
    "s_cbranch_vccz qk_Gredo35_%=\n\t" \
    "s_cmp_eq_u32 s68, 0\n\t" \
    "s_cbranch_scc1 qk_F36_%=\n\t" \
    "s_sub_u32 s68, s68, 1\n\t" \
    "s_sleep 1\n\t" \
    "s_branch qk_Gag34_%=\n\t" \
    "qk_F36_%=:\n\t" \
    "v_mov_b32 v160, 5767424\n\t" \
    "v_mov_b32 v161, 2\n\t" \
    "global_store_dword v160, v161, s[36:37] sc1\n\t" \
    "s_waitcnt vmcnt(0)\n\t" \
    "s_branch qk_Gredo35_%=\n\t" \
    "qk_Gredo35_%=:\n\t" \
    "v_mov_b32 v72, 0\n\t" \
    "v_mov_b32 v73, 0\n\t" \
    "v_mov_b32 v74, 0\n\t" \
    "v_mov_b32 v75, 0\n\t" \
    "v_mov_b32 v76, 0\n\t" \
    "v_mov_b32 v77, 0\n\t" \
    "v_mov_b32 v78, 0\n\t" \
    "v_mov_b32 v79, 0\n\t" \
    "v_mov_b32 v80, 0\n\t" \
    "v_mov_b32 v81, 0\n\t" \
    "v_mov_b32 v82, 0\n\t" \
    "v_mov_b32 v83, 0\n\t" \
    "v_mov_b32 v84, 0\n\t" \
    "v_mov_b32 v85, 0\n\t" \
    "v_mov_b32 v86, 0\n\t" \
    "v_mov_b32 v87, 0\n\t" \
    "v_smfmac_f32_16x16x64_bf16 v[72:75], v[36:39], a[0:7], v10\n\t" \
    "v_smfmac_f32_16x16x64_bf16 v[72:75], v[40:43], a[8:15], v10\n\t" \
    "v_smfmac_f32_16x16x64_bf16 v[72:75], v[48:51], a[16:23], v10\n\t" \
    "v_smfmac_f32_16x16x64_bf16 v[72:75], v[52:55], a[24:31], v10\n\t" \
    "v_smfmac_f32_16x16x64_bf16 v[72:75], v[56:59], a[32:39], v10\n\t" \
    "v_smfmac_f32_16x16x64_bf16 v[72:75], v[60:63], a[40:47], v10\n\t" \
    "v_smfmac_f32_16x16x64_bf16 v[72:75], v[64:67], a[48:55], v10\n\t" \
    "v_smfmac_f32_16x16x64_bf16 v[72:75], v[68:71], a[56:63], v10\n\t" \
    "v_smfmac_f32_16x16x64_bf16 v[72:75], v[44:47], a[64:71], v10\n\t" \
    "v_smfmac_f32_16x16x64_bf16 v[76:79], v[36:39], a[72:79], v10\n\t" \
    "v_smfmac_f32_16x16x64_bf16 v[76:79], v[40:43], a[80:87], v10\n\t" \
    "v_smfmac_f32_16x16x64_bf16 v[76:79], v[48:51], a[88:95], v10\n\t" \
    "v_smfmac_f32_16x16x64_bf16 v[76:79], v[52:55], a[96:103], v10\n\t" \
    "v_smfmac_f32_16x16x64_bf16 v[76:79], v[56:59], a[104:111], v10\n\t" \
    "v_smfmac_f32_16x16x64_bf16 v[76:79], v[60:63], a[112:119], v10\n\t" \
    "v_smfmac_f32_16x16x64_bf16 v[76:79], v[64:67], a[120:127], v10\n\t" \
    "v_smfmac_f32_16x16x64_bf16 v[76:79], v[68:71], a[128:135], v10\n\t" \
    "v_smfmac_f32_16x16x64_bf16 v[76:79], v[44:47], a[136:143], v10\n\t" \
    "v_smfmac_f32_16x16x64_bf16 v[80:83], v[36:39], a[144:151], v10\n\t" \
    "v_smfmac_f32_16x16x64_bf16 v[80:83], v[40:43], a[152:159], v10\n\t" \
    "v_smfmac_f32_16x16x64_bf16 v[80:83], v[48:51], a[160:167], v10\n\t" \
    "v_smfmac_f32_16x16x64_bf16 v[80:83], v[52:55], a[168:175], v10\n\t" \
    "v_smfmac_f32_16x16x64_bf16 v[80:83], v[56:59], a[176:183], v10\n\t" \
    "v_smfmac_f32_16x16x64_bf16 v[80:83], v[60:63], a[184:191], v10\n\t" \
    "v_smfmac_f32_16x16x64_bf16 v[80:83], v[64:67], a[192:199], v10\n\t" \
    "v_smfmac_f32_16x16x64_bf16 v[80:83], v[68:71], a[200:207], v10\n\t" \
    "v_smfmac_f32_16x16x64_bf16 v[80:83], v[44:47], a[208:215], v10\n\t" \
    "v_smfmac_f32_16x16x64_bf16 v[84:87], v[36:39], a[216:223], v10\n\t" \
    "v_smfmac_f32_16x16x64_bf16 v[84:87], v[40:43], a[224:231], v10\n\t" \
    "v_smfmac_f32_16x16x64_bf16 v[84:87], v[48:51], a[232:239], v10\n\t" \
    "v_smfmac_f32_16x16x64_bf16 v[84:87], v[52:55], a[240:247], v10\n\t" \
    "v_smfmac_f32_16x16x64_bf16 v[84:87], v[56:59], a[248:255], v10\n\t" \
    "v_smfmac_f32_16x16x64_bf16 v[84:87], v[60:63], v[224:231], v10\n\t" \
    "v_smfmac_f32_16x16x64_bf16 v[84:87], v[64:67], v[232:239], v10\n\t" \
    "v_smfmac_f32_16x16x64_bf16 v[84:87], v[68:71], v[240:247], v10\n\t" \
    "v_smfmac_f32_16x16x64_bf16 v[84:87], v[44:47], v[248:255], v10\n\t" \
    "v_add_f32 v72, v72, v74\n\t" \
    "v_add_f32 v73, v73, v75\n\t" \
    "v_cvt_pk_bf16_f32 v156, v72, v73\n\t" \
    "ds_write_b32 v12, v156 offset:0\n\t" \
    "v_add_f32 v76, v76, v78\n\t" \
    "v_add_f32 v77, v77, v79\n\t" \
    "v_cvt_pk_bf16_f32 v157, v76, v77\n\t" \
    "ds_write_b32 v12, v157 offset:64\n\t" \
    "v_add_f32 v80, v80, v82\n\t" \
    "v_add_f32 v81, v81, v83\n\t" \
    "v_cvt_pk_bf16_f32 v158, v80, v81\n\t" \
    "ds_write_b32 v16, v158 offset:0\n\t" \
    "v_pk_add_f32 v[84:85], v[84:85], v[86:87]\n\t" \
    "v_cvt_pk_bf16_f32 v159, v84, v85\n\t" \
    "ds_write_b32 v16, v159 offset:256\n\t" \
    "s_branch qk_Gback24_%=\n\t" \
    "qk_qs26_%=:\n\t" \
    "s_cmp_eq_u32 s91, s65\n\t" \
    "s_cbranch_scc1 qk_qb228_%=\n\t" \
    "s_mov_b32 s91, s65\n\t" \
    "s_nop 7\n\t" \
    "ds_read_b64_tr_b16 v[132:133], v13 offset:0\n\t" \
    "ds_read_b64_tr_b16 v[134:135], v13 offset:128\n\t" \
    "ds_read_b64_tr_b16 v[136:137], v13 offset:1024\n\t" \
    "ds_read_b64_tr_b16 v[138:139], v13 offset:1152\n\t" \
    "v_mov_b32 v88, 0\n\t" \
    "v_mov_b32 v89, 0\n\t" \
    "v_mov_b32 v90, 0\n\t" \
    "v_mov_b32 v91, 0\n\t" \
    "v_mov_b32 v92, 0\n\t" \
    "v_mov_b32 v93, 0\n\t" \
    "v_mov_b32 v94, 0\n\t" \
    "v_mov_b32 v95, 0\n\t" \
    "v_mov_b32 v96, 0\n\t" \
    "v_mov_b32 v97, 0\n\t" \
    "v_mov_b32 v98, 0\n\t" \
    "v_mov_b32 v99, 0\n\t" \
    "s_waitcnt lgkmcnt(0)\n\t" \
    "v_smfmac_f32_16x16x64_bf16 v[88:91], v[132:135], v[100:107], v10\n\t" \
    "v_smfmac_f32_16x16x64_bf16 v[92:95], v[132:135], v[116:123], v10\n\t" \
    "v_smfmac_f32_16x16x64_bf16 v[88:91], v[136:139], v[108:115], v10\n\t" \
    "v_smfmac_f32_16x16x64_bf16 v[92:95], v[136:139], v[124:131], v10\n\t" \
    "s_nop 7\n\t" \
    "s_branch qk_qb27_%=\n\t" \
    "qk_last15_%=:\n\t" \
    "s_endpgm\n\t" \
    "qk_loop12_%=:\n\t" \
    "v_mov_b32 v72, 0\n\t" \
    "v_mov_b32 v73, 0\n\t" \
    "v_mov_b32 v74, 0\n\t" \
    "v_mov_b32 v75, 0\n\t" \
    "v_mov_b32 v76, 0\n\t" \
    "v_mov_b32 v77, 0\n\t" \
    "v_mov_b32 v78, 0\n\t" \
    "v_mov_b32 v79, 0\n\t" \
    "v_mov_b32 v80, 0\n\t" \
    "v_mov_b32 v81, 0\n\t" \
    "v_mov_b32 v82, 0\n\t" \
    "v_mov_b32 v83, 0\n\t" \
    "v_mov_b32 v84, 0\n\t" \
    "v_mov_b32 v85, 0\n\t" \
    "v_mov_b32 v86, 0\n\t" \
    "v_mov_b32 v87, 0\n\t" \
    "v_mov_b32 v88, 0\n\t" \
    "v_mov_b32 v89, 0\n\t" \
    "v_mov_b32 v90, 0\n\t" \
    "v_mov_b32 v91, 0\n\t" \
    "v_mov_b32 v92, 0\n\t" \
    "v_mov_b32 v93, 0\n\t" \
    "v_mov_b32 v94, 0\n\t" \
    "v_mov_b32 v95, 0\n\t" \
    "v_mov_b32 v96, 0\n\t" \
    "v_mov_b32 v97, 0\n\t" \
    "v_mov_b32 v98, 0\n\t" \
    "v_mov_b32 v99, 0\n\t" \
    "s_add_u32 s69, s65, 0\n\t" \
    "s_and_b32 s69, s69, 7\n\t" \
    "s_lshl_b32 s69, s69, 18\n\t" \
    "s_add_u32 s69, s69, s62\n\t" \
    "s_add_u32 s46, s36, s69\n\t" \
    "s_addc_u32 s47, s37, 0\n\t" \
    "s_sub_u32 s70, s79, s74\n\t" \
    "s_max_i32 s70, s70, 0\n\t" \
    "s_min_i32 s70, s70, 16000\n\t" \
    "s_lshl_b32 s70, s70, 1\n\t" \
    "s_mul_hi_u32 s70, s70, s77\n\t" \
    "s_add_u32 s70, s70, s73\n\t" \
    ".p2align 6\n\t" \
    "s_nop 0\n\t" \
    "s_nop 0\n\t" \
    "s_nop 0\n\t" \
    "s_nop 0\n\t" \
    "s_nop 0\n\t" \
    "s_nop 0\n\t" \
    "qk_go38_%=:\n\t" \
    "s_getpc_b64 s[30:31]\n\t" \
    "qk_Wp40_%=:\n\t" \
    "s_add_u32 s30, s30, qk_We41_%=-qk_Wp40_%=\n\t" \
    "s_addc_u32 s31, s31, 0\n\t" \
    "s_sub_u32 s70, s70, 96\n\t" \
    "qk_W39_%=:\n\t" \
    "s_memtime s[56:57]\n\t" \
    "s_waitcnt lgkmcnt(0)\n\t" \
    "s_sub_u32 s69, s70, s56\n\t" \
    "s_cmp_gt_i32 s69, 80\n\t" \
    "s_cbranch_scc1 qk_W39_%=\n\t" \
    "s_max_i32 s69, s69, 0\n\t" \
    "s_and_b32 s69, s69, -4\n\t" \
    "s_sub_u32 s30, s30, s69\n\t" \
    "s_subb_u32 s31, s31, 0\n\t" \
    "s_setpc_b64 s[30:31]\n\t" \
    "s_nop 0\n\t" \
    "s_nop 0\n\t" \
    "s_nop 0\n\t" \
    "s_nop 0\n\t" \
    "s_nop 0\n\t" \
    "s_nop 0\n\t" \
    "s_nop 0\n\t" \
    "s_nop 0\n\t" \
    "s_nop 0\n\t" \
    "s_nop 0\n\t" \
    "s_nop 0\n\t" \
    "s_nop 0\n\t" \
    "s_nop 0\n\t" \
    "s_nop 0\n\t" \
    "s_nop 0\n\t" \
    "s_nop 0\n\t" \
    "s_nop 0\n\t" \
    "s_nop 0\n\t" \
    "s_nop 0\n\t" \
    "s_nop 0\n\t" \
    "qk_We41_%=:\n\t" \
    "qk_G42_%=:\n\t" \
    "global_load_dwordx4 v[36:39], v1, s[46:47] sc1\n\t" \
    "global_load_dword v161, v2, s[46:47] sc1\n\t" \
    "global_load_dwordx4 v[40:43], v23, s[46:47] sc1\n\t" \
    "s_waitcnt vmcnt(2)\n\t" \
    "qk_Garr47_%=:\n\t" \
    "ds_write_b128 v3, v[36:39]\n\t" \
    "v_smfmac_f32_16x16x64_bf16 v[72:75], v[36:39], a[16:23], v10\n\t" \
    "s_waitcnt vmcnt(1)\n\t" \
    "ds_write_b32 v22, v161 offset:19712\n\t" \
    "v_smfmac_f32_16x16x64_bf16 v[76:79], v[36:39], a[88:95], v10\n\t" \
    "s_waitcnt vmcnt(0)\n\t" \
    "ds_write_b128 v3, v[40:43] offset:1024\n\t" \
    "v_max3_u32 v160, v36, v40, v161\n\t" \
    "v_cmp_eq_u32 vcc, -1, v160\n\t" \
    "v_smfmac_f32_16x16x64_bf16 v[80:83], v[36:39], a[160:167], v10\n\t" \
    "v_smfmac_f32_16x16x64_bf16 v[84:87], v[36:39], a[232:239], v10\n\t" \
    "v_smfmac_f32_16x16x64_bf16 v[72:75], v[40:43], a[24:31], v10\n\t" \
    "s_cbranch_vccnz qk_Gbad43_%=\n\t" \
    "qk_Gc44_%=:\n\t" \
    "s_waitcnt lgkmcnt(0)\n\t" \
    "s_barrier\n\t" \
    "ds_read_b128 v[48:51], v4\n\t" \
    "ds_read_b128 v[52:55], v5\n\t" \
    "v_smfmac_f32_16x16x64_bf16 v[76:79], v[40:43], a[96:103], v10\n\t" \
    "ds_read_b128 v[56:59], v6\n\t" \
    "ds_read_b128 v[60:63], v7\n\t" \
    "v_smfmac_f32_16x16x64_bf16 v[80:83], v[40:43], a[168:175], v10\n\t" \
    "ds_read_b128 v[64:67], v8\n\t" \
    "v_smfmac_f32_16x16x64_bf16 v[84:87], v[40:43], a[240:247], v10\n\t" \
    "ds_read_b128 v[68:71], v9\n\t" \
    "s_waitcnt lgkmcnt(5)\n\t" \
    "v_smfmac_f32_16x16x64_bf16 v[72:75], v[48:51], a[32:39], v10\n\t" \
    "ds_read_b128 v[44:47], v0 offset:19456\n\t" \
    "s_waitcnt lgkmcnt(5)\n\t" \
    "v_smfmac_f32_16x16x64_bf16 v[72:75], v[52:55], a[40:47], v10\n\t" \
    "s_waitcnt lgkmcnt(4)\n\t" \
    "v_smfmac_f32_16x16x64_bf16 v[72:75], v[56:59], a[48:55], v10\n\t" \
    "s_waitcnt lgkmcnt(3)\n\t" \
    "v_smfmac_f32_16x16x64_bf16 v[72:75], v[60:63], a[56:63], v10\n\t" \
    "s_waitcnt lgkmcnt(2)\n\t" \
    "v_smfmac_f32_16x16x64_bf16 v[72:75], v[64:67], a[0:7], v10\n\t" \
    "s_waitcnt lgkmcnt(1)\n\t" \
    "v_smfmac_f32_16x16x64_bf16 v[72:75], v[68:71], a[8:15], v10\n\t" \
    "s_waitcnt lgkmcnt(0)\n\t" \
    "v_smfmac_f32_16x16x64_bf16 v[72:75], v[44:47], a[64:71], v10\n\t" \
    "s_nop 2\n\t" \
    "v_smfmac_f32_16x16x64_bf16 v[76:79], v[48:51], a[104:111], v10\n\t" \
    "s_nop 2\n\t" \
    "v_smfmac_f32_16x16x64_bf16 v[76:79], v[52:55], a[112:119], v10\n\t" \
    "v_add_f32 v72, v72, v74\n\t" \
    "v_smfmac_f32_16x16x64_bf16 v[76:79], v[56:59], a[120:127], v10\n\t" \
    "v_add_f32 v73, v73, v75\n\t" \
    "v_smfmac_f32_16x16x64_bf16 v[76:79], v[60:63], a[128:135], v10\n\t" \
    "v_cvt_pk_bf16_f32 v156, v72, v73\n\t" \
    "v_smfmac_f32_16x16x64_bf16 v[76:79], v[64:67], a[72:79], v10\n\t" \
    "ds_write_b32 v12, v156 offset:0\n\t" \
    "v_smfmac_f32_16x16x64_bf16 v[76:79], v[68:71], a[80:87], v10\n\t" \
    "v_smfmac_f32_16x16x64_bf16 v[76:79], v[44:47], a[136:143], v10\n\t" \
    "s_nop 2\n\t" \
    "v_smfmac_f32_16x16x64_bf16 v[80:83], v[48:51], a[176:183], v10\n\t" \
    "s_nop 2\n\t" \
    "v_smfmac_f32_16x16x64_bf16 v[80:83], v[52:55], a[184:191], v10\n\t" \
    "v_add_f32 v76, v76, v78\n\t" \
    "v_smfmac_f32_16x16x64_bf16 v[80:83], v[56:59], a[192:199], v10\n\t" \
    "v_add_f32 v77, v77, v79\n\t" \
    "v_smfmac_f32_16x16x64_bf16 v[80:83], v[60:63], a[200:207], v10\n\t" \
    "v_cvt_pk_bf16_f32 v157, v76, v77\n\t" \
    "v_smfmac_f32_16x16x64_bf16 v[80:83], v[64:67], a[144:151], v10\n\t" \
    "ds_write_b32 v12, v157 offset:64\n\t" \
    "v_smfmac_f32_16x16x64_bf16 v[80:83], v[68:71], a[152:159], v10\n\t" \
    "v_smfmac_f32_16x16x64_bf16 v[80:83], v[44:47], a[208:215], v10\n\t" \
    "s_nop 2\n\t" \
    "v_smfmac_f32_16x16x64_bf16 v[84:87], v[48:51], a[248:255], v10\n\t" \
    "s_nop 2\n\t" \
    "v_smfmac_f32_16x16x64_bf16 v[84:87], v[52:55], v[224:231], v10\n\t" \
    "v_add_f32 v80, v80, v82\n\t" \
    "v_smfmac_f32_16x16x64_bf16 v[84:87], v[56:59], v[232:239], v10\n\t" \
    "ds_read_b64_tr_b16 v[132:133], v13 offset:0\n\t" \
    "v_add_f32 v81, v81, v83\n\t" \
    "v_smfmac_f32_16x16x64_bf16 v[84:87], v[60:63], v[240:247], v10\n\t" \
    "ds_read_b64_tr_b16 v[134:135], v13 offset:128\n\t" \
    "v_cvt_pk_bf16_f32 v158, v80, v81\n\t" \
    "v_smfmac_f32_16x16x64_bf16 v[84:87], v[64:67], a[216:223], v10\n\t" \
    "ds_write_b32 v16, v158 offset:0\n\t" \
    "v_smfmac_f32_16x16x64_bf16 v[84:87], v[68:71], a[224:231], v10\n\t" \
    "ds_read_b64_tr_b16 v[136:137], v13 offset:1024\n\t" \
    "ds_read_b64_tr_b16 v[138:139], v13 offset:1152\n\t" \
    "v_smfmac_f32_16x16x64_bf16 v[84:87], v[44:47], v[248:255], v10\n\t" \
    "s_waitcnt lgkmcnt(3)\n\t" \
    "v_smfmac_f32_16x16x64_bf16 v[88:91], v[132:135], v[100:107], v10\n\t" \
    "s_nop 4\n\t" \
    "v_cmp_u_f32 vcc, v72, v73\n\t" \
    "v_pk_add_f32 v[84:85], v[84:85], v[86:87]\n\t" \
    "v_cvt_pk_bf16_f32 v159, v84, v85\n\t" \
    "ds_write_b32 v16, v159 offset:256\n\t" \
    "v_smfmac_f32_16x16x64_bf16 v[92:95], v[132:135], v[116:123], v10\n\t" \
    "s_waitcnt lgkmcnt(1)\n\t" \
    "v_smfmac_f32_16x16x64_bf16 v[88:91], v[136:139], v[108:115], v10\n\t" \
    "s_cbranch_vccnz qk_Gslow45_%=\n\t" \
    "qk_Gback46_%=:\n\t" \
    "v_smfmac_f32_16x16x64_bf16 v[92:95], v[136:139], v[124:131], v10\n\t" \
    "s_waitcnt lgkmcnt(0)\n\t" \
    "s_barrier\n\t" \
    "ds_read_b64_tr_b16 v[140:141], v17 offset:0\n\t" \
    "ds_read_b64_tr_b16 v[142:143], v17 offset:256\n\t" \
    "ds_read_b64_tr_b16 v[144:145], v17 offset:512\n\t" \
    "ds_read_b64_tr_b16 v[146:147], v17 offset:768\n\t" \
    "ds_read_b64_tr_b16 v[148:149], v17 offset:1024\n\t" \
    "ds_read_b64_tr_b16 v[150:151], v17 offset:1280\n\t" \
    "ds_read_b64_tr_b16 v[152:153], v17 offset:1536\n\t" \
    "ds_read_b64_tr_b16 v[154:155], v17 offset:1792\n\t" \
    "qk_qb49_%=:\n\t" \
    "v_add_f32 v168, v89, v91\n\t" \
    "v_add_f32 v169, v93, v95\n\t" \
    "v_cmp_u_f32 vcc, v168, v169\n\t" \
    "v_cvt_pk_bf16_f32 v168, 0, v168\n\t" \
    "v_cvt_pk_bf16_f32 v169, 0, v169\n\t" \
    "s_waitcnt lgkmcnt(4)\n\t" \
    "v_smfmac_f32_16x16x64_bf16 v[96:99], v[132:135], v[140:147], v10\n\t" \
    "s_waitcnt lgkmcnt(0)\n\t" \
    "v_smfmac_f32_16x16x64_bf16 v[96:99], v[136:139], v[148:155], v10\n\t" \
    "s_cbranch_vccnz qk_qs48_%=\n\t" \
    "qk_qb250_%=:\n\t" \
    "v_mul_f32 v168, v168, v26\n\t" \
    "v_mul_f32 v169, v169, v26\n\t" \
    "v_cvt_pk_bf16_f32 v168, 0, v168\n\t" \
    "v_cvt_pk_bf16_f32 v169, 0, v169\n\t" \
    "v_max_f32 v178, v168, v169\n\t" \
    "v_mul_f32 v168, v168, v28\n\t" \
    "v_mul_f32 v169, v169, v28\n\t" \
    "v_max_f32_dpp v178, v178, v178 row_mirror row_mask:0xf bank_mask:0xf\n\t" \
    "v_add_f32 v172, v97, v99\n\t" \
    "v_cvt_pk_bf16_f32 v172, 0, v172\n\t" \
    "v_mul_f32 v172, v172, v26\n\t" \
    "v_cvt_pk_bf16_f32 v172, 0, v172\n\t" \
    "v_max_f32 v173, v178, v172\n\t" \
    "v_mul_f32 v172, v172, v28\n\t" \
    "s_nop 0\n\t" \
    "v_max_f32_dpp v173, v173, v173 quad_perm:[1,0,3,2] row_mask:0xf bank_mask:0xf\n\t" \
    "s_nop 1\n\t" \
    "v_max_f32_dpp v173, v173, v173 quad_perm:[2,3,0,1] row_mask:0xf bank_mask:0xf\n\t" \
    "s_nop 1\n\t" \
    "v_max_f32_dpp v173, v173, v173 row_half_mirror row_mask:0xf bank_mask:0xf\n\t" \
    "v_fma_f32 v172, -v173, v28, v172\n\t" \
    "v_fma_f32 v168, -v173, v28, v168\n\t" \
    "v_fma_f32 v169, -v173, v28, v169\n\t" \
    "v_exp_f32 v172, v172\n\t" \
    "v_exp_f32 v168, v168\n\t" \
    "v_exp_f32 v169, v169\n\t" \
    "v_fma_f32 v173, v172, 0.5, v168\n\t" \
    "v_add_f32 v173, v173, v169\n\t" \
    "v_mov_b32_dpp v174, v172 quad_perm:[0,0,2,2] row_mask:0xf bank_mask:0xf\n\t" \
    "v_mov_b32_dpp v175, v172 quad_perm:[1,1,3,3] row_mask:0xf bank_mask:0xf\n\t" \
    "v_add_f32_dpp v173, v173, v173 quad_perm:[1,0,3,2] row_mask:0xf bank_mask:0xf\n\t" \
    "v_mov_b32_dpp v176, v168 quad_perm:[1,0,3,2] row_mask:0xf bank_mask:0xf\n\t" \
    "v_mov_b32_dpp v177, v169 quad_perm:[1,0,3,2] row_mask:0xf bank_mask:0xf\n\t" \
    "v_add_f32_dpp v173, v173, v173 quad_perm:[2,3,0,1] row_mask:0xf bank_mask:0xf\n\t" \
    "v_cndmask_b32 v168, v168, v177, s[48:49]\n\t" \
    "v_cndmask_b32 v169, v176, v169, s[48:49]\n\t" \
    "v_add_f32_dpp v173, v173, v173 row_half_mirror row_mask:0xf bank_mask:0xf\n\t" \
    "s_nop 1\n\t" \
    "v_add_f32_dpp v173, v173, v173 row_mirror row_mask:0xf bank_mask:0xf\n\t" \
    "v_rcp_f32 v173, v173\n\t" \
    "s_nop 0\n\t" \
    "v_pk_mul_f32 v[168:169], v[168:169], v[172:173] op_sel:[0,1] op_sel_hi:[1,1]\n\t" \
    "v_cvt_pk_bf16_f32 v176, v168, v169\n\t" \
    "global_store_dword v18, v176, s[46:47] sc1\n\t" \
    "global_store_dword v180, v176, s[46:47] sc1\n\t" \
    "global_store_dword v181, v176, s[46:47] sc1\n\t" \
    "global_store_dword v182, v176, s[46:47] sc1\n\t" \
    "v_pk_mul_f32 v[174:175], v[174:175], v[172:173] op_sel:[0,1] op_sel_hi:[1,1]\n\t" \
    "v_cvt_pk_bf16_f32 v177, v174, v175\n\t" \
    "global_store_dword v19, v177, s[46:47] sc1\n\t" \
    "s_cmp_eq_u32 s65, 63\n\t" \
    "s_cbranch_scc1 qk_last37_%=\n\t" \
    "v_readfirstlane_b32 s69, v25\n\t" \
    "s_add_u32 s65, s65, 1\n\t" \
    "s_max_i32 s83, s83, s69\n\t" \
    "s_cmp_gt_u32 s65, 7\n\t" \
    "s_cselect_b32 s83, 99, s83\n\t" \
    "v_mov_b32 v72, 0\n\t" \
    "v_mov_b32 v73, 0\n\t" \
    "v_mov_b32 v74, 0\n\t" \
    "v_mov_b32 v75, 0\n\t" \
    "v_mov_b32 v76, 0\n\t" \
    "v_mov_b32 v77, 0\n\t" \
    "v_mov_b32 v78, 0\n\t" \
    "v_mov_b32 v79, 0\n\t" \
    "v_mov_b32 v80, 0\n\t" \
    "v_mov_b32 v81, 0\n\t" \
    "v_mov_b32 v82, 0\n\t" \
    "v_mov_b32 v83, 0\n\t" \
    "v_mov_b32 v84, 0\n\t" \
    "v_mov_b32 v85, 0\n\t" \
    "v_mov_b32 v86, 0\n\t" \
    "v_mov_b32 v87, 0\n\t" \
    "v_mov_b32 v88, 0\n\t" \
    "v_mov_b32 v89, 0\n\t" \
    "v_mov_b32 v90, 0\n\t" \
    "v_mov_b32 v91, 0\n\t" \
    "v_mov_b32 v92, 0\n\t" \
    "v_mov_b32 v93, 0\n\t" \
    "v_mov_b32 v94, 0\n\t" \
    "v_mov_b32 v95, 0\n\t" \
    "v_mov_b32 v96, 0\n\t" \
    "v_mov_b32 v97, 0\n\t" \
    "v_mov_b32 v98, 0\n\t" \
    "v_mov_b32 v99, 0\n\t" \
    "s_add_u32 s69, s65, 0\n\t" \
    "s_and_b32 s69, s69, 7\n\t" \
    "s_lshl_b32 s69, s69, 18\n\t" \
    "s_add_u32 s69, s69, s62\n\t" \
    "s_add_u32 s46, s36, s69\n\t" \
    "s_addc_u32 s47, s37, 0\n\t" \
    "s_mul_i32 s70, s67, 7\n\t" \
    "s_add_u32 s69, s65, 0\n\t" \
    "s_xor_b32 s69, s69, s70\n\t" \
    "s_and_b32 s69, s69, 63\n\t" \
    "s_lshl_b32 s69, s69, 13\n\t" \
    "s_mul_i32 s70, s67, 524288\n\t" \
    "s_add_u32 s69, s69, s70\n\t" \
    "s_add_u32 s69, s69, 23068672\n\t" \
    "s_add_u32 s38, s36, s69\n\t" \
    "s_addc_u32 s39, s37, 0\n\t" \
    "qk_WF51_%=:\n\t" \
    "s_cmp_ge_i32 s83, s65\n\t" \
    "s_cbranch_scc1 qk_WFok52_%=\n\t" \
    "global_load_dword v160, v24, s[100:101] sc1\n\t" \
    "s_waitcnt vmcnt(0)\n\t" \
    "v_readfirstlane_b32 s83, v160\n\t" \
    "s_nop 1\n\t" \
    "s_cmp_ge_i32 s83, s65\n\t" \
    "s_cbranch_scc1 qk_WFok52_%=\n\t" \
    "s_cmp_eq_u32 s68, 0\n\t" \
    "s_cbranch_scc1 qk_F53_%=\n\t" \
    "s_sub_u32 s68, s68, 1\n\t" \
    "s_sleep 2\n\t" \
    "s_branch qk_WF51_%=\n\t" \
    "qk_F53_%=:\n\t" \
    "v_mov_b32 v160, 5767424\n\t" \
    "v_mov_b32 v161, 9\n\t" \
    "global_store_dword v160, v161, s[36:37] sc1\n\t" \
    "s_waitcnt vmcnt(0)\n\t" \
    "s_branch qk_WFok52_%=\n\t" \
    "qk_WFok52_%=:\n\t" \
    "global_load_dwordx4 v[100:103], v0, s[38:39] offset:0 sc1\n\t" \
    "global_load_dwordx4 v[104:107], v0, s[38:39] offset:2048 sc1\n\t" \
    "global_load_dwordx4 v[108:111], v15, s[38:39] offset:0 sc1\n\t" \
    "global_load_dwordx4 v[112:115], v15, s[38:39] offset:2048 sc1\n\t" \
    "global_load_dwordx4 v[116:119], v0, s[38:39] offset:1024 sc1\n\t" \
    "global_load_dwordx4 v[120:123], v0, s[38:39] offset:3072 sc1\n\t" \
    "global_load_dwordx4 v[124:127], v15, s[38:39] offset:1024 sc1\n\t" \
    "global_load_dwordx4 v[128:131], v15, s[38:39] offset:3072 sc1\n\t" \
    "s_cmp_gt_i32 s83, s65\n\t" \
    "s_cbranch_scc1 qk_nf54_%=\n\t" \
    "global_load_dword v25, v24, s[100:101] sc1\n\t" \
    "qk_nf54_%=:\n\t" \
    "s_barrier\n\t" \
    "ds_read_b32 v168, v11 offset:20480\n\t" \
    "s_waitcnt lgkmcnt(0)\n\t" \
    "v_readfirstlane_b32 s70, v168\n\t" \
    "ds_write_b32 v12, v31 offset:0\n\t" \
    "ds_write_b32 v12, v31 offset:64\n\t" \
    "s_branch qk_go38_%=\n\t" \
    "qk_Gbad43_%=:\n\t" \
    "s_waitcnt vmcnt(0) lgkmcnt(0)\n\t" \
    "s_nop 7\n\t" \
    "v_mov_b32 v72, 0\n\t" \
    "v_mov_b32 v73, 0\n\t" \
    "v_mov_b32 v74, 0\n\t" \
    "v_mov_b32 v75, 0\n\t" \
    "v_mov_b32 v76, 0\n\t" \
    "v_mov_b32 v77, 0\n\t" \
    "v_mov_b32 v78, 0\n\t" \
    "v_mov_b32 v79, 0\n\t" \
    "v_mov_b32 v80, 0\n\t" \
    "v_mov_b32 v81, 0\n\t" \
    "v_mov_b32 v82, 0\n\t" \
    "v_mov_b32 v83, 0\n\t" \
    "v_mov_b32 v84, 0\n\t" \
    "v_mov_b32 v85, 0\n\t" \
    "v_mov_b32 v86, 0\n\t" \
    "v_mov_b32 v87, 0\n\t" \
    "s_cmp_eq_u32 s68, 0\n\t" \
    "s_cbranch_scc1 qk_F55_%=\n\t" \
    "s_sub_u32 s68, s68, 1\n\t" \
    "s_branch qk_G42_%=\n\t" \
    "qk_F55_%=:\n\t" \
    "v_mov_b32 v160, 5767424\n\t" \
    "v_mov_b32 v161, 1\n\t" \
    "global_store_dword v160, v161, s[36:37] sc1\n\t" \
    "s_waitcnt vmcnt(0)\n\t" \
    "s_branch qk_Gc44_%=\n\t" \
    "qk_Gslow45_%=:\n\t" \
    "v_max_u32 v160, v36, v37\n\t" \
    "v_max3_u32 v160, v160, v38, v39\n\t" \
    "v_max3_u32 v160, v160, v40, v41\n\t" \
    "v_max3_u32 v160, v160, v42, v43\n\t" \
    "v_max3_u32 v160, v160, v44, v45\n\t" \
    "v_max3_u32 v160, v160, v46, v47\n\t" \
    "v_max3_u32 v160, v160, v48, v49\n\t" \
    "v_max3_u32 v160, v160, v50, v51\n\t" \
    "v_max3_u32 v160, v160, v52, v53\n\t" \
    "v_max3_u32 v160, v160, v54, v55\n\t" \
    "v_max3_u32 v160, v160, v56, v57\n\t" \
    "v_max3_u32 v160, v160, v58, v59\n\t" \
    "v_max3_u32 v160, v160, v60, v61\n\t" \
    "v_max3_u32 v160, v160, v62, v63\n\t" \
    "v_max3_u32 v160, v160, v64, v65\n\t" \
    "v_max3_u32 v160, v160, v66, v67\n\t" \
    "v_max3_u32 v160, v160, v68, v69\n\t" \
    "v_max3_u32 v160, v160, v70, v71\n\t" \
    "v_cmp_eq_u32 vcc, -1, v160\n\t" \
    "s_cbranch_vccz qk_Gback46_%=\n\t" \
    "s_waitcnt lgkmcnt(0)\n\t" \
    "v_sub_u32 v160, v1, v3\n\t" \
    "s_nop 0\n\t" \
    "v_readfirstlane_b32 s69, v160\n\t" \
    "v_add_u32 v163, 20480, v0\n\t" \
    "s_nop 7\n\t" \
    "s_add_u32 s30, s46, s69\n\t" \
    "s_addc_u32 s31, s47, 0\n\t" \
    "qk_Gag56_%=:\n\t" \
    "global_load_dwordx4 v[36:39], v1, s[46:47] sc1\n\t" \
    "global_load_dwordx4 v[40:43], v23, s[46:47] sc1\n\t" \
    "global_load_dwordx4 v[44:47], v163, s[30:31] sc1\n\t" \
    "global_load_dwordx4 v[48:51], v4, s[30:31] sc1\n\t" \
    "global_load_dwordx4 v[52:55], v5, s[30:31] sc1\n\t" \
    "global_load_dwordx4 v[56:59], v6, s[30:31] sc1\n\t" \
    "global_load_dwordx4 v[60:63], v7, s[30:31] sc1\n\t" \
    "global_load_dwordx4 v[64:67], v8, s[30:31] sc1\n\t" \
    "global_load_dwordx4 v[68:71], v9, s[30:31] sc1\n\t" \
    "s_waitcnt vmcnt(0)\n\t" \
    "v_max_u32 v160, v36, v37\n\t" \
    "v_max3_u32 v160, v160, v38, v39\n\t" \
    "v_max3_u32 v160, v160, v40, v41\n\t" \
    "v_max3_u32 v160, v160, v42, v43\n\t" \
    "v_max3_u32 v160, v160, v44, v45\n\t" \
    "v_max3_u32 v160, v160, v46, v47\n\t" \
    "v_max3_u32 v160, v160, v48, v49\n\t" \
    "v_max3_u32 v160, v160, v50, v51\n\t" \
    "v_max3_u32 v160, v160, v52, v53\n\t" \
    "v_max3_u32 v160, v160, v54, v55\n\t" \
    "v_max3_u32 v160, v160, v56, v57\n\t" \
    "v_max3_u32 v160, v160, v58, v59\n\t" \
    "v_max3_u32 v160, v160, v60, v61\n\t" \
    "v_max3_u32 v160, v160, v62, v63\n\t" \
    "v_max3_u32 v160, v160, v64, v65\n\t" \
    "v_max3_u32 v160, v160, v66, v67\n\t" \
    "v_max3_u32 v160, v160, v68, v69\n\t" \
    "v_max3_u32 v160, v160, v70, v71\n\t" \
    "v_cmp_eq_u32 vcc, -1, v160\n\t" \
    "s_cbranch_vccz qk_Gredo57_%=\n\t" \
    "s_cmp_eq_u32 s68, 0\n\t" \
    "s_cbranch_scc1 qk_F58_%=\n\t" \
    "s_sub_u32 s68, s68, 1\n\t" \
    "s_sleep 1\n\t" \
    "s_branch qk_Gag56_%=\n\t" \
    "qk_F58_%=:\n\t" \
    "v_mov_b32 v160, 5767424\n\t" \
    "v_mov_b32 v161, 2\n\t" \
    "global_store_dword v160, v161, s[36:37] sc1\n\t" \
    "s_waitcnt vmcnt(0)\n\t" \
    "s_branch qk_Gredo57_%=\n\t" \
    "qk_Gredo57_%=:\n\t" \
    "v_mov_b32 v72, 0\n\t" \
    "v_mov_b32 v73, 0\n\t" \
    "v_mov_b32 v74, 0\n\t" \
    "v_mov_b32 v75, 0\n\t" \
    "v_mov_b32 v76, 0\n\t" \
    "v_mov_b32 v77, 0\n\t" \
    "v_mov_b32 v78, 0\n\t" \
    "v_mov_b32 v79, 0\n\t" \
    "v_mov_b32 v80, 0\n\t" \
    "v_mov_b32 v81, 0\n\t" \
    "v_mov_b32 v82, 0\n\t" \
    "v_mov_b32 v83, 0\n\t" \
    "v_mov_b32 v84, 0\n\t" \
    "v_mov_b32 v85, 0\n\t" \
    "v_mov_b32 v86, 0\n\t" \
    "v_mov_b32 v87, 0\n\t" \
    "v_smfmac_f32_16x16x64_bf16 v[72:75], v[36:39], a[16:23], v10\n\t" \
    "v_smfmac_f32_16x16x64_bf16 v[72:75], v[40:43], a[24:31], v10\n\t" \
    "v_smfmac_f32_16x16x64_bf16 v[72:75], v[48:51], a[32:39], v10\n\t" \
    "v_smfmac_f32_16x16x64_bf16 v[72:75], v[52:55], a[40:47], v10\n\t" \
    "v_smfmac_f32_16x16x64_bf16 v[72:75], v[56:59], a[48:55], v10\n\t" \
    "v_smfmac_f32_16x16x64_bf16 v[72:75], v[60:63], a[56:63], v10\n\t" \
    "v_smfmac_f32_16x16x64_bf16 v[72:75], v[64:67], a[0:7], v10\n\t" \
    "v_smfmac_f32_16x16x64_bf16 v[72:75], v[68:71], a[8:15], v10\n\t" \
    "v_smfmac_f32_16x16x64_bf16 v[72:75], v[44:47], a[64:71], v10\n\t" \
    "v_smfmac_f32_16x16x64_bf16 v[76:79], v[36:39], a[88:95], v10\n\t" \
    "v_smfmac_f32_16x16x64_bf16 v[76:79], v[40:43], a[96:103], v10\n\t" \
    "v_smfmac_f32_16x16x64_bf16 v[76:79], v[48:51], a[104:111], v10\n\t" \
    "v_smfmac_f32_16x16x64_bf16 v[76:79], v[52:55], a[112:119], v10\n\t" \
    "v_smfmac_f32_16x16x64_bf16 v[76:79], v[56:59], a[120:127], v10\n\t" \
    "v_smfmac_f32_16x16x64_bf16 v[76:79], v[60:63], a[128:135], v10\n\t" \
    "v_smfmac_f32_16x16x64_bf16 v[76:79], v[64:67], a[72:79], v10\n\t" \
    "v_smfmac_f32_16x16x64_bf16 v[76:79], v[68:71], a[80:87], v10\n\t" \
    "v_smfmac_f32_16x16x64_bf16 v[76:79], v[44:47], a[136:143], v10\n\t" \
    "v_smfmac_f32_16x16x64_bf16 v[80:83], v[36:39], a[160:167], v10\n\t" \
    "v_smfmac_f32_16x16x64_bf16 v[80:83], v[40:43], a[168:175], v10\n\t" \
    "v_smfmac_f32_16x16x64_bf16 v[80:83], v[48:51], a[176:183], v10\n\t" \
    "v_smfmac_f32_16x16x64_bf16 v[80:83], v[52:55], a[184:191], v10\n\t" \
    "v_smfmac_f32_16x16x64_bf16 v[80:83], v[56:59], a[192:199], v10\n\t" \
    "v_smfmac_f32_16x16x64_bf16 v[80:83], v[60:63], a[200:207], v10\n\t" \
    "v_smfmac_f32_16x16x64_bf16 v[80:83], v[64:67], a[144:151], v10\n\t" \
    "v_smfmac_f32_16x16x64_bf16 v[80:83], v[68:71], a[152:159], v10\n\t" \
    "v_smfmac_f32_16x16x64_bf16 v[80:83], v[44:47], a[208:215], v10\n\t" \
    "v_smfmac_f32_16x16x64_bf16 v[84:87], v[36:39], a[232:239], v10\n\t" \
    "v_smfmac_f32_16x16x64_bf16 v[84:87], v[40:43], a[240:247], v10\n\t" \
    "v_smfmac_f32_16x16x64_bf16 v[84:87], v[48:51], a[248:255], v10\n\t" \
    "v_smfmac_f32_16x16x64_bf16 v[84:87], v[52:55], v[224:231], v10\n\t" \
    "v_smfmac_f32_16x16x64_bf16 v[84:87], v[56:59], v[232:239], v10\n\t" \
    "v_smfmac_f32_16x16x64_bf16 v[84:87], v[60:63], v[240:247], v10\n\t" \
    "v_smfmac_f32_16x16x64_bf16 v[84:87], v[64:67], a[216:223], v10\n\t" \
    "v_smfmac_f32_16x16x64_bf16 v[84:87], v[68:71], a[224:231], v10\n\t" \
    "v_smfmac_f32_16x16x64_bf16 v[84:87], v[44:47], v[248:255], v10\n\t" \
    "v_add_f32 v72, v72, v74\n\t" \
    "v_add_f32 v73, v73, v75\n\t" \
    "v_cvt_pk_bf16_f32 v156, v72, v73\n\t" \
    "ds_write_b32 v12, v156 offset:0\n\t" \
    "v_add_f32 v76, v76, v78\n\t" \
    "v_add_f32 v77, v77, v79\n\t" \
    "v_cvt_pk_bf16_f32 v157, v76, v77\n\t" \
    "ds_write_b32 v12, v157 offset:64\n\t" \
    "v_add_f32 v80, v80, v82\n\t" \
    "v_add_f32 v81, v81, v83\n\t" \
    "v_cvt_pk_bf16_f32 v158, v80, v81\n\t" \
    "ds_write_b32 v16, v158 offset:0\n\t" \
    "v_pk_add_f32 v[84:85], v[84:85], v[86:87]\n\t" \
    "v_cvt_pk_bf16_f32 v159, v84, v85\n\t" \
    "ds_write_b32 v16, v159 offset:256\n\t" \
    "s_branch qk_Gback46_%=\n\t" \
    "qk_qs48_%=:\n\t" \
    "s_cmp_eq_u32 s91, s65\n\t" \
    "s_cbranch_scc1 qk_qb250_%=\n\t" \
    "s_mov_b32 s91, s65\n\t" \
    "s_nop 7\n\t" \
    "ds_read_b64_tr_b16 v[132:133], v13 offset:0\n\t" \
    "ds_read_b64_tr_b16 v[134:135], v13 offset:128\n\t" \
    "ds_read_b64_tr_b16 v[136:137], v13 offset:1024\n\t" \
    "ds_read_b64_tr_b16 v[138:139], v13 offset:1152\n\t" \
    "v_mov_b32 v88, 0\n\t" \
    "v_mov_b32 v89, 0\n\t" \
    "v_mov_b32 v90, 0\n\t" \
    "v_mov_b32 v91, 0\n\t" \
    "v_mov_b32 v92, 0\n\t" \
    "v_mov_b32 v93, 0\n\t" \
    "v_mov_b32 v94, 0\n\t" \
    "v_mov_b32 v95, 0\n\t" \
    "v_mov_b32 v96, 0\n\t" \
    "v_mov_b32 v97, 0\n\t" \
    "v_mov_b32 v98, 0\n\t" \
    "v_mov_b32 v99, 0\n\t" \
    "s_waitcnt lgkmcnt(0)\n\t" \
    "v_smfmac_f32_16x16x64_bf16 v[88:91], v[132:135], v[100:107], v10\n\t" \
    "v_smfmac_f32_16x16x64_bf16 v[92:95], v[132:135], v[116:123], v10\n\t" \
    "v_smfmac_f32_16x16x64_bf16 v[88:91], v[136:139], v[108:115], v10\n\t" \
    "v_smfmac_f32_16x16x64_bf16 v[92:95], v[136:139], v[124:131], v10\n\t" \
    "s_nop 7\n\t" \
    "s_branch qk_qb49_%=\n\t" \
    "qk_last37_%=:\n\t" \
    "s_endpgm\n\t" \
    "qk_loop13_%=:\n\t" \
    "v_mov_b32 v72, 0\n\t" \
    "v_mov_b32 v73, 0\n\t" \
    "v_mov_b32 v74, 0\n\t" \
    "v_mov_b32 v75, 0\n\t" \
    "v_mov_b32 v76, 0\n\t" \
    "v_mov_b32 v77, 0\n\t" \
    "v_mov_b32 v78, 0\n\t" \
    "v_mov_b32 v79, 0\n\t" \
    "v_mov_b32 v80, 0\n\t" \
    "v_mov_b32 v81, 0\n\t" \
    "v_mov_b32 v82, 0\n\t" \
    "v_mov_b32 v83, 0\n\t" \
    "v_mov_b32 v84, 0\n\t" \
    "v_mov_b32 v85, 0\n\t" \
    "v_mov_b32 v86, 0\n\t" \
    "v_mov_b32 v87, 0\n\t" \
    "s_add_u32 s69, s65, 0\n\t" \
    "s_and_b32 s69, s69, 7\n\t" \
    "s_lshl_b32 s69, s69, 18\n\t" \
    "s_add_u32 s69, s69, s62\n\t" \
    "s_add_u32 s46, s36, s69\n\t" \
    "s_addc_u32 s47, s37, 0\n\t" \
    "s_mul_i32 s70, s67, 7\n\t" \
    "s_add_u32 s69, s65, 0\n\t" \
    "s_xor_b32 s69, s69, s70\n\t" \
    "s_and_b32 s69, s69, 63\n\t" \
    "s_lshl_b32 s69, s69, 12\n\t" \
    "s_mul_i32 s70, s67, 262144\n\t" \
    "s_add_u32 s69, s69, s70\n\t" \
    "s_add_u32 s69, s69, 39845888\n\t" \
    "s_add_u32 s40, s36, s69\n\t" \
    "s_addc_u32 s41, s37, 0\n\t" \
    "s_sub_u32 s91, s65, 1\n\t" \
    "v_mov_b32 v25, s91\n\t" \
    "s_sub_u32 s70, s79, s74\n\t" \
    "s_max_i32 s70, s70, 0\n\t" \
    "s_min_i32 s70, s70, 16000\n\t" \
    "s_lshl_b32 s70, s70, 1\n\t" \
    "s_mul_hi_u32 s70, s70, s77\n\t" \
    "s_add_u32 s70, s70, s73\n\t" \
    ".p2align 6\n\t" \
    "s_nop 0\n\t" \
    "s_nop 0\n\t" \
    "s_nop 0\n\t" \
    "s_nop 0\n\t" \
    "s_nop 0\n\t" \
    "s_nop 0\n\t" \
    "qk_go60_%=:\n\t" \
    "s_getpc_b64 s[30:31]\n\t" \
    "qk_Wp64_%=:\n\t" \
    "s_add_u32 s30, s30, qk_We65_%=-qk_Wp64_%=\n\t" \
    "s_addc_u32 s31, s31, 0\n\t" \
    "s_sub_u32 s70, s70, 96\n\t" \
    "qk_W63_%=:\n\t" \
    "s_memtime s[56:57]\n\t" \
    "s_waitcnt lgkmcnt(0)\n\t" \
    "s_sub_u32 s69, s70, s56\n\t" \
    "s_cmp_gt_i32 s69, 80\n\t" \
    "s_cbranch_scc1 qk_W63_%=\n\t" \
    "s_max_i32 s69, s69, 0\n\t" \
    "s_and_b32 s69, s69, -4\n\t" \
    "s_sub_u32 s30, s30, s69\n\t" \
    "s_subb_u32 s31, s31, 0\n\t" \
    "s_setpc_b64 s[30:31]\n\t" \
    "s_nop 0\n\t" \
    "s_nop 0\n\t" \
    "s_nop 0\n\t" \
    "s_nop 0\n\t" \
    "s_nop 0\n\t" \
    "s_nop 0\n\t" \
    "s_nop 0\n\t" \
    "s_nop 0\n\t" \
    "s_nop 0\n\t" \
    "s_nop 0\n\t" \
    "s_nop 0\n\t" \
    "s_nop 0\n\t" \
    "s_nop 0\n\t" \
    "s_nop 0\n\t" \
    "s_nop 0\n\t" \
    "s_nop 0\n\t" \
    "s_nop 0\n\t" \
    "s_nop 0\n\t" \
    "s_nop 0\n\t" \
    "s_nop 0\n\t" \
    "qk_We65_%=:\n\t" \
    "qk_G66_%=:\n\t" \
    "global_load_dwordx4 v[36:39], v1, s[46:47] sc1\n\t" \
    "global_load_dword v161, v2, s[46:47] sc1\n\t" \
    "global_load_dwordx4 v[40:43], v23, s[46:47] sc1\n\t" \
    "s_waitcnt vmcnt(2)\n\t" \
    "qk_Garr71_%=:\n\t" \
    "ds_write_b128 v3, v[36:39]\n\t" \
    "v_smfmac_f32_16x16x64_bf16 v[72:75], v[36:39], a[32:39], v10\n\t" \
    "s_waitcnt vmcnt(1)\n\t" \
    "ds_write_b32 v22, v161 offset:19968\n\t" \
    "v_smfmac_f32_16x16x64_bf16 v[76:79], v[36:39], a[104:111], v10\n\t" \
    "s_waitcnt vmcnt(0)\n\t" \
    "ds_write_b128 v3, v[40:43] offset:1024\n\t" \
    "v_max3_u32 v160, v36, v40, v161\n\t" \
    "v_cmp_eq_u32 vcc, -1, v160\n\t" \
    "v_smfmac_f32_16x16x64_bf16 v[80:83], v[36:39], a[176:183], v10\n\t" \
    "v_smfmac_f32_16x16x64_bf16 v[84:87], v[36:39], a[248:255], v10\n\t" \
    "v_smfmac_f32_16x16x64_bf16 v[72:75], v[40:43], a[40:47], v10\n\t" \
    "s_cbranch_vccnz qk_Gbad67_%=\n\t" \
    "qk_Gc68_%=:\n\t" \
    "s_cmp_ge_u32 s91, 57\n\t" \
    "s_cbranch_scc1 qk_kf61_%=\n\t" \
    "qk_kb62_%=:\n\t" \
    "s_waitcnt lgkmcnt(0)\n\t" \
    "s_barrier\n\t" \
    "ds_read_b128 v[48:51], v4\n\t" \
    "ds_read_b128 v[52:55], v5\n\t" \
    "v_smfmac_f32_16x16x64_bf16 v[76:79], v[40:43], a[112:119], v10\n\t" \
    "ds_read_b128 v[56:59], v6\n\t" \
    "ds_read_b128 v[60:63], v7\n\t" \
    "v_smfmac_f32_16x16x64_bf16 v[80:83], v[40:43], a[184:191], v10\n\t" \
    "ds_read_b128 v[64:67], v8\n\t" \
    "v_smfmac_f32_16x16x64_bf16 v[84:87], v[40:43], v[224:231], v10\n\t" \
    "ds_read_b128 v[68:71], v9\n\t" \
    "s_waitcnt lgkmcnt(5)\n\t" \
    "v_smfmac_f32_16x16x64_bf16 v[72:75], v[48:51], a[48:55], v10\n\t" \
    "ds_read_b128 v[44:47], v0 offset:19456\n\t" \
    "s_waitcnt lgkmcnt(5)\n\t" \
    "v_smfmac_f32_16x16x64_bf16 v[72:75], v[52:55], a[56:63], v10\n\t" \
    "s_waitcnt lgkmcnt(4)\n\t" \
    "v_smfmac_f32_16x16x64_bf16 v[72:75], v[56:59], a[0:7], v10\n\t" \
    "s_waitcnt lgkmcnt(3)\n\t" \
    "v_smfmac_f32_16x16x64_bf16 v[72:75], v[60:63], a[8:15], v10\n\t" \
    "s_waitcnt lgkmcnt(2)\n\t" \
    "v_smfmac_f32_16x16x64_bf16 v[72:75], v[64:67], a[16:23], v10\n\t" \
    "s_waitcnt lgkmcnt(1)\n\t" \
    "v_smfmac_f32_16x16x64_bf16 v[72:75], v[68:71], a[24:31], v10\n\t" \
    "s_waitcnt lgkmcnt(0)\n\t" \
    "v_smfmac_f32_16x16x64_bf16 v[72:75], v[44:47], a[64:71], v10\n\t" \
    "s_nop 2\n\t" \
    "v_smfmac_f32_16x16x64_bf16 v[76:79], v[48:51], a[120:127], v10\n\t" \
    "s_nop 2\n\t" \
    "v_smfmac_f32_16x16x64_bf16 v[76:79], v[52:55], a[128:135], v10\n\t" \
    "v_add_f32 v72, v72, v74\n\t" \
    "v_smfmac_f32_16x16x64_bf16 v[76:79], v[56:59], a[72:79], v10\n\t" \
    "v_add_f32 v73, v73, v75\n\t" \
    "v_smfmac_f32_16x16x64_bf16 v[76:79], v[60:63], a[80:87], v10\n\t" \
    "v_cvt_pk_bf16_f32 v156, v72, v73\n\t" \
    "v_smfmac_f32_16x16x64_bf16 v[76:79], v[64:67], a[88:95], v10\n\t" \
    "ds_write_b32 v12, v156 offset:0\n\t" \
    "v_smfmac_f32_16x16x64_bf16 v[76:79], v[68:71], a[96:103], v10\n\t" \
    "v_smfmac_f32_16x16x64_bf16 v[76:79], v[44:47], a[136:143], v10\n\t" \
    "s_nop 2\n\t" \
    "v_smfmac_f32_16x16x64_bf16 v[80:83], v[48:51], a[192:199], v10\n\t" \
    "s_nop 2\n\t" \
    "v_smfmac_f32_16x16x64_bf16 v[80:83], v[52:55], a[200:207], v10\n\t" \
    "v_add_f32 v76, v76, v78\n\t" \
    "v_smfmac_f32_16x16x64_bf16 v[80:83], v[56:59], a[144:151], v10\n\t" \
    "v_add_f32 v77, v77, v79\n\t" \
    "v_smfmac_f32_16x16x64_bf16 v[80:83], v[60:63], a[152:159], v10\n\t" \
    "v_cvt_pk_bf16_f32 v157, v76, v77\n\t" \
    "v_smfmac_f32_16x16x64_bf16 v[80:83], v[64:67], a[160:167], v10\n\t" \
    "ds_write_b32 v12, v157 offset:64\n\t" \
    "v_smfmac_f32_16x16x64_bf16 v[80:83], v[68:71], a[168:175], v10\n\t" \
    "v_smfmac_f32_16x16x64_bf16 v[80:83], v[44:47], a[208:215], v10\n\t" \
    "s_nop 2\n\t" \
    "v_smfmac_f32_16x16x64_bf16 v[84:87], v[48:51], v[232:239], v10\n\t" \
    "s_nop 2\n\t" \
    "v_smfmac_f32_16x16x64_bf16 v[84:87], v[52:55], v[240:247], v10\n\t" \
    "v_add_f32 v80, v80, v82\n\t" \
    "v_smfmac_f32_16x16x64_bf16 v[84:87], v[56:59], a[216:223], v10\n\t" \
    "v_add_f32 v81, v81, v83\n\t" \
    "v_smfmac_f32_16x16x64_bf16 v[84:87], v[60:63], a[224:231], v10\n\t" \
    "v_cvt_pk_bf16_f32 v158, v80, v81\n\t" \
    "v_smfmac_f32_16x16x64_bf16 v[84:87], v[64:67], a[232:239], v10\n\t" \
    "ds_write_b32 v16, v158 offset:0\n\t" \
    "v_smfmac_f32_16x16x64_bf16 v[84:87], v[68:71], a[240:247], v10\n\t" \
    "v_smfmac_f32_16x16x64_bf16 v[84:87], v[44:47], v[248:255], v10\n\t" \
    "s_nop 6\n\t" \
    "v_cmp_u_f32 vcc, v72, v73\n\t" \
    "v_pk_add_f32 v[84:85], v[84:85], v[86:87]\n\t" \
    "v_cvt_pk_bf16_f32 v159, v84, v85\n\t" \
    "ds_write_b32 v16, v159 offset:256\n\t" \
    "s_cbranch_vccnz qk_Gslow69_%=\n\t" \
    "qk_Gback70_%=:\n\t" \
    "s_waitcnt lgkmcnt(0)\n\t" \
    "s_barrier\n\t" \
    "s_sleep 1\n\t" \
    "ds_read_b64_tr_b16 v[140:141], v17 offset:0\n\t" \
    "ds_read_b64_tr_b16 v[142:143], v17 offset:256\n\t" \
    "ds_read_b64_tr_b16 v[144:145], v17 offset:512\n\t" \
    "ds_read_b64_tr_b16 v[146:147], v17 offset:768\n\t" \
    "ds_read_b64_tr_b16 v[148:149], v17 offset:1024\n\t" \
    "ds_read_b64_tr_b16 v[150:151], v17 offset:1280\n\t" \
    "ds_read_b64_tr_b16 v[152:153], v17 offset:1536\n\t" \
    "ds_read_b64_tr_b16 v[154:155], v17 offset:1792\n\t" \
    "s_waitcnt lgkmcnt(0)\n\t" \
    "s_cmp_eq_u32 s65, 63\n\t" \
    "s_cbranch_scc1 qk_last59_%=\n\t" \
    "s_cmp_ge_u32 s65, 58\n\t" \
    "s_cbranch_scc1 qk_nsl72_%=\n\t" \
    "s_sleep 6\n\t" \
    "global_store_dword v14, v25, s[100:101] sc1\n\t" \
    "qk_nsl72_%=:\n\t" \
    "s_mov_b64 exec, s[58:59]\n\t" \
    "global_store_dwordx4 v0, v[140:143], s[40:41] offset:0 sc1\n\t" \
    "global_store_dwordx4 v0, v[144:147], s[40:41] offset:1024 sc1\n\t" \
    "global_store_dwordx4 v0, v[148:151], s[40:41] offset:2048 sc1\n\t" \
    "global_store_dwordx4 v0, v[152:155], s[40:41] offset:3072 sc1\n\t" \
    "s_mov_b64 exec, -1\n\t" \
    "s_add_u32 s65, s65, 1\n\t" \
    "v_mov_b32 v72, 0\n\t" \
    "v_mov_b32 v73, 0\n\t" \
    "v_mov_b32 v74, 0\n\t" \
    "v_mov_b32 v75, 0\n\t" \
    "v_mov_b32 v76, 0\n\t" \
    "v_mov_b32 v77, 0\n\t" \
    "v_mov_b32 v78, 0\n\t" \
    "v_mov_b32 v79, 0\n\t" \
    "v_mov_b32 v80, 0\n\t" \
    "v_mov_b32 v81, 0\n\t" \
    "v_mov_b32 v82, 0\n\t" \
    "v_mov_b32 v83, 0\n\t" \
    "v_mov_b32 v84, 0\n\t" \
    "v_mov_b32 v85, 0\n\t" \
    "v_mov_b32 v86, 0\n\t" \
    "v_mov_b32 v87, 0\n\t" \
    "s_add_u32 s69, s65, 0\n\t" \
    "s_and_b32 s69, s69, 7\n\t" \
    "s_lshl_b32 s69, s69, 18\n\t" \
    "s_add_u32 s69, s69, s62\n\t" \
    "s_add_u32 s46, s36, s69\n\t" \
    "s_addc_u32 s47, s37, 0\n\t" \
    "s_mul_i32 s70, s67, 7\n\t" \
    "s_add_u32 s69, s65, 0\n\t" \
    "s_xor_b32 s69, s69, s70\n\t" \
    "s_and_b32 s69, s69, 63\n\t" \
    "s_lshl_b32 s69, s69, 12\n\t" \
    "s_mul_i32 s70, s67, 262144\n\t" \
    "s_add_u32 s69, s69, s70\n\t" \
    "s_add_u32 s69, s69, 39845888\n\t" \
    "s_add_u32 s40, s36, s69\n\t" \
    "s_addc_u32 s41, s37, 0\n\t" \
    "s_sub_u32 s91, s65, 1\n\t" \
    "v_mov_b32 v25, s91\n\t" \
    "s_barrier\n\t" \
    "ds_read_b32 v168, v11 offset:20480\n\t" \
    "s_waitcnt lgkmcnt(0)\n\t" \
    "v_readfirstlane_b32 s70, v168\n\t" \
    "ds_write_b32 v12, v31 offset:0\n\t" \
    "ds_write_b32 v12, v31 offset:64\n\t" \
    "s_branch qk_go60_%=\n\t" \
    "qk_Gbad67_%=:\n\t" \
    "s_waitcnt vmcnt(0) lgkmcnt(0)\n\t" \
    "s_nop 7\n\t" \
    "v_mov_b32 v72, 0\n\t" \
    "v_mov_b32 v73, 0\n\t" \
    "v_mov_b32 v74, 0\n\t" \
    "v_mov_b32 v75, 0\n\t" \
    "v_mov_b32 v76, 0\n\t" \
    "v_mov_b32 v77, 0\n\t" \
    "v_mov_b32 v78, 0\n\t" \
    "v_mov_b32 v79, 0\n\t" \
    "v_mov_b32 v80, 0\n\t" \
    "v_mov_b32 v81, 0\n\t" \
    "v_mov_b32 v82, 0\n\t" \
    "v_mov_b32 v83, 0\n\t" \
    "v_mov_b32 v84, 0\n\t" \
    "v_mov_b32 v85, 0\n\t" \
    "v_mov_b32 v86, 0\n\t" \
    "v_mov_b32 v87, 0\n\t" \
    "s_cmp_eq_u32 s68, 0\n\t" \
    "s_cbranch_scc1 qk_F73_%=\n\t" \
    "s_sub_u32 s68, s68, 1\n\t" \
    "s_branch qk_G66_%=\n\t" \
    "qk_F73_%=:\n\t" \
    "v_mov_b32 v160, 5767424\n\t" \
    "v_mov_b32 v161, 1\n\t" \
    "global_store_dword v160, v161, s[36:37] sc1\n\t" \
    "s_waitcnt vmcnt(0)\n\t" \
    "s_branch qk_Gc68_%=\n\t" \
    "qk_Gslow69_%=:\n\t" \
    "v_max_u32 v160, v36, v37\n\t" \
    "v_max3_u32 v160, v160, v38, v39\n\t" \
    "v_max3_u32 v160, v160, v40, v41\n\t" \
    "v_max3_u32 v160, v160, v42, v43\n\t" \
    "v_max3_u32 v160, v160, v44, v45\n\t" \
    "v_max3_u32 v160, v160, v46, v47\n\t" \
    "v_max3_u32 v160, v160, v48, v49\n\t" \
    "v_max3_u32 v160, v160, v50, v51\n\t" \
    "v_max3_u32 v160, v160, v52, v53\n\t" \
    "v_max3_u32 v160, v160, v54, v55\n\t" \
    "v_max3_u32 v160, v160, v56, v57\n\t" \
    "v_max3_u32 v160, v160, v58, v59\n\t" \
    "v_max3_u32 v160, v160, v60, v61\n\t" \
    "v_max3_u32 v160, v160, v62, v63\n\t" \
    "v_max3_u32 v160, v160, v64, v65\n\t" \
    "v_max3_u32 v160, v160, v66, v67\n\t" \
    "v_max3_u32 v160, v160, v68, v69\n\t" \
    "v_max3_u32 v160, v160, v70, v71\n\t" \
    "v_cmp_eq_u32 vcc, -1, v160\n\t" \
    "s_cbranch_vccz qk_Gback70_%=\n\t" \
    "s_waitcnt lgkmcnt(0)\n\t" \
    "v_sub_u32 v160, v1, v3\n\t" \
    "s_nop 0\n\t" \
    "v_readfirstlane_b32 s69, v160\n\t" \
    "v_add_u32 v163, 20480, v0\n\t" \
    "s_nop 7\n\t" \
    "s_add_u32 s30, s46, s69\n\t" \
    "s_addc_u32 s31, s47, 0\n\t" \
    "qk_Gag74_%=:\n\t" \
    "global_load_dwordx4 v[36:39], v1, s[46:47] sc1\n\t" \
    "global_load_dwordx4 v[40:43], v23, s[46:47] sc1\n\t" \
    "global_load_dwordx4 v[44:47], v163, s[30:31] sc1\n\t" \
    "global_load_dwordx4 v[48:51], v4, s[30:31] sc1\n\t" \
    "global_load_dwordx4 v[52:55], v5, s[30:31] sc1\n\t" \
    "global_load_dwordx4 v[56:59], v6, s[30:31] sc1\n\t" \
    "global_load_dwordx4 v[60:63], v7, s[30:31] sc1\n\t" \
    "global_load_dwordx4 v[64:67], v8, s[30:31] sc1\n\t" \
    "global_load_dwordx4 v[68:71], v9, s[30:31] sc1\n\t" \
    "s_waitcnt vmcnt(0)\n\t" \
    "v_max_u32 v160, v36, v37\n\t" \
    "v_max3_u32 v160, v160, v38, v39\n\t" \
    "v_max3_u32 v160, v160, v40, v41\n\t" \
    "v_max3_u32 v160, v160, v42, v43\n\t" \
    "v_max3_u32 v160, v160, v44, v45\n\t" \
    "v_max3_u32 v160, v160, v46, v47\n\t" \
    "v_max3_u32 v160, v160, v48, v49\n\t" \
    "v_max3_u32 v160, v160, v50, v51\n\t" \
    "v_max3_u32 v160, v160, v52, v53\n\t" \
    "v_max3_u32 v160, v160, v54, v55\n\t" \
    "v_max3_u32 v160, v160, v56, v57\n\t" \
    "v_max3_u32 v160, v160, v58, v59\n\t" \
    "v_max3_u32 v160, v160, v60, v61\n\t" \
    "v_max3_u32 v160, v160, v62, v63\n\t" \
    "v_max3_u32 v160, v160, v64, v65\n\t" \
    "v_max3_u32 v160, v160, v66, v67\n\t" \
    "v_max3_u32 v160, v160, v68, v69\n\t" \
    "v_max3_u32 v160, v160, v70, v71\n\t" \
    "v_cmp_eq_u32 vcc, -1, v160\n\t" \
    "s_cbranch_vccz qk_Gredo75_%=\n\t" \
    "s_cmp_eq_u32 s68, 0\n\t" \
    "s_cbranch_scc1 qk_F76_%=\n\t" \
    "s_sub_u32 s68, s68, 1\n\t" \
    "s_sleep 1\n\t" \
    "s_branch qk_Gag74_%=\n\t" \
    "qk_F76_%=:\n\t" \
    "v_mov_b32 v160, 5767424\n\t" \
    "v_mov_b32 v161, 2\n\t" \
    "global_store_dword v160, v161, s[36:37] sc1\n\t" \
    "s_waitcnt vmcnt(0)\n\t" \
    "s_branch qk_Gredo75_%=\n\t" \
    "qk_Gredo75_%=:\n\t" \
    "v_mov_b32 v72, 0\n\t" \
    "v_mov_b32 v73, 0\n\t" \
    "v_mov_b32 v74, 0\n\t" \
    "v_mov_b32 v75, 0\n\t" \
    "v_mov_b32 v76, 0\n\t" \
    "v_mov_b32 v77, 0\n\t" \
    "v_mov_b32 v78, 0\n\t" \
    "v_mov_b32 v79, 0\n\t" \
    "v_mov_b32 v80, 0\n\t" \
    "v_mov_b32 v81, 0\n\t" \
    "v_mov_b32 v82, 0\n\t" \
    "v_mov_b32 v83, 0\n\t" \
    "v_mov_b32 v84, 0\n\t" \
    "v_mov_b32 v85, 0\n\t" \
    "v_mov_b32 v86, 0\n\t" \
    "v_mov_b32 v87, 0\n\t" \
    "v_smfmac_f32_16x16x64_bf16 v[72:75], v[36:39], a[32:39], v10\n\t" \
    "v_smfmac_f32_16x16x64_bf16 v[72:75], v[40:43], a[40:47], v10\n\t" \
    "v_smfmac_f32_16x16x64_bf16 v[72:75], v[48:51], a[48:55], v10\n\t" \
    "v_smfmac_f32_16x16x64_bf16 v[72:75], v[52:55], a[56:63], v10\n\t" \
    "v_smfmac_f32_16x16x64_bf16 v[72:75], v[56:59], a[0:7], v10\n\t" \
    "v_smfmac_f32_16x16x64_bf16 v[72:75], v[60:63], a[8:15], v10\n\t" \
    "v_smfmac_f32_16x16x64_bf16 v[72:75], v[64:67], a[16:23], v10\n\t" \
    "v_smfmac_f32_16x16x64_bf16 v[72:75], v[68:71], a[24:31], v10\n\t" \
    "v_smfmac_f32_16x16x64_bf16 v[72:75], v[44:47], a[64:71], v10\n\t" \
    "v_smfmac_f32_16x16x64_bf16 v[76:79], v[36:39], a[104:111], v10\n\t" \
    "v_smfmac_f32_16x16x64_bf16 v[76:79], v[40:43], a[112:119], v10\n\t" \
    "v_smfmac_f32_16x16x64_bf16 v[76:79], v[48:51], a[120:127], v10\n\t" \
    "v_smfmac_f32_16x16x64_bf16 v[76:79], v[52:55], a[128:135], v10\n\t" \
    "v_smfmac_f32_16x16x64_bf16 v[76:79], v[56:59], a[72:79], v10\n\t" \
    "v_smfmac_f32_16x16x64_bf16 v[76:79], v[60:63], a[80:87], v10\n\t" \
    "v_smfmac_f32_16x16x64_bf16 v[76:79], v[64:67], a[88:95], v10\n\t" \
    "v_smfmac_f32_16x16x64_bf16 v[76:79], v[68:71], a[96:103], v10\n\t" \
    "v_smfmac_f32_16x16x64_bf16 v[76:79], v[44:47], a[136:143], v10\n\t" \
    "v_smfmac_f32_16x16x64_bf16 v[80:83], v[36:39], a[176:183], v10\n\t" \
    "v_smfmac_f32_16x16x64_bf16 v[80:83], v[40:43], a[184:191], v10\n\t" \
    "v_smfmac_f32_16x16x64_bf16 v[80:83], v[48:51], a[192:199], v10\n\t" \
    "v_smfmac_f32_16x16x64_bf16 v[80:83], v[52:55], a[200:207], v10\n\t" \
    "v_smfmac_f32_16x16x64_bf16 v[80:83], v[56:59], a[144:151], v10\n\t" \
    "v_smfmac_f32_16x16x64_bf16 v[80:83], v[60:63], a[152:159], v10\n\t" \
    "v_smfmac_f32_16x16x64_bf16 v[80:83], v[64:67], a[160:167], v10\n\t" \
    "v_smfmac_f32_16x16x64_bf16 v[80:83], v[68:71], a[168:175], v10\n\t" \
    "v_smfmac_f32_16x16x64_bf16 v[80:83], v[44:47], a[208:215], v10\n\t" \
    "v_smfmac_f32_16x16x64_bf16 v[84:87], v[36:39], a[248:255], v10\n\t" \
    "v_smfmac_f32_16x16x64_bf16 v[84:87], v[40:43], v[224:231], v10\n\t" \
    "v_smfmac_f32_16x16x64_bf16 v[84:87], v[48:51], v[232:239], v10\n\t" \
    "v_smfmac_f32_16x16x64_bf16 v[84:87], v[52:55], v[240:247], v10\n\t" \
    "v_smfmac_f32_16x16x64_bf16 v[84:87], v[56:59], a[216:223], v10\n\t" \
    "v_smfmac_f32_16x16x64_bf16 v[84:87], v[60:63], a[224:231], v10\n\t" \
    "v_smfmac_f32_16x16x64_bf16 v[84:87], v[64:67], a[232:239], v10\n\t" \
    "v_smfmac_f32_16x16x64_bf16 v[84:87], v[68:71], a[240:247], v10\n\t" \
    "v_smfmac_f32_16x16x64_bf16 v[84:87], v[44:47], v[248:255], v10\n\t" \
    "v_add_f32 v72, v72, v74\n\t" \
    "v_add_f32 v73, v73, v75\n\t" \
    "v_cvt_pk_bf16_f32 v156, v72, v73\n\t" \
    "ds_write_b32 v12, v156 offset:0\n\t" \
    "v_add_f32 v76, v76, v78\n\t" \
    "v_add_f32 v77, v77, v79\n\t" \
    "v_cvt_pk_bf16_f32 v157, v76, v77\n\t" \
    "ds_write_b32 v12, v157 offset:64\n\t" \
    "v_add_f32 v80, v80, v82\n\t" \
    "v_add_f32 v81, v81, v83\n\t" \
    "v_cvt_pk_bf16_f32 v158, v80, v81\n\t" \
    "ds_write_b32 v16, v158 offset:0\n\t" \
    "v_pk_add_f32 v[84:85], v[84:85], v[86:87]\n\t" \
    "v_cvt_pk_bf16_f32 v159, v84, v85\n\t" \
    "ds_write_b32 v16, v159 offset:256\n\t" \
    "s_branch qk_Gback70_%=\n\t" \
    "qk_kf61_%=:\n\t" \
    "global_store_dword v14, v25, s[100:101] sc1\n\t" \
    "s_branch qk_kb62_%=\n\t" \
    "qk_last59_%=:\n\t" \
    "s_mov_b64 exec, s[58:59]\n\t" \
    "global_store_dwordx4 v0, v[140:143], s[40:41] offset:0 sc1\n\t" \
    "global_store_dwordx4 v0, v[144:147], s[40:41] offset:1024 sc1\n\t" \
    "global_store_dwordx4 v0, v[148:151], s[40:41] offset:2048 sc1\n\t" \
    "global_store_dwordx4 v0, v[152:155], s[40:41] offset:3072 sc1\n\t" \
    "s_mov_b64 exec, -1\n\t" \
    "s_endpgm\n\t" \
    "qk_loop14_%=:\n\t" \
    "s_mov_b32 s38, 0x1b8d00\n\t" \
    "s_mov_b32 s39, 0x0\n\t" \
    "v_mov_b32 v72, 0\n\t" \
    "v_mov_b32 v73, 0\n\t" \
    "v_mov_b32 v74, 0\n\t" \
    "v_mov_b32 v75, 0\n\t" \
    "v_mov_b32 v76, 0\n\t" \
    "v_mov_b32 v77, 0\n\t" \
    "v_mov_b32 v78, 0\n\t" \
    "v_mov_b32 v79, 0\n\t" \
    "v_mov_b32 v80, 0\n\t" \
    "v_mov_b32 v81, 0\n\t" \
    "v_mov_b32 v82, 0\n\t" \
    "v_mov_b32 v83, 0\n\t" \
    "v_mov_b32 v84, 0\n\t" \
    "v_mov_b32 v85, 0\n\t" \
    "v_mov_b32 v86, 0\n\t" \
    "v_mov_b32 v87, 0\n\t" \
    "s_add_u32 s69, s65, 0\n\t" \
    "s_and_b32 s69, s69, 7\n\t" \
    "s_lshl_b32 s69, s69, 18\n\t" \
    "s_add_u32 s69, s69, s62\n\t" \
    "s_add_u32 s46, s36, s69\n\t" \
    "s_addc_u32 s47, s37, 0\n\t" \
    "s_lshr_b64 s[38:39], s[38:39], 4\n\t" \
    "s_and_b32 s97, s38, 15\n\t" \
    "s_lshl_b32 s97, s97, 6\n\t" \
    "s_and_b32 s69, s65, 7\n\t" \
    "s_lshl_b32 s69, s69, 18\n\t" \
    "s_add_u32 s69, s69, s63\n\t" \
    "s_add_u32 s52, s36, s69\n\t" \
    "s_addc_u32 s53, s37, 0\n\t" \
    "s_sub_u32 s70, s79, s74\n\t" \
    "s_max_i32 s70, s70, 0\n\t" \
    "s_min_i32 s70, s70, 16000\n\t" \
    "s_lshl_b32 s70, s70, 1\n\t" \
    "s_mul_hi_u32 s70, s70, s77\n\t" \
    "s_add_u32 s70, s70, s73\n\t" \
    ".p2align 6\n\t" \
    "s_nop 0\n\t" \
    "s_nop 0\n\t" \
    "s_nop 0\n\t" \
    "s_nop 0\n\t" \
    "s_nop 0\n\t" \
    "s_nop 0\n\t" \
    "qk_go78_%=:\n\t" \
    "s_getpc_b64 s[30:31]\n\t" \
    "qk_Wp80_%=:\n\t" \
    "s_add_u32 s30, s30, qk_We81_%=-qk_Wp80_%=\n\t" \
    "s_addc_u32 s31, s31, 0\n\t" \
    "s_sub_u32 s70, s70, 96\n\t" \
    "qk_W79_%=:\n\t" \
    "s_memtime s[56:57]\n\t" \
    "s_waitcnt lgkmcnt(0)\n\t" \
    "s_sub_u32 s69, s70, s56\n\t" \
    "s_cmp_gt_i32 s69, 80\n\t" \
    "s_cbranch_scc1 qk_W79_%=\n\t" \
    "s_max_i32 s69, s69, 0\n\t" \
    "s_and_b32 s69, s69, -4\n\t" \
    "s_sub_u32 s30, s30, s69\n\t" \
    "s_subb_u32 s31, s31, 0\n\t" \
    "s_setpc_b64 s[30:31]\n\t" \
    "s_nop 0\n\t" \
    "s_nop 0\n\t" \
    "s_nop 0\n\t" \
    "s_nop 0\n\t" \
    "s_nop 0\n\t" \
    "s_nop 0\n\t" \
    "s_nop 0\n\t" \
    "s_nop 0\n\t" \
    "s_nop 0\n\t" \
    "s_nop 0\n\t" \
    "s_nop 0\n\t" \
    "s_nop 0\n\t" \
    "s_nop 0\n\t" \
    "s_nop 0\n\t" \
    "s_nop 0\n\t" \
    "s_nop 0\n\t" \
    "s_nop 0\n\t" \
    "s_nop 0\n\t" \
    "s_nop 0\n\t" \
    "s_nop 0\n\t" \
    "qk_We81_%=:\n\t" \
    "qk_G82_%=:\n\t" \
    "global_load_dwordx4 v[36:39], v1, s[46:47] sc1\n\t" \
    "global_load_dword v161, v2, s[46:47] sc1\n\t" \
    "global_load_dwordx4 v[40:43], v23, s[46:47] sc1\n\t" \
    "s_mul_hi_u32 s69, s84, s78\n\t" \
    "s_add_u32 s33, s74, s69\n\t" \
    "s_add_u32 s33, s33, s97\n\t" \
    "s_lshr_b32 s69, s90, 16\n\t" \
    "s_add_u32 s34, s33, s69\n\t" \
    "s_and_b32 s69, s90, 0xffff\n\t" \
    "s_sub_u32 s35, s80, s74\n\t" \
    "s_sub_u32 s35, s35, s69\n\t" \
    "s_add_u32 s99, s35, 240\n\t" \
    "s_add_u32 s98, s33, 448\n\t" \
    "s_sub_u32 s96, s79, s74\n\t" \
    "s_cmp_le_u32 s65, 0\n\t" \
    "s_cselect_b32 s69, 112, 0\n\t" \
    "s_add_u32 s69, s69, 448\n\t" \
    "s_add_u32 s96, s96, s69\n\t" \
    "s_max_i32 s35, s35, s96\n\t" \
    "s_add_u32 s99, s35, 716\n\t" \
    "s_mul_i32 s69, s35, 10\n\t" \
    "s_ashr_i32 s69, s69, 4\n\t" \
    "s_sub_u32 s69, s33, s69\n\t" \
    "v_lshrrev_b32_e64 v163, 1, s69\n\t" \
    "s_waitcnt vmcnt(2)\n\t" \
    "qk_Garr87_%=:\n\t" \
    "ds_write_b128 v3, v[36:39]\n\t" \
    "v_smfmac_f32_16x16x64_bf16 v[72:75], v[36:39], a[48:55], v10\n\t" \
    "s_waitcnt vmcnt(1)\n\t" \
    "ds_write_b32 v22, v161 offset:20224\n\t" \
    "v_smfmac_f32_16x16x64_bf16 v[76:79], v[36:39], a[120:127], v10\n\t" \
    "s_waitcnt vmcnt(0)\n\t" \
    "ds_write_b128 v3, v[40:43] offset:1024\n\t" \
    "v_max3_u32 v160, v36, v40, v161\n\t" \
    "v_cmp_eq_u32 vcc, -1, v160\n\t" \
    "v_smfmac_f32_16x16x64_bf16 v[80:83], v[36:39], a[192:199], v10\n\t" \
    "v_smfmac_f32_16x16x64_bf16 v[84:87], v[36:39], v[232:239], v10\n\t" \
    "v_smfmac_f32_16x16x64_bf16 v[72:75], v[40:43], a[56:63], v10\n\t" \
    "s_cbranch_vccnz qk_Gbad83_%=\n\t" \
    "qk_Gc84_%=:\n\t" \
    "s_waitcnt lgkmcnt(0)\n\t" \
    "s_barrier\n\t" \
    "s_memtime s[56:57]\n\t" \
    "ds_read_b128 v[48:51], v4\n\t" \
    "ds_read_b128 v[52:55], v5\n\t" \
    "v_smfmac_f32_16x16x64_bf16 v[76:79], v[40:43], a[128:135], v10\n\t" \
    "ds_read_b128 v[56:59], v6\n\t" \
    "ds_read_b128 v[60:63], v7\n\t" \
    "v_smfmac_f32_16x16x64_bf16 v[80:83], v[40:43], a[200:207], v10\n\t" \
    "ds_read_b128 v[64:67], v8\n\t" \
    "ds_read_b128 v[68:71], v9\n\t" \
    "v_smfmac_f32_16x16x64_bf16 v[84:87], v[40:43], v[240:247], v10\n\t" \
    "ds_read_b128 v[44:47], v0 offset:19456\n\t" \
    "s_waitcnt lgkmcnt(6)\n\t" \
    "v_smfmac_f32_16x16x64_bf16 v[72:75], v[48:51], a[0:7], v10\n\t" \
    "s_sub_u32 s69, s56, s73\n\t" \
    "s_mul_hi_u32 s69, s69, s78\n\t" \
    "s_waitcnt lgkmcnt(5)\n\t" \
    "v_smfmac_f32_16x16x64_bf16 v[72:75], v[52:55], a[8:15], v10\n\t" \
    "s_max_i32 s69, s69, s96\n\t" \
    "s_max_i32 s70, s69, s35\n\t" \
    "s_waitcnt lgkmcnt(4)\n\t" \
    "v_smfmac_f32_16x16x64_bf16 v[72:75], v[56:59], a[16:23], v10\n\t" \
    "s_min_i32 s70, s70, s99\n\t" \
    "s_mul_i32 s70, s70, 10\n\t" \
    "s_waitcnt lgkmcnt(3)\n\t" \
    "v_smfmac_f32_16x16x64_bf16 v[72:75], v[60:63], a[24:31], v10\n\t" \
    "s_lshl4_add_u32 s70, s69, s70\n\t" \
    "s_ashr_i32 s70, s70, 5\n\t" \
    "v_add_lshl_u32 v162, s70, v163, 1\n\t" \
    "s_waitcnt lgkmcnt(2)\n\t" \
    "v_smfmac_f32_16x16x64_bf16 v[72:75], v[64:67], a[32:39], v10\n\t" \
    "global_store_dword v21, v162, s[52:53] sc1\n\t" \
    "s_waitcnt lgkmcnt(1)\n\t" \
    "v_smfmac_f32_16x16x64_bf16 v[72:75], v[68:71], a[40:47], v10\n\t" \
    "s_waitcnt lgkmcnt(0)\n\t" \
    "v_smfmac_f32_16x16x64_bf16 v[72:75], v[44:47], a[64:71], v10\n\t" \
    "s_nop 2\n\t" \
    "v_smfmac_f32_16x16x64_bf16 v[76:79], v[48:51], a[72:79], v10\n\t" \
    "s_nop 2\n\t" \
    "v_smfmac_f32_16x16x64_bf16 v[76:79], v[52:55], a[80:87], v10\n\t" \
    "v_add_f32 v72, v72, v74\n\t" \
    "v_smfmac_f32_16x16x64_bf16 v[76:79], v[56:59], a[88:95], v10\n\t" \
    "v_add_f32 v73, v73, v75\n\t" \
    "v_smfmac_f32_16x16x64_bf16 v[76:79], v[60:63], a[96:103], v10\n\t" \
    "v_cvt_pk_bf16_f32 v156, v72, v73\n\t" \
    "v_smfmac_f32_16x16x64_bf16 v[76:79], v[64:67], a[104:111], v10\n\t" \
    "ds_write_b32 v12, v156 offset:0\n\t" \
    "v_smfmac_f32_16x16x64_bf16 v[76:79], v[68:71], a[112:119], v10\n\t" \
    "v_smfmac_f32_16x16x64_bf16 v[76:79], v[44:47], a[136:143], v10\n\t" \
    "s_nop 2\n\t" \
    "v_smfmac_f32_16x16x64_bf16 v[80:83], v[48:51], a[144:151], v10\n\t" \
    "s_nop 2\n\t" \
    "v_smfmac_f32_16x16x64_bf16 v[80:83], v[52:55], a[152:159], v10\n\t" \
    "v_add_f32 v76, v76, v78\n\t" \
    "v_smfmac_f32_16x16x64_bf16 v[80:83], v[56:59], a[160:167], v10\n\t" \
    "v_add_f32 v77, v77, v79\n\t" \
    "v_smfmac_f32_16x16x64_bf16 v[80:83], v[60:63], a[168:175], v10\n\t" \
    "v_cvt_pk_bf16_f32 v157, v76, v77\n\t" \
    "v_smfmac_f32_16x16x64_bf16 v[80:83], v[64:67], a[176:183], v10\n\t" \
    "ds_write_b32 v12, v157 offset:64\n\t" \
    "v_smfmac_f32_16x16x64_bf16 v[80:83], v[68:71], a[184:191], v10\n\t" \
    "v_smfmac_f32_16x16x64_bf16 v[80:83], v[44:47], a[208:215], v10\n\t" \
    "s_nop 2\n\t" \
    "v_smfmac_f32_16x16x64_bf16 v[84:87], v[48:51], a[216:223], v10\n\t" \
    "s_nop 2\n\t" \
    "v_smfmac_f32_16x16x64_bf16 v[84:87], v[52:55], a[224:231], v10\n\t" \
    "v_add_f32 v80, v80, v82\n\t" \
    "v_smfmac_f32_16x16x64_bf16 v[84:87], v[56:59], a[232:239], v10\n\t" \
    "v_add_f32 v81, v81, v83\n\t" \
    "v_smfmac_f32_16x16x64_bf16 v[84:87], v[60:63], a[240:247], v10\n\t" \
    "v_cvt_pk_bf16_f32 v158, v80, v81\n\t" \
    "v_smfmac_f32_16x16x64_bf16 v[84:87], v[64:67], a[248:255], v10\n\t" \
    "ds_write_b32 v16, v158 offset:0\n\t" \
    "v_smfmac_f32_16x16x64_bf16 v[84:87], v[68:71], v[224:231], v10\n\t" \
    "v_smfmac_f32_16x16x64_bf16 v[84:87], v[44:47], v[248:255], v10\n\t" \
    "s_nop 6\n\t" \
    "v_cmp_u_f32 vcc, v72, v73\n\t" \
    "v_pk_add_f32 v[84:85], v[84:85], v[86:87]\n\t" \
    "v_cvt_pk_bf16_f32 v159, v84, v85\n\t" \
    "ds_write_b32 v16, v159 offset:256\n\t" \
    "s_cbranch_vccnz qk_Gslow85_%=\n\t" \
    "qk_Gback86_%=:\n\t" \
    "s_waitcnt lgkmcnt(0)\n\t" \
    "s_barrier\n\t" \
    "s_cmp_eq_u32 s65, 63\n\t" \
    "s_cbranch_scc1 qk_last77_%=\n\t" \
    "s_cmp_lg_u32 s65, 0\n\t" \
    "s_cbranch_scc1 qk_ng88_%=\n\t" \
    "v_mov_b32 v168, 5769216\n\t" \
    "v_mov_b32 v169, 1\n\t" \
    "s_mov_b64 exec, 1\n\t" \
    "global_atomic_add v170, v168, v169, s[36:37] sc0 sc1\n\t" \
    "s_mov_b64 exec, -1\n\t" \
    "v_mbcnt_lo_u32_b32 v168, -1, 0\n\t" \
    "v_and_b32 v169, 3, v168\n\t" \
    "v_lshlrev_b32 v169, 16, v169\n\t" \
    "v_bfe_u32 v171, v168, 2, 2\n\t" \
    "v_lshl_add_u32 v169, v171, 11, v169\n\t" \
    "v_bfe_u32 v171, v168, 4, 1\n\t" \
    "v_lshl_add_u32 v169, v171, 14, v169\n\t" \
    "v_add_u32 v169, s63, v169\n\t" \
    "s_waitcnt vmcnt(0)\n\t" \
    "v_readfirstlane_b32 s69, v170\n\t" \
    "s_nop 1\n\t" \
    "s_cmp_lg_u32 s69, 126\n\t" \
    "s_cbranch_scc1 qk_go89_%=\n\t" \
    "s_memrealtime s[56:57]\n\t" \
    "s_waitcnt lgkmcnt(0)\n\t" \
    "s_lshl_b32 s69, s56, 4\n\t" \
    "s_add_u32 s69, s69, 2960\n\t" \
    "s_add_u32 s70, s56, 24\n\t" \
    "s_lshr_b32 s70, s70, 4\n\t" \
    "v_lshrrev_b32 v171, 2, v168\n\t" \
    "v_add_u32 v171, s70, v171\n\t" \
    "v_and_b32 v171, 63, v171\n\t" \
    "v_lshlrev_b32 v171, 6, v171\n\t" \
    "v_and_b32 v170, 3, v168\n\t" \
    "v_lshl_add_u32 v171, v170, 12, v171\n\t" \
    "v_add_u32 v171, 5783552, v171\n\t" \
    "v_mov_b32 v168, s69\n\t" \
    "s_mov_b32 exec_hi, 0\n\t" \
    "global_store_dword v171, v168, s[36:37] sc1\n\t" \
    "global_store_dword v169, v168, s[36:37] sc1\n\t" \
    "s_mov_b32 exec_hi, -1\n\t" \
    "qk_go89_%=:\n\t" \
    "v_mov_b32 v21, v33\n\t" \
    "qk_ng88_%=:\n\t" \
    "s_sub_u32 s70, s80, s74\n\t" \
    "s_max_i32 s70, s70, 0\n\t" \
    "s_min_i32 s70, s70, 16000\n\t" \
    "s_lshl_b32 s70, s70, 1\n\t" \
    "s_mul_hi_u32 s70, s70, s77\n\t" \
    "s_add_u32 s70, s70, s73\n\t" \
    "qk_W90_%=:\n\t" \
    "s_memtime s[56:57]\n\t" \
    "s_waitcnt lgkmcnt(0)\n\t" \
    "s_sub_u32 s69, s70, s56\n\t" \
    "s_cmp_gt_i32 s69, 24\n\t" \
    "s_cbranch_scc1 qk_W90_%=\n\t" \
    "global_load_dword v160, v20, s[52:53] sc1\n\t" \
    "s_waitcnt lgkmcnt(0)\n\t" \
    "s_mov_b32 s70, 116\n\t" \
    "qk_E94_%=:\n\t" \
    "s_memtime s[56:57]\n\t" \
    "s_memrealtime s[2:3]\n\t" \
    "s_nop 0\n\t" \
    "s_memrealtime s[4:5]\n\t" \
    "s_nop 0\n\t" \
    "s_memrealtime s[6:7]\n\t" \
    "s_nop 0\n\t" \
    "s_memrealtime s[8:9]\n\t" \
    "s_nop 0\n\t" \
    "s_memrealtime s[10:11]\n\t" \
    "s_nop 0\n\t" \
    "s_memrealtime s[12:13]\n\t" \
    "s_nop 0\n\t" \
    "s_memrealtime s[14:15]\n\t" \
    "s_nop 0\n\t" \
    "s_memrealtime s[16:17]\n\t" \
    "s_nop 0\n\t" \
    "s_memrealtime s[18:19]\n\t" \
    "s_nop 0\n\t" \
    "s_memrealtime s[20:21]\n\t" \
    "s_nop 0\n\t" \
    "s_memrealtime s[22:23]\n\t" \
    "s_nop 0\n\t" \
    "s_memrealtime s[24:25]\n\t" \
    "s_nop 0\n\t" \
    "s_memrealtime s[26:27]\n\t" \
    "s_nop 0\n\t" \
    "s_memrealtime s[28:29]\n\t" \
    "s_memtime s[54:55]\n\t" \
    "s_add_u32 s70, s70, 8\n\t" \
    "s_waitcnt lgkmcnt(0)\n\t" \
    "s_sub_u32 s69, s54, s56\n\t" \
    "s_cmp_gt_u32 s69, s70\n\t" \
    "s_cbranch_scc1 qk_E94_%=\n\t" \
    "s_add_u32 s2, s2, s4\n\t" \
    "s_add_u32 s6, s6, s8\n\t" \
    "s_add_u32 s10, s10, s12\n\t" \
    "s_add_u32 s14, s14, s16\n\t" \
    "s_add_u32 s18, s18, s20\n\t" \
    "s_add_u32 s22, s22, s24\n\t" \
    "s_add_u32 s2, s2, s6\n\t" \
    "s_add_u32 s10, s10, s14\n\t" \
    "s_add_u32 s18, s18, s22\n\t" \
    "s_add_u32 s2, s2, s26\n\t" \
    "s_add_u32 s10, s10, s18\n\t" \
    "s_add_u32 s2, s2, s10\n\t" \
    "s_mul_i32 s70, s28, 13\n\t" \
    "s_sub_u32 s70, s70, s2\n\t" \
    "s_min_u32 s70, s70, 52\n\t" \
    "s_mul_i32 s69, s69, s70\n\t" \
    "s_mul_hi_u32 s69, s69, 76695844\n\t" \
    "s_mov_b32 s75, s73\n\t" \
    "s_mov_b32 s76, s74\n\t" \
    "s_add_u32 s73, s56, s69\n\t" \
    "s_lshl_b32 s74, s28, 4\n\t" \
    "s_sub_u32 s69, s73, s75\n\t" \
    "s_sub_u32 s70, s74, s76\n\t" \
    "s_sub_u32 s71, s70, 1600\n\t" \
    "s_cmp_lt_u32 s71, 400000\n\t" \
    "s_cbranch_scc0 qk_K95_%=\n\t" \
    "v_cvt_f32_u32 v168, s69\n\t" \
    "v_cvt_f32_u32 v169, s70\n\t" \
    "v_rcp_f32 v170, v168\n\t" \
    "v_rcp_f32 v171, v169\n\t" \
    "v_mul_f32 v170, v170, v169\n\t" \
    "v_mul_f32 v171, v171, v168\n\t" \
    "v_mul_f32 v170, 0x4f800000, v170\n\t" \
    "v_mul_f32 v171, 0x4f000000, v171\n\t" \
    "v_cvt_u32_f32 v170, v170\n\t" \
    "v_cvt_u32_f32 v171, v171\n\t" \
    "v_readfirstlane_b32 s69, v170\n\t" \
    "v_readfirstlane_b32 s70, v171\n\t" \
    "s_nop 1\n\t" \
    "s_min_u32 s71, s65, 1\n\t" \
    "s_sub_u32 s69, s69, s78\n\t" \
    "s_ashr_i32 s69, s69, s71\n\t" \
    "s_add_u32 s78, s78, s69\n\t" \
    "s_sub_u32 s70, s70, s77\n\t" \
    "s_ashr_i32 s70, s70, s71\n\t" \
    "s_add_u32 s77, s77, s70\n\t" \
    "qk_K95_%=:\n\t" \
    "s_add_u32 s65, s65, 1\n\t" \
    "v_mov_b32 v72, 0\n\t" \
    "v_mov_b32 v73, 0\n\t" \
    "v_mov_b32 v74, 0\n\t" \
    "v_mov_b32 v75, 0\n\t" \
    "v_mov_b32 v76, 0\n\t" \
    "v_mov_b32 v77, 0\n\t" \
    "v_mov_b32 v78, 0\n\t" \
    "v_mov_b32 v79, 0\n\t" \
    "v_mov_b32 v80, 0\n\t" \
    "v_mov_b32 v81, 0\n\t" \
    "v_mov_b32 v82, 0\n\t" \
    "v_mov_b32 v83, 0\n\t" \
    "v_mov_b32 v84, 0\n\t" \
    "v_mov_b32 v85, 0\n\t" \
    "v_mov_b32 v86, 0\n\t" \
    "v_mov_b32 v87, 0\n\t" \
    "s_add_u32 s69, s65, 0\n\t" \
    "s_and_b32 s69, s69, 7\n\t" \
    "s_lshl_b32 s69, s69, 18\n\t" \
    "s_add_u32 s69, s69, s62\n\t" \
    "s_add_u32 s46, s36, s69\n\t" \
    "s_addc_u32 s47, s37, 0\n\t" \
    "s_lshr_b64 s[38:39], s[38:39], 4\n\t" \
    "s_and_b32 s97, s38, 15\n\t" \
    "s_lshl_b32 s97, s97, 6\n\t" \
    "s_waitcnt vmcnt(0)\n\t" \
    "v_cmp_eq_u32 vcc, -1, v160\n\t" \
    "s_cbranch_vccz qk_Hok92_%=\n\t" \
    "qk_H91_%=:\n\t" \
    "global_load_dword v160, v20, s[52:53] sc1\n\t" \
    "s_waitcnt vmcnt(0)\n\t" \
    "v_cmp_eq_u32 vcc, -1, v160\n\t" \
    "s_cbranch_vccz qk_Hc93_%=\n\t" \
    "s_cmp_eq_u32 s68, 0\n\t" \
    "s_cbranch_scc1 qk_F96_%=\n\t" \
    "s_sub_u32 s68, s68, 1\n\t" \
    "s_sleep 2\n\t" \
    "s_branch qk_H91_%=\n\t" \
    "qk_F96_%=:\n\t" \
    "v_mov_b32 v161, 5767424\n\t" \
    "v_mov_b32 v162, 5\n\t" \
    "global_store_dword v161, v162, s[36:37] sc1\n\t" \
    "s_waitcnt vmcnt(0)\n\t" \
    "s_branch qk_Hc93_%=\n\t" \
    "qk_Hc93_%=:\n\t" \
    "s_memtime s[56:57]\n\t" \
    "s_waitcnt lgkmcnt(0)\n\t" \
    "s_sub_u32 s69, s56, s73\n\t" \
    "s_cmp_lt_u32 s69, 3000\n\t" \
    "s_cbranch_scc1 qk_St97_%=\n\t" \
    "s_waitcnt lgkmcnt(0)\n\t" \
    "s_mov_b32 s70, 116\n\t" \
    "qk_E98_%=:\n\t" \
    "s_memtime s[56:57]\n\t" \
    "s_memrealtime s[2:3]\n\t" \
    "s_nop 0\n\t" \
    "s_memrealtime s[4:5]\n\t" \
    "s_nop 0\n\t" \
    "s_memrealtime s[6:7]\n\t" \
    "s_nop 0\n\t" \
    "s_memrealtime s[8:9]\n\t" \
    "s_nop 0\n\t" \
    "s_memrealtime s[10:11]\n\t" \
    "s_nop 0\n\t" \
    "s_memrealtime s[12:13]\n\t" \
    "s_nop 0\n\t" \
    "s_memrealtime s[14:15]\n\t" \
    "s_nop 0\n\t" \
    "s_memrealtime s[16:17]\n\t" \
    "s_nop 0\n\t" \
    "s_memrealtime s[18:19]\n\t" \
    "s_nop 0\n\t" \
    "s_memrealtime s[20:21]\n\t" \
    "s_nop 0\n\t" \
    "s_memrealtime s[22:23]\n\t" \
    "s_nop 0\n\t" \
    "s_memrealtime s[24:25]\n\t" \
    "s_nop 0\n\t" \
    "s_memrealtime s[26:27]\n\t" \
    "s_nop 0\n\t" \
    "s_memrealtime s[28:29]\n\t" \
    "s_memtime s[54:55]\n\t" \
    "s_add_u32 s70, s70, 8\n\t" \
    "s_waitcnt lgkmcnt(0)\n\t" \
    "s_sub_u32 s69, s54, s56\n\t" \
    "s_cmp_gt_u32 s69, s70\n\t" \
    "s_cbranch_scc1 qk_E98_%=\n\t" \
    "s_add_u32 s2, s2, s4\n\t" \
    "s_add_u32 s6, s6, s8\n\t" \
    "s_add_u32 s10, s10, s12\n\t" \
    "s_add_u32 s14, s14, s16\n\t" \
    "s_add_u32 s18, s18, s20\n\t" \
    "s_add_u32 s22, s22, s24\n\t" \
    "s_add_u32 s2, s2, s6\n\t" \
    "s_add_u32 s10, s10, s14\n\t" \
    "s_add_u32 s18, s18, s22\n\t" \
    "s_add_u32 s2, s2, s26\n\t" \
    "s_add_u32 s10, s10, s18\n\t" \
    "s_add_u32 s2, s2, s10\n\t" \
    "s_mul_i32 s70, s28, 13\n\t" \
    "s_sub_u32 s70, s70, s2\n\t" \
    "s_min_u32 s70, s70, 52\n\t" \
    "s_mul_i32 s69, s69, s70\n\t" \
    "s_mul_hi_u32 s69, s69, 76695844\n\t" \
    "s_mov_b32 s75, s73\n\t" \
    "s_mov_b32 s76, s74\n\t" \
    "s_add_u32 s73, s56, s69\n\t" \
    "s_lshl_b32 s74, s28, 4\n\t" \
    "qk_St97_%=:\n\t" \
    "qk_Hok92_%=:\n\t" \
    "v_max_u32_dpp v160, v160, v160 row_ror:1 row_mask:0xf bank_mask:0xf\n\t" \
    "s_nop 1\n\t" \
    "v_max_u32_dpp v160, v160, v160 row_ror:2 row_mask:0xf bank_mask:0xf\n\t" \
    "s_nop 1\n\t" \
    "v_max_u32_dpp v160, v160, v160 row_ror:4 row_mask:0xf bank_mask:0xf\n\t" \
    "s_nop 1\n\t" \
    "v_max_u32_dpp v160, v160, v160 row_ror:8 row_mask:0xf bank_mask:0xf\n\t" \
    "s_nop 0\n\t" \
    "v_readlane_b32 s69, v160, 0\n\t" \
    "v_readlane_b32 s71, v160, 16\n\t" \
    "s_nop 1\n\t" \
    "s_max_u32 s81, s69, s71\n\t" \
    "s_mul_hi_u32 s69, s85, s78\n\t" \
    "s_add_u32 s79, s81, s69\n\t" \
    "s_add_u32 s79, s79, s87\n\t" \
    "s_cmp_ge_u32 s65, 53\n\t" \
    "s_cselect_b32 s69, 32, 0\n\t" \
    "s_sub_u32 s79, s79, s69\n\t" \
    "s_cmp_lt_u32 s65, 6\n\t" \
    "s_cselect_b32 s69, 48, 0\n\t" \
    "s_add_u32 s79, s79, s69\n\t" \
    "s_add_u32 s80, s79, s88\n\t" \
    "s_sub_u32 s70, s79, s74\n\t" \
    "s_max_i32 s70, s70, 0\n\t" \
    "s_min_i32 s70, s70, 16000\n\t" \
    "s_lshl_b32 s70, s70, 1\n\t" \
    "s_mul_hi_u32 s70, s70, s77\n\t" \
    "s_add_u32 s70, s70, s73\n\t" \
    "v_mov_b32 v168, s70\n\t" \
    "ds_write_b32 v11, v168 offset:20480\n\t" \
    "s_and_b32 s69, s65, 7\n\t" \
    "s_lshl_b32 s69, s69, 18\n\t" \
    "s_add_u32 s69, s69, s63\n\t" \
    "s_add_u32 s52, s36, s69\n\t" \
    "s_addc_u32 s53, s37, 0\n\t" \
    "s_waitcnt lgkmcnt(0)\n\t" \
    "s_barrier\n\t" \
    "ds_write_b32 v12, v31 offset:0\n\t" \
    "ds_write_b32 v12, v31 offset:64\n\t" \
    "s_branch qk_go78_%=\n\t" \
    "qk_Gbad83_%=:\n\t" \
    "s_waitcnt vmcnt(0) lgkmcnt(0)\n\t" \
    "s_nop 7\n\t" \
    "v_mov_b32 v72, 0\n\t" \
    "v_mov_b32 v73, 0\n\t" \
    "v_mov_b32 v74, 0\n\t" \
    "v_mov_b32 v75, 0\n\t" \
    "v_mov_b32 v76, 0\n\t" \
    "v_mov_b32 v77, 0\n\t" \
    "v_mov_b32 v78, 0\n\t" \
    "v_mov_b32 v79, 0\n\t" \
    "v_mov_b32 v80, 0\n\t" \
    "v_mov_b32 v81, 0\n\t" \
    "v_mov_b32 v82, 0\n\t" \
    "v_mov_b32 v83, 0\n\t" \
    "v_mov_b32 v84, 0\n\t" \
    "v_mov_b32 v85, 0\n\t" \
    "v_mov_b32 v86, 0\n\t" \
    "v_mov_b32 v87, 0\n\t" \
    "s_cmp_eq_u32 s68, 0\n\t" \
    "s_cbranch_scc1 qk_F99_%=\n\t" \
    "s_sub_u32 s68, s68, 1\n\t" \
    "s_branch qk_G82_%=\n\t" \
    "qk_F99_%=:\n\t" \
    "v_mov_b32 v160, 5767424\n\t" \
    "v_mov_b32 v161, 1\n\t" \
    "global_store_dword v160, v161, s[36:37] sc1\n\t" \
    "s_waitcnt vmcnt(0)\n\t" \
    "s_branch qk_Gc84_%=\n\t" \
    "qk_Gslow85_%=:\n\t" \
    "v_max_u32 v160, v36, v37\n\t" \
    "v_max3_u32 v160, v160, v38, v39\n\t" \
    "v_max3_u32 v160, v160, v40, v41\n\t" \
    "v_max3_u32 v160, v160, v42, v43\n\t" \
    "v_max3_u32 v160, v160, v44, v45\n\t" \
    "v_max3_u32 v160, v160, v46, v47\n\t" \
    "v_max3_u32 v160, v160, v48, v49\n\t" \
    "v_max3_u32 v160, v160, v50, v51\n\t" \
    "v_max3_u32 v160, v160, v52, v53\n\t" \
    "v_max3_u32 v160, v160, v54, v55\n\t" \
    "v_max3_u32 v160, v160, v56, v57\n\t" \
    "v_max3_u32 v160, v160, v58, v59\n\t" \
    "v_max3_u32 v160, v160, v60, v61\n\t" \
    "v_max3_u32 v160, v160, v62, v63\n\t" \
    "v_max3_u32 v160, v160, v64, v65\n\t" \
    "v_max3_u32 v160, v160, v66, v67\n\t" \
    "v_max3_u32 v160, v160, v68, v69\n\t" \
    "v_max3_u32 v160, v160, v70, v71\n\t" \
    "v_cmp_eq_u32 vcc, -1, v160\n\t" \
    "s_cbranch_vccz qk_Gback86_%=\n\t" \
    "s_waitcnt lgkmcnt(0)\n\t" \
    "v_sub_u32 v160, v1, v3\n\t" \
    "s_nop 0\n\t" \
    "v_readfirstlane_b32 s69, v160\n\t" \
    "v_add_u32 v163, 20480, v0\n\t" \
    "s_nop 7\n\t" \
    "s_add_u32 s30, s46, s69\n\t" \
    "s_addc_u32 s31, s47, 0\n\t" \
    "qk_Gag100_%=:\n\t" \
    "global_load_dwordx4 v[36:39], v1, s[46:47] sc1\n\t" \
    "global_load_dwordx4 v[40:43], v23, s[46:47] sc1\n\t" \
    "global_load_dwordx4 v[44:47], v163, s[30:31] sc1\n\t" \
    "global_load_dwordx4 v[48:51], v4, s[30:31] sc1\n\t" \
    "global_load_dwordx4 v[52:55], v5, s[30:31] sc1\n\t" \
    "global_load_dwordx4 v[56:59], v6, s[30:31] sc1\n\t" \
    "global_load_dwordx4 v[60:63], v7, s[30:31] sc1\n\t" \
    "global_load_dwordx4 v[64:67], v8, s[30:31] sc1\n\t" \
    "global_load_dwordx4 v[68:71], v9, s[30:31] sc1\n\t" \
    "s_waitcnt vmcnt(0)\n\t" \
    "v_max_u32 v160, v36, v37\n\t" \
    "v_max3_u32 v160, v160, v38, v39\n\t" \
    "v_max3_u32 v160, v160, v40, v41\n\t" \
    "v_max3_u32 v160, v160, v42, v43\n\t" \
    "v_max3_u32 v160, v160, v44, v45\n\t" \
    "v_max3_u32 v160, v160, v46, v47\n\t" \
    "v_max3_u32 v160, v160, v48, v49\n\t" \
    "v_max3_u32 v160, v160, v50, v51\n\t" \
    "v_max3_u32 v160, v160, v52, v53\n\t" \
    "v_max3_u32 v160, v160, v54, v55\n\t" \
    "v_max3_u32 v160, v160, v56, v57\n\t" \
    "v_max3_u32 v160, v160, v58, v59\n\t" \
    "v_max3_u32 v160, v160, v60, v61\n\t" \
    "v_max3_u32 v160, v160, v62, v63\n\t" \
    "v_max3_u32 v160, v160, v64, v65\n\t" \
    "v_max3_u32 v160, v160, v66, v67\n\t" \
    "v_max3_u32 v160, v160, v68, v69\n\t" \
    "v_max3_u32 v160, v160, v70, v71\n\t" \
    "v_cmp_eq_u32 vcc, -1, v160\n\t" \
    "s_cbranch_vccz qk_Gredo101_%=\n\t" \
    "s_cmp_eq_u32 s68, 0\n\t" \
    "s_cbranch_scc1 qk_F102_%=\n\t" \
    "s_sub_u32 s68, s68, 1\n\t" \
    "s_sleep 1\n\t" \
    "s_branch qk_Gag100_%=\n\t" \
    "qk_F102_%=:\n\t" \
    "v_mov_b32 v160, 5767424\n\t" \
    "v_mov_b32 v161, 2\n\t" \
    "global_store_dword v160, v161, s[36:37] sc1\n\t" \
    "s_waitcnt vmcnt(0)\n\t" \
    "s_branch qk_Gredo101_%=\n\t" \
    "qk_Gredo101_%=:\n\t" \
    "v_mov_b32 v72, 0\n\t" \
    "v_mov_b32 v73, 0\n\t" \
    "v_mov_b32 v74, 0\n\t" \
    "v_mov_b32 v75, 0\n\t" \
    "v_mov_b32 v76, 0\n\t" \
    "v_mov_b32 v77, 0\n\t" \
    "v_mov_b32 v78, 0\n\t" \
    "v_mov_b32 v79, 0\n\t" \
    "v_mov_b32 v80, 0\n\t" \
    "v_mov_b32 v81, 0\n\t" \
    "v_mov_b32 v82, 0\n\t" \
    "v_mov_b32 v83, 0\n\t" \
    "v_mov_b32 v84, 0\n\t" \
    "v_mov_b32 v85, 0\n\t" \
    "v_mov_b32 v86, 0\n\t" \
    "v_mov_b32 v87, 0\n\t" \
    "v_smfmac_f32_16x16x64_bf16 v[72:75], v[36:39], a[48:55], v10\n\t" \
    "v_smfmac_f32_16x16x64_bf16 v[72:75], v[40:43], a[56:63], v10\n\t" \
    "v_smfmac_f32_16x16x64_bf16 v[72:75], v[48:51], a[0:7], v10\n\t" \
    "v_smfmac_f32_16x16x64_bf16 v[72:75], v[52:55], a[8:15], v10\n\t" \
    "v_smfmac_f32_16x16x64_bf16 v[72:75], v[56:59], a[16:23], v10\n\t" \
    "v_smfmac_f32_16x16x64_bf16 v[72:75], v[60:63], a[24:31], v10\n\t" \
    "v_smfmac_f32_16x16x64_bf16 v[72:75], v[64:67], a[32:39], v10\n\t" \
    "v_smfmac_f32_16x16x64_bf16 v[72:75], v[68:71], a[40:47], v10\n\t" \
    "v_smfmac_f32_16x16x64_bf16 v[72:75], v[44:47], a[64:71], v10\n\t" \
    "v_smfmac_f32_16x16x64_bf16 v[76:79], v[36:39], a[120:127], v10\n\t" \
    "v_smfmac_f32_16x16x64_bf16 v[76:79], v[40:43], a[128:135], v10\n\t" \
    "v_smfmac_f32_16x16x64_bf16 v[76:79], v[48:51], a[72:79], v10\n\t" \
    "v_smfmac_f32_16x16x64_bf16 v[76:79], v[52:55], a[80:87], v10\n\t" \
    "v_smfmac_f32_16x16x64_bf16 v[76:79], v[56:59], a[88:95], v10\n\t" \
    "v_smfmac_f32_16x16x64_bf16 v[76:79], v[60:63], a[96:103], v10\n\t" \
    "v_smfmac_f32_16x16x64_bf16 v[76:79], v[64:67], a[104:111], v10\n\t" \
    "v_smfmac_f32_16x16x64_bf16 v[76:79], v[68:71], a[112:119], v10\n\t" \
    "v_smfmac_f32_16x16x64_bf16 v[76:79], v[44:47], a[136:143], v10\n\t" \
    "v_smfmac_f32_16x16x64_bf16 v[80:83], v[36:39], a[192:199], v10\n\t" \
    "v_smfmac_f32_16x16x64_bf16 v[80:83], v[40:43], a[200:207], v10\n\t" \
    "v_smfmac_f32_16x16x64_bf16 v[80:83], v[48:51], a[144:151], v10\n\t" \
    "v_smfmac_f32_16x16x64_bf16 v[80:83], v[52:55], a[152:159], v10\n\t" \
    "v_smfmac_f32_16x16x64_bf16 v[80:83], v[56:59], a[160:167], v10\n\t" \
    "v_smfmac_f32_16x16x64_bf16 v[80:83], v[60:63], a[168:175], v10\n\t" \
    "v_smfmac_f32_16x16x64_bf16 v[80:83], v[64:67], a[176:183], v10\n\t" \
    "v_smfmac_f32_16x16x64_bf16 v[80:83], v[68:71], a[184:191], v10\n\t" \
    "v_smfmac_f32_16x16x64_bf16 v[80:83], v[44:47], a[208:215], v10\n\t" \
    "v_smfmac_f32_16x16x64_bf16 v[84:87], v[36:39], v[232:239], v10\n\t" \
    "v_smfmac_f32_16x16x64_bf16 v[84:87], v[40:43], v[240:247], v10\n\t" \
    "v_smfmac_f32_16x16x64_bf16 v[84:87], v[48:51], a[216:223], v10\n\t" \
    "v_smfmac_f32_16x16x64_bf16 v[84:87], v[52:55], a[224:231], v10\n\t" \
    "v_smfmac_f32_16x16x64_bf16 v[84:87], v[56:59], a[232:239], v10\n\t" \
    "v_smfmac_f32_16x16x64_bf16 v[84:87], v[60:63], a[240:247], v10\n\t" \
    "v_smfmac_f32_16x16x64_bf16 v[84:87], v[64:67], a[248:255], v10\n\t" \
    "v_smfmac_f32_16x16x64_bf16 v[84:87], v[68:71], v[224:231], v10\n\t" \
    "v_smfmac_f32_16x16x64_bf16 v[84:87], v[44:47], v[248:255], v10\n\t" \
    "v_add_f32 v72, v72, v74\n\t" \
    "v_add_f32 v73, v73, v75\n\t" \
    "v_cvt_pk_bf16_f32 v156, v72, v73\n\t" \
    "ds_write_b32 v12, v156 offset:0\n\t" \
    "v_add_f32 v76, v76, v78\n\t" \
    "v_add_f32 v77, v77, v79\n\t" \
    "v_cvt_pk_bf16_f32 v157, v76, v77\n\t" \
    "ds_write_b32 v12, v157 offset:64\n\t" \
    "v_add_f32 v80, v80, v82\n\t" \
    "v_add_f32 v81, v81, v83\n\t" \
    "v_cvt_pk_bf16_f32 v158, v80, v81\n\t" \
    "ds_write_b32 v16, v158 offset:0\n\t" \
    "v_pk_add_f32 v[84:85], v[84:85], v[86:87]\n\t" \
    "v_cvt_pk_bf16_f32 v159, v84, v85\n\t" \
    "ds_write_b32 v16, v159 offset:256\n\t" \
    "s_branch qk_Gback86_%=\n\t" \
    "qk_last77_%=:\n\t" \
    "s_endpgm\n\t" \
    "qk_last_%=:\n\t" \
    ""
#define ASM_V \
    "s_load_dwordx2 s[42:43], %3, 56\n\t" \
    "s_load_dwordx2 s[44:45], %3, 64\n\t" \
    "s_load_dwordx2 s[36:37], %3, 0x58\n\t" \
    "s_load_dwordx2 s[50:51], %3, 0x48\n\t" \
    "s_load_dwordx8 s[84:91], %3, 0\n\t" \
    "s_mov_b32 s66, %4\n\t" \
    "s_mov_b32 s67, %5\n\t" \
    "s_mov_b32 s68, 262144\n\t" \
    "s_mov_b32 s72, 0\n\t" \
    "s_mov_b32 s83, -1\n\t" \
    "s_mov_b32 s65, 0\n\t" \
    "s_lshl_b32 s82, s66, 10\n\t" \
    "s_waitcnt lgkmcnt(0)\n\t" \
    "s_add_u32 s46, s42, 2097152\n\t" \
    "s_addc_u32 s47, s43, 0\n\t" \
    "s_add_u32 m0, s82, 65536\n\t" \
    "s_nop 0\n\t" \
    "global_load_lds_dwordx4 v200, s[46:47]\n\t" \
    "s_add_u32 s46, s42, 2228224\n\t" \
    "s_addc_u32 s47, s43, 0\n\t" \
    "s_add_u32 m0, s82, 69632\n\t" \
    "s_nop 0\n\t" \
    "global_load_lds_dwordx4 v200, s[46:47]\n\t" \
    "s_add_u32 s46, s42, 2359296\n\t" \
    "s_addc_u32 s47, s43, 0\n\t" \
    "s_add_u32 m0, s82, 73728\n\t" \
    "s_nop 0\n\t" \
    "global_load_lds_dwordx4 v200, s[46:47]\n\t" \
    "s_add_u32 s46, s42, 2490368\n\t" \
    "s_addc_u32 s47, s43, 0\n\t" \
    "s_add_u32 m0, s82, 77824\n\t" \
    "s_nop 0\n\t" \
    "global_load_lds_dwordx4 v200, s[46:47]\n\t" \
    "s_add_u32 s46, s42, 2621440\n\t" \
    "s_addc_u32 s47, s43, 0\n\t" \
    "s_add_u32 m0, s82, 81920\n\t" \
    "s_nop 0\n\t" \
    "global_load_lds_dwordx4 v200, s[46:47]\n\t" \
    "s_add_u32 s46, s42, 2752512\n\t" \
    "s_addc_u32 s47, s43, 0\n\t" \
    "s_add_u32 m0, s82, 86016\n\t" \
    "s_nop 0\n\t" \
    "global_load_lds_dwordx4 v200, s[46:47]\n\t" \
    "s_add_u32 s46, s42, 2883584\n\t" \
    "s_addc_u32 s47, s43, 0\n\t" \
    "s_add_u32 m0, s82, 90112\n\t" \
    "s_nop 0\n\t" \
    "global_load_lds_dwordx4 v200, s[46:47]\n\t" \
    "s_add_u32 s46, s42, 3014656\n\t" \
    "s_addc_u32 s47, s43, 0\n\t" \
    "s_add_u32 m0, s82, 94208\n\t" \
    "s_nop 0\n\t" \
    "global_load_lds_dwordx4 v200, s[46:47]\n\t" \
    "s_add_u32 s46, s42, 3145728\n\t" \
    "s_addc_u32 s47, s43, 0\n\t" \
    "s_add_u32 m0, s82, 98304\n\t" \
    "s_nop 0\n\t" \
    "global_load_lds_dwordx4 v200, s[46:47]\n\t" \
    "s_add_u32 s46, s42, 3276800\n\t" \
    "s_addc_u32 s47, s43, 0\n\t" \
    "s_add_u32 m0, s82, 102400\n\t" \
    "s_nop 0\n\t" \
    "global_load_lds_dwordx4 v200, s[46:47]\n\t" \
    "s_add_u32 s46, s42, 3407872\n\t" \
    "s_addc_u32 s47, s43, 0\n\t" \
    "s_add_u32 m0, s82, 106496\n\t" \
    "s_nop 0\n\t" \
    "global_load_lds_dwordx4 v200, s[46:47]\n\t" \
    "s_add_u32 s46, s42, 3538944\n\t" \
    "s_addc_u32 s47, s43, 0\n\t" \
    "s_add_u32 m0, s82, 110592\n\t" \
    "s_nop 0\n\t" \
    "global_load_lds_dwordx4 v200, s[46:47]\n\t" \
    "s_add_u32 s46, s42, 3670016\n\t" \
    "s_addc_u32 s47, s43, 0\n\t" \
    "s_add_u32 m0, s82, 114688\n\t" \
    "s_nop 0\n\t" \
    "global_load_lds_dwordx4 v200, s[46:47]\n\t" \
    "s_add_u32 s46, s42, 3801088\n\t" \
    "s_addc_u32 s47, s43, 0\n\t" \
    "s_add_u32 m0, s82, 118784\n\t" \
    "s_nop 0\n\t" \
    "global_load_lds_dwordx4 v200, s[46:47]\n\t" \
    "s_add_u32 s46, s42, 3932160\n\t" \
    "s_addc_u32 s47, s43, 0\n\t" \
    "s_add_u32 m0, s82, 122880\n\t" \
    "s_nop 0\n\t" \
    "global_load_lds_dwordx4 v200, s[46:47]\n\t" \
    "s_add_u32 s46, s42, 4063232\n\t" \
    "s_addc_u32 s47, s43, 0\n\t" \
    "s_add_u32 m0, s82, 126976\n\t" \
    "s_nop 0\n\t" \
    "global_load_lds_dwordx4 v200, s[46:47]\n\t" \
    "s_add_u32 s46, s42, 4194304\n\t" \
    "s_addc_u32 s47, s43, 0\n\t" \
    "s_add_u32 m0, s82, 131072\n\t" \
    "s_nop 0\n\t" \
    "global_load_lds_dwordx4 v200, s[46:47]\n\t" \
    "s_add_u32 s46, s42, 4325376\n\t" \
    "s_addc_u32 s47, s43, 0\n\t" \
    "s_add_u32 m0, s82, 135168\n\t" \
    "s_nop 0\n\t" \
    "global_load_lds_dwordx4 v200, s[46:47]\n\t" \
    "s_add_u32 s46, s42, 4456448\n\t" \
    "s_addc_u32 s47, s43, 0\n\t" \
    "s_add_u32 m0, s82, 139264\n\t" \
    "s_nop 0\n\t" \
    "global_load_lds_dwordx4 v200, s[46:47]\n\t" \
    "s_add_u32 s46, s42, 4587520\n\t" \
    "s_addc_u32 s47, s43, 0\n\t" \
    "s_add_u32 m0, s82, 143360\n\t" \
    "s_nop 0\n\t" \
    "global_load_lds_dwordx4 v200, s[46:47]\n\t" \
    "s_mov_b32 s77, 3019898880\n\t" \
    "s_mov_b32 s78, 3054198966\n\t" \
    "s_waitcnt lgkmcnt(0)\n\t" \
    "s_mov_b32 s70, 116\n\t" \
    "vv_E1_%=:\n\t" \
    "s_memtime s[56:57]\n\t" \
    "s_memrealtime s[2:3]\n\t" \
    "s_nop 0\n\t" \
    "s_memrealtime s[4:5]\n\t" \
    "s_nop 0\n\t" \
    "s_memrealtime s[6:7]\n\t" \
    "s_nop 0\n\t" \
    "s_memrealtime s[8:9]\n\t" \
    "s_nop 0\n\t" \
    "s_memrealtime s[10:11]\n\t" \
    "s_nop 0\n\t" \
    "s_memrealtime s[12:13]\n\t" \
    "s_nop 0\n\t" \
    "s_memrealtime s[14:15]\n\t" \
    "s_nop 0\n\t" \
    "s_memrealtime s[16:17]\n\t" \
    "s_nop 0\n\t" \
    "s_memrealtime s[18:19]\n\t" \
    "s_nop 0\n\t" \
    "s_memrealtime s[20:21]\n\t" \
    "s_nop 0\n\t" \
    "s_memrealtime s[22:23]\n\t" \
    "s_nop 0\n\t" \
    "s_memrealtime s[24:25]\n\t" \
    "s_nop 0\n\t" \
    "s_memrealtime s[26:27]\n\t" \
    "s_nop 0\n\t" \
    "s_memrealtime s[28:29]\n\t" \
    "s_memtime s[54:55]\n\t" \
    "s_add_u32 s70, s70, 8\n\t" \
    "s_waitcnt lgkmcnt(0)\n\t" \
    "s_sub_u32 s69, s54, s56\n\t" \
    "s_cmp_gt_u32 s69, s70\n\t" \
    "s_cbranch_scc1 vv_E1_%=\n\t" \
    "s_add_u32 s2, s2, s4\n\t" \
    "s_add_u32 s6, s6, s8\n\t" \
    "s_add_u32 s10, s10, s12\n\t" \
    "s_add_u32 s14, s14, s16\n\t" \
    "s_add_u32 s18, s18, s20\n\t" \
    "s_add_u32 s22, s22, s24\n\t" \
    "s_add_u32 s2, s2, s6\n\t" \
    "s_add_u32 s10, s10, s14\n\t" \
    "s_add_u32 s18, s18, s22\n\t" \
    "s_add_u32 s2, s2, s26\n\t" \
    "s_add_u32 s10, s10, s18\n\t" \
    "s_add_u32 s2, s2, s10\n\t" \
    "s_mul_i32 s70, s28, 13\n\t" \
    "s_sub_u32 s70, s70, s2\n\t" \
    "s_min_u32 s70, s70, 52\n\t" \
    "s_mul_i32 s69, s69, s70\n\t" \
    "s_mul_hi_u32 s69, s69, 76695844\n\t" \
    "s_mov_b32 s75, s73\n\t" \
    "s_mov_b32 s76, s74\n\t" \
    "s_add_u32 s73, s56, s69\n\t" \
    "s_lshl_b32 s74, s28, 4\n\t" \
    "s_waitcnt vmcnt(32) lgkmcnt(0)\n\t" \
    "s_barrier\n\t" \
    "ds_read_b64_tr_b16 a[0:1], v201 offset:0\n\t" \
    "ds_read_b64_tr_b16 a[2:3], v201 offset:1024\n\t" \
    "ds_read_b64_tr_b16 a[4:5], v201 offset:8192\n\t" \
    "ds_read_b64_tr_b16 a[6:7], v201 offset:9216\n\t" \
    "ds_read_b64_tr_b16 a[72:73], v202 offset:0\n\t" \
    "ds_read_b64_tr_b16 a[74:75], v202 offset:1024\n\t" \
    "ds_read_b64_tr_b16 a[76:77], v202 offset:8192\n\t" \
    "ds_read_b64_tr_b16 a[78:79], v202 offset:9216\n\t" \
    "s_waitcnt vmcnt(28) lgkmcnt(0)\n\t" \
    "s_barrier\n\t" \
    "ds_read_b64_tr_b16 a[8:9], v201 offset:16384\n\t" \
    "ds_read_b64_tr_b16 a[10:11], v201 offset:17408\n\t" \
    "ds_read_b64_tr_b16 a[12:13], v201 offset:24576\n\t" \
    "ds_read_b64_tr_b16 a[14:15], v201 offset:25600\n\t" \
    "ds_read_b64_tr_b16 a[80:81], v202 offset:16384\n\t" \
    "ds_read_b64_tr_b16 a[82:83], v202 offset:17408\n\t" \
    "ds_read_b64_tr_b16 a[84:85], v202 offset:24576\n\t" \
    "ds_read_b64_tr_b16 a[86:87], v202 offset:25600\n\t" \
    "s_waitcnt vmcnt(24) lgkmcnt(0)\n\t" \
    "s_barrier\n\t" \
    "ds_read_b64_tr_b16 a[16:17], v201 offset:32768\n\t" \
    "ds_read_b64_tr_b16 a[18:19], v201 offset:33792\n\t" \
    "ds_read_b64_tr_b16 a[20:21], v201 offset:40960\n\t" \
    "ds_read_b64_tr_b16 a[22:23], v201 offset:41984\n\t" \
    "ds_read_b64_tr_b16 a[88:89], v202 offset:32768\n\t" \
    "ds_read_b64_tr_b16 a[90:91], v202 offset:33792\n\t" \
    "ds_read_b64_tr_b16 a[92:93], v202 offset:40960\n\t" \
    "ds_read_b64_tr_b16 a[94:95], v202 offset:41984\n\t" \
    "s_waitcnt vmcnt(20) lgkmcnt(0)\n\t" \
    "s_barrier\n\t" \
    "ds_read_b64_tr_b16 a[24:25], v201 offset:49152\n\t" \
    "ds_read_b64_tr_b16 a[26:27], v201 offset:50176\n\t" \
    "ds_read_b64_tr_b16 a[28:29], v201 offset:57344\n\t" \
    "ds_read_b64_tr_b16 a[30:31], v201 offset:58368\n\t" \
    "ds_read_b64_tr_b16 a[96:97], v202 offset:49152\n\t" \
    "ds_read_b64_tr_b16 a[98:99], v202 offset:50176\n\t" \
    "ds_read_b64_tr_b16 a[100:101], v202 offset:57344\n\t" \
    "ds_read_b64_tr_b16 a[102:103], v202 offset:58368\n\t" \
    "s_add_u32 s46, s44, 0\n\t" \
    "s_addc_u32 s47, s45, 0\n\t" \
    "s_mul_i32 s69, s82, 9\n\t" \
    "s_add_u32 m0, s69, 0\n\t" \
    "s_nop 0\n\t" \
    "global_load_lds_dwordx4 v203, s[46:47]\n\t" \
    "s_add_u32 m0, s69, 1024\n\t" \
    "s_nop 0\n\t" \
    "global_load_lds_dwordx4 v204, s[46:47]\n\t" \
    "s_add_u32 m0, s69, 2048\n\t" \
    "s_nop 0\n\t" \
    "global_load_lds_dwordx4 v205, s[46:47]\n\t" \
    "s_add_u32 m0, s69, 3072\n\t" \
    "s_nop 0\n\t" \
    "global_load_lds_dwordx4 v206, s[46:47]\n\t" \
    "s_add_u32 m0, s69, 4096\n\t" \
    "s_nop 0\n\t" \
    "global_load_lds_dwordx4 v207, s[46:47]\n\t" \
    "s_add_u32 m0, s69, 5120\n\t" \
    "s_nop 0\n\t" \
    "global_load_lds_dwordx4 v208, s[46:47]\n\t" \
    "s_add_u32 m0, s69, 6144\n\t" \
    "s_nop 0\n\t" \
    "global_load_lds_dwordx4 v209, s[46:47]\n\t" \
    "s_add_u32 m0, s69, 7168\n\t" \
    "s_nop 0\n\t" \
    "global_load_lds_dwordx4 v210, s[46:47]\n\t" \
    "s_add_u32 m0, s69, 8192\n\t" \
    "s_nop 0\n\t" \
    "global_load_lds_dwordx4 v211, s[46:47]\n\t" \
    "s_waitcnt vmcnt(25) lgkmcnt(0)\n\t" \
    "s_barrier\n\t" \
    "v_add_u32 v201, 0x10000, v201\n\t" \
    "v_add_u32 v202, 0x10000, v202\n\t" \
    "ds_read_b64_tr_b16 a[32:33], v201 offset:0\n\t" \
    "ds_read_b64_tr_b16 a[34:35], v201 offset:1024\n\t" \
    "ds_read_b64_tr_b16 a[36:37], v201 offset:8192\n\t" \
    "ds_read_b64_tr_b16 a[38:39], v201 offset:9216\n\t" \
    "ds_read_b64_tr_b16 a[104:105], v202 offset:0\n\t" \
    "ds_read_b64_tr_b16 a[106:107], v202 offset:1024\n\t" \
    "ds_read_b64_tr_b16 a[108:109], v202 offset:8192\n\t" \
    "ds_read_b64_tr_b16 a[110:111], v202 offset:9216\n\t" \
    "s_waitcnt vmcnt(21) lgkmcnt(0)\n\t" \
    "s_barrier\n\t" \
    "ds_read_b64_tr_b16 a[40:41], v201 offset:16384\n\t" \
    "ds_read_b64_tr_b16 a[42:43], v201 offset:17408\n\t" \
    "ds_read_b64_tr_b16 a[44:45], v201 offset:24576\n\t" \
    "ds_read_b64_tr_b16 a[46:47], v201 offset:25600\n\t" \
    "ds_read_b64_tr_b16 a[112:113], v202 offset:16384\n\t" \
    "ds_read_b64_tr_b16 a[114:115], v202 offset:17408\n\t" \
    "ds_read_b64_tr_b16 a[116:117], v202 offset:24576\n\t" \
    "ds_read_b64_tr_b16 a[118:119], v202 offset:25600\n\t" \
    "s_add_u32 s46, s44, 36864\n\t" \
    "s_addc_u32 s47, s45, 0\n\t" \
    "s_mul_i32 s69, s82, 9\n\t" \
    "s_add_u32 m0, s69, 36864\n\t" \
    "s_nop 0\n\t" \
    "global_load_lds_dwordx4 v203, s[46:47]\n\t" \
    "s_add_u32 m0, s69, 37888\n\t" \
    "s_nop 0\n\t" \
    "global_load_lds_dwordx4 v204, s[46:47]\n\t" \
    "s_add_u32 m0, s69, 38912\n\t" \
    "s_nop 0\n\t" \
    "global_load_lds_dwordx4 v205, s[46:47]\n\t" \
    "s_add_u32 m0, s69, 39936\n\t" \
    "s_nop 0\n\t" \
    "global_load_lds_dwordx4 v206, s[46:47]\n\t" \
    "s_add_u32 m0, s69, 40960\n\t" \
    "s_nop 0\n\t" \
    "global_load_lds_dwordx4 v207, s[46:47]\n\t" \
    "s_add_u32 m0, s69, 41984\n\t" \
    "s_nop 0\n\t" \
    "global_load_lds_dwordx4 v208, s[46:47]\n\t" \
    "s_add_u32 m0, s69, 43008\n\t" \
    "s_nop 0\n\t" \
    "global_load_lds_dwordx4 v209, s[46:47]\n\t" \
    "s_add_u32 m0, s69, 44032\n\t" \
    "s_nop 0\n\t" \
    "global_load_lds_dwordx4 v210, s[46:47]\n\t" \
    "s_add_u32 m0, s69, 45056\n\t" \
    "s_nop 0\n\t" \
    "global_load_lds_dwordx4 v211, s[46:47]\n\t" \
    "s_waitcnt vmcnt(26) lgkmcnt(0)\n\t" \
    "s_barrier\n\t" \
    "ds_read_b64_tr_b16 a[48:49], v201 offset:32768\n\t" \
    "ds_read_b64_tr_b16 a[50:51], v201 offset:33792\n\t" \
    "ds_read_b64_tr_b16 a[52:53], v201 offset:40960\n\t" \
    "ds_read_b64_tr_b16 a[54:55], v201 offset:41984\n\t" \
    "ds_read_b64_tr_b16 a[120:121], v202 offset:32768\n\t" \
    "ds_read_b64_tr_b16 a[122:123], v202 offset:33792\n\t" \
    "ds_read_b64_tr_b16 a[124:125], v202 offset:40960\n\t" \
    "ds_read_b64_tr_b16 a[126:127], v202 offset:41984\n\t" \
    "s_waitcnt vmcnt(22) lgkmcnt(0)\n\t" \
    "s_barrier\n\t" \
    "ds_read_b64_tr_b16 a[56:57], v201 offset:49152\n\t" \
    "ds_read_b64_tr_b16 a[58:59], v201 offset:50176\n\t" \
    "ds_read_b64_tr_b16 a[60:61], v201 offset:57344\n\t" \
    "ds_read_b64_tr_b16 a[62:63], v201 offset:58368\n\t" \
    "ds_read_b64_tr_b16 a[128:129], v202 offset:49152\n\t" \
    "ds_read_b64_tr_b16 a[130:131], v202 offset:50176\n\t" \
    "ds_read_b64_tr_b16 a[132:133], v202 offset:57344\n\t" \
    "ds_read_b64_tr_b16 a[134:135], v202 offset:58368\n\t" \
    "s_add_u32 s46, s44, 73728\n\t" \
    "s_addc_u32 s47, s45, 0\n\t" \
    "s_mul_i32 s69, s82, 9\n\t" \
    "s_add_u32 m0, s69, 73728\n\t" \
    "s_nop 0\n\t" \
    "global_load_lds_dwordx4 v203, s[46:47]\n\t" \
    "s_add_u32 m0, s69, 74752\n\t" \
    "s_nop 0\n\t" \
    "global_load_lds_dwordx4 v204, s[46:47]\n\t" \
    "s_add_u32 m0, s69, 75776\n\t" \
    "s_nop 0\n\t" \
    "global_load_lds_dwordx4 v205, s[46:47]\n\t" \
    "s_add_u32 m0, s69, 76800\n\t" \
    "s_nop 0\n\t" \
    "global_load_lds_dwordx4 v206, s[46:47]\n\t" \
    "s_add_u32 m0, s69, 77824\n\t" \
    "s_nop 0\n\t" \
    "global_load_lds_dwordx4 v207, s[46:47]\n\t" \
    "s_add_u32 m0, s69, 78848\n\t" \
    "s_nop 0\n\t" \
    "global_load_lds_dwordx4 v208, s[46:47]\n\t" \
    "s_add_u32 m0, s69, 79872\n\t" \
    "s_nop 0\n\t" \
    "global_load_lds_dwordx4 v209, s[46:47]\n\t" \
    "s_add_u32 m0, s69, 80896\n\t" \
    "s_nop 0\n\t" \
    "global_load_lds_dwordx4 v210, s[46:47]\n\t" \
    "s_add_u32 m0, s69, 81920\n\t" \
    "s_nop 0\n\t" \
    "global_load_lds_dwordx4 v211, s[46:47]\n\t" \
    "v_mbcnt_lo_u32_b32 v140, -1, 0\n\t" \
    "v_mbcnt_hi_u32_b32 v140, -1, v140\n\t" \
    "v_lshlrev_b32 v140, 6, v140\n\t" \
    "v_add_u32 v140, 6225920, v140\n\t" \
    "v_mov_b32 v141, 6234112\n\t" \
    "global_load_dword v141, v141, s[36:37] sc1\n\t" \
    "global_load_dwordx2 v[142:143], v140, s[36:37] sc1\n\t" \
    "global_load_dword v144, v140, s[36:37] offset:2048 sc1\n\t" \
    "s_waitcnt vmcnt(30) lgkmcnt(0)\n\t" \
    "s_barrier\n\t" \
    "v_add_u32 v201, 0x10000, v201\n\t" \
    "v_add_u32 v202, 0x10000, v202\n\t" \
    "ds_read_b64_tr_b16 a[64:65], v201 offset:0\n\t" \
    "ds_read_b64_tr_b16 a[66:67], v201 offset:1024\n\t" \
    "ds_read_b64_tr_b16 a[68:69], v201 offset:8192\n\t" \
    "ds_read_b64_tr_b16 a[70:71], v201 offset:9216\n\t" \
    "ds_read_b64_tr_b16 a[136:137], v202 offset:0\n\t" \
    "ds_read_b64_tr_b16 a[138:139], v202 offset:1024\n\t" \
    "ds_read_b64_tr_b16 a[140:141], v202 offset:8192\n\t" \
    "ds_read_b64_tr_b16 a[142:143], v202 offset:9216\n\t" \
    "v_add_u32 v201, 0xfffe0000, v201\n\t" \
    "v_add_u32 v202, 0xfffe0000, v202\n\t" \
    "s_waitcnt lgkmcnt(0)\n\t" \
    "s_barrier\n\t" \
    "s_add_u32 s46, s44, 110592\n\t" \
    "s_addc_u32 s47, s45, 0\n\t" \
    "s_mul_i32 s69, s82, 9\n\t" \
    "s_add_u32 m0, s69, 110592\n\t" \
    "s_nop 0\n\t" \
    "global_load_lds_dwordx4 v203, s[46:47]\n\t" \
    "s_add_u32 m0, s69, 111616\n\t" \
    "s_nop 0\n\t" \
    "global_load_lds_dwordx4 v204, s[46:47]\n\t" \
    "s_add_u32 m0, s69, 112640\n\t" \
    "s_nop 0\n\t" \
    "global_load_lds_dwordx4 v205, s[46:47]\n\t" \
    "s_add_u32 m0, s69, 113664\n\t" \
    "s_nop 0\n\t" \
    "global_load_lds_dwordx4 v206, s[46:47]\n\t" \
    "s_add_u32 m0, s69, 114688\n\t" \
    "s_nop 0\n\t" \
    "global_load_lds_dwordx4 v207, s[46:47]\n\t" \
    "s_add_u32 m0, s69, 115712\n\t" \
    "s_nop 0\n\t" \
    "global_load_lds_dwordx4 v208, s[46:47]\n\t" \
    "s_add_u32 m0, s69, 116736\n\t" \
    "s_nop 0\n\t" \
    "global_load_lds_dwordx4 v209, s[46:47]\n\t" \
    "s_add_u32 m0, s69, 117760\n\t" \
    "s_nop 0\n\t" \
    "global_load_lds_dwordx4 v210, s[46:47]\n\t" \
    "s_add_u32 m0, s69, 118784\n\t" \
    "s_nop 0\n\t" \
    "global_load_lds_dwordx4 v211, s[46:47]\n\t" \
    "s_waitcnt vmcnt(21) lgkmcnt(0)\n\t" \
    "s_barrier\n\t" \
    "ds_read_b64_tr_b16 a[144:145], v212 offset:0\n\t" \
    "ds_read_b64_tr_b16 a[146:147], v212 offset:2304\n\t" \
    "ds_read_b64_tr_b16 a[148:149], v212 offset:18432\n\t" \
    "ds_read_b64_tr_b16 a[150:151], v212 offset:20736\n\t" \
    "s_waitcnt lgkmcnt(6)\n\t" \
    "ds_read_b64_tr_b16 a[176:177], v213 offset:0\n\t" \
    "ds_read_b64_tr_b16 a[178:179], v213 offset:2304\n\t" \
    "ds_read_b64_tr_b16 a[180:181], v213 offset:18432\n\t" \
    "ds_read_b64_tr_b16 a[182:183], v213 offset:20736\n\t" \
    "s_waitcnt lgkmcnt(6)\n\t" \
    "ds_read_b64_tr_b16 a[208:209], v212 offset:128\n\t" \
    "ds_read_b64_tr_b16 a[210:211], v212 offset:2432\n\t" \
    "ds_read_b64_tr_b16 a[212:213], v212 offset:18560\n\t" \
    "ds_read_b64_tr_b16 a[214:215], v212 offset:20864\n\t" \
    "s_waitcnt lgkmcnt(6)\n\t" \
    "ds_read_b64_tr_b16 a[240:241], v213 offset:128\n\t" \
    "ds_read_b64_tr_b16 a[242:243], v213 offset:2432\n\t" \
    "ds_read_b64_tr_b16 a[244:245], v213 offset:18560\n\t" \
    "ds_read_b64_tr_b16 a[246:247], v213 offset:20864\n\t" \
    "s_waitcnt lgkmcnt(6)\n\t" \
    "ds_read_b64_tr_b16 a[152:153], v214 offset:0\n\t" \
    "ds_read_b64_tr_b16 a[154:155], v214 offset:2304\n\t" \
    "ds_read_b64_tr_b16 a[156:157], v214 offset:18432\n\t" \
    "ds_read_b64_tr_b16 a[158:159], v214 offset:20736\n\t" \
    "s_waitcnt lgkmcnt(6)\n\t" \
    "ds_read_b64_tr_b16 a[184:185], v215 offset:0\n\t" \
    "ds_read_b64_tr_b16 a[186:187], v215 offset:2304\n\t" \
    "ds_read_b64_tr_b16 a[188:189], v215 offset:18432\n\t" \
    "ds_read_b64_tr_b16 a[190:191], v215 offset:20736\n\t" \
    "s_waitcnt lgkmcnt(6)\n\t" \
    "ds_read_b64_tr_b16 a[216:217], v214 offset:128\n\t" \
    "ds_read_b64_tr_b16 a[218:219], v214 offset:2432\n\t" \
    "ds_read_b64_tr_b16 a[220:221], v214 offset:18560\n\t" \
    "ds_read_b64_tr_b16 a[222:223], v214 offset:20864\n\t" \
    "s_waitcnt lgkmcnt(6)\n\t" \
    "ds_read_b64_tr_b16 a[248:249], v215 offset:128\n\t" \
    "ds_read_b64_tr_b16 a[250:251], v215 offset:2432\n\t" \
    "ds_read_b64_tr_b16 a[252:253], v215 offset:18560\n\t" \
    "ds_read_b64_tr_b16 a[254:255], v215 offset:20864\n\t" \
    "s_waitcnt lgkmcnt(6)\n\t" \
    "ds_read_b64_tr_b16 v[240:241], v24 offset:0\n\t" \
    "ds_read_b64_tr_b16 v[242:243], v24 offset:2304\n\t" \
    "ds_read_b64_tr_b16 v[244:245], v24 offset:18432\n\t" \
    "ds_read_b64_tr_b16 v[246:247], v24 offset:20736\n\t" \
    "v_add_u32 v212, 0x12000, v212\n\t" \
    "v_add_u32 v213, 0x12000, v213\n\t" \
    "v_add_u32 v214, 0x12000, v214\n\t" \
    "v_add_u32 v215, 0x12000, v215\n\t" \
    "v_add_u32 v24, 0x12000, v24\n\t" \
    "s_waitcnt vmcnt(9)\n\t" \
    "vv_C2_%=:\n\t" \
    "v_cmp_ne_u32 s[54:55], v142, v144\n\t" \
    "v_sub_u32 v142, v142, v141\n\t" \
    "v_cmp_ne_u32 vcc, 1, v142\n\t" \
    "s_nop 1\n\t" \
    "s_or_b64 vcc, vcc, s[54:55]\n\t" \
    "s_cbranch_scc0 vv_Cok3_%=\n\t" \
    "s_cmp_eq_u32 s68, 0\n\t" \
    "s_cbranch_scc1 vv_F5_%=\n\t" \
    "s_sub_u32 s68, s68, 1\n\t" \
    "s_sleep 6\n\t" \
    "s_branch vv_Cag4_%=\n\t" \
    "vv_F5_%=:\n\t" \
    "v_mov_b32 v144, 5767424\n\t" \
    "v_mov_b32 v145, 4\n\t" \
    "global_store_dword v144, v145, s[36:37] sc1\n\t" \
    "s_waitcnt vmcnt(0)\n\t" \
    "s_branch vv_Cok3_%=\n\t" \
    "vv_Cag4_%=:\n\t" \
    "global_load_dwordx2 v[142:143], v140, s[36:37] sc1\n\t" \
    "global_load_dword v144, v140, s[36:37] offset:2048 sc1\n\t" \
    "s_waitcnt vmcnt(0)\n\t" \
    "s_branch vv_C2_%=\n\t" \
    "vv_Cok3_%=:\n\t" \
    "v_readlane_b32 s69, v143, 2\n\t" \
    "v_readlane_b32 s70, v143, 3\n\t" \
    "s_nop 1\n\t" \
    "s_cmp_lt_u32 s70, s69\n\t" \
    "s_cselect_b32 s69, 0, 8192\n\t" \
    "s_add_u32 s100, s36, s69\n\t" \
    "s_addc_u32 s101, s37, 0\n\t" \
    "s_cmp_lg_u32 s66, 0\n\t" \
    "s_cbranch_scc1 vv_Ca6_%=\n\t" \
    "v_mov_b32 v140, 5768192\n\t" \
    "s_mov_b64 exec, 1\n\t" \
    "global_atomic_add v140, v142, s[36:37] sc1\n\t" \
    "s_mov_b64 exec, -1\n\t" \
    "vv_Ca6_%=:\n\t" \
    "v_mov_b32_dpp v140, v143 quad_perm:[1,0,3,2] row_mask:0xf bank_mask:0xf\n\t" \
    "v_cmp_lt_u32 s[44:45], v140, v143\n\t" \
    "s_nop 1\n\t" \
    "s_bitcmp1_b32 s44, 0\n\t" \
    "s_cselect_b32 s62, 8192, 0\n\t" \
    "s_bitcmp1_b32 s44, 2\n\t" \
    "s_cselect_b32 s63, 8192, 0\n\t" \
    "s_or_b32 s63, s63, 0x200000\n\t" \
    "v_min_u32 v140, v140, v143\n\t" \
    "v_cmp_lt_u32 vcc, 660, v140\n\t" \
    "s_nop 1\n\t" \
    "s_bcnt1_i32_b64 s69, vcc\n\t" \
    "s_cmp_gt_u32 s69, 16\n\t" \
    "s_cselect_b32 s69, 1, 0\n\t" \
    "s_mul_i32 s70, s69, 160\n\t" \
    "s_add_u32 s86, s86, s70\n\t" \
    "s_mul_i32 s70, s69, 536\n\t" \
    "s_add_u32 s87, s87, s70\n\t" \
    "s_mul_i32 s70, s69, 256\n\t" \
    "s_add_u32 s88, s88, s70\n\t" \
    "s_waitcnt vmcnt(0) lgkmcnt(0)\n\t" \
    "s_barrier\n\t" \
    "ds_read_b64_tr_b16 a[160:161], v212 offset:0\n\t" \
    "ds_read_b64_tr_b16 a[162:163], v212 offset:2304\n\t" \
    "ds_read_b64_tr_b16 a[164:165], v212 offset:18432\n\t" \
    "ds_read_b64_tr_b16 a[166:167], v212 offset:20736\n\t" \
    "s_waitcnt lgkmcnt(6)\n\t" \
    "ds_read_b64_tr_b16 a[192:193], v213 offset:0\n\t" \
    "ds_read_b64_tr_b16 a[194:195], v213 offset:2304\n\t" \
    "ds_read_b64_tr_b16 a[196:197], v213 offset:18432\n\t" \
    "ds_read_b64_tr_b16 a[198:199], v213 offset:20736\n\t" \
    "s_waitcnt lgkmcnt(6)\n\t" \
    "ds_read_b64_tr_b16 a[224:225], v212 offset:128\n\t" \
    "ds_read_b64_tr_b16 a[226:227], v212 offset:2432\n\t" \
    "ds_read_b64_tr_b16 a[228:229], v212 offset:18560\n\t" \
    "ds_read_b64_tr_b16 a[230:231], v212 offset:20864\n\t" \
    "s_waitcnt lgkmcnt(6)\n\t" \
    "ds_read_b64_tr_b16 v[224:225], v213 offset:128\n\t" \
    "ds_read_b64_tr_b16 v[226:227], v213 offset:2432\n\t" \
    "ds_read_b64_tr_b16 v[228:229], v213 offset:18560\n\t" \
    "ds_read_b64_tr_b16 v[230:231], v213 offset:20864\n\t" \
    "s_waitcnt lgkmcnt(6)\n\t" \
    "ds_read_b64_tr_b16 a[168:169], v214 offset:0\n\t" \
    "ds_read_b64_tr_b16 a[170:171], v214 offset:2304\n\t" \
    "ds_read_b64_tr_b16 a[172:173], v214 offset:18432\n\t" \
    "ds_read_b64_tr_b16 a[174:175], v214 offset:20736\n\t" \
    "s_waitcnt lgkmcnt(6)\n\t" \
    "ds_read_b64_tr_b16 a[200:201], v215 offset:0\n\t" \
    "ds_read_b64_tr_b16 a[202:203], v215 offset:2304\n\t" \
    "ds_read_b64_tr_b16 a[204:205], v215 offset:18432\n\t" \
    "ds_read_b64_tr_b16 a[206:207], v215 offset:20736\n\t" \
    "s_waitcnt lgkmcnt(6)\n\t" \
    "ds_read_b64_tr_b16 a[232:233], v214 offset:128\n\t" \
    "ds_read_b64_tr_b16 a[234:235], v214 offset:2432\n\t" \
    "ds_read_b64_tr_b16 a[236:237], v214 offset:18560\n\t" \
    "ds_read_b64_tr_b16 a[238:239], v214 offset:20864\n\t" \
    "s_waitcnt lgkmcnt(6)\n\t" \
    "ds_read_b64_tr_b16 v[232:233], v215 offset:128\n\t" \
    "ds_read_b64_tr_b16 v[234:235], v215 offset:2432\n\t" \
    "ds_read_b64_tr_b16 v[236:237], v215 offset:18560\n\t" \
    "ds_read_b64_tr_b16 v[238:239], v215 offset:20864\n\t" \
    "s_waitcnt lgkmcnt(6)\n\t" \
    "ds_read_b64_tr_b16 v[248:249], v24 offset:0\n\t" \
    "ds_read_b64_tr_b16 v[250:251], v24 offset:2304\n\t" \
    "ds_read_b64_tr_b16 v[252:253], v24 offset:18432\n\t" \
    "ds_read_b64_tr_b16 v[254:255], v24 offset:20736\n\t" \
    "v_add_u32 v212, 0x12000, v212\n\t" \
    "v_add_u32 v213, 0x12000, v213\n\t" \
    "v_add_u32 v214, 0x12000, v214\n\t" \
    "v_add_u32 v215, 0x12000, v215\n\t" \
    "v_add_u32 v24, 0x12000, v24\n\t" \
    "s_waitcnt lgkmcnt(0)\n\t" \
    "s_barrier\n\t" \
    "s_mov_b32 s79, s74\n\t" \
    "s_sub_u32 s80, s74, 32000\n\t" \
    "v_mov_b32 v72, 0\n\t" \
    "v_mov_b32 v73, 0\n\t" \
    "v_mov_b32 v74, 0\n\t" \
    "v_mov_b32 v75, 0\n\t" \
    "v_mov_b32 v76, 0\n\t" \
    "v_mov_b32 v77, 0\n\t" \
    "v_mov_b32 v78, 0\n\t" \
    "v_mov_b32 v79, 0\n\t" \
    "v_mov_b32 v108, 0\n\t" \
    "v_mov_b32 v109, 0\n\t" \
    "v_mov_b32 v110, 0\n\t" \
    "v_mov_b32 v111, 0\n\t" \
    "v_mov_b32 v112, 0\n\t" \
    "v_mov_b32 v113, 0\n\t" \
    "v_mov_b32 v114, 0\n\t" \
    "v_mov_b32 v115, 0\n\t" \
    "v_mov_b32 v116, 0\n\t" \
    "v_mov_b32 v117, 0\n\t" \
    "v_mov_b32 v118, 0\n\t" \
    "v_mov_b32 v119, 0\n\t" \
    "v_mov_b32 v120, 0\n\t" \
    "v_mov_b32 v121, 0\n\t" \
    "v_mov_b32 v122, 0\n\t" \
    "v_mov_b32 v123, 0\n\t" \
    "v_mov_b32 v124, 0\n\t" \
    "v_mov_b32 v125, 0\n\t" \
    "v_mov_b32 v126, 0\n\t" \
    "v_mov_b32 v127, 0\n\t" \
    "s_add_u32 s69, s65, 0\n\t" \
    "s_and_b32 s69, s69, 7\n\t" \
    "s_lshl_b32 s69, s69, 18\n\t" \
    "s_add_u32 s69, s69, s62\n\t" \
    "s_add_u32 s46, s36, s69\n\t" \
    "s_addc_u32 s47, s37, 0\n\t" \
    "s_add_u32 s70, s65, 0\n\t" \
    "s_mul_i32 s69, s70, 171\n\t" \
    "s_lshr_b32 s69, s69, 9\n\t" \
    "s_mul_i32 s70, s70, 589824\n\t" \
    "s_mul_i32 s71, s69, 327680\n\t" \
    "s_add_u32 s70, s70, s71\n\t" \
    "s_lshl1_add_u32 s69, s69, 4\n\t" \
    "s_bitcmp1_b64 s[44:45], s69\n\t" \
    "s_cselect_b32 s69, 8192, 0\n\t" \
    "s_add_u32 s69, s69, s70\n\t" \
    "s_add_u32 s69, s69, 123731968\n\t" \
    "s_add_u32 s38, s36, s69\n\t" \
    "s_addc_u32 s39, s37, 0\n\t" \
    "s_and_b32 s69, s69, 0xffe00000\n\t" \
    "s_add_u32 s69, s69, 1823744\n\t" \
    "v_mov_b32 v17, s69\n\t" \
    "global_load_dword v17, v17, s[36:37]\n\t" \
    "s_and_b32 s69, s65, 7\n\t" \
    "s_lshl_b32 s69, s69, 18\n\t" \
    "s_add_u32 s69, s69, s63\n\t" \
    "s_add_u32 s52, s36, s69\n\t" \
    "s_addc_u32 s53, s37, 0\n\t" \
    "v_add_u32 v34, 16384, v1\n\t" \
    "v_subrev_u32 v35, 16384, v2\n\t" \
    "v_mov_b32 v100, 0\n\t" \
    "v_mov_b32 v101, 0\n\t" \
    "v_mov_b32 v102, 0\n\t" \
    "v_mov_b32 v103, 0\n\t" \
    "s_sub_u32 s70, s79, s74\n\t" \
    "s_max_i32 s70, s70, 0\n\t" \
    "s_min_i32 s70, s70, 16000\n\t" \
    "s_lshl_b32 s70, s70, 1\n\t" \
    "s_mul_hi_u32 s70, s70, s77\n\t" \
    "s_add_u32 s70, s70, s73\n\t" \
    "s_cmp_eq_u32 s66, 1\n\t" \
    "s_cbranch_scc1 vv_loop10_%=\n\t" \
    "s_cmp_eq_u32 s66, 2\n\t" \
    "s_cbranch_scc1 vv_loop11_%=\n\t" \
    "s_cmp_eq_u32 s66, 3\n\t" \
    "s_cbranch_scc1 vv_loop12_%=\n\t" \
    ".p2align 6\n\t" \
    "s_nop 0\n\t" \
    "s_nop 0\n\t" \
    "s_nop 0\n\t" \
    "s_nop 0\n\t" \
    "s_nop 0\n\t" \
    "s_nop 0\n\t" \
    "vv_loop9_%=:\n\t" \
    "s_getpc_b64 s[30:31]\n\t" \
    "vv_Wp15_%=:\n\t" \
    "s_add_u32 s30, s30, vv_We16_%=-vv_Wp15_%=\n\t" \
    "s_addc_u32 s31, s31, 0\n\t" \
    "s_sub_u32 s70, s70, -54\n\t" \
    "vv_W14_%=:\n\t" \
    "s_memtime s[56:57]\n\t" \
    "s_waitcnt lgkmcnt(0)\n\t" \
    "s_sub_u32 s69, s70, s56\n\t" \
    "s_cmp_gt_i32 s69, 80\n\t" \
    "s_cbranch_scc1 vv_W14_%=\n\t" \
    "s_max_i32 s69, s69, 0\n\t" \
    "s_and_b32 s69, s69, -4\n\t" \
    "s_sub_u32 s30, s30, s69\n\t" \
    "s_subb_u32 s31, s31, 0\n\t" \
    "s_setpc_b64 s[30:31]\n\t" \
    "s_nop 0\n\t" \
    "s_nop 0\n\t" \
    "s_nop 0\n\t" \
    "s_nop 0\n\t" \
    "s_nop 0\n\t" \
    "s_nop 0\n\t" \
    "s_nop 0\n\t" \
    "s_nop 0\n\t" \
    "s_nop 0\n\t" \
    "s_nop 0\n\t" \
    "s_nop 0\n\t" \
    "s_nop 0\n\t" \
    "s_nop 0\n\t" \
    "s_nop 0\n\t" \
    "s_nop 0\n\t" \
    "s_nop 0\n\t" \
    "s_nop 0\n\t" \
    "s_nop 0\n\t" \
    "s_nop 0\n\t" \
    "s_nop 0\n\t" \
    "vv_We16_%=:\n\t" \
    "vv_G17_%=:\n\t" \
    "global_load_dwordx4 v[36:39], v1, s[46:47] sc1\n\t" \
    "global_load_dword v141, v2, s[46:47] sc1\n\t" \
    "global_load_dwordx4 v[40:43], v23, s[46:47] sc1\n\t" \
    "s_waitcnt vmcnt(2)\n\t" \
    "vv_Garr22_%=:\n\t" \
    "ds_write_b128 v3, v[36:39]\n\t" \
    "v_smfmac_f32_16x16x64_bf16 v[72:75], v[36:39], a[0:7], v10\n\t" \
    "s_waitcnt vmcnt(1)\n\t" \
    "ds_write_b32 v22, v141 offset:19456\n\t" \
    "s_waitcnt vmcnt(0)\n\t" \
    "ds_write_b128 v3, v[40:43] offset:1024\n\t" \
    "v_max3_u32 v140, v36, v40, v141\n\t" \
    "v_cmp_eq_u32 vcc, -1, v140\n\t" \
    "s_cbranch_vccnz vv_Gbad18_%=\n\t" \
    "vv_Gc19_%=:\n\t" \
    "s_waitcnt lgkmcnt(0)\n\t" \
    "s_barrier\n\t" \
    "ds_read_b128 v[48:51], v4\n\t" \
    "ds_read_b128 v[52:55], v5\n\t" \
    "v_smfmac_f32_16x16x64_bf16 v[76:79], v[36:39], a[72:79], v10\n\t" \
    "ds_read_b128 v[56:59], v6\n\t" \
    "ds_read_b128 v[60:63], v7\n\t" \
    "v_smfmac_f32_16x16x64_bf16 v[72:75], v[40:43], a[8:15], v10\n\t" \
    "ds_read_b128 v[64:67], v8\n\t" \
    "v_smfmac_f32_16x16x64_bf16 v[76:79], v[40:43], a[80:87], v10\n\t" \
    "ds_read_b128 v[68:71], v9\n\t" \
    "s_waitcnt lgkmcnt(5)\n\t" \
    "v_smfmac_f32_16x16x64_bf16 v[72:75], v[48:51], a[16:23], v10\n\t" \
    "ds_read_b128 v[44:47], v0 offset:19456\n\t" \
    "s_waitcnt lgkmcnt(5)\n\t" \
    "v_smfmac_f32_16x16x64_bf16 v[72:75], v[52:55], a[24:31], v10\n\t" \
    "s_waitcnt lgkmcnt(4)\n\t" \
    "v_smfmac_f32_16x16x64_bf16 v[72:75], v[56:59], a[32:39], v10\n\t" \
    "s_waitcnt lgkmcnt(3)\n\t" \
    "v_smfmac_f32_16x16x64_bf16 v[72:75], v[60:63], a[40:47], v10\n\t" \
    "s_waitcnt lgkmcnt(2)\n\t" \
    "v_smfmac_f32_16x16x64_bf16 v[72:75], v[64:67], a[48:55], v10\n\t" \
    "s_waitcnt lgkmcnt(1)\n\t" \
    "v_smfmac_f32_16x16x64_bf16 v[72:75], v[68:71], a[56:63], v10\n\t" \
    "s_waitcnt lgkmcnt(0)\n\t" \
    "v_smfmac_f32_16x16x64_bf16 v[72:75], v[44:47], a[64:71], v10\n\t" \
    "s_nop 2\n\t" \
    "v_smfmac_f32_16x16x64_bf16 v[76:79], v[48:51], a[88:95], v10\n\t" \
    "s_nop 2\n\t" \
    "v_smfmac_f32_16x16x64_bf16 v[76:79], v[52:55], a[96:103], v10\n\t" \
    "v_add_f32 v72, v72, v74\n\t" \
    "v_smfmac_f32_16x16x64_bf16 v[76:79], v[56:59], a[104:111], v10\n\t" \
    "v_add_f32 v73, v73, v75\n\t" \
    "v_smfmac_f32_16x16x64_bf16 v[76:79], v[60:63], a[112:119], v10\n\t" \
    "v_cvt_pk_bf16_f32 v80, v72, v73\n\t" \
    "v_smfmac_f32_16x16x64_bf16 v[76:79], v[64:67], a[120:127], v10\n\t" \
    "ds_write_b32 v12, v80 offset:0\n\t" \
    "v_smfmac_f32_16x16x64_bf16 v[76:79], v[68:71], a[128:135], v10\n\t" \
    "v_smfmac_f32_16x16x64_bf16 v[76:79], v[44:47], a[136:143], v10\n\t" \
    "s_nop 6\n\t" \
    "v_cmp_u_f32 vcc, v72, v73\n\t" \
    "v_pk_add_f32 v[76:77], v[76:77], v[78:79]\n\t" \
    "v_cvt_pk_bf16_f32 v81, v76, v77\n\t" \
    "ds_write_b32 v12, v81 offset:256\n\t" \
    "s_cbranch_vccnz vv_Gslow20_%=\n\t" \
    "vv_Gback21_%=:\n\t" \
    "s_waitcnt lgkmcnt(0)\n\t" \
    "s_barrier\n\t" \
    "ds_read_b64_tr_b16 v[84:85], v13 offset:0\n\t" \
    "v_smfmac_f32_16x16x64_bf16 v[100:103], v[36:39], a[0:7], v10\n\t" \
    "ds_read_b64_tr_b16 v[86:87], v13 offset:64\n\t" \
    "ds_read_b64_tr_b16 v[92:93], v13 offset:1152\n\t" \
    "v_smfmac_f32_16x16x64_bf16 v[100:103], v[36:39], a[0:7], v10\n\t" \
    "ds_read_b64_tr_b16 v[94:95], v13 offset:1216\n\t" \
    "ds_read_b64_tr_b16 v[88:89], v25 offset:0\n\t" \
    "v_smfmac_f32_16x16x64_bf16 v[100:103], v[36:39], a[0:7], v10\n\t" \
    "ds_read_b64_tr_b16 v[90:91], v25 offset:64\n\t" \
    "s_waitcnt lgkmcnt(4)\n\t" \
    "v_smfmac_f32_16x16x64_bf16 v[124:127], v[84:87], v[240:247], v19\n\t" \
    "ds_read_b64_tr_b16 v[96:97], v25 offset:1152\n\t" \
    "ds_read_b64_tr_b16 v[98:99], v25 offset:1216\n\t" \
    "s_waitcnt lgkmcnt(4)\n\t" \
    "v_smfmac_f32_16x16x64_bf16 v[124:127], v[92:95], v[248:255], v19\n\t" \
    "s_nop 2\n\t" \
    "v_smfmac_f32_16x16x64_bf16 v[108:111], v[84:87], a[144:151], v19\n\t" \
    "s_nop 2\n\t" \
    "v_smfmac_f32_16x16x64_bf16 v[108:111], v[92:95], a[160:167], v19\n\t" \
    "s_nop 1\n\t" \
    "v_permlane32_swap_b32 v124, v126\n\t" \
    "v_smfmac_f32_16x16x64_bf16 v[112:115], v[84:87], a[176:183], v19\n\t" \
    "s_nop 1\n\t" \
    "v_permlane32_swap_b32 v125, v127\n\t" \
    "v_smfmac_f32_16x16x64_bf16 v[112:115], v[92:95], a[192:199], v19\n\t" \
    "s_nop 0\n\t" \
    "v_add_f32 v124, v124, v126\n\t" \
    "s_waitcnt lgkmcnt(2)\n\t" \
    "v_smfmac_f32_16x16x64_bf16 v[108:111], v[88:91], a[152:159], v19\n\t" \
    "s_nop 1\n\t" \
    "v_add_f32 v125, v125, v127\n\t" \
    "v_smfmac_f32_16x16x64_bf16 v[112:115], v[88:91], a[184:191], v19\n\t" \
    "s_nop 0\n\t" \
    "v_cvt_pk_bf16_f32 v136, v124, v125\n\t" \
    "s_waitcnt lgkmcnt(0)\n\t" \
    "v_smfmac_f32_16x16x64_bf16 v[108:111], v[96:99], a[168:175], v19\n\t" \
    "s_nop 1\n\t" \
    "v_cvt_pk_bf16_f32 v137, v124, v125\n\t" \
    "v_smfmac_f32_16x16x64_bf16 v[112:115], v[96:99], a[200:207], v19\n\t" \
    "s_nop 1\n\t" \
    "v_permlane32_swap_b32 v136, v137\n\t" \
    "v_smfmac_f32_16x16x64_bf16 v[116:119], v[84:87], a[208:215], v19\n\t" \
    "s_mov_b32 exec_hi, 0\n\t" \
    "global_store_dwordx2 v16, v[136:137], s[38:39] sc1\n\t" \
    "s_mov_b32 exec_hi, -1\n\t" \
    "v_cvt_pk_bf16_f32 v128, v108, v109\n\t" \
    "v_smfmac_f32_16x16x64_bf16 v[116:119], v[88:91], a[216:223], v19\n\t" \
    "s_nop 1\n\t" \
    "v_cvt_pk_bf16_f32 v129, v110, v111\n\t" \
    "v_smfmac_f32_16x16x64_bf16 v[116:119], v[92:95], a[224:231], v19\n\t" \
    "s_nop 1\n\t" \
    "v_cvt_pk_bf16_f32 v130, v112, v113\n\t" \
    "v_smfmac_f32_16x16x64_bf16 v[116:119], v[96:99], a[232:239], v19\n\t" \
    "s_nop 1\n\t" \
    "v_cvt_pk_bf16_f32 v131, v114, v115\n\t" \
    "v_smfmac_f32_16x16x64_bf16 v[120:123], v[84:87], a[240:247], v19\n\t" \
    "s_nop 1\n\t" \
    "v_permlane16_swap_b32 v128, v130\n\t" \
    "v_smfmac_f32_16x16x64_bf16 v[120:123], v[88:91], a[248:255], v19\n\t" \
    "s_nop 1\n\t" \
    "v_permlane16_swap_b32 v129, v131\n\t" \
    "v_smfmac_f32_16x16x64_bf16 v[120:123], v[92:95], v[224:231], v19\n\t" \
    "s_nop 0\n\t" \
    "global_store_dwordx4 v14, v[128:131], s[38:39] sc1\n\t" \
    "v_cvt_pk_bf16_f32 v132, v116, v117\n\t" \
    "v_smfmac_f32_16x16x64_bf16 v[120:123], v[96:99], v[232:239], v19\n\t" \
    "s_nop 1\n\t" \
    "v_cvt_pk_bf16_f32 v133, v118, v119\n\t" \
    "s_nop 4\n\t" \
    "v_cvt_pk_bf16_f32 v134, v120, v121\n\t" \
    "v_cvt_pk_bf16_f32 v135, v122, v123\n\t" \
    "s_nop 0\n\t" \
    "v_permlane16_swap_b32 v132, v134\n\t" \
    "v_permlane16_swap_b32 v133, v135\n\t" \
    "global_store_dwordx4 v15, v[132:135], s[38:39] sc1\n\t" \
    "s_cmp_eq_u32 s65, 63\n\t" \
    "s_cbranch_scc1 vv_end13_%=\n\t" \
    "s_cmp_lg_u32 s66, 0\n\t" \
    "s_cbranch_scc1 vv_SF23_%=\n\t" \
    "s_lshl_b32 s69, s67, 7\n\t" \
    "s_add_u32 s69, s69, 3182592\n\t" \
    "s_add_u32 s70, s65, -1\n\t" \
    "v_mov_b32 v142, s69\n\t" \
    "v_mov_b32 v143, s70\n\t" \
    "s_mov_b64 exec, 1\n\t" \
    "global_store_dword v142, v143, s[100:101] sc1\n\t" \
    "s_mov_b64 exec, -1\n\t" \
    "vv_SF23_%=:\n\t" \
    "s_add_u32 s65, s65, 1\n\t" \
    "v_mov_b32 v72, 0\n\t" \
    "v_mov_b32 v73, 0\n\t" \
    "v_mov_b32 v74, 0\n\t" \
    "v_mov_b32 v75, 0\n\t" \
    "v_mov_b32 v76, 0\n\t" \
    "v_mov_b32 v77, 0\n\t" \
    "v_mov_b32 v78, 0\n\t" \
    "v_mov_b32 v79, 0\n\t" \
    "v_mov_b32 v108, 0\n\t" \
    "v_mov_b32 v109, 0\n\t" \
    "v_mov_b32 v110, 0\n\t" \
    "v_mov_b32 v111, 0\n\t" \
    "v_mov_b32 v112, 0\n\t" \
    "v_mov_b32 v113, 0\n\t" \
    "v_mov_b32 v114, 0\n\t" \
    "v_mov_b32 v115, 0\n\t" \
    "v_mov_b32 v116, 0\n\t" \
    "v_mov_b32 v117, 0\n\t" \
    "v_mov_b32 v118, 0\n\t" \
    "v_mov_b32 v119, 0\n\t" \
    "v_mov_b32 v120, 0\n\t" \
    "v_mov_b32 v121, 0\n\t" \
    "v_mov_b32 v122, 0\n\t" \
    "v_mov_b32 v123, 0\n\t" \
    "v_mov_b32 v124, 0\n\t" \
    "v_mov_b32 v125, 0\n\t" \
    "v_mov_b32 v126, 0\n\t" \
    "v_mov_b32 v127, 0\n\t" \
    "s_add_u32 s69, s65, 0\n\t" \
    "s_and_b32 s69, s69, 7\n\t" \
    "s_lshl_b32 s69, s69, 18\n\t" \
    "s_add_u32 s69, s69, s62\n\t" \
    "s_add_u32 s46, s36, s69\n\t" \
    "s_addc_u32 s47, s37, 0\n\t" \
    "s_add_u32 s70, s65, 0\n\t" \
    "s_mul_i32 s69, s70, 171\n\t" \
    "s_lshr_b32 s69, s69, 9\n\t" \
    "s_mul_i32 s70, s70, 589824\n\t" \
    "s_mul_i32 s71, s69, 327680\n\t" \
    "s_add_u32 s70, s70, s71\n\t" \
    "s_lshl1_add_u32 s69, s69, 4\n\t" \
    "s_bitcmp1_b64 s[44:45], s69\n\t" \
    "s_cselect_b32 s69, 8192, 0\n\t" \
    "s_add_u32 s69, s69, s70\n\t" \
    "s_add_u32 s69, s69, 123731968\n\t" \
    "s_add_u32 s38, s36, s69\n\t" \
    "s_addc_u32 s39, s37, 0\n\t" \
    "s_and_b32 s69, s69, 0xffe00000\n\t" \
    "s_add_u32 s69, s69, 1823744\n\t" \
    "v_mov_b32 v17, s69\n\t" \
    "global_load_dword v17, v17, s[36:37]\n\t" \
    "s_and_b32 s69, s65, 7\n\t" \
    "s_lshl_b32 s69, s69, 18\n\t" \
    "s_add_u32 s69, s69, s63\n\t" \
    "s_add_u32 s52, s36, s69\n\t" \
    "s_addc_u32 s53, s37, 0\n\t" \
    "s_barrier\n\t" \
    "ds_read_b32 v160, v11 offset:20480\n\t" \
    "s_waitcnt lgkmcnt(0)\n\t" \
    "v_readfirstlane_b32 s70, v160\n\t" \
    "s_branch vv_loop9_%=\n\t" \
    "vv_Gbad18_%=:\n\t" \
    "s_waitcnt vmcnt(0) lgkmcnt(0)\n\t" \
    "s_nop 7\n\t" \
    "v_mov_b32 v72, 0\n\t" \
    "v_mov_b32 v73, 0\n\t" \
    "v_mov_b32 v74, 0\n\t" \
    "v_mov_b32 v75, 0\n\t" \
    "v_mov_b32 v76, 0\n\t" \
    "v_mov_b32 v77, 0\n\t" \
    "v_mov_b32 v78, 0\n\t" \
    "v_mov_b32 v79, 0\n\t" \
    "s_cmp_eq_u32 s68, 0\n\t" \
    "s_cbranch_scc1 vv_F24_%=\n\t" \
    "s_sub_u32 s68, s68, 1\n\t" \
    "s_branch vv_G17_%=\n\t" \
    "vv_F24_%=:\n\t" \
    "v_mov_b32 v140, 5767424\n\t" \
    "v_mov_b32 v141, 1\n\t" \
    "global_store_dword v140, v141, s[36:37] sc1\n\t" \
    "s_waitcnt vmcnt(0)\n\t" \
    "s_branch vv_Gc19_%=\n\t" \
    "vv_Gslow20_%=:\n\t" \
    "v_max_u32 v140, v36, v37\n\t" \
    "v_max3_u32 v140, v140, v38, v39\n\t" \
    "v_max3_u32 v140, v140, v40, v41\n\t" \
    "v_max3_u32 v140, v140, v42, v43\n\t" \
    "v_max3_u32 v140, v140, v44, v45\n\t" \
    "v_max3_u32 v140, v140, v46, v47\n\t" \
    "v_max3_u32 v140, v140, v48, v49\n\t" \
    "v_max3_u32 v140, v140, v50, v51\n\t" \
    "v_max3_u32 v140, v140, v52, v53\n\t" \
    "v_max3_u32 v140, v140, v54, v55\n\t" \
    "v_max3_u32 v140, v140, v56, v57\n\t" \
    "v_max3_u32 v140, v140, v58, v59\n\t" \
    "v_max3_u32 v140, v140, v60, v61\n\t" \
    "v_max3_u32 v140, v140, v62, v63\n\t" \
    "v_max3_u32 v140, v140, v64, v65\n\t" \
    "v_max3_u32 v140, v140, v66, v67\n\t" \
    "v_max3_u32 v140, v140, v68, v69\n\t" \
    "v_max3_u32 v140, v140, v70, v71\n\t" \
    "v_cmp_eq_u32 vcc, -1, v140\n\t" \
    "s_cbranch_vccz vv_Gback21_%=\n\t" \
    "s_waitcnt lgkmcnt(0)\n\t" \
    "v_sub_u32 v140, v1, v3\n\t" \
    "s_nop 0\n\t" \
    "v_readfirstlane_b32 s69, v140\n\t" \
    "v_add_u32 v143, 20480, v0\n\t" \
    "s_nop 7\n\t" \
    "s_add_u32 s30, s46, s69\n\t" \
    "s_addc_u32 s31, s47, 0\n\t" \
    "vv_Gag25_%=:\n\t" \
    "global_load_dwordx4 v[36:39], v1, s[46:47] sc1\n\t" \
    "global_load_dwordx4 v[40:43], v23, s[46:47] sc1\n\t" \
    "global_load_dwordx4 v[44:47], v143, s[30:31] sc1\n\t" \
    "global_load_dwordx4 v[48:51], v4, s[30:31] sc1\n\t" \
    "global_load_dwordx4 v[52:55], v5, s[30:31] sc1\n\t" \
    "global_load_dwordx4 v[56:59], v6, s[30:31] sc1\n\t" \
    "global_load_dwordx4 v[60:63], v7, s[30:31] sc1\n\t" \
    "global_load_dwordx4 v[64:67], v8, s[30:31] sc1\n\t" \
    "global_load_dwordx4 v[68:71], v9, s[30:31] sc1\n\t" \
    "s_waitcnt vmcnt(0)\n\t" \
    "v_max_u32 v140, v36, v37\n\t" \
    "v_max3_u32 v140, v140, v38, v39\n\t" \
    "v_max3_u32 v140, v140, v40, v41\n\t" \
    "v_max3_u32 v140, v140, v42, v43\n\t" \
    "v_max3_u32 v140, v140, v44, v45\n\t" \
    "v_max3_u32 v140, v140, v46, v47\n\t" \
    "v_max3_u32 v140, v140, v48, v49\n\t" \
    "v_max3_u32 v140, v140, v50, v51\n\t" \
    "v_max3_u32 v140, v140, v52, v53\n\t" \
    "v_max3_u32 v140, v140, v54, v55\n\t" \
    "v_max3_u32 v140, v140, v56, v57\n\t" \
    "v_max3_u32 v140, v140, v58, v59\n\t" \
    "v_max3_u32 v140, v140, v60, v61\n\t" \
    "v_max3_u32 v140, v140, v62, v63\n\t" \
    "v_max3_u32 v140, v140, v64, v65\n\t" \
    "v_max3_u32 v140, v140, v66, v67\n\t" \
    "v_max3_u32 v140, v140, v68, v69\n\t" \
    "v_max3_u32 v140, v140, v70, v71\n\t" \
    "v_cmp_eq_u32 vcc, -1, v140\n\t" \
    "s_cbranch_vccz vv_Gredo26_%=\n\t" \
    "s_cmp_eq_u32 s68, 0\n\t" \
    "s_cbranch_scc1 vv_F27_%=\n\t" \
    "s_sub_u32 s68, s68, 1\n\t" \
    "s_sleep 1\n\t" \
    "s_branch vv_Gag25_%=\n\t" \
    "vv_F27_%=:\n\t" \
    "v_mov_b32 v140, 5767424\n\t" \
    "v_mov_b32 v141, 2\n\t" \
    "global_store_dword v140, v141, s[36:37] sc1\n\t" \
    "s_waitcnt vmcnt(0)\n\t" \
    "s_branch vv_Gredo26_%=\n\t" \
    "vv_Gredo26_%=:\n\t" \
    "v_mov_b32 v72, 0\n\t" \
    "v_mov_b32 v73, 0\n\t" \
    "v_mov_b32 v74, 0\n\t" \
    "v_mov_b32 v75, 0\n\t" \
    "v_mov_b32 v76, 0\n\t" \
    "v_mov_b32 v77, 0\n\t" \
    "v_mov_b32 v78, 0\n\t" \
    "v_mov_b32 v79, 0\n\t" \
    "v_smfmac_f32_16x16x64_bf16 v[72:75], v[36:39], a[0:7], v10\n\t" \
    "v_smfmac_f32_16x16x64_bf16 v[72:75], v[40:43], a[8:15], v10\n\t" \
    "v_smfmac_f32_16x16x64_bf16 v[72:75], v[48:51], a[16:23], v10\n\t" \
    "v_smfmac_f32_16x16x64_bf16 v[72:75], v[52:55], a[24:31], v10\n\t" \
    "v_smfmac_f32_16x16x64_bf16 v[72:75], v[56:59], a[32:39], v10\n\t" \
    "v_smfmac_f32_16x16x64_bf16 v[72:75], v[60:63], a[40:47], v10\n\t" \
    "v_smfmac_f32_16x16x64_bf16 v[72:75], v[64:67], a[48:55], v10\n\t" \
    "v_smfmac_f32_16x16x64_bf16 v[72:75], v[68:71], a[56:63], v10\n\t" \
    "v_smfmac_f32_16x16x64_bf16 v[72:75], v[44:47], a[64:71], v10\n\t" \
    "v_smfmac_f32_16x16x64_bf16 v[76:79], v[36:39], a[72:79], v10\n\t" \
    "v_smfmac_f32_16x16x64_bf16 v[76:79], v[40:43], a[80:87], v10\n\t" \
    "v_smfmac_f32_16x16x64_bf16 v[76:79], v[48:51], a[88:95], v10\n\t" \
    "v_smfmac_f32_16x16x64_bf16 v[76:79], v[52:55], a[96:103], v10\n\t" \
    "v_smfmac_f32_16x16x64_bf16 v[76:79], v[56:59], a[104:111], v10\n\t" \
    "v_smfmac_f32_16x16x64_bf16 v[76:79], v[60:63], a[112:119], v10\n\t" \
    "v_smfmac_f32_16x16x64_bf16 v[76:79], v[64:67], a[120:127], v10\n\t" \
    "v_smfmac_f32_16x16x64_bf16 v[76:79], v[68:71], a[128:135], v10\n\t" \
    "v_smfmac_f32_16x16x64_bf16 v[76:79], v[44:47], a[136:143], v10\n\t" \
    "v_add_f32 v72, v72, v74\n\t" \
    "v_add_f32 v73, v73, v75\n\t" \
    "v_cvt_pk_bf16_f32 v80, v72, v73\n\t" \
    "ds_write_b32 v12, v80 offset:0\n\t" \
    "s_nop 3\n\t" \
    "v_pk_add_f32 v[76:77], v[76:77], v[78:79]\n\t" \
    "v_cvt_pk_bf16_f32 v81, v76, v77\n\t" \
    "ds_write_b32 v12, v81 offset:256\n\t" \
    "s_branch vv_Gback21_%=\n\t" \
    "vv_end13_%=:\n\t" \
    "s_endpgm\n\t" \
    ".p2align 6\n\t" \
    "s_nop 0\n\t" \
    "s_nop 0\n\t" \
    "s_nop 0\n\t" \
    "s_nop 0\n\t" \
    "s_nop 0\n\t" \
    "s_nop 0\n\t" \
    "vv_loop10_%=:\n\t" \
    "s_getpc_b64 s[30:31]\n\t" \
    "vv_Wp30_%=:\n\t" \
    "s_add_u32 s30, s30, vv_We31_%=-vv_Wp30_%=\n\t" \
    "s_addc_u32 s31, s31, 0\n\t" \
    "s_sub_u32 s70, s70, -54\n\t" \
    "vv_W29_%=:\n\t" \
    "s_memtime s[56:57]\n\t" \
    "s_waitcnt lgkmcnt(0)\n\t" \
    "s_sub_u32 s69, s70, s56\n\t" \
    "s_cmp_gt_i32 s69, 80\n\t" \
    "s_cbranch_scc1 vv_W29_%=\n\t" \
    "s_max_i32 s69, s69, 0\n\t" \
    "s_and_b32 s69, s69, -4\n\t" \
    "s_sub_u32 s30, s30, s69\n\t" \
    "s_subb_u32 s31, s31, 0\n\t" \
    "s_setpc_b64 s[30:31]\n\t" \
    "s_nop 0\n\t" \
    "s_nop 0\n\t" \
    "s_nop 0\n\t" \
    "s_nop 0\n\t" \
    "s_nop 0\n\t" \
    "s_nop 0\n\t" \
    "s_nop 0\n\t" \
    "s_nop 0\n\t" \
    "s_nop 0\n\t" \
    "s_nop 0\n\t" \
    "s_nop 0\n\t" \
    "s_nop 0\n\t" \
    "s_nop 0\n\t" \
    "s_nop 0\n\t" \
    "s_nop 0\n\t" \
    "s_nop 0\n\t" \
    "s_nop 0\n\t" \
    "s_nop 0\n\t" \
    "s_nop 0\n\t" \
    "s_nop 0\n\t" \
    "vv_We31_%=:\n\t" \
    "vv_G32_%=:\n\t" \
    "global_load_dwordx4 v[36:39], v1, s[46:47] sc1\n\t" \
    "global_load_dword v141, v2, s[46:47] sc1\n\t" \
    "global_load_dwordx4 v[40:43], v23, s[46:47] sc1\n\t" \
    "s_waitcnt vmcnt(2)\n\t" \
    "vv_Garr37_%=:\n\t" \
    "ds_write_b128 v3, v[36:39]\n\t" \
    "v_smfmac_f32_16x16x64_bf16 v[72:75], v[36:39], a[16:23], v10\n\t" \
    "s_waitcnt vmcnt(1)\n\t" \
    "ds_write_b32 v22, v141 offset:19712\n\t" \
    "s_waitcnt vmcnt(0)\n\t" \
    "ds_write_b128 v3, v[40:43] offset:1024\n\t" \
    "v_max3_u32 v140, v36, v40, v141\n\t" \
    "v_cmp_eq_u32 vcc, -1, v140\n\t" \
    "s_cbranch_vccnz vv_Gbad33_%=\n\t" \
    "vv_Gc34_%=:\n\t" \
    "s_waitcnt lgkmcnt(0)\n\t" \
    "s_barrier\n\t" \
    "ds_read_b128 v[48:51], v4\n\t" \
    "ds_read_b128 v[52:55], v5\n\t" \
    "v_smfmac_f32_16x16x64_bf16 v[76:79], v[36:39], a[88:95], v10\n\t" \
    "ds_read_b128 v[56:59], v6\n\t" \
    "ds_read_b128 v[60:63], v7\n\t" \
    "v_smfmac_f32_16x16x64_bf16 v[72:75], v[40:43], a[24:31], v10\n\t" \
    "ds_read_b128 v[64:67], v8\n\t" \
    "v_smfmac_f32_16x16x64_bf16 v[76:79], v[40:43], a[96:103], v10\n\t" \
    "ds_read_b128 v[68:71], v9\n\t" \
    "s_waitcnt lgkmcnt(5)\n\t" \
    "v_smfmac_f32_16x16x64_bf16 v[72:75], v[48:51], a[32:39], v10\n\t" \
    "ds_read_b128 v[44:47], v0 offset:19456\n\t" \
    "s_waitcnt lgkmcnt(5)\n\t" \
    "v_smfmac_f32_16x16x64_bf16 v[72:75], v[52:55], a[40:47], v10\n\t" \
    "s_waitcnt lgkmcnt(4)\n\t" \
    "v_smfmac_f32_16x16x64_bf16 v[72:75], v[56:59], a[48:55], v10\n\t" \
    "s_waitcnt lgkmcnt(3)\n\t" \
    "v_smfmac_f32_16x16x64_bf16 v[72:75], v[60:63], a[56:63], v10\n\t" \
    "s_waitcnt lgkmcnt(2)\n\t" \
    "v_smfmac_f32_16x16x64_bf16 v[72:75], v[64:67], a[0:7], v10\n\t" \
    "s_waitcnt lgkmcnt(1)\n\t" \
    "v_smfmac_f32_16x16x64_bf16 v[72:75], v[68:71], a[8:15], v10\n\t" \
    "s_waitcnt lgkmcnt(0)\n\t" \
    "v_smfmac_f32_16x16x64_bf16 v[72:75], v[44:47], a[64:71], v10\n\t" \
    "s_nop 2\n\t" \
    "v_smfmac_f32_16x16x64_bf16 v[76:79], v[48:51], a[104:111], v10\n\t" \
    "s_nop 2\n\t" \
    "v_smfmac_f32_16x16x64_bf16 v[76:79], v[52:55], a[112:119], v10\n\t" \
    "v_add_f32 v72, v72, v74\n\t" \
    "v_smfmac_f32_16x16x64_bf16 v[76:79], v[56:59], a[120:127], v10\n\t" \
    "v_add_f32 v73, v73, v75\n\t" \
    "v_smfmac_f32_16x16x64_bf16 v[76:79], v[60:63], a[128:135], v10\n\t" \
    "v_cvt_pk_bf16_f32 v80, v72, v73\n\t" \
    "v_smfmac_f32_16x16x64_bf16 v[76:79], v[64:67], a[72:79], v10\n\t" \
    "ds_write_b32 v12, v80 offset:0\n\t" \
    "v_smfmac_f32_16x16x64_bf16 v[76:79], v[68:71], a[80:87], v10\n\t" \
    "v_smfmac_f32_16x16x64_bf16 v[76:79], v[44:47], a[136:143], v10\n\t" \
    "s_nop 6\n\t" \
    "v_cmp_u_f32 vcc, v72, v73\n\t" \
    "v_pk_add_f32 v[76:77], v[76:77], v[78:79]\n\t" \
    "v_cvt_pk_bf16_f32 v81, v76, v77\n\t" \
    "ds_write_b32 v12, v81 offset:256\n\t" \
    "s_cbranch_vccnz vv_Gslow35_%=\n\t" \
    "vv_Gback36_%=:\n\t" \
    "s_waitcnt lgkmcnt(0)\n\t" \
    "s_barrier\n\t" \
    "ds_read_b64_tr_b16 v[84:85], v13 offset:0\n\t" \
    "v_smfmac_f32_16x16x64_bf16 v[100:103], v[36:39], a[0:7], v10\n\t" \
    "ds_read_b64_tr_b16 v[86:87], v13 offset:64\n\t" \
    "ds_read_b64_tr_b16 v[92:93], v13 offset:1152\n\t" \
    "v_smfmac_f32_16x16x64_bf16 v[100:103], v[36:39], a[0:7], v10\n\t" \
    "ds_read_b64_tr_b16 v[94:95], v13 offset:1216\n\t" \
    "ds_read_b64_tr_b16 v[88:89], v25 offset:0\n\t" \
    "v_smfmac_f32_16x16x64_bf16 v[100:103], v[36:39], a[0:7], v10\n\t" \
    "ds_read_b64_tr_b16 v[90:91], v25 offset:64\n\t" \
    "s_waitcnt lgkmcnt(4)\n\t" \
    "v_smfmac_f32_16x16x64_bf16 v[124:127], v[84:87], v[240:247], v19\n\t" \
    "ds_read_b64_tr_b16 v[96:97], v25 offset:1152\n\t" \
    "ds_read_b64_tr_b16 v[98:99], v25 offset:1216\n\t" \
    "s_waitcnt lgkmcnt(4)\n\t" \
    "v_smfmac_f32_16x16x64_bf16 v[124:127], v[92:95], v[248:255], v19\n\t" \
    "s_nop 2\n\t" \
    "v_smfmac_f32_16x16x64_bf16 v[108:111], v[84:87], a[144:151], v19\n\t" \
    "s_nop 2\n\t" \
    "v_smfmac_f32_16x16x64_bf16 v[108:111], v[92:95], a[160:167], v19\n\t" \
    "s_nop 1\n\t" \
    "v_permlane32_swap_b32 v124, v126\n\t" \
    "v_smfmac_f32_16x16x64_bf16 v[112:115], v[84:87], a[176:183], v19\n\t" \
    "s_nop 1\n\t" \
    "v_permlane32_swap_b32 v125, v127\n\t" \
    "v_smfmac_f32_16x16x64_bf16 v[112:115], v[92:95], a[192:199], v19\n\t" \
    "s_nop 0\n\t" \
    "v_add_f32 v124, v124, v126\n\t" \
    "s_waitcnt lgkmcnt(2)\n\t" \
    "v_smfmac_f32_16x16x64_bf16 v[108:111], v[88:91], a[152:159], v19\n\t" \
    "s_nop 1\n\t" \
    "v_add_f32 v125, v125, v127\n\t" \
    "v_smfmac_f32_16x16x64_bf16 v[112:115], v[88:91], a[184:191], v19\n\t" \
    "s_nop 0\n\t" \
    "v_cvt_pk_bf16_f32 v136, v124, v125\n\t" \
    "s_waitcnt lgkmcnt(0)\n\t" \
    "v_smfmac_f32_16x16x64_bf16 v[108:111], v[96:99], a[168:175], v19\n\t" \
    "s_nop 1\n\t" \
    "v_cvt_pk_bf16_f32 v137, v124, v125\n\t" \
    "v_smfmac_f32_16x16x64_bf16 v[112:115], v[96:99], a[200:207], v19\n\t" \
    "s_nop 1\n\t" \
    "v_permlane32_swap_b32 v136, v137\n\t" \
    "v_smfmac_f32_16x16x64_bf16 v[116:119], v[84:87], a[208:215], v19\n\t" \
    "s_mov_b32 exec_hi, 0\n\t" \
    "global_store_dwordx2 v16, v[136:137], s[38:39] sc1\n\t" \
    "s_mov_b32 exec_hi, -1\n\t" \
    "v_cvt_pk_bf16_f32 v128, v108, v109\n\t" \
    "v_smfmac_f32_16x16x64_bf16 v[116:119], v[88:91], a[216:223], v19\n\t" \
    "s_nop 1\n\t" \
    "v_cvt_pk_bf16_f32 v129, v110, v111\n\t" \
    "v_smfmac_f32_16x16x64_bf16 v[116:119], v[92:95], a[224:231], v19\n\t" \
    "s_nop 1\n\t" \
    "v_cvt_pk_bf16_f32 v130, v112, v113\n\t" \
    "v_smfmac_f32_16x16x64_bf16 v[116:119], v[96:99], a[232:239], v19\n\t" \
    "s_nop 1\n\t" \
    "v_cvt_pk_bf16_f32 v131, v114, v115\n\t" \
    "v_smfmac_f32_16x16x64_bf16 v[120:123], v[84:87], a[240:247], v19\n\t" \
    "s_nop 1\n\t" \
    "v_permlane16_swap_b32 v128, v130\n\t" \
    "v_smfmac_f32_16x16x64_bf16 v[120:123], v[88:91], a[248:255], v19\n\t" \
    "s_nop 1\n\t" \
    "v_permlane16_swap_b32 v129, v131\n\t" \
    "v_smfmac_f32_16x16x64_bf16 v[120:123], v[92:95], v[224:231], v19\n\t" \
    "s_nop 0\n\t" \
    "global_store_dwordx4 v14, v[128:131], s[38:39] sc1\n\t" \
    "v_cvt_pk_bf16_f32 v132, v116, v117\n\t" \
    "v_smfmac_f32_16x16x64_bf16 v[120:123], v[96:99], v[232:239], v19\n\t" \
    "s_nop 1\n\t" \
    "v_cvt_pk_bf16_f32 v133, v118, v119\n\t" \
    "s_nop 4\n\t" \
    "v_cvt_pk_bf16_f32 v134, v120, v121\n\t" \
    "v_cvt_pk_bf16_f32 v135, v122, v123\n\t" \
    "s_nop 0\n\t" \
    "v_permlane16_swap_b32 v132, v134\n\t" \
    "v_permlane16_swap_b32 v133, v135\n\t" \
    "global_store_dwordx4 v15, v[132:135], s[38:39] sc1\n\t" \
    "s_cmp_eq_u32 s65, 63\n\t" \
    "s_cbranch_scc1 vv_end28_%=\n\t" \
    "s_add_u32 s65, s65, 1\n\t" \
    "v_mov_b32 v72, 0\n\t" \
    "v_mov_b32 v73, 0\n\t" \
    "v_mov_b32 v74, 0\n\t" \
    "v_mov_b32 v75, 0\n\t" \
    "v_mov_b32 v76, 0\n\t" \
    "v_mov_b32 v77, 0\n\t" \
    "v_mov_b32 v78, 0\n\t" \
    "v_mov_b32 v79, 0\n\t" \
    "v_mov_b32 v108, 0\n\t" \
    "v_mov_b32 v109, 0\n\t" \
    "v_mov_b32 v110, 0\n\t" \
    "v_mov_b32 v111, 0\n\t" \
    "v_mov_b32 v112, 0\n\t" \
    "v_mov_b32 v113, 0\n\t" \
    "v_mov_b32 v114, 0\n\t" \
    "v_mov_b32 v115, 0\n\t" \
    "v_mov_b32 v116, 0\n\t" \
    "v_mov_b32 v117, 0\n\t" \
    "v_mov_b32 v118, 0\n\t" \
    "v_mov_b32 v119, 0\n\t" \
    "v_mov_b32 v120, 0\n\t" \
    "v_mov_b32 v121, 0\n\t" \
    "v_mov_b32 v122, 0\n\t" \
    "v_mov_b32 v123, 0\n\t" \
    "v_mov_b32 v124, 0\n\t" \
    "v_mov_b32 v125, 0\n\t" \
    "v_mov_b32 v126, 0\n\t" \
    "v_mov_b32 v127, 0\n\t" \
    "s_add_u32 s69, s65, 0\n\t" \
    "s_and_b32 s69, s69, 7\n\t" \
    "s_lshl_b32 s69, s69, 18\n\t" \
    "s_add_u32 s69, s69, s62\n\t" \
    "s_add_u32 s46, s36, s69\n\t" \
    "s_addc_u32 s47, s37, 0\n\t" \
    "s_add_u32 s70, s65, 0\n\t" \
    "s_mul_i32 s69, s70, 171\n\t" \
    "s_lshr_b32 s69, s69, 9\n\t" \
    "s_mul_i32 s70, s70, 589824\n\t" \
    "s_mul_i32 s71, s69, 327680\n\t" \
    "s_add_u32 s70, s70, s71\n\t" \
    "s_lshl1_add_u32 s69, s69, 4\n\t" \
    "s_bitcmp1_b64 s[44:45], s69\n\t" \
    "s_cselect_b32 s69, 8192, 0\n\t" \
    "s_add_u32 s69, s69, s70\n\t" \
    "s_add_u32 s69, s69, 123731968\n\t" \
    "s_add_u32 s38, s36, s69\n\t" \
    "s_addc_u32 s39, s37, 0\n\t" \
    "s_and_b32 s69, s69, 0xffe00000\n\t" \
    "s_add_u32 s69, s69, 1823744\n\t" \
    "v_mov_b32 v17, s69\n\t" \
    "global_load_dword v17, v17, s[36:37]\n\t" \
    "s_and_b32 s69, s65, 7\n\t" \
    "s_lshl_b32 s69, s69, 18\n\t" \
    "s_add_u32 s69, s69, s63\n\t" \
    "s_add_u32 s52, s36, s69\n\t" \
    "s_addc_u32 s53, s37, 0\n\t" \
    "s_barrier\n\t" \
    "ds_read_b32 v160, v11 offset:20480\n\t" \
    "s_waitcnt lgkmcnt(0)\n\t" \
    "v_readfirstlane_b32 s70, v160\n\t" \
    "s_branch vv_loop10_%=\n\t" \
    "vv_Gbad33_%=:\n\t" \
    "s_waitcnt vmcnt(0) lgkmcnt(0)\n\t" \
    "s_nop 7\n\t" \
    "v_mov_b32 v72, 0\n\t" \
    "v_mov_b32 v73, 0\n\t" \
    "v_mov_b32 v74, 0\n\t" \
    "v_mov_b32 v75, 0\n\t" \
    "v_mov_b32 v76, 0\n\t" \
    "v_mov_b32 v77, 0\n\t" \
    "v_mov_b32 v78, 0\n\t" \
    "v_mov_b32 v79, 0\n\t" \
    "s_cmp_eq_u32 s68, 0\n\t" \
    "s_cbranch_scc1 vv_F38_%=\n\t" \
    "s_sub_u32 s68, s68, 1\n\t" \
    "s_branch vv_G32_%=\n\t" \
    "vv_F38_%=:\n\t" \
    "v_mov_b32 v140, 5767424\n\t" \
    "v_mov_b32 v141, 1\n\t" \
    "global_store_dword v140, v141, s[36:37] sc1\n\t" \
    "s_waitcnt vmcnt(0)\n\t" \
    "s_branch vv_Gc34_%=\n\t" \
    "vv_Gslow35_%=:\n\t" \
    "v_max_u32 v140, v36, v37\n\t" \
    "v_max3_u32 v140, v140, v38, v39\n\t" \
    "v_max3_u32 v140, v140, v40, v41\n\t" \
    "v_max3_u32 v140, v140, v42, v43\n\t" \
    "v_max3_u32 v140, v140, v44, v45\n\t" \
    "v_max3_u32 v140, v140, v46, v47\n\t" \
    "v_max3_u32 v140, v140, v48, v49\n\t" \
    "v_max3_u32 v140, v140, v50, v51\n\t" \
    "v_max3_u32 v140, v140, v52, v53\n\t" \
    "v_max3_u32 v140, v140, v54, v55\n\t" \
    "v_max3_u32 v140, v140, v56, v57\n\t" \
    "v_max3_u32 v140, v140, v58, v59\n\t" \
    "v_max3_u32 v140, v140, v60, v61\n\t" \
    "v_max3_u32 v140, v140, v62, v63\n\t" \
    "v_max3_u32 v140, v140, v64, v65\n\t" \
    "v_max3_u32 v140, v140, v66, v67\n\t" \
    "v_max3_u32 v140, v140, v68, v69\n\t" \
    "v_max3_u32 v140, v140, v70, v71\n\t" \
    "v_cmp_eq_u32 vcc, -1, v140\n\t" \
    "s_cbranch_vccz vv_Gback36_%=\n\t" \
    "s_waitcnt lgkmcnt(0)\n\t" \
    "v_sub_u32 v140, v1, v3\n\t" \
    "s_nop 0\n\t" \
    "v_readfirstlane_b32 s69, v140\n\t" \
    "v_add_u32 v143, 20480, v0\n\t" \
    "s_nop 7\n\t" \
    "s_add_u32 s30, s46, s69\n\t" \
    "s_addc_u32 s31, s47, 0\n\t" \
    "vv_Gag39_%=:\n\t" \
    "global_load_dwordx4 v[36:39], v1, s[46:47] sc1\n\t" \
    "global_load_dwordx4 v[40:43], v23, s[46:47] sc1\n\t" \
    "global_load_dwordx4 v[44:47], v143, s[30:31] sc1\n\t" \
    "global_load_dwordx4 v[48:51], v4, s[30:31] sc1\n\t" \
    "global_load_dwordx4 v[52:55], v5, s[30:31] sc1\n\t" \
    "global_load_dwordx4 v[56:59], v6, s[30:31] sc1\n\t" \
    "global_load_dwordx4 v[60:63], v7, s[30:31] sc1\n\t" \
    "global_load_dwordx4 v[64:67], v8, s[30:31] sc1\n\t" \
    "global_load_dwordx4 v[68:71], v9, s[30:31] sc1\n\t" \
    "s_waitcnt vmcnt(0)\n\t" \
    "v_max_u32 v140, v36, v37\n\t" \
    "v_max3_u32 v140, v140, v38, v39\n\t" \
    "v_max3_u32 v140, v140, v40, v41\n\t" \
    "v_max3_u32 v140, v140, v42, v43\n\t" \
    "v_max3_u32 v140, v140, v44, v45\n\t" \
    "v_max3_u32 v140, v140, v46, v47\n\t" \
    "v_max3_u32 v140, v140, v48, v49\n\t" \
    "v_max3_u32 v140, v140, v50, v51\n\t" \
    "v_max3_u32 v140, v140, v52, v53\n\t" \
    "v_max3_u32 v140, v140, v54, v55\n\t" \
    "v_max3_u32 v140, v140, v56, v57\n\t" \
    "v_max3_u32 v140, v140, v58, v59\n\t" \
    "v_max3_u32 v140, v140, v60, v61\n\t" \
    "v_max3_u32 v140, v140, v62, v63\n\t" \
    "v_max3_u32 v140, v140, v64, v65\n\t" \
    "v_max3_u32 v140, v140, v66, v67\n\t" \
    "v_max3_u32 v140, v140, v68, v69\n\t" \
    "v_max3_u32 v140, v140, v70, v71\n\t" \
    "v_cmp_eq_u32 vcc, -1, v140\n\t" \
    "s_cbranch_vccz vv_Gredo40_%=\n\t" \
    "s_cmp_eq_u32 s68, 0\n\t" \
    "s_cbranch_scc1 vv_F41_%=\n\t" \
    "s_sub_u32 s68, s68, 1\n\t" \
    "s_sleep 1\n\t" \
    "s_branch vv_Gag39_%=\n\t" \
    "vv_F41_%=:\n\t" \
    "v_mov_b32 v140, 5767424\n\t" \
    "v_mov_b32 v141, 2\n\t" \
    "global_store_dword v140, v141, s[36:37] sc1\n\t" \
    "s_waitcnt vmcnt(0)\n\t" \
    "s_branch vv_Gredo40_%=\n\t" \
    "vv_Gredo40_%=:\n\t" \
    "v_mov_b32 v72, 0\n\t" \
    "v_mov_b32 v73, 0\n\t" \
    "v_mov_b32 v74, 0\n\t" \
    "v_mov_b32 v75, 0\n\t" \
    "v_mov_b32 v76, 0\n\t" \
    "v_mov_b32 v77, 0\n\t" \
    "v_mov_b32 v78, 0\n\t" \
    "v_mov_b32 v79, 0\n\t" \
    "v_smfmac_f32_16x16x64_bf16 v[72:75], v[36:39], a[16:23], v10\n\t" \
    "v_smfmac_f32_16x16x64_bf16 v[72:75], v[40:43], a[24:31], v10\n\t" \
    "v_smfmac_f32_16x16x64_bf16 v[72:75], v[48:51], a[32:39], v10\n\t" \
    "v_smfmac_f32_16x16x64_bf16 v[72:75], v[52:55], a[40:47], v10\n\t" \
    "v_smfmac_f32_16x16x64_bf16 v[72:75], v[56:59], a[48:55], v10\n\t" \
    "v_smfmac_f32_16x16x64_bf16 v[72:75], v[60:63], a[56:63], v10\n\t" \
    "v_smfmac_f32_16x16x64_bf16 v[72:75], v[64:67], a[0:7], v10\n\t" \
    "v_smfmac_f32_16x16x64_bf16 v[72:75], v[68:71], a[8:15], v10\n\t" \
    "v_smfmac_f32_16x16x64_bf16 v[72:75], v[44:47], a[64:71], v10\n\t" \
    "v_smfmac_f32_16x16x64_bf16 v[76:79], v[36:39], a[88:95], v10\n\t" \
    "v_smfmac_f32_16x16x64_bf16 v[76:79], v[40:43], a[96:103], v10\n\t" \
    "v_smfmac_f32_16x16x64_bf16 v[76:79], v[48:51], a[104:111], v10\n\t" \
    "v_smfmac_f32_16x16x64_bf16 v[76:79], v[52:55], a[112:119], v10\n\t" \
    "v_smfmac_f32_16x16x64_bf16 v[76:79], v[56:59], a[120:127], v10\n\t" \
    "v_smfmac_f32_16x16x64_bf16 v[76:79], v[60:63], a[128:135], v10\n\t" \
    "v_smfmac_f32_16x16x64_bf16 v[76:79], v[64:67], a[72:79], v10\n\t" \
    "v_smfmac_f32_16x16x64_bf16 v[76:79], v[68:71], a[80:87], v10\n\t" \
    "v_smfmac_f32_16x16x64_bf16 v[76:79], v[44:47], a[136:143], v10\n\t" \
    "v_add_f32 v72, v72, v74\n\t" \
    "v_add_f32 v73, v73, v75\n\t" \
    "v_cvt_pk_bf16_f32 v80, v72, v73\n\t" \
    "ds_write_b32 v12, v80 offset:0\n\t" \
    "s_nop 3\n\t" \
    "v_pk_add_f32 v[76:77], v[76:77], v[78:79]\n\t" \
    "v_cvt_pk_bf16_f32 v81, v76, v77\n\t" \
    "ds_write_b32 v12, v81 offset:256\n\t" \
    "s_branch vv_Gback36_%=\n\t" \
    "vv_end28_%=:\n\t" \
    "s_endpgm\n\t" \
    ".p2align 6\n\t" \
    "s_nop 0\n\t" \
    "s_nop 0\n\t" \
    "s_nop 0\n\t" \
    "s_nop 0\n\t" \
    "s_nop 0\n\t" \
    "s_nop 0\n\t" \
    "vv_loop11_%=:\n\t" \
    "s_getpc_b64 s[30:31]\n\t" \
    "vv_Wp44_%=:\n\t" \
    "s_add_u32 s30, s30, vv_We45_%=-vv_Wp44_%=\n\t" \
    "s_addc_u32 s31, s31, 0\n\t" \
    "s_sub_u32 s70, s70, -54\n\t" \
    "vv_W43_%=:\n\t" \
    "s_memtime s[56:57]\n\t" \
    "s_waitcnt lgkmcnt(0)\n\t" \
    "s_sub_u32 s69, s70, s56\n\t" \
    "s_cmp_gt_i32 s69, 80\n\t" \
    "s_cbranch_scc1 vv_W43_%=\n\t" \
    "s_max_i32 s69, s69, 0\n\t" \
    "s_and_b32 s69, s69, -4\n\t" \
    "s_sub_u32 s30, s30, s69\n\t" \
    "s_subb_u32 s31, s31, 0\n\t" \
    "s_setpc_b64 s[30:31]\n\t" \
    "s_nop 0\n\t" \
    "s_nop 0\n\t" \
    "s_nop 0\n\t" \
    "s_nop 0\n\t" \
    "s_nop 0\n\t" \
    "s_nop 0\n\t" \
    "s_nop 0\n\t" \
    "s_nop 0\n\t" \
    "s_nop 0\n\t" \
    "s_nop 0\n\t" \
    "s_nop 0\n\t" \
    "s_nop 0\n\t" \
    "s_nop 0\n\t" \
    "s_nop 0\n\t" \
    "s_nop 0\n\t" \
    "s_nop 0\n\t" \
    "s_nop 0\n\t" \
    "s_nop 0\n\t" \
    "s_nop 0\n\t" \
    "s_nop 0\n\t" \
    "vv_We45_%=:\n\t" \
    "vv_G46_%=:\n\t" \
    "global_load_dwordx4 v[36:39], v1, s[46:47] sc1\n\t" \
    "global_load_dword v141, v2, s[46:47] sc1\n\t" \
    "global_load_dwordx4 v[40:43], v23, s[46:47] sc1\n\t" \
    "s_waitcnt vmcnt(2)\n\t" \
    "vv_Garr51_%=:\n\t" \
    "ds_write_b128 v3, v[36:39]\n\t" \
    "v_smfmac_f32_16x16x64_bf16 v[72:75], v[36:39], a[32:39], v10\n\t" \
    "s_waitcnt vmcnt(1)\n\t" \
    "ds_write_b32 v22, v141 offset:19968\n\t" \
    "s_waitcnt vmcnt(0)\n\t" \
    "ds_write_b128 v3, v[40:43] offset:1024\n\t" \
    "v_max3_u32 v140, v36, v40, v141\n\t" \
    "v_cmp_eq_u32 vcc, -1, v140\n\t" \
    "s_cbranch_vccnz vv_Gbad47_%=\n\t" \
    "vv_Gc48_%=:\n\t" \
    "s_waitcnt lgkmcnt(0)\n\t" \
    "s_barrier\n\t" \
    "ds_read_b128 v[48:51], v4\n\t" \
    "ds_read_b128 v[52:55], v5\n\t" \
    "v_smfmac_f32_16x16x64_bf16 v[76:79], v[36:39], a[104:111], v10\n\t" \
    "ds_read_b128 v[56:59], v6\n\t" \
    "ds_read_b128 v[60:63], v7\n\t" \
    "v_smfmac_f32_16x16x64_bf16 v[72:75], v[40:43], a[40:47], v10\n\t" \
    "ds_read_b128 v[64:67], v8\n\t" \
    "v_smfmac_f32_16x16x64_bf16 v[76:79], v[40:43], a[112:119], v10\n\t" \
    "ds_read_b128 v[68:71], v9\n\t" \
    "s_waitcnt lgkmcnt(5)\n\t" \
    "v_smfmac_f32_16x16x64_bf16 v[72:75], v[48:51], a[48:55], v10\n\t" \
    "ds_read_b128 v[44:47], v0 offset:19456\n\t" \
    "s_waitcnt lgkmcnt(5)\n\t" \
    "v_smfmac_f32_16x16x64_bf16 v[72:75], v[52:55], a[56:63], v10\n\t" \
    "s_waitcnt lgkmcnt(4)\n\t" \
    "v_smfmac_f32_16x16x64_bf16 v[72:75], v[56:59], a[0:7], v10\n\t" \
    "s_waitcnt lgkmcnt(3)\n\t" \
    "v_smfmac_f32_16x16x64_bf16 v[72:75], v[60:63], a[8:15], v10\n\t" \
    "s_waitcnt lgkmcnt(2)\n\t" \
    "v_smfmac_f32_16x16x64_bf16 v[72:75], v[64:67], a[16:23], v10\n\t" \
    "s_waitcnt lgkmcnt(1)\n\t" \
    "v_smfmac_f32_16x16x64_bf16 v[72:75], v[68:71], a[24:31], v10\n\t" \
    "s_waitcnt lgkmcnt(0)\n\t" \
    "v_smfmac_f32_16x16x64_bf16 v[72:75], v[44:47], a[64:71], v10\n\t" \
    "s_nop 2\n\t" \
    "v_smfmac_f32_16x16x64_bf16 v[76:79], v[48:51], a[120:127], v10\n\t" \
    "s_nop 2\n\t" \
    "v_smfmac_f32_16x16x64_bf16 v[76:79], v[52:55], a[128:135], v10\n\t" \
    "v_add_f32 v72, v72, v74\n\t" \
    "v_smfmac_f32_16x16x64_bf16 v[76:79], v[56:59], a[72:79], v10\n\t" \
    "v_add_f32 v73, v73, v75\n\t" \
    "v_smfmac_f32_16x16x64_bf16 v[76:79], v[60:63], a[80:87], v10\n\t" \
    "v_cvt_pk_bf16_f32 v80, v72, v73\n\t" \
    "v_smfmac_f32_16x16x64_bf16 v[76:79], v[64:67], a[88:95], v10\n\t" \
    "ds_write_b32 v12, v80 offset:0\n\t" \
    "v_smfmac_f32_16x16x64_bf16 v[76:79], v[68:71], a[96:103], v10\n\t" \
    "v_smfmac_f32_16x16x64_bf16 v[76:79], v[44:47], a[136:143], v10\n\t" \
    "s_nop 6\n\t" \
    "v_cmp_u_f32 vcc, v72, v73\n\t" \
    "v_pk_add_f32 v[76:77], v[76:77], v[78:79]\n\t" \
    "v_cvt_pk_bf16_f32 v81, v76, v77\n\t" \
    "ds_write_b32 v12, v81 offset:256\n\t" \
    "s_cbranch_vccnz vv_Gslow49_%=\n\t" \
    "vv_Gback50_%=:\n\t" \
    "s_waitcnt lgkmcnt(0)\n\t" \
    "s_barrier\n\t" \
    "ds_read_b64_tr_b16 v[84:85], v13 offset:0\n\t" \
    "v_smfmac_f32_16x16x64_bf16 v[100:103], v[36:39], a[0:7], v10\n\t" \
    "ds_read_b64_tr_b16 v[86:87], v13 offset:64\n\t" \
    "ds_read_b64_tr_b16 v[92:93], v13 offset:1152\n\t" \
    "v_smfmac_f32_16x16x64_bf16 v[100:103], v[36:39], a[0:7], v10\n\t" \
    "ds_read_b64_tr_b16 v[94:95], v13 offset:1216\n\t" \
    "ds_read_b64_tr_b16 v[88:89], v25 offset:0\n\t" \
    "v_smfmac_f32_16x16x64_bf16 v[100:103], v[36:39], a[0:7], v10\n\t" \
    "ds_read_b64_tr_b16 v[90:91], v25 offset:64\n\t" \
    "s_waitcnt lgkmcnt(4)\n\t" \
    "v_smfmac_f32_16x16x64_bf16 v[124:127], v[84:87], v[240:247], v19\n\t" \
    "ds_read_b64_tr_b16 v[96:97], v25 offset:1152\n\t" \
    "ds_read_b64_tr_b16 v[98:99], v25 offset:1216\n\t" \
    "s_waitcnt lgkmcnt(4)\n\t" \
    "v_smfmac_f32_16x16x64_bf16 v[124:127], v[92:95], v[248:255], v19\n\t" \
    "s_nop 2\n\t" \
    "v_smfmac_f32_16x16x64_bf16 v[108:111], v[84:87], a[144:151], v19\n\t" \
    "s_nop 2\n\t" \
    "v_smfmac_f32_16x16x64_bf16 v[108:111], v[92:95], a[160:167], v19\n\t" \
    "s_nop 1\n\t" \
    "v_permlane32_swap_b32 v124, v126\n\t" \
    "v_smfmac_f32_16x16x64_bf16 v[112:115], v[84:87], a[176:183], v19\n\t" \
    "s_nop 1\n\t" \
    "v_permlane32_swap_b32 v125, v127\n\t" \
    "v_smfmac_f32_16x16x64_bf16 v[112:115], v[92:95], a[192:199], v19\n\t" \
    "s_nop 0\n\t" \
    "v_add_f32 v124, v124, v126\n\t" \
    "s_waitcnt lgkmcnt(2)\n\t" \
    "v_smfmac_f32_16x16x64_bf16 v[108:111], v[88:91], a[152:159], v19\n\t" \
    "s_nop 1\n\t" \
    "v_add_f32 v125, v125, v127\n\t" \
    "v_smfmac_f32_16x16x64_bf16 v[112:115], v[88:91], a[184:191], v19\n\t" \
    "s_nop 0\n\t" \
    "v_cvt_pk_bf16_f32 v136, v124, v125\n\t" \
    "s_waitcnt lgkmcnt(0)\n\t" \
    "v_smfmac_f32_16x16x64_bf16 v[108:111], v[96:99], a[168:175], v19\n\t" \
    "s_nop 1\n\t" \
    "v_cvt_pk_bf16_f32 v137, v124, v125\n\t" \
    "v_smfmac_f32_16x16x64_bf16 v[112:115], v[96:99], a[200:207], v19\n\t" \
    "s_nop 1\n\t" \
    "v_permlane32_swap_b32 v136, v137\n\t" \
    "v_smfmac_f32_16x16x64_bf16 v[116:119], v[84:87], a[208:215], v19\n\t" \
    "s_mov_b32 exec_hi, 0\n\t" \
    "global_store_dwordx2 v16, v[136:137], s[38:39] sc1\n\t" \
    "s_mov_b32 exec_hi, -1\n\t" \
    "v_cvt_pk_bf16_f32 v128, v108, v109\n\t" \
    "v_smfmac_f32_16x16x64_bf16 v[116:119], v[88:91], a[216:223], v19\n\t" \
    "s_nop 1\n\t" \
    "v_cvt_pk_bf16_f32 v129, v110, v111\n\t" \
    "v_smfmac_f32_16x16x64_bf16 v[116:119], v[92:95], a[224:231], v19\n\t" \
    "s_nop 1\n\t" \
    "v_cvt_pk_bf16_f32 v130, v112, v113\n\t" \
    "v_smfmac_f32_16x16x64_bf16 v[116:119], v[96:99], a[232:239], v19\n\t" \
    "s_nop 1\n\t" \
    "v_cvt_pk_bf16_f32 v131, v114, v115\n\t" \
    "v_smfmac_f32_16x16x64_bf16 v[120:123], v[84:87], a[240:247], v19\n\t" \
    "s_nop 1\n\t" \
    "v_permlane16_swap_b32 v128, v130\n\t" \
    "v_smfmac_f32_16x16x64_bf16 v[120:123], v[88:91], a[248:255], v19\n\t" \
    "s_nop 1\n\t" \
    "v_permlane16_swap_b32 v129, v131\n\t" \
    "v_smfmac_f32_16x16x64_bf16 v[120:123], v[92:95], v[224:231], v19\n\t" \
    "s_nop 0\n\t" \
    "global_store_dwordx4 v14, v[128:131], s[38:39] sc1\n\t" \
    "v_cvt_pk_bf16_f32 v132, v116, v117\n\t" \
    "v_smfmac_f32_16x16x64_bf16 v[120:123], v[96:99], v[232:239], v19\n\t" \
    "s_nop 1\n\t" \
    "v_cvt_pk_bf16_f32 v133, v118, v119\n\t" \
    "s_nop 4\n\t" \
    "v_cvt_pk_bf16_f32 v134, v120, v121\n\t" \
    "v_cvt_pk_bf16_f32 v135, v122, v123\n\t" \
    "s_nop 0\n\t" \
    "v_permlane16_swap_b32 v132, v134\n\t" \
    "v_permlane16_swap_b32 v133, v135\n\t" \
    "global_store_dwordx4 v15, v[132:135], s[38:39] sc1\n\t" \
    "s_cmp_eq_u32 s65, 63\n\t" \
    "s_cbranch_scc1 vv_end42_%=\n\t" \
    "s_add_u32 s65, s65, 1\n\t" \
    "v_mov_b32 v72, 0\n\t" \
    "v_mov_b32 v73, 0\n\t" \
    "v_mov_b32 v74, 0\n\t" \
    "v_mov_b32 v75, 0\n\t" \
    "v_mov_b32 v76, 0\n\t" \
    "v_mov_b32 v77, 0\n\t" \
    "v_mov_b32 v78, 0\n\t" \
    "v_mov_b32 v79, 0\n\t" \
    "v_mov_b32 v108, 0\n\t" \
    "v_mov_b32 v109, 0\n\t" \
    "v_mov_b32 v110, 0\n\t" \
    "v_mov_b32 v111, 0\n\t" \
    "v_mov_b32 v112, 0\n\t" \
    "v_mov_b32 v113, 0\n\t" \
    "v_mov_b32 v114, 0\n\t" \
    "v_mov_b32 v115, 0\n\t" \
    "v_mov_b32 v116, 0\n\t" \
    "v_mov_b32 v117, 0\n\t" \
    "v_mov_b32 v118, 0\n\t" \
    "v_mov_b32 v119, 0\n\t" \
    "v_mov_b32 v120, 0\n\t" \
    "v_mov_b32 v121, 0\n\t" \
    "v_mov_b32 v122, 0\n\t" \
    "v_mov_b32 v123, 0\n\t" \
    "v_mov_b32 v124, 0\n\t" \
    "v_mov_b32 v125, 0\n\t" \
    "v_mov_b32 v126, 0\n\t" \
    "v_mov_b32 v127, 0\n\t" \
    "s_add_u32 s69, s65, 0\n\t" \
    "s_and_b32 s69, s69, 7\n\t" \
    "s_lshl_b32 s69, s69, 18\n\t" \
    "s_add_u32 s69, s69, s62\n\t" \
    "s_add_u32 s46, s36, s69\n\t" \
    "s_addc_u32 s47, s37, 0\n\t" \
    "s_add_u32 s70, s65, 0\n\t" \
    "s_mul_i32 s69, s70, 171\n\t" \
    "s_lshr_b32 s69, s69, 9\n\t" \
    "s_mul_i32 s70, s70, 589824\n\t" \
    "s_mul_i32 s71, s69, 327680\n\t" \
    "s_add_u32 s70, s70, s71\n\t" \
    "s_lshl1_add_u32 s69, s69, 4\n\t" \
    "s_bitcmp1_b64 s[44:45], s69\n\t" \
    "s_cselect_b32 s69, 8192, 0\n\t" \
    "s_add_u32 s69, s69, s70\n\t" \
    "s_add_u32 s69, s69, 123731968\n\t" \
    "s_add_u32 s38, s36, s69\n\t" \
    "s_addc_u32 s39, s37, 0\n\t" \
    "s_and_b32 s69, s69, 0xffe00000\n\t" \
    "s_add_u32 s69, s69, 1823744\n\t" \
    "v_mov_b32 v17, s69\n\t" \
    "global_load_dword v17, v17, s[36:37]\n\t" \
    "s_and_b32 s69, s65, 7\n\t" \
    "s_lshl_b32 s69, s69, 18\n\t" \
    "s_add_u32 s69, s69, s63\n\t" \
    "s_add_u32 s52, s36, s69\n\t" \
    "s_addc_u32 s53, s37, 0\n\t" \
    "s_barrier\n\t" \
    "ds_read_b32 v160, v11 offset:20480\n\t" \
    "s_waitcnt lgkmcnt(0)\n\t" \
    "v_readfirstlane_b32 s70, v160\n\t" \
    "s_branch vv_loop11_%=\n\t" \
    "vv_Gbad47_%=:\n\t" \
    "s_waitcnt vmcnt(0) lgkmcnt(0)\n\t" \
    "s_nop 7\n\t" \
    "v_mov_b32 v72, 0\n\t" \
    "v_mov_b32 v73, 0\n\t" \
    "v_mov_b32 v74, 0\n\t" \
    "v_mov_b32 v75, 0\n\t" \
    "v_mov_b32 v76, 0\n\t" \
    "v_mov_b32 v77, 0\n\t" \
    "v_mov_b32 v78, 0\n\t" \
    "v_mov_b32 v79, 0\n\t" \
    "s_cmp_eq_u32 s68, 0\n\t" \
    "s_cbranch_scc1 vv_F52_%=\n\t" \
    "s_sub_u32 s68, s68, 1\n\t" \
    "s_branch vv_G46_%=\n\t" \
    "vv_F52_%=:\n\t" \
    "v_mov_b32 v140, 5767424\n\t" \
    "v_mov_b32 v141, 1\n\t" \
    "global_store_dword v140, v141, s[36:37] sc1\n\t" \
    "s_waitcnt vmcnt(0)\n\t" \
    "s_branch vv_Gc48_%=\n\t" \
    "vv_Gslow49_%=:\n\t" \
    "v_max_u32 v140, v36, v37\n\t" \
    "v_max3_u32 v140, v140, v38, v39\n\t" \
    "v_max3_u32 v140, v140, v40, v41\n\t" \
    "v_max3_u32 v140, v140, v42, v43\n\t" \
    "v_max3_u32 v140, v140, v44, v45\n\t" \
    "v_max3_u32 v140, v140, v46, v47\n\t" \
    "v_max3_u32 v140, v140, v48, v49\n\t" \
    "v_max3_u32 v140, v140, v50, v51\n\t" \
    "v_max3_u32 v140, v140, v52, v53\n\t" \
    "v_max3_u32 v140, v140, v54, v55\n\t" \
    "v_max3_u32 v140, v140, v56, v57\n\t" \
    "v_max3_u32 v140, v140, v58, v59\n\t" \
    "v_max3_u32 v140, v140, v60, v61\n\t" \
    "v_max3_u32 v140, v140, v62, v63\n\t" \
    "v_max3_u32 v140, v140, v64, v65\n\t" \
    "v_max3_u32 v140, v140, v66, v67\n\t" \
    "v_max3_u32 v140, v140, v68, v69\n\t" \
    "v_max3_u32 v140, v140, v70, v71\n\t" \
    "v_cmp_eq_u32 vcc, -1, v140\n\t" \
    "s_cbranch_vccz vv_Gback50_%=\n\t" \
    "s_waitcnt lgkmcnt(0)\n\t" \
    "v_sub_u32 v140, v1, v3\n\t" \
    "s_nop 0\n\t" \
    "v_readfirstlane_b32 s69, v140\n\t" \
    "v_add_u32 v143, 20480, v0\n\t" \
    "s_nop 7\n\t" \
    "s_add_u32 s30, s46, s69\n\t" \
    "s_addc_u32 s31, s47, 0\n\t" \
    "vv_Gag53_%=:\n\t" \
    "global_load_dwordx4 v[36:39], v1, s[46:47] sc1\n\t" \
    "global_load_dwordx4 v[40:43], v23, s[46:47] sc1\n\t" \
    "global_load_dwordx4 v[44:47], v143, s[30:31] sc1\n\t" \
    "global_load_dwordx4 v[48:51], v4, s[30:31] sc1\n\t" \
    "global_load_dwordx4 v[52:55], v5, s[30:31] sc1\n\t" \
    "global_load_dwordx4 v[56:59], v6, s[30:31] sc1\n\t" \
    "global_load_dwordx4 v[60:63], v7, s[30:31] sc1\n\t" \
    "global_load_dwordx4 v[64:67], v8, s[30:31] sc1\n\t" \
    "global_load_dwordx4 v[68:71], v9, s[30:31] sc1\n\t" \
    "s_waitcnt vmcnt(0)\n\t" \
    "v_max_u32 v140, v36, v37\n\t" \
    "v_max3_u32 v140, v140, v38, v39\n\t" \
    "v_max3_u32 v140, v140, v40, v41\n\t" \
    "v_max3_u32 v140, v140, v42, v43\n\t" \
    "v_max3_u32 v140, v140, v44, v45\n\t" \
    "v_max3_u32 v140, v140, v46, v47\n\t" \
    "v_max3_u32 v140, v140, v48, v49\n\t" \
    "v_max3_u32 v140, v140, v50, v51\n\t" \
    "v_max3_u32 v140, v140, v52, v53\n\t" \
    "v_max3_u32 v140, v140, v54, v55\n\t" \
    "v_max3_u32 v140, v140, v56, v57\n\t" \
    "v_max3_u32 v140, v140, v58, v59\n\t" \
    "v_max3_u32 v140, v140, v60, v61\n\t" \
    "v_max3_u32 v140, v140, v62, v63\n\t" \
    "v_max3_u32 v140, v140, v64, v65\n\t" \
    "v_max3_u32 v140, v140, v66, v67\n\t" \
    "v_max3_u32 v140, v140, v68, v69\n\t" \
    "v_max3_u32 v140, v140, v70, v71\n\t" \
    "v_cmp_eq_u32 vcc, -1, v140\n\t" \
    "s_cbranch_vccz vv_Gredo54_%=\n\t" \
    "s_cmp_eq_u32 s68, 0\n\t" \
    "s_cbranch_scc1 vv_F55_%=\n\t" \
    "s_sub_u32 s68, s68, 1\n\t" \
    "s_sleep 1\n\t" \
    "s_branch vv_Gag53_%=\n\t" \
    "vv_F55_%=:\n\t" \
    "v_mov_b32 v140, 5767424\n\t" \
    "v_mov_b32 v141, 2\n\t" \
    "global_store_dword v140, v141, s[36:37] sc1\n\t" \
    "s_waitcnt vmcnt(0)\n\t" \
    "s_branch vv_Gredo54_%=\n\t" \
    "vv_Gredo54_%=:\n\t" \
    "v_mov_b32 v72, 0\n\t" \
    "v_mov_b32 v73, 0\n\t" \
    "v_mov_b32 v74, 0\n\t" \
    "v_mov_b32 v75, 0\n\t" \
    "v_mov_b32 v76, 0\n\t" \
    "v_mov_b32 v77, 0\n\t" \
    "v_mov_b32 v78, 0\n\t" \
    "v_mov_b32 v79, 0\n\t" \
    "v_smfmac_f32_16x16x64_bf16 v[72:75], v[36:39], a[32:39], v10\n\t" \
    "v_smfmac_f32_16x16x64_bf16 v[72:75], v[40:43], a[40:47], v10\n\t" \
    "v_smfmac_f32_16x16x64_bf16 v[72:75], v[48:51], a[48:55], v10\n\t" \
    "v_smfmac_f32_16x16x64_bf16 v[72:75], v[52:55], a[56:63], v10\n\t" \
    "v_smfmac_f32_16x16x64_bf16 v[72:75], v[56:59], a[0:7], v10\n\t" \
    "v_smfmac_f32_16x16x64_bf16 v[72:75], v[60:63], a[8:15], v10\n\t" \
    "v_smfmac_f32_16x16x64_bf16 v[72:75], v[64:67], a[16:23], v10\n\t" \
    "v_smfmac_f32_16x16x64_bf16 v[72:75], v[68:71], a[24:31], v10\n\t" \
    "v_smfmac_f32_16x16x64_bf16 v[72:75], v[44:47], a[64:71], v10\n\t" \
    "v_smfmac_f32_16x16x64_bf16 v[76:79], v[36:39], a[104:111], v10\n\t" \
    "v_smfmac_f32_16x16x64_bf16 v[76:79], v[40:43], a[112:119], v10\n\t" \
    "v_smfmac_f32_16x16x64_bf16 v[76:79], v[48:51], a[120:127], v10\n\t" \
    "v_smfmac_f32_16x16x64_bf16 v[76:79], v[52:55], a[128:135], v10\n\t" \
    "v_smfmac_f32_16x16x64_bf16 v[76:79], v[56:59], a[72:79], v10\n\t" \
    "v_smfmac_f32_16x16x64_bf16 v[76:79], v[60:63], a[80:87], v10\n\t" \
    "v_smfmac_f32_16x16x64_bf16 v[76:79], v[64:67], a[88:95], v10\n\t" \
    "v_smfmac_f32_16x16x64_bf16 v[76:79], v[68:71], a[96:103], v10\n\t" \
    "v_smfmac_f32_16x16x64_bf16 v[76:79], v[44:47], a[136:143], v10\n\t" \
    "v_add_f32 v72, v72, v74\n\t" \
    "v_add_f32 v73, v73, v75\n\t" \
    "v_cvt_pk_bf16_f32 v80, v72, v73\n\t" \
    "ds_write_b32 v12, v80 offset:0\n\t" \
    "s_nop 3\n\t" \
    "v_pk_add_f32 v[76:77], v[76:77], v[78:79]\n\t" \
    "v_cvt_pk_bf16_f32 v81, v76, v77\n\t" \
    "ds_write_b32 v12, v81 offset:256\n\t" \
    "s_branch vv_Gback50_%=\n\t" \
    "vv_end42_%=:\n\t" \
    "s_endpgm\n\t" \
    ".p2align 6\n\t" \
    "s_nop 0\n\t" \
    "s_nop 0\n\t" \
    "s_nop 0\n\t" \
    "s_nop 0\n\t" \
    "s_nop 0\n\t" \
    "s_nop 0\n\t" \
    "vv_loop12_%=:\n\t" \
    "s_getpc_b64 s[30:31]\n\t" \
    "vv_Wp58_%=:\n\t" \
    "s_add_u32 s30, s30, vv_We59_%=-vv_Wp58_%=\n\t" \
    "s_addc_u32 s31, s31, 0\n\t" \
    "s_sub_u32 s70, s70, -54\n\t" \
    "vv_W57_%=:\n\t" \
    "s_memtime s[56:57]\n\t" \
    "s_waitcnt lgkmcnt(0)\n\t" \
    "s_sub_u32 s69, s70, s56\n\t" \
    "s_cmp_gt_i32 s69, 80\n\t" \
    "s_cbranch_scc1 vv_W57_%=\n\t" \
    "s_max_i32 s69, s69, 0\n\t" \
    "s_and_b32 s69, s69, -4\n\t" \
    "s_sub_u32 s30, s30, s69\n\t" \
    "s_subb_u32 s31, s31, 0\n\t" \
    "s_setpc_b64 s[30:31]\n\t" \
    "s_nop 0\n\t" \
    "s_nop 0\n\t" \
    "s_nop 0\n\t" \
    "s_nop 0\n\t" \
    "s_nop 0\n\t" \
    "s_nop 0\n\t" \
    "s_nop 0\n\t" \
    "s_nop 0\n\t" \
    "s_nop 0\n\t" \
    "s_nop 0\n\t" \
    "s_nop 0\n\t" \
    "s_nop 0\n\t" \
    "s_nop 0\n\t" \
    "s_nop 0\n\t" \
    "s_nop 0\n\t" \
    "s_nop 0\n\t" \
    "s_nop 0\n\t" \
    "s_nop 0\n\t" \
    "s_nop 0\n\t" \
    "s_nop 0\n\t" \
    "vv_We59_%=:\n\t" \
    "vv_G60_%=:\n\t" \
    "global_load_dwordx4 v[36:39], v1, s[46:47] sc1\n\t" \
    "global_load_dword v141, v2, s[46:47] sc1\n\t" \
    "global_load_dwordx4 v[40:43], v23, s[46:47] sc1\n\t" \
    "s_waitcnt vmcnt(2)\n\t" \
    "vv_Garr65_%=:\n\t" \
    "ds_write_b128 v3, v[36:39]\n\t" \
    "v_smfmac_f32_16x16x64_bf16 v[72:75], v[36:39], a[48:55], v10\n\t" \
    "s_waitcnt vmcnt(1)\n\t" \
    "ds_write_b32 v22, v141 offset:20224\n\t" \
    "s_waitcnt vmcnt(0)\n\t" \
    "ds_write_b128 v3, v[40:43] offset:1024\n\t" \
    "v_max3_u32 v140, v36, v40, v141\n\t" \
    "v_cmp_eq_u32 vcc, -1, v140\n\t" \
    "s_cbranch_vccnz vv_Gbad61_%=\n\t" \
    "vv_Gc62_%=:\n\t" \
    "s_waitcnt lgkmcnt(0)\n\t" \
    "s_barrier\n\t" \
    "ds_read_b128 v[48:51], v4\n\t" \
    "ds_read_b128 v[52:55], v5\n\t" \
    "v_smfmac_f32_16x16x64_bf16 v[76:79], v[36:39], a[120:127], v10\n\t" \
    "ds_read_b128 v[56:59], v6\n\t" \
    "ds_read_b128 v[60:63], v7\n\t" \
    "v_smfmac_f32_16x16x64_bf16 v[72:75], v[40:43], a[56:63], v10\n\t" \
    "ds_read_b128 v[64:67], v8\n\t" \
    "v_smfmac_f32_16x16x64_bf16 v[76:79], v[40:43], a[128:135], v10\n\t" \
    "ds_read_b128 v[68:71], v9\n\t" \
    "s_waitcnt lgkmcnt(5)\n\t" \
    "v_smfmac_f32_16x16x64_bf16 v[72:75], v[48:51], a[0:7], v10\n\t" \
    "ds_read_b128 v[44:47], v0 offset:19456\n\t" \
    "s_waitcnt lgkmcnt(5)\n\t" \
    "v_smfmac_f32_16x16x64_bf16 v[72:75], v[52:55], a[8:15], v10\n\t" \
    "s_waitcnt lgkmcnt(4)\n\t" \
    "v_smfmac_f32_16x16x64_bf16 v[72:75], v[56:59], a[16:23], v10\n\t" \
    "s_waitcnt lgkmcnt(3)\n\t" \
    "v_smfmac_f32_16x16x64_bf16 v[72:75], v[60:63], a[24:31], v10\n\t" \
    "s_waitcnt lgkmcnt(2)\n\t" \
    "v_smfmac_f32_16x16x64_bf16 v[72:75], v[64:67], a[32:39], v10\n\t" \
    "s_waitcnt lgkmcnt(1)\n\t" \
    "v_smfmac_f32_16x16x64_bf16 v[72:75], v[68:71], a[40:47], v10\n\t" \
    "s_waitcnt lgkmcnt(0)\n\t" \
    "v_smfmac_f32_16x16x64_bf16 v[72:75], v[44:47], a[64:71], v10\n\t" \
    "s_nop 2\n\t" \
    "v_smfmac_f32_16x16x64_bf16 v[76:79], v[48:51], a[72:79], v10\n\t" \
    "s_nop 2\n\t" \
    "v_smfmac_f32_16x16x64_bf16 v[76:79], v[52:55], a[80:87], v10\n\t" \
    "v_add_f32 v72, v72, v74\n\t" \
    "v_smfmac_f32_16x16x64_bf16 v[76:79], v[56:59], a[88:95], v10\n\t" \
    "v_add_f32 v73, v73, v75\n\t" \
    "v_smfmac_f32_16x16x64_bf16 v[76:79], v[60:63], a[96:103], v10\n\t" \
    "v_cvt_pk_bf16_f32 v80, v72, v73\n\t" \
    "v_smfmac_f32_16x16x64_bf16 v[76:79], v[64:67], a[104:111], v10\n\t" \
    "ds_write_b32 v12, v80 offset:0\n\t" \
    "v_smfmac_f32_16x16x64_bf16 v[76:79], v[68:71], a[112:119], v10\n\t" \
    "v_smfmac_f32_16x16x64_bf16 v[76:79], v[44:47], a[136:143], v10\n\t" \
    "s_nop 6\n\t" \
    "v_cmp_u_f32 vcc, v72, v73\n\t" \
    "v_pk_add_f32 v[76:77], v[76:77], v[78:79]\n\t" \
    "v_cvt_pk_bf16_f32 v81, v76, v77\n\t" \
    "ds_write_b32 v12, v81 offset:256\n\t" \
    "s_cbranch_vccnz vv_Gslow63_%=\n\t" \
    "vv_Gback64_%=:\n\t" \
    "s_waitcnt lgkmcnt(0)\n\t" \
    "s_barrier\n\t" \
    "ds_read_b64_tr_b16 v[84:85], v13 offset:0\n\t" \
    "v_smfmac_f32_16x16x64_bf16 v[100:103], v[36:39], a[0:7], v10\n\t" \
    "ds_read_b64_tr_b16 v[86:87], v13 offset:64\n\t" \
    "ds_read_b64_tr_b16 v[92:93], v13 offset:1152\n\t" \
    "v_smfmac_f32_16x16x64_bf16 v[100:103], v[36:39], a[0:7], v10\n\t" \
    "ds_read_b64_tr_b16 v[94:95], v13 offset:1216\n\t" \
    "ds_read_b64_tr_b16 v[88:89], v25 offset:0\n\t" \
    "v_smfmac_f32_16x16x64_bf16 v[100:103], v[36:39], a[0:7], v10\n\t" \
    "ds_read_b64_tr_b16 v[90:91], v25 offset:64\n\t" \
    "s_waitcnt lgkmcnt(4)\n\t" \
    "v_smfmac_f32_16x16x64_bf16 v[124:127], v[84:87], v[240:247], v19\n\t" \
    "ds_read_b64_tr_b16 v[96:97], v25 offset:1152\n\t" \
    "ds_read_b64_tr_b16 v[98:99], v25 offset:1216\n\t" \
    "s_waitcnt lgkmcnt(4)\n\t" \
    "v_smfmac_f32_16x16x64_bf16 v[124:127], v[92:95], v[248:255], v19\n\t" \
    "s_nop 2\n\t" \
    "v_smfmac_f32_16x16x64_bf16 v[108:111], v[84:87], a[144:151], v19\n\t" \
    "s_nop 2\n\t" \
    "v_smfmac_f32_16x16x64_bf16 v[108:111], v[92:95], a[160:167], v19\n\t" \
    "s_nop 1\n\t" \
    "v_permlane32_swap_b32 v124, v126\n\t" \
    "v_smfmac_f32_16x16x64_bf16 v[112:115], v[84:87], a[176:183], v19\n\t" \
    "s_nop 1\n\t" \
    "v_permlane32_swap_b32 v125, v127\n\t" \
    "v_smfmac_f32_16x16x64_bf16 v[112:115], v[92:95], a[192:199], v19\n\t" \
    "s_nop 0\n\t" \
    "v_add_f32 v124, v124, v126\n\t" \
    "s_waitcnt lgkmcnt(2)\n\t" \
    "v_smfmac_f32_16x16x64_bf16 v[108:111], v[88:91], a[152:159], v19\n\t" \
    "s_nop 1\n\t" \
    "v_add_f32 v125, v125, v127\n\t" \
    "v_smfmac_f32_16x16x64_bf16 v[112:115], v[88:91], a[184:191], v19\n\t" \
    "s_nop 0\n\t" \
    "v_cvt_pk_bf16_f32 v136, v124, v125\n\t" \
    "s_waitcnt lgkmcnt(0)\n\t" \
    "v_smfmac_f32_16x16x64_bf16 v[108:111], v[96:99], a[168:175], v19\n\t" \
    "s_nop 1\n\t" \
    "v_cvt_pk_bf16_f32 v137, v124, v125\n\t" \
    "v_smfmac_f32_16x16x64_bf16 v[112:115], v[96:99], a[200:207], v19\n\t" \
    "s_nop 1\n\t" \
    "v_permlane32_swap_b32 v136, v137\n\t" \
    "v_smfmac_f32_16x16x64_bf16 v[116:119], v[84:87], a[208:215], v19\n\t" \
    "s_mov_b32 exec_hi, 0\n\t" \
    "global_store_dwordx2 v16, v[136:137], s[38:39] sc1\n\t" \
    "s_mov_b32 exec_hi, -1\n\t" \
    "v_cvt_pk_bf16_f32 v128, v108, v109\n\t" \
    "v_smfmac_f32_16x16x64_bf16 v[116:119], v[88:91], a[216:223], v19\n\t" \
    "s_nop 1\n\t" \
    "v_cvt_pk_bf16_f32 v129, v110, v111\n\t" \
    "v_smfmac_f32_16x16x64_bf16 v[116:119], v[92:95], a[224:231], v19\n\t" \
    "s_nop 1\n\t" \
    "v_cvt_pk_bf16_f32 v130, v112, v113\n\t" \
    "v_smfmac_f32_16x16x64_bf16 v[116:119], v[96:99], a[232:239], v19\n\t" \
    "s_nop 1\n\t" \
    "v_cvt_pk_bf16_f32 v131, v114, v115\n\t" \
    "v_smfmac_f32_16x16x64_bf16 v[120:123], v[84:87], a[240:247], v19\n\t" \
    "s_nop 1\n\t" \
    "v_permlane16_swap_b32 v128, v130\n\t" \
    "v_smfmac_f32_16x16x64_bf16 v[120:123], v[88:91], a[248:255], v19\n\t" \
    "s_nop 1\n\t" \
    "v_permlane16_swap_b32 v129, v131\n\t" \
    "v_smfmac_f32_16x16x64_bf16 v[120:123], v[92:95], v[224:231], v19\n\t" \
    "s_nop 0\n\t" \
    "global_store_dwordx4 v14, v[128:131], s[38:39] sc1\n\t" \
    "v_cvt_pk_bf16_f32 v132, v116, v117\n\t" \
    "v_smfmac_f32_16x16x64_bf16 v[120:123], v[96:99], v[232:239], v19\n\t" \
    "s_nop 1\n\t" \
    "v_cvt_pk_bf16_f32 v133, v118, v119\n\t" \
    "s_nop 4\n\t" \
    "v_cvt_pk_bf16_f32 v134, v120, v121\n\t" \
    "v_cvt_pk_bf16_f32 v135, v122, v123\n\t" \
    "s_nop 0\n\t" \
    "v_permlane16_swap_b32 v132, v134\n\t" \
    "v_permlane16_swap_b32 v133, v135\n\t" \
    "global_store_dwordx4 v15, v[132:135], s[38:39] sc1\n\t" \
    "global_load_dword v140, v20, s[52:53] sc1\n\t" \
    "s_cmp_eq_u32 s65, 63\n\t" \
    "s_cbranch_scc1 vv_end56_%=\n\t" \
    "s_cmp_lg_u32 s65, 0\n\t" \
    "s_cbranch_scc1 vv_ng66_%=\n\t" \
    "v_mov_b32 v160, 5769216\n\t" \
    "v_mov_b32 v161, 1\n\t" \
    "s_mov_b64 exec, 1\n\t" \
    "global_atomic_add v162, v160, v161, s[36:37] sc0 sc1\n\t" \
    "s_mov_b64 exec, -1\n\t" \
    "v_mbcnt_lo_u32_b32 v160, -1, 0\n\t" \
    "v_and_b32 v161, 3, v160\n\t" \
    "v_lshlrev_b32 v161, 16, v161\n\t" \
    "v_bfe_u32 v163, v160, 2, 2\n\t" \
    "v_lshl_add_u32 v161, v163, 11, v161\n\t" \
    "v_bfe_u32 v163, v160, 4, 1\n\t" \
    "v_lshl_add_u32 v161, v163, 14, v161\n\t" \
    "v_add_u32 v161, s63, v161\n\t" \
    "s_waitcnt vmcnt(0)\n\t" \
    "v_readfirstlane_b32 s69, v162\n\t" \
    "s_nop 1\n\t" \
    "s_cmp_lg_u32 s69, 126\n\t" \
    "s_cbranch_scc1 vv_go67_%=\n\t" \
    "s_memrealtime s[56:57]\n\t" \
    "s_waitcnt lgkmcnt(0)\n\t" \
    "s_lshl_b32 s69, s56, 4\n\t" \
    "s_add_u32 s69, s69, 2960\n\t" \
    "s_add_u32 s70, s56, 24\n\t" \
    "s_lshr_b32 s70, s70, 4\n\t" \
    "v_lshrrev_b32 v163, 2, v160\n\t" \
    "v_add_u32 v163, s70, v163\n\t" \
    "v_and_b32 v163, 63, v163\n\t" \
    "v_lshlrev_b32 v163, 6, v163\n\t" \
    "v_and_b32 v162, 3, v160\n\t" \
    "v_lshl_add_u32 v163, v162, 12, v163\n\t" \
    "v_add_u32 v163, 5783552, v163\n\t" \
    "v_mov_b32 v160, s69\n\t" \
    "s_mov_b32 exec_hi, 0\n\t" \
    "global_store_dword v163, v160, s[36:37] sc1\n\t" \
    "global_store_dword v161, v160, s[36:37] sc1\n\t" \
    "s_mov_b32 exec_hi, -1\n\t" \
    "vv_go67_%=:\n\t" \
    "vv_ng66_%=:\n\t" \
    "s_waitcnt lgkmcnt(0)\n\t" \
    "s_mov_b32 s70, 116\n\t" \
    "vv_E71_%=:\n\t" \
    "s_memtime s[56:57]\n\t" \
    "s_memrealtime s[2:3]\n\t" \
    "s_nop 0\n\t" \
    "s_memrealtime s[4:5]\n\t" \
    "s_nop 0\n\t" \
    "s_memrealtime s[6:7]\n\t" \
    "s_nop 0\n\t" \
    "s_memrealtime s[8:9]\n\t" \
    "s_nop 0\n\t" \
    "s_memrealtime s[10:11]\n\t" \
    "s_nop 0\n\t" \
    "s_memrealtime s[12:13]\n\t" \
    "s_nop 0\n\t" \
    "s_memrealtime s[14:15]\n\t" \
    "s_nop 0\n\t" \
    "s_memrealtime s[16:17]\n\t" \
    "s_nop 0\n\t" \
    "s_memrealtime s[18:19]\n\t" \
    "s_nop 0\n\t" \
    "s_memrealtime s[20:21]\n\t" \
    "s_nop 0\n\t" \
    "s_memrealtime s[22:23]\n\t" \
    "s_nop 0\n\t" \
    "s_memrealtime s[24:25]\n\t" \
    "s_nop 0\n\t" \
    "s_memrealtime s[26:27]\n\t" \
    "s_nop 0\n\t" \
    "s_memrealtime s[28:29]\n\t" \
    "s_memtime s[54:55]\n\t" \
    "s_add_u32 s70, s70, 8\n\t" \
    "s_waitcnt lgkmcnt(0)\n\t" \
    "s_sub_u32 s69, s54, s56\n\t" \
    "s_cmp_gt_u32 s69, s70\n\t" \
    "s_cbranch_scc1 vv_E71_%=\n\t" \
    "s_add_u32 s2, s2, s4\n\t" \
    "s_add_u32 s6, s6, s8\n\t" \
    "s_add_u32 s10, s10, s12\n\t" \
    "s_add_u32 s14, s14, s16\n\t" \
    "s_add_u32 s18, s18, s20\n\t" \
    "s_add_u32 s22, s22, s24\n\t" \
    "s_add_u32 s2, s2, s6\n\t" \
    "s_add_u32 s10, s10, s14\n\t" \
    "s_add_u32 s18, s18, s22\n\t" \
    "s_add_u32 s2, s2, s26\n\t" \
    "s_add_u32 s10, s10, s18\n\t" \
    "s_add_u32 s2, s2, s10\n\t" \
    "s_mul_i32 s70, s28, 13\n\t" \
    "s_sub_u32 s70, s70, s2\n\t" \
    "s_min_u32 s70, s70, 52\n\t" \
    "s_mul_i32 s69, s69, s70\n\t" \
    "s_mul_hi_u32 s69, s69, 76695844\n\t" \
    "s_mov_b32 s75, s73\n\t" \
    "s_mov_b32 s76, s74\n\t" \
    "s_add_u32 s73, s56, s69\n\t" \
    "s_lshl_b32 s74, s28, 4\n\t" \
    "s_sub_u32 s69, s73, s75\n\t" \
    "s_sub_u32 s70, s74, s76\n\t" \
    "s_sub_u32 s71, s70, 1600\n\t" \
    "s_cmp_lt_u32 s71, 400000\n\t" \
    "s_cbranch_scc0 vv_K72_%=\n\t" \
    "v_cvt_f32_u32 v160, s69\n\t" \
    "v_cvt_f32_u32 v161, s70\n\t" \
    "v_rcp_f32 v162, v160\n\t" \
    "v_rcp_f32 v163, v161\n\t" \
    "v_mul_f32 v162, v162, v161\n\t" \
    "v_mul_f32 v163, v163, v160\n\t" \
    "v_mul_f32 v162, 0x4f800000, v162\n\t" \
    "v_mul_f32 v163, 0x4f000000, v163\n\t" \
    "v_cvt_u32_f32 v162, v162\n\t" \
    "v_cvt_u32_f32 v163, v163\n\t" \
    "v_readfirstlane_b32 s69, v162\n\t" \
    "v_readfirstlane_b32 s70, v163\n\t" \
    "s_nop 1\n\t" \
    "s_min_u32 s71, s65, 1\n\t" \
    "s_sub_u32 s69, s69, s78\n\t" \
    "s_ashr_i32 s69, s69, s71\n\t" \
    "s_add_u32 s78, s78, s69\n\t" \
    "s_sub_u32 s70, s70, s77\n\t" \
    "s_ashr_i32 s70, s70, s71\n\t" \
    "s_add_u32 s77, s77, s70\n\t" \
    "vv_K72_%=:\n\t" \
    "s_add_u32 s65, s65, 1\n\t" \
    "v_mov_b32 v72, 0\n\t" \
    "v_mov_b32 v73, 0\n\t" \
    "v_mov_b32 v74, 0\n\t" \
    "v_mov_b32 v75, 0\n\t" \
    "v_mov_b32 v76, 0\n\t" \
    "v_mov_b32 v77, 0\n\t" \
    "v_mov_b32 v78, 0\n\t" \
    "v_mov_b32 v79, 0\n\t" \
    "v_mov_b32 v108, 0\n\t" \
    "v_mov_b32 v109, 0\n\t" \
    "v_mov_b32 v110, 0\n\t" \
    "v_mov_b32 v111, 0\n\t" \
    "v_mov_b32 v112, 0\n\t" \
    "v_mov_b32 v113, 0\n\t" \
    "v_mov_b32 v114, 0\n\t" \
    "v_mov_b32 v115, 0\n\t" \
    "v_mov_b32 v116, 0\n\t" \
    "v_mov_b32 v117, 0\n\t" \
    "v_mov_b32 v118, 0\n\t" \
    "v_mov_b32 v119, 0\n\t" \
    "v_mov_b32 v120, 0\n\t" \
    "v_mov_b32 v121, 0\n\t" \
    "v_mov_b32 v122, 0\n\t" \
    "v_mov_b32 v123, 0\n\t" \
    "v_mov_b32 v124, 0\n\t" \
    "v_mov_b32 v125, 0\n\t" \
    "v_mov_b32 v126, 0\n\t" \
    "v_mov_b32 v127, 0\n\t" \
    "s_add_u32 s69, s65, 0\n\t" \
    "s_and_b32 s69, s69, 7\n\t" \
    "s_lshl_b32 s69, s69, 18\n\t" \
    "s_add_u32 s69, s69, s62\n\t" \
    "s_add_u32 s46, s36, s69\n\t" \
    "s_addc_u32 s47, s37, 0\n\t" \
    "s_add_u32 s70, s65, 0\n\t" \
    "s_mul_i32 s69, s70, 171\n\t" \
    "s_lshr_b32 s69, s69, 9\n\t" \
    "s_mul_i32 s70, s70, 589824\n\t" \
    "s_mul_i32 s71, s69, 327680\n\t" \
    "s_add_u32 s70, s70, s71\n\t" \
    "s_lshl1_add_u32 s69, s69, 4\n\t" \
    "s_bitcmp1_b64 s[44:45], s69\n\t" \
    "s_cselect_b32 s69, 8192, 0\n\t" \
    "s_add_u32 s69, s69, s70\n\t" \
    "s_add_u32 s69, s69, 123731968\n\t" \
    "s_add_u32 s38, s36, s69\n\t" \
    "s_addc_u32 s39, s37, 0\n\t" \
    "s_and_b32 s69, s69, 0xffe00000\n\t" \
    "s_add_u32 s69, s69, 1823744\n\t" \
    "v_mov_b32 v17, s69\n\t" \
    "global_load_dword v17, v17, s[36:37]\n\t" \
    "s_waitcnt vmcnt(0)\n\t" \
    "v_cmp_eq_u32 vcc, -1, v140\n\t" \
    "s_cbranch_vccz vv_Hok69_%=\n\t" \
    "vv_H68_%=:\n\t" \
    "global_load_dword v140, v20, s[52:53] sc1\n\t" \
    "s_waitcnt vmcnt(0)\n\t" \
    "v_cmp_eq_u32 vcc, -1, v140\n\t" \
    "s_cbranch_vccz vv_Hc70_%=\n\t" \
    "s_cmp_eq_u32 s68, 0\n\t" \
    "s_cbranch_scc1 vv_F73_%=\n\t" \
    "s_sub_u32 s68, s68, 1\n\t" \
    "s_sleep 2\n\t" \
    "s_branch vv_H68_%=\n\t" \
    "vv_F73_%=:\n\t" \
    "v_mov_b32 v141, 5767424\n\t" \
    "v_mov_b32 v142, 6\n\t" \
    "global_store_dword v141, v142, s[36:37] sc1\n\t" \
    "s_waitcnt vmcnt(0)\n\t" \
    "s_branch vv_Hc70_%=\n\t" \
    "vv_Hc70_%=:\n\t" \
    "s_memtime s[56:57]\n\t" \
    "s_waitcnt lgkmcnt(0)\n\t" \
    "s_sub_u32 s69, s56, s73\n\t" \
    "s_cmp_lt_u32 s69, 3000\n\t" \
    "s_cbranch_scc1 vv_St74_%=\n\t" \
    "s_waitcnt lgkmcnt(0)\n\t" \
    "s_mov_b32 s70, 116\n\t" \
    "vv_E75_%=:\n\t" \
    "s_memtime s[56:57]\n\t" \
    "s_memrealtime s[2:3]\n\t" \
    "s_nop 0\n\t" \
    "s_memrealtime s[4:5]\n\t" \
    "s_nop 0\n\t" \
    "s_memrealtime s[6:7]\n\t" \
    "s_nop 0\n\t" \
    "s_memrealtime s[8:9]\n\t" \
    "s_nop 0\n\t" \
    "s_memrealtime s[10:11]\n\t" \
    "s_nop 0\n\t" \
    "s_memrealtime s[12:13]\n\t" \
    "s_nop 0\n\t" \
    "s_memrealtime s[14:15]\n\t" \
    "s_nop 0\n\t" \
    "s_memrealtime s[16:17]\n\t" \
    "s_nop 0\n\t" \
    "s_memrealtime s[18:19]\n\t" \
    "s_nop 0\n\t" \
    "s_memrealtime s[20:21]\n\t" \
    "s_nop 0\n\t" \
    "s_memrealtime s[22:23]\n\t" \
    "s_nop 0\n\t" \
    "s_memrealtime s[24:25]\n\t" \
    "s_nop 0\n\t" \
    "s_memrealtime s[26:27]\n\t" \
    "s_nop 0\n\t" \
    "s_memrealtime s[28:29]\n\t" \
    "s_memtime s[54:55]\n\t" \
    "s_add_u32 s70, s70, 8\n\t" \
    "s_waitcnt lgkmcnt(0)\n\t" \
    "s_sub_u32 s69, s54, s56\n\t" \
    "s_cmp_gt_u32 s69, s70\n\t" \
    "s_cbranch_scc1 vv_E75_%=\n\t" \
    "s_add_u32 s2, s2, s4\n\t" \
    "s_add_u32 s6, s6, s8\n\t" \
    "s_add_u32 s10, s10, s12\n\t" \
    "s_add_u32 s14, s14, s16\n\t" \
    "s_add_u32 s18, s18, s20\n\t" \
    "s_add_u32 s22, s22, s24\n\t" \
    "s_add_u32 s2, s2, s6\n\t" \
    "s_add_u32 s10, s10, s14\n\t" \
    "s_add_u32 s18, s18, s22\n\t" \
    "s_add_u32 s2, s2, s26\n\t" \
    "s_add_u32 s10, s10, s18\n\t" \
    "s_add_u32 s2, s2, s10\n\t" \
    "s_mul_i32 s70, s28, 13\n\t" \
    "s_sub_u32 s70, s70, s2\n\t" \
    "s_min_u32 s70, s70, 52\n\t" \
    "s_mul_i32 s69, s69, s70\n\t" \
    "s_mul_hi_u32 s69, s69, 76695844\n\t" \
    "s_mov_b32 s75, s73\n\t" \
    "s_mov_b32 s76, s74\n\t" \
    "s_add_u32 s73, s56, s69\n\t" \
    "s_lshl_b32 s74, s28, 4\n\t" \
    "vv_St74_%=:\n\t" \
    "vv_Hok69_%=:\n\t" \
    "v_max_u32_dpp v140, v140, v140 row_ror:1 row_mask:0xf bank_mask:0xf\n\t" \
    "s_nop 1\n\t" \
    "v_max_u32_dpp v140, v140, v140 row_ror:2 row_mask:0xf bank_mask:0xf\n\t" \
    "s_nop 1\n\t" \
    "v_max_u32_dpp v140, v140, v140 row_ror:4 row_mask:0xf bank_mask:0xf\n\t" \
    "s_nop 1\n\t" \
    "v_max_u32_dpp v140, v140, v140 row_ror:8 row_mask:0xf bank_mask:0xf\n\t" \
    "s_nop 0\n\t" \
    "v_readlane_b32 s69, v140, 0\n\t" \
    "v_readlane_b32 s71, v140, 16\n\t" \
    "s_nop 1\n\t" \
    "s_max_u32 s81, s69, s71\n\t" \
    "s_mul_hi_u32 s69, s85, s78\n\t" \
    "s_add_u32 s79, s81, s69\n\t" \
    "s_add_u32 s79, s79, s87\n\t" \
    "s_cmp_ge_u32 s65, 53\n\t" \
    "s_cselect_b32 s69, 32, 0\n\t" \
    "s_sub_u32 s79, s79, s69\n\t" \
    "s_cmp_lt_u32 s65, 6\n\t" \
    "s_cselect_b32 s69, 48, 0\n\t" \
    "s_add_u32 s79, s79, s69\n\t" \
    "s_add_u32 s80, s79, s88\n\t" \
    "s_sub_u32 s70, s79, s74\n\t" \
    "s_max_i32 s70, s70, 0\n\t" \
    "s_min_i32 s70, s70, 16000\n\t" \
    "s_lshl_b32 s70, s70, 1\n\t" \
    "s_mul_hi_u32 s70, s70, s77\n\t" \
    "s_add_u32 s70, s70, s73\n\t" \
    "v_mov_b32 v160, s70\n\t" \
    "ds_write_b32 v11, v160 offset:20480\n\t" \
    "s_and_b32 s69, s65, 7\n\t" \
    "s_lshl_b32 s69, s69, 18\n\t" \
    "s_add_u32 s69, s69, s63\n\t" \
    "s_add_u32 s52, s36, s69\n\t" \
    "s_addc_u32 s53, s37, 0\n\t" \
    "s_waitcnt lgkmcnt(0)\n\t" \
    "s_barrier\n\t" \
    "s_branch vv_loop12_%=\n\t" \
    "vv_Gbad61_%=:\n\t" \
    "s_waitcnt vmcnt(0) lgkmcnt(0)\n\t" \
    "s_nop 7\n\t" \
    "v_mov_b32 v72, 0\n\t" \
    "v_mov_b32 v73, 0\n\t" \
    "v_mov_b32 v74, 0\n\t" \
    "v_mov_b32 v75, 0\n\t" \
    "v_mov_b32 v76, 0\n\t" \
    "v_mov_b32 v77, 0\n\t" \
    "v_mov_b32 v78, 0\n\t" \
    "v_mov_b32 v79, 0\n\t" \
    "s_cmp_eq_u32 s68, 0\n\t" \
    "s_cbranch_scc1 vv_F76_%=\n\t" \
    "s_sub_u32 s68, s68, 1\n\t" \
    "s_branch vv_G60_%=\n\t" \
    "vv_F76_%=:\n\t" \
    "v_mov_b32 v140, 5767424\n\t" \
    "v_mov_b32 v141, 1\n\t" \
    "global_store_dword v140, v141, s[36:37] sc1\n\t" \
    "s_waitcnt vmcnt(0)\n\t" \
    "s_branch vv_Gc62_%=\n\t" \
    "vv_Gslow63_%=:\n\t" \
    "v_max_u32 v140, v36, v37\n\t" \
    "v_max3_u32 v140, v140, v38, v39\n\t" \
    "v_max3_u32 v140, v140, v40, v41\n\t" \
    "v_max3_u32 v140, v140, v42, v43\n\t" \
    "v_max3_u32 v140, v140, v44, v45\n\t" \
    "v_max3_u32 v140, v140, v46, v47\n\t" \
    "v_max3_u32 v140, v140, v48, v49\n\t" \
    "v_max3_u32 v140, v140, v50, v51\n\t" \
    "v_max3_u32 v140, v140, v52, v53\n\t" \
    "v_max3_u32 v140, v140, v54, v55\n\t" \
    "v_max3_u32 v140, v140, v56, v57\n\t" \
    "v_max3_u32 v140, v140, v58, v59\n\t" \
    "v_max3_u32 v140, v140, v60, v61\n\t" \
    "v_max3_u32 v140, v140, v62, v63\n\t" \
    "v_max3_u32 v140, v140, v64, v65\n\t" \
    "v_max3_u32 v140, v140, v66, v67\n\t" \
    "v_max3_u32 v140, v140, v68, v69\n\t" \
    "v_max3_u32 v140, v140, v70, v71\n\t" \
    "v_cmp_eq_u32 vcc, -1, v140\n\t" \
    "s_cbranch_vccz vv_Gback64_%=\n\t" \
    "s_waitcnt lgkmcnt(0)\n\t" \
    "v_sub_u32 v140, v1, v3\n\t" \
    "s_nop 0\n\t" \
    "v_readfirstlane_b32 s69, v140\n\t" \
    "v_add_u32 v143, 20480, v0\n\t" \
    "s_nop 7\n\t" \
    "s_add_u32 s30, s46, s69\n\t" \
    "s_addc_u32 s31, s47, 0\n\t" \
    "vv_Gag77_%=:\n\t" \
    "global_load_dwordx4 v[36:39], v1, s[46:47] sc1\n\t" \
    "global_load_dwordx4 v[40:43], v23, s[46:47] sc1\n\t" \
    "global_load_dwordx4 v[44:47], v143, s[30:31] sc1\n\t" \
    "global_load_dwordx4 v[48:51], v4, s[30:31] sc1\n\t" \
    "global_load_dwordx4 v[52:55], v5, s[30:31] sc1\n\t" \
    "global_load_dwordx4 v[56:59], v6, s[30:31] sc1\n\t" \
    "global_load_dwordx4 v[60:63], v7, s[30:31] sc1\n\t" \
    "global_load_dwordx4 v[64:67], v8, s[30:31] sc1\n\t" \
    "global_load_dwordx4 v[68:71], v9, s[30:31] sc1\n\t" \
    "s_waitcnt vmcnt(0)\n\t" \
    "v_max_u32 v140, v36, v37\n\t" \
    "v_max3_u32 v140, v140, v38, v39\n\t" \
    "v_max3_u32 v140, v140, v40, v41\n\t" \
    "v_max3_u32 v140, v140, v42, v43\n\t" \
    "v_max3_u32 v140, v140, v44, v45\n\t" \
    "v_max3_u32 v140, v140, v46, v47\n\t" \
    "v_max3_u32 v140, v140, v48, v49\n\t" \
    "v_max3_u32 v140, v140, v50, v51\n\t" \
    "v_max3_u32 v140, v140, v52, v53\n\t" \
    "v_max3_u32 v140, v140, v54, v55\n\t" \
    "v_max3_u32 v140, v140, v56, v57\n\t" \
    "v_max3_u32 v140, v140, v58, v59\n\t" \
    "v_max3_u32 v140, v140, v60, v61\n\t" \
    "v_max3_u32 v140, v140, v62, v63\n\t" \
    "v_max3_u32 v140, v140, v64, v65\n\t" \
    "v_max3_u32 v140, v140, v66, v67\n\t" \
    "v_max3_u32 v140, v140, v68, v69\n\t" \
    "v_max3_u32 v140, v140, v70, v71\n\t" \
    "v_cmp_eq_u32 vcc, -1, v140\n\t" \
    "s_cbranch_vccz vv_Gredo78_%=\n\t" \
    "s_cmp_eq_u32 s68, 0\n\t" \
    "s_cbranch_scc1 vv_F79_%=\n\t" \
    "s_sub_u32 s68, s68, 1\n\t" \
    "s_sleep 1\n\t" \
    "s_branch vv_Gag77_%=\n\t" \
    "vv_F79_%=:\n\t" \
    "v_mov_b32 v140, 5767424\n\t" \
    "v_mov_b32 v141, 2\n\t" \
    "global_store_dword v140, v141, s[36:37] sc1\n\t" \
    "s_waitcnt vmcnt(0)\n\t" \
    "s_branch vv_Gredo78_%=\n\t" \
    "vv_Gredo78_%=:\n\t" \
    "v_mov_b32 v72, 0\n\t" \
    "v_mov_b32 v73, 0\n\t" \
    "v_mov_b32 v74, 0\n\t" \
    "v_mov_b32 v75, 0\n\t" \
    "v_mov_b32 v76, 0\n\t" \
    "v_mov_b32 v77, 0\n\t" \
    "v_mov_b32 v78, 0\n\t" \
    "v_mov_b32 v79, 0\n\t" \
    "v_smfmac_f32_16x16x64_bf16 v[72:75], v[36:39], a[48:55], v10\n\t" \
    "v_smfmac_f32_16x16x64_bf16 v[72:75], v[40:43], a[56:63], v10\n\t" \
    "v_smfmac_f32_16x16x64_bf16 v[72:75], v[48:51], a[0:7], v10\n\t" \
    "v_smfmac_f32_16x16x64_bf16 v[72:75], v[52:55], a[8:15], v10\n\t" \
    "v_smfmac_f32_16x16x64_bf16 v[72:75], v[56:59], a[16:23], v10\n\t" \
    "v_smfmac_f32_16x16x64_bf16 v[72:75], v[60:63], a[24:31], v10\n\t" \
    "v_smfmac_f32_16x16x64_bf16 v[72:75], v[64:67], a[32:39], v10\n\t" \
    "v_smfmac_f32_16x16x64_bf16 v[72:75], v[68:71], a[40:47], v10\n\t" \
    "v_smfmac_f32_16x16x64_bf16 v[72:75], v[44:47], a[64:71], v10\n\t" \
    "v_smfmac_f32_16x16x64_bf16 v[76:79], v[36:39], a[120:127], v10\n\t" \
    "v_smfmac_f32_16x16x64_bf16 v[76:79], v[40:43], a[128:135], v10\n\t" \
    "v_smfmac_f32_16x16x64_bf16 v[76:79], v[48:51], a[72:79], v10\n\t" \
    "v_smfmac_f32_16x16x64_bf16 v[76:79], v[52:55], a[80:87], v10\n\t" \
    "v_smfmac_f32_16x16x64_bf16 v[76:79], v[56:59], a[88:95], v10\n\t" \
    "v_smfmac_f32_16x16x64_bf16 v[76:79], v[60:63], a[96:103], v10\n\t" \
    "v_smfmac_f32_16x16x64_bf16 v[76:79], v[64:67], a[104:111], v10\n\t" \
    "v_smfmac_f32_16x16x64_bf16 v[76:79], v[68:71], a[112:119], v10\n\t" \
    "v_smfmac_f32_16x16x64_bf16 v[76:79], v[44:47], a[136:143], v10\n\t" \
    "v_add_f32 v72, v72, v74\n\t" \
    "v_add_f32 v73, v73, v75\n\t" \
    "v_cvt_pk_bf16_f32 v80, v72, v73\n\t" \
    "ds_write_b32 v12, v80 offset:0\n\t" \
    "s_nop 3\n\t" \
    "v_pk_add_f32 v[76:77], v[76:77], v[78:79]\n\t" \
    "v_cvt_pk_bf16_f32 v81, v76, v77\n\t" \
    "ds_write_b32 v12, v81 offset:256\n\t" \
    "s_branch vv_Gback64_%=\n\t" \
    "vv_end56_%=:\n\t" \
    "s_endpgm\n\t" \
    "vv_last_%=:\n\t" \
    ""
#define ASM_R \
    "s_mov_b64 s[36:37], %0\n\t" \
    "s_mov_b64 s[42:43], %1\n\t" \
    "s_mov_b64 s[44:45], %2\n\t" \
    "s_mov_b32 s66, %3\n\t" \
    "s_mov_b32 s67, %4\n\t" \
    "s_mov_b64 s[50:51], %5\n\t" \
    "s_mov_b32 s82, %7\n\t" \
    "s_load_dwordx8 s[84:91], %6, 0\n\t" \
    "v_mbcnt_lo_u32_b32 v223, -1, 0\n\t" \
    "v_mbcnt_hi_u32_b32 v223, -1, v223\n\t" \
    "s_getpc_b64 s[46:47]\n\t" \
    "rr_pc_%=:\n\t" \
    "s_lshl_b32 s69, s66, 13\n\t" \
    "v_lshl_add_u32 v222, v223, 7, s69\n\t" \
    "v_min_u32 v222, rr_last_%=-rr_pc_%=-4, v222\n\t" \
    "global_load_dword v222, v222, s[46:47]\n\t" \
    "v_lshlrev_b32 v223, 2, v223\n\t" \
    "v_add_u32 v223, s82, v223\n\t" \
    "ds_read_b32 v0, v223 offset:0\n\t" \
    "ds_read_b32 v1, v223 offset:256\n\t" \
    "ds_read_b32 v2, v223 offset:512\n\t" \
    "ds_read_b32 v3, v223 offset:768\n\t" \
    "ds_read_b32 v4, v223 offset:1024\n\t" \
    "ds_read_b32 v5, v223 offset:1280\n\t" \
    "ds_read_b32 v6, v223 offset:1536\n\t" \
    "ds_read_b32 v7, v223 offset:1792\n\t" \
    "ds_read_b32 v8, v223 offset:2048\n\t" \
    "ds_read_b32 v9, v223 offset:2304\n\t" \
    "ds_read_b32 v10, v223 offset:2560\n\t" \
    "ds_read_b32 v11, v223 offset:2816\n\t" \
    "s_waitcnt lgkmcnt(0)\n\t" \
    "ds_read_b32 v12, v223 offset:3072\n\t" \
    "ds_read_b32 v13, v223 offset:3328\n\t" \
    "ds_read_b32 v14, v223 offset:3584\n\t" \
    "ds_read_b32 v15, v223 offset:3840\n\t" \
    "ds_read_b32 v16, v223 offset:4096\n\t" \
    "ds_read_b32 v17, v223 offset:4352\n\t" \
    "ds_read_b32 v18, v223 offset:4608\n\t" \
    "ds_read_b32 v19, v223 offset:4864\n\t" \
    "ds_read_b32 v20, v223 offset:5120\n\t" \
    "ds_read_b32 v21, v223 offset:5376\n\t" \
    "ds_read_b32 v22, v223 offset:5632\n\t" \
    "ds_read_b32 v23, v223 offset:5888\n\t" \
    "s_waitcnt lgkmcnt(0)\n\t" \
    "ds_read_b32 v24, v223 offset:6144\n\t" \
    "ds_read_b32 v25, v223 offset:6400\n\t" \
    "ds_read_b32 v26, v223 offset:6656\n\t" \
    "ds_read_b32 v27, v223 offset:6912\n\t" \
    "ds_read_b32 v28, v223 offset:7168\n\t" \
    "ds_read_b32 v29, v223 offset:7424\n\t" \
    "ds_read_b32 v30, v223 offset:7680\n\t" \
    "ds_read_b32 v31, v223 offset:7936\n\t" \
    "ds_read_b32 v32, v223 offset:8192\n\t" \
    "ds_read_b32 v33, v223 offset:8448\n\t" \
    "ds_read_b32 v34, v223 offset:8704\n\t" \
    "ds_read_b32 v35, v223 offset:8960\n\t" \
    "s_waitcnt lgkmcnt(0)\n\t" \
    "ds_read_b32 v36, v223 offset:9216\n\t" \
    "ds_read_b32 v37, v223 offset:9472\n\t" \
    "ds_read_b32 v38, v223 offset:9728\n\t" \
    "ds_read_b32 v39, v223 offset:9984\n\t" \
    "ds_read_b32 v40, v223 offset:10240\n\t" \
    "ds_read_b32 v41, v223 offset:10496\n\t" \
    "ds_read_b32 v42, v223 offset:10752\n\t" \
    "ds_read_b32 v43, v223 offset:11008\n\t" \
    "ds_read_b32 v44, v223 offset:11264\n\t" \
    "ds_read_b32 v45, v223 offset:11520\n\t" \
    "ds_read_b32 v183, v223 offset:11776\n\t" \
    "ds_read_b32 v184, v223 offset:12032\n\t" \
    "s_waitcnt lgkmcnt(0)\n\t" \
    "ds_read_b32 v185, v223 offset:12288\n\t" \
    "ds_read_b32 v186, v223 offset:12544\n\t" \
    "ds_read_b32 v187, v223 offset:12800\n\t" \
    "ds_read_b32 v188, v223 offset:13056\n\t" \
    "s_waitcnt lgkmcnt(0)\n\t" \
    "s_mov_b32 s68, 262144\n\t" \
    "s_mov_b32 s72, 0\n\t" \
    "s_mov_b32 s83, -1\n\t" \
    "s_mov_b32 s65, 0\n\t" \
    "v_xor_b32 v180, 65536, v2\n\t" \
    "v_xor_b32 v181, 131072, v2\n\t" \
    "v_xor_b32 v182, 196608, v2\n\t" \
    "v_xor_b32 v204, 0x204000, v180\n\t" \
    "v_xor_b32 v205, 0x204000, v181\n\t" \
    "v_xor_b32 v206, 0x204000, v182\n\t" \
    "v_xor_b32 v207, 0x204000, v2\n\t" \
    "v_add_u32 v189, 4096, v7\n\t" \
    "v_add_u32 v190, 16384, v7\n\t" \
    "v_add_u32 v191, 20480, v7\n\t" \
    "s_mul_i32 s69, s67, 57\n\t" \
    "s_lshr_b32 s69, s69, 9\n\t" \
    "s_mul_i32 s69, s69, 9\n\t" \
    "s_sub_u32 s69, s67, s69\n\t" \
    "s_cmp_lt_u32 s69, 4\n\t" \
    "s_cselect_b32 s70, 21, 16\n\t" \
    "s_cmp_eq_u32 s69, 8\n\t" \
    "s_cselect_b32 s70, 26, s70\n\t" \
    "s_lshr_b32 s97, s91, s70\n\t" \
    "s_and_b32 s97, s97, 31\n\t" \
    "s_min_u32 s97, s97, 0\n\t" \
    "s_lshl_b32 s97, s97, 4\n\t" \
    "s_mov_b32 s77, 3019898880\n\t" \
    "s_mov_b32 s78, 3054198966\n\t" \
    "s_waitcnt lgkmcnt(0)\n\t" \
    "s_mov_b32 s70, 116\n\t" \
    "rr_E1_%=:\n\t" \
    "s_memtime s[56:57]\n\t" \
    "s_memrealtime s[2:3]\n\t" \
    "s_nop 0\n\t" \
    "s_memrealtime s[4:5]\n\t" \
    "s_nop 0\n\t" \
    "s_memrealtime s[6:7]\n\t" \
    "s_nop 0\n\t" \
    "s_memrealtime s[8:9]\n\t" \
    "s_nop 0\n\t" \
    "s_memrealtime s[10:11]\n\t" \
    "s_nop 0\n\t" \
    "s_memrealtime s[12:13]\n\t" \
    "s_nop 0\n\t" \
    "s_memrealtime s[14:15]\n\t" \
    "s_nop 0\n\t" \
    "s_memrealtime s[16:17]\n\t" \
    "s_nop 0\n\t" \
    "s_memrealtime s[18:19]\n\t" \
    "s_nop 0\n\t" \
    "s_memrealtime s[20:21]\n\t" \
    "s_nop 0\n\t" \
    "s_memrealtime s[22:23]\n\t" \
    "s_nop 0\n\t" \
    "s_memrealtime s[24:25]\n\t" \
    "s_nop 0\n\t" \
    "s_memrealtime s[26:27]\n\t" \
    "s_nop 0\n\t" \
    "s_memrealtime s[28:29]\n\t" \
    "s_memtime s[54:55]\n\t" \
    "s_add_u32 s70, s70, 8\n\t" \
    "s_waitcnt lgkmcnt(0)\n\t" \
    "s_sub_u32 s69, s54, s56\n\t" \
    "s_cmp_gt_u32 s69, s70\n\t" \
    "s_cbranch_scc1 rr_E1_%=\n\t" \
    "s_add_u32 s2, s2, s4\n\t" \
    "s_add_u32 s6, s6, s8\n\t" \
    "s_add_u32 s10, s10, s12\n\t" \
    "s_add_u32 s14, s14, s16\n\t" \
    "s_add_u32 s18, s18, s20\n\t" \
    "s_add_u32 s22, s22, s24\n\t" \
    "s_add_u32 s2, s2, s6\n\t" \
    "s_add_u32 s10, s10, s14\n\t" \
    "s_add_u32 s18, s18, s22\n\t" \
    "s_add_u32 s2, s2, s26\n\t" \
    "s_add_u32 s10, s10, s18\n\t" \
    "s_add_u32 s2, s2, s10\n\t" \
    "s_mul_i32 s70, s28, 13\n\t" \
    "s_sub_u32 s70, s70, s2\n\t" \
    "s_min_u32 s70, s70, 52\n\t" \
    "s_mul_i32 s69, s69, s70\n\t" \
    "s_mul_hi_u32 s69, s69, 76695844\n\t" \
    "s_mov_b32 s75, s73\n\t" \
    "s_mov_b32 s76, s74\n\t" \
    "s_add_u32 s73, s56, s69\n\t" \
    "s_lshl_b32 s74, s28, 4\n\t" \
    "v_mov_b32 v172, 0xd800\n\t" \
    "v_mov_b32 v173, 0x20d800\n\t" \
    "global_load_dword v172, v172, s[36:37]\n\t" \
    "global_load_dword v173, v173, s[36:37]\n\t" \
    "s_mov_b32 s62, 0\n\t" \
    "s_mov_b32 s63, 0x200000\n\t" \
    "s_mov_b64 s[44:45], 0\n\t" \
    "v_mov_b32 v170, 0\n\t" \
    "s_mov_b32 s65, s66\n\t" \
    "s_lshl_b32 s58, s67, 14\n\t" \
    "s_sub_u32 s80, s74, 32000\n\t" \
    "s_mov_b32 s64, 1\n\t" \
    "s_mov_b32 s65, 5\n\t" \
    "s_mov_b32 s83, 99\n\t" \
    "s_add_u32 s70, s65, 59\n\t" \
    "s_mul_i32 s69, s70, 171\n\t" \
    "s_lshr_b32 s69, s69, 9\n\t" \
    "s_mul_i32 s70, s70, 589824\n\t" \
    "s_mul_i32 s71, s69, 327680\n\t" \
    "s_add_u32 s70, s70, s71\n\t" \
    "s_lshl1_add_u32 s69, s69, 4\n\t" \
    "s_bitcmp1_b64 s[44:45], s69\n\t" \
    "s_cselect_b32 s69, 8192, 0\n\t" \
    "s_add_u32 s69, s69, s70\n\t" \
    "s_add_u32 s69, s69, 123731968\n\t" \
    "s_add_u32 s69, s69, s58\n\t" \
    "s_add_u32 s40, s36, s69\n\t" \
    "s_addc_u32 s41, s37, 0\n\t" \
    "v_mov_b32 v192, 0\n\t" \
    "s_mov_b64 exec, 1\n\t" \
    "s_branch rr_act3_%=\n\t" \
    "rr_g214_%=:\n\t" \
    "s_mov_b64 exec, -1\n\t" \
    "s_waitcnt vmcnt(0)\n\t" \
    "v_mbcnt_lo_u32_b32 v164, -1, 0\n\t" \
    "v_mbcnt_hi_u32_b32 v164, -1, v164\n\t" \
    "v_lshlrev_b32 v164, 6, v164\n\t" \
    "v_add_u32 v164, 6225920, v164\n\t" \
    "v_mov_b32 v165, 6234112\n\t" \
    "global_load_dword v165, v165, s[36:37] sc1\n\t" \
    "global_load_dwordx2 v[166:167], v164, s[36:37] sc1\n\t" \
    "global_load_dword v168, v164, s[36:37] offset:2048 sc1\n\t" \
    "s_waitcnt vmcnt(0)\n\t" \
    "rr_C15_%=:\n\t" \
    "v_cmp_ne_u32 s[54:55], v166, v168\n\t" \
    "v_sub_u32 v166, v166, v165\n\t" \
    "v_cmp_ne_u32 vcc, 1, v166\n\t" \
    "s_nop 1\n\t" \
    "s_or_b64 vcc, vcc, s[54:55]\n\t" \
    "s_cbranch_scc0 rr_Cok16_%=\n\t" \
    "s_cmp_eq_u32 s68, 0\n\t" \
    "s_cbranch_scc1 rr_F18_%=\n\t" \
    "s_sub_u32 s68, s68, 1\n\t" \
    "s_sleep 6\n\t" \
    "s_branch rr_Cag17_%=\n\t" \
    "rr_F18_%=:\n\t" \
    "v_mov_b32 v168, 5767424\n\t" \
    "v_mov_b32 v169, 4\n\t" \
    "global_store_dword v168, v169, s[36:37] sc1\n\t" \
    "s_waitcnt vmcnt(0)\n\t" \
    "s_branch rr_Cok16_%=\n\t" \
    "rr_Cag17_%=:\n\t" \
    "global_load_dwordx2 v[166:167], v164, s[36:37] sc1\n\t" \
    "global_load_dword v168, v164, s[36:37] offset:2048 sc1\n\t" \
    "s_waitcnt vmcnt(0)\n\t" \
    "s_branch rr_C15_%=\n\t" \
    "rr_Cok16_%=:\n\t" \
    "v_readlane_b32 s69, v167, 2\n\t" \
    "v_readlane_b32 s70, v167, 3\n\t" \
    "s_nop 1\n\t" \
    "s_cmp_lt_u32 s70, s69\n\t" \
    "s_cselect_b32 s69, 0, 8192\n\t" \
    "s_add_u32 s100, s36, s69\n\t" \
    "s_addc_u32 s101, s37, 0\n\t" \
    "s_cmp_lg_u32 s66, 0\n\t" \
    "s_cbranch_scc1 rr_Ca19_%=\n\t" \
    "v_mov_b32 v164, 5768192\n\t" \
    "s_mov_b64 exec, 1\n\t" \
    "global_atomic_add v164, v166, s[36:37] sc1\n\t" \
    "s_mov_b64 exec, -1\n\t" \
    "rr_Ca19_%=:\n\t" \
    "v_mov_b32_dpp v164, v167 quad_perm:[1,0,3,2] row_mask:0xf bank_mask:0xf\n\t" \
    "v_cmp_lt_u32 s[44:45], v164, v167\n\t" \
    "s_nop 1\n\t" \
    "s_bitcmp1_b32 s44, 0\n\t" \
    "s_cselect_b32 s62, 8192, 0\n\t" \
    "s_bitcmp1_b32 s44, 2\n\t" \
    "s_cselect_b32 s63, 8192, 0\n\t" \
    "s_or_b32 s63, s63, 0x200000\n\t" \
    "v_min_u32 v164, v164, v167\n\t" \
    "v_cmp_lt_u32 vcc, 660, v164\n\t" \
    "s_nop 1\n\t" \
    "s_bcnt1_i32_b64 s69, vcc\n\t" \
    "s_cmp_gt_u32 s69, 16\n\t" \
    "s_cselect_b32 s69, 1, 0\n\t" \
    "s_mul_i32 s70, s69, 160\n\t" \
    "s_add_u32 s86, s86, s70\n\t" \
    "s_mul_i32 s70, s69, 536\n\t" \
    "s_add_u32 s87, s87, s70\n\t" \
    "s_mul_i32 s70, s69, 256\n\t" \
    "s_add_u32 s88, s88, s70\n\t" \
    "v_mov_b32 v172, -1\n\t" \
    "v_mov_b32 v173, -1\n\t" \
    "v_mov_b32 v174, -1\n\t" \
    "v_mov_b32 v175, -1\n\t" \
    "v_mov_b32 v176, -1\n\t" \
    "s_sub_u32 s69, s63, s62\n\t" \
    "s_sub_u32 s69, s69, 0x200000\n\t" \
    "v_bfe_u32 v164, v186, 21, 1\n\t" \
    "v_mad_i32_i24 v186, v164, s69, v186\n\t" \
    "v_bfe_u32 v164, v187, 21, 1\n\t" \
    "v_mad_i32_i24 v187, v164, s69, v187\n\t" \
    "v_bfe_u32 v164, v188, 21, 1\n\t" \
    "v_mad_i32_i24 v188, v164, s69, v188\n\t" \
    "v_add_u32 v204, s69, v204\n\t" \
    "v_add_u32 v205, s69, v205\n\t" \
    "v_add_u32 v206, s69, v206\n\t" \
    "v_add_u32 v207, s69, v207\n\t" \
    "s_mov_b32 s64, 0\n\t" \
    "s_mov_b32 s65, s66\n\t" \
    "s_mov_b32 s83, 1\n\t" \
    "s_mov_b32 s77, 3154116608\n\t" \
    "rr_pf9_%=:\n\t" \
    "s_cmp_gt_u32 s65, 8\n\t" \
    "s_cselect_b32 s83, 99, s83\n\t" \
    "v_cmp_gt_i32 vcc, s65, v176\n\t" \
    "s_nop 1\n\t" \
    "s_max_i32 s69, s83, s65\n\t" \
    "s_cmp_eq_u64 vcc, 0\n\t" \
    "s_cselect_b32 s83, s69, s83\n\t" \
    "s_lshl_b32 s69, s65, 11\n\t" \
    "s_add_u32 s69, s69, 5795840\n\t" \
    "v_and_b32 v169, 0x7c0, v7\n\t" \
    "v_add_u32 v169, s69, v169\n\t" \
    "s_cmp_lg_u32 s65, 2\n\t" \
    "s_cbranch_scc1 rr_n2s20_%=\n\t" \
    "s_sleep 10\n\t" \
    "rr_n2s20_%=:\n\t" \
    "rr_WF21_%=:\n\t" \
    "s_cmp_ge_i32 s83, s65\n\t" \
    "s_cbranch_scc1 rr_WFok22_%=\n\t" \
    "global_load_dword v164, v169, s[36:37] sc1\n\t" \
    "s_waitcnt vmcnt(0)\n\t" \
    "v_min_i32_dpp v164, v164, v164 row_ror:1 row_mask:0xf bank_mask:0xf\n\t" \
    "s_nop 1\n\t" \
    "v_min_i32_dpp v164, v164, v164 row_ror:2 row_mask:0xf bank_mask:0xf\n\t" \
    "s_nop 1\n\t" \
    "v_min_i32_dpp v164, v164, v164 row_ror:4 row_mask:0xf bank_mask:0xf\n\t" \
    "s_nop 1\n\t" \
    "v_min_i32_dpp v164, v164, v164 row_ror:8 row_mask:0xf bank_mask:0xf\n\t" \
    "s_nop 0\n\t" \
    "v_readlane_b32 s69, v164, 0\n\t" \
    "v_readlane_b32 s70, v164, 16\n\t" \
    "s_nop 1\n\t" \
    "s_min_i32 s83, s69, s70\n\t" \
    "s_cmp_ge_i32 s83, s65\n\t" \
    "s_cbranch_scc1 rr_WFok22_%=\n\t" \
    "s_cmp_eq_u32 s68, 0\n\t" \
    "s_cbranch_scc1 rr_F23_%=\n\t" \
    "s_sub_u32 s68, s68, 1\n\t" \
    "s_sleep 2\n\t" \
    "s_branch rr_WF21_%=\n\t" \
    "rr_F23_%=:\n\t" \
    "v_mov_b32 v164, 5767424\n\t" \
    "v_mov_b32 v165, 10\n\t" \
    "global_store_dword v164, v165, s[36:37] sc1\n\t" \
    "s_waitcnt vmcnt(0)\n\t" \
    "s_branch rr_WFok22_%=\n\t" \
    "rr_WFok22_%=:\n\t" \
    "s_mul_i32 s70, s67, 7\n\t" \
    "s_add_u32 s69, s65, 0\n\t" \
    "s_xor_b32 s69, s69, s70\n\t" \
    "s_and_b32 s69, s69, 63\n\t" \
    "s_lshl_b32 s69, s69, 15\n\t" \
    "s_mul_i32 s70, s67, 2097152\n\t" \
    "s_add_u32 s69, s69, s70\n\t" \
    "s_add_u32 s69, s69, 48234496\n\t" \
    "s_add_u32 s38, s36, s69\n\t" \
    "s_addc_u32 s39, s37, 0\n\t" \
    "s_add_u32 s70, s65, 0\n\t" \
    "s_mul_i32 s69, s70, 171\n\t" \
    "s_lshr_b32 s69, s69, 9\n\t" \
    "s_mul_i32 s70, s70, 589824\n\t" \
    "s_mul_i32 s71, s69, 327680\n\t" \
    "s_add_u32 s70, s70, s71\n\t" \
    "s_lshl1_add_u32 s69, s69, 4\n\t" \
    "s_bitcmp1_b64 s[44:45], s69\n\t" \
    "s_cselect_b32 s69, 8192, 0\n\t" \
    "s_add_u32 s69, s69, s70\n\t" \
    "s_add_u32 s69, s69, 123731968\n\t" \
    "s_add_u32 s69, s69, s58\n\t" \
    "s_add_u32 s40, s36, s69\n\t" \
    "s_addc_u32 s41, s37, 0\n\t" \
    "s_and_b32 s69, s69, 0xffe00000\n\t" \
    "s_add_u32 s69, s69, 1823744\n\t" \
    "v_mov_b32 v168, s69\n\t" \
    "global_load_dword v168, v168, s[36:37]\n\t" \
    "s_cmp_gt_u32 s65, 61\n\t" \
    "s_cbranch_scc1 rr_idle2_%=\n\t" \
    "s_add_u32 s69, s65, 2\n\t" \
    "s_and_b32 s69, s69, 7\n\t" \
    "s_lshl_b32 s69, s69, 18\n\t" \
    "s_add_u32 s69, s69, s62\n\t" \
    "s_add_u32 s46, s36, s69\n\t" \
    "s_addc_u32 s47, s37, 0\n\t" \
    "global_store_dwordx4 v183, v[172:175], s[46:47] sc1\n\t" \
    "global_store_dwordx4 v184, v[172:175], s[46:47] sc1\n\t" \
    "global_store_dwordx4 v185, v[172:175], s[46:47] sc1\n\t" \
    "global_store_dwordx4 v186, v[172:175], s[46:47] sc1\n\t" \
    "global_store_dwordx4 v187, v[172:175], s[46:47] sc1\n\t" \
    "rr_idle2_%=:\n\t" \
    "v_mov_b32 v200, 0\n\t" \
    "s_cmp_lg_u32 s67, 35\n\t" \
    "s_cbranch_scc1 rr_cke24_%=\n\t" \
    "s_cmp_lt_u32 s65, 7\n\t" \
    "s_cbranch_scc1 rr_cke24_%=\n\t" \
    "s_lshl_b32 s69, s65, 7\n\t" \
    "s_add_u32 s69, s69, 5775360\n\t" \
    "v_mov_b32 v201, s69\n\t" \
    "global_load_dword v200, v201, s[36:37] sc1\n\t" \
    "rr_cke24_%=:\n\t" \
    "s_cmp_gt_u32 s65, 62\n\t" \
    "s_cbranch_scc1 rr_n225_%=\n\t" \
    "s_cmp_lt_u32 s65, 2\n\t" \
    "s_cbranch_scc1 rr_nw226_%=\n\t" \
    "s_mul_hi_u32 s69, s85, s78\n\t" \
    "s_add_u32 s79, s81, s69\n\t" \
    "s_add_u32 s79, s79, s87\n\t" \
    "s_cmp_ge_u32 s65, 54\n\t" \
    "s_cselect_b32 s69, 32, 0\n\t" \
    "s_sub_u32 s79, s79, s69\n\t" \
    "s_cmp_lt_u32 s65, 7\n\t" \
    "s_cselect_b32 s69, 48, 0\n\t" \
    "s_add_u32 s79, s79, s69\n\t" \
    "s_add_u32 s80, s79, s88\n\t" \
    "s_cmp_gt_u32 s65, 4\n\t" \
    "s_cselect_b32 s71, 240, 64\n\t" \
    "s_add_u32 s71, s71, s79\n\t" \
    "s_sub_u32 s70, s71, s74\n\t" \
    "s_max_i32 s70, s70, 0\n\t" \
    "s_min_i32 s70, s70, 16000\n\t" \
    "s_lshl_b32 s70, s70, 1\n\t" \
    "s_mul_hi_u32 s70, s70, s77\n\t" \
    "s_add_u32 s70, s70, s73\n\t" \
    "rr_W27_%=:\n\t" \
    "s_memtime s[56:57]\n\t" \
    "s_waitcnt lgkmcnt(0)\n\t" \
    "s_sub_u32 s69, s70, s56\n\t" \
    "s_cmp_gt_i32 s69, 24\n\t" \
    "s_cbranch_scc1 rr_W27_%=\n\t" \
    "s_branch rr_g113_%=\n\t" \
    "rr_nw226_%=:\n\t" \
    "s_mov_b32 s96, 0xaaaaaaaa\n\t" \
    "v_mov_b32 v166, -1\n\t" \
    "v_readfirstlane_b32 s71, v7\n\t" \
    "s_nop 1\n\t" \
    "s_lshr_b32 s71, s71, 4\n\t" \
    "s_add_u32 s71, s71, 5783552\n\t" \
    "rr_g012_%=:\n\t" \
    "s_memrealtime s[54:55]\n\t" \
    "s_waitcnt lgkmcnt(0)\n\t" \
    "s_bfe_u32 s69, s54, 0x60004\n\t" \
    "s_lshl_b32 s69, s69, 6\n\t" \
    "s_add_u32 s69, s69, s71\n\t" \
    "v_mov_b32 v165, s69\n\t" \
    "global_load_dword v169, v165, s[36:37] sc1\n\t" \
    "s_and_b32 s69, s68, 63\n\t" \
    "s_cbranch_scc1 rr_gf28_%=\n\t" \
    "global_load_dword v166, v6, s[100:101] sc1\n\t" \
    "rr_gf28_%=:\n\t" \
    "s_waitcnt vmcnt(0)\n\t" \
    "v_cmp_ne_u32 s[30:31], -1, v169\n\t" \
    "v_cmp_le_i32 vcc, s65, v166\n\t" \
    "s_nop 1\n\t" \
    "s_lshr_b32 s69, vcc_lo, 1\n\t" \
    "s_and_b32 s69, s69, vcc_lo\n\t" \
    "s_or_b32 s69, s69, s30\n\t" \
    "s_andn2_b32 s69, s69, s96\n\t" \
    "s_cbranch_scc0 rr_gs29_%=\n\t" \
    "s_or_b32 s96, s96, s69\n\t" \
    "s_cmp_eq_u32 s66, 0\n\t" \
    "s_cbranch_scc1 rr_w030_%=\n\t" \
    "s_sleep 8\n\t" \
    "rr_w030_%=:\n\t" \
    "s_bitcmp0_b32 s69, 0\n\t" \
    "s_cbranch_scc1 rr_gk31_%=\n\t" \
    "global_load_dwordx4 a[0:3], v26, s[38:39] offset:0 sc1\n\t" \
    "global_load_dwordx4 a[4:7], v26, s[38:39] offset:1024 sc1\n\t" \
    "rr_gk31_%=:\n\t" \
    "s_bitcmp0_b32 s69, 2\n\t" \
    "s_cbranch_scc1 rr_gk32_%=\n\t" \
    "global_load_dwordx4 a[8:11], v27, s[38:39] offset:0 sc1\n\t" \
    "global_load_dwordx4 a[12:15], v27, s[38:39] offset:1024 sc1\n\t" \
    "rr_gk32_%=:\n\t" \
    "s_bitcmp0_b32 s69, 4\n\t" \
    "s_cbranch_scc1 rr_gk33_%=\n\t" \
    "global_load_dwordx4 a[16:19], v28, s[38:39] offset:0 sc1\n\t" \
    "global_load_dwordx4 a[20:23], v28, s[38:39] offset:1024 sc1\n\t" \
    "rr_gk33_%=:\n\t" \
    "s_bitcmp0_b32 s69, 6\n\t" \
    "s_cbranch_scc1 rr_gk34_%=\n\t" \
    "global_load_dwordx4 a[24:27], v29, s[38:39] offset:0 sc1\n\t" \
    "global_load_dwordx4 a[28:31], v29, s[38:39] offset:1024 sc1\n\t" \
    "rr_gk34_%=:\n\t" \
    "s_bitcmp0_b32 s69, 8\n\t" \
    "s_cbranch_scc1 rr_gk35_%=\n\t" \
    "global_load_dwordx4 a[32:35], v30, s[38:39] offset:0 sc1\n\t" \
    "global_load_dwordx4 a[36:39], v30, s[38:39] offset:1024 sc1\n\t" \
    "rr_gk35_%=:\n\t" \
    "s_bitcmp0_b32 s69, 10\n\t" \
    "s_cbranch_scc1 rr_gk36_%=\n\t" \
    "global_load_dwordx4 a[40:43], v31, s[38:39] offset:0 sc1\n\t" \
    "global_load_dwordx4 a[44:47], v31, s[38:39] offset:1024 sc1\n\t" \
    "rr_gk36_%=:\n\t" \
    "s_bitcmp0_b32 s69, 12\n\t" \
    "s_cbranch_scc1 rr_gk37_%=\n\t" \
    "global_load_dwordx4 a[48:51], v32, s[38:39] offset:0 sc1\n\t" \
    "global_load_dwordx4 a[52:55], v32, s[38:39] offset:1024 sc1\n\t" \
    "rr_gk37_%=:\n\t" \
    "s_bitcmp0_b32 s69, 14\n\t" \
    "s_cbranch_scc1 rr_gk38_%=\n\t" \
    "global_load_dwordx4 a[56:59], v33, s[38:39] offset:0 sc1\n\t" \
    "global_load_dwordx4 a[60:63], v33, s[38:39] offset:1024 sc1\n\t" \
    "rr_gk38_%=:\n\t" \
    "s_bitcmp0_b32 s69, 16\n\t" \
    "s_cbranch_scc1 rr_gk39_%=\n\t" \
    "global_load_dwordx4 a[64:67], v34, s[38:39] offset:0 sc1\n\t" \
    "global_load_dwordx4 a[68:71], v34, s[38:39] offset:1024 sc1\n\t" \
    "rr_gk39_%=:\n\t" \
    "s_bitcmp0_b32 s69, 18\n\t" \
    "s_cbranch_scc1 rr_gk40_%=\n\t" \
    "global_load_dwordx4 a[72:75], v35, s[38:39] offset:0 sc1\n\t" \
    "global_load_dwordx4 a[76:79], v35, s[38:39] offset:1024 sc1\n\t" \
    "rr_gk40_%=:\n\t" \
    "s_bitcmp0_b32 s69, 20\n\t" \
    "s_cbranch_scc1 rr_gk41_%=\n\t" \
    "global_load_dwordx4 a[80:83], v36, s[38:39] offset:0 sc1\n\t" \
    "global_load_dwordx4 a[84:87], v36, s[38:39] offset:1024 sc1\n\t" \
    "rr_gk41_%=:\n\t" \
    "s_bitcmp0_b32 s69, 22\n\t" \
    "s_cbranch_scc1 rr_gk42_%=\n\t" \
    "global_load_dwordx4 a[88:91], v37, s[38:39] offset:0 sc1\n\t" \
    "global_load_dwordx4 a[92:95], v37, s[38:39] offset:1024 sc1\n\t" \
    "rr_gk42_%=:\n\t" \
    "s_bitcmp0_b32 s69, 24\n\t" \
    "s_cbranch_scc1 rr_gk43_%=\n\t" \
    "global_load_dwordx4 a[96:99], v38, s[38:39] offset:0 sc1\n\t" \
    "global_load_dwordx4 a[100:103], v38, s[38:39] offset:1024 sc1\n\t" \
    "rr_gk43_%=:\n\t" \
    "s_bitcmp0_b32 s69, 26\n\t" \
    "s_cbranch_scc1 rr_gk44_%=\n\t" \
    "global_load_dwordx4 a[104:107], v39, s[38:39] offset:0 sc1\n\t" \
    "global_load_dwordx4 a[108:111], v39, s[38:39] offset:1024 sc1\n\t" \
    "rr_gk44_%=:\n\t" \
    "s_bitcmp0_b32 s69, 28\n\t" \
    "s_cbranch_scc1 rr_gk45_%=\n\t" \
    "global_load_dwordx4 a[112:115], v40, s[38:39] offset:0 sc1\n\t" \
    "global_load_dwordx4 a[116:119], v40, s[38:39] offset:1024 sc1\n\t" \
    "rr_gk45_%=:\n\t" \
    "s_bitcmp0_b32 s69, 30\n\t" \
    "s_cbranch_scc1 rr_gk46_%=\n\t" \
    "global_load_dwordx4 a[120:123], v41, s[38:39] offset:0 sc1\n\t" \
    "global_load_dwordx4 a[124:127], v41, s[38:39] offset:1024 sc1\n\t" \
    "rr_gk46_%=:\n\t" \
    "s_cmp_eq_u32 s96, -1\n\t" \
    "s_cbranch_scc1 rr_n225_%=\n\t" \
    "rr_gs29_%=:\n\t" \
    "s_cmp_eq_u32 s68, 0\n\t" \
    "s_cbranch_scc1 rr_F47_%=\n\t" \
    "s_sub_u32 s68, s68, 1\n\t" \
    "s_sleep 1\n\t" \
    "s_branch rr_g012_%=\n\t" \
    "rr_F47_%=:\n\t" \
    "v_mov_b32 v167, 5767424\n\t" \
    "v_mov_b32 v168, 16\n\t" \
    "global_store_dword v167, v168, s[36:37] sc1\n\t" \
    "s_waitcnt vmcnt(0)\n\t" \
    "s_branch rr_n225_%=\n\t" \
    "rr_g113_%=:\n\t" \
    "global_load_dwordx4 a[0:3], v26, s[38:39] offset:0 sc1\n\t" \
    "global_load_dwordx4 a[4:7], v26, s[38:39] offset:1024 sc1\n\t" \
    "global_load_dwordx4 a[8:11], v27, s[38:39] offset:0 sc1\n\t" \
    "global_load_dwordx4 a[12:15], v27, s[38:39] offset:1024 sc1\n\t" \
    "global_load_dwordx4 a[16:19], v28, s[38:39] offset:0 sc1\n\t" \
    "global_load_dwordx4 a[20:23], v28, s[38:39] offset:1024 sc1\n\t" \
    "global_load_dwordx4 a[24:27], v29, s[38:39] offset:0 sc1\n\t" \
    "global_load_dwordx4 a[28:31], v29, s[38:39] offset:1024 sc1\n\t" \
    "global_load_dwordx4 a[32:35], v30, s[38:39] offset:0 sc1\n\t" \
    "global_load_dwordx4 a[36:39], v30, s[38:39] offset:1024 sc1\n\t" \
    "global_load_dwordx4 a[40:43], v31, s[38:39] offset:0 sc1\n\t" \
    "global_load_dwordx4 a[44:47], v31, s[38:39] offset:1024 sc1\n\t" \
    "global_load_dwordx4 a[48:51], v32, s[38:39] offset:0 sc1\n\t" \
    "global_load_dwordx4 a[52:55], v32, s[38:39] offset:1024 sc1\n\t" \
    "global_load_dwordx4 a[56:59], v33, s[38:39] offset:0 sc1\n\t" \
    "global_load_dwordx4 a[60:63], v33, s[38:39] offset:1024 sc1\n\t" \
    "global_load_dwordx4 a[64:67], v34, s[38:39] offset:0 sc1\n\t" \
    "global_load_dwordx4 a[68:71], v34, s[38:39] offset:1024 sc1\n\t" \
    "global_load_dwordx4 a[72:75], v35, s[38:39] offset:0 sc1\n\t" \
    "global_load_dwordx4 a[76:79], v35, s[38:39] offset:1024 sc1\n\t" \
    "global_load_dwordx4 a[80:83], v36, s[38:39] offset:0 sc1\n\t" \
    "global_load_dwordx4 a[84:87], v36, s[38:39] offset:1024 sc1\n\t" \
    "global_load_dwordx4 a[88:91], v37, s[38:39] offset:0 sc1\n\t" \
    "global_load_dwordx4 a[92:95], v37, s[38:39] offset:1024 sc1\n\t" \
    "global_load_dwordx4 a[96:99], v38, s[38:39] offset:0 sc1\n\t" \
    "global_load_dwordx4 a[100:103], v38, s[38:39] offset:1024 sc1\n\t" \
    "global_load_dwordx4 a[104:107], v39, s[38:39] offset:0 sc1\n\t" \
    "global_load_dwordx4 a[108:111], v39, s[38:39] offset:1024 sc1\n\t" \
    "global_load_dwordx4 a[112:115], v40, s[38:39] offset:0 sc1\n\t" \
    "global_load_dwordx4 a[116:119], v40, s[38:39] offset:1024 sc1\n\t" \
    "global_load_dwordx4 a[120:123], v41, s[38:39] offset:0 sc1\n\t" \
    "global_load_dwordx4 a[124:127], v41, s[38:39] offset:1024 sc1\n\t" \
    "rr_n225_%=:\n\t" \
    "s_cmp_eq_u32 s65, 0\n\t" \
    "s_cbranch_scc1 rr_act3_%=\n\t" \
    "s_waitcnt vmcnt(0)\n\t" \
    "rr_ckl49_%=:\n\t" \
    "v_cmp_eq_u32 vcc, -1, v200\n\t" \
    "s_cbranch_vccz rr_ck48_%=\n\t" \
    "global_load_dword v200, v201, s[36:37] sc1\n\t" \
    "s_waitcnt vmcnt(0)\n\t" \
    "s_cmp_eq_u32 s68, 0\n\t" \
    "s_cbranch_scc1 rr_F50_%=\n\t" \
    "s_sub_u32 s68, s68, 1\n\t" \
    "s_sleep 1\n\t" \
    "s_branch rr_ckl49_%=\n\t" \
    "rr_F50_%=:\n\t" \
    "v_mov_b32 v166, 5767424\n\t" \
    "v_mov_b32 v167, 15\n\t" \
    "global_store_dword v166, v167, s[36:37] sc1\n\t" \
    "s_waitcnt vmcnt(0)\n\t" \
    "s_branch rr_ck48_%=\n\t" \
    "rr_ck48_%=:\n\t" \
    "s_barrier\n\t" \
    "s_cmp_gt_u32 s65, 62\n\t" \
    "s_cbranch_scc1 rr_end4_%=\n\t" \
    "ds_read_b32 v164, v9\n\t" \
    "s_waitcnt lgkmcnt(0)\n\t" \
    "v_readfirstlane_b32 s81, v164\n\t" \
    "s_nop 1\n\t" \
    "s_cmp_lg_u32 s65, 1\n\t" \
    "s_cbranch_scc1 rr_n151_%=\n\t" \
    "s_waitcnt lgkmcnt(0)\n\t" \
    "s_mov_b32 s70, 116\n\t" \
    "rr_E52_%=:\n\t" \
    "s_memtime s[56:57]\n\t" \
    "s_memrealtime s[2:3]\n\t" \
    "s_nop 0\n\t" \
    "s_memrealtime s[4:5]\n\t" \
    "s_nop 0\n\t" \
    "s_memrealtime s[6:7]\n\t" \
    "s_nop 0\n\t" \
    "s_memrealtime s[8:9]\n\t" \
    "s_nop 0\n\t" \
    "s_memrealtime s[10:11]\n\t" \
    "s_nop 0\n\t" \
    "s_memrealtime s[12:13]\n\t" \
    "s_nop 0\n\t" \
    "s_memrealtime s[14:15]\n\t" \
    "s_nop 0\n\t" \
    "s_memrealtime s[16:17]\n\t" \
    "s_nop 0\n\t" \
    "s_memrealtime s[18:19]\n\t" \
    "s_nop 0\n\t" \
    "s_memrealtime s[20:21]\n\t" \
    "s_nop 0\n\t" \
    "s_memrealtime s[22:23]\n\t" \
    "s_nop 0\n\t" \
    "s_memrealtime s[24:25]\n\t" \
    "s_nop 0\n\t" \
    "s_memrealtime s[26:27]\n\t" \
    "s_nop 0\n\t" \
    "s_memrealtime s[28:29]\n\t" \
    "s_memtime s[54:55]\n\t" \
    "s_add_u32 s70, s70, 8\n\t" \
    "s_waitcnt lgkmcnt(0)\n\t" \
    "s_sub_u32 s69, s54, s56\n\t" \
    "s_cmp_gt_u32 s69, s70\n\t" \
    "s_cbranch_scc1 rr_E52_%=\n\t" \
    "s_add_u32 s2, s2, s4\n\t" \
    "s_add_u32 s6, s6, s8\n\t" \
    "s_add_u32 s10, s10, s12\n\t" \
    "s_add_u32 s14, s14, s16\n\t" \
    "s_add_u32 s18, s18, s20\n\t" \
    "s_add_u32 s22, s22, s24\n\t" \
    "s_add_u32 s2, s2, s6\n\t" \
    "s_add_u32 s10, s10, s14\n\t" \
    "s_add_u32 s18, s18, s22\n\t" \
    "s_add_u32 s2, s2, s26\n\t" \
    "s_add_u32 s10, s10, s18\n\t" \
    "s_add_u32 s2, s2, s10\n\t" \
    "s_mul_i32 s70, s28, 13\n\t" \
    "s_sub_u32 s70, s70, s2\n\t" \
    "s_min_u32 s70, s70, 52\n\t" \
    "s_mul_i32 s69, s69, s70\n\t" \
    "s_mul_hi_u32 s69, s69, 76695844\n\t" \
    "s_mov_b32 s75, s73\n\t" \
    "s_mov_b32 s76, s74\n\t" \
    "s_add_u32 s73, s56, s69\n\t" \
    "s_lshl_b32 s74, s28, 4\n\t" \
    "rr_n151_%=:\n\t" \
    "s_mul_hi_u32 s69, s85, s78\n\t" \
    "s_add_u32 s79, s81, s69\n\t" \
    "s_add_u32 s79, s79, s87\n\t" \
    "s_cmp_ge_u32 s65, 53\n\t" \
    "s_cselect_b32 s69, 32, 0\n\t" \
    "s_sub_u32 s79, s79, s69\n\t" \
    "s_cmp_lt_u32 s65, 6\n\t" \
    "s_cselect_b32 s69, 48, 0\n\t" \
    "s_add_u32 s79, s79, s69\n\t" \
    "s_add_u32 s80, s79, s88\n\t" \
    "s_sub_u32 s70, s79, s74\n\t" \
    "s_max_i32 s70, s70, 0\n\t" \
    "s_min_i32 s70, s70, 16000\n\t" \
    "s_lshl_b32 s70, s70, 1\n\t" \
    "s_mul_hi_u32 s70, s70, s77\n\t" \
    "s_add_u32 s70, s70, s73\n\t" \
    "rr_W53_%=:\n\t" \
    "s_memtime s[56:57]\n\t" \
    "s_waitcnt lgkmcnt(0)\n\t" \
    "s_sub_u32 s69, s70, s56\n\t" \
    "s_cmp_gt_i32 s69, 24\n\t" \
    "s_cbranch_scc1 rr_W53_%=\n\t" \
    "rr_act3_%=:\n\t" \
    "s_add_u32 s69, s65, 0\n\t" \
    "s_and_b32 s69, s69, 7\n\t" \
    "s_lshl_b32 s69, s69, 18\n\t" \
    "s_add_u32 s69, s69, s62\n\t" \
    "s_add_u32 s46, s36, s69\n\t" \
    "s_addc_u32 s47, s37, 0\n\t" \
    "s_add_u32 s69, s65, 1\n\t" \
    "s_and_b32 s69, s69, 7\n\t" \
    "s_lshl_b32 s69, s69, 18\n\t" \
    "s_add_u32 s69, s69, s62\n\t" \
    "s_add_u32 s48, s36, s69\n\t" \
    "s_addc_u32 s49, s37, 0\n\t" \
    "s_and_b32 s69, s65, 7\n\t" \
    "s_lshl_b32 s69, s69, 18\n\t" \
    "s_add_u32 s69, s69, s63\n\t" \
    "s_add_u32 s52, s36, s69\n\t" \
    "s_addc_u32 s53, s37, 0\n\t" \
    "s_sub_u32 s69, s65, 2\n\t" \
    "s_cmp_ge_u32 s69, 5\n\t" \
    "s_cbranch_scc1 rr_nf54_%=\n\t" \
    "s_cmp_lg_u32 s64, 0\n\t" \
    "s_cbranch_scc1 rr_nf54_%=\n\t" \
    "v_add_u32 v169, 4096, v169\n\t" \
    "global_load_dword v176, v169, s[36:37] sc1\n\t" \
    "rr_nf54_%=:\n\t" \
    "s_waitcnt lgkmcnt(0)\n\t" \
    "s_mov_b32 s70, 116\n\t" \
    "rr_E55_%=:\n\t" \
    "s_memtime s[56:57]\n\t" \
    "s_memrealtime s[2:3]\n\t" \
    "s_nop 0\n\t" \
    "s_memrealtime s[4:5]\n\t" \
    "s_nop 0\n\t" \
    "s_memrealtime s[6:7]\n\t" \
    "s_nop 0\n\t" \
    "s_memrealtime s[8:9]\n\t" \
    "s_nop 0\n\t" \
    "s_memrealtime s[10:11]\n\t" \
    "s_nop 0\n\t" \
    "s_memrealtime s[12:13]\n\t" \
    "s_nop 0\n\t" \
    "s_memrealtime s[14:15]\n\t" \
    "s_nop 0\n\t" \
    "s_memrealtime s[16:17]\n\t" \
    "s_nop 0\n\t" \
    "s_memrealtime s[18:19]\n\t" \
    "s_nop 0\n\t" \
    "s_memrealtime s[20:21]\n\t" \
    "s_nop 0\n\t" \
    "s_memrealtime s[22:23]\n\t" \
    "s_nop 0\n\t" \
    "s_memrealtime s[24:25]\n\t" \
    "s_nop 0\n\t" \
    "s_memrealtime s[26:27]\n\t" \
    "s_nop 0\n\t" \
    "s_memrealtime s[28:29]\n\t" \
    "s_memtime s[54:55]\n\t" \
    "s_add_u32 s70, s70, 8\n\t" \
    "s_waitcnt lgkmcnt(0)\n\t" \
    "s_sub_u32 s69, s54, s56\n\t" \
    "s_cmp_gt_u32 s69, s70\n\t" \
    "s_cbranch_scc1 rr_E55_%=\n\t" \
    "s_add_u32 s2, s2, s4\n\t" \
    "s_add_u32 s6, s6, s8\n\t" \
    "s_add_u32 s10, s10, s12\n\t" \
    "s_add_u32 s14, s14, s16\n\t" \
    "s_add_u32 s18, s18, s20\n\t" \
    "s_add_u32 s22, s22, s24\n\t" \
    "s_add_u32 s2, s2, s6\n\t" \
    "s_add_u32 s10, s10, s14\n\t" \
    "s_add_u32 s18, s18, s22\n\t" \
    "s_add_u32 s2, s2, s26\n\t" \
    "s_add_u32 s10, s10, s18\n\t" \
    "s_add_u32 s2, s2, s10\n\t" \
    "s_mul_i32 s70, s28, 13\n\t" \
    "s_sub_u32 s70, s70, s2\n\t" \
    "s_min_u32 s70, s70, 52\n\t" \
    "s_mul_i32 s69, s69, s70\n\t" \
    "s_mul_hi_u32 s69, s69, 76695844\n\t" \
    "s_mov_b32 s75, s73\n\t" \
    "s_mov_b32 s76, s74\n\t" \
    "s_add_u32 s73, s56, s69\n\t" \
    "s_lshl_b32 s74, s28, 4\n\t" \
    "s_sub_u32 s69, s73, s75\n\t" \
    "s_sub_u32 s70, s74, s76\n\t" \
    "s_sub_u32 s71, s70, 1600\n\t" \
    "s_cmp_lt_u32 s71, 400000\n\t" \
    "s_cbranch_scc0 rr_K56_%=\n\t" \
    "v_cvt_f32_u32 v164, s69\n\t" \
    "v_cvt_f32_u32 v165, s70\n\t" \
    "v_rcp_f32 v166, v164\n\t" \
    "v_rcp_f32 v167, v165\n\t" \
    "v_mul_f32 v166, v166, v165\n\t" \
    "v_mul_f32 v167, v167, v164\n\t" \
    "v_mul_f32 v166, 0x4f800000, v166\n\t" \
    "v_mul_f32 v167, 0x4f000000, v167\n\t" \
    "v_cvt_u32_f32 v166, v166\n\t" \
    "v_cvt_u32_f32 v167, v167\n\t" \
    "v_readfirstlane_b32 s69, v166\n\t" \
    "v_readfirstlane_b32 s70, v167\n\t" \
    "s_nop 1\n\t" \
    "s_lshr_b32 s71, s65, 1\n\t" \
    "s_min_u32 s71, s71, 1\n\t" \
    "s_sub_u32 s69, s69, s78\n\t" \
    "s_ashr_i32 s69, s69, s71\n\t" \
    "s_add_u32 s78, s78, s69\n\t" \
    "s_sub_u32 s70, s70, s77\n\t" \
    "s_ashr_i32 s70, s70, s71\n\t" \
    "s_add_u32 s77, s77, s70\n\t" \
    "rr_K56_%=:\n\t" \
    "s_mov_b32 s91, s64\n\t" \
    "s_cmp_lg_u32 s64, 0\n\t" \
    "s_cselect_b32 s77, 0, s77\n\t" \
    "v_mov_b32 v160, 0\n\t" \
    "v_mov_b32 v161, 0\n\t" \
    "v_mov_b32 v162, 0\n\t" \
    "v_mov_b32 v163, 0\n\t" \
    ".p2align 6\n\t" \
    "s_nop 0\n\t" \
    "s_nop 0\n\t" \
    "s_sub_u32 s70, s80, s74\n\t" \
    "s_max_i32 s70, s70, 0\n\t" \
    "s_min_i32 s70, s70, 16000\n\t" \
    "s_lshl_b32 s70, s70, 1\n\t" \
    "s_mul_hi_u32 s70, s70, s77\n\t" \
    "s_add_u32 s70, s70, s73\n\t" \
    "s_getpc_b64 s[30:31]\n\t" \
    "rr_Wp59_%=:\n\t" \
    "s_add_u32 s30, s30, rr_We60_%=-rr_Wp59_%=\n\t" \
    "s_addc_u32 s31, s31, 0\n\t" \
    "s_sub_u32 s70, s70, 96\n\t" \
    "rr_W58_%=:\n\t" \
    "s_memtime s[56:57]\n\t" \
    "s_waitcnt lgkmcnt(0)\n\t" \
    "s_sub_u32 s69, s70, s56\n\t" \
    "s_cmp_gt_i32 s69, 80\n\t" \
    "s_cbranch_scc1 rr_W58_%=\n\t" \
    "s_max_i32 s69, s69, 0\n\t" \
    "s_and_b32 s69, s69, -4\n\t" \
    "s_sub_u32 s30, s30, s69\n\t" \
    "s_subb_u32 s31, s31, 0\n\t" \
    "s_setpc_b64 s[30:31]\n\t" \
    "s_nop 0\n\t" \
    "s_nop 0\n\t" \
    "s_nop 0\n\t" \
    "s_nop 0\n\t" \
    "s_nop 0\n\t" \
    "s_nop 0\n\t" \
    "s_nop 0\n\t" \
    "s_nop 0\n\t" \
    "s_nop 0\n\t" \
    "s_nop 0\n\t" \
    "s_nop 0\n\t" \
    "s_nop 0\n\t" \
    "s_nop 0\n\t" \
    "s_nop 0\n\t" \
    "s_nop 0\n\t" \
    "s_nop 0\n\t" \
    "s_nop 0\n\t" \
    "s_nop 0\n\t" \
    "s_nop 0\n\t" \
    "s_nop 0\n\t" \
    "rr_We60_%=:\n\t" \
    "global_load_dword v192, v7, s[52:53] offset:0 sc1\n\t" \
    "s_sub_u32 s71, s86, s74\n\t" \
    "s_lshl_b32 s71, s71, 2\n\t" \
    "s_lshr_b32 s33, s77, 1\n\t" \
    "s_sub_u32 s34, s73, 176\n\t" \
    "s_mov_b32 s96, 0\n\t" \
    "s_getpc_b64 s[30:31]\n\t" \
    "rr_rpc64_%=:\n\t" \
    "s_add_u32 s50, s30, rr_pgo67_%=-rr_rpc64_%=\n\t" \
    "s_addc_u32 s51, s31, 0\n\t" \
    "s_add_u32 s30, s30, rr_again5_%=-rr_rpc64_%=\n\t" \
    "s_addc_u32 s31, s31, 0\n\t" \
    "s_nop 7\n\t" \
    "s_nop 7\n\t" \
    "s_nop 7\n\t" \
    "s_nop 7\n\t" \
    "s_nop 3\n\t" \
    "global_load_dword v193, v7, s[52:53] offset:2048 sc1\n\t" \
    "s_nop 7\n\t" \
    "s_nop 7\n\t" \
    "s_nop 7\n\t" \
    "s_nop 7\n\t" \
    "s_nop 7\n\t" \
    "s_nop 7\n\t" \
    "global_load_dword v194, v189, s[52:53] offset:0 sc1\n\t" \
    "s_waitcnt vmcnt(2)\n\t" \
    "rr_hok61_%=:\n\t" \
    "s_memtime s[54:55]\n\t" \
    "v_max_u32_dpp v192, v192, v192 row_ror:1 row_mask:0xf bank_mask:0xf\n\t" \
    "s_nop 1\n\t" \
    "v_max_u32_dpp v192, v192, v192 row_ror:2 row_mask:0xf bank_mask:0xf\n\t" \
    "s_nop 1\n\t" \
    "v_max_u32_dpp v192, v192, v192 row_ror:4 row_mask:0xf bank_mask:0xf\n\t" \
    "s_nop 1\n\t" \
    "v_max_u32_dpp v192, v192, v192 row_ror:8 row_mask:0xf bank_mask:0xf\n\t" \
    "s_nop 0\n\t" \
    "v_readlane_b32 s69, v192, 0\n\t" \
    "v_readlane_b32 s70, v192, 16\n\t" \
    "s_waitcnt lgkmcnt(0)\n\t" \
    "rr_hmax70_%=:\n\t" \
    "s_sub_u32 s56, s34, s54\n\t" \
    "s_max_u32 s81, s69, s70\n\t" \
    "s_cmp_eq_u32 s81, -1\n\t" \
    "s_cbranch_scc1 rr_hlate62_%=\n\t" \
    "s_lshl2_add_u32 s70, s81, s71\n\t" \
    "s_mul_hi_i32 s70, s70, s33\n\t" \
    "s_add_u32 s69, s70, s56\n\t" \
    "s_max_i32 s69, s69, 0\n\t" \
    "s_cmp_gt_i32 s69, 80\n\t" \
    "s_cbranch_scc1 rr_rpoll65_%=\n\t" \
    "rr_rback66_%=:\n\t" \
    "s_and_b32 s69, s69, -4\n\t" \
    "s_sub_u32 s30, s30, s69\n\t" \
    "s_subb_u32 s31, s31, 0\n\t" \
    "s_setpc_b64 s[30:31]\n\t" \
    "rr_rpoll65_%=:\n\t" \
    "s_min_i32 s70, s70, 24000\n\t" \
    "s_add_u32 s70, s70, s73\n\t" \
    "s_sub_u32 s70, s70, 124\n\t" \
    "s_sub_u32 s70, s70, s96\n\t" \
    "rr_wl63_%=:\n\t" \
    "s_memtime s[56:57]\n\t" \
    "s_waitcnt lgkmcnt(0)\n\t" \
    "s_sub_u32 s69, s70, s56\n\t" \
    "s_cmp_gt_i32 s69, 80\n\t" \
    "s_cbranch_scc1 rr_wl63_%=\n\t" \
    "s_max_i32 s69, s69, 0\n\t" \
    "s_branch rr_rback66_%=\n\t" \
    "s_nop 0\n\t" \
    "s_nop 0\n\t" \
    "s_nop 0\n\t" \
    "s_nop 0\n\t" \
    "s_nop 0\n\t" \
    "s_nop 0\n\t" \
    "s_nop 0\n\t" \
    "s_nop 0\n\t" \
    "s_nop 0\n\t" \
    "s_nop 0\n\t" \
    "s_nop 0\n\t" \
    "s_nop 0\n\t" \
    "s_nop 0\n\t" \
    "s_nop 0\n\t" \
    "s_nop 0\n\t" \
    "s_nop 0\n\t" \
    "s_nop 0\n\t" \
    "s_nop 0\n\t" \
    "s_nop 0\n\t" \
    "s_nop 0\n\t" \
    "rr_again5_%=:\n\t" \
    "rr_pgo67_%=:\n\t" \
    "global_load_dwordx4 v[48:51], v10, s[46:47] sc1\n\t" \
    "global_load_dwordx4 v[52:55], v11, s[46:47] sc1\n\t" \
    "global_load_dwordx4 v[56:59], v12, s[46:47] sc1\n\t" \
    "global_load_dwordx4 v[60:63], v13, s[46:47] sc1\n\t" \
    "global_load_dwordx4 v[64:67], v14, s[46:47] sc1\n\t" \
    "global_load_dwordx4 v[68:71], v15, s[46:47] sc1\n\t" \
    "global_load_dwordx4 v[72:75], v16, s[46:47] sc1\n\t" \
    "global_load_dwordx4 v[76:79], v17, s[46:47] sc1\n\t" \
    "global_load_dwordx4 v[80:83], v18, s[46:47] sc1\n\t" \
    "global_load_dwordx4 v[84:87], v19, s[46:47] sc1\n\t" \
    "global_load_dwordx4 v[88:91], v20, s[46:47] sc1\n\t" \
    "global_load_dwordx4 v[92:95], v21, s[46:47] sc1\n\t" \
    "global_load_dwordx4 v[96:99], v22, s[46:47] sc1\n\t" \
    "global_load_dwordx4 v[100:103], v23, s[46:47] sc1\n\t" \
    "global_load_dwordx4 v[104:107], v24, s[46:47] sc1\n\t" \
    "global_load_dwordx4 v[108:111], v25, s[46:47] sc1\n\t" \
    "global_load_dwordx4 v[112:115], v42, s[46:47] sc1\n\t" \
    "global_load_dwordx4 v[116:119], v43, s[46:47] sc1\n\t" \
    "global_load_dwordx4 v[120:123], v44, s[46:47] sc1\n\t" \
    "global_load_dwordx4 v[124:127], v45, s[46:47] sc1\n\t" \
    "global_load_dwordx4 v[128:131], v0, s[40:41] offset:0 sc1\n\t" \
    "global_load_dwordx4 v[132:135], v0, s[40:41] offset:1024 sc1\n\t" \
    "global_load_dwordx4 v[136:139], v0, s[40:41] offset:2048 sc1\n\t" \
    "global_load_dwordx4 v[140:143], v0, s[40:41] offset:3072 sc1\n\t" \
    "global_load_dwordx4 v[144:147], v1, s[40:41] offset:0 sc1\n\t" \
    "global_load_dwordx4 v[148:151], v1, s[40:41] offset:1024 sc1\n\t" \
    "global_load_dwordx4 v[152:155], v1, s[40:41] offset:2048 sc1\n\t" \
    "global_load_dwordx4 v[156:159], v1, s[40:41] offset:3072 sc1\n\t" \
    "s_cmp_lg_u32 s91, 0\n\t" \
    "s_cbranch_scc1 rr_nob8_%=\n\t" \
    "rr_hbk71_%=:\n\t" \
    "v_mov_b32 v164, s81\n\t" \
    "ds_write_b32 v9, v164\n\t" \
    "s_waitcnt lgkmcnt(0)\n\t" \
    "s_barrier\n\t" \
    "rr_nob8_%=:\n\t" \
    "s_waitcnt vmcnt(27)\n\t" \
    "v_smfmac_f32_16x16x64_bf16 v[160:163], v[48:51], a[0:7], v4\n\t" \
    "s_waitcnt vmcnt(26)\n\t" \
    "v_smfmac_f32_16x16x64_bf16 v[160:163], v[52:55], a[8:15], v4\n\t" \
    "s_waitcnt vmcnt(25)\n\t" \
    "v_smfmac_f32_16x16x64_bf16 v[160:163], v[56:59], a[16:23], v4\n\t" \
    "s_waitcnt vmcnt(24)\n\t" \
    "v_smfmac_f32_16x16x64_bf16 v[160:163], v[60:63], a[24:31], v4\n\t" \
    "s_waitcnt vmcnt(23)\n\t" \
    "v_smfmac_f32_16x16x64_bf16 v[160:163], v[64:67], a[32:39], v4\n\t" \
    "s_waitcnt vmcnt(22)\n\t" \
    "v_smfmac_f32_16x16x64_bf16 v[160:163], v[68:71], a[40:47], v4\n\t" \
    "s_waitcnt vmcnt(21)\n\t" \
    "v_smfmac_f32_16x16x64_bf16 v[160:163], v[72:75], a[48:55], v4\n\t" \
    "s_waitcnt vmcnt(20)\n\t" \
    "v_smfmac_f32_16x16x64_bf16 v[160:163], v[76:79], a[56:63], v4\n\t" \
    "s_waitcnt vmcnt(19)\n\t" \
    "v_smfmac_f32_16x16x64_bf16 v[160:163], v[80:83], a[64:71], v4\n\t" \
    "s_waitcnt vmcnt(18)\n\t" \
    "v_smfmac_f32_16x16x64_bf16 v[160:163], v[84:87], a[72:79], v4\n\t" \
    "s_waitcnt vmcnt(17)\n\t" \
    "v_smfmac_f32_16x16x64_bf16 v[160:163], v[88:91], a[80:87], v4\n\t" \
    "s_waitcnt vmcnt(16)\n\t" \
    "v_smfmac_f32_16x16x64_bf16 v[160:163], v[92:95], a[88:95], v4\n\t" \
    "s_waitcnt vmcnt(15)\n\t" \
    "v_smfmac_f32_16x16x64_bf16 v[160:163], v[96:99], a[96:103], v4\n\t" \
    "s_waitcnt vmcnt(14)\n\t" \
    "v_smfmac_f32_16x16x64_bf16 v[160:163], v[100:103], a[104:111], v4\n\t" \
    "s_waitcnt vmcnt(13)\n\t" \
    "v_smfmac_f32_16x16x64_bf16 v[160:163], v[104:107], a[112:119], v4\n\t" \
    "s_waitcnt vmcnt(12)\n\t" \
    "v_smfmac_f32_16x16x64_bf16 v[160:163], v[108:111], a[120:127], v4\n\t" \
    "s_waitcnt vmcnt(6)\n\t" \
    "v_smfmac_f32_16x16x64_bf16 v[160:163], v[112:115], v[128:135], v4\n\t" \
    "s_waitcnt vmcnt(4)\n\t" \
    "v_smfmac_f32_16x16x64_bf16 v[160:163], v[116:119], v[136:143], v4\n\t" \
    "s_waitcnt vmcnt(2)\n\t" \
    "v_smfmac_f32_16x16x64_bf16 v[160:163], v[120:123], v[144:151], v4\n\t" \
    "s_waitcnt vmcnt(0)\n\t" \
    "v_smfmac_f32_16x16x64_bf16 v[160:163], v[124:127], v[152:159], v4\n\t" \
    "s_nop 7\n\t" \
    "v_pk_add_f32 v[160:161], v[160:161], v[162:163]\n\t" \
    "v_add_f32 v166, v160, v161\n\t" \
    "v_cvt_pk_bf16_f32 v164, v160, v161\n\t" \
    "s_nop 0\n\t" \
    "v_add_f32_dpp v166, v166, v166 quad_perm:[1,0,3,2] row_mask:0xf bank_mask:0xf\n\t" \
    "v_mov_b32_dpp v165, v164 quad_perm:[1,0,3,2] row_mask:0xf bank_mask:0xf\n\t" \
    "v_cmp_u_f32 vcc, v166, v166\n\t" \
    "v_perm_b32 v167, v165, v164, v5\n\t" \
    "s_nop 0\n\t" \
    "v_cndmask_b32 v164, v167, v172, vcc\n\t" \
    "rr_cont7_%=:\n\t" \
    "global_store_dword v180, v164, s[48:49] sc1\n\t" \
    "global_store_dword v181, v164, s[48:49] sc1\n\t" \
    "global_store_dword v182, v164, s[48:49] sc1\n\t" \
    "global_store_dword v2, v164, s[48:49] sc1\n\t" \
    "s_cmp_lg_u32 s64, 0\n\t" \
    "s_cbranch_scc1 rr_g214_%=\n\t" \
    "s_cbranch_vccnz rr_bad6_%=\n\t" \
    "s_cmp_eq_u32 s65, 62\n\t" \
    "s_cbranch_scc1 rr_end4_%=\n\t" \
    "s_add_u32 s65, s65, 2\n\t" \
    "s_branch rr_pf9_%=\n\t" \
    "rr_bad6_%=:\n\t" \
    "s_cmp_lg_u32 s64, 0\n\t" \
    "s_cbranch_scc1 rr_cont7_%=\n\t" \
    "s_waitcnt lgkmcnt(0)\n\t" \
    "v_max_u32 v165, v48, v49\n\t" \
    "v_max3_u32 v165, v165, v50, v51\n\t" \
    "v_max3_u32 v165, v165, v52, v53\n\t" \
    "v_max3_u32 v165, v165, v54, v55\n\t" \
    "v_max3_u32 v165, v165, v56, v57\n\t" \
    "v_max3_u32 v165, v165, v58, v59\n\t" \
    "v_max3_u32 v165, v165, v60, v61\n\t" \
    "v_max3_u32 v165, v165, v62, v63\n\t" \
    "v_max3_u32 v165, v165, v64, v65\n\t" \
    "v_max3_u32 v165, v165, v66, v67\n\t" \
    "v_max3_u32 v165, v165, v68, v69\n\t" \
    "v_max3_u32 v165, v165, v70, v71\n\t" \
    "v_max3_u32 v165, v165, v72, v73\n\t" \
    "v_max3_u32 v165, v165, v74, v75\n\t" \
    "v_max3_u32 v165, v165, v76, v77\n\t" \
    "v_max3_u32 v165, v165, v78, v79\n\t" \
    "v_max3_u32 v165, v165, v80, v81\n\t" \
    "v_max3_u32 v165, v165, v82, v83\n\t" \
    "v_max3_u32 v165, v165, v84, v85\n\t" \
    "v_max3_u32 v165, v165, v86, v87\n\t" \
    "v_max3_u32 v165, v165, v88, v89\n\t" \
    "v_max3_u32 v165, v165, v90, v91\n\t" \
    "v_max3_u32 v165, v165, v92, v93\n\t" \
    "v_max3_u32 v165, v165, v94, v95\n\t" \
    "v_max3_u32 v165, v165, v96, v97\n\t" \
    "v_max3_u32 v165, v165, v98, v99\n\t" \
    "v_max3_u32 v165, v165, v100, v101\n\t" \
    "v_max3_u32 v165, v165, v102, v103\n\t" \
    "v_max3_u32 v165, v165, v104, v105\n\t" \
    "v_max3_u32 v165, v165, v106, v107\n\t" \
    "v_max3_u32 v165, v165, v108, v109\n\t" \
    "v_max3_u32 v165, v165, v110, v111\n\t" \
    "v_max3_u32 v165, v165, v112, v113\n\t" \
    "v_max3_u32 v165, v165, v114, v115\n\t" \
    "v_max3_u32 v165, v165, v116, v117\n\t" \
    "v_max3_u32 v165, v165, v118, v119\n\t" \
    "v_max3_u32 v165, v165, v120, v121\n\t" \
    "v_max3_u32 v165, v165, v122, v123\n\t" \
    "v_max3_u32 v165, v165, v124, v125\n\t" \
    "v_max3_u32 v165, v165, v126, v127\n\t" \
    "v_max_u32 v166, v128, v129\n\t" \
    "v_max3_u32 v166, v166, v130, v131\n\t" \
    "v_max3_u32 v166, v166, v132, v133\n\t" \
    "v_max3_u32 v166, v166, v134, v135\n\t" \
    "v_max3_u32 v166, v166, v136, v137\n\t" \
    "v_max3_u32 v166, v166, v138, v139\n\t" \
    "v_max3_u32 v166, v166, v140, v141\n\t" \
    "v_max3_u32 v166, v166, v142, v143\n\t" \
    "v_max3_u32 v166, v166, v144, v145\n\t" \
    "v_max3_u32 v166, v166, v146, v147\n\t" \
    "v_max3_u32 v166, v166, v148, v149\n\t" \
    "v_max3_u32 v166, v166, v150, v151\n\t" \
    "v_max3_u32 v166, v166, v152, v153\n\t" \
    "v_max3_u32 v166, v166, v154, v155\n\t" \
    "v_max3_u32 v166, v166, v156, v157\n\t" \
    "v_max3_u32 v166, v166, v158, v159\n\t" \
    "v_max_u32 v165, v165, v166\n\t" \
    "v_mov_b32 v164, v167\n\t" \
    "v_cmp_eq_u32 vcc, -1, v165\n\t" \
    "s_cbranch_vccz rr_cont7_%=\n\t" \
    "s_mov_b64 vcc, 0\n\t" \
    "s_mov_b32 s91, 1\n\t" \
    "s_mov_b32 s96, 0\n\t" \
    "v_mov_b32 v160, 0\n\t" \
    "v_mov_b32 v161, 0\n\t" \
    "v_mov_b32 v162, 0\n\t" \
    "v_mov_b32 v163, 0\n\t" \
    "s_cmp_eq_u32 s68, 0\n\t" \
    "s_cbranch_scc1 rr_F74_%=\n\t" \
    "s_sub_u32 s68, s68, 1\n\t" \
    "s_branch rr_again5_%=\n\t" \
    "rr_F74_%=:\n\t" \
    "v_mov_b32 v166, 5767424\n\t" \
    "v_mov_b32 v167, 3\n\t" \
    "global_store_dword v166, v167, s[36:37] sc1\n\t" \
    "s_waitcnt vmcnt(0)\n\t" \
    "s_branch rr_cont7_%=\n\t" \
    "rr_hlate62_%=:\n\t" \
    "s_cmp_lg_u32 s64, 0\n\t" \
    "s_cbranch_scc1 rr_hpd73_%=\n\t" \
    "global_load_dword v195, v189, s[52:53] offset:2048 sc1\n\t" \
    "s_waitcnt vmcnt(2)\n\t" \
    "v_mov_b32 v192, v193\n\t" \
    "v_cmp_eq_u32 vcc, -1, v193\n\t" \
    "s_cbranch_vccz rr_hok61_%=\n\t" \
    "global_load_dword v196, v190, s[52:53] offset:0 sc1\n\t" \
    "s_waitcnt vmcnt(2)\n\t" \
    "v_mov_b32 v192, v194\n\t" \
    "v_cmp_eq_u32 vcc, -1, v194\n\t" \
    "s_cbranch_vccz rr_hok61_%=\n\t" \
    "global_load_dword v197, v190, s[52:53] offset:2048 sc1\n\t" \
    "s_waitcnt vmcnt(2)\n\t" \
    "v_mov_b32 v192, v195\n\t" \
    "v_cmp_eq_u32 vcc, -1, v195\n\t" \
    "s_cbranch_vccz rr_hok61_%=\n\t" \
    "s_waitcnt vmcnt(1)\n\t" \
    "v_mov_b32 v192, v196\n\t" \
    "v_cmp_eq_u32 vcc, -1, v196\n\t" \
    "s_cbranch_vccz rr_hok61_%=\n\t" \
    "s_waitcnt vmcnt(0)\n\t" \
    "v_mov_b32 v192, v197\n\t" \
    "v_cmp_eq_u32 vcc, -1, v197\n\t" \
    "s_cbranch_vccz rr_hok61_%=\n\t" \
    "rr_hp72_%=:\n\t" \
    "global_load_dword v192, v191, s[52:53] offset:0 sc1\n\t" \
    "s_waitcnt vmcnt(0)\n\t" \
    "v_cmp_eq_u32 vcc, -1, v192\n\t" \
    "s_cbranch_vccz rr_hpd73_%=\n\t" \
    "s_cmp_eq_u32 s68, 0\n\t" \
    "s_cbranch_scc1 rr_F75_%=\n\t" \
    "s_sub_u32 s68, s68, 1\n\t" \
    "s_sleep 1\n\t" \
    "s_branch rr_hp72_%=\n\t" \
    "rr_F75_%=:\n\t" \
    "v_mov_b32 v166, 5767424\n\t" \
    "v_mov_b32 v167, 7\n\t" \
    "global_store_dword v166, v167, s[36:37] sc1\n\t" \
    "s_waitcnt vmcnt(0)\n\t" \
    "s_branch rr_hpd73_%=\n\t" \
    "rr_hpd73_%=:\n\t" \
    "v_and_b32 v192, -2, v192\n\t" \
    "s_waitcnt lgkmcnt(0)\n\t" \
    "s_mov_b32 s70, 116\n\t" \
    "rr_E76_%=:\n\t" \
    "s_memtime s[56:57]\n\t" \
    "s_memrealtime s[2:3]\n\t" \
    "s_nop 0\n\t" \
    "s_memrealtime s[4:5]\n\t" \
    "s_nop 0\n\t" \
    "s_memrealtime s[6:7]\n\t" \
    "s_nop 0\n\t" \
    "s_memrealtime s[8:9]\n\t" \
    "s_nop 0\n\t" \
    "s_memrealtime s[10:11]\n\t" \
    "s_nop 0\n\t" \
    "s_memrealtime s[12:13]\n\t" \
    "s_nop 0\n\t" \
    "s_memrealtime s[14:15]\n\t" \
    "s_nop 0\n\t" \
    "s_memrealtime s[16:17]\n\t" \
    "s_nop 0\n\t" \
    "s_memrealtime s[18:19]\n\t" \
    "s_nop 0\n\t" \
    "s_memrealtime s[20:21]\n\t" \
    "s_nop 0\n\t" \
    "s_memrealtime s[22:23]\n\t" \
    "s_nop 0\n\t" \
    "s_memrealtime s[24:25]\n\t" \
    "s_nop 0\n\t" \
    "s_memrealtime s[26:27]\n\t" \
    "s_nop 0\n\t" \
    "s_memrealtime s[28:29]\n\t" \
    "s_memtime s[54:55]\n\t" \
    "s_add_u32 s70, s70, 8\n\t" \
    "s_waitcnt lgkmcnt(0)\n\t" \
    "s_sub_u32 s69, s54, s56\n\t" \
    "s_cmp_gt_u32 s69, s70\n\t" \
    "s_cbranch_scc1 rr_E76_%=\n\t" \
    "s_add_u32 s2, s2, s4\n\t" \
    "s_add_u32 s6, s6, s8\n\t" \
    "s_add_u32 s10, s10, s12\n\t" \
    "s_add_u32 s14, s14, s16\n\t" \
    "s_add_u32 s18, s18, s20\n\t" \
    "s_add_u32 s22, s22, s24\n\t" \
    "s_add_u32 s2, s2, s6\n\t" \
    "s_add_u32 s10, s10, s14\n\t" \
    "s_add_u32 s18, s18, s22\n\t" \
    "s_add_u32 s2, s2, s26\n\t" \
    "s_add_u32 s10, s10, s18\n\t" \
    "s_add_u32 s2, s2, s10\n\t" \
    "s_mul_i32 s70, s28, 13\n\t" \
    "s_sub_u32 s70, s70, s2\n\t" \
    "s_min_u32 s70, s70, 52\n\t" \
    "s_mul_i32 s69, s69, s70\n\t" \
    "s_mul_hi_u32 s69, s69, 76695844\n\t" \
    "s_mov_b32 s75, s73\n\t" \
    "s_mov_b32 s76, s74\n\t" \
    "s_add_u32 s73, s56, s69\n\t" \
    "s_lshl_b32 s74, s28, 4\n\t" \
    "s_sub_u32 s71, s86, s74\n\t" \
    "s_lshl_b32 s71, s71, 2\n\t" \
    "s_lshr_b32 s33, s77, 1\n\t" \
    "s_sub_u32 s34, s73, 176\n\t" \
    "s_branch rr_hok61_%=\n\t" \
    "rr_end4_%=:\n\t" \
    "s_endpgm\n\t" \
    "rr_last_%=:\n\t" \
    ""
#define ASM_H \
    "s_mov_b64 s[36:37], %0\n\t" \
    "s_mov_b64 s[42:43], %1\n\t" \
    "s_mov_b64 s[44:45], %2\n\t" \
    "s_mov_b32 s66, %3\n\t" \
    "s_mov_b32 s67, %4\n\t" \
    "s_mov_b64 s[50:51], %5\n\t" \
    "s_mov_b32 s82, %7\n\t" \
    "s_load_dwordx8 s[84:91], %6, 0\n\t" \
    "v_mbcnt_lo_u32_b32 v223, -1, 0\n\t" \
    "v_mbcnt_hi_u32_b32 v223, -1, v223\n\t" \
    "s_getpc_b64 s[46:47]\n\t" \
    "hh_pc_%=:\n\t" \
    "s_lshl_b32 s69, s66, 13\n\t" \
    "v_lshl_add_u32 v222, v223, 7, s69\n\t" \
    "v_min_u32 v222, hh_last_%=-hh_pc_%=-4, v222\n\t" \
    "global_load_dword v222, v222, s[46:47]\n\t" \
    "v_lshlrev_b32 v223, 2, v223\n\t" \
    "v_add_u32 v223, s82, v223\n\t" \
    "ds_read_b32 v0, v223 offset:0\n\t" \
    "ds_read_b32 v1, v223 offset:256\n\t" \
    "ds_read_b32 v2, v223 offset:512\n\t" \
    "ds_read_b32 v3, v223 offset:768\n\t" \
    "ds_read_b32 v4, v223 offset:1024\n\t" \
    "ds_read_b32 v5, v223 offset:1280\n\t" \
    "ds_read_b32 v6, v223 offset:1536\n\t" \
    "ds_read_b32 v7, v223 offset:1792\n\t" \
    "ds_read_b32 v8, v223 offset:2048\n\t" \
    "ds_read_b32 v9, v223 offset:2304\n\t" \
    "ds_read_b32 v10, v223 offset:2560\n\t" \
    "ds_read_b32 v11, v223 offset:2816\n\t" \
    "s_waitcnt lgkmcnt(0)\n\t" \
    "ds_read_b32 v12, v223 offset:3072\n\t" \
    "ds_read_b32 v13, v223 offset:3328\n\t" \
    "ds_read_b32 v14, v223 offset:3584\n\t" \
    "ds_read_b32 v15, v223 offset:3840\n\t" \
    "ds_read_b32 v16, v223 offset:4096\n\t" \
    "ds_read_b32 v17, v223 offset:4352\n\t" \
    "s_waitcnt lgkmcnt(0)\n\t" \
    "s_mov_b32 s68, 262144\n\t" \
    "s_mov_b32 s72, 0\n\t" \
    "s_mov_b32 s83, -1\n\t" \
    "s_mov_b32 s65, 0\n\t" \
    "s_mov_b32 s77, 3019898880\n\t" \
    "s_mov_b32 s78, 3054198966\n\t" \
    "s_waitcnt lgkmcnt(0)\n\t" \
    "s_mov_b32 s70, 116\n\t" \
    "hh_E1_%=:\n\t" \
    "s_memtime s[56:57]\n\t" \
    "s_memrealtime s[2:3]\n\t" \
    "s_nop 0\n\t" \
    "s_memrealtime s[4:5]\n\t" \
    "s_nop 0\n\t" \
    "s_memrealtime s[6:7]\n\t" \
    "s_nop 0\n\t" \
    "s_memrealtime s[8:9]\n\t" \
    "s_nop 0\n\t" \
    "s_memrealtime s[10:11]\n\t" \
    "s_nop 0\n\t" \
    "s_memrealtime s[12:13]\n\t" \
    "s_nop 0\n\t" \
    "s_memrealtime s[14:15]\n\t" \
    "s_nop 0\n\t" \
    "s_memrealtime s[16:17]\n\t" \
    "s_nop 0\n\t" \
    "s_memrealtime s[18:19]\n\t" \
    "s_nop 0\n\t" \
    "s_memrealtime s[20:21]\n\t" \
    "s_nop 0\n\t" \
    "s_memrealtime s[22:23]\n\t" \
    "s_nop 0\n\t" \
    "s_memrealtime s[24:25]\n\t" \
    "s_nop 0\n\t" \
    "s_memrealtime s[26:27]\n\t" \
    "s_nop 0\n\t" \
    "s_memrealtime s[28:29]\n\t" \
    "s_memtime s[54:55]\n\t" \
    "s_add_u32 s70, s70, 8\n\t" \
    "s_waitcnt lgkmcnt(0)\n\t" \
    "s_sub_u32 s69, s54, s56\n\t" \
    "s_cmp_gt_u32 s69, s70\n\t" \
    "s_cbranch_scc1 hh_E1_%=\n\t" \
    "s_add_u32 s2, s2, s4\n\t" \
    "s_add_u32 s6, s6, s8\n\t" \
    "s_add_u32 s10, s10, s12\n\t" \
    "s_add_u32 s14, s14, s16\n\t" \
    "s_add_u32 s18, s18, s20\n\t" \
    "s_add_u32 s22, s22, s24\n\t" \
    "s_add_u32 s2, s2, s6\n\t" \
    "s_add_u32 s10, s10, s14\n\t" \
    "s_add_u32 s18, s18, s22\n\t" \
    "s_add_u32 s2, s2, s26\n\t" \
    "s_add_u32 s10, s10, s18\n\t" \
    "s_add_u32 s2, s2, s10\n\t" \
    "s_mul_i32 s70, s28, 13\n\t" \
    "s_sub_u32 s70, s70, s2\n\t" \
    "s_min_u32 s70, s70, 52\n\t" \
    "s_mul_i32 s69, s69, s70\n\t" \
    "s_mul_hi_u32 s69, s69, 76695844\n\t" \
    "s_mov_b32 s75, s73\n\t" \
    "s_mov_b32 s76, s74\n\t" \
    "s_add_u32 s73, s56, s69\n\t" \
    "s_lshl_b32 s74, s28, 4\n\t" \
    "v_mbcnt_lo_u32_b32 v30, -1, 0\n\t" \
    "v_mbcnt_hi_u32_b32 v30, -1, v30\n\t" \
    "v_lshlrev_b32 v30, 6, v30\n\t" \
    "v_add_u32 v30, 6225920, v30\n\t" \
    "v_mov_b32 v31, 6234112\n\t" \
    "global_load_dword v31, v31, s[36:37] sc1\n\t" \
    "global_load_dwordx2 v[32:33], v30, s[36:37] sc1\n\t" \
    "global_load_dword v34, v30, s[36:37] offset:2048 sc1\n\t" \
    "s_waitcnt vmcnt(0)\n\t" \
    "hh_C2_%=:\n\t" \
    "v_cmp_ne_u32 s[54:55], v32, v34\n\t" \
    "v_sub_u32 v32, v32, v31\n\t" \
    "v_cmp_ne_u32 vcc, 1, v32\n\t" \
    "s_nop 1\n\t" \
    "s_or_b64 vcc, vcc, s[54:55]\n\t" \
    "s_cbranch_scc0 hh_Cok3_%=\n\t" \
    "s_cmp_eq_u32 s68, 0\n\t" \
    "s_cbranch_scc1 hh_F5_%=\n\t" \
    "s_sub_u32 s68, s68, 1\n\t" \
    "s_sleep 6\n\t" \
    "s_branch hh_Cag4_%=\n\t" \
    "hh_F5_%=:\n\t" \
    "v_mov_b32 v34, 5767424\n\t" \
    "v_mov_b32 v35, 4\n\t" \
    "global_store_dword v34, v35, s[36:37] sc1\n\t" \
    "s_waitcnt vmcnt(0)\n\t" \
    "s_branch hh_Cok3_%=\n\t" \
    "hh_Cag4_%=:\n\t" \
    "global_load_dwordx2 v[32:33], v30, s[36:37] sc1\n\t" \
    "global_load_dword v34, v30, s[36:37] offset:2048 sc1\n\t" \
    "s_waitcnt vmcnt(0)\n\t" \
    "s_branch hh_C2_%=\n\t" \
    "hh_Cok3_%=:\n\t" \
    "v_readlane_b32 s69, v33, 2\n\t" \
    "v_readlane_b32 s70, v33, 3\n\t" \
    "s_nop 1\n\t" \
    "s_cmp_lt_u32 s70, s69\n\t" \
    "s_cselect_b32 s69, 0, 8192\n\t" \
    "s_add_u32 s100, s36, s69\n\t" \
    "s_addc_u32 s101, s37, 0\n\t" \
    "v_mov_b32_dpp v30, v33 quad_perm:[1,0,3,2] row_mask:0xf bank_mask:0xf\n\t" \
    "v_cmp_lt_u32 s[44:45], v30, v33\n\t" \
    "s_nop 1\n\t" \
    "s_bitcmp1_b32 s44, 0\n\t" \
    "s_cselect_b32 s62, 8192, 0\n\t" \
    "s_bitcmp1_b32 s44, 2\n\t" \
    "s_cselect_b32 s63, 8192, 0\n\t" \
    "s_or_b32 s63, s63, 0x200000\n\t" \
    "v_min_u32 v30, v30, v33\n\t" \
    "v_cmp_lt_u32 vcc, 660, v30\n\t" \
    "s_nop 1\n\t" \
    "s_bcnt1_i32_b64 s69, vcc\n\t" \
    "s_cmp_gt_u32 s69, 16\n\t" \
    "s_cselect_b32 s69, 1, 0\n\t" \
    "s_mul_i32 s70, s69, 160\n\t" \
    "s_add_u32 s86, s86, s70\n\t" \
    "s_mul_i32 s70, s69, 536\n\t" \
    "s_add_u32 s87, s87, s70\n\t" \
    "s_mul_i32 s70, s69, 256\n\t" \
    "s_add_u32 s88, s88, s70\n\t" \
    "s_waitcnt lgkmcnt(0)\n\t" \
    "s_mov_b32 s70, 116\n\t" \
    "hh_E6_%=:\n\t" \
    "s_memtime s[56:57]\n\t" \
    "s_memrealtime s[2:3]\n\t" \
    "s_nop 0\n\t" \
    "s_memrealtime s[4:5]\n\t" \
    "s_nop 0\n\t" \
    "s_memrealtime s[6:7]\n\t" \
    "s_nop 0\n\t" \
    "s_memrealtime s[8:9]\n\t" \
    "s_nop 0\n\t" \
    "s_memrealtime s[10:11]\n\t" \
    "s_nop 0\n\t" \
    "s_memrealtime s[12:13]\n\t" \
    "s_nop 0\n\t" \
    "s_memrealtime s[14:15]\n\t" \
    "s_nop 0\n\t" \
    "s_memrealtime s[16:17]\n\t" \
    "s_nop 0\n\t" \
    "s_memrealtime s[18:19]\n\t" \
    "s_nop 0\n\t" \
    "s_memrealtime s[20:21]\n\t" \
    "s_nop 0\n\t" \
    "s_memrealtime s[22:23]\n\t" \
    "s_nop 0\n\t" \
    "s_memrealtime s[24:25]\n\t" \
    "s_nop 0\n\t" \
    "s_memrealtime s[26:27]\n\t" \
    "s_nop 0\n\t" \
    "s_memrealtime s[28:29]\n\t" \
    "s_memtime s[54:55]\n\t" \
    "s_add_u32 s70, s70, 8\n\t" \
    "s_waitcnt lgkmcnt(0)\n\t" \
    "s_sub_u32 s69, s54, s56\n\t" \
    "s_cmp_gt_u32 s69, s70\n\t" \
    "s_cbranch_scc1 hh_E6_%=\n\t" \
    "s_add_u32 s2, s2, s4\n\t" \
    "s_add_u32 s6, s6, s8\n\t" \
    "s_add_u32 s10, s10, s12\n\t" \
    "s_add_u32 s14, s14, s16\n\t" \
    "s_add_u32 s18, s18, s20\n\t" \
    "s_add_u32 s22, s22, s24\n\t" \
    "s_add_u32 s2, s2, s6\n\t" \
    "s_add_u32 s10, s10, s14\n\t" \
    "s_add_u32 s18, s18, s22\n\t" \
    "s_add_u32 s2, s2, s26\n\t" \
    "s_add_u32 s10, s10, s18\n\t" \
    "s_add_u32 s2, s2, s10\n\t" \
    "s_mul_i32 s70, s28, 13\n\t" \
    "s_sub_u32 s70, s70, s2\n\t" \
    "s_min_u32 s70, s70, 52\n\t" \
    "s_mul_i32 s69, s69, s70\n\t" \
    "s_mul_hi_u32 s69, s69, 76695844\n\t" \
    "s_mov_b32 s75, s73\n\t" \
    "s_mov_b32 s76, s74\n\t" \
    "s_add_u32 s73, s56, s69\n\t" \
    "s_lshl_b32 s74, s28, 4\n\t" \
    "s_sub_u32 s69, s73, s75\n\t" \
    "s_sub_u32 s70, s74, s76\n\t" \
    "s_sub_u32 s71, s70, 1600\n\t" \
    "s_cmp_lt_u32 s71, 400000\n\t" \
    "s_cbranch_scc0 hh_K7_%=\n\t" \
    "v_cvt_f32_u32 v30, s69\n\t" \
    "v_cvt_f32_u32 v31, s70\n\t" \
    "v_rcp_f32 v32, v30\n\t" \
    "v_rcp_f32 v33, v31\n\t" \
    "v_mul_f32 v32, v32, v31\n\t" \
    "v_mul_f32 v33, v33, v30\n\t" \
    "v_mul_f32 v32, 0x4f800000, v32\n\t" \
    "v_mul_f32 v33, 0x4f000000, v33\n\t" \
    "v_cvt_u32_f32 v32, v32\n\t" \
    "v_cvt_u32_f32 v33, v33\n\t" \
    "v_readfirstlane_b32 s69, v32\n\t" \
    "v_readfirstlane_b32 s70, v33\n\t" \
    "s_nop 1\n\t" \
    "s_mov_b32 s78, s69\n\t" \
    "s_mov_b32 s77, s70\n\t" \
    "hh_K7_%=:\n\t" \
    "s_sub_u32 s80, s74, 32000\n\t" \
    "v_mov_b32 v38, -1\n\t" \
    "v_mov_b32 v39, -1\n\t" \
    "s_sub_u32 s69, s100, s36\n\t" \
    "v_cmp_eq_u32 vcc, 0, v3\n\t" \
    "v_mov_b32 v30, s69\n\t" \
    "s_nop 0\n\t" \
    "v_cndmask_b32 v30, 0, v30, vcc\n\t" \
    "v_add_u32 v2, v2, v30\n\t" \
    "s_mov_b32 m0, 155648\n\t" \
    "s_mul_i32 s96, s67, 16\n\t" \
    "s_add_u32 s96, s96, 64\n\t" \
    "hh_loop8_%=:\n\t" \
    "s_and_b32 s69, s65, 7\n\t" \
    "s_lshl_b32 s69, s69, 18\n\t" \
    "s_add_u32 s69, s69, s63\n\t" \
    "s_add_u32 s52, s36, s69\n\t" \
    "s_addc_u32 s53, s37, 0\n\t" \
    "s_add_u32 s71, s80, 608\n\t" \
    "s_sub_u32 s70, s71, s74\n\t" \
    "s_max_i32 s70, s70, 0\n\t" \
    "s_min_i32 s70, s70, 16000\n\t" \
    "s_lshl_b32 s70, s70, 1\n\t" \
    "s_mul_hi_u32 s70, s70, s77\n\t" \
    "s_add_u32 s70, s70, s73\n\t" \
    "hh_W11_%=:\n\t" \
    "s_memtime s[56:57]\n\t" \
    "s_waitcnt lgkmcnt(0)\n\t" \
    "s_sub_u32 s69, s70, s56\n\t" \
    "s_cmp_gt_i32 s69, 24\n\t" \
    "s_cbranch_scc1 hh_W11_%=\n\t" \
    "global_load_dword v30, v1, s[52:53] sc1\n\t" \
    "s_cmp_lt_u32 s65, 7\n\t" \
    "s_cselect_b32 s69, -2, 63\n\t" \
    "v_mov_b32 v38, s69\n\t" \
    "s_sub_u32 s69, s65, 2\n\t" \
    "s_cmp_ge_u32 s69, 5\n\t" \
    "s_cbranch_scc1 hh_ne12_%=\n\t" \
    "v_mad_u32_u24 v39, s65, v3, v2\n\t" \
    "global_load_dword v38, v39, s[36:37] sc1\n\t" \
    "hh_ne12_%=:\n\t" \
    "s_waitcnt lgkmcnt(0)\n\t" \
    "s_mov_b32 s70, 116\n\t" \
    "hh_E16_%=:\n\t" \
    "s_memtime s[56:57]\n\t" \
    "s_memrealtime s[2:3]\n\t" \
    "s_nop 0\n\t" \
    "s_memrealtime s[4:5]\n\t" \
    "s_nop 0\n\t" \
    "s_memrealtime s[6:7]\n\t" \
    "s_nop 0\n\t" \
    "s_memrealtime s[8:9]\n\t" \
    "s_nop 0\n\t" \
    "s_memrealtime s[10:11]\n\t" \
    "s_nop 0\n\t" \
    "s_memrealtime s[12:13]\n\t" \
    "s_nop 0\n\t" \
    "s_memrealtime s[14:15]\n\t" \
    "s_nop 0\n\t" \
    "s_memrealtime s[16:17]\n\t" \
    "s_nop 0\n\t" \
    "s_memrealtime s[18:19]\n\t" \
    "s_nop 0\n\t" \
    "s_memrealtime s[20:21]\n\t" \
    "s_nop 0\n\t" \
    "s_memrealtime s[22:23]\n\t" \
    "s_nop 0\n\t" \
    "s_memrealtime s[24:25]\n\t" \
    "s_nop 0\n\t" \
    "s_memrealtime s[26:27]\n\t" \
    "s_nop 0\n\t" \
    "s_memrealtime s[28:29]\n\t" \
    "s_memtime s[54:55]\n\t" \
    "s_add_u32 s70, s70, 8\n\t" \
    "s_waitcnt lgkmcnt(0)\n\t" \
    "s_sub_u32 s69, s54, s56\n\t" \
    "s_cmp_gt_u32 s69, s70\n\t" \
    "s_cbranch_scc1 hh_E16_%=\n\t" \
    "s_add_u32 s2, s2, s4\n\t" \
    "s_add_u32 s6, s6, s8\n\t" \
    "s_add_u32 s10, s10, s12\n\t" \
    "s_add_u32 s14, s14, s16\n\t" \
    "s_add_u32 s18, s18, s20\n\t" \
    "s_add_u32 s22, s22, s24\n\t" \
    "s_add_u32 s2, s2, s6\n\t" \
    "s_add_u32 s10, s10, s14\n\t" \
    "s_add_u32 s18, s18, s22\n\t" \
    "s_add_u32 s2, s2, s26\n\t" \
    "s_add_u32 s10, s10, s18\n\t" \
    "s_add_u32 s2, s2, s10\n\t" \
    "s_mul_i32 s70, s28, 13\n\t" \
    "s_sub_u32 s70, s70, s2\n\t" \
    "s_min_u32 s70, s70, 52\n\t" \
    "s_mul_i32 s69, s69, s70\n\t" \
    "s_mul_hi_u32 s69, s69, 76695844\n\t" \
    "s_mov_b32 s75, s73\n\t" \
    "s_mov_b32 s76, s74\n\t" \
    "s_add_u32 s73, s56, s69\n\t" \
    "s_lshl_b32 s74, s28, 4\n\t" \
    "s_sub_u32 s69, s73, s75\n\t" \
    "s_sub_u32 s70, s74, s76\n\t" \
    "s_sub_u32 s71, s70, 1600\n\t" \
    "s_cmp_lt_u32 s71, 400000\n\t" \
    "s_cbranch_scc0 hh_K17_%=\n\t" \
    "v_cvt_f32_u32 v34, s69\n\t" \
    "v_cvt_f32_u32 v35, s70\n\t" \
    "v_rcp_f32 v36, v34\n\t" \
    "v_rcp_f32 v37, v35\n\t" \
    "v_mul_f32 v36, v36, v35\n\t" \
    "v_mul_f32 v37, v37, v34\n\t" \
    "v_mul_f32 v36, 0x4f800000, v36\n\t" \
    "v_mul_f32 v37, 0x4f000000, v37\n\t" \
    "v_cvt_u32_f32 v36, v36\n\t" \
    "v_cvt_u32_f32 v37, v37\n\t" \
    "v_readfirstlane_b32 s69, v36\n\t" \
    "v_readfirstlane_b32 s70, v37\n\t" \
    "s_nop 1\n\t" \
    "s_sub_u32 s69, s69, s78\n\t" \
    "s_ashr_i32 s69, s69, 1\n\t" \
    "s_add_u32 s78, s78, s69\n\t" \
    "s_sub_u32 s70, s70, s77\n\t" \
    "s_ashr_i32 s70, s70, 1\n\t" \
    "s_add_u32 s77, s77, s70\n\t" \
    "hh_K17_%=:\n\t" \
    "s_waitcnt vmcnt(0)\n\t" \
    "v_cmp_eq_u32 vcc, -1, v30\n\t" \
    "s_cbranch_vccz hh_Hok14_%=\n\t" \
    "hh_H13_%=:\n\t" \
    "global_load_dword v30, v1, s[52:53] sc1\n\t" \
    "s_waitcnt vmcnt(0)\n\t" \
    "v_cmp_eq_u32 vcc, -1, v30\n\t" \
    "s_cbranch_vccz hh_Hc15_%=\n\t" \
    "s_cmp_eq_u32 s68, 0\n\t" \
    "s_cbranch_scc1 hh_F18_%=\n\t" \
    "s_sub_u32 s68, s68, 1\n\t" \
    "s_sleep 2\n\t" \
    "s_branch hh_H13_%=\n\t" \
    "hh_F18_%=:\n\t" \
    "v_mov_b32 v31, 5767424\n\t" \
    "v_mov_b32 v32, 14\n\t" \
    "global_store_dword v31, v32, s[36:37] sc1\n\t" \
    "s_waitcnt vmcnt(0)\n\t" \
    "s_branch hh_Hc15_%=\n\t" \
    "hh_Hc15_%=:\n\t" \
    "s_waitcnt lgkmcnt(0)\n\t" \
    "s_mov_b32 s70, 116\n\t" \
    "hh_E19_%=:\n\t" \
    "s_memtime s[56:57]\n\t" \
    "s_memrealtime s[2:3]\n\t" \
    "s_nop 0\n\t" \
    "s_memrealtime s[4:5]\n\t" \
    "s_nop 0\n\t" \
    "s_memrealtime s[6:7]\n\t" \
    "s_nop 0\n\t" \
    "s_memrealtime s[8:9]\n\t" \
    "s_nop 0\n\t" \
    "s_memrealtime s[10:11]\n\t" \
    "s_nop 0\n\t" \
    "s_memrealtime s[12:13]\n\t" \
    "s_nop 0\n\t" \
    "s_memrealtime s[14:15]\n\t" \
    "s_nop 0\n\t" \
    "s_memrealtime s[16:17]\n\t" \
    "s_nop 0\n\t" \
    "s_memrealtime s[18:19]\n\t" \
    "s_nop 0\n\t" \
    "s_memrealtime s[20:21]\n\t" \
    "s_nop 0\n\t" \
    "s_memrealtime s[22:23]\n\t" \
    "s_nop 0\n\t" \
    "s_memrealtime s[24:25]\n\t" \
    "s_nop 0\n\t" \
    "s_memrealtime s[26:27]\n\t" \
    "s_nop 0\n\t" \
    "s_memrealtime s[28:29]\n\t" \
    "s_memtime s[54:55]\n\t" \
    "s_add_u32 s70, s70, 8\n\t" \
    "s_waitcnt lgkmcnt(0)\n\t" \
    "s_sub_u32 s69, s54, s56\n\t" \
    "s_cmp_gt_u32 s69, s70\n\t" \
    "s_cbranch_scc1 hh_E19_%=\n\t" \
    "s_add_u32 s2, s2, s4\n\t" \
    "s_add_u32 s6, s6, s8\n\t" \
    "s_add_u32 s10, s10, s12\n\t" \
    "s_add_u32 s14, s14, s16\n\t" \
    "s_add_u32 s18, s18, s20\n\t" \
    "s_add_u32 s22, s22, s24\n\t" \
    "s_add_u32 s2, s2, s6\n\t" \
    "s_add_u32 s10, s10, s14\n\t" \
    "s_add_u32 s18, s18, s22\n\t" \
    "s_add_u32 s2, s2, s26\n\t" \
    "s_add_u32 s10, s10, s18\n\t" \
    "s_add_u32 s2, s2, s10\n\t" \
    "s_mul_i32 s70, s28, 13\n\t" \
    "s_sub_u32 s70, s70, s2\n\t" \
    "s_min_u32 s70, s70, 52\n\t" \
    "s_mul_i32 s69, s69, s70\n\t" \
    "s_mul_hi_u32 s69, s69, 76695844\n\t" \
    "s_mov_b32 s75, s73\n\t" \
    "s_mov_b32 s76, s74\n\t" \
    "s_add_u32 s73, s56, s69\n\t" \
    "s_lshl_b32 s74, s28, 4\n\t" \
    "s_sub_u32 s69, s73, s75\n\t" \
    "s_sub_u32 s70, s74, s76\n\t" \
    "s_sub_u32 s71, s70, 1600\n\t" \
    "s_cmp_lt_u32 s71, 400000\n\t" \
    "s_cbranch_scc0 hh_K20_%=\n\t" \
    "v_cvt_f32_u32 v34, s69\n\t" \
    "v_cvt_f32_u32 v35, s70\n\t" \
    "v_rcp_f32 v36, v34\n\t" \
    "v_rcp_f32 v37, v35\n\t" \
    "v_mul_f32 v36, v36, v35\n\t" \
    "v_mul_f32 v37, v37, v34\n\t" \
    "v_mul_f32 v36, 0x4f800000, v36\n\t" \
    "v_mul_f32 v37, 0x4f000000, v37\n\t" \
    "v_cvt_u32_f32 v36, v36\n\t" \
    "v_cvt_u32_f32 v37, v37\n\t" \
    "v_readfirstlane_b32 s69, v36\n\t" \
    "v_readfirstlane_b32 s70, v37\n\t" \
    "s_nop 1\n\t" \
    "s_sub_u32 s69, s69, s78\n\t" \
    "s_ashr_i32 s69, s69, 1\n\t" \
    "s_add_u32 s78, s78, s69\n\t" \
    "s_sub_u32 s70, s70, s77\n\t" \
    "s_ashr_i32 s70, s70, 1\n\t" \
    "s_add_u32 s77, s77, s70\n\t" \
    "hh_K20_%=:\n\t" \
    "hh_Hok14_%=:\n\t" \
    "v_max_u32_dpp v30, v30, v30 row_ror:1 row_mask:0xf bank_mask:0xf\n\t" \
    "s_nop 1\n\t" \
    "v_max_u32_dpp v30, v30, v30 row_ror:2 row_mask:0xf bank_mask:0xf\n\t" \
    "s_nop 1\n\t" \
    "v_max_u32_dpp v30, v30, v30 row_ror:4 row_mask:0xf bank_mask:0xf\n\t" \
    "s_nop 1\n\t" \
    "v_max_u32_dpp v30, v30, v30 row_ror:8 row_mask:0xf bank_mask:0xf\n\t" \
    "s_nop 0\n\t" \
    "v_readlane_b32 s69, v30, 0\n\t" \
    "v_readlane_b32 s71, v30, 16\n\t" \
    "s_nop 1\n\t" \
    "s_max_u32 s81, s69, s71\n\t" \
    "s_mul_hi_u32 s69, s85, s78\n\t" \
    "s_add_u32 s79, s81, s69\n\t" \
    "s_add_u32 s79, s79, s87\n\t" \
    "s_cmp_ge_u32 s65, 52\n\t" \
    "s_cselect_b32 s69, 32, 0\n\t" \
    "s_sub_u32 s79, s79, s69\n\t" \
    "s_cmp_lt_u32 s65, 5\n\t" \
    "s_cselect_b32 s69, 48, 0\n\t" \
    "s_add_u32 s79, s79, s69\n\t" \
    "s_add_u32 s80, s79, s88\n\t" \
    "s_add_u32 s65, s65, 1\n\t" \
    "s_cmp_gt_u32 s65, 62\n\t" \
    "s_cbranch_scc1 hh_end10_%=\n\t" \
    "v_lshrrev_b32 v39, 11, v3\n\t" \
    "v_sub_u32 v38, v38, v39\n\t" \
    "s_min_u32 s69, s65, 32\n\t" \
    "v_cmp_ge_i32 vcc, s69, v38\n\t" \
    "s_cbranch_vccnz hh_loop8_%=\n\t" \
    "v_lshrrev_b32 v20, 15, v11\n\t" \
    "v_add3_u32 v20, v20, s65, 1\n\t" \
    "v_min_u32 v20, 63, v20\n\t" \
    "v_mul_u32_u24 v20, v20, v11\n\t" \
    "v_xor_b32 v20, v20, v4\n\t" \
    "v_lshrrev_b32 v21, 15, v12\n\t" \
    "v_add3_u32 v21, v21, s65, 1\n\t" \
    "v_min_u32 v21, 63, v21\n\t" \
    "v_mul_u32_u24 v21, v21, v12\n\t" \
    "v_xor_b32 v21, v21, v5\n\t" \
    "v_lshrrev_b32 v22, 15, v13\n\t" \
    "v_add3_u32 v22, v22, s65, 1\n\t" \
    "v_min_u32 v22, 63, v22\n\t" \
    "v_mul_u32_u24 v22, v22, v13\n\t" \
    "v_xor_b32 v22, v22, v6\n\t" \
    "v_lshrrev_b32 v23, 15, v14\n\t" \
    "v_add3_u32 v23, v23, s65, 1\n\t" \
    "v_min_u32 v23, 63, v23\n\t" \
    "v_mul_u32_u24 v23, v23, v14\n\t" \
    "v_xor_b32 v23, v23, v7\n\t" \
    "v_lshrrev_b32 v24, 15, v15\n\t" \
    "v_add3_u32 v24, v24, s65, 1\n\t" \
    "v_min_u32 v24, 63, v24\n\t" \
    "v_mul_u32_u24 v24, v24, v15\n\t" \
    "v_xor_b32 v24, v24, v8\n\t" \
    "v_lshrrev_b32 v25, 15, v16\n\t" \
    "v_add3_u32 v25, v25, s65, 1\n\t" \
    "v_min_u32 v25, 63, v25\n\t" \
    "v_mul_u32_u24 v25, v25, v16\n\t" \
    "v_xor_b32 v25, v25, v9\n\t" \
    "v_lshrrev_b32 v26, 15, v17\n\t" \
    "v_add3_u32 v26, v26, s65, 1\n\t" \
    "v_min_u32 v26, 63, v26\n\t" \
    "v_mul_u32_u24 v26, v26, v17\n\t" \
    "v_xor_b32 v26, v26, v10\n\t" \
    "s_add_u32 s71, s79, s96\n\t" \
    "s_sub_u32 s70, s71, s74\n\t" \
    "s_max_i32 s70, s70, 0\n\t" \
    "s_min_i32 s70, s70, 16000\n\t" \
    "s_lshl_b32 s70, s70, 1\n\t" \
    "s_mul_hi_u32 s70, s70, s77\n\t" \
    "s_add_u32 s70, s70, s73\n\t" \
    "s_getpc_b64 s[30:31]\n\t" \
    "hh_Wp23_%=:\n\t" \
    "s_add_u32 s30, s30, hh_We24_%=-hh_Wp23_%=\n\t" \
    "s_addc_u32 s31, s31, 0\n\t" \
    "s_sub_u32 s70, s70, 96\n\t" \
    "hh_W22_%=:\n\t" \
    "s_memtime s[56:57]\n\t" \
    "s_waitcnt lgkmcnt(0)\n\t" \
    "s_sub_u32 s69, s70, s56\n\t" \
    "s_cmp_gt_i32 s69, 80\n\t" \
    "s_cbranch_scc1 hh_W22_%=\n\t" \
    "s_max_i32 s69, s69, 0\n\t" \
    "s_and_b32 s69, s69, -4\n\t" \
    "s_sub_u32 s30, s30, s69\n\t" \
    "s_subb_u32 s31, s31, 0\n\t" \
    "s_setpc_b64 s[30:31]\n\t" \
    "s_nop 0\n\t" \
    "s_nop 0\n\t" \
    "s_nop 0\n\t" \
    "s_nop 0\n\t" \
    "s_nop 0\n\t" \
    "s_nop 0\n\t" \
    "s_nop 0\n\t" \
    "s_nop 0\n\t" \
    "s_nop 0\n\t" \
    "s_nop 0\n\t" \
    "s_nop 0\n\t" \
    "s_nop 0\n\t" \
    "s_nop 0\n\t" \
    "s_nop 0\n\t" \
    "s_nop 0\n\t" \
    "s_nop 0\n\t" \
    "s_nop 0\n\t" \
    "s_nop 0\n\t" \
    "s_nop 0\n\t" \
    "s_nop 0\n\t" \
    "hh_We24_%=:\n\t" \
    "global_load_lds_dword v26, s[36:37] sc1\n\t" \
    "s_sleep 2\n\t" \
    "s_nop 5\n\t" \
    "global_load_lds_dword v25, s[36:37] sc1\n\t" \
    "s_sleep 2\n\t" \
    "s_nop 5\n\t" \
    "global_load_lds_dword v24, s[36:37] sc1\n\t" \
    "s_sleep 2\n\t" \
    "s_nop 5\n\t" \
    "global_load_lds_dword v23, s[36:37] sc1\n\t" \
    "s_sleep 2\n\t" \
    "s_nop 5\n\t" \
    "global_load_lds_dword v22, s[36:37] sc1\n\t" \
    "s_sleep 2\n\t" \
    "s_nop 5\n\t" \
    "global_load_lds_dword v21, s[36:37] sc1\n\t" \
    "s_sleep 2\n\t" \
    "s_nop 5\n\t" \
    "global_load_lds_dword v20, s[36:37] sc1\n\t" \
    "s_branch hh_loop8_%=\n\t" \
    "hh_end10_%=:\n\t" \
    "s_waitcnt vmcnt(0)\n\t" \
    "s_endpgm\n\t" \
    "hh_last_%=:\n\t" \
    ""
#define ASM_GX \
    "s_load_dwordx2 s[42:43], %3, 48\n\t" \
    "s_load_dwordx2 s[44:45], %3, 40\n\t" \
    "s_load_dwordx2 s[36:37], %3, 0x58\n\t" \
    "s_load_dwordx2 s[50:51], %3, 0x48\n\t" \
    "s_mov_b32 s66, %4\n\t" \
    "s_mov_b32 s67, %5\n\t" \
    "s_mov_b32 s68, 262144\n\t" \
    "s_mov_b32 s72, 0\n\t" \
    "s_mov_b32 s83, -1\n\t" \
    "s_mov_b32 s65, 0\n\t" \
    "s_lshl_b32 s82, s66, 10\n\t" \
    "s_waitcnt lgkmcnt(0)\n\t" \
    "s_mul_i32 s89, s66, 9216\n\t" \
    "s_mov_b32 s90, 0\n\t" \
    "s_add_u32 s46, s42, 2097152\n\t" \
    "s_addc_u32 s47, s43, 0\n\t" \
    "s_add_u32 m0, s82, 65536\n\t" \
    "s_nop 0\n\t" \
    "global_load_lds_dwordx4 v200, s[46:47]\n\t" \
    "s_add_u32 s46, s42, 2228224\n\t" \
    "s_addc_u32 s47, s43, 0\n\t" \
    "s_add_u32 m0, s82, 69632\n\t" \
    "s_nop 0\n\t" \
    "global_load_lds_dwordx4 v200, s[46:47]\n\t" \
    "s_add_u32 s46, s42, 2359296\n\t" \
    "s_addc_u32 s47, s43, 0\n\t" \
    "s_add_u32 m0, s82, 73728\n\t" \
    "s_nop 0\n\t" \
    "global_load_lds_dwordx4 v200, s[46:47]\n\t" \
    "s_add_u32 s46, s42, 2490368\n\t" \
    "s_addc_u32 s47, s43, 0\n\t" \
    "s_add_u32 m0, s82, 77824\n\t" \
    "s_nop 0\n\t" \
    "global_load_lds_dwordx4 v200, s[46:47]\n\t" \
    "s_add_u32 s46, s42, 2621440\n\t" \
    "s_addc_u32 s47, s43, 0\n\t" \
    "s_add_u32 m0, s82, 81920\n\t" \
    "s_nop 0\n\t" \
    "global_load_lds_dwordx4 v200, s[46:47]\n\t" \
    "s_add_u32 s46, s42, 2752512\n\t" \
    "s_addc_u32 s47, s43, 0\n\t" \
    "s_add_u32 m0, s82, 86016\n\t" \
    "s_nop 0\n\t" \
    "global_load_lds_dwordx4 v200, s[46:47]\n\t" \
    "s_add_u32 s46, s42, 2883584\n\t" \
    "s_addc_u32 s47, s43, 0\n\t" \
    "s_add_u32 m0, s82, 90112\n\t" \
    "s_nop 0\n\t" \
    "global_load_lds_dwordx4 v200, s[46:47]\n\t" \
    "s_add_u32 s46, s42, 3014656\n\t" \
    "s_addc_u32 s47, s43, 0\n\t" \
    "s_add_u32 m0, s82, 94208\n\t" \
    "s_nop 0\n\t" \
    "global_load_lds_dwordx4 v200, s[46:47]\n\t" \
    "s_add_u32 s46, s42, 3145728\n\t" \
    "s_addc_u32 s47, s43, 0\n\t" \
    "s_add_u32 m0, s82, 98304\n\t" \
    "s_nop 0\n\t" \
    "global_load_lds_dwordx4 v200, s[46:47]\n\t" \
    "s_add_u32 s46, s42, 3276800\n\t" \
    "s_addc_u32 s47, s43, 0\n\t" \
    "s_add_u32 m0, s82, 102400\n\t" \
    "s_nop 0\n\t" \
    "global_load_lds_dwordx4 v200, s[46:47]\n\t" \
    "s_add_u32 s46, s42, 3407872\n\t" \
    "s_addc_u32 s47, s43, 0\n\t" \
    "s_add_u32 m0, s82, 106496\n\t" \
    "s_nop 0\n\t" \
    "global_load_lds_dwordx4 v200, s[46:47]\n\t" \
    "s_add_u32 s46, s42, 3538944\n\t" \
    "s_addc_u32 s47, s43, 0\n\t" \
    "s_add_u32 m0, s82, 110592\n\t" \
    "s_nop 0\n\t" \
    "global_load_lds_dwordx4 v200, s[46:47]\n\t" \
    "s_add_u32 s46, s42, 3670016\n\t" \
    "s_addc_u32 s47, s43, 0\n\t" \
    "s_add_u32 m0, s82, 114688\n\t" \
    "s_nop 0\n\t" \
    "global_load_lds_dwordx4 v200, s[46:47]\n\t" \
    "s_add_u32 s46, s42, 3801088\n\t" \
    "s_addc_u32 s47, s43, 0\n\t" \
    "s_add_u32 m0, s82, 118784\n\t" \
    "s_nop 0\n\t" \
    "global_load_lds_dwordx4 v200, s[46:47]\n\t" \
    "s_add_u32 s46, s42, 3932160\n\t" \
    "s_addc_u32 s47, s43, 0\n\t" \
    "s_add_u32 m0, s82, 122880\n\t" \
    "s_nop 0\n\t" \
    "global_load_lds_dwordx4 v200, s[46:47]\n\t" \
    "s_add_u32 s46, s42, 4063232\n\t" \
    "s_addc_u32 s47, s43, 0\n\t" \
    "s_add_u32 m0, s82, 126976\n\t" \
    "s_nop 0\n\t" \
    "global_load_lds_dwordx4 v200, s[46:47]\n\t" \
    "s_add_u32 s46, s42, 4194304\n\t" \
    "s_addc_u32 s47, s43, 0\n\t" \
    "s_add_u32 m0, s82, 131072\n\t" \
    "s_nop 0\n\t" \
    "global_load_lds_dwordx4 v200, s[46:47]\n\t" \
    "s_add_u32 s46, s42, 4325376\n\t" \
    "s_addc_u32 s47, s43, 0\n\t" \
    "s_add_u32 m0, s82, 135168\n\t" \
    "s_nop 0\n\t" \
    "global_load_lds_dwordx4 v200, s[46:47]\n\t" \
    "s_add_u32 s46, s42, 4456448\n\t" \
    "s_addc_u32 s47, s43, 0\n\t" \
    "s_add_u32 m0, s82, 139264\n\t" \
    "s_nop 0\n\t" \
    "global_load_lds_dwordx4 v200, s[46:47]\n\t" \
    "s_add_u32 s46, s42, 4587520\n\t" \
    "s_addc_u32 s47, s43, 0\n\t" \
    "s_add_u32 m0, s82, 143360\n\t" \
    "s_nop 0\n\t" \
    "global_load_lds_dwordx4 v200, s[46:47]\n\t" \
    "global_load_dwordx4 v[120:123], v203, s[50:51]\n\t" \
    "global_load_dwordx4 v[124:127], v204, s[50:51]\n\t" \
    "global_load_dwordx4 v[128:131], v205, s[50:51]\n\t" \
    "global_load_dwordx4 v[132:135], v206, s[50:51]\n\t" \
    "global_load_dwordx4 v[136:139], v207, s[50:51]\n\t" \
    "global_load_dwordx4 v[140:143], v208, s[50:51]\n\t" \
    "global_load_dwordx4 v[144:147], v209, s[50:51]\n\t" \
    "global_load_dwordx4 v[148:151], v210, s[50:51]\n\t" \
    "global_load_dwordx4 v[152:155], v211, s[50:51]\n\t" \
    "s_add_u32 s52, s50, 36864\n\t" \
    "s_addc_u32 s53, s51, 0\n\t" \
    "global_load_dwordx4 v[72:75], v203, s[52:53]\n\t" \
    "global_load_dwordx4 v[76:79], v204, s[52:53]\n\t" \
    "global_load_dwordx4 v[80:83], v205, s[52:53]\n\t" \
    "global_load_dwordx4 v[84:87], v206, s[52:53]\n\t" \
    "global_load_dwordx4 v[88:91], v207, s[52:53]\n\t" \
    "global_load_dwordx4 v[92:95], v208, s[52:53]\n\t" \
    "global_load_dwordx4 v[96:99], v209, s[52:53]\n\t" \
    "global_load_dwordx4 v[100:103], v210, s[52:53]\n\t" \
    "global_load_dwordx4 v[104:107], v211, s[52:53]\n\t" \
    "s_waitcnt vmcnt(50) lgkmcnt(0)\n\t" \
    "s_barrier\n\t" \
    "ds_read_b64_tr_b16 a[144:145], v201 offset:0\n\t" \
    "ds_read_b64_tr_b16 a[146:147], v201 offset:1024\n\t" \
    "ds_read_b64_tr_b16 a[148:149], v201 offset:8192\n\t" \
    "ds_read_b64_tr_b16 a[150:151], v201 offset:9216\n\t" \
    "ds_read_b64_tr_b16 a[216:217], v202 offset:0\n\t" \
    "ds_read_b64_tr_b16 a[218:219], v202 offset:1024\n\t" \
    "ds_read_b64_tr_b16 a[220:221], v202 offset:8192\n\t" \
    "ds_read_b64_tr_b16 a[222:223], v202 offset:9216\n\t" \
    "s_waitcnt vmcnt(46) lgkmcnt(0)\n\t" \
    "s_barrier\n\t" \
    "ds_read_b64_tr_b16 a[152:153], v201 offset:16384\n\t" \
    "ds_read_b64_tr_b16 a[154:155], v201 offset:17408\n\t" \
    "ds_read_b64_tr_b16 a[156:157], v201 offset:24576\n\t" \
    "ds_read_b64_tr_b16 a[158:159], v201 offset:25600\n\t" \
    "ds_read_b64_tr_b16 a[224:225], v202 offset:16384\n\t" \
    "ds_read_b64_tr_b16 a[226:227], v202 offset:17408\n\t" \
    "ds_read_b64_tr_b16 a[228:229], v202 offset:24576\n\t" \
    "ds_read_b64_tr_b16 a[230:231], v202 offset:25600\n\t" \
    "s_waitcnt vmcnt(42) lgkmcnt(0)\n\t" \
    "s_barrier\n\t" \
    "ds_read_b64_tr_b16 a[160:161], v201 offset:32768\n\t" \
    "ds_read_b64_tr_b16 a[162:163], v201 offset:33792\n\t" \
    "ds_read_b64_tr_b16 a[164:165], v201 offset:40960\n\t" \
    "ds_read_b64_tr_b16 a[166:167], v201 offset:41984\n\t" \
    "ds_read_b64_tr_b16 a[232:233], v202 offset:32768\n\t" \
    "ds_read_b64_tr_b16 a[234:235], v202 offset:33792\n\t" \
    "ds_read_b64_tr_b16 a[236:237], v202 offset:40960\n\t" \
    "ds_read_b64_tr_b16 a[238:239], v202 offset:41984\n\t" \
    "s_waitcnt vmcnt(38) lgkmcnt(0)\n\t" \
    "s_barrier\n\t" \
    "ds_read_b64_tr_b16 a[168:169], v201 offset:49152\n\t" \
    "ds_read_b64_tr_b16 a[170:171], v201 offset:50176\n\t" \
    "ds_read_b64_tr_b16 a[172:173], v201 offset:57344\n\t" \
    "ds_read_b64_tr_b16 a[174:175], v201 offset:58368\n\t" \
    "ds_read_b64_tr_b16 a[240:241], v202 offset:49152\n\t" \
    "ds_read_b64_tr_b16 a[242:243], v202 offset:50176\n\t" \
    "ds_read_b64_tr_b16 a[244:245], v202 offset:57344\n\t" \
    "ds_read_b64_tr_b16 a[246:247], v202 offset:58368\n\t" \
    "v_mbcnt_lo_u32_b32 v164, -1, 0\n\t" \
    "v_mbcnt_hi_u32_b32 v164, -1, v164\n\t" \
    "v_lshlrev_b32 v164, 6, v164\n\t" \
    "v_add_u32 v164, 6225920, v164\n\t" \
    "v_mov_b32 v165, 6234112\n\t" \
    "global_load_dword v165, v165, s[36:37] sc1\n\t" \
    "global_load_dwordx2 v[166:167], v164, s[36:37] sc1\n\t" \
    "global_load_dword v168, v164, s[36:37] offset:2048 sc1\n\t" \
    "s_waitcnt vmcnt(37) lgkmcnt(0)\n\t" \
    "s_barrier\n\t" \
    "v_add_u32 v201, 0x10000, v201\n\t" \
    "v_add_u32 v202, 0x10000, v202\n\t" \
    "ds_read_b64_tr_b16 a[176:177], v201 offset:0\n\t" \
    "ds_read_b64_tr_b16 a[178:179], v201 offset:1024\n\t" \
    "ds_read_b64_tr_b16 a[180:181], v201 offset:8192\n\t" \
    "ds_read_b64_tr_b16 a[182:183], v201 offset:9216\n\t" \
    "ds_read_b64_tr_b16 a[248:249], v202 offset:0\n\t" \
    "ds_read_b64_tr_b16 a[250:251], v202 offset:1024\n\t" \
    "ds_read_b64_tr_b16 a[252:253], v202 offset:8192\n\t" \
    "ds_read_b64_tr_b16 a[254:255], v202 offset:9216\n\t" \
    "s_waitcnt vmcnt(33) lgkmcnt(0)\n\t" \
    "s_barrier\n\t" \
    "ds_read_b64_tr_b16 a[184:185], v201 offset:16384\n\t" \
    "ds_read_b64_tr_b16 a[186:187], v201 offset:17408\n\t" \
    "ds_read_b64_tr_b16 a[188:189], v201 offset:24576\n\t" \
    "ds_read_b64_tr_b16 a[190:191], v201 offset:25600\n\t" \
    "ds_read_b64_tr_b16 v[224:225], v202 offset:16384\n\t" \
    "ds_read_b64_tr_b16 v[226:227], v202 offset:17408\n\t" \
    "ds_read_b64_tr_b16 v[228:229], v202 offset:24576\n\t" \
    "ds_read_b64_tr_b16 v[230:231], v202 offset:25600\n\t" \
    "s_waitcnt vmcnt(29) lgkmcnt(0)\n\t" \
    "s_barrier\n\t" \
    "ds_read_b64_tr_b16 a[192:193], v201 offset:32768\n\t" \
    "ds_read_b64_tr_b16 a[194:195], v201 offset:33792\n\t" \
    "ds_read_b64_tr_b16 a[196:197], v201 offset:40960\n\t" \
    "ds_read_b64_tr_b16 a[198:199], v201 offset:41984\n\t" \
    "ds_read_b64_tr_b16 v[232:233], v202 offset:32768\n\t" \
    "ds_read_b64_tr_b16 v[234:235], v202 offset:33792\n\t" \
    "ds_read_b64_tr_b16 v[236:237], v202 offset:40960\n\t" \
    "ds_read_b64_tr_b16 v[238:239], v202 offset:41984\n\t" \
    "s_waitcnt vmcnt(25) lgkmcnt(0)\n\t" \
    "s_barrier\n\t" \
    "ds_read_b64_tr_b16 a[200:201], v201 offset:49152\n\t" \
    "ds_read_b64_tr_b16 a[202:203], v201 offset:50176\n\t" \
    "ds_read_b64_tr_b16 a[204:205], v201 offset:57344\n\t" \
    "ds_read_b64_tr_b16 a[206:207], v201 offset:58368\n\t" \
    "ds_read_b64_tr_b16 v[240:241], v202 offset:49152\n\t" \
    "ds_read_b64_tr_b16 v[242:243], v202 offset:50176\n\t" \
    "ds_read_b64_tr_b16 v[244:245], v202 offset:57344\n\t" \
    "ds_read_b64_tr_b16 v[246:247], v202 offset:58368\n\t" \
    "s_waitcnt vmcnt(21) lgkmcnt(0)\n\t" \
    "s_barrier\n\t" \
    "v_add_u32 v201, 0x10000, v201\n\t" \
    "v_add_u32 v202, 0x10000, v202\n\t" \
    "ds_read_b64_tr_b16 a[208:209], v201 offset:0\n\t" \
    "ds_read_b64_tr_b16 a[210:211], v201 offset:1024\n\t" \
    "ds_read_b64_tr_b16 a[212:213], v201 offset:8192\n\t" \
    "ds_read_b64_tr_b16 a[214:215], v201 offset:9216\n\t" \
    "ds_read_b64_tr_b16 v[248:249], v202 offset:0\n\t" \
    "ds_read_b64_tr_b16 v[250:251], v202 offset:1024\n\t" \
    "ds_read_b64_tr_b16 v[252:253], v202 offset:8192\n\t" \
    "ds_read_b64_tr_b16 v[254:255], v202 offset:9216\n\t" \
    "v_add_u32 v201, 0xfffe0000, v201\n\t" \
    "v_add_u32 v202, 0xfffe0000, v202\n\t" \
    "s_waitcnt lgkmcnt(0)\n\t" \
    "s_barrier\n\t" \
    "s_waitcnt vmcnt(0)\n\t" \
    "ds_write_b128 v3, v[120:123] offset:0\n\t" \
    "ds_write_b128 v3, v[124:127] offset:1024\n\t" \
    "ds_write_b128 v3, v[128:131] offset:2048\n\t" \
    "ds_write_b128 v3, v[132:135] offset:3072\n\t" \
    "ds_write_b128 v3, v[136:139] offset:4096\n\t" \
    "ds_write_b128 v3, v[140:143] offset:5120\n\t" \
    "ds_write_b128 v3, v[144:147] offset:6144\n\t" \
    "ds_write_b128 v3, v[148:151] offset:7168\n\t" \
    "ds_write_b128 v3, v[152:155] offset:8192\n\t" \
    "ds_write_b128 v3, v[72:75] offset:36864\n\t" \
    "ds_write_b128 v3, v[76:79] offset:37888\n\t" \
    "ds_write_b128 v3, v[80:83] offset:38912\n\t" \
    "ds_write_b128 v3, v[84:87] offset:39936\n\t" \
    "ds_write_b128 v3, v[88:91] offset:40960\n\t" \
    "ds_write_b128 v3, v[92:95] offset:41984\n\t" \
    "ds_write_b128 v3, v[96:99] offset:43008\n\t" \
    "ds_write_b128 v3, v[100:103] offset:44032\n\t" \
    "ds_write_b128 v3, v[104:107] offset:45056\n\t" \
    "gx_C1_%=:\n\t" \
    "v_cmp_ne_u32 s[54:55], v166, v168\n\t" \
    "v_sub_u32 v166, v166, v165\n\t" \
    "v_cmp_ne_u32 vcc, 1, v166\n\t" \
    "s_nop 1\n\t" \
    "s_or_b64 vcc, vcc, s[54:55]\n\t" \
    "s_cbranch_scc0 gx_Cok2_%=\n\t" \
    "s_cmp_eq_u32 s68, 0\n\t" \
    "s_cbranch_scc1 gx_F4_%=\n\t" \
    "s_sub_u32 s68, s68, 1\n\t" \
    "s_sleep 6\n\t" \
    "s_branch gx_Cag3_%=\n\t" \
    "gx_F4_%=:\n\t" \
    "v_mov_b32 v168, 5767424\n\t" \
    "v_mov_b32 v169, 4\n\t" \
    "global_store_dword v168, v169, s[36:37] sc1\n\t" \
    "s_waitcnt vmcnt(0)\n\t" \
    "s_branch gx_Cok2_%=\n\t" \
    "gx_Cag3_%=:\n\t" \
    "global_load_dwordx2 v[166:167], v164, s[36:37] sc1\n\t" \
    "global_load_dword v168, v164, s[36:37] offset:2048 sc1\n\t" \
    "s_waitcnt vmcnt(0)\n\t" \
    "s_branch gx_C1_%=\n\t" \
    "gx_Cok2_%=:\n\t" \
    "v_readlane_b32 s69, v167, 2\n\t" \
    "v_readlane_b32 s70, v167, 3\n\t" \
    "s_nop 1\n\t" \
    "s_cmp_lt_u32 s70, s69\n\t" \
    "s_cselect_b32 s69, 0, 8192\n\t" \
    "s_add_u32 s100, s36, s69\n\t" \
    "s_addc_u32 s101, s37, 0\n\t" \
    "s_cmp_lg_u32 s66, 0\n\t" \
    "s_cbranch_scc1 gx_Ca5_%=\n\t" \
    "v_mov_b32 v164, 5768192\n\t" \
    "s_mov_b64 exec, 1\n\t" \
    "global_atomic_add v164, v166, s[36:37] sc1\n\t" \
    "s_mov_b64 exec, -1\n\t" \
    "gx_Ca5_%=:\n\t" \
    "s_waitcnt lgkmcnt(0)\n\t" \
    "s_barrier\n\t" \
    "ds_read_b128 v[24:27], v1 offset:0\n\t" \
    "ds_read_b128 v[28:31], v1 offset:18432\n\t" \
    "ds_read_b128 v[32:35], v2 offset:0\n\t" \
    "ds_read_b128 v[36:39], v2 offset:18432\n\t" \
    "ds_read_b128 v[40:43], v1 offset:128\n\t" \
    "ds_read_b128 v[44:47], v1 offset:18560\n\t" \
    "gx_l16_%=:\n\t" \
    "s_add_u32 s84, s65, 2\n\t" \
    "s_min_u32 s69, s84, 63\n\t" \
    "s_mul_i32 s69, s69, 36864\n\t" \
    "s_add_u32 s52, s50, s69\n\t" \
    "s_addc_u32 s53, s51, 0\n\t" \
    "s_add_u32 s90, s90, 36864\n\t" \
    "s_cmp_eq_u32 s90, 147456\n\t" \
    "s_cselect_b32 s90, 0, s90\n\t" \
    "s_add_u32 s91, s90, 36864\n\t" \
    "s_cmp_eq_u32 s91, 147456\n\t" \
    "s_cselect_b32 s91, 0, s91\n\t" \
    "s_add_u32 s91, s91, s89\n\t" \
    "v_add_u32 v22, s90, v0\n\t" \
    "v_xor_b32 v23, 64, v22\n\t" \
    "s_mul_i32 s70, s67, 7\n\t" \
    "s_add_u32 s69, s65, 0\n\t" \
    "s_xor_b32 s69, s69, s70\n\t" \
    "s_and_b32 s69, s69, 63\n\t" \
    "s_lshl_b32 s69, s69, 13\n\t" \
    "s_mul_i32 s70, s67, 524288\n\t" \
    "s_add_u32 s69, s69, s70\n\t" \
    "s_add_u32 s69, s69, 23068672\n\t" \
    "s_add_u32 s40, s36, s69\n\t" \
    "s_addc_u32 s41, s37, 0\n\t" \
    "s_waitcnt lgkmcnt(5)\n\t" \
    "v_mfma_f32_16x16x32_bf16 v[72:75], a[144:147], v[24:27], 0\n\t" \
    "ds_read_b128 v[48:51], v2 offset:128\n\t" \
    "s_waitcnt lgkmcnt(5)\n\t" \
    "v_mfma_f32_16x16x32_bf16 v[76:79], a[144:147], v[28:31], 0\n\t" \
    "ds_read_b128 v[52:55], v2 offset:18560\n\t" \
    "v_mfma_f32_16x16x32_bf16 v[80:83], a[216:219], v[24:27], 0\n\t" \
    "s_add_u32 m0, s91, 0\n\t" \
    "v_mfma_f32_16x16x32_bf16 v[84:87], a[216:219], v[28:31], 0\n\t" \
    "global_load_lds_dwordx4 v203, s[52:53]\n\t" \
    "s_waitcnt lgkmcnt(5)\n\t" \
    "v_mfma_f32_16x16x32_bf16 v[72:75], a[148:151], v[32:35], v[72:75]\n\t" \
    "ds_read_b128 v[56:59], v1 offset:256\n\t" \
    "s_waitcnt lgkmcnt(5)\n\t" \
    "v_mfma_f32_16x16x32_bf16 v[76:79], a[148:151], v[36:39], v[76:79]\n\t" \
    "ds_read_b128 v[60:63], v1 offset:18688\n\t" \
    "v_mfma_f32_16x16x32_bf16 v[80:83], a[220:223], v[32:35], v[80:83]\n\t" \
    "s_add_u32 m0, s91, 1024\n\t" \
    "v_mfma_f32_16x16x32_bf16 v[84:87], a[220:223], v[36:39], v[84:87]\n\t" \
    "global_load_lds_dwordx4 v204, s[52:53]\n\t" \
    "s_waitcnt lgkmcnt(5)\n\t" \
    "v_mfma_f32_16x16x32_bf16 v[72:75], a[152:155], v[40:43], v[72:75]\n\t" \
    "ds_read_b128 v[64:67], v2 offset:256\n\t" \
    "s_waitcnt lgkmcnt(5)\n\t" \
    "v_mfma_f32_16x16x32_bf16 v[76:79], a[152:155], v[44:47], v[76:79]\n\t" \
    "ds_read_b128 v[68:71], v2 offset:18688\n\t" \
    "v_mfma_f32_16x16x32_bf16 v[80:83], a[224:227], v[40:43], v[80:83]\n\t" \
    "s_add_u32 m0, s91, 2048\n\t" \
    "v_mfma_f32_16x16x32_bf16 v[84:87], a[224:227], v[44:47], v[84:87]\n\t" \
    "global_load_lds_dwordx4 v205, s[52:53]\n\t" \
    "s_waitcnt lgkmcnt(5)\n\t" \
    "v_mfma_f32_16x16x32_bf16 v[72:75], a[156:159], v[48:51], v[72:75]\n\t" \
    "ds_read_b128 v[24:27], v1 offset:384\n\t" \
    "s_waitcnt lgkmcnt(5)\n\t" \
    "v_mfma_f32_16x16x32_bf16 v[76:79], a[156:159], v[52:55], v[76:79]\n\t" \
    "ds_read_b128 v[28:31], v1 offset:18816\n\t" \
    "v_mfma_f32_16x16x32_bf16 v[80:83], a[228:231], v[48:51], v[80:83]\n\t" \
    "s_add_u32 m0, s91, 3072\n\t" \
    "v_mfma_f32_16x16x32_bf16 v[84:87], a[228:231], v[52:55], v[84:87]\n\t" \
    "global_load_lds_dwordx4 v206, s[52:53]\n\t" \
    "s_waitcnt lgkmcnt(5)\n\t" \
    "v_mfma_f32_16x16x32_bf16 v[72:75], a[160:163], v[56:59], v[72:75]\n\t" \
    "ds_read_b128 v[32:35], v2 offset:384\n\t" \
    "s_waitcnt lgkmcnt(5)\n\t" \
    "v_mfma_f32_16x16x32_bf16 v[76:79], a[160:163], v[60:63], v[76:79]\n\t" \
    "ds_read_b128 v[36:39], v2 offset:18816\n\t" \
    "v_mfma_f32_16x16x32_bf16 v[80:83], a[232:235], v[56:59], v[80:83]\n\t" \
    "s_add_u32 m0, s91, 4096\n\t" \
    "v_mfma_f32_16x16x32_bf16 v[84:87], a[232:235], v[60:63], v[84:87]\n\t" \
    "global_load_lds_dwordx4 v207, s[52:53]\n\t" \
    "s_waitcnt lgkmcnt(5)\n\t" \
    "v_mfma_f32_16x16x32_bf16 v[72:75], a[164:167], v[64:67], v[72:75]\n\t" \
    "ds_read_b128 v[40:43], v1 offset:512\n\t" \
    "s_waitcnt lgkmcnt(5)\n\t" \
    "v_mfma_f32_16x16x32_bf16 v[76:79], a[164:167], v[68:71], v[76:79]\n\t" \
    "ds_read_b128 v[44:47], v1 offset:18944\n\t" \
    "v_mfma_f32_16x16x32_bf16 v[80:83], a[236:239], v[64:67], v[80:83]\n\t" \
    "s_add_u32 m0, s91, 5120\n\t" \
    "v_mfma_f32_16x16x32_bf16 v[84:87], a[236:239], v[68:71], v[84:87]\n\t" \
    "global_load_lds_dwordx4 v208, s[52:53]\n\t" \
    "s_waitcnt lgkmcnt(5)\n\t" \
    "v_mfma_f32_16x16x32_bf16 v[72:75], a[168:171], v[24:27], v[72:75]\n\t" \
    "ds_read_b128 v[48:51], v2 offset:512\n\t" \
    "s_waitcnt lgkmcnt(5)\n\t" \
    "v_mfma_f32_16x16x32_bf16 v[76:79], a[168:171], v[28:31], v[76:79]\n\t" \
    "ds_read_b128 v[52:55], v2 offset:18944\n\t" \
    "v_mfma_f32_16x16x32_bf16 v[80:83], a[240:243], v[24:27], v[80:83]\n\t" \
    "s_add_u32 m0, s91, 6144\n\t" \
    "v_mfma_f32_16x16x32_bf16 v[84:87], a[240:243], v[28:31], v[84:87]\n\t" \
    "global_load_lds_dwordx4 v209, s[52:53]\n\t" \
    "s_waitcnt lgkmcnt(5)\n\t" \
    "v_mfma_f32_16x16x32_bf16 v[72:75], a[172:175], v[32:35], v[72:75]\n\t" \
    "ds_read_b128 v[56:59], v1 offset:640\n\t" \
    "s_waitcnt lgkmcnt(5)\n\t" \
    "v_mfma_f32_16x16x32_bf16 v[76:79], a[172:175], v[36:39], v[76:79]\n\t" \
    "ds_read_b128 v[60:63], v1 offset:19072\n\t" \
    "v_mfma_f32_16x16x32_bf16 v[80:83], a[244:247], v[32:35], v[80:83]\n\t" \
    "s_add_u32 m0, s91, 7168\n\t" \
    "v_mfma_f32_16x16x32_bf16 v[84:87], a[244:247], v[36:39], v[84:87]\n\t" \
    "global_load_lds_dwordx4 v210, s[52:53]\n\t" \
    "s_waitcnt lgkmcnt(5)\n\t" \
    "v_mfma_f32_16x16x32_bf16 v[72:75], a[176:179], v[40:43], v[72:75]\n\t" \
    "ds_read_b128 v[64:67], v2 offset:640\n\t" \
    "s_waitcnt lgkmcnt(5)\n\t" \
    "v_mfma_f32_16x16x32_bf16 v[76:79], a[176:179], v[44:47], v[76:79]\n\t" \
    "ds_read_b128 v[68:71], v2 offset:19072\n\t" \
    "v_mfma_f32_16x16x32_bf16 v[80:83], a[248:251], v[40:43], v[80:83]\n\t" \
    "s_add_u32 m0, s91, 8192\n\t" \
    "v_mfma_f32_16x16x32_bf16 v[84:87], a[248:251], v[44:47], v[84:87]\n\t" \
    "global_load_lds_dwordx4 v211, s[52:53]\n\t" \
    "s_waitcnt lgkmcnt(5)\n\t" \
    "v_mfma_f32_16x16x32_bf16 v[72:75], a[180:183], v[48:51], v[72:75]\n\t" \
    "ds_read_b128 v[24:27], v1 offset:768\n\t" \
    "s_waitcnt lgkmcnt(5)\n\t" \
    "v_mfma_f32_16x16x32_bf16 v[76:79], a[180:183], v[52:55], v[76:79]\n\t" \
    "ds_read_b128 v[28:31], v1 offset:19200\n\t" \
    "v_mfma_f32_16x16x32_bf16 v[80:83], a[252:255], v[48:51], v[80:83]\n\t" \
    "v_mfma_f32_16x16x32_bf16 v[84:87], a[252:255], v[52:55], v[84:87]\n\t" \
    "s_waitcnt lgkmcnt(5)\n\t" \
    "v_mfma_f32_16x16x32_bf16 v[72:75], a[184:187], v[56:59], v[72:75]\n\t" \
    "ds_read_b128 v[32:35], v2 offset:768\n\t" \
    "s_waitcnt lgkmcnt(5)\n\t" \
    "v_mfma_f32_16x16x32_bf16 v[76:79], a[184:187], v[60:63], v[76:79]\n\t" \
    "ds_read_b128 v[36:39], v2 offset:19200\n\t" \
    "v_mfma_f32_16x16x32_bf16 v[80:83], v[224:227], v[56:59], v[80:83]\n\t" \
    "s_waitcnt vmcnt(11)\n\t" \
    "s_barrier\n\t" \
    "v_mfma_f32_16x16x32_bf16 v[84:87], v[224:227], v[60:63], v[84:87]\n\t" \
    "s_sub_u32 s71, s65, 1\n\t" \
    "s_min_i32 s71, s71, 0\n\t" \
    "s_sub_u32 s69, s65, 2\n\t" \
    "s_max_i32 s71, s71, s69\n\t" \
    "s_cmp_lg_u32 s66, 0\n\t" \
    "s_cbranch_scc1 gx_SF9_%=\n\t" \
    "s_lshl_b32 s69, s67, 7\n\t" \
    "s_add_u32 s69, s69, 3149824\n\t" \
    "s_add_u32 s70, s71, 0\n\t" \
    "v_mov_b32 v156, s69\n\t" \
    "v_mov_b32 v157, s70\n\t" \
    "s_mov_b64 exec, 1\n\t" \
    "global_store_dword v156, v157, s[100:101] sc1\n\t" \
    "s_mov_b64 exec, -1\n\t" \
    "gx_SF9_%=:\n\t" \
    "s_waitcnt lgkmcnt(5)\n\t" \
    "v_mfma_f32_16x16x32_bf16 v[72:75], a[188:191], v[64:67], v[72:75]\n\t" \
    "ds_read_b128 v[40:43], v1 offset:896\n\t" \
    "s_waitcnt lgkmcnt(5)\n\t" \
    "v_mfma_f32_16x16x32_bf16 v[76:79], a[188:191], v[68:71], v[76:79]\n\t" \
    "ds_read_b128 v[44:47], v1 offset:19328\n\t" \
    "v_mfma_f32_16x16x32_bf16 v[80:83], v[228:231], v[64:67], v[80:83]\n\t" \
    "v_mfma_f32_16x16x32_bf16 v[84:87], v[228:231], v[68:71], v[84:87]\n\t" \
    "s_waitcnt lgkmcnt(5)\n\t" \
    "v_mfma_f32_16x16x32_bf16 v[72:75], a[192:195], v[24:27], v[72:75]\n\t" \
    "ds_read_b128 v[48:51], v2 offset:896\n\t" \
    "s_waitcnt lgkmcnt(5)\n\t" \
    "v_mfma_f32_16x16x32_bf16 v[76:79], a[192:195], v[28:31], v[76:79]\n\t" \
    "ds_read_b128 v[52:55], v2 offset:19328\n\t" \
    "v_mfma_f32_16x16x32_bf16 v[80:83], v[232:235], v[24:27], v[80:83]\n\t" \
    "v_mfma_f32_16x16x32_bf16 v[84:87], v[232:235], v[28:31], v[84:87]\n\t" \
    "s_waitcnt lgkmcnt(5)\n\t" \
    "v_mfma_f32_16x16x32_bf16 v[72:75], a[196:199], v[32:35], v[72:75]\n\t" \
    "ds_read_b128 v[56:59], v1 offset:1024\n\t" \
    "s_waitcnt lgkmcnt(5)\n\t" \
    "v_mfma_f32_16x16x32_bf16 v[76:79], a[196:199], v[36:39], v[76:79]\n\t" \
    "ds_read_b128 v[60:63], v1 offset:19456\n\t" \
    "v_mfma_f32_16x16x32_bf16 v[80:83], v[236:239], v[32:35], v[80:83]\n\t" \
    "v_mfma_f32_16x16x32_bf16 v[84:87], v[236:239], v[36:39], v[84:87]\n\t" \
    "s_waitcnt lgkmcnt(5)\n\t" \
    "v_mfma_f32_16x16x32_bf16 v[72:75], a[200:203], v[40:43], v[72:75]\n\t" \
    "ds_read_b128 v[64:67], v2 offset:1024\n\t" \
    "s_waitcnt lgkmcnt(5)\n\t" \
    "v_mfma_f32_16x16x32_bf16 v[76:79], a[200:203], v[44:47], v[76:79]\n\t" \
    "ds_read_b128 v[68:71], v2 offset:19456\n\t" \
    "v_mfma_f32_16x16x32_bf16 v[80:83], v[240:243], v[40:43], v[80:83]\n\t" \
    "v_mfma_f32_16x16x32_bf16 v[84:87], v[240:243], v[44:47], v[84:87]\n\t" \
    "s_waitcnt lgkmcnt(5)\n\t" \
    "v_mfma_f32_16x16x32_bf16 v[72:75], a[204:207], v[48:51], v[72:75]\n\t" \
    "ds_read_b128 v[24:27], v22 offset:0\n\t" \
    "s_waitcnt lgkmcnt(5)\n\t" \
    "v_mfma_f32_16x16x32_bf16 v[76:79], a[204:207], v[52:55], v[76:79]\n\t" \
    "ds_read_b128 v[28:31], v22 offset:18432\n\t" \
    "v_mfma_f32_16x16x32_bf16 v[80:83], v[244:247], v[48:51], v[80:83]\n\t" \
    "v_mfma_f32_16x16x32_bf16 v[84:87], v[244:247], v[52:55], v[84:87]\n\t" \
    "s_waitcnt lgkmcnt(5)\n\t" \
    "v_mfma_f32_16x16x32_bf16 v[72:75], a[208:211], v[56:59], v[72:75]\n\t" \
    "ds_read_b128 v[32:35], v23 offset:0\n\t" \
    "s_waitcnt lgkmcnt(5)\n\t" \
    "v_mfma_f32_16x16x32_bf16 v[76:79], a[208:211], v[60:63], v[76:79]\n\t" \
    "ds_read_b128 v[36:39], v23 offset:18432\n\t" \
    "v_mfma_f32_16x16x32_bf16 v[80:83], v[248:251], v[56:59], v[80:83]\n\t" \
    "v_mfma_f32_16x16x32_bf16 v[84:87], v[248:251], v[60:63], v[84:87]\n\t" \
    "s_waitcnt lgkmcnt(5)\n\t" \
    "v_mfma_f32_16x16x32_bf16 v[72:75], a[212:215], v[64:67], v[72:75]\n\t" \
    "ds_read_b128 v[40:43], v22 offset:128\n\t" \
    "s_waitcnt lgkmcnt(5)\n\t" \
    "v_mfma_f32_16x16x32_bf16 v[76:79], a[212:215], v[68:71], v[76:79]\n\t" \
    "ds_read_b128 v[44:47], v22 offset:18560\n\t" \
    "v_mfma_f32_16x16x32_bf16 v[80:83], v[252:255], v[64:67], v[80:83]\n\t" \
    "v_mfma_f32_16x16x32_bf16 v[84:87], v[252:255], v[68:71], v[84:87]\n\t" \
    "v_mov_b32 v1, v22\n\t" \
    "v_mov_b32 v2, v23\n\t" \
    "v_cvt_pk_bf16_f32 v104, v72, v73\n\t" \
    "v_cvt_pk_bf16_f32 v105, v74, v75\n\t" \
    "s_nop 2\n\t" \
    "v_cvt_pk_bf16_f32 v106, v80, v81\n\t" \
    "v_cvt_pk_bf16_f32 v107, v82, v83\n\t" \
    "global_store_dwordx4 v8, v[104:107], s[40:41] offset:0 sc1\n\t" \
    "v_cvt_pk_bf16_f32 v108, v76, v77\n\t" \
    "v_cvt_pk_bf16_f32 v109, v78, v79\n\t" \
    "v_cvt_pk_bf16_f32 v110, v84, v85\n\t" \
    "v_cvt_pk_bf16_f32 v111, v86, v87\n\t" \
    "global_store_dwordx4 v8, v[108:111], s[40:41] offset:1024 sc1\n\t" \
    "s_cmp_ge_u32 s65, 1\n\t" \
    "s_cbranch_scc1 gx_nofast7_%=\n\t" \
    "s_waitcnt vmcnt(0)\n\t" \
    "s_barrier\n\t" \
    "s_cmp_lg_u32 s66, 0\n\t" \
    "s_cbranch_scc1 gx_SF10_%=\n\t" \
    "s_lshl_b32 s69, s67, 7\n\t" \
    "s_add_u32 s69, s69, 3149824\n\t" \
    "s_add_u32 s70, s65, 0\n\t" \
    "v_mov_b32 v156, s69\n\t" \
    "v_mov_b32 v157, s70\n\t" \
    "s_mov_b64 exec, 1\n\t" \
    "global_store_dword v156, v157, s[100:101] sc1\n\t" \
    "s_mov_b64 exec, -1\n\t" \
    "gx_SF10_%=:\n\t" \
    "gx_nofast7_%=:\n\t" \
    "s_sub_u32 s69, s65, 5\n\t" \
    "s_cmp_gt_u32 s69, 4\n\t" \
    "s_cbranch_scc1 gx_nt11_%=\n\t" \
    "s_lshl_b32 s69, s69, 7\n\t" \
    "s_lshl_b32 s70, s66, 5\n\t" \
    "s_add_u32 s69, s69, s70\n\t" \
    "v_lshrrev_b32 v156, 3, v6\n\t" \
    "v_add_u32 v156, s69, v156\n\t" \
    "v_min_u32 v156, 575, v156\n\t" \
    "v_and_b32 v157, 4, v6\n\t" \
    "v_lshlrev_b32 v157, 5, v157\n\t" \
    "v_lshl_add_u32 v156, v156, 13, v157\n\t" \
    "s_lshl_b32 s69, s67, 8\n\t" \
    "v_add_u32 v156, s69, v156\n\t" \
    "s_mov_b32 m0, 155648\n\t" \
    "s_nop 0\n\t" \
    "global_load_lds_dword v156, s[44:45]\n\t" \
    "gx_nt11_%=:\n\t" \
    "s_add_u32 s65, s65, 1\n\t" \
    "s_cmp_lt_u32 s65, 10\n\t" \
    "s_cbranch_scc1 gx_l16_%=\n\t" \
    "s_waitcnt vmcnt(0) lgkmcnt(0)\n\t" \
    "s_barrier\n\t" \
    "s_cmp_lg_u32 s66, 0\n\t" \
    "s_cbranch_scc1 gx_SF12_%=\n\t" \
    "s_lshl_b32 s69, s67, 7\n\t" \
    "s_add_u32 s69, s69, 3149824\n\t" \
    "s_add_u32 s70, s65, -1\n\t" \
    "v_mov_b32 v156, s69\n\t" \
    "v_mov_b32 v157, s70\n\t" \
    "s_mov_b64 exec, 1\n\t" \
    "global_store_dword v156, v157, s[100:101] sc1\n\t" \
    "s_mov_b64 exec, -1\n\t" \
    "gx_SF12_%=:\n\t" \
    "s_add_u32 s46, s44, 0\n\t" \
    "s_addc_u32 s47, s45, 0\n\t" \
    "s_add_u32 m0, s82, 0\n\t" \
    "s_nop 0\n\t" \
    "global_load_lds_dwordx4 v200, s[46:47]\n\t" \
    "s_add_u32 s46, s44, 131072\n\t" \
    "s_addc_u32 s47, s45, 0\n\t" \
    "s_add_u32 m0, s82, 4096\n\t" \
    "s_nop 0\n\t" \
    "global_load_lds_dwordx4 v200, s[46:47]\n\t" \
    "s_add_u32 s46, s44, 262144\n\t" \
    "s_addc_u32 s47, s45, 0\n\t" \
    "s_add_u32 m0, s82, 8192\n\t" \
    "s_nop 0\n\t" \
    "global_load_lds_dwordx4 v200, s[46:47]\n\t" \
    "s_add_u32 s46, s44, 393216\n\t" \
    "s_addc_u32 s47, s45, 0\n\t" \
    "s_add_u32 m0, s82, 12288\n\t" \
    "s_nop 0\n\t" \
    "global_load_lds_dwordx4 v200, s[46:47]\n\t" \
    "s_add_u32 s46, s44, 524288\n\t" \
    "s_addc_u32 s47, s45, 0\n\t" \
    "s_add_u32 m0, s82, 16384\n\t" \
    "s_nop 0\n\t" \
    "global_load_lds_dwordx4 v200, s[46:47]\n\t" \
    "s_add_u32 s46, s44, 655360\n\t" \
    "s_addc_u32 s47, s45, 0\n\t" \
    "s_add_u32 m0, s82, 20480\n\t" \
    "s_nop 0\n\t" \
    "global_load_lds_dwordx4 v200, s[46:47]\n\t" \
    "s_add_u32 s46, s44, 786432\n\t" \
    "s_addc_u32 s47, s45, 0\n\t" \
    "s_add_u32 m0, s82, 24576\n\t" \
    "s_nop 0\n\t" \
    "global_load_lds_dwordx4 v200, s[46:47]\n\t" \
    "s_add_u32 s46, s44, 917504\n\t" \
    "s_addc_u32 s47, s45, 0\n\t" \
    "s_add_u32 m0, s82, 28672\n\t" \
    "s_nop 0\n\t" \
    "global_load_lds_dwordx4 v200, s[46:47]\n\t" \
    "s_add_u32 s46, s44, 1048576\n\t" \
    "s_addc_u32 s47, s45, 0\n\t" \
    "s_add_u32 m0, s82, 32768\n\t" \
    "s_nop 0\n\t" \
    "global_load_lds_dwordx4 v200, s[46:47]\n\t" \
    "s_add_u32 s46, s44, 1179648\n\t" \
    "s_addc_u32 s47, s45, 0\n\t" \
    "s_add_u32 m0, s82, 36864\n\t" \
    "s_nop 0\n\t" \
    "global_load_lds_dwordx4 v200, s[46:47]\n\t" \
    "s_add_u32 s46, s44, 1310720\n\t" \
    "s_addc_u32 s47, s45, 0\n\t" \
    "s_add_u32 m0, s82, 40960\n\t" \
    "s_nop 0\n\t" \
    "global_load_lds_dwordx4 v200, s[46:47]\n\t" \
    "s_add_u32 s46, s44, 1441792\n\t" \
    "s_addc_u32 s47, s45, 0\n\t" \
    "s_add_u32 m0, s82, 45056\n\t" \
    "s_nop 0\n\t" \
    "global_load_lds_dwordx4 v200, s[46:47]\n\t" \
    "s_add_u32 s46, s44, 1572864\n\t" \
    "s_addc_u32 s47, s45, 0\n\t" \
    "s_add_u32 m0, s82, 49152\n\t" \
    "s_nop 0\n\t" \
    "global_load_lds_dwordx4 v200, s[46:47]\n\t" \
    "s_add_u32 s46, s44, 1703936\n\t" \
    "s_addc_u32 s47, s45, 0\n\t" \
    "s_add_u32 m0, s82, 53248\n\t" \
    "s_nop 0\n\t" \
    "global_load_lds_dwordx4 v200, s[46:47]\n\t" \
    "s_add_u32 s46, s44, 1835008\n\t" \
    "s_addc_u32 s47, s45, 0\n\t" \
    "s_add_u32 m0, s82, 57344\n\t" \
    "s_nop 0\n\t" \
    "global_load_lds_dwordx4 v200, s[46:47]\n\t" \
    "s_add_u32 s46, s44, 1966080\n\t" \
    "s_addc_u32 s47, s45, 0\n\t" \
    "s_add_u32 m0, s82, 61440\n\t" \
    "s_nop 0\n\t" \
    "global_load_lds_dwordx4 v200, s[46:47]\n\t" \
    "s_add_u32 s46, s44, 2097152\n\t" \
    "s_addc_u32 s47, s45, 0\n\t" \
    "s_add_u32 m0, s82, 65536\n\t" \
    "s_nop 0\n\t" \
    "global_load_lds_dwordx4 v200, s[46:47]\n\t" \
    "s_add_u32 s46, s44, 2228224\n\t" \
    "s_addc_u32 s47, s45, 0\n\t" \
    "s_add_u32 m0, s82, 69632\n\t" \
    "s_nop 0\n\t" \
    "global_load_lds_dwordx4 v200, s[46:47]\n\t" \
    "s_add_u32 s46, s44, 2359296\n\t" \
    "s_addc_u32 s47, s45, 0\n\t" \
    "s_add_u32 m0, s82, 73728\n\t" \
    "s_nop 0\n\t" \
    "global_load_lds_dwordx4 v200, s[46:47]\n\t" \
    "s_add_u32 s46, s44, 2490368\n\t" \
    "s_addc_u32 s47, s45, 0\n\t" \
    "s_add_u32 m0, s82, 77824\n\t" \
    "s_nop 0\n\t" \
    "global_load_lds_dwordx4 v200, s[46:47]\n\t" \
    "s_add_u32 s46, s44, 2621440\n\t" \
    "s_addc_u32 s47, s45, 0\n\t" \
    "s_add_u32 m0, s82, 81920\n\t" \
    "s_nop 0\n\t" \
    "global_load_lds_dwordx4 v200, s[46:47]\n\t" \
    "s_add_u32 s46, s44, 2752512\n\t" \
    "s_addc_u32 s47, s45, 0\n\t" \
    "s_add_u32 m0, s82, 86016\n\t" \
    "s_nop 0\n\t" \
    "global_load_lds_dwordx4 v200, s[46:47]\n\t" \
    "s_add_u32 s46, s44, 2883584\n\t" \
    "s_addc_u32 s47, s45, 0\n\t" \
    "s_add_u32 m0, s82, 90112\n\t" \
    "s_nop 0\n\t" \
    "global_load_lds_dwordx4 v200, s[46:47]\n\t" \
    "s_add_u32 s46, s44, 3014656\n\t" \
    "s_addc_u32 s47, s45, 0\n\t" \
    "s_add_u32 m0, s82, 94208\n\t" \
    "s_nop 0\n\t" \
    "global_load_lds_dwordx4 v200, s[46:47]\n\t" \
    "s_add_u32 s46, s44, 3145728\n\t" \
    "s_addc_u32 s47, s45, 0\n\t" \
    "s_add_u32 m0, s82, 98304\n\t" \
    "s_nop 0\n\t" \
    "global_load_lds_dwordx4 v200, s[46:47]\n\t" \
    "s_add_u32 s46, s44, 3276800\n\t" \
    "s_addc_u32 s47, s45, 0\n\t" \
    "s_add_u32 m0, s82, 102400\n\t" \
    "s_nop 0\n\t" \
    "global_load_lds_dwordx4 v200, s[46:47]\n\t" \
    "s_add_u32 s46, s44, 3407872\n\t" \
    "s_addc_u32 s47, s45, 0\n\t" \
    "s_add_u32 m0, s82, 106496\n\t" \
    "s_nop 0\n\t" \
    "global_load_lds_dwordx4 v200, s[46:47]\n\t" \
    "s_add_u32 s46, s44, 3538944\n\t" \
    "s_addc_u32 s47, s45, 0\n\t" \
    "s_add_u32 m0, s82, 110592\n\t" \
    "s_nop 0\n\t" \
    "global_load_lds_dwordx4 v200, s[46:47]\n\t" \
    "s_add_u32 s46, s44, 3670016\n\t" \
    "s_addc_u32 s47, s45, 0\n\t" \
    "s_add_u32 m0, s82, 114688\n\t" \
    "s_nop 0\n\t" \
    "global_load_lds_dwordx4 v200, s[46:47]\n\t" \
    "s_add_u32 s46, s44, 3801088\n\t" \
    "s_addc_u32 s47, s45, 0\n\t" \
    "s_add_u32 m0, s82, 118784\n\t" \
    "s_nop 0\n\t" \
    "global_load_lds_dwordx4 v200, s[46:47]\n\t" \
    "s_add_u32 s46, s44, 3932160\n\t" \
    "s_addc_u32 s47, s45, 0\n\t" \
    "s_add_u32 m0, s82, 122880\n\t" \
    "s_nop 0\n\t" \
    "global_load_lds_dwordx4 v200, s[46:47]\n\t" \
    "s_add_u32 s46, s44, 4063232\n\t" \
    "s_addc_u32 s47, s45, 0\n\t" \
    "s_add_u32 m0, s82, 126976\n\t" \
    "s_nop 0\n\t" \
    "global_load_lds_dwordx4 v200, s[46:47]\n\t" \
    "s_add_u32 s46, s44, 4194304\n\t" \
    "s_addc_u32 s47, s45, 0\n\t" \
    "s_add_u32 m0, s82, 131072\n\t" \
    "s_nop 0\n\t" \
    "global_load_lds_dwordx4 v200, s[46:47]\n\t" \
    "s_add_u32 s46, s44, 4325376\n\t" \
    "s_addc_u32 s47, s45, 0\n\t" \
    "s_add_u32 m0, s82, 135168\n\t" \
    "s_nop 0\n\t" \
    "global_load_lds_dwordx4 v200, s[46:47]\n\t" \
    "s_add_u32 s46, s44, 4456448\n\t" \
    "s_addc_u32 s47, s45, 0\n\t" \
    "s_add_u32 m0, s82, 139264\n\t" \
    "s_nop 0\n\t" \
    "global_load_lds_dwordx4 v200, s[46:47]\n\t" \
    "s_add_u32 s46, s44, 4587520\n\t" \
    "s_addc_u32 s47, s45, 0\n\t" \
    "s_add_u32 m0, s82, 143360\n\t" \
    "s_nop 0\n\t" \
    "global_load_lds_dwordx4 v200, s[46:47]\n\t" \
    "global_load_dwordx4 v[120:123], v203, s[50:51]\n\t" \
    "global_load_dwordx4 v[124:127], v204, s[50:51]\n\t" \
    "global_load_dwordx4 v[128:131], v205, s[50:51]\n\t" \
    "global_load_dwordx4 v[132:135], v206, s[50:51]\n\t" \
    "global_load_dwordx4 v[136:139], v207, s[50:51]\n\t" \
    "global_load_dwordx4 v[140:143], v208, s[50:51]\n\t" \
    "global_load_dwordx4 v[144:147], v209, s[50:51]\n\t" \
    "global_load_dwordx4 v[148:151], v210, s[50:51]\n\t" \
    "global_load_dwordx4 v[152:155], v211, s[50:51]\n\t" \
    "s_add_u32 s52, s50, 368640\n\t" \
    "s_addc_u32 s53, s51, 0\n\t" \
    "global_load_dwordx4 v[72:75], v203, s[52:53]\n\t" \
    "global_load_dwordx4 v[76:79], v204, s[52:53]\n\t" \
    "global_load_dwordx4 v[80:83], v205, s[52:53]\n\t" \
    "global_load_dwordx4 v[84:87], v206, s[52:53]\n\t" \
    "global_load_dwordx4 v[88:91], v207, s[52:53]\n\t" \
    "global_load_dwordx4 v[92:95], v208, s[52:53]\n\t" \
    "global_load_dwordx4 v[96:99], v209, s[52:53]\n\t" \
    "global_load_dwordx4 v[100:103], v210, s[52:53]\n\t" \
    "global_load_dwordx4 v[104:107], v211, s[52:53]\n\t" \
    "s_waitcnt vmcnt(50) lgkmcnt(0)\n\t" \
    "s_barrier\n\t" \
    "ds_read_b64_tr_b16 a[0:1], v201 offset:0\n\t" \
    "ds_read_b64_tr_b16 a[2:3], v201 offset:1024\n\t" \
    "ds_read_b64_tr_b16 a[4:5], v201 offset:8192\n\t" \
    "ds_read_b64_tr_b16 a[6:7], v201 offset:9216\n\t" \
    "ds_read_b64_tr_b16 a[72:73], v202 offset:0\n\t" \
    "ds_read_b64_tr_b16 a[74:75], v202 offset:1024\n\t" \
    "ds_read_b64_tr_b16 a[76:77], v202 offset:8192\n\t" \
    "ds_read_b64_tr_b16 a[78:79], v202 offset:9216\n\t" \
    "s_waitcnt vmcnt(46) lgkmcnt(0)\n\t" \
    "s_barrier\n\t" \
    "ds_read_b64_tr_b16 a[8:9], v201 offset:16384\n\t" \
    "ds_read_b64_tr_b16 a[10:11], v201 offset:17408\n\t" \
    "ds_read_b64_tr_b16 a[12:13], v201 offset:24576\n\t" \
    "ds_read_b64_tr_b16 a[14:15], v201 offset:25600\n\t" \
    "ds_read_b64_tr_b16 a[80:81], v202 offset:16384\n\t" \
    "ds_read_b64_tr_b16 a[82:83], v202 offset:17408\n\t" \
    "ds_read_b64_tr_b16 a[84:85], v202 offset:24576\n\t" \
    "ds_read_b64_tr_b16 a[86:87], v202 offset:25600\n\t" \
    "s_waitcnt vmcnt(42) lgkmcnt(0)\n\t" \
    "s_barrier\n\t" \
    "ds_read_b64_tr_b16 a[16:17], v201 offset:32768\n\t" \
    "ds_read_b64_tr_b16 a[18:19], v201 offset:33792\n\t" \
    "ds_read_b64_tr_b16 a[20:21], v201 offset:40960\n\t" \
    "ds_read_b64_tr_b16 a[22:23], v201 offset:41984\n\t" \
    "ds_read_b64_tr_b16 a[88:89], v202 offset:32768\n\t" \
    "ds_read_b64_tr_b16 a[90:91], v202 offset:33792\n\t" \
    "ds_read_b64_tr_b16 a[92:93], v202 offset:40960\n\t" \
    "ds_read_b64_tr_b16 a[94:95], v202 offset:41984\n\t" \
    "s_waitcnt vmcnt(38) lgkmcnt(0)\n\t" \
    "s_barrier\n\t" \
    "ds_read_b64_tr_b16 a[24:25], v201 offset:49152\n\t" \
    "ds_read_b64_tr_b16 a[26:27], v201 offset:50176\n\t" \
    "ds_read_b64_tr_b16 a[28:29], v201 offset:57344\n\t" \
    "ds_read_b64_tr_b16 a[30:31], v201 offset:58368\n\t" \
    "ds_read_b64_tr_b16 a[96:97], v202 offset:49152\n\t" \
    "ds_read_b64_tr_b16 a[98:99], v202 offset:50176\n\t" \
    "ds_read_b64_tr_b16 a[100:101], v202 offset:57344\n\t" \
    "ds_read_b64_tr_b16 a[102:103], v202 offset:58368\n\t" \
    "s_waitcnt vmcnt(34) lgkmcnt(0)\n\t" \
    "s_barrier\n\t" \
    "v_add_u32 v201, 0x10000, v201\n\t" \
    "v_add_u32 v202, 0x10000, v202\n\t" \
    "ds_read_b64_tr_b16 a[32:33], v201 offset:0\n\t" \
    "ds_read_b64_tr_b16 a[34:35], v201 offset:1024\n\t" \
    "ds_read_b64_tr_b16 a[36:37], v201 offset:8192\n\t" \
    "ds_read_b64_tr_b16 a[38:39], v201 offset:9216\n\t" \
    "ds_read_b64_tr_b16 a[104:105], v202 offset:0\n\t" \
    "ds_read_b64_tr_b16 a[106:107], v202 offset:1024\n\t" \
    "ds_read_b64_tr_b16 a[108:109], v202 offset:8192\n\t" \
    "ds_read_b64_tr_b16 a[110:111], v202 offset:9216\n\t" \
    "s_waitcnt vmcnt(30) lgkmcnt(0)\n\t" \
    "s_barrier\n\t" \
    "ds_read_b64_tr_b16 a[40:41], v201 offset:16384\n\t" \
    "ds_read_b64_tr_b16 a[42:43], v201 offset:17408\n\t" \
    "ds_read_b64_tr_b16 a[44:45], v201 offset:24576\n\t" \
    "ds_read_b64_tr_b16 a[46:47], v201 offset:25600\n\t" \
    "ds_read_b64_tr_b16 a[112:113], v202 offset:16384\n\t" \
    "ds_read_b64_tr_b16 a[114:115], v202 offset:17408\n\t" \
    "ds_read_b64_tr_b16 a[116:117], v202 offset:24576\n\t" \
    "ds_read_b64_tr_b16 a[118:119], v202 offset:25600\n\t" \
    "s_waitcnt vmcnt(26) lgkmcnt(0)\n\t" \
    "s_barrier\n\t" \
    "ds_read_b64_tr_b16 a[48:49], v201 offset:32768\n\t" \
    "ds_read_b64_tr_b16 a[50:51], v201 offset:33792\n\t" \
    "ds_read_b64_tr_b16 a[52:53], v201 offset:40960\n\t" \
    "ds_read_b64_tr_b16 a[54:55], v201 offset:41984\n\t" \
    "ds_read_b64_tr_b16 a[120:121], v202 offset:32768\n\t" \
    "ds_read_b64_tr_b16 a[122:123], v202 offset:33792\n\t" \
    "ds_read_b64_tr_b16 a[124:125], v202 offset:40960\n\t" \
    "ds_read_b64_tr_b16 a[126:127], v202 offset:41984\n\t" \
    "s_waitcnt vmcnt(22) lgkmcnt(0)\n\t" \
    "s_barrier\n\t" \
    "ds_read_b64_tr_b16 a[56:57], v201 offset:49152\n\t" \
    "ds_read_b64_tr_b16 a[58:59], v201 offset:50176\n\t" \
    "ds_read_b64_tr_b16 a[60:61], v201 offset:57344\n\t" \
    "ds_read_b64_tr_b16 a[62:63], v201 offset:58368\n\t" \
    "ds_read_b64_tr_b16 a[128:129], v202 offset:49152\n\t" \
    "ds_read_b64_tr_b16 a[130:131], v202 offset:50176\n\t" \
    "ds_read_b64_tr_b16 a[132:133], v202 offset:57344\n\t" \
    "ds_read_b64_tr_b16 a[134:135], v202 offset:58368\n\t" \
    "s_waitcnt vmcnt(18) lgkmcnt(0)\n\t" \
    "s_barrier\n\t" \
    "v_add_u32 v201, 0x10000, v201\n\t" \
    "v_add_u32 v202, 0x10000, v202\n\t" \
    "ds_read_b64_tr_b16 a[64:65], v201 offset:0\n\t" \
    "ds_read_b64_tr_b16 a[66:67], v201 offset:1024\n\t" \
    "ds_read_b64_tr_b16 a[68:69], v201 offset:8192\n\t" \
    "ds_read_b64_tr_b16 a[70:71], v201 offset:9216\n\t" \
    "ds_read_b64_tr_b16 a[136:137], v202 offset:0\n\t" \
    "ds_read_b64_tr_b16 a[138:139], v202 offset:1024\n\t" \
    "ds_read_b64_tr_b16 a[140:141], v202 offset:8192\n\t" \
    "ds_read_b64_tr_b16 a[142:143], v202 offset:9216\n\t" \
    "v_add_u32 v201, 0xfffe0000, v201\n\t" \
    "v_add_u32 v202, 0xfffe0000, v202\n\t" \
    "s_waitcnt lgkmcnt(0)\n\t" \
    "s_barrier\n\t" \
    "s_waitcnt vmcnt(0)\n\t" \
    "ds_write_b128 v3, v[120:123] offset:0\n\t" \
    "ds_write_b128 v3, v[124:127] offset:1024\n\t" \
    "ds_write_b128 v3, v[128:131] offset:2048\n\t" \
    "ds_write_b128 v3, v[132:135] offset:3072\n\t" \
    "ds_write_b128 v3, v[136:139] offset:4096\n\t" \
    "ds_write_b128 v3, v[140:143] offset:5120\n\t" \
    "ds_write_b128 v3, v[144:147] offset:6144\n\t" \
    "ds_write_b128 v3, v[148:151] offset:7168\n\t" \
    "ds_write_b128 v3, v[152:155] offset:8192\n\t" \
    "ds_write_b128 v3, v[72:75] offset:36864\n\t" \
    "ds_write_b128 v3, v[76:79] offset:37888\n\t" \
    "ds_write_b128 v3, v[80:83] offset:38912\n\t" \
    "ds_write_b128 v3, v[84:87] offset:39936\n\t" \
    "ds_write_b128 v3, v[88:91] offset:40960\n\t" \
    "ds_write_b128 v3, v[92:95] offset:41984\n\t" \
    "ds_write_b128 v3, v[96:99] offset:43008\n\t" \
    "ds_write_b128 v3, v[100:103] offset:44032\n\t" \
    "ds_write_b128 v3, v[104:107] offset:45056\n\t" \
    "s_mov_b32 s65, 0\n\t" \
    "v_mov_b32 v1, v0\n\t" \
    "v_xor_b32 v2, 64, v0\n\t" \
    "s_mov_b32 s90, 0\n\t" \
    "s_add_u32 s91, s89, 0x12000\n\t" \
    "s_add_u32 s85, s91, s89\n\t" \
    "s_add_u32 s52, s50, 36864\n\t" \
    "s_addc_u32 s53, s51, 0\n\t" \
    "s_mul_i32 s69, s67, 8192\n\t" \
    "s_add_u32 s69, s69, 175112192\n\t" \
    "s_add_u32 s42, s36, s69\n\t" \
    "s_addc_u32 s43, s37, 0\n\t" \
    "s_mov_b64 s[40:41], s[42:43]\n\t" \
    "s_waitcnt lgkmcnt(0)\n\t" \
    "s_barrier\n\t" \
    "ds_read_b128 v[24:27], v1 offset:0\n\t" \
    "ds_read_b128 v[28:31], v1 offset:18432\n\t" \
    "ds_read_b128 v[32:35], v2 offset:0\n\t" \
    "ds_read_b128 v[36:39], v2 offset:18432\n\t" \
    "ds_read_b128 v[40:43], v1 offset:128\n\t" \
    "ds_read_b128 v[44:47], v1 offset:18560\n\t" \
    ".p2align 6\n\t" \
    "s_nop 0\n\t" \
    "s_nop 0\n\t" \
    "gx_l28_%=:\n\t" \
    "s_waitcnt lgkmcnt(5)\n\t" \
    "v_mfma_f32_16x16x32_bf16 v[88:91], a[0:3], v[24:27], 0\n\t" \
    "ds_read_b128 v[48:51], v2 offset:128\n\t" \
    "s_waitcnt lgkmcnt(5)\n\t" \
    "v_mfma_f32_16x16x32_bf16 v[92:95], a[0:3], v[28:31], 0\n\t" \
    "ds_read_b128 v[52:55], v2 offset:18560\n\t" \
    "v_mfma_f32_16x16x32_bf16 v[96:99], a[72:75], v[24:27], 0\n\t" \
    "s_add_u32 m0, s91, 0\n\t" \
    "v_mfma_f32_16x16x32_bf16 v[100:103], a[72:75], v[28:31], 0\n\t" \
    "global_load_lds_dwordx4 v203, s[52:53]\n\t" \
    "s_waitcnt lgkmcnt(5)\n\t" \
    "v_mfma_f32_16x16x32_bf16 v[88:91], a[4:7], v[32:35], v[88:91]\n\t" \
    "ds_read_b128 v[56:59], v1 offset:256\n\t" \
    "s_waitcnt lgkmcnt(5)\n\t" \
    "v_mfma_f32_16x16x32_bf16 v[92:95], a[4:7], v[36:39], v[92:95]\n\t" \
    "ds_read_b128 v[60:63], v1 offset:18688\n\t" \
    "v_mfma_f32_16x16x32_bf16 v[96:99], a[76:79], v[32:35], v[96:99]\n\t" \
    "s_add_u32 m0, s91, 1024\n\t" \
    "v_mfma_f32_16x16x32_bf16 v[100:103], a[76:79], v[36:39], v[100:103]\n\t" \
    "global_load_lds_dwordx4 v204, s[52:53]\n\t" \
    "s_waitcnt lgkmcnt(5)\n\t" \
    "v_mfma_f32_16x16x32_bf16 v[88:91], a[8:11], v[40:43], v[88:91]\n\t" \
    "ds_read_b128 v[64:67], v2 offset:256\n\t" \
    "s_waitcnt lgkmcnt(5)\n\t" \
    "v_mfma_f32_16x16x32_bf16 v[92:95], a[8:11], v[44:47], v[92:95]\n\t" \
    "ds_read_b128 v[68:71], v2 offset:18688\n\t" \
    "v_cvt_pk_bf16_f32 v104, v72, v73\n\t" \
    "v_mfma_f32_16x16x32_bf16 v[96:99], a[80:83], v[40:43], v[96:99]\n\t" \
    "s_add_u32 m0, s91, 2048\n\t" \
    "v_cvt_pk_bf16_f32 v105, v74, v75\n\t" \
    "v_mfma_f32_16x16x32_bf16 v[100:103], a[80:83], v[44:47], v[100:103]\n\t" \
    "global_load_lds_dwordx4 v205, s[52:53]\n\t" \
    "s_waitcnt lgkmcnt(5)\n\t" \
    "v_mfma_f32_16x16x32_bf16 v[88:91], a[12:15], v[48:51], v[88:91]\n\t" \
    "ds_read_b128 v[24:27], v1 offset:384\n\t" \
    "s_waitcnt lgkmcnt(5)\n\t" \
    "v_mfma_f32_16x16x32_bf16 v[92:95], a[12:15], v[52:55], v[92:95]\n\t" \
    "ds_read_b128 v[28:31], v1 offset:18816\n\t" \
    "v_cvt_pk_bf16_f32 v106, v80, v81\n\t" \
    "v_mfma_f32_16x16x32_bf16 v[96:99], a[84:87], v[48:51], v[96:99]\n\t" \
    "s_add_u32 m0, s91, 3072\n\t" \
    "v_cvt_pk_bf16_f32 v107, v82, v83\n\t" \
    "v_mfma_f32_16x16x32_bf16 v[100:103], a[84:87], v[52:55], v[100:103]\n\t" \
    "global_load_lds_dwordx4 v206, s[52:53]\n\t" \
    "s_waitcnt lgkmcnt(5)\n\t" \
    "v_mfma_f32_16x16x32_bf16 v[88:91], a[16:19], v[56:59], v[88:91]\n\t" \
    "ds_read_b128 v[32:35], v2 offset:384\n\t" \
    "s_waitcnt lgkmcnt(5)\n\t" \
    "v_mfma_f32_16x16x32_bf16 v[92:95], a[16:19], v[60:63], v[92:95]\n\t" \
    "ds_read_b128 v[36:39], v2 offset:18816\n\t" \
    "global_store_dwordx4 v8, v[104:107], s[40:41] offset:0 sc1\n\t" \
    "v_mfma_f32_16x16x32_bf16 v[96:99], a[88:91], v[56:59], v[96:99]\n\t" \
    "s_add_u32 m0, s91, 4096\n\t" \
    "v_cvt_pk_bf16_f32 v108, v76, v77\n\t" \
    "v_mfma_f32_16x16x32_bf16 v[100:103], a[88:91], v[60:63], v[100:103]\n\t" \
    "global_load_lds_dwordx4 v207, s[52:53]\n\t" \
    "s_waitcnt lgkmcnt(5)\n\t" \
    "v_mfma_f32_16x16x32_bf16 v[88:91], a[20:23], v[64:67], v[88:91]\n\t" \
    "ds_read_b128 v[40:43], v1 offset:512\n\t" \
    "s_waitcnt lgkmcnt(5)\n\t" \
    "v_mfma_f32_16x16x32_bf16 v[92:95], a[20:23], v[68:71], v[92:95]\n\t" \
    "ds_read_b128 v[44:47], v1 offset:18944\n\t" \
    "v_cvt_pk_bf16_f32 v109, v78, v79\n\t" \
    "v_mfma_f32_16x16x32_bf16 v[96:99], a[92:95], v[64:67], v[96:99]\n\t" \
    "s_add_u32 m0, s91, 5120\n\t" \
    "v_cvt_pk_bf16_f32 v110, v84, v85\n\t" \
    "v_mfma_f32_16x16x32_bf16 v[100:103], a[92:95], v[68:71], v[100:103]\n\t" \
    "global_load_lds_dwordx4 v208, s[52:53]\n\t" \
    "s_waitcnt lgkmcnt(5)\n\t" \
    "v_mfma_f32_16x16x32_bf16 v[88:91], a[24:27], v[24:27], v[88:91]\n\t" \
    "ds_read_b128 v[48:51], v2 offset:512\n\t" \
    "s_waitcnt lgkmcnt(5)\n\t" \
    "v_mfma_f32_16x16x32_bf16 v[92:95], a[24:27], v[28:31], v[92:95]\n\t" \
    "ds_read_b128 v[52:55], v2 offset:18944\n\t" \
    "v_cvt_pk_bf16_f32 v111, v86, v87\n\t" \
    "v_mfma_f32_16x16x32_bf16 v[96:99], a[96:99], v[24:27], v[96:99]\n\t" \
    "s_add_u32 m0, s91, 6144\n\t" \
    "global_store_dwordx4 v8, v[108:111], s[40:41] offset:1024 sc1\n\t" \
    "v_mfma_f32_16x16x32_bf16 v[100:103], a[96:99], v[28:31], v[100:103]\n\t" \
    "global_load_lds_dwordx4 v209, s[52:53]\n\t" \
    "s_waitcnt lgkmcnt(5)\n\t" \
    "v_mfma_f32_16x16x32_bf16 v[88:91], a[28:31], v[32:35], v[88:91]\n\t" \
    "ds_read_b128 v[56:59], v1 offset:640\n\t" \
    "s_waitcnt lgkmcnt(5)\n\t" \
    "v_mfma_f32_16x16x32_bf16 v[92:95], a[28:31], v[36:39], v[92:95]\n\t" \
    "ds_read_b128 v[60:63], v1 offset:19072\n\t" \
    "v_mfma_f32_16x16x32_bf16 v[96:99], a[100:103], v[32:35], v[96:99]\n\t" \
    "s_add_u32 m0, s91, 7168\n\t" \
    "v_mfma_f32_16x16x32_bf16 v[100:103], a[100:103], v[36:39], v[100:103]\n\t" \
    "global_load_lds_dwordx4 v210, s[52:53]\n\t" \
    "s_waitcnt lgkmcnt(5)\n\t" \
    "v_mfma_f32_16x16x32_bf16 v[88:91], a[32:35], v[40:43], v[88:91]\n\t" \
    "ds_read_b128 v[64:67], v2 offset:640\n\t" \
    "s_waitcnt lgkmcnt(5)\n\t" \
    "v_mfma_f32_16x16x32_bf16 v[92:95], a[32:35], v[44:47], v[92:95]\n\t" \
    "ds_read_b128 v[68:71], v2 offset:19072\n\t" \
    "v_mfma_f32_16x16x32_bf16 v[96:99], a[104:107], v[40:43], v[96:99]\n\t" \
    "s_add_u32 m0, s91, 8192\n\t" \
    "v_mfma_f32_16x16x32_bf16 v[100:103], a[104:107], v[44:47], v[100:103]\n\t" \
    "global_load_lds_dwordx4 v211, s[52:53]\n\t" \
    "s_waitcnt lgkmcnt(5)\n\t" \
    "v_mfma_f32_16x16x32_bf16 v[88:91], a[36:39], v[48:51], v[88:91]\n\t" \
    "ds_read_b128 v[24:27], v1 offset:768\n\t" \
    "s_waitcnt lgkmcnt(5)\n\t" \
    "v_mfma_f32_16x16x32_bf16 v[92:95], a[36:39], v[52:55], v[92:95]\n\t" \
    "ds_read_b128 v[28:31], v1 offset:19200\n\t" \
    "v_mfma_f32_16x16x32_bf16 v[96:99], a[108:111], v[48:51], v[96:99]\n\t" \
    "v_mfma_f32_16x16x32_bf16 v[100:103], a[108:111], v[52:55], v[100:103]\n\t" \
    "s_waitcnt lgkmcnt(5)\n\t" \
    "v_mfma_f32_16x16x32_bf16 v[88:91], a[40:43], v[56:59], v[88:91]\n\t" \
    "ds_read_b128 v[32:35], v2 offset:768\n\t" \
    "s_waitcnt lgkmcnt(5)\n\t" \
    "v_mfma_f32_16x16x32_bf16 v[92:95], a[40:43], v[60:63], v[92:95]\n\t" \
    "ds_read_b128 v[36:39], v2 offset:19200\n\t" \
    "v_mfma_f32_16x16x32_bf16 v[96:99], a[112:115], v[56:59], v[96:99]\n\t" \
    "s_waitcnt vmcnt(11)\n\t" \
    "s_barrier\n\t" \
    "v_mfma_f32_16x16x32_bf16 v[100:103], a[112:115], v[60:63], v[100:103]\n\t" \
    "s_cmp_lg_u32 s66, 0\n\t" \
    "s_cbranch_scc1 gx_SF13_%=\n\t" \
    "s_lshl_b32 s69, s67, 7\n\t" \
    "s_add_u32 s69, s69, 3211264\n\t" \
    "s_add_u32 s70, s65, -1\n\t" \
    "v_mov_b32 v156, s69\n\t" \
    "v_mov_b32 v157, s70\n\t" \
    "s_mov_b64 exec, 1\n\t" \
    "global_store_dword v156, v157, s[100:101] sc1\n\t" \
    "s_mov_b64 exec, -1\n\t" \
    "gx_SF13_%=:\n\t" \
    "s_waitcnt lgkmcnt(5)\n\t" \
    "v_mfma_f32_16x16x32_bf16 v[88:91], a[44:47], v[64:67], v[88:91]\n\t" \
    "ds_read_b128 v[40:43], v1 offset:896\n\t" \
    "s_waitcnt lgkmcnt(5)\n\t" \
    "v_mfma_f32_16x16x32_bf16 v[92:95], a[44:47], v[68:71], v[92:95]\n\t" \
    "ds_read_b128 v[44:47], v1 offset:19328\n\t" \
    "v_mfma_f32_16x16x32_bf16 v[96:99], a[116:119], v[64:67], v[96:99]\n\t" \
    "v_add_u32 v22, 0x9000, v1\n\t" \
    "v_add_u32 v23, 0x9000, v2\n\t" \
    "v_mfma_f32_16x16x32_bf16 v[100:103], a[116:119], v[68:71], v[100:103]\n\t" \
    "s_cmp_lt_u32 s65, 53\n\t" \
    "s_cselect_b32 s84, 11, 2\n\t" \
    "s_waitcnt lgkmcnt(5)\n\t" \
    "v_mfma_f32_16x16x32_bf16 v[88:91], a[48:51], v[24:27], v[88:91]\n\t" \
    "ds_read_b128 v[48:51], v2 offset:896\n\t" \
    "s_waitcnt lgkmcnt(5)\n\t" \
    "v_mfma_f32_16x16x32_bf16 v[92:95], a[48:51], v[28:31], v[92:95]\n\t" \
    "ds_read_b128 v[52:55], v2 offset:19328\n\t" \
    "v_mfma_f32_16x16x32_bf16 v[96:99], a[120:123], v[24:27], v[96:99]\n\t" \
    "s_add_u32 s84, s65, s84\n\t" \
    "s_min_u32 s69, s84, 63\n\t" \
    "v_mfma_f32_16x16x32_bf16 v[100:103], a[120:123], v[28:31], v[100:103]\n\t" \
    "s_mul_i32 s69, s69, 36864\n\t" \
    "s_add_u32 s48, s50, s69\n\t" \
    "s_waitcnt lgkmcnt(5)\n\t" \
    "v_mfma_f32_16x16x32_bf16 v[88:91], a[52:55], v[32:35], v[88:91]\n\t" \
    "ds_read_b128 v[56:59], v1 offset:1024\n\t" \
    "s_waitcnt lgkmcnt(5)\n\t" \
    "v_mfma_f32_16x16x32_bf16 v[92:95], a[52:55], v[36:39], v[92:95]\n\t" \
    "ds_read_b128 v[60:63], v1 offset:19456\n\t" \
    "v_mfma_f32_16x16x32_bf16 v[96:99], a[124:127], v[32:35], v[96:99]\n\t" \
    "s_addc_u32 s49, s51, 0\n\t" \
    "s_mul_i32 s70, s67, 7\n\t" \
    "v_mfma_f32_16x16x32_bf16 v[100:103], a[124:127], v[36:39], v[100:103]\n\t" \
    "s_add_u32 s69, s65, 0\n\t" \
    "s_xor_b32 s69, s69, s70\n\t" \
    "s_waitcnt lgkmcnt(5)\n\t" \
    "v_mfma_f32_16x16x32_bf16 v[88:91], a[56:59], v[40:43], v[88:91]\n\t" \
    "ds_read_b128 v[64:67], v2 offset:1024\n\t" \
    "s_waitcnt lgkmcnt(5)\n\t" \
    "v_mfma_f32_16x16x32_bf16 v[92:95], a[56:59], v[44:47], v[92:95]\n\t" \
    "ds_read_b128 v[68:71], v2 offset:19456\n\t" \
    "v_mfma_f32_16x16x32_bf16 v[96:99], a[128:131], v[40:43], v[96:99]\n\t" \
    "s_and_b32 s69, s69, 63\n\t" \
    "s_lshl_b32 s69, s69, 13\n\t" \
    "v_mfma_f32_16x16x32_bf16 v[100:103], a[128:131], v[44:47], v[100:103]\n\t" \
    "s_mul_i32 s70, s67, 524288\n\t" \
    "s_add_u32 s69, s69, s70\n\t" \
    "s_waitcnt lgkmcnt(5)\n\t" \
    "v_mfma_f32_16x16x32_bf16 v[88:91], a[60:63], v[48:51], v[88:91]\n\t" \
    "ds_read_b128 v[24:27], v22 offset:0\n\t" \
    "s_waitcnt lgkmcnt(5)\n\t" \
    "v_mfma_f32_16x16x32_bf16 v[92:95], a[60:63], v[52:55], v[92:95]\n\t" \
    "ds_read_b128 v[28:31], v22 offset:18432\n\t" \
    "v_mfma_f32_16x16x32_bf16 v[96:99], a[132:135], v[48:51], v[96:99]\n\t" \
    "s_add_u32 s69, s69, 6291456\n\t" \
    "s_add_u32 s38, s36, s69\n\t" \
    "v_mfma_f32_16x16x32_bf16 v[100:103], a[132:135], v[52:55], v[100:103]\n\t" \
    "s_addc_u32 s39, s37, 0\n\t" \
    "s_waitcnt lgkmcnt(5)\n\t" \
    "v_mfma_f32_16x16x32_bf16 v[88:91], a[64:67], v[56:59], v[88:91]\n\t" \
    "ds_read_b128 v[32:35], v23 offset:0\n\t" \
    "s_waitcnt lgkmcnt(5)\n\t" \
    "v_mfma_f32_16x16x32_bf16 v[92:95], a[64:67], v[60:63], v[92:95]\n\t" \
    "ds_read_b128 v[36:39], v23 offset:18432\n\t" \
    "v_mfma_f32_16x16x32_bf16 v[96:99], a[136:139], v[56:59], v[96:99]\n\t" \
    "v_mfma_f32_16x16x32_bf16 v[100:103], a[136:139], v[60:63], v[100:103]\n\t" \
    "s_waitcnt lgkmcnt(5)\n\t" \
    "v_mfma_f32_16x16x32_bf16 v[88:91], a[68:71], v[64:67], v[88:91]\n\t" \
    "ds_read_b128 v[40:43], v22 offset:128\n\t" \
    "s_waitcnt lgkmcnt(5)\n\t" \
    "v_mfma_f32_16x16x32_bf16 v[92:95], a[68:71], v[68:71], v[92:95]\n\t" \
    "ds_read_b128 v[44:47], v22 offset:18560\n\t" \
    "v_mfma_f32_16x16x32_bf16 v[96:99], a[140:143], v[64:67], v[96:99]\n\t" \
    "v_mfma_f32_16x16x32_bf16 v[100:103], a[140:143], v[68:71], v[100:103]\n\t" \
    "s_waitcnt lgkmcnt(5)\n\t" \
    "v_mfma_f32_16x16x32_bf16 v[72:75], a[144:147], v[24:27], 0\n\t" \
    "ds_read_b128 v[48:51], v23 offset:128\n\t" \
    "s_waitcnt lgkmcnt(5)\n\t" \
    "v_mfma_f32_16x16x32_bf16 v[76:79], a[144:147], v[28:31], 0\n\t" \
    "ds_read_b128 v[52:55], v23 offset:18560\n\t" \
    "v_mfma_f32_16x16x32_bf16 v[80:83], a[216:219], v[24:27], 0\n\t" \
    "s_add_u32 m0, s91, 36864\n\t" \
    "v_mfma_f32_16x16x32_bf16 v[84:87], a[216:219], v[28:31], 0\n\t" \
    "global_load_lds_dwordx4 v203, s[48:49]\n\t" \
    "s_waitcnt lgkmcnt(5)\n\t" \
    "v_mfma_f32_16x16x32_bf16 v[72:75], a[148:151], v[32:35], v[72:75]\n\t" \
    "ds_read_b128 v[56:59], v22 offset:256\n\t" \
    "s_waitcnt lgkmcnt(5)\n\t" \
    "v_mfma_f32_16x16x32_bf16 v[76:79], a[148:151], v[36:39], v[76:79]\n\t" \
    "ds_read_b128 v[60:63], v22 offset:18688\n\t" \
    "v_mfma_f32_16x16x32_bf16 v[80:83], a[220:223], v[32:35], v[80:83]\n\t" \
    "s_add_u32 m0, s91, 37888\n\t" \
    "v_mfma_f32_16x16x32_bf16 v[84:87], a[220:223], v[36:39], v[84:87]\n\t" \
    "global_load_lds_dwordx4 v204, s[48:49]\n\t" \
    "s_waitcnt lgkmcnt(5)\n\t" \
    "v_mfma_f32_16x16x32_bf16 v[72:75], a[152:155], v[40:43], v[72:75]\n\t" \
    "ds_read_b128 v[64:67], v23 offset:256\n\t" \
    "s_waitcnt lgkmcnt(5)\n\t" \
    "v_mfma_f32_16x16x32_bf16 v[76:79], a[152:155], v[44:47], v[76:79]\n\t" \
    "ds_read_b128 v[68:71], v23 offset:18688\n\t" \
    "v_cvt_pk_bf16_f32 v104, v88, v89\n\t" \
    "v_mfma_f32_16x16x32_bf16 v[80:83], a[224:227], v[40:43], v[80:83]\n\t" \
    "s_add_u32 m0, s91, 38912\n\t" \
    "v_cvt_pk_bf16_f32 v105, v90, v91\n\t" \
    "v_mfma_f32_16x16x32_bf16 v[84:87], a[224:227], v[44:47], v[84:87]\n\t" \
    "global_load_lds_dwordx4 v205, s[48:49]\n\t" \
    "s_waitcnt lgkmcnt(5)\n\t" \
    "v_mfma_f32_16x16x32_bf16 v[72:75], a[156:159], v[48:51], v[72:75]\n\t" \
    "ds_read_b128 v[24:27], v22 offset:384\n\t" \
    "s_waitcnt lgkmcnt(5)\n\t" \
    "v_mfma_f32_16x16x32_bf16 v[76:79], a[156:159], v[52:55], v[76:79]\n\t" \
    "ds_read_b128 v[28:31], v22 offset:18816\n\t" \
    "v_cvt_pk_bf16_f32 v106, v96, v97\n\t" \
    "v_mfma_f32_16x16x32_bf16 v[80:83], a[228:231], v[48:51], v[80:83]\n\t" \
    "s_add_u32 m0, s91, 39936\n\t" \
    "v_cvt_pk_bf16_f32 v107, v98, v99\n\t" \
    "v_mfma_f32_16x16x32_bf16 v[84:87], a[228:231], v[52:55], v[84:87]\n\t" \
    "global_load_lds_dwordx4 v206, s[48:49]\n\t" \
    "s_waitcnt lgkmcnt(5)\n\t" \
    "v_mfma_f32_16x16x32_bf16 v[72:75], a[160:163], v[56:59], v[72:75]\n\t" \
    "ds_read_b128 v[32:35], v23 offset:384\n\t" \
    "s_waitcnt lgkmcnt(5)\n\t" \
    "v_mfma_f32_16x16x32_bf16 v[76:79], a[160:163], v[60:63], v[76:79]\n\t" \
    "ds_read_b128 v[36:39], v23 offset:18816\n\t" \
    "global_store_dwordx4 v8, v[104:107], s[38:39] offset:0 sc1\n\t" \
    "v_mfma_f32_16x16x32_bf16 v[80:83], a[232:235], v[56:59], v[80:83]\n\t" \
    "s_add_u32 m0, s91, 40960\n\t" \
    "v_cvt_pk_bf16_f32 v108, v92, v93\n\t" \
    "v_mfma_f32_16x16x32_bf16 v[84:87], a[232:235], v[60:63], v[84:87]\n\t" \
    "global_load_lds_dwordx4 v207, s[48:49]\n\t" \
    "s_waitcnt lgkmcnt(5)\n\t" \
    "v_mfma_f32_16x16x32_bf16 v[72:75], a[164:167], v[64:67], v[72:75]\n\t" \
    "ds_read_b128 v[40:43], v22 offset:512\n\t" \
    "s_waitcnt lgkmcnt(5)\n\t" \
    "v_mfma_f32_16x16x32_bf16 v[76:79], a[164:167], v[68:71], v[76:79]\n\t" \
    "ds_read_b128 v[44:47], v22 offset:18944\n\t" \
    "v_cvt_pk_bf16_f32 v109, v94, v95\n\t" \
    "v_mfma_f32_16x16x32_bf16 v[80:83], a[236:239], v[64:67], v[80:83]\n\t" \
    "s_add_u32 m0, s91, 41984\n\t" \
    "v_cvt_pk_bf16_f32 v110, v100, v101\n\t" \
    "v_mfma_f32_16x16x32_bf16 v[84:87], a[236:239], v[68:71], v[84:87]\n\t" \
    "global_load_lds_dwordx4 v208, s[48:49]\n\t" \
    "s_waitcnt lgkmcnt(5)\n\t" \
    "v_mfma_f32_16x16x32_bf16 v[72:75], a[168:171], v[24:27], v[72:75]\n\t" \
    "ds_read_b128 v[48:51], v23 offset:512\n\t" \
    "s_waitcnt lgkmcnt(5)\n\t" \
    "v_mfma_f32_16x16x32_bf16 v[76:79], a[168:171], v[28:31], v[76:79]\n\t" \
    "ds_read_b128 v[52:55], v23 offset:18944\n\t" \
    "v_cvt_pk_bf16_f32 v111, v102, v103\n\t" \
    "v_mfma_f32_16x16x32_bf16 v[80:83], a[240:243], v[24:27], v[80:83]\n\t" \
    "s_add_u32 m0, s91, 43008\n\t" \
    "global_store_dwordx4 v8, v[108:111], s[38:39] offset:1024 sc1\n\t" \
    "v_mfma_f32_16x16x32_bf16 v[84:87], a[240:243], v[28:31], v[84:87]\n\t" \
    "global_load_lds_dwordx4 v209, s[48:49]\n\t" \
    "s_waitcnt lgkmcnt(5)\n\t" \
    "v_mfma_f32_16x16x32_bf16 v[72:75], a[172:175], v[32:35], v[72:75]\n\t" \
    "ds_read_b128 v[56:59], v22 offset:640\n\t" \
    "s_waitcnt lgkmcnt(5)\n\t" \
    "v_mfma_f32_16x16x32_bf16 v[76:79], a[172:175], v[36:39], v[76:79]\n\t" \
    "ds_read_b128 v[60:63], v22 offset:19072\n\t" \
    "v_mfma_f32_16x16x32_bf16 v[80:83], a[244:247], v[32:35], v[80:83]\n\t" \
    "s_add_u32 m0, s91, 44032\n\t" \
    "v_mfma_f32_16x16x32_bf16 v[84:87], a[244:247], v[36:39], v[84:87]\n\t" \
    "global_load_lds_dwordx4 v210, s[48:49]\n\t" \
    "s_waitcnt lgkmcnt(5)\n\t" \
    "v_mfma_f32_16x16x32_bf16 v[72:75], a[176:179], v[40:43], v[72:75]\n\t" \
    "ds_read_b128 v[64:67], v23 offset:640\n\t" \
    "s_waitcnt lgkmcnt(5)\n\t" \
    "v_mfma_f32_16x16x32_bf16 v[76:79], a[176:179], v[44:47], v[76:79]\n\t" \
    "ds_read_b128 v[68:71], v23 offset:19072\n\t" \
    "v_mfma_f32_16x16x32_bf16 v[80:83], a[248:251], v[40:43], v[80:83]\n\t" \
    "s_add_u32 m0, s91, 45056\n\t" \
    "v_mfma_f32_16x16x32_bf16 v[84:87], a[248:251], v[44:47], v[84:87]\n\t" \
    "global_load_lds_dwordx4 v211, s[48:49]\n\t" \
    "s_waitcnt lgkmcnt(5)\n\t" \
    "v_mfma_f32_16x16x32_bf16 v[72:75], a[180:183], v[48:51], v[72:75]\n\t" \
    "ds_read_b128 v[24:27], v22 offset:768\n\t" \
    "s_waitcnt lgkmcnt(5)\n\t" \
    "v_mfma_f32_16x16x32_bf16 v[76:79], a[180:183], v[52:55], v[76:79]\n\t" \
    "ds_read_b128 v[28:31], v22 offset:19200\n\t" \
    "v_mfma_f32_16x16x32_bf16 v[80:83], a[252:255], v[48:51], v[80:83]\n\t" \
    "v_mfma_f32_16x16x32_bf16 v[84:87], a[252:255], v[52:55], v[84:87]\n\t" \
    "s_waitcnt lgkmcnt(5)\n\t" \
    "v_mfma_f32_16x16x32_bf16 v[72:75], a[184:187], v[56:59], v[72:75]\n\t" \
    "ds_read_b128 v[32:35], v23 offset:768\n\t" \
    "s_waitcnt lgkmcnt(5)\n\t" \
    "v_mfma_f32_16x16x32_bf16 v[76:79], a[184:187], v[60:63], v[76:79]\n\t" \
    "ds_read_b128 v[36:39], v23 offset:19200\n\t" \
    "v_mfma_f32_16x16x32_bf16 v[80:83], v[224:227], v[56:59], v[80:83]\n\t" \
    "s_waitcnt vmcnt(11)\n\t" \
    "s_barrier\n\t" \
    "v_mfma_f32_16x16x32_bf16 v[84:87], v[224:227], v[60:63], v[84:87]\n\t" \
    "s_add_u32 s71, s65, 9\n\t" \
    "s_cmp_lg_u32 s66, 0\n\t" \
    "s_cbranch_scc1 gx_SF14_%=\n\t" \
    "s_lshl_b32 s69, s67, 7\n\t" \
    "s_add_u32 s69, s69, 3149824\n\t" \
    "s_add_u32 s70, s71, 0\n\t" \
    "v_mov_b32 v156, s69\n\t" \
    "v_mov_b32 v157, s70\n\t" \
    "s_mov_b64 exec, 1\n\t" \
    "global_store_dword v156, v157, s[100:101] sc1\n\t" \
    "s_mov_b64 exec, -1\n\t" \
    "gx_SF14_%=:\n\t" \
    "s_waitcnt lgkmcnt(5)\n\t" \
    "v_mfma_f32_16x16x32_bf16 v[72:75], a[188:191], v[64:67], v[72:75]\n\t" \
    "ds_read_b128 v[40:43], v22 offset:896\n\t" \
    "s_waitcnt lgkmcnt(5)\n\t" \
    "v_mfma_f32_16x16x32_bf16 v[76:79], a[188:191], v[68:71], v[76:79]\n\t" \
    "ds_read_b128 v[44:47], v22 offset:19328\n\t" \
    "v_mfma_f32_16x16x32_bf16 v[80:83], v[228:231], v[64:67], v[80:83]\n\t" \
    "s_xor_b32 s90, s90, 0x12000\n\t" \
    "v_add_u32 v1, s90, v0\n\t" \
    "v_mfma_f32_16x16x32_bf16 v[84:87], v[228:231], v[68:71], v[84:87]\n\t" \
    "v_xor_b32 v2, 64, v1\n\t" \
    "s_add_u32 s84, s65, 2\n\t" \
    "s_waitcnt lgkmcnt(5)\n\t" \
    "v_mfma_f32_16x16x32_bf16 v[72:75], a[192:195], v[24:27], v[72:75]\n\t" \
    "ds_read_b128 v[48:51], v23 offset:896\n\t" \
    "s_waitcnt lgkmcnt(5)\n\t" \
    "v_mfma_f32_16x16x32_bf16 v[76:79], a[192:195], v[28:31], v[76:79]\n\t" \
    "ds_read_b128 v[52:55], v23 offset:19328\n\t" \
    "v_mfma_f32_16x16x32_bf16 v[80:83], v[232:235], v[24:27], v[80:83]\n\t" \
    "s_min_u32 s69, s84, 63\n\t" \
    "s_mul_i32 s69, s69, 36864\n\t" \
    "v_mfma_f32_16x16x32_bf16 v[84:87], v[232:235], v[28:31], v[84:87]\n\t" \
    "s_add_u32 s52, s50, s69\n\t" \
    "s_addc_u32 s53, s51, 0\n\t" \
    "s_waitcnt lgkmcnt(5)\n\t" \
    "v_mfma_f32_16x16x32_bf16 v[72:75], a[196:199], v[32:35], v[72:75]\n\t" \
    "ds_read_b128 v[56:59], v22 offset:1024\n\t" \
    "s_waitcnt lgkmcnt(5)\n\t" \
    "v_mfma_f32_16x16x32_bf16 v[76:79], a[196:199], v[36:39], v[76:79]\n\t" \
    "ds_read_b128 v[60:63], v22 offset:19456\n\t" \
    "v_mfma_f32_16x16x32_bf16 v[80:83], v[236:239], v[32:35], v[80:83]\n\t" \
    "s_mul_i32 s70, s67, 7\n\t" \
    "s_add_u32 s69, s65, 10\n\t" \
    "v_mfma_f32_16x16x32_bf16 v[84:87], v[236:239], v[36:39], v[84:87]\n\t" \
    "s_xor_b32 s69, s69, s70\n\t" \
    "s_and_b32 s69, s69, 63\n\t" \
    "s_waitcnt lgkmcnt(5)\n\t" \
    "v_mfma_f32_16x16x32_bf16 v[72:75], a[200:203], v[40:43], v[72:75]\n\t" \
    "ds_read_b128 v[64:67], v23 offset:1024\n\t" \
    "s_waitcnt lgkmcnt(5)\n\t" \
    "v_mfma_f32_16x16x32_bf16 v[76:79], a[200:203], v[44:47], v[76:79]\n\t" \
    "ds_read_b128 v[68:71], v23 offset:19456\n\t" \
    "v_mfma_f32_16x16x32_bf16 v[80:83], v[240:243], v[40:43], v[80:83]\n\t" \
    "s_lshl_b32 s69, s69, 13\n\t" \
    "s_mul_i32 s70, s67, 524288\n\t" \
    "v_mfma_f32_16x16x32_bf16 v[84:87], v[240:243], v[44:47], v[84:87]\n\t" \
    "s_add_u32 s69, s69, s70\n\t" \
    "s_add_u32 s69, s69, 23068672\n\t" \
    "s_waitcnt lgkmcnt(5)\n\t" \
    "v_mfma_f32_16x16x32_bf16 v[72:75], a[204:207], v[48:51], v[72:75]\n\t" \
    "ds_read_b128 v[24:27], v1 offset:0\n\t" \
    "s_waitcnt lgkmcnt(5)\n\t" \
    "v_mfma_f32_16x16x32_bf16 v[76:79], a[204:207], v[52:55], v[76:79]\n\t" \
    "ds_read_b128 v[28:31], v1 offset:18432\n\t" \
    "v_mfma_f32_16x16x32_bf16 v[80:83], v[244:247], v[48:51], v[80:83]\n\t" \
    "s_add_u32 s40, s36, s69\n\t" \
    "s_addc_u32 s41, s37, 0\n\t" \
    "v_mfma_f32_16x16x32_bf16 v[84:87], v[244:247], v[52:55], v[84:87]\n\t" \
    "s_sub_u32 s91, s85, s91\n\t" \
    "s_add_u32 s65, s65, 1\n\t" \
    "s_waitcnt lgkmcnt(5)\n\t" \
    "v_mfma_f32_16x16x32_bf16 v[72:75], a[208:211], v[56:59], v[72:75]\n\t" \
    "ds_read_b128 v[32:35], v2 offset:0\n\t" \
    "s_waitcnt lgkmcnt(5)\n\t" \
    "v_mfma_f32_16x16x32_bf16 v[76:79], a[208:211], v[60:63], v[76:79]\n\t" \
    "ds_read_b128 v[36:39], v2 offset:18432\n\t" \
    "v_mfma_f32_16x16x32_bf16 v[80:83], v[248:251], v[56:59], v[80:83]\n\t" \
    "v_mfma_f32_16x16x32_bf16 v[84:87], v[248:251], v[60:63], v[84:87]\n\t" \
    "s_waitcnt lgkmcnt(5)\n\t" \
    "v_mfma_f32_16x16x32_bf16 v[72:75], a[212:215], v[64:67], v[72:75]\n\t" \
    "ds_read_b128 v[40:43], v1 offset:128\n\t" \
    "s_waitcnt lgkmcnt(5)\n\t" \
    "v_mfma_f32_16x16x32_bf16 v[76:79], a[212:215], v[68:71], v[76:79]\n\t" \
    "ds_read_b128 v[44:47], v1 offset:18560\n\t" \
    "v_mfma_f32_16x16x32_bf16 v[80:83], v[252:255], v[64:67], v[80:83]\n\t" \
    "v_mfma_f32_16x16x32_bf16 v[84:87], v[252:255], v[68:71], v[84:87]\n\t" \
    "s_cmp_lt_u32 s65, 54\n\t" \
    "s_cbranch_scc1 gx_l28_%=\n\t" \
    "v_cvt_pk_bf16_f32 v104, v72, v73\n\t" \
    "v_cvt_pk_bf16_f32 v105, v74, v75\n\t" \
    "s_nop 2\n\t" \
    "v_cvt_pk_bf16_f32 v106, v80, v81\n\t" \
    "v_cvt_pk_bf16_f32 v107, v82, v83\n\t" \
    "global_store_dwordx4 v8, v[104:107], s[40:41] offset:0 sc1\n\t" \
    "v_cvt_pk_bf16_f32 v108, v76, v77\n\t" \
    "v_cvt_pk_bf16_f32 v109, v78, v79\n\t" \
    "v_cvt_pk_bf16_f32 v110, v84, v85\n\t" \
    "v_cvt_pk_bf16_f32 v111, v86, v87\n\t" \
    "global_store_dwordx4 v8, v[108:111], s[40:41] offset:1024 sc1\n\t" \
    "gx_l315_%=:\n\t" \
    "s_add_u32 s84, s65, 2\n\t" \
    "s_min_u32 s69, s84, 63\n\t" \
    "s_mul_i32 s69, s69, 36864\n\t" \
    "s_add_u32 s52, s50, s69\n\t" \
    "s_addc_u32 s53, s51, 0\n\t" \
    "s_add_u32 s90, s90, 36864\n\t" \
    "s_cmp_eq_u32 s90, 147456\n\t" \
    "s_cselect_b32 s90, 0, s90\n\t" \
    "s_add_u32 s91, s90, 36864\n\t" \
    "s_cmp_eq_u32 s91, 147456\n\t" \
    "s_cselect_b32 s91, 0, s91\n\t" \
    "s_add_u32 s91, s91, s89\n\t" \
    "v_add_u32 v22, s90, v0\n\t" \
    "v_xor_b32 v23, 64, v22\n\t" \
    "s_mul_i32 s70, s67, 7\n\t" \
    "s_add_u32 s69, s65, 0\n\t" \
    "s_xor_b32 s69, s69, s70\n\t" \
    "s_and_b32 s69, s69, 63\n\t" \
    "s_lshl_b32 s69, s69, 13\n\t" \
    "s_mul_i32 s70, s67, 524288\n\t" \
    "s_add_u32 s69, s69, s70\n\t" \
    "s_add_u32 s69, s69, 6291456\n\t" \
    "s_add_u32 s38, s36, s69\n\t" \
    "s_addc_u32 s39, s37, 0\n\t" \
    "s_waitcnt lgkmcnt(5)\n\t" \
    "v_mfma_f32_16x16x32_bf16 v[88:91], a[0:3], v[24:27], 0\n\t" \
    "ds_read_b128 v[48:51], v2 offset:128\n\t" \
    "s_waitcnt lgkmcnt(5)\n\t" \
    "v_mfma_f32_16x16x32_bf16 v[92:95], a[0:3], v[28:31], 0\n\t" \
    "ds_read_b128 v[52:55], v2 offset:18560\n\t" \
    "v_mfma_f32_16x16x32_bf16 v[96:99], a[72:75], v[24:27], 0\n\t" \
    "s_add_u32 m0, s91, 0\n\t" \
    "v_mfma_f32_16x16x32_bf16 v[100:103], a[72:75], v[28:31], 0\n\t" \
    "global_load_lds_dwordx4 v203, s[52:53]\n\t" \
    "s_waitcnt lgkmcnt(5)\n\t" \
    "v_mfma_f32_16x16x32_bf16 v[88:91], a[4:7], v[32:35], v[88:91]\n\t" \
    "ds_read_b128 v[56:59], v1 offset:256\n\t" \
    "s_waitcnt lgkmcnt(5)\n\t" \
    "v_mfma_f32_16x16x32_bf16 v[92:95], a[4:7], v[36:39], v[92:95]\n\t" \
    "ds_read_b128 v[60:63], v1 offset:18688\n\t" \
    "v_mfma_f32_16x16x32_bf16 v[96:99], a[76:79], v[32:35], v[96:99]\n\t" \
    "s_add_u32 m0, s91, 1024\n\t" \
    "v_mfma_f32_16x16x32_bf16 v[100:103], a[76:79], v[36:39], v[100:103]\n\t" \
    "global_load_lds_dwordx4 v204, s[52:53]\n\t" \
    "s_waitcnt lgkmcnt(5)\n\t" \
    "v_mfma_f32_16x16x32_bf16 v[88:91], a[8:11], v[40:43], v[88:91]\n\t" \
    "ds_read_b128 v[64:67], v2 offset:256\n\t" \
    "s_waitcnt lgkmcnt(5)\n\t" \
    "v_mfma_f32_16x16x32_bf16 v[92:95], a[8:11], v[44:47], v[92:95]\n\t" \
    "ds_read_b128 v[68:71], v2 offset:18688\n\t" \
    "v_mfma_f32_16x16x32_bf16 v[96:99], a[80:83], v[40:43], v[96:99]\n\t" \
    "s_add_u32 m0, s91, 2048\n\t" \
    "v_mfma_f32_16x16x32_bf16 v[100:103], a[80:83], v[44:47], v[100:103]\n\t" \
    "global_load_lds_dwordx4 v205, s[52:53]\n\t" \
    "s_waitcnt lgkmcnt(5)\n\t" \
    "v_mfma_f32_16x16x32_bf16 v[88:91], a[12:15], v[48:51], v[88:91]\n\t" \
    "ds_read_b128 v[24:27], v1 offset:384\n\t" \
    "s_waitcnt lgkmcnt(5)\n\t" \
    "v_mfma_f32_16x16x32_bf16 v[92:95], a[12:15], v[52:55], v[92:95]\n\t" \
    "ds_read_b128 v[28:31], v1 offset:18816\n\t" \
    "v_mfma_f32_16x16x32_bf16 v[96:99], a[84:87], v[48:51], v[96:99]\n\t" \
    "s_add_u32 m0, s91, 3072\n\t" \
    "v_mfma_f32_16x16x32_bf16 v[100:103], a[84:87], v[52:55], v[100:103]\n\t" \
    "global_load_lds_dwordx4 v206, s[52:53]\n\t" \
    "s_waitcnt lgkmcnt(5)\n\t" \
    "v_mfma_f32_16x16x32_bf16 v[88:91], a[16:19], v[56:59], v[88:91]\n\t" \
    "ds_read_b128 v[32:35], v2 offset:384\n\t" \
    "s_waitcnt lgkmcnt(5)\n\t" \
    "v_mfma_f32_16x16x32_bf16 v[92:95], a[16:19], v[60:63], v[92:95]\n\t" \
    "ds_read_b128 v[36:39], v2 offset:18816\n\t" \
    "v_mfma_f32_16x16x32_bf16 v[96:99], a[88:91], v[56:59], v[96:99]\n\t" \
    "s_add_u32 m0, s91, 4096\n\t" \
    "v_mfma_f32_16x16x32_bf16 v[100:103], a[88:91], v[60:63], v[100:103]\n\t" \
    "global_load_lds_dwordx4 v207, s[52:53]\n\t" \
    "s_waitcnt lgkmcnt(5)\n\t" \
    "v_mfma_f32_16x16x32_bf16 v[88:91], a[20:23], v[64:67], v[88:91]\n\t" \
    "ds_read_b128 v[40:43], v1 offset:512\n\t" \
    "s_waitcnt lgkmcnt(5)\n\t" \
    "v_mfma_f32_16x16x32_bf16 v[92:95], a[20:23], v[68:71], v[92:95]\n\t" \
    "ds_read_b128 v[44:47], v1 offset:18944\n\t" \
    "v_mfma_f32_16x16x32_bf16 v[96:99], a[92:95], v[64:67], v[96:99]\n\t" \
    "s_add_u32 m0, s91, 5120\n\t" \
    "v_mfma_f32_16x16x32_bf16 v[100:103], a[92:95], v[68:71], v[100:103]\n\t" \
    "global_load_lds_dwordx4 v208, s[52:53]\n\t" \
    "s_waitcnt lgkmcnt(5)\n\t" \
    "v_mfma_f32_16x16x32_bf16 v[88:91], a[24:27], v[24:27], v[88:91]\n\t" \
    "ds_read_b128 v[48:51], v2 offset:512\n\t" \
    "s_waitcnt lgkmcnt(5)\n\t" \
    "v_mfma_f32_16x16x32_bf16 v[92:95], a[24:27], v[28:31], v[92:95]\n\t" \
    "ds_read_b128 v[52:55], v2 offset:18944\n\t" \
    "v_mfma_f32_16x16x32_bf16 v[96:99], a[96:99], v[24:27], v[96:99]\n\t" \
    "s_add_u32 m0, s91, 6144\n\t" \
    "v_mfma_f32_16x16x32_bf16 v[100:103], a[96:99], v[28:31], v[100:103]\n\t" \
    "global_load_lds_dwordx4 v209, s[52:53]\n\t" \
    "s_waitcnt lgkmcnt(5)\n\t" \
    "v_mfma_f32_16x16x32_bf16 v[88:91], a[28:31], v[32:35], v[88:91]\n\t" \
    "ds_read_b128 v[56:59], v1 offset:640\n\t" \
    "s_waitcnt lgkmcnt(5)\n\t" \
    "v_mfma_f32_16x16x32_bf16 v[92:95], a[28:31], v[36:39], v[92:95]\n\t" \
    "ds_read_b128 v[60:63], v1 offset:19072\n\t" \
    "v_mfma_f32_16x16x32_bf16 v[96:99], a[100:103], v[32:35], v[96:99]\n\t" \
    "s_add_u32 m0, s91, 7168\n\t" \
    "v_mfma_f32_16x16x32_bf16 v[100:103], a[100:103], v[36:39], v[100:103]\n\t" \
    "global_load_lds_dwordx4 v210, s[52:53]\n\t" \
    "s_waitcnt lgkmcnt(5)\n\t" \
    "v_mfma_f32_16x16x32_bf16 v[88:91], a[32:35], v[40:43], v[88:91]\n\t" \
    "ds_read_b128 v[64:67], v2 offset:640\n\t" \
    "s_waitcnt lgkmcnt(5)\n\t" \
    "v_mfma_f32_16x16x32_bf16 v[92:95], a[32:35], v[44:47], v[92:95]\n\t" \
    "ds_read_b128 v[68:71], v2 offset:19072\n\t" \
    "v_mfma_f32_16x16x32_bf16 v[96:99], a[104:107], v[40:43], v[96:99]\n\t" \
    "s_add_u32 m0, s91, 8192\n\t" \
    "v_mfma_f32_16x16x32_bf16 v[100:103], a[104:107], v[44:47], v[100:103]\n\t" \
    "global_load_lds_dwordx4 v211, s[52:53]\n\t" \
    "s_waitcnt lgkmcnt(5)\n\t" \
    "v_mfma_f32_16x16x32_bf16 v[88:91], a[36:39], v[48:51], v[88:91]\n\t" \
    "ds_read_b128 v[24:27], v1 offset:768\n\t" \
    "s_waitcnt lgkmcnt(5)\n\t" \
    "v_mfma_f32_16x16x32_bf16 v[92:95], a[36:39], v[52:55], v[92:95]\n\t" \
    "ds_read_b128 v[28:31], v1 offset:19200\n\t" \
    "v_mfma_f32_16x16x32_bf16 v[96:99], a[108:111], v[48:51], v[96:99]\n\t" \
    "v_mfma_f32_16x16x32_bf16 v[100:103], a[108:111], v[52:55], v[100:103]\n\t" \
    "s_waitcnt lgkmcnt(5)\n\t" \
    "v_mfma_f32_16x16x32_bf16 v[88:91], a[40:43], v[56:59], v[88:91]\n\t" \
    "ds_read_b128 v[32:35], v2 offset:768\n\t" \
    "s_waitcnt lgkmcnt(5)\n\t" \
    "v_mfma_f32_16x16x32_bf16 v[92:95], a[40:43], v[60:63], v[92:95]\n\t" \
    "ds_read_b128 v[36:39], v2 offset:19200\n\t" \
    "v_mfma_f32_16x16x32_bf16 v[96:99], a[112:115], v[56:59], v[96:99]\n\t" \
    "s_waitcnt vmcnt(9)\n\t" \
    "s_barrier\n\t" \
    "v_mfma_f32_16x16x32_bf16 v[100:103], a[112:115], v[60:63], v[100:103]\n\t" \
    "s_cmp_lg_u32 s66, 0\n\t" \
    "s_cbranch_scc1 gx_SF16_%=\n\t" \
    "s_lshl_b32 s69, s67, 7\n\t" \
    "s_add_u32 s69, s69, 3211264\n\t" \
    "s_add_u32 s70, s65, -1\n\t" \
    "v_mov_b32 v156, s69\n\t" \
    "v_mov_b32 v157, s70\n\t" \
    "s_mov_b64 exec, 1\n\t" \
    "global_store_dword v156, v157, s[100:101] sc1\n\t" \
    "s_mov_b64 exec, -1\n\t" \
    "gx_SF16_%=:\n\t" \
    "s_cmp_lg_u32 s66, 1\n\t" \
    "s_cbranch_scc1 gx_SF17_%=\n\t" \
    "s_lshl_b32 s69, s67, 7\n\t" \
    "s_add_u32 s69, s69, 3149824\n\t" \
    "s_add_u32 s70, s65, 40\n\t" \
    "v_mov_b32 v156, s69\n\t" \
    "v_mov_b32 v157, s70\n\t" \
    "s_mov_b64 exec, 1\n\t" \
    "global_store_dword v156, v157, s[100:101] sc1\n\t" \
    "s_mov_b64 exec, -1\n\t" \
    "gx_SF17_%=:\n\t" \
    "s_waitcnt lgkmcnt(5)\n\t" \
    "v_mfma_f32_16x16x32_bf16 v[88:91], a[44:47], v[64:67], v[88:91]\n\t" \
    "ds_read_b128 v[40:43], v1 offset:896\n\t" \
    "s_waitcnt lgkmcnt(5)\n\t" \
    "v_mfma_f32_16x16x32_bf16 v[92:95], a[44:47], v[68:71], v[92:95]\n\t" \
    "ds_read_b128 v[44:47], v1 offset:19328\n\t" \
    "v_mfma_f32_16x16x32_bf16 v[96:99], a[116:119], v[64:67], v[96:99]\n\t" \
    "v_mfma_f32_16x16x32_bf16 v[100:103], a[116:119], v[68:71], v[100:103]\n\t" \
    "s_waitcnt lgkmcnt(5)\n\t" \
    "v_mfma_f32_16x16x32_bf16 v[88:91], a[48:51], v[24:27], v[88:91]\n\t" \
    "ds_read_b128 v[48:51], v2 offset:896\n\t" \
    "s_waitcnt lgkmcnt(5)\n\t" \
    "v_mfma_f32_16x16x32_bf16 v[92:95], a[48:51], v[28:31], v[92:95]\n\t" \
    "ds_read_b128 v[52:55], v2 offset:19328\n\t" \
    "v_mfma_f32_16x16x32_bf16 v[96:99], a[120:123], v[24:27], v[96:99]\n\t" \
    "v_mfma_f32_16x16x32_bf16 v[100:103], a[120:123], v[28:31], v[100:103]\n\t" \
    "s_waitcnt lgkmcnt(5)\n\t" \
    "v_mfma_f32_16x16x32_bf16 v[88:91], a[52:55], v[32:35], v[88:91]\n\t" \
    "ds_read_b128 v[56:59], v1 offset:1024\n\t" \
    "s_waitcnt lgkmcnt(5)\n\t" \
    "v_mfma_f32_16x16x32_bf16 v[92:95], a[52:55], v[36:39], v[92:95]\n\t" \
    "ds_read_b128 v[60:63], v1 offset:19456\n\t" \
    "v_mfma_f32_16x16x32_bf16 v[96:99], a[124:127], v[32:35], v[96:99]\n\t" \
    "v_mfma_f32_16x16x32_bf16 v[100:103], a[124:127], v[36:39], v[100:103]\n\t" \
    "s_waitcnt lgkmcnt(5)\n\t" \
    "v_mfma_f32_16x16x32_bf16 v[88:91], a[56:59], v[40:43], v[88:91]\n\t" \
    "ds_read_b128 v[64:67], v2 offset:1024\n\t" \
    "s_waitcnt lgkmcnt(5)\n\t" \
    "v_mfma_f32_16x16x32_bf16 v[92:95], a[56:59], v[44:47], v[92:95]\n\t" \
    "ds_read_b128 v[68:71], v2 offset:19456\n\t" \
    "v_mfma_f32_16x16x32_bf16 v[96:99], a[128:131], v[40:43], v[96:99]\n\t" \
    "v_mfma_f32_16x16x32_bf16 v[100:103], a[128:131], v[44:47], v[100:103]\n\t" \
    "s_waitcnt lgkmcnt(5)\n\t" \
    "v_mfma_f32_16x16x32_bf16 v[88:91], a[60:63], v[48:51], v[88:91]\n\t" \
    "ds_read_b128 v[24:27], v22 offset:0\n\t" \
    "s_waitcnt lgkmcnt(5)\n\t" \
    "v_mfma_f32_16x16x32_bf16 v[92:95], a[60:63], v[52:55], v[92:95]\n\t" \
    "ds_read_b128 v[28:31], v22 offset:18432\n\t" \
    "v_mfma_f32_16x16x32_bf16 v[96:99], a[132:135], v[48:51], v[96:99]\n\t" \
    "v_mfma_f32_16x16x32_bf16 v[100:103], a[132:135], v[52:55], v[100:103]\n\t" \
    "s_waitcnt lgkmcnt(5)\n\t" \
    "v_mfma_f32_16x16x32_bf16 v[88:91], a[64:67], v[56:59], v[88:91]\n\t" \
    "ds_read_b128 v[32:35], v23 offset:0\n\t" \
    "s_waitcnt lgkmcnt(5)\n\t" \
    "v_mfma_f32_16x16x32_bf16 v[92:95], a[64:67], v[60:63], v[92:95]\n\t" \
    "ds_read_b128 v[36:39], v23 offset:18432\n\t" \
    "v_mfma_f32_16x16x32_bf16 v[96:99], a[136:139], v[56:59], v[96:99]\n\t" \
    "v_mfma_f32_16x16x32_bf16 v[100:103], a[136:139], v[60:63], v[100:103]\n\t" \
    "s_waitcnt lgkmcnt(5)\n\t" \
    "v_mfma_f32_16x16x32_bf16 v[88:91], a[68:71], v[64:67], v[88:91]\n\t" \
    "ds_read_b128 v[40:43], v22 offset:128\n\t" \
    "s_waitcnt lgkmcnt(5)\n\t" \
    "v_mfma_f32_16x16x32_bf16 v[92:95], a[68:71], v[68:71], v[92:95]\n\t" \
    "ds_read_b128 v[44:47], v22 offset:18560\n\t" \
    "v_mfma_f32_16x16x32_bf16 v[96:99], a[140:143], v[64:67], v[96:99]\n\t" \
    "v_mfma_f32_16x16x32_bf16 v[100:103], a[140:143], v[68:71], v[100:103]\n\t" \
    "v_mov_b32 v1, v22\n\t" \
    "v_mov_b32 v2, v23\n\t" \
    "v_cvt_pk_bf16_f32 v104, v88, v89\n\t" \
    "v_cvt_pk_bf16_f32 v105, v90, v91\n\t" \
    "s_nop 2\n\t" \
    "v_cvt_pk_bf16_f32 v106, v96, v97\n\t" \
    "v_cvt_pk_bf16_f32 v107, v98, v99\n\t" \
    "global_store_dwordx4 v8, v[104:107], s[38:39] offset:0 sc1\n\t" \
    "v_cvt_pk_bf16_f32 v108, v92, v93\n\t" \
    "v_cvt_pk_bf16_f32 v109, v94, v95\n\t" \
    "v_cvt_pk_bf16_f32 v110, v100, v101\n\t" \
    "v_cvt_pk_bf16_f32 v111, v102, v103\n\t" \
    "global_store_dwordx4 v8, v[108:111], s[38:39] offset:1024 sc1\n\t" \
    "s_add_u32 s65, s65, 1\n\t" \
    "s_cmp_lt_u32 s65, 64\n\t" \
    "s_cbranch_scc1 gx_l315_%=\n\t" \
    "s_waitcnt vmcnt(0) lgkmcnt(0)\n\t" \
    "s_barrier\n\t" \
    "s_cmp_lg_u32 s66, 0\n\t" \
    "s_cbranch_scc1 gx_SF18_%=\n\t" \
    "s_lshl_b32 s69, s67, 7\n\t" \
    "s_add_u32 s69, s69, 3211264\n\t" \
    "s_add_u32 s70, s65, -1\n\t" \
    "v_mov_b32 v156, s69\n\t" \
    "v_mov_b32 v157, s70\n\t" \
    "s_mov_b64 exec, 1\n\t" \
    "global_store_dword v156, v157, s[100:101] sc1\n\t" \
    "s_mov_b64 exec, -1\n\t" \
    "gx_SF18_%=:\n\t" \
    "gx_last_%=:\n\t" \
    ""
#define ASM_GV \
    "s_load_dwordx2 s[42:43], %3, 56\n\t" \
    "s_load_dwordx2 s[44:45], %3, 64\n\t" \
    "s_load_dwordx2 s[36:37], %3, 0x58\n\t" \
    "s_load_dwordx2 s[50:51], %3, 0x48\n\t" \
    "s_mov_b32 s66, %4\n\t" \
    "s_mov_b32 s67, %5\n\t" \
    "s_mov_b32 s68, 262144\n\t" \
    "s_mov_b32 s72, 0\n\t" \
    "s_mov_b32 s83, -1\n\t" \
    "s_mov_b32 s65, 0\n\t" \
    "s_lshl_b32 s82, s66, 10\n\t" \
    "s_waitcnt lgkmcnt(0)\n\t" \
    "s_lshr_b32 s65, s67, 5\n\t" \
    "s_cselect_b32 s88, 24, 64\n\t" \
    "s_cmp_lt_u32 s65, 24\n\t" \
    "s_addc_u32 s84, s65, 1\n\t" \
    "s_cmp_lt_u32 s84, 24\n\t" \
    "s_addc_u32 s85, s84, 1\n\t" \
    "s_mov_b32 s86, -1\n\t" \
    "s_mov_b32 s87, -1\n\t" \
    "s_add_u32 s38, s36, 48234496\n\t" \
    "s_addc_u32 s39, s37, 0\n\t" \
    "s_and_b32 s69, s67, 31\n\t" \
    "s_mul_i32 s69, s69, 147456\n\t" \
    "s_add_u32 s44, s44, s69\n\t" \
    "s_addc_u32 s45, s45, 0\n\t" \
    "s_mul_i32 s89, s66, 9216\n\t" \
    "s_mov_b32 s90, 0\n\t" \
    "v_mov_b32 v196, -1\n\t" \
    "v_mov_b32 v197, -1\n\t" \
    "v_mov_b32 v198, -1\n\t" \
    "v_mov_b32 v199, -1\n\t" \
    "s_add_u32 s46, s42, 2097152\n\t" \
    "s_addc_u32 s47, s43, 0\n\t" \
    "s_add_u32 m0, s82, 65536\n\t" \
    "s_nop 0\n\t" \
    "global_load_lds_dwordx4 v200, s[46:47]\n\t" \
    "s_add_u32 s46, s42, 2228224\n\t" \
    "s_addc_u32 s47, s43, 0\n\t" \
    "s_add_u32 m0, s82, 69632\n\t" \
    "s_nop 0\n\t" \
    "global_load_lds_dwordx4 v200, s[46:47]\n\t" \
    "s_add_u32 s46, s42, 2359296\n\t" \
    "s_addc_u32 s47, s43, 0\n\t" \
    "s_add_u32 m0, s82, 73728\n\t" \
    "s_nop 0\n\t" \
    "global_load_lds_dwordx4 v200, s[46:47]\n\t" \
    "s_add_u32 s46, s42, 2490368\n\t" \
    "s_addc_u32 s47, s43, 0\n\t" \
    "s_add_u32 m0, s82, 77824\n\t" \
    "s_nop 0\n\t" \
    "global_load_lds_dwordx4 v200, s[46:47]\n\t" \
    "s_add_u32 s46, s42, 2621440\n\t" \
    "s_addc_u32 s47, s43, 0\n\t" \
    "s_add_u32 m0, s82, 81920\n\t" \
    "s_nop 0\n\t" \
    "global_load_lds_dwordx4 v200, s[46:47]\n\t" \
    "s_add_u32 s46, s42, 2752512\n\t" \
    "s_addc_u32 s47, s43, 0\n\t" \
    "s_add_u32 m0, s82, 86016\n\t" \
    "s_nop 0\n\t" \
    "global_load_lds_dwordx4 v200, s[46:47]\n\t" \
    "s_add_u32 s46, s42, 2883584\n\t" \
    "s_addc_u32 s47, s43, 0\n\t" \
    "s_add_u32 m0, s82, 90112\n\t" \
    "s_nop 0\n\t" \
    "global_load_lds_dwordx4 v200, s[46:47]\n\t" \
    "s_add_u32 s46, s42, 3014656\n\t" \
    "s_addc_u32 s47, s43, 0\n\t" \
    "s_add_u32 m0, s82, 94208\n\t" \
    "s_nop 0\n\t" \
    "global_load_lds_dwordx4 v200, s[46:47]\n\t" \
    "s_add_u32 s46, s42, 3145728\n\t" \
    "s_addc_u32 s47, s43, 0\n\t" \
    "s_add_u32 m0, s82, 98304\n\t" \
    "s_nop 0\n\t" \
    "global_load_lds_dwordx4 v200, s[46:47]\n\t" \
    "s_add_u32 s46, s42, 3276800\n\t" \
    "s_addc_u32 s47, s43, 0\n\t" \
    "s_add_u32 m0, s82, 102400\n\t" \
    "s_nop 0\n\t" \
    "global_load_lds_dwordx4 v200, s[46:47]\n\t" \
    "s_add_u32 s46, s42, 3407872\n\t" \
    "s_addc_u32 s47, s43, 0\n\t" \
    "s_add_u32 m0, s82, 106496\n\t" \
    "s_nop 0\n\t" \
    "global_load_lds_dwordx4 v200, s[46:47]\n\t" \
    "s_add_u32 s46, s42, 3538944\n\t" \
    "s_addc_u32 s47, s43, 0\n\t" \
    "s_add_u32 m0, s82, 110592\n\t" \
    "s_nop 0\n\t" \
    "global_load_lds_dwordx4 v200, s[46:47]\n\t" \
    "s_add_u32 s46, s42, 3670016\n\t" \
    "s_addc_u32 s47, s43, 0\n\t" \
    "s_add_u32 m0, s82, 114688\n\t" \
    "s_nop 0\n\t" \
    "global_load_lds_dwordx4 v200, s[46:47]\n\t" \
    "s_add_u32 s46, s42, 3801088\n\t" \
    "s_addc_u32 s47, s43, 0\n\t" \
    "s_add_u32 m0, s82, 118784\n\t" \
    "s_nop 0\n\t" \
    "global_load_lds_dwordx4 v200, s[46:47]\n\t" \
    "s_add_u32 s46, s42, 3932160\n\t" \
    "s_addc_u32 s47, s43, 0\n\t" \
    "s_add_u32 m0, s82, 122880\n\t" \
    "s_nop 0\n\t" \
    "global_load_lds_dwordx4 v200, s[46:47]\n\t" \
    "s_add_u32 s46, s42, 4063232\n\t" \
    "s_addc_u32 s47, s43, 0\n\t" \
    "s_add_u32 m0, s82, 126976\n\t" \
    "s_nop 0\n\t" \
    "global_load_lds_dwordx4 v200, s[46:47]\n\t" \
    "s_add_u32 s46, s42, 4194304\n\t" \
    "s_addc_u32 s47, s43, 0\n\t" \
    "s_add_u32 m0, s82, 131072\n\t" \
    "s_nop 0\n\t" \
    "global_load_lds_dwordx4 v200, s[46:47]\n\t" \
    "s_add_u32 s46, s42, 4325376\n\t" \
    "s_addc_u32 s47, s43, 0\n\t" \
    "s_add_u32 m0, s82, 135168\n\t" \
    "s_nop 0\n\t" \
    "global_load_lds_dwordx4 v200, s[46:47]\n\t" \
    "s_add_u32 s46, s42, 4456448\n\t" \
    "s_addc_u32 s47, s43, 0\n\t" \
    "s_add_u32 m0, s82, 139264\n\t" \
    "s_nop 0\n\t" \
    "global_load_lds_dwordx4 v200, s[46:47]\n\t" \
    "s_add_u32 s46, s42, 4587520\n\t" \
    "s_addc_u32 s47, s43, 0\n\t" \
    "s_add_u32 m0, s82, 143360\n\t" \
    "s_nop 0\n\t" \
    "global_load_lds_dwordx4 v200, s[46:47]\n\t" \
    "s_waitcnt vmcnt(32) lgkmcnt(0)\n\t" \
    "s_barrier\n\t" \
    "ds_read_b64_tr_b16 a[0:1], v201 offset:0\n\t" \
    "ds_read_b64_tr_b16 a[2:3], v201 offset:1024\n\t" \
    "ds_read_b64_tr_b16 a[4:5], v201 offset:8192\n\t" \
    "ds_read_b64_tr_b16 a[6:7], v201 offset:9216\n\t" \
    "ds_read_b64_tr_b16 a[72:73], v202 offset:0\n\t" \
    "ds_read_b64_tr_b16 a[74:75], v202 offset:1024\n\t" \
    "ds_read_b64_tr_b16 a[76:77], v202 offset:8192\n\t" \
    "ds_read_b64_tr_b16 a[78:79], v202 offset:9216\n\t" \
    "s_waitcnt vmcnt(28) lgkmcnt(0)\n\t" \
    "s_barrier\n\t" \
    "ds_read_b64_tr_b16 a[8:9], v201 offset:16384\n\t" \
    "ds_read_b64_tr_b16 a[10:11], v201 offset:17408\n\t" \
    "ds_read_b64_tr_b16 a[12:13], v201 offset:24576\n\t" \
    "ds_read_b64_tr_b16 a[14:15], v201 offset:25600\n\t" \
    "ds_read_b64_tr_b16 a[80:81], v202 offset:16384\n\t" \
    "ds_read_b64_tr_b16 a[82:83], v202 offset:17408\n\t" \
    "ds_read_b64_tr_b16 a[84:85], v202 offset:24576\n\t" \
    "ds_read_b64_tr_b16 a[86:87], v202 offset:25600\n\t" \
    "s_waitcnt vmcnt(24) lgkmcnt(0)\n\t" \
    "s_barrier\n\t" \
    "ds_read_b64_tr_b16 a[16:17], v201 offset:32768\n\t" \
    "ds_read_b64_tr_b16 a[18:19], v201 offset:33792\n\t" \
    "ds_read_b64_tr_b16 a[20:21], v201 offset:40960\n\t" \
    "ds_read_b64_tr_b16 a[22:23], v201 offset:41984\n\t" \
    "ds_read_b64_tr_b16 a[88:89], v202 offset:32768\n\t" \
    "ds_read_b64_tr_b16 a[90:91], v202 offset:33792\n\t" \
    "ds_read_b64_tr_b16 a[92:93], v202 offset:40960\n\t" \
    "ds_read_b64_tr_b16 a[94:95], v202 offset:41984\n\t" \
    "s_waitcnt vmcnt(20) lgkmcnt(0)\n\t" \
    "s_barrier\n\t" \
    "ds_read_b64_tr_b16 a[24:25], v201 offset:49152\n\t" \
    "ds_read_b64_tr_b16 a[26:27], v201 offset:50176\n\t" \
    "ds_read_b64_tr_b16 a[28:29], v201 offset:57344\n\t" \
    "ds_read_b64_tr_b16 a[30:31], v201 offset:58368\n\t" \
    "ds_read_b64_tr_b16 a[96:97], v202 offset:49152\n\t" \
    "ds_read_b64_tr_b16 a[98:99], v202 offset:50176\n\t" \
    "ds_read_b64_tr_b16 a[100:101], v202 offset:57344\n\t" \
    "ds_read_b64_tr_b16 a[102:103], v202 offset:58368\n\t" \
    "s_add_u32 s46, s44, 0\n\t" \
    "s_addc_u32 s47, s45, 0\n\t" \
    "s_mul_i32 s69, s82, 9\n\t" \
    "s_add_u32 m0, s69, 0\n\t" \
    "s_nop 0\n\t" \
    "global_load_lds_dwordx4 v203, s[46:47]\n\t" \
    "s_add_u32 m0, s69, 1024\n\t" \
    "s_nop 0\n\t" \
    "global_load_lds_dwordx4 v204, s[46:47]\n\t" \
    "s_add_u32 m0, s69, 2048\n\t" \
    "s_nop 0\n\t" \
    "global_load_lds_dwordx4 v205, s[46:47]\n\t" \
    "s_add_u32 m0, s69, 3072\n\t" \
    "s_nop 0\n\t" \
    "global_load_lds_dwordx4 v206, s[46:47]\n\t" \
    "s_add_u32 m0, s69, 4096\n\t" \
    "s_nop 0\n\t" \
    "global_load_lds_dwordx4 v207, s[46:47]\n\t" \
    "s_add_u32 m0, s69, 5120\n\t" \
    "s_nop 0\n\t" \
    "global_load_lds_dwordx4 v208, s[46:47]\n\t" \
    "s_add_u32 m0, s69, 6144\n\t" \
    "s_nop 0\n\t" \
    "global_load_lds_dwordx4 v209, s[46:47]\n\t" \
    "s_add_u32 m0, s69, 7168\n\t" \
    "s_nop 0\n\t" \
    "global_load_lds_dwordx4 v210, s[46:47]\n\t" \
    "s_add_u32 m0, s69, 8192\n\t" \
    "s_nop 0\n\t" \
    "global_load_lds_dwordx4 v211, s[46:47]\n\t" \
    "s_min_u32 s69, s65, 63\n\t" \
    "s_mul_i32 s69, s69, 36864\n\t" \
    "s_add_u32 s52, s50, s69\n\t" \
    "s_addc_u32 s53, s51, 0\n\t" \
    "global_load_dwordx4 v[120:123], v203, s[52:53]\n\t" \
    "global_load_dwordx4 v[124:127], v204, s[52:53]\n\t" \
    "global_load_dwordx4 v[128:131], v205, s[52:53]\n\t" \
    "global_load_dwordx4 v[132:135], v206, s[52:53]\n\t" \
    "global_load_dwordx4 v[136:139], v207, s[52:53]\n\t" \
    "global_load_dwordx4 v[140:143], v208, s[52:53]\n\t" \
    "global_load_dwordx4 v[144:147], v209, s[52:53]\n\t" \
    "global_load_dwordx4 v[148:151], v210, s[52:53]\n\t" \
    "global_load_dwordx4 v[152:155], v211, s[52:53]\n\t" \
    "s_waitcnt vmcnt(34) lgkmcnt(0)\n\t" \
    "s_barrier\n\t" \
    "v_add_u32 v201, 0x10000, v201\n\t" \
    "v_add_u32 v202, 0x10000, v202\n\t" \
    "ds_read_b64_tr_b16 a[32:33], v201 offset:0\n\t" \
    "ds_read_b64_tr_b16 a[34:35], v201 offset:1024\n\t" \
    "ds_read_b64_tr_b16 a[36:37], v201 offset:8192\n\t" \
    "ds_read_b64_tr_b16 a[38:39], v201 offset:9216\n\t" \
    "ds_read_b64_tr_b16 a[104:105], v202 offset:0\n\t" \
    "ds_read_b64_tr_b16 a[106:107], v202 offset:1024\n\t" \
    "ds_read_b64_tr_b16 a[108:109], v202 offset:8192\n\t" \
    "ds_read_b64_tr_b16 a[110:111], v202 offset:9216\n\t" \
    "s_waitcnt vmcnt(30) lgkmcnt(0)\n\t" \
    "s_barrier\n\t" \
    "ds_read_b64_tr_b16 a[40:41], v201 offset:16384\n\t" \
    "ds_read_b64_tr_b16 a[42:43], v201 offset:17408\n\t" \
    "ds_read_b64_tr_b16 a[44:45], v201 offset:24576\n\t" \
    "ds_read_b64_tr_b16 a[46:47], v201 offset:25600\n\t" \
    "ds_read_b64_tr_b16 a[112:113], v202 offset:16384\n\t" \
    "ds_read_b64_tr_b16 a[114:115], v202 offset:17408\n\t" \
    "ds_read_b64_tr_b16 a[116:117], v202 offset:24576\n\t" \
    "ds_read_b64_tr_b16 a[118:119], v202 offset:25600\n\t" \
    "s_add_u32 s46, s44, 36864\n\t" \
    "s_addc_u32 s47, s45, 0\n\t" \
    "s_mul_i32 s69, s82, 9\n\t" \
    "s_add_u32 m0, s69, 36864\n\t" \
    "s_nop 0\n\t" \
    "global_load_lds_dwordx4 v203, s[46:47]\n\t" \
    "s_add_u32 m0, s69, 37888\n\t" \
    "s_nop 0\n\t" \
    "global_load_lds_dwordx4 v204, s[46:47]\n\t" \
    "s_add_u32 m0, s69, 38912\n\t" \
    "s_nop 0\n\t" \
    "global_load_lds_dwordx4 v205, s[46:47]\n\t" \
    "s_add_u32 m0, s69, 39936\n\t" \
    "s_nop 0\n\t" \
    "global_load_lds_dwordx4 v206, s[46:47]\n\t" \
    "s_add_u32 m0, s69, 40960\n\t" \
    "s_nop 0\n\t" \
    "global_load_lds_dwordx4 v207, s[46:47]\n\t" \
    "s_add_u32 m0, s69, 41984\n\t" \
    "s_nop 0\n\t" \
    "global_load_lds_dwordx4 v208, s[46:47]\n\t" \
    "s_add_u32 m0, s69, 43008\n\t" \
    "s_nop 0\n\t" \
    "global_load_lds_dwordx4 v209, s[46:47]\n\t" \
    "s_add_u32 m0, s69, 44032\n\t" \
    "s_nop 0\n\t" \
    "global_load_lds_dwordx4 v210, s[46:47]\n\t" \
    "s_add_u32 m0, s69, 45056\n\t" \
    "s_nop 0\n\t" \
    "global_load_lds_dwordx4 v211, s[46:47]\n\t" \
    "s_waitcnt vmcnt(35) lgkmcnt(0)\n\t" \
    "s_barrier\n\t" \
    "ds_read_b64_tr_b16 a[48:49], v201 offset:32768\n\t" \
    "ds_read_b64_tr_b16 a[50:51], v201 offset:33792\n\t" \
    "ds_read_b64_tr_b16 a[52:53], v201 offset:40960\n\t" \
    "ds_read_b64_tr_b16 a[54:55], v201 offset:41984\n\t" \
    "ds_read_b64_tr_b16 a[120:121], v202 offset:32768\n\t" \
    "ds_read_b64_tr_b16 a[122:123], v202 offset:33792\n\t" \
    "ds_read_b64_tr_b16 a[124:125], v202 offset:40960\n\t" \
    "ds_read_b64_tr_b16 a[126:127], v202 offset:41984\n\t" \
    "s_waitcnt vmcnt(31) lgkmcnt(0)\n\t" \
    "s_barrier\n\t" \
    "ds_read_b64_tr_b16 a[56:57], v201 offset:49152\n\t" \
    "ds_read_b64_tr_b16 a[58:59], v201 offset:50176\n\t" \
    "ds_read_b64_tr_b16 a[60:61], v201 offset:57344\n\t" \
    "ds_read_b64_tr_b16 a[62:63], v201 offset:58368\n\t" \
    "ds_read_b64_tr_b16 a[128:129], v202 offset:49152\n\t" \
    "ds_read_b64_tr_b16 a[130:131], v202 offset:50176\n\t" \
    "ds_read_b64_tr_b16 a[132:133], v202 offset:57344\n\t" \
    "ds_read_b64_tr_b16 a[134:135], v202 offset:58368\n\t" \
    "s_add_u32 s46, s44, 73728\n\t" \
    "s_addc_u32 s47, s45, 0\n\t" \
    "s_mul_i32 s69, s82, 9\n\t" \
    "s_add_u32 m0, s69, 73728\n\t" \
    "s_nop 0\n\t" \
    "global_load_lds_dwordx4 v203, s[46:47]\n\t" \
    "s_add_u32 m0, s69, 74752\n\t" \
    "s_nop 0\n\t" \
    "global_load_lds_dwordx4 v204, s[46:47]\n\t" \
    "s_add_u32 m0, s69, 75776\n\t" \
    "s_nop 0\n\t" \
    "global_load_lds_dwordx4 v205, s[46:47]\n\t" \
    "s_add_u32 m0, s69, 76800\n\t" \
    "s_nop 0\n\t" \
    "global_load_lds_dwordx4 v206, s[46:47]\n\t" \
    "s_add_u32 m0, s69, 77824\n\t" \
    "s_nop 0\n\t" \
    "global_load_lds_dwordx4 v207, s[46:47]\n\t" \
    "s_add_u32 m0, s69, 78848\n\t" \
    "s_nop 0\n\t" \
    "global_load_lds_dwordx4 v208, s[46:47]\n\t" \
    "s_add_u32 m0, s69, 79872\n\t" \
    "s_nop 0\n\t" \
    "global_load_lds_dwordx4 v209, s[46:47]\n\t" \
    "s_add_u32 m0, s69, 80896\n\t" \
    "s_nop 0\n\t" \
    "global_load_lds_dwordx4 v210, s[46:47]\n\t" \
    "s_add_u32 m0, s69, 81920\n\t" \
    "s_nop 0\n\t" \
    "global_load_lds_dwordx4 v211, s[46:47]\n\t" \
    "v_mbcnt_lo_u32_b32 v164, -1, 0\n\t" \
    "v_mbcnt_hi_u32_b32 v164, -1, v164\n\t" \
    "v_lshlrev_b32 v164, 6, v164\n\t" \
    "v_add_u32 v164, 6225920, v164\n\t" \
    "v_mov_b32 v165, 6234112\n\t" \
    "global_load_dword v165, v165, s[36:37] sc1\n\t" \
    "global_load_dwordx2 v[166:167], v164, s[36:37] sc1\n\t" \
    "global_load_dword v168, v164, s[36:37] offset:2048 sc1\n\t" \
    "s_waitcnt vmcnt(39) lgkmcnt(0)\n\t" \
    "s_barrier\n\t" \
    "v_add_u32 v201, 0x10000, v201\n\t" \
    "v_add_u32 v202, 0x10000, v202\n\t" \
    "ds_read_b64_tr_b16 a[64:65], v201 offset:0\n\t" \
    "ds_read_b64_tr_b16 a[66:67], v201 offset:1024\n\t" \
    "ds_read_b64_tr_b16 a[68:69], v201 offset:8192\n\t" \
    "ds_read_b64_tr_b16 a[70:71], v201 offset:9216\n\t" \
    "ds_read_b64_tr_b16 a[136:137], v202 offset:0\n\t" \
    "ds_read_b64_tr_b16 a[138:139], v202 offset:1024\n\t" \
    "ds_read_b64_tr_b16 a[140:141], v202 offset:8192\n\t" \
    "ds_read_b64_tr_b16 a[142:143], v202 offset:9216\n\t" \
    "v_add_u32 v201, 0xfffe0000, v201\n\t" \
    "v_add_u32 v202, 0xfffe0000, v202\n\t" \
    "gv_jn2_%=:\n\t" \
    "s_waitcnt lgkmcnt(0)\n\t" \
    "s_barrier\n\t" \
    "s_add_u32 s46, s44, 110592\n\t" \
    "s_addc_u32 s47, s45, 0\n\t" \
    "s_mul_i32 s69, s82, 9\n\t" \
    "s_add_u32 m0, s69, 110592\n\t" \
    "s_nop 0\n\t" \
    "global_load_lds_dwordx4 v203, s[46:47]\n\t" \
    "s_add_u32 m0, s69, 111616\n\t" \
    "s_nop 0\n\t" \
    "global_load_lds_dwordx4 v204, s[46:47]\n\t" \
    "s_add_u32 m0, s69, 112640\n\t" \
    "s_nop 0\n\t" \
    "global_load_lds_dwordx4 v205, s[46:47]\n\t" \
    "s_add_u32 m0, s69, 113664\n\t" \
    "s_nop 0\n\t" \
    "global_load_lds_dwordx4 v206, s[46:47]\n\t" \
    "s_add_u32 m0, s69, 114688\n\t" \
    "s_nop 0\n\t" \
    "global_load_lds_dwordx4 v207, s[46:47]\n\t" \
    "s_add_u32 m0, s69, 115712\n\t" \
    "s_nop 0\n\t" \
    "global_load_lds_dwordx4 v208, s[46:47]\n\t" \
    "s_add_u32 m0, s69, 116736\n\t" \
    "s_nop 0\n\t" \
    "global_load_lds_dwordx4 v209, s[46:47]\n\t" \
    "s_add_u32 m0, s69, 117760\n\t" \
    "s_nop 0\n\t" \
    "global_load_lds_dwordx4 v210, s[46:47]\n\t" \
    "s_add_u32 m0, s69, 118784\n\t" \
    "s_nop 0\n\t" \
    "global_load_lds_dwordx4 v211, s[46:47]\n\t" \
    "s_waitcnt vmcnt(39) lgkmcnt(0)\n\t" \
    "s_barrier\n\t" \
    "ds_read_b64_tr_b16 a[144:145], v212 offset:0\n\t" \
    "ds_read_b64_tr_b16 a[146:147], v212 offset:18432\n\t" \
    "ds_read_b64_tr_b16 a[160:161], v213 offset:0\n\t" \
    "ds_read_b64_tr_b16 a[162:163], v213 offset:18432\n\t" \
    "ds_read_b64_tr_b16 a[176:177], v214 offset:0\n\t" \
    "ds_read_b64_tr_b16 a[178:179], v214 offset:18432\n\t" \
    "s_waitcnt lgkmcnt(6)\n\t" \
    "ds_read_b64_tr_b16 a[192:193], v215 offset:0\n\t" \
    "ds_read_b64_tr_b16 a[194:195], v215 offset:18432\n\t" \
    "ds_read_b64_tr_b16 a[208:209], v212 offset:128\n\t" \
    "ds_read_b64_tr_b16 a[210:211], v212 offset:18560\n\t" \
    "ds_read_b64_tr_b16 a[224:225], v213 offset:128\n\t" \
    "ds_read_b64_tr_b16 a[226:227], v213 offset:18560\n\t" \
    "s_waitcnt lgkmcnt(6)\n\t" \
    "ds_read_b64_tr_b16 a[240:241], v214 offset:128\n\t" \
    "ds_read_b64_tr_b16 a[242:243], v214 offset:18560\n\t" \
    "ds_read_b64_tr_b16 v[224:225], v215 offset:128\n\t" \
    "ds_read_b64_tr_b16 v[226:227], v215 offset:18560\n\t" \
    "ds_read_b64_tr_b16 v[240:241], v212 offset:256\n\t" \
    "ds_read_b64_tr_b16 v[242:243], v212 offset:18688\n\t" \
    "s_waitcnt lgkmcnt(6)\n\t" \
    "v_add_u32 v212, 0x9000, v212\n\t" \
    "v_add_u32 v213, 0x9000, v213\n\t" \
    "v_add_u32 v214, 0x9000, v214\n\t" \
    "v_add_u32 v215, 0x9000, v215\n\t" \
    "s_waitcnt vmcnt(21) lgkmcnt(0)\n\t" \
    "s_barrier\n\t" \
    "ds_read_b64_tr_b16 a[148:149], v212 offset:0\n\t" \
    "ds_read_b64_tr_b16 a[150:151], v212 offset:18432\n\t" \
    "ds_read_b64_tr_b16 a[164:165], v213 offset:0\n\t" \
    "ds_read_b64_tr_b16 a[166:167], v213 offset:18432\n\t" \
    "ds_read_b64_tr_b16 a[180:181], v214 offset:0\n\t" \
    "ds_read_b64_tr_b16 a[182:183], v214 offset:18432\n\t" \
    "s_waitcnt lgkmcnt(6)\n\t" \
    "ds_read_b64_tr_b16 a[196:197], v215 offset:0\n\t" \
    "ds_read_b64_tr_b16 a[198:199], v215 offset:18432\n\t" \
    "ds_read_b64_tr_b16 a[212:213], v212 offset:128\n\t" \
    "ds_read_b64_tr_b16 a[214:215], v212 offset:18560\n\t" \
    "ds_read_b64_tr_b16 a[228:229], v213 offset:128\n\t" \
    "ds_read_b64_tr_b16 a[230:231], v213 offset:18560\n\t" \
    "s_waitcnt lgkmcnt(6)\n\t" \
    "ds_read_b64_tr_b16 a[244:245], v214 offset:128\n\t" \
    "ds_read_b64_tr_b16 a[246:247], v214 offset:18560\n\t" \
    "ds_read_b64_tr_b16 v[228:229], v215 offset:128\n\t" \
    "ds_read_b64_tr_b16 v[230:231], v215 offset:18560\n\t" \
    "ds_read_b64_tr_b16 v[244:245], v212 offset:256\n\t" \
    "ds_read_b64_tr_b16 v[246:247], v212 offset:18688\n\t" \
    "s_waitcnt lgkmcnt(6)\n\t" \
    "v_add_u32 v212, 0x9000, v212\n\t" \
    "v_add_u32 v213, 0x9000, v213\n\t" \
    "v_add_u32 v214, 0x9000, v214\n\t" \
    "v_add_u32 v215, 0x9000, v215\n\t" \
    "ds_write_b128 v3, v[120:123] offset:0\n\t" \
    "ds_write_b128 v3, v[124:127] offset:1024\n\t" \
    "ds_write_b128 v3, v[128:131] offset:2048\n\t" \
    "ds_write_b128 v3, v[132:135] offset:3072\n\t" \
    "ds_write_b128 v3, v[136:139] offset:4096\n\t" \
    "ds_write_b128 v3, v[140:143] offset:5120\n\t" \
    "ds_write_b128 v3, v[144:147] offset:6144\n\t" \
    "ds_write_b128 v3, v[148:151] offset:7168\n\t" \
    "ds_write_b128 v3, v[152:155] offset:8192\n\t" \
    "s_waitcnt vmcnt(12) lgkmcnt(0)\n\t" \
    "s_barrier\n\t" \
    "ds_read_b64_tr_b16 a[152:153], v212 offset:0\n\t" \
    "ds_read_b64_tr_b16 a[154:155], v212 offset:18432\n\t" \
    "ds_read_b64_tr_b16 a[168:169], v213 offset:0\n\t" \
    "ds_read_b64_tr_b16 a[170:171], v213 offset:18432\n\t" \
    "ds_read_b64_tr_b16 a[184:185], v214 offset:0\n\t" \
    "ds_read_b64_tr_b16 a[186:187], v214 offset:18432\n\t" \
    "s_waitcnt lgkmcnt(6)\n\t" \
    "ds_read_b64_tr_b16 a[200:201], v215 offset:0\n\t" \
    "ds_read_b64_tr_b16 a[202:203], v215 offset:18432\n\t" \
    "ds_read_b64_tr_b16 a[216:217], v212 offset:128\n\t" \
    "ds_read_b64_tr_b16 a[218:219], v212 offset:18560\n\t" \
    "ds_read_b64_tr_b16 a[232:233], v213 offset:128\n\t" \
    "ds_read_b64_tr_b16 a[234:235], v213 offset:18560\n\t" \
    "s_waitcnt lgkmcnt(6)\n\t" \
    "ds_read_b64_tr_b16 a[248:249], v214 offset:128\n\t" \
    "ds_read_b64_tr_b16 a[250:251], v214 offset:18560\n\t" \
    "ds_read_b64_tr_b16 v[232:233], v215 offset:128\n\t" \
    "ds_read_b64_tr_b16 v[234:235], v215 offset:18560\n\t" \
    "ds_read_b64_tr_b16 v[248:249], v212 offset:256\n\t" \
    "ds_read_b64_tr_b16 v[250:251], v212 offset:18688\n\t" \
    "s_waitcnt lgkmcnt(6)\n\t" \
    "v_add_u32 v212, 0x9000, v212\n\t" \
    "v_add_u32 v213, 0x9000, v213\n\t" \
    "v_add_u32 v214, 0x9000, v214\n\t" \
    "v_add_u32 v215, 0x9000, v215\n\t" \
    "s_min_u32 s69, s84, 63\n\t" \
    "s_mul_i32 s69, s69, 36864\n\t" \
    "s_add_u32 s52, s50, s69\n\t" \
    "s_addc_u32 s53, s51, 0\n\t" \
    "s_add_u32 m0, s89, 36864\n\t" \
    "s_nop 0\n\t" \
    "global_load_lds_dwordx4 v203, s[52:53]\n\t" \
    "s_add_u32 m0, s89, 37888\n\t" \
    "s_nop 0\n\t" \
    "global_load_lds_dwordx4 v204, s[52:53]\n\t" \
    "s_add_u32 m0, s89, 38912\n\t" \
    "s_nop 0\n\t" \
    "global_load_lds_dwordx4 v205, s[52:53]\n\t" \
    "s_add_u32 m0, s89, 39936\n\t" \
    "s_nop 0\n\t" \
    "global_load_lds_dwordx4 v206, s[52:53]\n\t" \
    "s_add_u32 m0, s89, 40960\n\t" \
    "s_nop 0\n\t" \
    "global_load_lds_dwordx4 v207, s[52:53]\n\t" \
    "s_add_u32 m0, s89, 41984\n\t" \
    "s_nop 0\n\t" \
    "global_load_lds_dwordx4 v208, s[52:53]\n\t" \
    "s_add_u32 m0, s89, 43008\n\t" \
    "s_nop 0\n\t" \
    "global_load_lds_dwordx4 v209, s[52:53]\n\t" \
    "s_add_u32 m0, s89, 44032\n\t" \
    "s_nop 0\n\t" \
    "global_load_lds_dwordx4 v210, s[52:53]\n\t" \
    "s_add_u32 m0, s89, 45056\n\t" \
    "s_nop 0\n\t" \
    "global_load_lds_dwordx4 v211, s[52:53]\n\t" \
    "s_waitcnt vmcnt(18)\n\t" \
    "gv_C3_%=:\n\t" \
    "v_cmp_ne_u32 s[54:55], v166, v168\n\t" \
    "v_sub_u32 v166, v166, v165\n\t" \
    "v_cmp_ne_u32 vcc, 1, v166\n\t" \
    "s_nop 1\n\t" \
    "s_or_b64 vcc, vcc, s[54:55]\n\t" \
    "s_cbranch_scc0 gv_Cok4_%=\n\t" \
    "s_cmp_eq_u32 s68, 0\n\t" \
    "s_cbranch_scc1 gv_F6_%=\n\t" \
    "s_sub_u32 s68, s68, 1\n\t" \
    "s_sleep 6\n\t" \
    "s_branch gv_Cag5_%=\n\t" \
    "gv_F6_%=:\n\t" \
    "v_mov_b32 v168, 5767424\n\t" \
    "v_mov_b32 v169, 4\n\t" \
    "global_store_dword v168, v169, s[36:37] sc1\n\t" \
    "s_waitcnt vmcnt(0)\n\t" \
    "s_branch gv_Cok4_%=\n\t" \
    "gv_Cag5_%=:\n\t" \
    "global_load_dwordx2 v[166:167], v164, s[36:37] sc1\n\t" \
    "global_load_dword v168, v164, s[36:37] offset:2048 sc1\n\t" \
    "s_waitcnt vmcnt(0)\n\t" \
    "s_branch gv_C3_%=\n\t" \
    "gv_Cok4_%=:\n\t" \
    "v_readlane_b32 s69, v167, 2\n\t" \
    "v_readlane_b32 s70, v167, 3\n\t" \
    "s_nop 1\n\t" \
    "s_cmp_lt_u32 s70, s69\n\t" \
    "s_cselect_b32 s69, 0, 8192\n\t" \
    "s_add_u32 s100, s36, s69\n\t" \
    "s_addc_u32 s101, s37, 0\n\t" \
    "s_cmp_lg_u32 s66, 0\n\t" \
    "s_cbranch_scc1 gv_Ca7_%=\n\t" \
    "v_mov_b32 v164, 5768192\n\t" \
    "s_mov_b64 exec, 1\n\t" \
    "global_atomic_add v164, v166, s[36:37] sc1\n\t" \
    "s_mov_b64 exec, -1\n\t" \
    "gv_Ca7_%=:\n\t" \
    "v_mov_b32_dpp v164, v167 quad_perm:[1,0,3,2] row_mask:0xf bank_mask:0xf\n\t" \
    "v_cmp_lt_u32 s[44:45], v164, v167\n\t" \
    "s_nop 1\n\t" \
    "s_waitcnt lgkmcnt(0)\n\t" \
    "s_barrier\n\t" \
    "ds_read_b128 v[24:27], v1 offset:0\n\t" \
    "ds_read_b128 v[28:31], v1 offset:18432\n\t" \
    "ds_read_b128 v[32:35], v2 offset:0\n\t" \
    "ds_read_b128 v[36:39], v2 offset:18432\n\t" \
    "ds_read_b128 v[40:43], v1 offset:128\n\t" \
    "ds_read_b128 v[44:47], v1 offset:18560\n\t" \
    "s_mov_b64 vcc, 0\n\t" \
    ".p2align 6\n\t" \
    "s_nop 0\n\t" \
    "s_nop 0\n\t" \
    "gv_loop8_%=:\n\t" \
    "s_min_u32 s69, s85, 63\n\t" \
    "s_mul_i32 s69, s69, 36864\n\t" \
    "s_add_u32 s52, s50, s69\n\t" \
    "s_addc_u32 s53, s51, 0\n\t" \
    "s_add_u32 s90, s90, 36864\n\t" \
    "s_cmp_eq_u32 s90, 110592\n\t" \
    "s_cselect_b32 s90, 0, s90\n\t" \
    "s_add_u32 s91, s90, 36864\n\t" \
    "s_cmp_eq_u32 s91, 110592\n\t" \
    "s_cselect_b32 s91, 0, s91\n\t" \
    "s_add_u32 s91, s91, s89\n\t" \
    "v_add_u32 v22, s90, v0\n\t" \
    "v_xor_b32 v23, 64, v22\n\t" \
    "s_lshl_b32 s40, s65, 15\n\t" \
    "s_cselect_b32 s70, s65, 64\n\t" \
    "s_mul_i32 s69, s70, 171\n\t" \
    "s_lshr_b32 s69, s69, 9\n\t" \
    "s_mul_i32 s70, s70, 589824\n\t" \
    "s_mul_i32 s71, s69, 327680\n\t" \
    "s_add_u32 s70, s70, s71\n\t" \
    "s_lshl1_add_u32 s69, s69, 4\n\t" \
    "s_bitcmp1_b64 s[44:45], s69\n\t" \
    "s_cselect_b32 s69, 8192, 0\n\t" \
    "s_add_u32 s69, s69, s70\n\t" \
    "s_add_u32 s69, s69, 123731968\n\t" \
    "s_add_u32 s48, s36, s69\n\t" \
    "s_addc_u32 s49, s37, 0\n\t" \
    "s_waitcnt lgkmcnt(5)\n\t" \
    "v_mfma_f32_16x16x32_bf16 v[72:75], a[0:3], v[24:27], 0\n\t" \
    "ds_read_b128 v[48:51], v2 offset:128\n\t" \
    "s_waitcnt lgkmcnt(5)\n\t" \
    "v_mfma_f32_16x16x32_bf16 v[76:79], a[0:3], v[28:31], 0\n\t" \
    "ds_read_b128 v[52:55], v2 offset:18560\n\t" \
    "v_mfma_f32_16x16x32_bf16 v[80:83], a[72:75], v[24:27], 0\n\t" \
    "s_add_u32 m0, s91, 0\n\t" \
    "v_mfma_f32_16x16x32_bf16 v[84:87], a[72:75], v[28:31], 0\n\t" \
    "global_load_lds_dwordx4 v203, s[52:53]\n\t" \
    "s_waitcnt lgkmcnt(5)\n\t" \
    "v_mfma_f32_16x16x32_bf16 v[72:75], a[4:7], v[32:35], v[72:75]\n\t" \
    "ds_read_b128 v[56:59], v1 offset:256\n\t" \
    "s_waitcnt lgkmcnt(5)\n\t" \
    "v_mfma_f32_16x16x32_bf16 v[76:79], a[4:7], v[36:39], v[76:79]\n\t" \
    "ds_read_b128 v[60:63], v1 offset:18688\n\t" \
    "v_mfma_f32_16x16x32_bf16 v[80:83], a[76:79], v[32:35], v[80:83]\n\t" \
    "s_add_u32 m0, s91, 1024\n\t" \
    "v_mfma_f32_16x16x32_bf16 v[84:87], a[76:79], v[36:39], v[84:87]\n\t" \
    "global_load_lds_dwordx4 v204, s[52:53]\n\t" \
    "s_waitcnt lgkmcnt(5)\n\t" \
    "v_mfma_f32_16x16x32_bf16 v[72:75], a[8:11], v[40:43], v[72:75]\n\t" \
    "ds_read_b128 v[64:67], v2 offset:256\n\t" \
    "s_waitcnt lgkmcnt(5)\n\t" \
    "v_mfma_f32_16x16x32_bf16 v[76:79], a[8:11], v[44:47], v[76:79]\n\t" \
    "ds_read_b128 v[68:71], v2 offset:18688\n\t" \
    "v_mfma_f32_16x16x32_bf16 v[80:83], a[80:83], v[40:43], v[80:83]\n\t" \
    "s_add_u32 m0, s91, 2048\n\t" \
    "v_mfma_f32_16x16x32_bf16 v[84:87], a[80:83], v[44:47], v[84:87]\n\t" \
    "global_load_lds_dwordx4 v205, s[52:53]\n\t" \
    "s_waitcnt lgkmcnt(5)\n\t" \
    "v_mfma_f32_16x16x32_bf16 v[72:75], a[12:15], v[48:51], v[72:75]\n\t" \
    "ds_read_b128 v[24:27], v1 offset:384\n\t" \
    "s_waitcnt lgkmcnt(5)\n\t" \
    "v_mfma_f32_16x16x32_bf16 v[76:79], a[12:15], v[52:55], v[76:79]\n\t" \
    "ds_read_b128 v[28:31], v1 offset:18816\n\t" \
    "v_mfma_f32_16x16x32_bf16 v[80:83], a[84:87], v[48:51], v[80:83]\n\t" \
    "s_add_u32 m0, s91, 3072\n\t" \
    "v_mfma_f32_16x16x32_bf16 v[84:87], a[84:87], v[52:55], v[84:87]\n\t" \
    "global_load_lds_dwordx4 v206, s[52:53]\n\t" \
    "s_waitcnt lgkmcnt(5)\n\t" \
    "v_mfma_f32_16x16x32_bf16 v[72:75], a[16:19], v[56:59], v[72:75]\n\t" \
    "ds_read_b128 v[32:35], v2 offset:384\n\t" \
    "s_waitcnt lgkmcnt(5)\n\t" \
    "v_mfma_f32_16x16x32_bf16 v[76:79], a[16:19], v[60:63], v[76:79]\n\t" \
    "ds_read_b128 v[36:39], v2 offset:18816\n\t" \
    "v_mfma_f32_16x16x32_bf16 v[80:83], a[88:91], v[56:59], v[80:83]\n\t" \
    "s_add_u32 m0, s91, 4096\n\t" \
    "v_mfma_f32_16x16x32_bf16 v[84:87], a[88:91], v[60:63], v[84:87]\n\t" \
    "global_load_lds_dwordx4 v207, s[52:53]\n\t" \
    "s_waitcnt lgkmcnt(5)\n\t" \
    "v_mfma_f32_16x16x32_bf16 v[72:75], a[20:23], v[64:67], v[72:75]\n\t" \
    "ds_read_b128 v[40:43], v1 offset:512\n\t" \
    "s_waitcnt lgkmcnt(5)\n\t" \
    "v_mfma_f32_16x16x32_bf16 v[76:79], a[20:23], v[68:71], v[76:79]\n\t" \
    "ds_read_b128 v[44:47], v1 offset:18944\n\t" \
    "v_mfma_f32_16x16x32_bf16 v[80:83], a[92:95], v[64:67], v[80:83]\n\t" \
    "s_add_u32 m0, s91, 5120\n\t" \
    "v_mfma_f32_16x16x32_bf16 v[84:87], a[92:95], v[68:71], v[84:87]\n\t" \
    "global_load_lds_dwordx4 v208, s[52:53]\n\t" \
    "s_waitcnt lgkmcnt(5)\n\t" \
    "v_mfma_f32_16x16x32_bf16 v[72:75], a[24:27], v[24:27], v[72:75]\n\t" \
    "ds_read_b128 v[48:51], v2 offset:512\n\t" \
    "s_waitcnt lgkmcnt(5)\n\t" \
    "v_mfma_f32_16x16x32_bf16 v[76:79], a[24:27], v[28:31], v[76:79]\n\t" \
    "ds_read_b128 v[52:55], v2 offset:18944\n\t" \
    "v_mfma_f32_16x16x32_bf16 v[80:83], a[96:99], v[24:27], v[80:83]\n\t" \
    "s_add_u32 m0, s91, 6144\n\t" \
    "v_mfma_f32_16x16x32_bf16 v[84:87], a[96:99], v[28:31], v[84:87]\n\t" \
    "global_load_lds_dwordx4 v209, s[52:53]\n\t" \
    "s_waitcnt lgkmcnt(5)\n\t" \
    "v_mfma_f32_16x16x32_bf16 v[72:75], a[28:31], v[32:35], v[72:75]\n\t" \
    "ds_read_b128 v[56:59], v1 offset:640\n\t" \
    "s_waitcnt lgkmcnt(5)\n\t" \
    "v_mfma_f32_16x16x32_bf16 v[76:79], a[28:31], v[36:39], v[76:79]\n\t" \
    "ds_read_b128 v[60:63], v1 offset:19072\n\t" \
    "v_mfma_f32_16x16x32_bf16 v[80:83], a[100:103], v[32:35], v[80:83]\n\t" \
    "s_add_u32 m0, s91, 7168\n\t" \
    "v_mfma_f32_16x16x32_bf16 v[84:87], a[100:103], v[36:39], v[84:87]\n\t" \
    "global_load_lds_dwordx4 v210, s[52:53]\n\t" \
    "s_waitcnt lgkmcnt(5)\n\t" \
    "v_mfma_f32_16x16x32_bf16 v[72:75], a[32:35], v[40:43], v[72:75]\n\t" \
    "ds_read_b128 v[64:67], v2 offset:640\n\t" \
    "s_waitcnt lgkmcnt(5)\n\t" \
    "v_mfma_f32_16x16x32_bf16 v[76:79], a[32:35], v[44:47], v[76:79]\n\t" \
    "ds_read_b128 v[68:71], v2 offset:19072\n\t" \
    "v_mfma_f32_16x16x32_bf16 v[80:83], a[104:107], v[40:43], v[80:83]\n\t" \
    "s_add_u32 m0, s91, 8192\n\t" \
    "v_mfma_f32_16x16x32_bf16 v[84:87], a[104:107], v[44:47], v[84:87]\n\t" \
    "global_load_lds_dwordx4 v211, s[52:53]\n\t" \
    "s_waitcnt lgkmcnt(5)\n\t" \
    "v_mfma_f32_16x16x32_bf16 v[72:75], a[36:39], v[48:51], v[72:75]\n\t" \
    "ds_read_b128 v[24:27], v1 offset:768\n\t" \
    "s_waitcnt lgkmcnt(5)\n\t" \
    "v_mfma_f32_16x16x32_bf16 v[76:79], a[36:39], v[52:55], v[76:79]\n\t" \
    "ds_read_b128 v[28:31], v1 offset:19200\n\t" \
    "v_mfma_f32_16x16x32_bf16 v[80:83], a[108:111], v[48:51], v[80:83]\n\t" \
    "s_cbranch_vccnz gv_gol12_%=\n\t" \
    "gv_gob13_%=:\n\t" \
    "v_mfma_f32_16x16x32_bf16 v[84:87], a[108:111], v[52:55], v[84:87]\n\t" \
    "s_waitcnt lgkmcnt(5)\n\t" \
    "v_mfma_f32_16x16x32_bf16 v[72:75], a[40:43], v[56:59], v[72:75]\n\t" \
    "ds_read_b128 v[32:35], v2 offset:768\n\t" \
    "s_waitcnt lgkmcnt(5)\n\t" \
    "v_mfma_f32_16x16x32_bf16 v[76:79], a[40:43], v[60:63], v[76:79]\n\t" \
    "ds_read_b128 v[36:39], v2 offset:19200\n\t" \
    "v_mfma_f32_16x16x32_bf16 v[80:83], a[112:115], v[56:59], v[80:83]\n\t" \
    "v_mfma_f32_16x16x32_bf16 v[84:87], a[112:115], v[60:63], v[84:87]\n\t" \
    "global_store_dwordx4 v19, v[196:199], s[48:49] sc1\n\t" \
    "s_waitcnt lgkmcnt(5)\n\t" \
    "v_mfma_f32_16x16x32_bf16 v[72:75], a[44:47], v[64:67], v[72:75]\n\t" \
    "ds_read_b128 v[40:43], v1 offset:896\n\t" \
    "s_waitcnt lgkmcnt(5)\n\t" \
    "v_mfma_f32_16x16x32_bf16 v[76:79], a[44:47], v[68:71], v[76:79]\n\t" \
    "ds_read_b128 v[44:47], v1 offset:19328\n\t" \
    "v_mfma_f32_16x16x32_bf16 v[80:83], a[116:119], v[64:67], v[80:83]\n\t" \
    "v_mfma_f32_16x16x32_bf16 v[84:87], a[116:119], v[68:71], v[84:87]\n\t" \
    "s_waitcnt lgkmcnt(5)\n\t" \
    "v_mfma_f32_16x16x32_bf16 v[72:75], a[48:51], v[24:27], v[72:75]\n\t" \
    "ds_read_b128 v[48:51], v2 offset:896\n\t" \
    "s_waitcnt lgkmcnt(5)\n\t" \
    "v_mfma_f32_16x16x32_bf16 v[76:79], a[48:51], v[28:31], v[76:79]\n\t" \
    "ds_read_b128 v[52:55], v2 offset:19328\n\t" \
    "v_mfma_f32_16x16x32_bf16 v[80:83], a[120:123], v[24:27], v[80:83]\n\t" \
    "v_mfma_f32_16x16x32_bf16 v[84:87], a[120:123], v[28:31], v[84:87]\n\t" \
    "s_waitcnt lgkmcnt(5)\n\t" \
    "v_mfma_f32_16x16x32_bf16 v[72:75], a[52:55], v[32:35], v[72:75]\n\t" \
    "ds_read_b128 v[56:59], v1 offset:1024\n\t" \
    "s_waitcnt lgkmcnt(5)\n\t" \
    "v_mfma_f32_16x16x32_bf16 v[76:79], a[52:55], v[36:39], v[76:79]\n\t" \
    "ds_read_b128 v[60:63], v1 offset:19456\n\t" \
    "v_mfma_f32_16x16x32_bf16 v[80:83], a[124:127], v[32:35], v[80:83]\n\t" \
    "v_mfma_f32_16x16x32_bf16 v[84:87], a[124:127], v[36:39], v[84:87]\n\t" \
    "global_store_dwordx4 v20, v[196:199], s[48:49] sc1\n\t" \
    "s_waitcnt lgkmcnt(5)\n\t" \
    "v_mfma_f32_16x16x32_bf16 v[72:75], a[56:59], v[40:43], v[72:75]\n\t" \
    "ds_read_b128 v[64:67], v2 offset:1024\n\t" \
    "s_waitcnt lgkmcnt(5)\n\t" \
    "v_mfma_f32_16x16x32_bf16 v[76:79], a[56:59], v[44:47], v[76:79]\n\t" \
    "ds_read_b128 v[68:71], v2 offset:19456\n\t" \
    "v_mfma_f32_16x16x32_bf16 v[80:83], a[128:131], v[40:43], v[80:83]\n\t" \
    "v_mfma_f32_16x16x32_bf16 v[84:87], a[128:131], v[44:47], v[84:87]\n\t" \
    "s_waitcnt lgkmcnt(5)\n\t" \
    "v_mfma_f32_16x16x32_bf16 v[72:75], a[60:63], v[48:51], v[72:75]\n\t" \
    "s_waitcnt lgkmcnt(4)\n\t" \
    "v_mfma_f32_16x16x32_bf16 v[76:79], a[60:63], v[52:55], v[76:79]\n\t" \
    "v_mfma_f32_16x16x32_bf16 v[80:83], a[132:135], v[48:51], v[80:83]\n\t" \
    "v_mfma_f32_16x16x32_bf16 v[84:87], a[132:135], v[52:55], v[84:87]\n\t" \
    "s_waitcnt lgkmcnt(3)\n\t" \
    "v_mfma_f32_16x16x32_bf16 v[72:75], a[64:67], v[56:59], v[72:75]\n\t" \
    "s_waitcnt lgkmcnt(2)\n\t" \
    "v_mfma_f32_16x16x32_bf16 v[76:79], a[64:67], v[60:63], v[76:79]\n\t" \
    "v_mfma_f32_16x16x32_bf16 v[80:83], a[136:139], v[56:59], v[80:83]\n\t" \
    "v_mfma_f32_16x16x32_bf16 v[84:87], a[136:139], v[60:63], v[84:87]\n\t" \
    "global_store_dword v21, v196, s[48:49] sc1\n\t" \
    "s_waitcnt lgkmcnt(1)\n\t" \
    "v_mfma_f32_16x16x32_bf16 v[72:75], a[68:71], v[64:67], v[72:75]\n\t" \
    "s_waitcnt lgkmcnt(0)\n\t" \
    "v_mfma_f32_16x16x32_bf16 v[76:79], a[68:71], v[68:71], v[76:79]\n\t" \
    "v_mfma_f32_16x16x32_bf16 v[80:83], a[140:143], v[64:67], v[80:83]\n\t" \
    "v_mfma_f32_16x16x32_bf16 v[84:87], a[140:143], v[68:71], v[84:87]\n\t" \
    "v_mov_b32 v1, v22\n\t" \
    "v_mov_b32 v2, v23\n\t" \
    "s_nop 1\n\t" \
    "v_cvt_pk_bf16_f32 v104, v72, v73\n\t" \
    "v_cvt_pk_bf16_f32 v105, v74, v75\n\t" \
    "s_nop 0\n\t" \
    "v_cvt_pk_bf16_f32 v106, v80, v81\n\t" \
    "v_cvt_pk_bf16_f32 v107, v82, v83\n\t" \
    "ds_write_b128 v4, v[104:107] offset:0\n\t" \
    "v_cvt_pk_bf16_f32 v108, v76, v77\n\t" \
    "v_cvt_pk_bf16_f32 v109, v78, v79\n\t" \
    "v_cvt_pk_bf16_f32 v110, v84, v85\n\t" \
    "v_cvt_pk_bf16_f32 v111, v86, v87\n\t" \
    "ds_write_b128 v4, v[108:111] offset:1024\n\t" \
    "s_cmp_lt_i32 s86, 16\n\t" \
    "s_cbranch_scc1 gv_rest10_%=\n\t" \
    "gv_back11_%=:\n\t" \
    "s_waitcnt vmcnt(21) lgkmcnt(0)\n\t" \
    "s_barrier\n\t" \
    "ds_read_b128 v[164:167], v5 offset:0\n\t" \
    "ds_read_b128 v[168:171], v5 offset:1024\n\t" \
    "ds_read_b128 v[172:175], v5 offset:2048\n\t" \
    "ds_read_b128 v[176:179], v5 offset:3072\n\t" \
    "ds_read_b128 v[180:183], v5 offset:4096\n\t" \
    "ds_read_b128 v[184:187], v5 offset:5120\n\t" \
    "ds_read_b128 v[188:191], v5 offset:6144\n\t" \
    "ds_read_b128 v[192:195], v5 offset:7168\n\t" \
    "s_cmp_lt_i32 s86, 16\n\t" \
    "s_cselect_b32 s71, s86, s87\n\t" \
    "s_cmp_lg_u32 s66, 0\n\t" \
    "s_cbranch_scc1 gv_SF17_%=\n\t" \
    "s_lshl_b32 s69, s67, 7\n\t" \
    "s_add_u32 s69, s69, 3162112\n\t" \
    "s_add_u32 s70, s71, 0\n\t" \
    "v_mov_b32 v156, s69\n\t" \
    "v_mov_b32 v157, s70\n\t" \
    "s_mov_b64 exec, 1\n\t" \
    "global_store_dword v156, v157, s[100:101] sc1\n\t" \
    "s_mov_b64 exec, -1\n\t" \
    "gv_SF17_%=:\n\t" \
    "s_sub_u32 s69, s86, 2\n\t" \
    "s_lshr_b32 s69, s69, 3\n\t" \
    "s_xor_b32 s70, s66, 2\n\t" \
    "s_or_b32 s69, s69, s70\n\t" \
    "s_cbranch_scc1 gv_ef18_%=\n\t" \
    "s_lshr_b32 s69, s86, 1\n\t" \
    "s_lshl_b32 s69, s69, 6\n\t" \
    "s_add_u32 s69, s69, s67\n\t" \
    "s_lshl_b32 s69, s69, 6\n\t" \
    "s_add_u32 s69, s69, 5795840\n\t" \
    "v_mov_b32 v156, s69\n\t" \
    "v_mov_b32 v157, s86\n\t" \
    "global_store_dword v156, v157, s[36:37] sc1\n\t" \
    "gv_ef18_%=:\n\t" \
    "v_xor_b32 v4, 0x2000, v4\n\t" \
    "v_xor_b32 v5, 0x2000, v5\n\t" \
    "s_waitcnt lgkmcnt(7)\n\t" \
    "v_mfma_f32_16x16x32_bf16 v[72:75], v[164:167], a[144:147], 0\n\t" \
    "s_waitcnt lgkmcnt(6)\n\t" \
    "v_mfma_f32_16x16x32_bf16 v[76:79], v[168:171], a[144:147], 0\n\t" \
    "s_waitcnt lgkmcnt(5)\n\t" \
    "v_mfma_f32_16x16x32_bf16 v[72:75], v[172:175], a[148:151], v[72:75]\n\t" \
    "s_waitcnt lgkmcnt(4)\n\t" \
    "v_mfma_f32_16x16x32_bf16 v[76:79], v[176:179], a[148:151], v[76:79]\n\t" \
    "s_waitcnt lgkmcnt(3)\n\t" \
    "v_mfma_f32_16x16x32_bf16 v[72:75], v[180:183], a[152:155], v[72:75]\n\t" \
    "s_waitcnt lgkmcnt(2)\n\t" \
    "v_mfma_f32_16x16x32_bf16 v[76:79], v[184:187], a[152:155], v[76:79]\n\t" \
    "s_waitcnt lgkmcnt(1)\n\t" \
    "v_mfma_f32_16x16x32_bf16 v[72:75], v[188:191], a[156:159], v[72:75]\n\t" \
    "s_waitcnt lgkmcnt(0)\n\t" \
    "v_mfma_f32_16x16x32_bf16 v[76:79], v[192:195], a[156:159], v[76:79]\n\t" \
    "v_mfma_f32_16x16x32_bf16 v[80:83], v[164:167], a[160:163], 0\n\t" \
    "v_mfma_f32_16x16x32_bf16 v[84:87], v[168:171], a[160:163], 0\n\t" \
    "v_mfma_f32_16x16x32_bf16 v[80:83], v[172:175], a[164:167], v[80:83]\n\t" \
    "v_mfma_f32_16x16x32_bf16 v[84:87], v[176:179], a[164:167], v[84:87]\n\t" \
    "v_mfma_f32_16x16x32_bf16 v[80:83], v[180:183], a[168:171], v[80:83]\n\t" \
    "v_mfma_f32_16x16x32_bf16 v[84:87], v[184:187], a[168:171], v[84:87]\n\t" \
    "v_cvt_pk_bf16_f32 v104, v72, v73\n\t" \
    "v_mfma_f32_16x16x32_bf16 v[80:83], v[188:191], a[172:175], v[80:83]\n\t" \
    "v_cvt_pk_bf16_f32 v105, v74, v75\n\t" \
    "v_mfma_f32_16x16x32_bf16 v[84:87], v[192:195], a[172:175], v[84:87]\n\t" \
    "v_cvt_pk_bf16_f32 v106, v76, v77\n\t" \
    "v_mfma_f32_16x16x32_bf16 v[88:91], v[164:167], a[176:179], 0\n\t" \
    "v_cvt_pk_bf16_f32 v107, v78, v79\n\t" \
    "v_mfma_f32_16x16x32_bf16 v[92:95], v[168:171], a[176:179], 0\n\t" \
    "v_xor_b32 v18, s40, v8\n\t" \
    "v_mfma_f32_16x16x32_bf16 v[88:91], v[172:175], a[180:183], v[88:91]\n\t" \
    "global_store_dwordx4 v18, v[104:107], s[38:39] sc1\n\t" \
    "v_mfma_f32_16x16x32_bf16 v[92:95], v[176:179], a[180:183], v[92:95]\n\t" \
    "v_mfma_f32_16x16x32_bf16 v[88:91], v[180:183], a[184:187], v[88:91]\n\t" \
    "v_mfma_f32_16x16x32_bf16 v[92:95], v[184:187], a[184:187], v[92:95]\n\t" \
    "v_cvt_pk_bf16_f32 v108, v80, v81\n\t" \
    "v_mfma_f32_16x16x32_bf16 v[88:91], v[188:191], a[188:191], v[88:91]\n\t" \
    "v_cvt_pk_bf16_f32 v109, v82, v83\n\t" \
    "v_mfma_f32_16x16x32_bf16 v[92:95], v[192:195], a[188:191], v[92:95]\n\t" \
    "v_cvt_pk_bf16_f32 v110, v84, v85\n\t" \
    "v_mfma_f32_16x16x32_bf16 v[72:75], v[164:167], a[192:195], 0\n\t" \
    "v_cvt_pk_bf16_f32 v111, v86, v87\n\t" \
    "v_mfma_f32_16x16x32_bf16 v[76:79], v[168:171], a[192:195], 0\n\t" \
    "v_xor_b32 v18, s40, v9\n\t" \
    "v_mfma_f32_16x16x32_bf16 v[72:75], v[172:175], a[196:199], v[72:75]\n\t" \
    "global_store_dwordx4 v18, v[108:111], s[38:39] sc1\n\t" \
    "v_mfma_f32_16x16x32_bf16 v[76:79], v[176:179], a[196:199], v[76:79]\n\t" \
    "v_mfma_f32_16x16x32_bf16 v[72:75], v[180:183], a[200:203], v[72:75]\n\t" \
    "v_mfma_f32_16x16x32_bf16 v[76:79], v[184:187], a[200:203], v[76:79]\n\t" \
    "v_cvt_pk_bf16_f32 v104, v88, v89\n\t" \
    "v_mfma_f32_16x16x32_bf16 v[72:75], v[188:191], a[204:207], v[72:75]\n\t" \
    "v_cvt_pk_bf16_f32 v105, v90, v91\n\t" \
    "v_mfma_f32_16x16x32_bf16 v[76:79], v[192:195], a[204:207], v[76:79]\n\t" \
    "v_cvt_pk_bf16_f32 v106, v92, v93\n\t" \
    "v_mfma_f32_16x16x32_bf16 v[80:83], v[164:167], a[208:211], 0\n\t" \
    "v_cvt_pk_bf16_f32 v107, v94, v95\n\t" \
    "v_mfma_f32_16x16x32_bf16 v[84:87], v[168:171], a[208:211], 0\n\t" \
    "v_xor_b32 v18, s40, v10\n\t" \
    "v_mfma_f32_16x16x32_bf16 v[80:83], v[172:175], a[212:215], v[80:83]\n\t" \
    "global_store_dwordx4 v18, v[104:107], s[38:39] sc1\n\t" \
    "v_mfma_f32_16x16x32_bf16 v[84:87], v[176:179], a[212:215], v[84:87]\n\t" \
    "v_mfma_f32_16x16x32_bf16 v[80:83], v[180:183], a[216:219], v[80:83]\n\t" \
    "v_mfma_f32_16x16x32_bf16 v[84:87], v[184:187], a[216:219], v[84:87]\n\t" \
    "v_cvt_pk_bf16_f32 v108, v72, v73\n\t" \
    "v_mfma_f32_16x16x32_bf16 v[80:83], v[188:191], a[220:223], v[80:83]\n\t" \
    "v_cvt_pk_bf16_f32 v109, v74, v75\n\t" \
    "v_mfma_f32_16x16x32_bf16 v[84:87], v[192:195], a[220:223], v[84:87]\n\t" \
    "v_cvt_pk_bf16_f32 v110, v76, v77\n\t" \
    "v_mfma_f32_16x16x32_bf16 v[88:91], v[164:167], a[224:227], 0\n\t" \
    "v_cvt_pk_bf16_f32 v111, v78, v79\n\t" \
    "v_mfma_f32_16x16x32_bf16 v[92:95], v[168:171], a[224:227], 0\n\t" \
    "v_xor_b32 v18, s40, v11\n\t" \
    "v_mfma_f32_16x16x32_bf16 v[88:91], v[172:175], a[228:231], v[88:91]\n\t" \
    "global_store_dwordx4 v18, v[108:111], s[38:39] sc1\n\t" \
    "v_mfma_f32_16x16x32_bf16 v[92:95], v[176:179], a[228:231], v[92:95]\n\t" \
    "v_mfma_f32_16x16x32_bf16 v[88:91], v[180:183], a[232:235], v[88:91]\n\t" \
    "v_mfma_f32_16x16x32_bf16 v[92:95], v[184:187], a[232:235], v[92:95]\n\t" \
    "v_cvt_pk_bf16_f32 v104, v80, v81\n\t" \
    "v_mfma_f32_16x16x32_bf16 v[88:91], v[188:191], a[236:239], v[88:91]\n\t" \
    "v_cvt_pk_bf16_f32 v105, v82, v83\n\t" \
    "v_mfma_f32_16x16x32_bf16 v[92:95], v[192:195], a[236:239], v[92:95]\n\t" \
    "v_cvt_pk_bf16_f32 v106, v84, v85\n\t" \
    "v_mfma_f32_16x16x32_bf16 v[72:75], v[164:167], a[240:243], 0\n\t" \
    "v_cvt_pk_bf16_f32 v107, v86, v87\n\t" \
    "v_mfma_f32_16x16x32_bf16 v[76:79], v[168:171], a[240:243], 0\n\t" \
    "v_xor_b32 v18, s40, v12\n\t" \
    "v_mfma_f32_16x16x32_bf16 v[72:75], v[172:175], a[244:247], v[72:75]\n\t" \
    "global_store_dwordx4 v18, v[104:107], s[38:39] sc1\n\t" \
    "v_mfma_f32_16x16x32_bf16 v[76:79], v[176:179], a[244:247], v[76:79]\n\t" \
    "v_mfma_f32_16x16x32_bf16 v[72:75], v[180:183], a[248:251], v[72:75]\n\t" \
    "v_mfma_f32_16x16x32_bf16 v[76:79], v[184:187], a[248:251], v[76:79]\n\t" \
    "v_cvt_pk_bf16_f32 v108, v88, v89\n\t" \
    "v_mfma_f32_16x16x32_bf16 v[72:75], v[188:191], a[252:255], v[72:75]\n\t" \
    "v_cvt_pk_bf16_f32 v109, v90, v91\n\t" \
    "v_mfma_f32_16x16x32_bf16 v[76:79], v[192:195], a[252:255], v[76:79]\n\t" \
    "v_cvt_pk_bf16_f32 v110, v92, v93\n\t" \
    "v_mfma_f32_16x16x32_bf16 v[80:83], v[164:167], v[224:227], 0\n\t" \
    "v_cvt_pk_bf16_f32 v111, v94, v95\n\t" \
    "v_mfma_f32_16x16x32_bf16 v[84:87], v[168:171], v[224:227], 0\n\t" \
    "v_xor_b32 v18, s40, v13\n\t" \
    "v_mfma_f32_16x16x32_bf16 v[80:83], v[172:175], v[228:231], v[80:83]\n\t" \
    "global_store_dwordx4 v18, v[108:111], s[38:39] sc1\n\t" \
    "v_mfma_f32_16x16x32_bf16 v[84:87], v[176:179], v[228:231], v[84:87]\n\t" \
    "v_mfma_f32_16x16x32_bf16 v[80:83], v[180:183], v[232:235], v[80:83]\n\t" \
    "v_mfma_f32_16x16x32_bf16 v[84:87], v[184:187], v[232:235], v[84:87]\n\t" \
    "v_cvt_pk_bf16_f32 v104, v72, v73\n\t" \
    "v_mfma_f32_16x16x32_bf16 v[80:83], v[188:191], v[236:239], v[80:83]\n\t" \
    "v_cvt_pk_bf16_f32 v105, v74, v75\n\t" \
    "v_mfma_f32_16x16x32_bf16 v[84:87], v[192:195], v[236:239], v[84:87]\n\t" \
    "v_cvt_pk_bf16_f32 v106, v76, v77\n\t" \
    "v_mfma_f32_16x16x32_bf16 v[88:91], v[164:167], v[240:243], 0\n\t" \
    "v_cvt_pk_bf16_f32 v107, v78, v79\n\t" \
    "ds_read_b128 v[24:27], v22 offset:0\n\t" \
    "v_mfma_f32_16x16x32_bf16 v[92:95], v[168:171], v[240:243], 0\n\t" \
    "v_xor_b32 v18, s40, v14\n\t" \
    "ds_read_b128 v[28:31], v22 offset:18432\n\t" \
    "v_mfma_f32_16x16x32_bf16 v[88:91], v[172:175], v[244:247], v[88:91]\n\t" \
    "global_store_dwordx4 v18, v[104:107], s[38:39] sc1\n\t" \
    "ds_read_b128 v[32:35], v23 offset:0\n\t" \
    "v_mfma_f32_16x16x32_bf16 v[92:95], v[176:179], v[244:247], v[92:95]\n\t" \
    "ds_read_b128 v[36:39], v23 offset:18432\n\t" \
    "v_mfma_f32_16x16x32_bf16 v[88:91], v[180:183], v[248:251], v[88:91]\n\t" \
    "ds_read_b128 v[40:43], v22 offset:128\n\t" \
    "v_mfma_f32_16x16x32_bf16 v[92:95], v[184:187], v[248:251], v[92:95]\n\t" \
    "v_cvt_pk_bf16_f32 v108, v80, v81\n\t" \
    "ds_read_b128 v[44:47], v22 offset:18560\n\t" \
    "v_mfma_f32_16x16x32_bf16 v[88:91], v[188:191], v[252:255], v[88:91]\n\t" \
    "v_cvt_pk_bf16_f32 v109, v82, v83\n\t" \
    "v_mfma_f32_16x16x32_bf16 v[92:95], v[192:195], v[252:255], v[92:95]\n\t" \
    "v_cvt_pk_bf16_f32 v110, v84, v85\n\t" \
    "v_cvt_pk_bf16_f32 v111, v86, v87\n\t" \
    "v_xor_b32 v18, s40, v15\n\t" \
    "global_store_dwordx4 v18, v[108:111], s[38:39] sc1\n\t" \
    "s_nop 1\n\t" \
    "v_cvt_pk_bf16_f32 v104, v88, v89\n\t" \
    "v_cvt_pk_bf16_f32 v105, v90, v91\n\t" \
    "v_cvt_pk_bf16_f32 v106, v92, v93\n\t" \
    "v_cvt_pk_bf16_f32 v107, v94, v95\n\t" \
    "v_xor_b32 v18, s40, v16\n\t" \
    "global_store_dwordx4 v18, v[104:107], s[38:39] sc1\n\t" \
    "s_cmp_ge_u32 s65, 2\n\t" \
    "s_cbranch_scc1 gv_nofast9_%=\n\t" \
    "s_waitcnt vmcnt(0)\n\t" \
    "s_barrier\n\t" \
    "s_cmp_lg_u32 s66, 0\n\t" \
    "s_cbranch_scc1 gv_SF19_%=\n\t" \
    "s_lshl_b32 s69, s67, 7\n\t" \
    "s_add_u32 s69, s69, 3162112\n\t" \
    "s_add_u32 s70, s65, 0\n\t" \
    "v_mov_b32 v156, s69\n\t" \
    "v_mov_b32 v157, s70\n\t" \
    "s_mov_b64 exec, 1\n\t" \
    "global_store_dword v156, v157, s[100:101] sc1\n\t" \
    "s_mov_b64 exec, -1\n\t" \
    "gv_SF19_%=:\n\t" \
    "s_cmp_lg_u32 s66, 1\n\t" \
    "s_cbranch_scc1 gv_nofast9_%=\n\t" \
    "v_mov_b32 v120, 5769216\n\t" \
    "v_mov_b32 v121, 1\n\t" \
    "s_mov_b64 exec, 1\n\t" \
    "global_atomic_add v122, v120, v121, s[36:37] sc0 sc1\n\t" \
    "s_mov_b64 exec, -1\n\t" \
    "s_mov_b64 vcc, -1\n\t" \
    "gv_nofast9_%=:\n\t" \
    "s_mov_b32 s87, s86\n\t" \
    "s_mov_b32 s86, s65\n\t" \
    "s_mov_b32 s65, s84\n\t" \
    "s_mov_b32 s84, s85\n\t" \
    "s_cmp_lt_u32 s84, 24\n\t" \
    "s_addc_u32 s85, s84, 1\n\t" \
    "s_cmp_lt_u32 s65, s88\n\t" \
    "s_cbranch_scc1 gv_loop8_%=\n\t" \
    "s_waitcnt vmcnt(0) lgkmcnt(0)\n\t" \
    "s_barrier\n\t" \
    "s_mov_b32 s65, 99\n\t" \
    "s_cmp_lg_u32 s66, 0\n\t" \
    "s_cbranch_scc1 gv_SF21_%=\n\t" \
    "s_lshl_b32 s69, s67, 7\n\t" \
    "s_add_u32 s69, s69, 3162112\n\t" \
    "s_add_u32 s70, s65, 0\n\t" \
    "v_mov_b32 v156, s69\n\t" \
    "v_mov_b32 v157, s70\n\t" \
    "s_mov_b64 exec, 1\n\t" \
    "global_store_dword v156, v157, s[100:101] sc1\n\t" \
    "s_mov_b64 exec, -1\n\t" \
    "gv_SF21_%=:\n\t" \
    "s_branch gv_end22_%=\n\t" \
    "gv_gol12_%=:\n\t" \
    "s_mov_b64 vcc, 0\n\t" \
    "v_mbcnt_lo_u32_b32 v120, -1, 0\n\t" \
    "v_and_b32 v121, 3, v120\n\t" \
    "v_lshlrev_b32 v121, 16, v121\n\t" \
    "v_bfe_u32 v123, v120, 2, 2\n\t" \
    "v_lshl_add_u32 v121, v123, 11, v121\n\t" \
    "v_bfe_u32 v123, v120, 4, 1\n\t" \
    "v_lshl_add_u32 v121, v123, 14, v121\n\t" \
    "s_bitcmp1_b32 s44, 2\n\t" \
    "s_cselect_b32 s63, 8192, 0\n\t" \
    "s_or_b32 s63, s63, 0x200000\n\t" \
    "v_add_u32 v121, s63, v121\n\t" \
    "s_waitcnt vmcnt(9)\n\t" \
    "v_readfirstlane_b32 s69, v122\n\t" \
    "s_nop 1\n\t" \
    "s_cmp_lg_u32 s69, 126\n\t" \
    "s_cbranch_scc1 gv_go23_%=\n\t" \
    "s_memrealtime s[56:57]\n\t" \
    "s_waitcnt lgkmcnt(0)\n\t" \
    "s_lshl_b32 s69, s56, 4\n\t" \
    "s_add_u32 s69, s69, 2960\n\t" \
    "s_add_u32 s70, s56, 24\n\t" \
    "s_lshr_b32 s70, s70, 4\n\t" \
    "v_lshrrev_b32 v123, 2, v120\n\t" \
    "v_add_u32 v123, s70, v123\n\t" \
    "v_and_b32 v123, 63, v123\n\t" \
    "v_lshlrev_b32 v123, 6, v123\n\t" \
    "v_and_b32 v122, 3, v120\n\t" \
    "v_lshl_add_u32 v123, v122, 12, v123\n\t" \
    "v_add_u32 v123, 5783552, v123\n\t" \
    "v_mov_b32 v120, s69\n\t" \
    "s_mov_b32 exec_hi, 0\n\t" \
    "global_store_dword v123, v120, s[36:37] sc1\n\t" \
    "global_store_dword v121, v120, s[36:37] sc1\n\t" \
    "s_mov_b32 exec_hi, -1\n\t" \
    "gv_go23_%=:\n\t" \
    "s_branch gv_gob13_%=\n\t" \
    "gv_rest10_%=:\n\t" \
    "s_waitcnt vmcnt(12) lgkmcnt(0)\n\t" \
    "s_cmp_ge_i32 s86, 0\n\t" \
    "s_cbranch_scc1 gv_back11_%=\n\t" \
    "s_barrier\n\t" \
    "ds_read_b64_tr_b16 a[156:157], v212 offset:0\n\t" \
    "ds_read_b64_tr_b16 a[158:159], v212 offset:18432\n\t" \
    "ds_read_b64_tr_b16 a[172:173], v213 offset:0\n\t" \
    "ds_read_b64_tr_b16 a[174:175], v213 offset:18432\n\t" \
    "ds_read_b64_tr_b16 a[188:189], v214 offset:0\n\t" \
    "ds_read_b64_tr_b16 a[190:191], v214 offset:18432\n\t" \
    "s_waitcnt lgkmcnt(6)\n\t" \
    "ds_read_b64_tr_b16 a[204:205], v215 offset:0\n\t" \
    "ds_read_b64_tr_b16 a[206:207], v215 offset:18432\n\t" \
    "ds_read_b64_tr_b16 a[220:221], v212 offset:128\n\t" \
    "ds_read_b64_tr_b16 a[222:223], v212 offset:18560\n\t" \
    "ds_read_b64_tr_b16 a[236:237], v213 offset:128\n\t" \
    "ds_read_b64_tr_b16 a[238:239], v213 offset:18560\n\t" \
    "s_waitcnt lgkmcnt(6)\n\t" \
    "ds_read_b64_tr_b16 a[252:253], v214 offset:128\n\t" \
    "ds_read_b64_tr_b16 a[254:255], v214 offset:18560\n\t" \
    "ds_read_b64_tr_b16 v[236:237], v215 offset:128\n\t" \
    "ds_read_b64_tr_b16 v[238:239], v215 offset:18560\n\t" \
    "ds_read_b64_tr_b16 v[252:253], v212 offset:256\n\t" \
    "ds_read_b64_tr_b16 v[254:255], v212 offset:18688\n\t" \
    "s_waitcnt lgkmcnt(6)\n\t" \
    "v_add_u32 v212, 0x9000, v212\n\t" \
    "v_add_u32 v213, 0x9000, v213\n\t" \
    "v_add_u32 v214, 0x9000, v214\n\t" \
    "v_add_u32 v215, 0x9000, v215\n\t" \
    "s_branch gv_back11_%=\n\t" \
    "gv_end22_%=:\n\t" \
    "gv_last_%=:\n\t" \
    ""
#define ASM_CV \
    "v_mov_b32 v0, %0\n\t" \
    "v_mov_b32 v2, %2\n\t" \
    "v_subrev_u32 v1, 768, %1\n\t" \
    "v_mov_b32 v4, 0\n\t" \
    "v_mov_b32 v5, 0\n\t" \
    "v_mov_b32 v6, 0\n\t" \
    "v_mov_b32 v7, 0\n\t" \
    "v_mov_b32 v8, 0\n\t" \
    "v_mov_b32 v9, 0\n\t" \
    "v_mov_b32 v10, 0\n\t" \
    "v_mov_b32 v11, 0\n\t" \
    "v_mov_b32 v12, 0\n\t" \
    "v_mov_b32 v13, 0\n\t" \
    "v_mov_b32 v14, 0\n\t" \
    "v_mov_b32 v15, 0\n\t" \
    "v_mov_b32 v16, 0\n\t" \
    "v_mov_b32 v17, 0\n\t" \
    "v_mov_b32 v18, 0\n\t" \
    "v_mov_b32 v19, 0\n\t" \
    "v_mov_b32 v20, 0\n\t" \
    "v_mov_b32 v21, 0\n\t" \
    "v_mov_b32 v22, 0\n\t" \
    "v_mov_b32 v23, 0\n\t" \
    "v_mov_b32 v24, 0\n\t" \
    "v_mov_b32 v25, 0\n\t" \
    "v_mov_b32 v26, 0\n\t" \
    "v_mov_b32 v27, 0\n\t" \
    "v_mov_b32 v28, 0\n\t" \
    "v_mov_b32 v29, 0\n\t" \
    "v_mov_b32 v30, 0\n\t" \
    "v_mov_b32 v31, 0\n\t" \
    "v_mov_b32 v32, 0\n\t" \
    "v_mov_b32 v33, 0\n\t" \
    "v_mov_b32 v34, 0\n\t" \
    "v_mov_b32 v35, 0\n\t" \
    "s_waitcnt lgkmcnt(0)\n\t" \
    "ds_read_b64 v[36:37], v0 offset:0\n\t" \
    "ds_read_b64 v[38:39], v0 offset:8\n\t" \
    "ds_read_b64 v[40:41], v0 offset:32\n\t" \
    "ds_read_b64 v[42:43], v0 offset:40\n\t" \
    "ds_read_b128 v[44:47], v1 offset:768\n\t" \
    "ds_read_b128 v[48:51], v1 offset:2304\n\t" \
    "ds_read_b128 v[52:55], v1 offset:3840\n\t" \
    "ds_read_b128 v[56:59], v1 offset:5376\n\t" \
    "s_mov_b32 s69, 8\n\t" \
    "cv_c1_%=:\n\t" \
    "s_waitcnt lgkmcnt(3)\n\t" \
    "v_mfma_f32_16x16x32_bf16 v[4:7], v[36:39], v[44:47], v[4:7]\n\t" \
    "ds_read_b64 v[76:77], v0 offset:64\n\t" \
    "ds_read_b64 v[78:79], v0 offset:72\n\t" \
    "ds_read_b64 v[80:81], v0 offset:96\n\t" \
    "v_mfma_f32_16x16x32_bf16 v[20:23], v[40:43], v[44:47], v[20:23]\n\t" \
    "ds_read_b64 v[82:83], v0 offset:104\n\t" \
    "ds_read_b128 v[84:87], v1 offset:672\n\t" \
    "ds_read_b128 v[88:91], v1 offset:2208\n\t" \
    "s_waitcnt lgkmcnt(8)\n\t" \
    "v_mfma_f32_16x16x32_bf16 v[8:11], v[36:39], v[48:51], v[8:11]\n\t" \
    "ds_read_b128 v[92:95], v1 offset:3744\n\t" \
    "ds_read_b128 v[96:99], v1 offset:5280\n\t" \
    "v_mfma_f32_16x16x32_bf16 v[24:27], v[40:43], v[48:51], v[24:27]\n\t" \
    "s_waitcnt lgkmcnt(9)\n\t" \
    "v_mfma_f32_16x16x32_bf16 v[12:15], v[36:39], v[52:55], v[12:15]\n\t" \
    "v_mfma_f32_16x16x32_bf16 v[28:31], v[40:43], v[52:55], v[28:31]\n\t" \
    "s_waitcnt lgkmcnt(8)\n\t" \
    "v_mfma_f32_16x16x32_bf16 v[16:19], v[36:39], v[56:59], v[16:19]\n\t" \
    "v_mfma_f32_16x16x32_bf16 v[32:35], v[40:43], v[56:59], v[32:35]\n\t" \
    "s_waitcnt lgkmcnt(3)\n\t" \
    "v_mfma_f32_16x16x32_bf16 v[4:7], v[76:79], v[84:87], v[4:7]\n\t" \
    "ds_read_b64 v[36:37], v0 offset:128\n\t" \
    "ds_read_b64 v[38:39], v0 offset:136\n\t" \
    "ds_read_b64 v[40:41], v0 offset:160\n\t" \
    "v_mfma_f32_16x16x32_bf16 v[20:23], v[80:83], v[84:87], v[20:23]\n\t" \
    "ds_read_b64 v[42:43], v0 offset:168\n\t" \
    "ds_read_b128 v[44:47], v1 offset:576\n\t" \
    "ds_read_b128 v[48:51], v1 offset:2112\n\t" \
    "s_waitcnt lgkmcnt(8)\n\t" \
    "v_mfma_f32_16x16x32_bf16 v[8:11], v[76:79], v[88:91], v[8:11]\n\t" \
    "ds_read_b128 v[52:55], v1 offset:3648\n\t" \
    "ds_read_b128 v[56:59], v1 offset:5184\n\t" \
    "v_mfma_f32_16x16x32_bf16 v[24:27], v[80:83], v[88:91], v[24:27]\n\t" \
    "s_waitcnt lgkmcnt(9)\n\t" \
    "v_mfma_f32_16x16x32_bf16 v[12:15], v[76:79], v[92:95], v[12:15]\n\t" \
    "v_mfma_f32_16x16x32_bf16 v[28:31], v[80:83], v[92:95], v[28:31]\n\t" \
    "s_waitcnt lgkmcnt(8)\n\t" \
    "v_mfma_f32_16x16x32_bf16 v[16:19], v[76:79], v[96:99], v[16:19]\n\t" \
    "v_mfma_f32_16x16x32_bf16 v[32:35], v[80:83], v[96:99], v[32:35]\n\t" \
    "v_add_u32 v0, 128, v0\n\t" \
    "v_subrev_u32 v1, 192, v1\n\t" \
    "s_sub_u32 s69, s69, 1\n\t" \
    "s_cmp_lg_u32 s69, 0\n\t" \
    "s_cbranch_scc1 cv_c1_%=\n\t" \
    "s_waitcnt lgkmcnt(0)\n\t" \
    "ds_read_b64 v[36:37], v0 offset:0\n\t" \
    "ds_read_b64 v[38:39], v0 offset:8\n\t" \
    "ds_read_b64 v[40:41], v0 offset:32\n\t" \
    "ds_read_b64 v[42:43], v0 offset:40\n\t" \
    "ds_read_b128 v[48:51], v1 offset:2304\n\t" \
    "ds_read_b128 v[52:55], v1 offset:3840\n\t" \
    "ds_read_b128 v[56:59], v1 offset:5376\n\t" \
    "ds_read_b64 v[76:77], v0 offset:64\n\t" \
    "ds_read_b64 v[78:79], v0 offset:72\n\t" \
    "ds_read_b64 v[80:81], v0 offset:96\n\t" \
    "ds_read_b64 v[82:83], v0 offset:104\n\t" \
    "ds_read_b128 v[88:91], v1 offset:2208\n\t" \
    "ds_read_b128 v[92:95], v1 offset:3744\n\t" \
    "ds_read_b128 v[96:99], v1 offset:5280\n\t" \
    "s_mov_b32 s69, 4\n\t" \
    "cv_c2_%=:\n\t" \
    "s_waitcnt lgkmcnt(9)\n\t" \
    "v_mfma_f32_16x16x32_bf16 v[8:11], v[36:39], v[48:51], v[8:11]\n\t" \
    "ds_read_b64 v[116:117], v0 offset:128\n\t" \
    "ds_read_b64 v[118:119], v0 offset:136\n\t" \
    "ds_read_b64 v[120:121], v0 offset:160\n\t" \
    "v_mfma_f32_16x16x32_bf16 v[24:27], v[40:43], v[48:51], v[24:27]\n\t" \
    "ds_read_b64 v[122:123], v0 offset:168\n\t" \
    "ds_read_b128 v[128:131], v1 offset:2112\n\t" \
    "ds_read_b128 v[132:135], v1 offset:3648\n\t" \
    "s_waitcnt lgkmcnt(14)\n\t" \
    "v_mfma_f32_16x16x32_bf16 v[12:15], v[36:39], v[52:55], v[12:15]\n\t" \
    "ds_read_b128 v[136:139], v1 offset:5184\n\t" \
    "v_mfma_f32_16x16x32_bf16 v[28:31], v[40:43], v[52:55], v[28:31]\n\t" \
    "s_waitcnt lgkmcnt(14)\n\t" \
    "v_mfma_f32_16x16x32_bf16 v[16:19], v[36:39], v[56:59], v[16:19]\n\t" \
    "ds_read_b64 v[156:157], v0 offset:192\n\t" \
    "v_mfma_f32_16x16x32_bf16 v[32:35], v[40:43], v[56:59], v[32:35]\n\t" \
    "s_waitcnt lgkmcnt(10)\n\t" \
    "v_mfma_f32_16x16x32_bf16 v[8:11], v[76:79], v[88:91], v[8:11]\n\t" \
    "ds_read_b64 v[158:159], v0 offset:200\n\t" \
    "ds_read_b64 v[160:161], v0 offset:224\n\t" \
    "ds_read_b64 v[162:163], v0 offset:232\n\t" \
    "v_mfma_f32_16x16x32_bf16 v[24:27], v[80:83], v[88:91], v[24:27]\n\t" \
    "ds_read_b128 v[168:171], v1 offset:2016\n\t" \
    "ds_read_b128 v[172:175], v1 offset:3552\n\t" \
    "s_waitcnt lgkmcnt(14)\n\t" \
    "v_mfma_f32_16x16x32_bf16 v[12:15], v[76:79], v[92:95], v[12:15]\n\t" \
    "ds_read_b128 v[176:179], v1 offset:5088\n\t" \
    "v_mfma_f32_16x16x32_bf16 v[28:31], v[80:83], v[92:95], v[28:31]\n\t" \
    "s_waitcnt lgkmcnt(14)\n\t" \
    "v_mfma_f32_16x16x32_bf16 v[16:19], v[76:79], v[96:99], v[16:19]\n\t" \
    "v_mfma_f32_16x16x32_bf16 v[32:35], v[80:83], v[96:99], v[32:35]\n\t" \
    "s_waitcnt lgkmcnt(9)\n\t" \
    "v_mfma_f32_16x16x32_bf16 v[8:11], v[116:119], v[128:131], v[8:11]\n\t" \
    "ds_read_b64 v[36:37], v0 offset:256\n\t" \
    "ds_read_b64 v[38:39], v0 offset:264\n\t" \
    "ds_read_b64 v[40:41], v0 offset:288\n\t" \
    "v_mfma_f32_16x16x32_bf16 v[24:27], v[120:123], v[128:131], v[24:27]\n\t" \
    "ds_read_b64 v[42:43], v0 offset:296\n\t" \
    "ds_read_b128 v[48:51], v1 offset:1920\n\t" \
    "ds_read_b128 v[52:55], v1 offset:3456\n\t" \
    "s_waitcnt lgkmcnt(14)\n\t" \
    "v_mfma_f32_16x16x32_bf16 v[12:15], v[116:119], v[132:135], v[12:15]\n\t" \
    "ds_read_b128 v[56:59], v1 offset:4992\n\t" \
    "v_mfma_f32_16x16x32_bf16 v[28:31], v[120:123], v[132:135], v[28:31]\n\t" \
    "s_waitcnt lgkmcnt(14)\n\t" \
    "v_mfma_f32_16x16x32_bf16 v[16:19], v[116:119], v[136:139], v[16:19]\n\t" \
    "ds_read_b64 v[76:77], v0 offset:320\n\t" \
    "v_mfma_f32_16x16x32_bf16 v[32:35], v[120:123], v[136:139], v[32:35]\n\t" \
    "s_waitcnt lgkmcnt(10)\n\t" \
    "v_mfma_f32_16x16x32_bf16 v[8:11], v[156:159], v[168:171], v[8:11]\n\t" \
    "ds_read_b64 v[78:79], v0 offset:328\n\t" \
    "ds_read_b64 v[80:81], v0 offset:352\n\t" \
    "ds_read_b64 v[82:83], v0 offset:360\n\t" \
    "v_mfma_f32_16x16x32_bf16 v[24:27], v[160:163], v[168:171], v[24:27]\n\t" \
    "ds_read_b128 v[88:91], v1 offset:1824\n\t" \
    "ds_read_b128 v[92:95], v1 offset:3360\n\t" \
    "s_waitcnt lgkmcnt(14)\n\t" \
    "v_mfma_f32_16x16x32_bf16 v[12:15], v[156:159], v[172:175], v[12:15]\n\t" \
    "ds_read_b128 v[96:99], v1 offset:4896\n\t" \
    "v_mfma_f32_16x16x32_bf16 v[28:31], v[160:163], v[172:175], v[28:31]\n\t" \
    "s_waitcnt lgkmcnt(14)\n\t" \
    "v_mfma_f32_16x16x32_bf16 v[16:19], v[156:159], v[176:179], v[16:19]\n\t" \
    "v_mfma_f32_16x16x32_bf16 v[32:35], v[160:163], v[176:179], v[32:35]\n\t" \
    "v_add_u32 v0, 256, v0\n\t" \
    "v_subrev_u32 v1, 384, v1\n\t" \
    "s_sub_u32 s69, s69, 1\n\t" \
    "s_cmp_lg_u32 s69, 0\n\t" \
    "s_cbranch_scc1 cv_c2_%=\n\t" \
    "s_waitcnt lgkmcnt(0)\n\t" \
    "s_mov_b32 s69, 4\n\t" \
    "cv_c3_%=:\n\t" \
    "s_waitcnt lgkmcnt(7)\n\t" \
    "v_mfma_f32_16x16x32_bf16 v[12:15], v[36:39], v[52:55], v[12:15]\n\t" \
    "ds_read_b64 v[116:117], v0 offset:128\n\t" \
    "ds_read_b64 v[118:119], v0 offset:136\n\t" \
    "ds_read_b64 v[120:121], v0 offset:160\n\t" \
    "v_mfma_f32_16x16x32_bf16 v[28:31], v[40:43], v[52:55], v[28:31]\n\t" \
    "ds_read_b64 v[122:123], v0 offset:168\n\t" \
    "ds_read_b128 v[132:135], v1 offset:3648\n\t" \
    "ds_read_b128 v[136:139], v1 offset:5184\n\t" \
    "s_waitcnt lgkmcnt(12)\n\t" \
    "v_mfma_f32_16x16x32_bf16 v[16:19], v[36:39], v[56:59], v[16:19]\n\t" \
    "ds_read_b64 v[156:157], v0 offset:192\n\t" \
    "ds_read_b64 v[158:159], v0 offset:200\n\t" \
    "ds_read_b64 v[160:161], v0 offset:224\n\t" \
    "v_mfma_f32_16x16x32_bf16 v[32:35], v[40:43], v[56:59], v[32:35]\n\t" \
    "s_waitcnt lgkmcnt(10)\n\t" \
    "v_mfma_f32_16x16x32_bf16 v[12:15], v[76:79], v[92:95], v[12:15]\n\t" \
    "ds_read_b64 v[162:163], v0 offset:232\n\t" \
    "ds_read_b128 v[172:175], v1 offset:3552\n\t" \
    "ds_read_b128 v[176:179], v1 offset:5088\n\t" \
    "v_mfma_f32_16x16x32_bf16 v[28:31], v[80:83], v[92:95], v[28:31]\n\t" \
    "s_waitcnt lgkmcnt(12)\n\t" \
    "v_mfma_f32_16x16x32_bf16 v[16:19], v[76:79], v[96:99], v[16:19]\n\t" \
    "v_mfma_f32_16x16x32_bf16 v[32:35], v[80:83], v[96:99], v[32:35]\n\t" \
    "s_waitcnt lgkmcnt(7)\n\t" \
    "v_mfma_f32_16x16x32_bf16 v[12:15], v[116:119], v[132:135], v[12:15]\n\t" \
    "ds_read_b64 v[36:37], v0 offset:256\n\t" \
    "ds_read_b64 v[38:39], v0 offset:264\n\t" \
    "ds_read_b64 v[40:41], v0 offset:288\n\t" \
    "v_mfma_f32_16x16x32_bf16 v[28:31], v[120:123], v[132:135], v[28:31]\n\t" \
    "ds_read_b64 v[42:43], v0 offset:296\n\t" \
    "ds_read_b128 v[52:55], v1 offset:3456\n\t" \
    "ds_read_b128 v[56:59], v1 offset:4992\n\t" \
    "s_waitcnt lgkmcnt(12)\n\t" \
    "v_mfma_f32_16x16x32_bf16 v[16:19], v[116:119], v[136:139], v[16:19]\n\t" \
    "ds_read_b64 v[76:77], v0 offset:320\n\t" \
    "ds_read_b64 v[78:79], v0 offset:328\n\t" \
    "ds_read_b64 v[80:81], v0 offset:352\n\t" \
    "v_mfma_f32_16x16x32_bf16 v[32:35], v[120:123], v[136:139], v[32:35]\n\t" \
    "s_waitcnt lgkmcnt(10)\n\t" \
    "v_mfma_f32_16x16x32_bf16 v[12:15], v[156:159], v[172:175], v[12:15]\n\t" \
    "ds_read_b64 v[82:83], v0 offset:360\n\t" \
    "ds_read_b128 v[92:95], v1 offset:3360\n\t" \
    "ds_read_b128 v[96:99], v1 offset:4896\n\t" \
    "v_mfma_f32_16x16x32_bf16 v[28:31], v[160:163], v[172:175], v[28:31]\n\t" \
    "s_waitcnt lgkmcnt(12)\n\t" \
    "v_mfma_f32_16x16x32_bf16 v[16:19], v[156:159], v[176:179], v[16:19]\n\t" \
    "v_mfma_f32_16x16x32_bf16 v[32:35], v[160:163], v[176:179], v[32:35]\n\t" \
    "v_add_u32 v0, 256, v0\n\t" \
    "v_subrev_u32 v1, 384, v1\n\t" \
    "s_sub_u32 s69, s69, 1\n\t" \
    "s_cmp_lg_u32 s69, 0\n\t" \
    "s_cbranch_scc1 cv_c3_%=\n\t" \
    "s_waitcnt lgkmcnt(0)\n\t" \
    "s_mov_b32 s69, 4\n\t" \
    "cv_c4_%=:\n\t" \
    "s_waitcnt lgkmcnt(5)\n\t" \
    "v_mfma_f32_16x16x32_bf16 v[16:19], v[36:39], v[56:59], v[16:19]\n\t" \
    "ds_read_b64 v[116:117], v0 offset:128\n\t" \
    "ds_read_b64 v[118:119], v0 offset:136\n\t" \
    "ds_read_b64 v[120:121], v0 offset:160\n\t" \
    "v_mfma_f32_16x16x32_bf16 v[32:35], v[40:43], v[56:59], v[32:35]\n\t" \
    "ds_read_b64 v[122:123], v0 offset:168\n\t" \
    "ds_read_b128 v[136:139], v1 offset:5184\n\t" \
    "ds_read_b64 v[156:157], v0 offset:192\n\t" \
    "s_waitcnt lgkmcnt(6)\n\t" \
    "v_mfma_f32_16x16x32_bf16 v[16:19], v[76:79], v[96:99], v[16:19]\n\t" \
    "ds_read_b64 v[158:159], v0 offset:200\n\t" \
    "ds_read_b64 v[160:161], v0 offset:224\n\t" \
    "ds_read_b64 v[162:163], v0 offset:232\n\t" \
    "v_mfma_f32_16x16x32_bf16 v[32:35], v[80:83], v[96:99], v[32:35]\n\t" \
    "ds_read_b128 v[176:179], v1 offset:5088\n\t" \
    "s_waitcnt lgkmcnt(5)\n\t" \
    "v_mfma_f32_16x16x32_bf16 v[16:19], v[116:119], v[136:139], v[16:19]\n\t" \
    "ds_read_b64 v[36:37], v0 offset:256\n\t" \
    "ds_read_b64 v[38:39], v0 offset:264\n\t" \
    "ds_read_b64 v[40:41], v0 offset:288\n\t" \
    "v_mfma_f32_16x16x32_bf16 v[32:35], v[120:123], v[136:139], v[32:35]\n\t" \
    "ds_read_b64 v[42:43], v0 offset:296\n\t" \
    "ds_read_b128 v[56:59], v1 offset:4992\n\t" \
    "ds_read_b64 v[76:77], v0 offset:320\n\t" \
    "s_waitcnt lgkmcnt(6)\n\t" \
    "v_mfma_f32_16x16x32_bf16 v[16:19], v[156:159], v[176:179], v[16:19]\n\t" \
    "ds_read_b64 v[78:79], v0 offset:328\n\t" \
    "ds_read_b64 v[80:81], v0 offset:352\n\t" \
    "ds_read_b64 v[82:83], v0 offset:360\n\t" \
    "v_mfma_f32_16x16x32_bf16 v[32:35], v[160:163], v[176:179], v[32:35]\n\t" \
    "ds_read_b128 v[96:99], v1 offset:4896\n\t" \
    "v_add_u32 v0, 256, v0\n\t" \
    "v_subrev_u32 v1, 384, v1\n\t" \
    "s_sub_u32 s69, s69, 1\n\t" \
    "s_cmp_lg_u32 s69, 0\n\t" \
    "s_cbranch_scc1 cv_c4_%=\n\t" \
    "s_waitcnt lgkmcnt(0)\n\t" \
    "s_nop 7\n\t" \
    "ds_write_b128 v2, v[4:7] offset:0\n\t" \
    "ds_write_b128 v2, v[8:11] offset:1024\n\t" \
    "ds_write_b128 v2, v[12:15] offset:2048\n\t" \
    "ds_write_b128 v2, v[16:19] offset:3072\n\t" \
    "ds_write_b128 v2, v[20:23] offset:4096\n\t" \
    "ds_write_b128 v2, v[24:27] offset:5120\n\t" \
    "ds_write_b128 v2, v[28:31] offset:6144\n\t" \
    "ds_write_b128 v2, v[32:35] offset:7168\n\t" \
    "s_waitcnt lgkmcnt(0)\n\t" \
    "cv_last_%=:\n\t" \
    ""
#define ASM_EA \
    "s_bitcmp1_b32 %1, 2\n\t" \
    "s_cselect_b32 s40, 0x30, 0x28\n\t" \
    "s_cmp_lt_u32 %1, 64\n\t" \
    "s_cselect_b32 s40, s40, 0x38\n\t" \
    "s_load_dwordx2 s[42:43], %0, s40\n\t" \
    "s_load_dword s48, %0, 0x40\n\t" \
    "s_getpc_b64 s[44:45]\n\t" \
    "ea_pc_%=:\n\t" \
    "s_bfe_u32 s40, %1, 0x20003\n\t" \
    "s_lshl_b32 s40, s40, 15\n\t" \
    "v_lshl_add_u32 v1, %2, 7, s40\n\t" \
    "v_min_u32 v1, code_end_marker-ea_pc_%=, v1\n\t" \
    "s_mov_b32 m0, 155648\n\t" \
    "v_readfirstlane_b32 s41, %2\n\t" \
    "global_load_lds_dword v1, s[44:45]\n\t" \
    "s_bitcmp1_b32 %1, 2\n\t" \
    "s_cselect_b32 s40, 24, 16\n\t" \
    "s_lshr_b32 s47, %1, 3\n\t" \
    "s_cmp_ge_u32 s47, s40\n\t" \
    "s_cbranch_scc1 ea_done1_%=\n\t" \
    "s_mov_b32 s46, pad_qk-ea_pc_%=\n\t" \
    "s_mov_b32 s40, pad_vv-ea_pc_%=\n\t" \
    "s_bitcmp1_b32 %1, 2\n\t" \
    "s_cselect_b32 s46, pad_gx-ea_pc_%=, s46\n\t" \
    "s_cselect_b32 s40, pad_gv-ea_pc_%=, s40\n\t" \
    "s_cmp_lt_u32 %1, 64\n\t" \
    "s_cselect_b32 s40, s46, s40\n\t" \
    "s_add_u32 s44, s44, s40\n\t" \
    "s_addc_u32 s45, s45, 0\n\t" \
    "s_swappc_b64 s[46:47], s[44:45]\n\t" \
    "s_lshl_b32 m0, s41, 4\n\t" \
    "s_and_b32 s40, %1, 3\n\t" \
    "s_lshl_b32 s40, s40, 11\n\t" \
    "s_bfe_u32 s46, %1, 0x30003\n\t" \
    "s_lshl_b32 s46, s46, 8\n\t" \
    "s_add_u32 s40, s40, s46\n\t" \
    "s_and_b32 s41, s41, 0x80\n\t" \
    "s_xor_b32 s40, s40, s41\n\t" \
    "v_lshrrev_b32 v1, 3, %2\n\t" \
    "v_and_b32 v1, 6, v1\n\t" \
    "v_xor_b32 v1, v1, %2\n\t" \
    "v_and_b32 v1, 15, v1\n\t" \
    "v_lshlrev_b32 v1, 4, v1\n\t" \
    "v_xor_b32 v1, s40, v1\n\t" \
    "v_lshrrev_b32 v2, 4, %2\n\t" \
    "v_lshl_add_u32 v1, v2, 13, v1\n\t" \
    "s_mov_b32 s40, 16\n\t" \
    "s_waitcnt lgkmcnt(0)\n\t" \
    "ea_lp2_%=:\n\t" \
    "global_load_lds_dwordx4 v1, s[42:43]\n\t" \
    "v_add_u32 v1, 0x20000, v1\n\t" \
    "s_add_u32 m0, m0, 4096\n\t" \
    "s_sub_u32 s40, s40, 1\n\t" \
    "s_cmp_lg_u32 s40, 0\n\t" \
    "s_cbranch_scc1 ea_lp2_%=\n\t" \
    "ea_done1_%=:\n\t" \
    "s_waitcnt lgkmcnt(0)\n\t" \
    "ea_last_%=:\n\t" \
    ""
#define ASM_CLOB "v0", "v1", "v2", "v3", "v4", "v5", "v6", "v7", "v8", "v9", "v10", "v11", "v12", "v13", "v14", "v15", "v16", "v17", "v18", "v19", "v20", "v21", "v22", "v23", "v24", "v25", "v26", "v27", "v28", "v29", "v30", "v31", "v32", "v33", "v34", "v35", "v36", "v37", "v38", "v39", "v40", "v41", "v42", "v43", "v44", "v45", "v46", "v47", "v48", "v49", "v50", "v51", "v52", "v53", "v54", "v55", "v56", "v57", "v58", "v59", "v60", "v61", "v62", "v63", "v64", "v65", "v66", "v67", "v68", "v69", "v70", "v71", "v72", "v73", "v74", "v75", "v76", "v77", "v78", "v79", "v80", "v81", "v82", "v83", "v84", "v85", "v86", "v87", "v88", "v89", "v90", "v91", "v92", "v93", "v94", "v95", "v96", "v97", "v98", "v99", "v100", "v101", "v102", "v103", "v104", "v105", "v106", "v107", "v108", "v109", "v110", "v111", "v112", "v113", "v114", "v115", "v116", "v117", "v118", "v119", "v120", "v121", "v122", "v123", "v124", "v125", "v126", "v127", "v128", "v129", "v130", "v131", "v132", "v133", "v134", "v135", "v136", "v137", "v138", "v139", "v140", "v141", "v142", "v143", "v144", "v145", "v146", "v147", "v148", "v149", "v150", "v151", "v152", "v153", "v154", "v155", "v156", "v157", "v158", "v159", "v160", "v161", "v162", "v163", "v164", "v165", "v166", "v167", "v168", "v169", "v170", "v171", "v172", "v173", "v174", "v175", "v176", "v177", "v178", "v179", "v180", "v181", "v182", "v183", "v184", "v185", "v186", "v187", "v188", "v189", "v190", "v191", "v192", "v193", "v194", "v195", "v196", "v197", "v198", "v199", "v200", "v201", "v202", "v203", "v204", "v205", "v206", "v207", "v208", "v209", "v210", "v211", "v212", "v213", "v214", "v215", "v222", "v223", "v224", "v225", "v226", "v227", "v228", "v229", "v230", "v231", "v232", "v233", "v234", "v235", "v236", "v237", "v238", "v239", "v240", "v241", "v242", "v243", "v244", "v245", "v246", "v247", "v248", "v249", "v250", "v251", "v252", "v253", "v254", "v255", "a0", "a1", "a2", "a3", "a4", "a5", "a6", "a7", "a8", "a9", "a10", "a11", "a12", "a13", "a14", "a15", "a16", "a17", "a18", "a19", "a20", "a21", "a22", "a23", "a24", "a25", "a26", "a27", "a28", "a29", "a30", "a31", "a32", "a33", "a34", "a35", "a36", "a37", "a38", "a39", "a40", "a41", "a42", "a43", "a44", "a45", "a46", "a47", "a48", "a49", "a50", "a51", "a52", "a53", "a54", "a55", "a56", "a57", "a58", "a59", "a60", "a61", "a62", "a63", "a64", "a65", "a66", "a67", "a68", "a69", "a70", "a71", "a72", "a73", "a74", "a75", "a76", "a77", "a78", "a79", "a80", "a81", "a82", "a83", "a84", "a85", "a86", "a87", "a88", "a89", "a90", "a91", "a92", "a93", "a94", "a95", "a96", "a97", "a98", "a99", "a100", "a101", "a102", "a103", "a104", "a105", "a106", "a107", "a108", "a109", "a110", "a111", "a112", "a113", "a114", "a115", "a116", "a117", "a118", "a119", "a120", "a121", "a122", "a123", "a124", "a125", "a126", "a127", "a128", "a129", "a130", "a131", "a132", "a133", "a134", "a135", "a136", "a137", "a138", "a139", "a140", "a141", "a142", "a143", "a144", "a145", "a146", "a147", "a148", "a149", "a150", "a151", "a152", "a153", "a154", "a155", "a156", "a157", "a158", "a159", "a160", "a161", "a162", "a163", "a164", "a165", "a166", "a167", "a168", "a169", "a170", "a171", "a172", "a173", "a174", "a175", "a176", "a177", "a178", "a179", "a180", "a181", "a182", "a183", "a184", "a185", "a186", "a187", "a188", "a189", "a190", "a191", "a192", "a193", "a194", "a195", "a196", "a197", "a198", "a199", "a200", "a201", "a202", "a203", "a204", "a205", "a206", "a207", "a208", "a209", "a210", "a211", "a212", "a213", "a214", "a215", "a216", "a217", "a218", "a219", "a220", "a221", "a222", "a223", "a224", "a225", "a226", "a227", "a228", "a229", "a230", "a231", "a232", "a233", "a234", "a235", "a236", "a237", "a238", "a239", "a240", "a241", "a242", "a243", "a244", "a245", "a246", "a247", "a248", "a249", "a250", "a251", "a252", "a253", "a254", "a255", "s33", "s34", "s35", "s36", "s37", "s38", "s39", "s40", "s41", "s42", "s43", "s44", "s45", "s46", "s47", "s48", "s49", "s50", "s51", "s52", "s53", "s54", "s55", "s56", "s57", "s58", "s59", "s60", "s61", "s62", "s63", "s64", "s65", "s66", "s67", "s68", "s69", "s70", "s71", "s72", "s73", "s74", "s75", "s76", "s77", "s78", "s79", "s80", "s81", "s82", "s83", "s84", "s85", "s86", "s87", "s88", "s89", "s90", "s91", "s92", "s93", "s94", "s95", "s96", "s97", "s98", "s99", "s100", "s101", "vcc", "scc", "memory"
#define ASM_CLOBW "v32", "v33", "v34", "v35", "v36", "v37", "v38", "v39", "v40", "v41", "v42", "v43", "v44", "v45", "v46", "v47", "v48", "v49", "v50", "v51", "v52", "v53", "v54", "v55", "v56", "v57", "v58", "v59", "v60", "v61", "v62", "v63", "v64", "v65", "v66", "v67", "v68", "v69", "v70", "v71", "v72", "v73", "v74", "v75", "v76", "v77", "v78", "v79", "v80", "v81", "v82", "v83", "v84", "v85", "v86", "v87", "v88", "v89", "v90", "v91", "v92", "v93", "v94", "v95", "v96", "v97", "v98", "v99", "v100", "v101", "v102", "v103", "v104", "v105", "v106", "v107", "v108", "v109", "v110", "v111", "v112", "v113", "v114", "v115", "v116", "v117", "v118", "v119", "v120", "v121", "v122", "v123", "v124", "v125", "v126", "v127", "v128", "v129", "v130", "v131", "v132", "v133", "v134", "v135", "v136", "v137", "v138", "v139", "v140", "v141", "v142", "v143", "v144", "v145", "v146", "v147", "v148", "v149", "v150", "v151", "v152", "v153", "v154", "v155", "v156", "v157", "v158", "v159", "v160", "v161", "v162", "v163", "v164", "v165", "v166", "v167", "v168", "v169", "v170", "v171", "v172", "v173", "v174", "v175", "v176", "v177", "v178", "v179", "v180", "v181", "v182", "v183", "v184", "v185", "v186", "v187", "v188", "v189", "v190", "v191", "v192", "v193", "v194", "v195", "v196", "v197", "v198", "v199", "v222", "v223", "v224", "v225", "v226", "v227", "v228", "v229", "v230", "v231", "v232", "v233", "v234", "v235", "v236", "v237", "v238", "v239", "v240", "v241", "v242", "v243", "v244", "v245", "v246", "v247", "v248", "v249", "v250", "v251", "v252", "v253", "v254", "v255", "a0", "a1", "a2", "a3", "a4", "a5", "a6", "a7", "a8", "a9", "a10", "a11", "a12", "a13", "a14", "a15", "a16", "a17", "a18", "a19", "a20", "a21", "a22", "a23", "a24", "a25", "a26", "a27", "a28", "a29", "a30", "a31", "a32", "a33", "a34", "a35", "a36", "a37", "a38", "a39", "a40", "a41", "a42", "a43", "a44", "a45", "a46", "a47", "a48", "a49", "a50", "a51", "a52", "a53", "a54", "a55", "a56", "a57", "a58", "a59", "a60", "a61", "a62", "a63", "a64", "a65", "a66", "a67", "a68", "a69", "a70", "a71", "a72", "a73", "a74", "a75", "a76", "a77", "a78", "a79", "a80", "a81", "a82", "a83", "a84", "a85", "a86", "a87", "a88", "a89", "a90", "a91", "a92", "a93", "a94", "a95", "a96", "a97", "a98", "a99", "a100", "a101", "a102", "a103", "a104", "a105", "a106", "a107", "a108", "a109", "a110", "a111", "a112", "a113", "a114", "a115", "a116", "a117", "a118", "a119", "a120", "a121", "a122", "a123", "a124", "a125", "a126", "a127", "a128", "a129", "a130", "a131", "a132", "a133", "a134", "a135", "a136", "a137", "a138", "a139", "a140", "a141", "a142", "a143", "a144", "a145", "a146", "a147", "a148", "a149", "a150", "a151", "a152", "a153", "a154", "a155", "a156", "a157", "a158", "a159", "a160", "a161", "a162", "a163", "a164", "a165", "a166", "a167", "a168", "a169", "a170", "a171", "a172", "a173", "a174", "a175", "a176", "a177", "a178", "a179", "a180", "a181", "a182", "a183", "a184", "a185", "a186", "a187", "a188", "a189", "a190", "a191", "a192", "a193", "a194", "a195", "a196", "a197", "a198", "a199", "a200", "a201", "a202", "a203", "a204", "a205", "a206", "a207", "a208", "a209", "a210", "a211", "a212", "a213", "a214", "a215", "a216", "a217", "a218", "a219", "a220", "a221", "a222", "a223", "a224", "a225", "a226", "a227", "a228", "a229", "a230", "a231", "a232", "a233", "a234", "a235", "a236", "a237", "a238", "a239", "a240", "a241", "a242", "a243", "a244", "a245", "a246", "a247", "a248", "a249", "a250", "a251", "a252", "a253", "a254", "a255", "s33", "s34", "s35", "s36", "s37", "s38", "s39", "s40", "s41", "s42", "s43", "s44", "s45", "s46", "s47", "s48", "s49", "s50", "s51", "s52", "s53", "s54", "s55", "s56", "s57", "s58", "s59", "s60", "s61", "s62", "s63", "s64", "s65", "s66", "s67", "s68", "s69", "s70", "s71", "s72", "s73", "s74", "s75", "s76", "s77", "s78", "s79", "s80", "s81", "s82", "s83", "s84", "s85", "s86", "s87", "s88", "s89", "s90", "s91", "s92", "s93", "s94", "s95", "s96", "s97", "s98", "s99", "s100", "s101", "vcc", "scc", "memory"
#define CV_CLOB "v0", "v1", "v2", "v3", "v4", "v5", "v6", "v7", "v8", "v9", "v10", "v11", "v12", "v13", "v14", "v15", "v16", "v17", "v18", "v19", "v20", "v21", "v22", "v23", "v24", "v25", "v26", "v27", "v28", "v29", "v30", "v31", "v32", "v33", "v34", "v35", "v36", "v37", "v38", "v39", "v40", "v41", "v42", "v43", "v44", "v45", "v46", "v47", "v48", "v49", "v50", "v51", "v52", "v53", "v54", "v55", "v56", "v57", "v58", "v59", "v60", "v61", "v62", "v63", "v64", "v65", "v66", "v67", "v68", "v69", "v70", "v71", "v72", "v73", "v74", "v75", "v76", "v77", "v78", "v79", "v80", "v81", "v82", "v83", "v84", "v85", "v86", "v87", "v88", "v89", "v90", "v91", "v92", "v93", "v94", "v95", "v96", "v97", "v98", "v99", "v100", "v101", "v102", "v103", "v104", "v105", "v106", "v107", "v108", "v109", "v110", "v111", "v112", "v113", "v114", "v115", "v116", "v117", "v118", "v119", "v120", "v121", "v122", "v123", "v124", "v125", "v126", "v127", "v128", "v129", "v130", "v131", "v132", "v133", "v134", "v135", "v136", "v137", "v138", "v139", "v140", "v141", "v142", "v143", "v144", "v145", "v146", "v147", "v148", "v149", "v150", "v151", "v152", "v153", "v154", "v155", "v156", "v157", "v158", "v159", "v160", "v161", "v162", "v163", "v164", "v165", "v166", "v167", "v168", "v169", "v170", "v171", "v172", "v173", "v174", "v175", "v176", "v177", "v178", "v179", "v180", "v181", "v182", "v183", "v184", "v185", "v186", "v187", "v188", "v189", "v190", "v191", "v192", "v193", "v194", "v195", "s69", "scc", "memory"
static constexpr unsigned G_STEP = 0x10000;
static constexpr unsigned G_WS_CTL = 0x300000;
static constexpr unsigned G_WS_FIX = 0x580000;
static constexpr unsigned G_WS_DONE = 0x5f0000;
static constexpr unsigned G_WS_EP = 0x5f2000;
static constexpr unsigned G_WS_STAMPS = 0xc000000;
static constexpr unsigned G_C_TASK = 0x200;
static constexpr unsigned G_C_GX = 0x1000;
static constexpr unsigned G_C_GV = 0x4000;
static constexpr unsigned G_C_KM = 0x8000;
static constexpr unsigned G_C_UM = 0x9000;
static constexpr unsigned G_C_PC = 0xc000;
static constexpr unsigned G_C_YC = 0xd000;
static constexpr unsigned G_C_GQ = 0x10000;
static constexpr unsigned G_C_FX = 0x280000;
static constexpr unsigned G_C_ERR = 0x280100;
static constexpr unsigned G_C_ARR = 0x280400;
static constexpr unsigned G_C_GO = 0x280800;
static constexpr unsigned G_C_UT = 0x281000;
static constexpr unsigned G_C_RDY = 0x282000;
static constexpr unsigned G_C_GT = 0x284000;
static constexpr unsigned G_C_KG = 0x288000;
static constexpr unsigned G_C_EF = 0x287000;
static constexpr unsigned G_C_RW = 0x280000;
static constexpr unsigned G_WS_H = 0x0;
static constexpr unsigned G_HS8 = 0x80000;
static constexpr unsigned G_HS4 = 0x40000;
static constexpr unsigned G_UXS = 0x200000;
static constexpr unsigned G_UMS = 0x90000;
static constexpr unsigned G_PROBE = 0x1bd400;
static constexpr unsigned G_D_QX = 0x600000;
static constexpr unsigned G_D_KX = 0x1600000;
static constexpr unsigned G_D_KM = 0x2600000;
static constexpr unsigned G_D_UX = 0x2e00000;
static constexpr unsigned G_D_UM = 0x7600000;
static constexpr unsigned G_D_PX = 0xa200000;
static constexpr unsigned G_D_PM = 0xa600000;
static constexpr unsigned G_D_KT = 0xa700000;
static constexpr unsigned G_D_UT = 0xac00000;
static constexpr unsigned G_D_YB = 0xaf00000;
static constexpr unsigned G_D_END = 0xb200000;
static constexpr unsigned G_SKM = 0x7;
static constexpr unsigned G_S1 = 0x18;
static constexpr unsigned G_G2 = 0x0;
static constexpr unsigned G_S0 = 0x7;
static constexpr unsigned G_GVN = 0x2;
static constexpr unsigned G_STG = 0x9000;
static constexpr unsigned G_KROW = 0x30;
static constexpr unsigned G_NHL = 0x1;
#define FLG(x_) (kDefault[5])
#define FLH(x_) (kDefault[7])
static constexpr int kDefault[8] = {PRM_DEFAULT};
#define MFMA32(a_, b_, c_) __builtin_amdgcn_mfma_f32_16x16x32_bf16(__builtin_bit_cast(bf16x8, a_), __builtin_bit_cast(bf16x8, b_), c_, 0, 0, 0)
#define MFMA16(a_, b_, c_) __builtin_amdgcn_mfma_f32_16x16x16bf16_1k(__builtin_bit_cast(s16x4, a_), __builtin_bit_cast(s16x4, b_), c_, 0, 0, 0)
#define WLD8(d_, voff_, base_, off_) asm volatile("s_nop 4\n\tglobal_load_dwordx2 %0, %1, %2 offset:%3 sc1" : "=v"(d_) : "v"(voff_), "s"(base_), "i"(off_) : "memory")
#define WLD16(d_, voff_, base_, off_) asm volatile("s_nop 4\n\tglobal_load_dwordx4 %0, %1, %2 offset:%3 sc1" : "=v"(d_) : "v"(voff_), "s"(base_), "i"(off_) : "memory")
#define WST16(voff_, d_, base_, off_) asm volatile("s_nop 4\n\tglobal_store_dwordx4 %0, %1, %2 offset:%3 sc1\n\ts_nop 1" :: "v"(voff_), "v"(d_), "s"(base_), "i"(off_) : "memory")
#define OMOD ""
#define OST8(p_, d_) asm volatile("global_store_dwordx2 %0, %1, off " OMOD :: "v"(p_), "v"(d_) : "memory")
#define OST16(p_, d_) asm volatile("global_store_dwordx4 %0, %1, off " OMOD "\n\ts_nop 1" :: "v"(p_), "v"(d_) : "memory")
#define OWT 0
#define OWM "sc1"
#define OST16W(p_, d_) asm volatile("global_store_dwordx4 %0, %1, off " OWM "\n\ts_nop 1" :: "v"(p_), "v"(d_) : "memory")
#define WLD16X4(d0, d1, d2, d3, voff_, base_) asm volatile("s_nop 4\n\tglobal_load_dwordx4 %0, %4, %5 sc1\n\tglobal_load_dwordx4 %1, %4, %5 offset:1024 sc1\n\t" \
    "global_load_dwordx4 %2, %4, %5 offset:2048 sc1\n\tglobal_load_dwordx4 %3, %4, %5 offset:3072 sc1" \
    : "=&v"(d0), "=&v"(d1), "=&v"(d2), "=&v"(d3) : "v"(voff_), "s"(base_) : "memory")

__device__ __forceinline__ u32x4 pack8(const f32x4& a, const f32x4& b) {
    return u32x4{pk2bf(a[0], a[1]), pk2bf(a[2], a[3]), pk2bf(b[0], b[1]), pk2bf(b[2], b[3])};
}
template <class T> __device__ __forceinline__ T* uni(T* q) {
    const u64 v = (u64)q;
    unsigned lo = __builtin_amdgcn_readfirstlane((unsigned)v), hi = __builtin_amdgcn_readfirstlane((unsigned)(v >> 32));
    asm volatile("s_nop 4" : "+s"(lo), "+s"(hi));
    return (T*)(((u64)hi << 32) | lo);
}
#define WC(p_) ((char*)(p_).X)
template <bool FAST = false>
__device__ __forceinline__ void wait_word(const MainP& p, const char* f, int want, unsigned code) {
    unsigned tries = 0;
#pragma unroll 1
    while (__any((int)__hip_atomic_load((const gu32*)f, RLX_AGENT) < want)) {
        if (++tries > kWaitCap) { __hip_atomic_store((gu32*)(p.ws + G_WS_CTL + G_C_ERR), code, RLX_AGENT); break; }
        if ((tries & 1023u) == 0u && __hip_atomic_load((const gu32*)(p.ws + G_WS_CTL + G_C_ERR), RLX_AGENT) != 0xffffffffu) break;
        if (FAST) __builtin_amdgcn_s_sleep(1); else __builtin_amdgcn_s_sleep(10);
    }
}

__device__ __forceinline__ int skew(int s, int id) { return (s ^ (G_SKM * id)) & 63; }
__device__ __forceinline__ unsigned um_base(int s, unsigned nm) { return G_D_UM + (s / 3) * 0x200000 + (s % 3) * G_UMS + ((nm >> (2 + s / 3)) & 1) * 8192; }
typedef GAS unsigned long long gu64;
#define IST(k_, slot_)
__device__ __forceinline__ void init_done(char* ws, int slot, unsigned val) {
    const unsigned e = __hip_atomic_load((const gu32*)(ws + G_WS_EP), RLX_AGENT);
    __hip_atomic_store((gu64*)(ws + G_WS_DONE + slot * 64), ((u64)val << 32) | (e + 1u), RLX_AGENT);
}
__device__ __forceinline__ void init_wait(char* ws, int w, int lane) {
    if (w == 0) {
        const unsigned e = __hip_atomic_load((const gu32*)(ws + G_WS_EP), RLX_AGENT);
        for (unsigned tries = 0; tries < kWaitCap; ++tries) {
            const unsigned d0 = __hip_atomic_load((const gu32*)(ws + G_WS_DONE + lane * 64), RLX_AGENT), d1 = __hip_atomic_load((const gu32*)(ws + G_WS_DONE + 2048 + lane * 64), RLX_AGENT);
            if (!__any((d0 != e + 1u) | (d1 != e + 1u))) break;
            __builtin_amdgcn_s_sleep(8);
        }
    }
    __syncthreads();
    if (w == 0 && lane == 0) __hip_atomic_fetch_add((gu32*)(ws + G_WS_CTL + G_C_ARR), 1u, RLX_AGENT);
}
__device__ __forceinline__ void fill_block(char* ws, int b, int tid, const u32x4& v0, const u32x4& v1) {
    const unsigned o = b * 8192 + tid * 16, o2 = o + 4096;
    asm volatile("global_store_dwordx4 %0, %1, %2 sc1\n\ts_nop 1" :: "v"(o), "v"(v0), "s"(ws) : "memory");
    if (b >= 256 || (b & 6) != 6 || tid < 64)
        asm volatile("global_store_dwordx4 %0, %1, %2 sc1\n\ts_nop 1" :: "v"(o2), "v"(v1), "s"(ws) : "memory");
}
__device__ __forceinline__ void init_fill(char* ws, const u16* mem0, int f, int tid) {
    const u32x4 sent = {0xffffffffu, 0xffffffffu, 0xffffffffu, 0xffffffffu};
    u32x4 v0 = sent, v1 = sent;
    if (f < 32 && (f & 4)) {
#pragma unroll
        for (int k = 0; k < 2; ++k) {
            const int q = tid + 256 * k + ((f & 2) ? 256 : 0), m = q & 15;
            if (!(f & 2) || (q >= 512 && q < 576)) {
                const unsigned* src = (const unsigned*)(mem0 + (2 * (m >> 2) + (m & 1)) * DM + 16 * (q >> 4) + (m & 2));
                const u32x4 o = {min(src[0], 0xfffffffeu), min(src[2], 0xfffffffeu), min(src[4], 0xfffffffeu), min(src[6], 0xfffffffeu)};
                if (k) v1 = o; else v0 = o;
            }
        }
    }
#pragma unroll 1
    for (int q = f; q < (G_G2 ? 216 : 184); q += 48) {
        if (q >= 136 && q < 152 && (tid & 127)) { v0 = u32x4{0u, 0u, 0u, 0u}; v1 = v0; }
        fill_block(ws, q < 64 ? q : q < 136 ? q - 64 + G_D_UM / 8192 : q < 168 ? 256 + ((q - 136) >> 2) * 8 + (q & 3) : q < 178 ? q - 168 + G_WS_CTL / 8192 : q < 184 ? q - 178 + G_WS_FIX / 8192
                                      : 260 + ((q - 184) >> 2) * 8 + (q & 3), tid, v0, v1);
        v0 = sent; v1 = sent;
    }
    const unsigned e = __hip_atomic_load((const gu32*)(ws + G_WS_EP), RLX_AGENT);
    asm volatile("s_waitcnt vmcnt(0)" :: "v"(e) : "memory");
    __syncthreads();
    if (tid == 0) __hip_atomic_store((gu64*)(ws + G_WS_DONE + (48 + f) * 64), (u64)(e + 1u), RLX_AGENT);
    if (tid == 0) IST(0, 48 + f);
}
__device__ __forceinline__ void init_cal(char* ws, int id, int tid, int n) {
    if (tid < 64) {
        const int pg = id >> 1;
        char* a = ws + (pg < 2 ? pg * 0x200000u : G_D_UM + (pg - 2) * 0x200000u) + G_PROBE + (id & 1) * 8192;
        const u32x4 z = {0u, 0u, 0u, 0u};
        asm volatile("global_store_dwordx4 %0, %1, off sc1\n\tglobal_store_dwordx4 %0, %1, off offset:1024 sc1\n\ts_waitcnt vmcnt(0)" :: "v"(a + tid * 16), "v"(z) : "memory");
        if (tid == 0) {
            unsigned best = 0xffffu;
            const unsigned ts1 = (unsigned)__builtin_amdgcn_s_memrealtime() << 4;
#pragma unroll 1
            for (int it = 0; it < n; ++it) {
                const unsigned c0 = (unsigned)__builtin_amdgcn_s_memtime();
                const unsigned v = __hip_atomic_load((const gu32*)(a + it * 128), RLX_AGENT);
                asm volatile("s_waitcnt vmcnt(0)" :: "v"(v) : "memory");
                best = min(best, (unsigned)__builtin_amdgcn_s_memtime() - c0);
            }
            init_done(ws, id, best);
            IST(0, id);
        }
    }
    __syncthreads();
}
__device__ __forceinline__ unsigned near_mask(char* ws, int lane) {
    const gu32* q = (const gu32*)(ws + G_WS_DONE + (lane & 31) * 128 + 4);
    return (unsigned)__ballot(__hip_atomic_load(q + 16, RLX_AGENT) < __hip_atomic_load(q, RLX_AGENT));
}

#define TST(slot_, i_)
#define PIN8(a) asm volatile("" : "+v"(a[0]), "+v"(a[1]), "+v"(a[2]), "+v"(a[3]), "+v"(a[4]), "+v"(a[5]), "+v"(a[6]), "+v"(a[7]))
struct AOps { u32x4 q[4], k[8]; int fl; };
struct APtr { const char* q; const char* k; const char* m; unsigned voff, vkm, voffm, vfl; };
__device__ __forceinline__ void attn_batch(const MainP& p, AOps& o, const APtr& a) {
    const char* q2 = a.q + 4096; const char* k2 = a.k + 4096;
    WLD16(o.q[0], a.voff, a.q, 0); WLD16(o.q[1], a.voff, a.q, 2048); WLD16(o.q[2], a.voff, q2, 0); WLD16(o.q[3], a.voff, q2, 2048);
    WLD16X4(o.k[0], o.k[1], o.k[2], o.k[3], a.voff, a.k); WLD16X4(o.k[4], o.k[5], o.k[6], o.k[7], a.voff, k2);
    asm volatile("global_load_dword %0, %1, %2 sc1" : "=v"(o.fl) : "v"(a.vfl), "s"(WC(p)) : "memory");
}
__device__ __forceinline__ void attn_pin(AOps& o) {
    asm volatile("" : "+v"(o.fl), "+v"(o.q[0]), "+v"(o.q[1]), "+v"(o.q[2]), "+v"(o.q[3])); PIN8(o.k);
}
static constexpr int NFINE = 6;
static constexpr int NG1 = 64 - NFINE;
__device__ __forceinline__ void score_round4(float* r, const f32x4& x) {
    asm volatile("v_cvt_pk_bf16_f32 %0, 0, %4\n\tv_cvt_pk_bf16_f32 %1, 0, %5\n\tv_cvt_pk_bf16_f32 %2, 0, %6\n\tv_cvt_pk_bf16_f32 %3, 0, %7\n\t"
                 "v_mul_f32 %0, 0x3db504f3, %0\n\tv_mul_f32 %1, 0x3db504f3, %1\n\tv_mul_f32 %2, 0x3db504f3, %2\n\tv_mul_f32 %3, 0x3db504f3, %3\n\t"
                 "v_cvt_pk_bf16_f32 %0, 0, %0\n\tv_cvt_pk_bf16_f32 %1, 0, %1\n\tv_cvt_pk_bf16_f32 %2, 0, %2\n\tv_cvt_pk_bf16_f32 %3, 0, %3"
                 : "=&v"(r[0]), "=&v"(r[1]), "=&v"(r[2]), "=&v"(r[3]) : "v"(x[0]), "v"(x[1]), "v"(x[2]), "v"(x[3]));
}
__device__ __forceinline__ float vmax3(float a, float b, float c) { float r; asm("v_max3_f32 %0, %1, %2, %3" : "=v"(r) : "v"(a), "v"(b), "v"(c)); return r; }
template <int N>
__device__ __forceinline__ void attn_soft(float (&q)[12], u32x2& Pm, u32x4& Px) {
    float m = vmax3(q[0], q[1], q[2]);
#pragma unroll
    for (int i = 3; i < N; i += 2) m = vmax3(m, q[i], q[i + 1 < N ? i + 1 : i]);
    { auto a = __builtin_amdgcn_permlane16_swap(__float_as_uint(m), __float_as_uint(m), false, false); m = vmax3(m, __uint_as_float(a[0]), __uint_as_float(a[1])); }
    { auto a = __builtin_amdgcn_permlane32_swap(__float_as_uint(m), __float_as_uint(m), false, false); m = vmax3(m, __uint_as_float(a[0]), __uint_as_float(a[1])); }
    const float ml = -m * 1.4426950408889634f;
#pragma unroll
    for (int i = 0; i < N; ++i) q[i] = __builtin_amdgcn_exp2f(fmaf(q[i], 1.4426950408889634f, ml));
    float sum = 0.5f * ((q[0] + q[1]) + (q[2] + q[3])) + ((q[4] + q[5]) + (q[6] + q[7]));
    if (N > 8) sum += (q[8] + q[9]) + (q[10] + q[11]);
    sum = rowsum4(sum);
    const float inv = __builtin_amdgcn_rcpf(sum);
    Pm = u32x2{pk2bf(q[0] * inv, q[1] * inv), pk2bf(q[2] * inv, q[3] * inv)};
    Px = u32x4{pk2bf(q[4] * inv, q[5] * inv), pk2bf(q[6] * inv, q[7] * inv), N > 8 ? pk2bf(q[8] * inv, q[9] * inv) : 0u, N > 8 ? pk2bf(q[10] * inv, q[11] * inv) : 0u};
}
#define NASPLIT 12
#define HSL 1
#define NAG 2
static constexpr int NA = NASPLIT;
__device__ __forceinline__ int attn_next(int s) { return s < NA ? s + 2 : s + NAG; }
__device__ __forceinline__ unsigned attn_flag(int s, int hq) { return G_WS_CTL + G_C_PC + (s < NA ? s & 1 : (s - NA) % NAG) * 1024 + hq * 128; }
struct BOps { u32x4 q[8], k[8]; };
#define WLD16X4A(d0, d1, d2, d3, voff_, base_) asm volatile("s_nop 4\n\tglobal_load_dwordx4 %0, %4, %5 sc1\n\tglobal_load_dwordx4 %1, %4, %5 offset:1024 sc1\n\t" \
    "global_load_dwordx4 %2, %4, %5 offset:2048 sc1\n\tglobal_load_dwordx4 %3, %4, %5 offset:3072 sc1" \
    : "=&a"(d0), "=&a"(d1), "=&a"(d2), "=&a"(d3) : "v"(voff_), "s"(base_) : "memory")
#define PIN8A(a) asm volatile("" : "+a"(a[0]), "+a"(a[1]), "+a"(a[2]), "+a"(a[3]), "+a"(a[4]), "+a"(a[5]), "+a"(a[6]), "+a"(a[7]))
#define MFMA_AV0(acc, a_, b_) asm volatile("v_mfma_f32_16x16x32_bf16 %0, %1, %2, 0" : "=&v"(acc) : "a"(a_), "v"(b_))
#define MFMA_AV(acc, a_, b_) asm volatile("v_mfma_f32_16x16x32_bf16 %0, %1, %2, %0" : "+v"(acc) : "a"(a_), "v"(b_))
__device__ __forceinline__ void attn_batch8(const MainP& p, BOps& o, const APtr& a) {
    const char* q2 = a.q + 4096; const char* k2 = a.k + 4096;
    WLD16X4(o.q[0], o.q[1], o.q[2], o.q[3], a.voff, a.q); WLD16X4(o.q[4], o.q[5], o.q[6], o.q[7], a.voff, q2);
    WLD16X4A(o.k[0], o.k[1], o.k[2], o.k[3], a.voff, a.k); WLD16X4A(o.k[4], o.k[5], o.k[6], o.k[7], a.voff, k2);
}
__device__ __forceinline__ void attn_step(const MainP& p, BOps& o, BOps& n1, BOps& n2, APtr& a, int& kg, int& kk, int s, int h, int w, int lane) {
    const int g = lane >> 4, c = lane & 15;
    const float scale = 0.08838834764831845f;
    TST(s * 64 + h * 2, 0);
    const int s2 = min(attn_next(attn_next(s)), NSEG - 1);
    if (kg < s2 || kk < s) {
        VMC0();
        if (s < 50) wait_word(p, WC(p) + a.vfl, (lane & 1) ? s : s2, 13u); else wait_word<true>(p, WC(p) + a.vfl, (lane & 1) ? s : s2, 13u);
    }
    TST(s * 64 + h * 2, 1);
    a.q = p.ws + G_D_QX + (size_t)h * G_HS8 + skew(s2, h) * 8192; a.k = a.q + (G_D_KX - G_D_QX); a.m = p.ws + G_D_KM + (size_t)h * G_HS4 + skew(s, h) * 4096;
    __builtin_amdgcn_sched_barrier(0);
    u32x4 km[4];
    int fl;
    WLD16X4A(km[0], km[1], km[2], km[3], a.vkm, a.m);
    asm volatile("global_load_dword %0, %1, %2 sc1" : "=v"(fl) : "v"(a.vfl), "s"(WC(p)) : "memory");
    attn_batch8(p, n2, a);
    f32x4 y[5];
#pragma unroll
    for (int wc = 0; wc < 4; ++wc)
#pragma unroll
        for (int i = 0; i < 3; ++i) { if (wc == 0) MFMA_AV0(y[i], o.k[i >> 1], o.q[(i + 1) >> 1]); else MFMA_AV(y[i], o.k[wc * 2 + (i >> 1)], o.q[wc * 2 + ((i + 1) >> 1)]); }
    asm volatile("s_nop 7" : "+v"(y[0]), "+v"(y[1]), "+v"(y[2]));
    float q0[12], q1[12];
    score_round4(q0 + 4, y[0]); score_round4(q1 + 4, y[1]); score_round4(q1 + 8, y[2]);
#pragma unroll
    for (int i = 0; i < 4; ++i) {
        const bool vis = 4 * g + i <= c;
        q0[4 + i] = vis ? q0[4 + i] : -INFINITY; q1[8 + i] = vis ? q1[8 + i] : -INFINITY;
    }
    __builtin_amdgcn_sched_barrier(0);
    asm volatile("s_waitcnt vmcnt(16)\n\ts_barrier" : "+a"(km[0]), "+a"(km[1]), "+a"(km[2]), "+a"(km[3]), "+v"(fl) :: "memory");
    PIN8(n1.q); PIN8A(n1.k);
    { const unsigned fo = attn_flag(s, h >> 2); const int fv = s - 1;
      asm volatile("s_cmp_lg_u32 %3, 0\n\ts_cbranch_scc1 1f\n\tglobal_store_dword %0, %1, %2 sc1\n1:" :: "v"(fo), "v"(fv), "s"(WC(p)), "s"(w) : "scc", "memory"); }
#pragma unroll
    for (int wc = 0; wc < 4; ++wc)
#pragma unroll
        for (int i = 0; i < 2; ++i) { if (wc == 0) MFMA_AV0(y[3 + i], km[0], o.q[i]); else MFMA_AV(y[3 + i], km[wc], o.q[wc * 2 + i]); }
    asm volatile("s_nop 7" : "+v"(y[3]), "+v"(y[4]));
    kg = __builtin_amdgcn_readlane(fl, 0); kk = __builtin_amdgcn_readlane(fl, 1);
    score_round4(q0, y[3]); score_round4(q1, y[4]);
    u32x2 Pm0, Pm1; u32x4 Px0, Px1;
    attn_soft<8>(q0, Pm0, Px0); attn_soft<12>(q1, Pm1, Px1);
    char* pxb = p.ws + G_D_PX + (size_t)(s * 32 + h) * 2048;
    char* pmb = p.ws + G_D_PM + (size_t)s * 16384 + h * 256; char* pm2 = pmb + 8192;
    WST16(a.voff, Px0, pxb, 0); WST16(a.voff, Px1, pxb, 1024);
    asm volatile("global_store_dwordx2 %0, %1, %3 sc1\n\tglobal_store_dwordx2 %0, %2, %4 sc1" :: "v"(a.voffm), "v"(Pm0), "v"(Pm1), "s"(pmb), "s"(pm2) : "memory");
    { const unsigned fo = attn_flag(s, h >> 2);
      asm volatile("s_cmp_lt_u32 %4, 46\n\ts_cbranch_scc1 1f\n\ts_waitcnt vmcnt(0)\n\ts_barrier\n\ts_cmp_lg_u32 %3, 0\n\ts_cbranch_scc1 1f\n\tglobal_store_dword %0, %1, %2 sc1\n1:"
                   :: "v"(fo), "v"(s), "s"(WC(p)), "s"(w), "s"(s) : "scc", "memory"); }
    TST(s * 64 + h * 2, 2);
}
__device__ __forceinline__ void attn_role(const MainP& p, int s, int h, int w, int lane) {
    APtr a;
    a.q = p.ws + G_D_QX + (size_t)h * G_HS8 + skew(s, h) * 8192; a.k = a.q + (G_D_KX - G_D_QX);
    a.voff = lane * 16; a.vkm = (lane & 55) * 16; a.voffm = (lane & 15) * 16 + ((lane >> 4) & 1) * 8;
    a.vfl = G_WS_CTL + ((lane & 1) ? G_C_KM : G_C_GQ) + h * 128;
    BOps o0, o1, o2;
    wait_word(p, WC(p) + G_WS_CTL + G_C_GQ + h * 128, attn_next(s), 10u);
    attn_batch8(p, o0, a);
    a.q = p.ws + G_D_QX + (size_t)h * G_HS8 + skew(attn_next(s), h) * 8192; a.k = a.q + (G_D_KX - G_D_QX);
    attn_batch8(p, o1, a);
    VMC0();
    PIN8(o0.q); PIN8A(o0.k); PIN8(o1.q); PIN8A(o1.k);
    int kg = -1, kk = -1;
    static_assert(NA % 2 == 0, "ranges");
#pragma unroll 1
    for (;;) {
        attn_step(p, o0, o1, o2, a, kg, kk, s, h, w, lane);
        if ((s = attn_next(s)) >= NG1) break;
        attn_step(p, o1, o2, o0, a, kg, kk, s, h, w, lane);
        if ((s = attn_next(s)) >= NG1) break;
        attn_step(p, o2, o0, o1, a, kg, kk, s, h, w, lane);
        if ((s = attn_next(s)) >= NG1) break;
    }
    VMC0();
}
__device__ __forceinline__ unsigned umax4(const u32x4& v) { return max(max(v[0], v[1]), max(v[2], v[3])); }
__device__ __forceinline__ bool give_up(const MainP& p, unsigned& tries, unsigned code) {
    if (++tries <= kWaitCap / 4) return false;
    __hip_atomic_store((gu32*)(p.ws + G_WS_CTL + G_C_ERR), code, RLX_AGENT);
    return true;
}
__device__ __forceinline__ void task_attn_tail(const MainP& p, int s, int h, int w, int lane) {
    const int g = lane >> 4, c = lane & 15, qt = w & 1;
    const float scale = 0.08838834764831845f;
    APtr a;
    a.q = p.ws + G_D_QX + (size_t)h * G_HS8 + skew(s, h) * 8192 + qt * 1024; a.k = p.ws + G_D_KX + (size_t)h * G_HS8 + skew(s, h) * 8192; a.m = p.ws + G_D_KM + (size_t)h * G_HS4 + skew(s, h) * 4096;
    a.voff = lane * 16; a.vkm = (lane & 55) * 16; a.voffm = c * 16 + (g & 1) * 8; a.vfl = G_WS_CTL;
    AOps o;
    wait_word(p, WC(p) + G_WS_CTL + G_C_GQ + h * 128, s, 14u);
    attn_batch(p, o, a);
    VMC0();
    attn_pin(o);
    float q[12];
    u32x4 km[4];
#pragma unroll
    for (int kt = 1; kt >= -1; --kt) {
        if (kt < 0) {
            TST(s * 64 + h * 2 + qt, 0);
            wait_word(p, WC(p) + G_WS_CTL + G_C_KM + h * 128, s - 2, 15u);
            if (FLG(p.prm[5]) & 2) wait_word<true>(p, WC(p) + G_WS_CTL + G_C_KM + h * 128, s - 1, 15u);
            for (int i = 0; i < ((FLH(p.prm[7]) >> 14) & 15); ++i) __builtin_amdgcn_s_sleep(1);
            for (unsigned tries = 0;;) {
                WLD16X4(km[0], km[1], km[2], km[3], a.vkm, a.m);
                asm volatile("s_waitcnt vmcnt(0)" : "+v"(km[0]), "+v"(km[1]), "+v"(km[2]), "+v"(km[3]) :: "memory");
                if (!__any(max(max(umax4(km[0]), umax4(km[1])), max(umax4(km[2]), umax4(km[3]))) == 0xffffffffu) || give_up(p, tries, 16u)) break;
            }
            TST(s * 64 + h * 2 + qt, 1);
        }
        f32x4 y = {0.f, 0.f, 0.f, 0.f};
#pragma unroll
        for (int wc = 0; wc < 4; ++wc) y = MFMA32(kt < 0 ? km[wc] : o.k[wc * 2 + (kt < 0 ? 0 : kt)], o.q[wc], y);
#pragma unroll
        for (int i = 0; i < 4; ++i) q[4 + 4 * kt + i] = (16 * kt + 4 * g + i <= 16 * qt + c) ? bfr(bfr(y[i]) * scale) : -INFINITY;
    }
    u32x2 Pm; u32x4 Px;
    attn_soft<12>(q, Pm, Px);
    char* pxb = p.ws + G_D_PX + (size_t)(s * 32 + h) * 2048 + qt * 1024;
    char* pmb = p.ws + G_D_PM + (size_t)s * 16384 + h * 256 + qt * 8192;
    WST16(a.voff, Px, pxb, 0);
    asm volatile("global_store_dwordx2 %0, %1, %2 sc1" :: "v"(a.voffm), "v"(Pm), "s"(pmb) : "memory");
    TST(s * 64 + h * 2 + qt, 2);
}

#define RLD(d_, voff_, base_, off_) asm volatile("global_load_dwordx4 %0, %1, %2 offset:%3 sc1" : "=v"(d_) : "v"(voff_), "s"(base_), "i"(off_) : "memory")
#define RLDA(d_, voff_, base_, off_) asm volatile("global_load_dwordx4 %0, %1, %2 offset:%3 sc1" : "=a"(d_) : "v"(voff_), "s"(base_), "i"(off_) : "memory")
#define MFMA_ACC_A(acc, a_, b_) asm volatile("v_mfma_f32_16x16x32_bf16 %0, %1, %2, %0" : "+v"(acc) : "a"(a_), "v"(b_))
#define VMW(n_) asm volatile("s_waitcnt vmcnt(%0)" :: "i"(n_) : "memory")
template <int NT, int DEPTH, bool TAIL, int NM = 2>
__device__ __forceinline__ void task_out(const MainP& p, char* smem, int s, int ct0, int w, int lane, int m0 = 0) {
    const int g = lane >> 4, c = lane & 15;
    const unsigned voff = lane * 16;
    f32x4 acc[NT][2];
#pragma unroll
    for (int t = 0; t < NT; ++t) { acc[t][0] = f32x4{0.f, 0.f, 0.f, 0.f}; acc[t][1] = acc[t][0]; asm volatile("" : "+v"(acc[t][0]), "+v"(acc[t][1])); }
    const char* ub[NT];
#pragma unroll
    for (int t = 0; t < NT; ++t) ub[t] = uni(p.ws + G_D_UX + (size_t)(ct0 + t) * G_UXS + skew(s, ct0 + t) * 32768 + w * 8192);
    const char* pb = uni(p.ws + G_D_PX + (size_t)(s * 32 + w * 8) * 2048 + m0 * 1024);
    const char* um = uni(p.ws + um_base(s, p.prm[0]) + ct0 * 16384);
    unsigned vu[NT];
#pragma unroll
    for (int t = 0; t < NT; ++t) { vu[t] = t * 16384 + w * 2048 + voff; asm volatile("" : "+v"(vu[t])); }
    const char* pm = uni(p.ws + G_D_PM + (size_t)s * 16384 + w * 2048 + m0 * 8192);
    constexpr int NR = DEPTH + 1 > 10 ? 10 : DEPTH + 1;
    u32x4 a[NR][NT], b[10][2];
    unsigned vb = voff, va = voff;
    TST(4096 + (s * 36 + ct0) * 4 + w, 0);
    const int f0 = (int)__hip_atomic_load((const gu32*)(WC(p) + G_WS_CTL + (lane < 8 ? G_C_UM + (w * 8 + lane) * 128 : lane < 16 ? G_C_GV + ((s < (int)G_S1 ? s & 1 : 0) * 32 + w * 8 + lane - 8) * 128
                                                                                                                                : attn_flag(s, w * 2 + (lane & 1)) - G_WS_CTL)), RLX_AGENT);
    const bool early = !TAIL && !__any(f0 < s);
    if (!early) wait_word(p, WC(p) + G_WS_CTL + (lane < 8 ? G_C_GV + ((s < (int)G_S1 ? s & 1 : 0) * 32 + w * 8 + lane) * 128 : G_C_UM + w * 8 * 128), lane < 8 ? s : s - 3, 21u);
#define ALOAD(q) do { if ((q) < 8) { _Pragma("unroll") for (int t = 0; t < NT; ++t) RLDA(a[(q) % NR][t], va, ub[t], 0);  \
                                     asm volatile("v_add_u32 %0, 0x400, %0" : "+v"(va)); }                                            \
                      else { _Pragma("unroll") for (int t = 0; t < NT; ++t) RLDA(a[(q) % NR][t], vu[t], um, ((q) & 1) * 1024); } } while (0)
    constexpr int D0 = DEPTH < 8 ? DEPTH : 8;
    asm volatile("s_nop 4");
#pragma unroll
    for (int q = 0; q < D0; ++q) ALOAD(q);
    asm volatile("s_cmp_lg_u32 %0, 0\n\ts_cbranch_scc1 1f\n\ts_waitcnt vmcnt(0)\n1:\n\ts_waitcnt vmcnt(30)" :: "s"(__builtin_amdgcn_readfirstlane((int)early)) : "scc", "memory");
    unsigned tries = 0;
    if (TAIL) {
        const char* sp = pm + (lane & 7) * 256 + ((lane >> 3) & 1) * 8192;
        const int dl = (FLG(p.prm[5]) >> 26) & 31;
        if (dl) {
            wait_word<true>(p, WC(p) + G_WS_CTL + G_C_KM + ((ct0 >> 1) + w) * 128, s - 1, 25u);
            for (int i = 0; i < dl + (s < 63 ? 5 : 0); ++i) __builtin_amdgcn_s_sleep(2);
        } else
        while (__any(__hip_atomic_load((const gu32*)sp, RLX_AGENT) == 0xffffffffu) && !give_up(p, tries, 23u)) __builtin_amdgcn_s_sleep(1);
    } else if (!early) wait_word(p, WC(p) + G_WS_CTL + (lane < 8 ? G_C_UM + (w * 8 + lane) * 128 : attn_flag(s, w * 2 + (lane & 1)) - G_WS_CTL), s, 22u);
    if (!early) TST(4096 + (s * 36 + ct0) * 4 + w, 1);
    for (;;) {
    asm volatile("s_nop 4");
#pragma unroll
    for (int q = 0; q < 8; ++q) { RLD(b[q][0], vb, pb, 0); if (NM == 2) RLD(b[q][1], vb, pb, 1024); asm volatile("v_add_u32 %0, 0x800, %0" : "+v"(vb)); }
#pragma unroll
    for (int q = 8; q < 10; ++q) { RLD(b[q][0], voff, pm, (q & 1) * 1024); if (NM == 2) RLD(b[q][1], voff, pm + 8192, (q & 1) * 1024); }
#pragma unroll
    for (int q = D0; q < NR; ++q) ALOAD(q);
#pragma unroll
    for (int q = 0; q < 10; ++q) {
        const int issuedA = (NR + q < 10 ? NR + q : 10) - D0;
        if (q < D0) VMW(NM * (9 - q) + issuedA * NT); else VMW((issuedA - (q - D0 + 1)) * NT);
#pragma unroll
        for (int t = 0; t < NT; ++t) { MFMA_ACC_A(acc[t][0], a[q % NR][t], b[q][0]); if (NM == 2) MFMA_ACC_A(acc[t][1], a[q % NR][t], b[q][1]); }
        if (NR + q < 10) ALOAD(NR + q);
    }
    asm volatile("s_nop 7" ::: "memory");
#pragma unroll
    for (int t = 0; t < NT; ++t) asm volatile("" : "+v"(acc[t][0]), "+v"(acc[t][1]));
    if (!TAIL) break;
    f32x4 chk = acc[0][0];
#pragma unroll
    for (int t = 0; t < NT; ++t) chk += acc[t][1] + acc[t][0];
    const float ck = (chk[0] + chk[1]) + (chk[2] + chk[3]);
    if (!__any(ck != ck)) break;
    unsigned mx = 0;
#pragma unroll
    for (int q = 0; q < 10; ++q) { asm volatile("" : "+v"(b[q][0])); mx = max(mx, umax4(b[q][0])); if (NM == 2) { asm volatile("" : "+v"(b[q][1])); mx = max(mx, umax4(b[q][1])); } }
#pragma unroll
    for (int t = 0; t < NT; ++t) { asm volatile("" : "+a"(a[8 % NR][t]), "+a"(a[9 % NR][t])); mx = max(mx, max(umax4(a[8 % NR][t]), umax4(a[9 % NR][t]))); }
    if (!__any(mx == 0xffffffffu) || give_up(p, tries, 24u)) break;
    vb = voff;
#pragma unroll
    for (int t = 0; t < NT; ++t) { acc[t][0] = f32x4{0.f, 0.f, 0.f, 0.f}; acc[t][1] = acc[t][0]; asm volatile("" : "+v"(acc[t][0]), "+v"(acc[t][1])); }
    }
    TST(4096 + (s * 36 + ct0) * 4 + w, 2);
    f32x4* red = (f32x4*)smem;
#pragma unroll
    for (int t = 0; t < NT; ++t) { red[((w * NT + t) * 2 + 0) * 64 + lane] = acc[t][0]; if (NM == 2) red[((w * NT + t) * 2 + 1) * 64 + lane] = acc[t][1]; }
    __syncthreads();
    const int row = lane >> 2, pc = lane & 3;
#pragma unroll
    for (int it = 0; it < NT / 2 * NM; ++it)
        if ((it & 3) == w) {
            const int m = it % NM, t = 2 * (it / NM) + (pc >> 1);
            const f32x4* r0 = red + (t * 2 + m) * 64 + (pc & 1) * 32 + row;
            const f32x4 v0 = (r0[0] + r0[NT * 128]) + (r0[NT * 256] + r0[NT * 384]), v1 = (r0[16] + r0[NT * 128 + 16]) + (r0[NT * 256 + 16] + r0[NT * 384 + 16]);
            u16* o = p.out + (size_t)(32 * s + 16 * (m + m0) + row) * OUTW + 16 * ct0 + 32 * (it / NM) + 8 * pc;
            if (TAIL || s >= OWT) OST16W(o, pack8(v0, v1)); else OST16(o, pack8(v0, v1));
        }
}

__device__ __forceinline__ int index_of_pos(int pv) { return min(min(pv, 128 + (pv >> 1)), min(256 + (pv >> 2), 384 + (pv >> 3))); }
static constexpr int KROW = G_KROW;
static constexpr int KARR = 80 * KROW;
static constexpr int UCS = 2208;
struct BCh {
    u16 uc[4][UCS];
    f32x4 cst[NST];
    unsigned Kd[648];
    u16 Kh[KARR];
    u16 Kl[KARR];
};
static_assert(4 * sizeof(BCh) <= LDS_TOTAL - 32, "LDS");
__device__ __forceinline__ unsigned prm(unsigned hi, unsigned lo, unsigned sel) { return __builtin_amdgcn_perm(hi, lo, sel); }
#define BST(i) asm volatile("" ::: "memory")
#define UT_LOAD(p_) __builtin_nontemporal_load(p_)
template <class F>
__device__ __forceinline__ void task_ut(const MainP& p, char* smem, int rb, int w, int lane, F more) {
    const int tid = w * 64 + lane, g = lane >> 4, c = lane & 15;
    { char* ws = p.ws; if (tid == 0) IST(4, rb); }
    const u32x4* src = (const u32x4*)(p.U + (size_t)rb * 64 * DM);
    u32x4 v[18];
#pragma unroll
    for (int i = 0; i < 18; ++i) v[i] = UT_LOAD(src + tid + 256 * i);
#pragma unroll
    for (int i = 0; i < 18; ++i) { const int ch = tid + 256 * i, row = ch / 72; *(u32x4*)(smem + row * 1184 + (ch - 72 * row) * 16) = v[i]; }
    __syncthreads();
    { char* ws = p.ws; if (tid == 0) IST(3, rb); }
    const unsigned ad = (unsigned)(uintptr_t)smem + (16 * g + (c >> 2)) * 1184 + (w * 144 + 4 * (c & 3)) * 2;
    char* tw = smem + 77824 + w * 20736;
#pragma unroll
    for (int i = 0; i < 9; ++i) {
        u32x2 x0, x1, x2, x3;
        asm volatile("ds_read_b64_tr_b16 %0, %4 offset:%5\n\tds_read_b64_tr_b16 %1, %4 offset:%6\n\tds_read_b64_tr_b16 %2, %4 offset:%7\n\tds_read_b64_tr_b16 %3, %4 offset:%8\n\ts_waitcnt lgkmcnt(0)"
                     : "=&v"(x0), "=&v"(x1), "=&v"(x2), "=&v"(x3) : "v"(ad), "i"(i * 32), "i"(4 * 1184 + i * 32), "i"(8 * 1184 + i * 32), "i"(12 * 1184 + i * 32) : "memory");
        u32x4* o = (u32x4*)(tw + (16 * i + c) * 144 + g * 32);
        o[0] = u32x4{x0[0], x0[1], x1[0], x1[1]}; o[1] = u32x4{x2[0], x2[1], x3[0], x3[1]};
    }
    asm volatile("" ::: "memory");
    const unsigned vo = (w * 144 + (lane >> 3)) * (SL * 2) + rb * 128 + (lane & 7) * 16;
    const char* dst = p.ws + G_D_UT;
#pragma unroll
    for (int i = 0; i < 18; ++i) {
        const u32x4 a = *(const u32x4*)(tw + (8 * i + (lane >> 3)) * 144 + (lane & 7) * 16);
        const unsigned vi = vo + i * 8 * (SL * 2);
        WST16(vi, a, dst, 0);
    }
    VMC0();
    init_wait(p.ws, w, lane);
    if (tid == 0) __hip_atomic_store((gu32*)(p.ws + G_WS_CTL + G_C_UT + rb * 128), 1u, RLX_AGENT);
    { char* ws = p.ws; if (tid == 0) IST(2, rb); }
    more();
    VMC0();
    __syncthreads();
    if (tid == 0) __hip_atomic_store((gu32*)(p.ws + G_WS_CTL + G_C_UT + rb * 128), 2u, RLX_AGENT);
}
struct KPar { u16 ldt, dsk, pa[2][4]; };
__device__ __forceinline__ KPar kgen_par(const MainP& p, int d, int lane) {
    KPar k;
    k.ldt = p.logdt[d]; k.dsk = p.Dskip[d];
#pragma unroll
    for (int q = 0; q < 2; ++q) { const int i = d * NST + lane + 64 * q; k.pa[q][0] = p.logA[i]; k.pa[q][1] = p.Aim[i]; k.pa[q][2] = p.Cre[i]; k.pa[q][3] = p.Cim[i]; }
    return k;
}
__device__ __forceinline__ void task_s4d(const MainP& p, BCh& L, int d, int lane, bool first, int& nb, KPar& kp, int& pend) {
    const int g = lane >> 4, c = lane & 15;
    BST(0);
    const float dsk = bf2f(kp.dsk);
    const auto& pa = kp.pa;
    const float dt = bfr(expf(bf2f(kp.ldt)));
#pragma unroll
    for (int q = 0; q < 2; ++q) {
        const float ar = -bfr(expf(bf2f(pa[q][0])));
        const float cr = bf2f(pa[q][2]) * dt, ci = bf2f(pa[q][3]) * dt;
        L.cst[lane + 64 * q] = f32x4{ar * dt * 1.4426950408889634f, bf2f(pa[q][1]) * dt * 0.15915494309189535f,
                                     __log2f(hypotf(cr, ci)), atan2f(ci, cr) * 0.15915494309189535f};
    }
    const char* ut = uni(p.ws + G_D_UT + (size_t)d * 4096);
    const unsigned voff = lane * 16;
    u32x4 uu[4];
    unsigned tk = nb >= 0 ? 1u : 0u;
    __builtin_amdgcn_sched_barrier(0);
    asm volatile("s_mov_b64 exec, 1\n\tglobal_atomic_add %0, %1, %0, %2 sc0\n\ts_mov_b64 exec, -1" : "+v"(tk) : "v"(G_WS_CTL + G_C_TASK + 128), "s"(WC(p)) : "memory");
    const char* ue = uni(first ? (const char*)p.logA : ut);
    WLD16X4A(uu[0], uu[1], uu[2], uu[3], voff, ue);
    BST(1);
    {
        f32x4 acc[3];
#pragma unroll
        for (int v = 0; v < 3; ++v) acc[v] = f32x4{0.f, 0.f, 0.f, 0.f};
        auto base_of = [](int v, int r) { return v == 0 ? (r & 7) + 128 * (r >> 3) : v == 1 ? (r < 8 ? 256 + 2 * (r & 3) + 128 * (r >> 2) : 512 + 4 * (r & 1) + 128 * ((r - 8) >> 1))
                                                                                           : 1024 + 128 * min(r, 8); };
        const float b0 = (float)base_of(0, c), b1 = (float)base_of(1, c), b2 = (float)base_of(2, c), off = (float)(8 * c);
#define KG_POLAR(h_, l_, lg_, f_, a_, b_) do { const float m_ = __builtin_amdgcn_exp2f(fmaf(cc[0], b_, lg_)), p_ = __builtin_amdgcn_fractf(fmaf(a_, b_, f_)); \
            const float re_ = m_ * __builtin_amdgcn_cosf(p_), im_ = m_ * __builtin_amdgcn_sinf(p_);                                                            \
            h_ = pk2bf(re_, im_); l_ = pk2bf(re_ - lo2f(h_), im_ - hi2f(h_)); } while (0)
#pragma unroll 1
        for (int t = 0; t < NST / 16; ++t) {
            u32x4 fh, fl, ah[3], al[3];
#pragma unroll
            for (int j = 0; j < 4; ++j) {
                const f32x4 cc = L.cst[16 * t + 4 * g + j];
                KG_POLAR(fh[j], fl[j], 0.f, 0.f, -cc[1], off);
                KG_POLAR(ah[0][j], al[0][j], cc[2], cc[3], cc[1], b0); KG_POLAR(ah[1][j], al[1][j], cc[2], cc[3], cc[1], b1); KG_POLAR(ah[2][j], al[2][j], cc[2], cc[3], cc[1], b2);
            }
#pragma unroll
            for (int v = 0; v < 3; ++v) { acc[v] = MFMA32(al[v], fh, acc[v]); acc[v] = MFMA32(ah[v], fl, acc[v]); acc[v] = MFMA32(ah[v], fh, acc[v]); }
        }
        BST(2);
        auto split = [](float k) { const unsigned hi = pk2bf(k, 0.f) & 0xffffu; return hi | (pk2bf(0.f, k - __uint_as_float(hi << 16)) & 0xffff0000u); };
#pragma unroll
        for (int v = 0; v < 3; ++v)
#pragma unroll
            for (int i = 0; i < 4; ++i) {
                const int pos = base_of(v, 4 * g + i) + 8 * c;
                if (pos <= 2048) L.Kd[index_of_pos(pos)] = split(2.0f * acc[v][i]);
            }
    }
    __builtin_amdgcn_sched_barrier(0);
    asm volatile("s_waitcnt vmcnt(0)" : "+v"(tk), "+a"(uu[0]), "+a"(uu[1]), "+a"(uu[2]), "+a"(uu[3]) :: "memory");
    if (first) {
        wait_word(p, p.ws + G_WS_CTL + G_C_UT + (lane & 31) * 128, 1, 30u);
        WLD16X4A(uu[0], uu[1], uu[2], uu[3], voff, ut);
        asm volatile("s_waitcnt vmcnt(0)" : "+a"(uu[0]), "+a"(uu[1]), "+a"(uu[2]), "+a"(uu[3]) :: "memory");
    }
    if (pend >= 0 && lane == 0) __hip_atomic_fetch_add((gu32*)(WC(p) + G_WS_CTL + G_C_YC + pend * 128), 1u, RLX_AGENT);
    nb = __builtin_amdgcn_readfirstlane(tk) + 1;
    const u32x4 z4 = {0u, 0u, 0u, 0u};
    *(u32x4*)&L.Kh[8 * lane] = z4; *(u32x4*)&L.Kl[8 * lane] = z4;
    *(u32x4*)&L.Kh[512 + 8 * (lane & 31)] = z4; *(u32x4*)&L.Kl[512 + 8 * (lane & 31)] = z4;
    *(u32x4*)&L.uc[0][64 + SL] = z4;
    *(u32x4*)&L.uc[(lane >> 3) & 3][8 * (lane & 7)] = z4;
    asm volatile("" ::: "memory");
#pragma unroll
    for (int q = 0; q < 4; ++q) {
        const int e = lane + 64 * q, at = (16 + (e >> 2)) * KROW + 24 - 8 * (e & 3);
        unsigned k[8];
        const unsigned* kd = L.Kd + index_of_pos(8 * e);
        if (q == 0) {
            const bool two = lane >= 32;
#pragma unroll
            for (int r = 0; r < 8; ++r) k[7 - r] = kd[two ? (0x43222100 >> (4 * r)) & 7 : r];
        } else if (q == 1) {
            const unsigned k0 = kd[0], k1 = kd[1], k2 = kd[2];
            k[7] = k[6] = k[5] = k0; k[4] = k[3] = k[2] = k1; k[1] = k[0] = k2;
        } else {
            const unsigned k0 = kd[0], k1 = kd[1];
            k[7] = k[6] = k[5] = k[4] = k0; k[3] = (e & 1) ? k1 : k0; k[2] = k[1] = k[0] = k1;
        }
        u32x4 kh, kl;
#pragma unroll
        for (int j = 0; j < 4; ++j) { kh[j] = prm(k[2 * j + 1], k[2 * j], 0x05040100u); kl[j] = prm(k[2 * j + 1], k[2 * j], 0x07060302u); }
        *(u32x4*)&L.Kh[at] = kh;
        if (G_NHL == 2) *(u32x4*)&L.Kl[at] = kl;
    }
    BST(3);
#pragma unroll
    for (int q = 0; q < 4; ++q) *(u32x4*)&L.uc[0][64 + 8 * (lane + 64 * q)] = uu[q];
    asm volatile("" ::: "memory");
#pragma unroll
    for (int q = 0; q < 4; ++q) {
        const int e = lane + 64 * q;
        const int ue = 56 + 8 * e;
        const u32x4 a = *(const u32x4*)&L.uc[0][ue];
        const u32x2 b = *(const u32x2*)&L.uc[0][ue + 8];
        const unsigned s0 = __builtin_amdgcn_alignbit(a[1], a[0], 16), s1 = __builtin_amdgcn_alignbit(a[2], a[1], 16), s2 = __builtin_amdgcn_alignbit(a[3], a[2], 16),
                       s3 = __builtin_amdgcn_alignbit(b[0], a[3], 16), s4 = __builtin_amdgcn_alignbit(b[1], b[0], 16);
        *(u32x4*)&L.uc[1][ue] = u32x4{s0, s1, s2, s3};
        *(u32x4*)&L.uc[2][ue] = u32x4{a[1], a[2], a[3], b[0]};
        *(u32x4*)&L.uc[3][ue] = u32x4{s1, s2, s3, s4};
    }
    if (lane == 0) {
        const u32x4 a = *(const u32x4*)&L.uc[0][56 + SL];
        const unsigned s0 = __builtin_amdgcn_alignbit(a[1], a[0], 16), s1 = __builtin_amdgcn_alignbit(a[2], a[1], 16), s2 = __builtin_amdgcn_alignbit(a[3], a[2], 16), s3 = a[3] >> 16;
        *(u32x4*)&L.uc[1][56 + SL] = u32x4{s0, s1, s2, s3};
        *(u32x4*)&L.uc[2][56 + SL] = u32x4{a[1], a[2], a[3], 0u};
        *(u32x4*)&L.uc[3][56 + SL] = u32x4{s1, s2, s3, 0u};
    }
    kp = kgen_par(p, min(nb, DM - 1), lane);
    BST(4);
    {
        const int o = 33 + c + 8 * g;
        const unsigned ua = (unsigned)(uintptr_t)&L.uc[o & 3][o & ~3], ka = (unsigned)(uintptr_t)&L.Kh[(16 + c) * KROW + 8 * g];
        const f32x4* res = (const f32x4*)L.Kh + lane;
        asm volatile(ASM_CV :: "v"(ua), "v"(ka), "v"((unsigned)(uintptr_t)res) : CV_CLOB);
        asm volatile("" :: "v"(kp.ldt), "v"(kp.dsk), "v"(kp.pa[0][0]), "v"(kp.pa[0][1]), "v"(kp.pa[0][2]), "v"(kp.pa[0][3]), "v"(kp.pa[1][0]), "v"(kp.pa[1][1]), "v"(kp.pa[1][2]), "v"(kp.pa[1][3]));
        BST(5);
#pragma unroll
        for (int par = 0; par < 2; ++par)
#pragma unroll
            for (int t = 0; t < 4; ++t) {
                const int n = 16 * (par + 2 * (16 * t + c)) + 4 * g;
                const u32x2 uu = *(const u32x2*)&L.uc[0][64 + n];
                const f32x4 y4 = res[(par * 4 + t) * 64];
                const float r0 = bfr(y4[0]) + bfr(lo2f(uu[0]) * dsk), r1 = bfr(y4[1]) + bfr(hi2f(uu[0]) * dsk);
                const float r2 = bfr(y4[2]) + bfr(lo2f(uu[1]) * dsk), r3 = bfr(y4[3]) + bfr(hi2f(uu[1]) * dsk);
                *(u32x2*)&L.uc[1][n] = u32x2{pk2bf(r0, r1), pk2bf(r2, r3)};
            }
    }
    BST(6);
    char* yb = uni(p.ws + G_D_YB + (size_t)d * 4096);
#pragma unroll
    for (int q = 0; q < 4; ++q) { const u32x4 o = *(const u32x4*)&L.uc[1][8 * (lane + 64 * q)]; WST16(voff, o, yb, q * 1024); }
    pend = d >> 6;
    BST(7);
}
__device__ __forceinline__ void s4d_wave(const MainP& p, char* smem, int w, int lane, int quota) {
    int nb = 0, pend = -1;
    if (lane == 0) nb = (int)(__hip_atomic_fetch_add((gu32*)(WC(p) + G_WS_CTL + G_C_TASK + 128), 1u, RLX_AGENT) + 1u);
    nb = __builtin_amdgcn_readfirstlane(nb);
    KPar kp = kgen_par(p, min(nb, DM - 1), lane);
#pragma unroll 1
    for (int n = 0; n < quota; ++n) {
        const int d = nb;
        if (d >= DM) break;
        nb = n + 1 < quota ? 0 : -1;
        task_s4d(p, reinterpret_cast<BCh*>(smem)[w], d, lane, n == 0, nb, kp, pend);
    }
    VMC0();
    if (pend >= 0 && lane == 0) __hip_atomic_fetch_add((gu32*)(WC(p) + G_WS_CTL + G_C_YC + pend * 128), 1u, RLX_AGENT);
}
__device__ __forceinline__ void watch_role(const MainP& p, int lane) {
    const char* c = WC(p) + G_WS_CTL;
#pragma unroll 1
    for (int s = G_S0; s < NSEG; ++s) {
        const int n = min(s + 3, NSEG - 1);
        wait_word(p, c + G_C_GV + lane * 128, n - (n < (int)G_S1 ? (n ^ (lane >> 5)) & 1 : 0), 60u);
        wait_word(p, (lane < 32 ? c + G_C_GX : p.ws + G_WS_CTL + G_C_UT - 4096) + lane * 128, lane < 32 ? min(s + 2, NSEG - 1) : s < 40 ? -1 : 2, 61u);
        if (lane == 0) __hip_atomic_store((gu32*)(p.ws + G_WS_CTL + G_C_RDY + s * 128), 1u, RLX_AGENT);
    }
}
__device__ __forceinline__ void task_merge(const MainP& p, char* smem, int gr, int i, int w, int lane) {
    const int g = lane >> 4, c = lane & 15;
    wait_word(p, WC(p) + G_WS_CTL + G_C_YC + gr * 128, 63, 31u);
    const char* sb = uni(p.ws + G_D_YB + (size_t)gr * 262144 + i * 1024 + w * 256);
    const unsigned vt = g * 4096 + c * 16;
    u32x4 z[16];
#pragma unroll
    for (int j = 0; j < 16; ++j) { const char* sj = sb + j * 16384; WLD16(z[j], vt, sj, 0); }
    asm volatile("s_waitcnt vmcnt(0)" : "+v"(z[0]), "+v"(z[1]), "+v"(z[2]), "+v"(z[3]), "+v"(z[4]), "+v"(z[5]), "+v"(z[6]), "+v"(z[7]),
                 "+v"(z[8]), "+v"(z[9]), "+v"(z[10]), "+v"(z[11]), "+v"(z[12]), "+v"(z[13]), "+v"(z[14]), "+v"(z[15]) :: "memory");
    char* tw = smem + w * 17408;
#pragma unroll
    for (int j = 0; j < 16; ++j) *(u32x4*)(tw + (4 * j + g) * 272 + c * 16) = z[j];
    const unsigned ad = (unsigned)(uintptr_t)tw + (8 * g + (c >> 2)) * 272 + (c & 3) * 8;
    u16* orow = p.out + (size_t)(512 * i + 128 * w + c) * OUTW + DM + 64 * gr + 8 * g;
#pragma unroll
    for (int k = 0; k < 8; ++k) {
        u32x2 x0, x1, x2, x3;
        asm volatile("ds_read_b64_tr_b16 %0, %4 offset:%5\n\tds_read_b64_tr_b16 %1, %4 offset:%6\n\tds_read_b64_tr_b16 %2, %4 offset:%7\n\tds_read_b64_tr_b16 %3, %4 offset:%8\n\ts_waitcnt lgkmcnt(0)"
                     : "=&v"(x0), "=&v"(x1), "=&v"(x2), "=&v"(x3) : "v"(ad), "i"(k * 32), "i"(4 * 272 + k * 32), "i"(32 * 272 + k * 32), "i"(36 * 272 + k * 32) : "memory");
        OST16(orow + (size_t)16 * k * OUTW, (u32x4{x0[0], x0[1], x1[0], x1[1]}));
        OST16(orow + (size_t)16 * k * OUTW + 32, (u32x4{x2[0], x2[1], x3[0], x3[1]}));
    }
}

static constexpr int Q1 = NG1 * 6 + 36, Q2 = Q1 + (NFINE - 2) * 25;
static constexpr int NTASK = Q2 + 34 + 52;
__host__ __device__ __forceinline__ void task_of(int u, int m0, int& sg, int& r) {
    if (u < 6 * m0) { sg = u / 6; r = u % 6; }
    else if (u < 6 * m0 + 108) { sg = m0 + (u - 6 * m0) / 9; r = (u - 6 * m0) % 9; }
    else if (u < Q1) { sg = (u - 36) / 6; r = (u - 36) % 6; }
    else if (u < Q2) { sg = NG1 + (u - Q1) / 25; r = (u - Q1) % 25; }
    else { sg = u < Q2 + 34 ? 62 : 63; r = u < Q2 + 34 ? u - Q2 : u - Q2 - 34; }
}
__device__ __forceinline__ void worker_body(const MainP& p, char* smem, const int w, const int lane) {
    const int tid = w * 64 + lane;
    int* slot = (int*)(smem + LDS_TOTAL - 16);
    int nst = 0, nxt = -1;
    gu32* qa = (gu32*)(WC(p) + G_WS_CTL + G_C_TASK);
    const int m0 = (FLH(p.prm[7]) >> 4) & 63;
    for (;;) {
        if (tid == 0) {
            *slot = nxt >= 0 ? nxt : (int)(__hip_atomic_fetch_add(qa, 1u, RLX_AGENT) + 1u);
            nxt = -1;
        }
        __syncthreads();
        const int u = __builtin_amdgcn_readfirstlane(*slot);
        if (u >= NTASK) break;
        if (tid == 0 && u < Q1 - 140) nxt = (int)(__hip_atomic_fetch_add(qa, 1u, RLX_AGENT) + 1u);
        const bool last2 = u >= Q2;
        int sg, r;
        task_of(u, m0, sg, r);
        if (u < Q1) {
            if (r < 6) task_out<6, 6, false>(p, smem, sg, 6 * r, w, lane);
            else { const int m = 3 * (sg - m0) + r - 6; task_merge(p, smem, m >> 2, m & 3, w, lane); }
        }
        else if (r < 16) task_attn_tail(p, sg, 2 * r + (w >> 1), w, lane);
        else if (sg == 63) task_out<2, 9, true, 1>(p, smem, sg, (r - 16) & ~1, w, lane, r & 1);
        else if (last2) task_out<2, 9, true>(p, smem, sg, 2 * (r - 16), w, lane);
        else task_out<4, 9, true>(p, smem, sg, 4 * (r - 16), w, lane);
        if (last2) break;
        __syncthreads();
    }
}

__host__ __device__ constexpr unsigned phys(unsigned x) { return (x >> 13) * 16384 + (x & 8191); }
__host__ __device__ constexpr unsigned aMB(int f, int b) { return phys(f < 8 ? 16384 + f * 1024 + b : 28672 + b); }
__host__ __device__ constexpr unsigned aPX(int pr, int b) { return phys(pr * 1024 + b); }
__host__ __device__ constexpr unsigned aPM(int u, int b) { return phys(24576 + u * 1024 + b); }
static constexpr int L_KI = 8192, L_QI = 16384, L_VI = 147456;

__device__ __forceinline__ int lane_id() { int l; asm volatile("v_mbcnt_lo_u32_b32 %0, -1, 0\n\tv_mbcnt_hi_u32_b32 %0, -1, %0" : "=v"(l)); return l; }
typedef const __attribute__((address_space(4))) MainP* KArg;

__global__ __launch_bounds__(256, 1) void main_kernel(MainP p_) {
    __shared__ __attribute__((aligned(16))) char smem[LDS_TOTAL];
    KArg ka = (KArg)__builtin_amdgcn_kernarg_segment_ptr();
    static_assert(offsetof(MainP, W) == 0x28, "argument layout");
    asm volatile(ASM_EA :: "s"(ka), "s"(blockIdx.x), "v"(threadIdx.x) : "v1", "v2", "s40", "s41", "s42", "s43", "s44", "s45", "s46", "s47", "s48", "s49", "scc", "memory");
    asm volatile("" : "+s"(ka));
    const int b = blockIdx.x, x = b & 7, ix = b >> 3;
    const int w = __builtin_amdgcn_readfirstlane(threadIdx.x >> 6);
    const bool isr = x < 4 && ix >= 16;
    constexpr int NRF = 8 + 8 * G_GVN;
    if (__builtin_expect(ix < (x < 4 ? 16 : NRF), 1)) {
        const int h = (x & 3) * 8 + (ix & 7), id = h + (ix >> 4) * 32;
        const unsigned stb = 0;
#define UNR _Pragma("unroll")
#define CW(k, val) do { if ((k) < 16) ca[(k) & 15] = (unsigned)(val); else if ((k) < 32) cb[(k) & 15] = (unsigned)(val); else cl[((k) - 40) & 15] = (unsigned)(val); } while (0)
#define ROLEW(A_) asm volatile(A_ : "+{v[0:15]}"(ca), "+{v[16:31]}"(cb), "+{v[200:215]}"(cl) : "s"(ka), "s"(w), "s"(id), "s"(stb) : ASM_CLOBW)
#define PAD(n_) asm volatile("s_branch skip_" n_ "\npad_" n_ ":\n\ts_setpc_b64 s[46:47]\nskip_" n_ ":")
#define CW_ALL const int lane = lane_id(), g = lane >> 4, c = lane & 15, q = c >> 2; u32x16 ca = {}, cb = {}, cl = {};                                      \
        CW(0, lane * 16); CW(40, (4 * w + g) * 8192 + h * 256 + (c ^ (2 * g + 8 * (w >> 1))) * 16);                                                          \
        UNR for (int k = 0; k < 2; ++k) CW(41 + k, (8 * g + q) * 256 + ((4 * w + 2 * k + ((c >> 1) & 1)) ^ (2 * q + 8 * (g & 1))) * 16 + (c & 1) * 8)
#define CW_SRC(b_, key_) UNR for (int k = 0; k < 9; ++k) { const int pp = 64 * k + lane, r = pp / 72, pc = pp - 72 * r; CW(43 + k, (b_) + (8 * w + r) * 1152 + (pc ^ (key_)) * 16); }
#define UNIT(u_, key_) (((u_) >> 2) * 128 + (((u_) & 3) ^ (key_)) * 32 + (c & 3) * 8)
#define CW_NEAR CW(1, x * 65536 + aMB(2 * w, lane * 16)); CW(2, x * 65536 + aMB(8, w * 256 + lane * 4)); CW(3, 2 * w * 1024 + lane * 16);                            \
        UNR for (int i = 0; i < 6; ++i) CW(4 + i, ((2 * w + 2 + i) & 7) * 1024 + lane * 16);                                                                 \
        CW(10, (c & 2) ? 0xEEEE : 0x4444);                                                                                                                   \
        CW(20, x * 65536 + 22528 + (lane & 31) * 64); CW(21, lane < 32 ? (lane & 3) * 65536 + phys((lane >> 2) * 2048) + h * 64 : 32768 + h * 128);          \
        CW(22, lane * 4); CW(23, x * 65536 + aMB(2 * w + 1, lane * 16))
#define CW_FAR CW(0, c * 1152 + (g ^ (c >> 1)) * 16); CW(1, ca[0]); CW(2, ca[0] ^ 64); CW(3, w * 9216 + lane * 16); CW_SRC(0, (r >> 1) + 4 * (w & 1))                                   \
        CW(4, L_VI + w * 2048 + lane * 16); CW(5, L_VI + lane * 16); CW(6, lane * 4)
        if (x >= 4 && ix >= 8) {
            PAD("gv"); CW_ALL; CW_FAR;
            UNR for (int t = 0; t < 9; ++t) CW(8 + t, (9 * w + t) * G_UXS + skew(0, 9 * w + t) * 32768 + h * 1024 + lane * 16);
            UNR for (int k = 0; k < 2; ++k) CW(19 + k, (9 * w + 4 * k + g) * 16384 + h * 256 + c * 16);
            CW(21, (9 * w + 8) * 16384 + h * 256 + lane * 4);
            UNR for (int k = 0; k < 4; ++k) CW(52 + k, (4 * g + q) * 1152 + ((9 * w + k) >> 2) * 128 + ((2 * ((9 * w + k) & 3) + ((c >> 1) & 1)) ^ (2 * g + (q >> 1))) * 16 + (c & 1) * 8);
            ROLEW(ASM_GV);
        } else if (x >= 4) {
            PAD("gx"); CW_ALL; CW_FAR;
            CW(8, w * 2048 + lane * 16);
            ROLEW(ASM_GX);
        } else if (ix < 8) {
            PAD("qk"); CW_ALL; CW_NEAR;
            CW(12, L_QI + (16 * w + (c & 13)) * 32 + 8 * g + 4 * ((c >> 1) & 1));
            CW(13, L_QI + (8 * g + q) * 32 + (c & 3) * 8);
            CW(14, G_WS_CTL + G_C_KM + h * 128); CW(15, lane * 16 + 4096);
            CW(16, L_KI + (32 * w + c) * 16 + 4 * g);
            CW(17, L_KI + (4 * g + q) * 16 + (c & 1) * 8);
            const int mm = 8 * (w & 1) + 4 * (g >> 1) + (g & 1);
            CW(18, (x ^ 3) * 65536 + aPX(h >> 1, (h & 1) * 512 + (w & 1) * 256 + g * 64 + (c >> 3) * 32 + ((c >> 1) & 1) * 16 + (2 * (q & 1) + (c & 1)) * 4));
            CW(19, ((c & 1) + 2 * (c >> 3)) * 65536 + aPM(h >> 3, ((h >> 1) & 3) * 256 + (mm + 2 * ((c >> 1) & 1)) * 16 + (h & 1) * 8 + (q & 1) * 4));
            CW(24, G_WS_CTL + G_C_GX + h * 128);
            ROLEW(ASM_QK);
        } else {
            PAD("vv"); CW_ALL; CW_NEAR; CW_SRC(h * 147456, r & 4);
            const int tq = 9 * w + 2 * (g & 1) + (g >> 1);
            CW(14, tq * 16384 + h * 256 + c * 16);
            CW(15, (tq + 4) * 16384 + h * 256 + c * 16);
            CW(16, (9 * w + 8) * 16384 + h * 256 + c * 16 + (g & 1) * 8);
            CW(19, (c & 8) ? 0xEEEE : 0x4444);
            CW(12, L_QI + w * 576 + c * 16 + 4 * g);
            CW(13, L_QI + (8 * g + q) * 16 + (c & 1) * 8 + ((c >> 1) & 1) * 576);
            CW(25, L_QI + (8 * g + q) * 16 + (c & 1) * 8 + (1 - ((c >> 1) & 1)) * 576);
            UNR for (int k = 0; k < 4; ++k) CW(52 + k, ((k >> 1) ^ (q >> 1)) * 36864 + (4 * g + (q & 1)) * 1152 + UNIT(9 * w + 2 * (k & 1) + (q >> 1), 2 * (g & 1)));
            CW(24, (q >> 1) * 36864 + (4 * g + (q & 1)) * 1152 + UNIT(9 * w + 8, 2 * (g & 1)));
            ROLEW(ASM_V);
        }
    } else {
    if (isr && (ix < 25 || ix >= 29)) {
        const int id = x * 12 + (ix < 25 ? ix - 13 : ix - 29);
        init_cal(ka->ws, id, threadIdx.x, (FLG(ka->prm[5]) >> 16) & 15);
        if (FLG(ka->prm[5]) & 4) {
            const unsigned o = (id * 384 + threadIdx.x) * 128, o2 = o + (threadIdx.x & 128 ? 16384 : 32768);
            const char* a = (const char*)ka->X + o; const char* b2 = (const char*)ka->U + o;
            const char* c2 = (const char*)(w < 2 ? ka->X : ka->U) + o2;
            const char* ps = (const char*)(w == 0 ? ka->logA : w == 1 ? ka->Aim : w == 2 ? ka->Cre : ka->Cim) + id * 3072 + min((int)(threadIdx.x & 63), 23) * 128;
            asm volatile("s_mov_b32 m0, %4\n\ts_nop 0\n\tglobal_load_lds_dword %0, off\n\tglobal_load_lds_dword %1, off\n\tglobal_load_lds_dword %2, off\n\tglobal_load_lds_dword %3, off"
                         :: "v"(a), "v"(c2), "v"(b2), "v"(ps), "s"(163072) : "memory");
        }
    }
    if (x >= 4 ? ix >= 24 : (ix >= 25 && ix < 29)) init_fill(ka->ws, ka->mem0, x >= 4 ? (ix - 24) * 4 + (x & 3) : 32 + (ix - 25) * 4 + x, threadIdx.x);
    if (x >= 4 && !(FLG(ka->prm[5]) & 1)) {
        const char* src = (const char*)ka->W[3] + (size_t)((x & 3) * 8 + (ix & 7)) * 147456 + threadIdx.x * 128;
#pragma unroll
        for (int i = 0; i < 5; ++i)
            if (i < 4 || w < 2) asm volatile("s_mov_b32 m0, %1\n\ts_nop 0\n\tglobal_load_lds_dword %0, off" :: "v"(src + i * 32768), "s"(155648) : "memory");
    }
    if (isr && ix >= 25) {
        for (int i = 0; i < (ix < 29 ? 3 : 0) + HSL; ++i) __builtin_amdgcn_s_sleep(40);
        init_wait(ka->ws, w, threadIdx.x & 63);
    }
    asm volatile("" : "+s"(ka));
    if (x < 4 && (ix < 25 || w == 0)) {
        const int lane = threadIdx.x & 63, g = lane >> 4, c = lane & 15;
        const unsigned lb = w * G_STG;
        unsigned* ct = (unsigned*)(smem + lb) + lane;
        asm volatile("" :: "v"(ct) : "memory");
#define CTW(k, val) ct[(k) * 64] = (unsigned)(val)
        const u64 ws = (u64)ka->ws, prm = (u64)ka;
        const u64 stb = 0;
        CTW(0, lane * 16);
        const int j = x * 9 + ix - 16;
        if (ix >= 25) {
            CTW(1, x * 65536 + 22528 + (lane & 31) * 64);
            CTW(2, G_WS_CTL + (lane < 32 ? G_C_EF + 3 * 2048 + lane * 64 : G_C_GX + (8 * x + (lane & 7)) * 128)); CTW(3, lane < 32 ? 2048 : 0);
            for (int k = 0; k < 7; ++k) {
                const int q = min(ix - 25 + 7 * k, 43), id = q < 36 ? 9 * x + (q >> 2) : 8 * x + q - 36;
                CTW(4 + k, (q < 36 ? G_D_UX + id * G_UXS + skew(0, id) * 32768 + (q & 3) * 8192 : G_D_KX + id * G_HS8 + skew(0, id) * 8192) + lane * 128);
                CTW(11 + k, q < 36 ? 32768 : 8192);
            }
        } else {
            const int rot = (FLG(ka->prm[5]) & 16) ? 0 : (x ^ 3) * 4;
            if (w >= 2) {
                return;
            }
            CTW(1, lane * 16 + 4096);
            CTW(2, x * 65536 + aMB(j >> 2, (j & 3) * 256 + (4 * g + 2 * ((c >> 1) & 1) + (c & 1)) * 16 + (c >> 2) * 4));
            for (int k = 0; k < 6; ++k) { const int q = 9 * k + ix - 16; CTW(46 + k, x * 65536 + lane * 16 + (q < 29 ? phys(q * 1024) : 0x200000 + phys((q < 45 ? q - 29 : q < 53 ? q - 21 : 20) * 1024))); }
            CTW(4, (c & 2) ? 0xEEEE : 0x4444); CTW(5, (c & 1) ? 0x03020706u : 0x05040100u);
            CTW(6, G_WS_CTL + G_C_GV + ((w & (G_GVN - 1)) * 32 + ((lane & 31) ^ (2 * rot))) * 128); CTW(7, x * 65536 + (lane & 31) * 64); CTW(8, lane * 4); CTW(9, 131072);
            for (int k = 0; k < 16; ++k) { const int i = k ^ rot; CTW(10 + k, x * 65536 + aPX(i, (g >> 1) * 512 + (2 * (c >> 2) + (c & 1)) * 64 + (g & 1) * 32 + ((c >> 1) & 1) * 16)); CTW(26 + k, i * 2048 + lane * 16); }
            for (int k = 0; k < 4; ++k) CTW(42 + k, x * 65536 + aPM(k, lane * 16));
        }
        const int id = ix >= 25 ? ix - 25 : j;
#define ROLE(A_) asm volatile(A_ :: "s"(ws), "s"(ws), "s"(ws), "s"(w), "s"(id), "s"(ws), "s"(prm), "s"(lb), "s"(stb) : ASM_CLOB)
        if (ix >= 25) ROLE(ASM_H);
        else ROLE(ASM_R);
    }
    else if (x >= 4 && ix >= 24) {
        if (ix >= 25) for (int i = 0; i < ((FLG(ka->prm[5]) >> 5) & 7); ++i) __builtin_amdgcn_s_sleep(36);
        const char* xs = (const char*)ka->X + (size_t)((ix - 24) * 4 + (x & 3)) * 73728 + threadIdx.x * 128;
        const int f = (ix - 24) * 4 + (x & 3);
        const u16* ps = (w == 0 ? ka->logA : w == 1 ? ka->Aim : w == 2 ? ka->Cre : ka->Cim) + f * 2304 + min((int)(threadIdx.x & 63), 35) * 64;
        if (ix < 25 || !(FLG(ka->prm[5]) & 4))
        asm volatile("s_mov_b32 m0, %4\n\ts_nop 0\n\tglobal_load_lds_dword %0, off\n\tglobal_load_lds_dword %1, off\n\tglobal_load_lds_dword %2, off\n\tglobal_load_lds_dword %3, off"
                     :: "v"(xs), "v"(xs + 32768), "v"(xs + 65536 - w * 8192), "v"(ps), "s"(163072) : "memory");
        if (ix < 25) for (int i = 0; i < ((FLG(ka->prm[5]) >> 5) & 7); ++i) __builtin_amdgcn_s_sleep(36);
    }
    }
    asm volatile("" : "+s"(ka));
    MainP p;
    p.prm[5] = ka->prm[5]; p.prm[7] = ka->prm[7];
    p.U = ka->U; p.ws = ka->ws; p.logA = ka->logA; p.Aim = ka->Aim; p.Cre = ka->Cre; p.Cim = ka->Cim; p.logdt = ka->logdt; p.Dskip = ka->Dskip; p.out = ka->out;
    if (x >= 4 && ix >= 24) {
        const u32x4 sent = {0xffffffffu, 0xffffffffu, 0xffffffffu, 0xffffffffu};
        const int tid = w * 64 + lane_id(), h = (ix - 24) * 4 + (x & 3);
        task_ut(p, smem, h, w, lane_id(), [&] {
#pragma unroll 1
            for (int sg = NG1; sg < 64; ++sg) {
                const unsigned ok = G_D_KM + h * G_HS4 + skew(sg, h) * 4096 + tid * 16;
                const unsigned op = tid < 128 ? G_D_PX + (sg * 32 + h) * 2048 + tid * 16 : G_D_PM + sg * 16384 + ((tid >> 4) & 1) * 8192 + h * 256 + (tid & 15) * 16;
                asm volatile("global_store_dwordx4 %0, %2, %3 sc1\n\tglobal_store_dwordx4 %1, %2, %3 sc1\n\ts_nop 1" :: "v"(ok), "v"(op), "v"(sent), "s"(p.ws) : "memory");
            }
        });
    }
    p.prm[0] = near_mask(p.ws, lane_id());
    p.X = (const u16*)(p.ws + ((p.prm[0] & 2) ? 0 : 8192));
    {
        const int nq = x < 4 ? (w + ((FLH(p.prm[7]) >> 1) & 3) > 3 ? 0 : 1000) : ix < 8 ? (FLG(p.prm[5]) >> 8) & 3 : ix < 16 ? (FLG(p.prm[5]) >> 10) & 3 : (ix & 2) && ((31 - ix) >> 2) < NAG ? 0 : ix < 24 ? (ix < 16 + ((FLH(p.prm[7]) >> 10) & 15) ? (FLG(p.prm[5]) >> 12) & 3 : 0) : (FLG(p.prm[5]) >> 14) & 3;
        if (b == 239 && w == 0) watch_role(p, lane_id());
        else if (nq || b == 239) {
            for (int i = 0; i < (int)(((ix & 7) * 4 + w) * ((FLG(p.prm[5]) >> 23) & 7)); ++i) __builtin_amdgcn_s_sleep(3);
            s4d_wave(p, smem, w, lane_id(), nq == 3 || b == 239 ? 1000 : nq);
        }
        if (x < 4) return;
        __syncthreads();
    }
    if ((ix & 18) == 18 && ((31 - ix) >> 2) < NAG) { attn_role(p, ((31 - ix) >> 2) + (ix < 24 ? NA : 0), (x & 3) * 8 + (ix & 1) * 4 + w, w, lane_id());
        if (b == 255) {
            wait_word(p, p.ws + G_WS_CTL + G_C_ARR, 255, 50u);
            if (w == 0 && lane_id() == 0) __hip_atomic_fetch_add((gu32*)(p.ws + G_WS_EP), 1u, RLX_AGENT);
        }
        __syncthreads();
    }
    worker_body(p, smem, w, lane_id());
}

extern "C" __global__ void code_end_marker() {}

static constexpr int g_prm[8] = {PRM_DEFAULT};

extern "C" void kernel_launch(void* const* d_in, const int* in_sizes, int n_in, void* d_out, int out_size,
                              void* d_ws, size_t ws_size, hipStream_t stream) {
    char* ws = (char*)(((uintptr_t)d_ws + 0x1FFFFF) & ~(uintptr_t)0x1FFFFF);
    MainP p;
    for (int i = 0; i < 8; ++i) p.prm[i] = g_prm[i];
    p.mem0 = (const u16*)d_in[1];
    for (int i = 0; i < 4; ++i) p.W[i] = (const u16*)d_in[2 + i];
    p.X = (const u16*)d_in[0]; p.U = (const u16*)d_in[7]; p.ws = ws;
    p.logA = (const u16*)d_in[8]; p.Aim = (const u16*)d_in[9]; p.Cre = (const u16*)d_in[10]; p.Cim = (const u16*)d_in[11];
    p.logdt = (const u16*)d_in[12]; p.Dskip = (const u16*)d_in[13]; p.out = (u16*)d_out;
    main_kernel<<<256, 256, 0, stream>>>(p);
}
```

```cpp
#include <hip/hip_runtime.h>
#include <hip/hip_bf16.h>
#include <stdint.h>
#include <stddef.h>

typedef __attribute__((ext_vector_type(8))) __bf16 bf16x8;
typedef __attribute__((ext_vector_type(4))) short s16x4;
typedef __attribute__((ext_vector_type(4))) float f32x4;
typedef __attribute__((ext_vector_type(4))) unsigned u32x4;
typedef __attribute__((ext_vector_type(2))) unsigned u32x2;
typedef __attribute__((ext_vector_type(2))) float f32x2;
typedef __attribute__((ext_vector_type(16))) unsigned u32x16;
typedef unsigned short u16;
typedef unsigned long long u64;

#define GAS __attribute__((address_space(1)))
typedef GAS unsigned gu32;
#define RLX_AGENT __ATOMIC_RELAXED, __HIP_MEMORY_SCOPE_AGENT

static constexpr int DM = 576, NH = 32, DH = 128, MM = 8, SEG = 32, NSEG = 64, SL = 2048;
static constexpr int HD = NH * DH;
static constexpr int OUTW = 2 * DM;
static constexpr int NST = 128;

__device__ __forceinline__ float bf2f(u16 h) { return __uint_as_float(((unsigned)h) << 16); }
typedef __attribute__((ext_vector_type(2))) __bf16 bf16x2_t;
__device__ __forceinline__ unsigned pk2bf(float a, float b) {
    bf16x2_t v; v[0] = (__bf16)a; v[1] = (__bf16)b;
    return __builtin_bit_cast(unsigned, v);
}
__device__ __forceinline__ float bfr(float f) { return __uint_as_float(pk2bf(f, f) << 16); }
__device__ __forceinline__ float lo2f(unsigned u) { return __uint_as_float(u << 16); }
__device__ __forceinline__ float hi2f(unsigned u) { return __uint_as_float(u & 0xffff0000u); }
__device__ __forceinline__ float rowmax4(float x) {
    auto a = __builtin_amdgcn_permlane16_swap(__float_as_uint(x), __float_as_uint(x), false, false);
    x = fmaxf(__uint_as_float(a[0]), __uint_as_float(a[1]));
    auto b = __builtin_amdgcn_permlane32_swap(__float_as_uint(x), __float_as_uint(x), false, false);
    return fmaxf(__uint_as_float(b[0]), __uint_as_float(b[1]));
}
__device__ __forceinline__ float rowsum4(float x) {
    auto a = __builtin_amdgcn_permlane16_swap(__float_as_uint(x), __float_as_uint(x), false, false);
    x = __uint_as_float(a[0]) + __uint_as_float(a[1]);
    auto b = __builtin_amdgcn_permlane32_swap(__float_as_uint(x), __float_as_uint(x), false, false);
    return __uint_as_float(b[0]) + __uint_as_float(b[1]);
}

struct MainP {
    int prm[8];
    const u16* mem0; const u16* W[4]; const u16* X; const u16* U;
    char* ws;
    const u16* logA; const u16* Aim; const u16* Cre; const u16* Cim; const u16* logdt; const u16* Dskip;
    u16* out;
};
static constexpr int LDS_TOTAL = 163840;
static constexpr unsigned kWaitCap = 1u << 18;
#define VMC0() asm volatile("s_waitcnt vmcnt(0)" ::: "memory")
#define PRM_DEFAULT 1020, 600, 120, 656, 760, 1434873899, 3145976, 52931
#define ASM_QK \
    "s_load_dwordx2 s[42:43], %3, 40\n\t" \
    "s_load_dwordx2 s[44:45], %3, 48\n\t" \
    "s_load_dwordx2 s[36:37], %3, 0x58\n\t" \
    "s_load_dwordx2 s[50:51], %3, 0x48\n\t" \
    "s_load_dwordx8 s[84:91], %3, 0\n\t" \
    "s_mov_b32 s66, %4\n\t" \
    "s_mov_b32 s67, %5\n\t" \
    "s_mov_b32 s68, 262144\n\t" \
    "s_mov_b32 s72, 0\n\t" \
    "s_mov_b32 s83, -1\n\t" \
    "s_mov_b32 s65, 0\n\t" \
    "s_lshl_b32 s82, s66, 10\n\t" \
    "s_waitcnt lgkmcnt(0)\n\t" \
    "s_add_u32 s46, s42, 2097152\n\t" \
    "s_addc_u32 s47, s43, 0\n\t" \
    "s_add_u32 m0, s82, 65536\n\t" \
    "s_nop 0\n\t" \
    "global_load_lds_dwordx4 v200, s[46:47]\n\t" \
    "s_add_u32 s46, s42, 2228224\n\t" \
    "s_addc_u32 s47, s43, 0\n\t" \
    "s_add_u32 m0, s82, 69632\n\t" \
    "s_nop 0\n\t" \
    "global_load_lds_dwordx4 v200, s[46:47]\n\t" \
    "s_add_u32 s46, s42, 2359296\n\t" \
    "s_addc_u32 s47, s43, 0\n\t" \
    "s_add_u32 m0, s82, 73728\n\t" \
    "s_nop 0\n\t" \
    "global_load_lds_dwordx4 v200, s[46:47]\n\t" \
    "s_add_u32 s46, s42, 2490368\n\t" \
    "s_addc_u32 s47, s43, 0\n\t" \
    "s_add_u32 m0, s82, 77824\n\t" \
    "s_nop 0\n\t" \
    "global_load_lds_dwordx4 v200, s[46:47]\n\t" \
    "s_add_u32 s46, s42, 2621440\n\t" \
    "s_addc_u32 s47, s43, 0\n\t" \
    "s_add_u32 m0, s82, 81920\n\t" \
    "s_nop 0\n\t" \
    "global_load_lds_dwordx4 v200, s[46:47]\n\t" \
    "s_add_u32 s46, s42, 2752512\n\t" \
    "s_addc_u32 s47, s43, 0\n\t" \
    "s_add_u32 m0, s82, 86016\n\t" \
    "s_nop 0\n\t" \
    "global_load_lds_dwordx4 v200, s[46:47]\n\t" \
    "s_add_u32 s46, s42, 2883584\n\t" \
    "s_addc_u32 s47, s43, 0\n\t" \
    "s_add_u32 m0, s82, 90112\n\t" \
    "s_nop 0\n\t" \
    "global_load_lds_dwordx4 v200, s[46:47]\n\t" \
    "s_add_u32 s46, s42, 3014656\n\t" \
    "s_addc_u32 s47, s43, 0\n\t" \
    "s_add_u32 m0, s82, 94208\n\t" \
    "s_nop 0\n\t" \
    "global_load_lds_dwordx4 v200, s[46:47]\n\t" \
    "s_add_u32 s46, s42, 3145728\n\t" \
    "s_addc_u32 s47, s43, 0\n\t" \
    "s_add_u32 m0, s82, 98304\n\t" \
    "s_nop 0\n\t" \
    "global_load_lds_dwordx4 v200, s[46:47]\n\t" \
    "s_add_u32 s46, s42, 3276800\n\t" \
    "s_addc_u32 s47, s43, 0\n\t" \
    "s_add_u32 m0, s82, 102400\n\t" \
    "s_nop 0\n\t" \
    "global_load_lds_dwordx4 v200, s[46:47]\n\t" \
    "s_add_u32 s46, s42, 3407872\n\t" \
    "s_addc_u32 s47, s43, 0\n\t" \
    "s_add_u32 m0, s82, 106496\n\t" \
    "s_nop 0\n\t" \
    "global_load_lds_dwordx4 v200, s[46:47]\n\t" \
    "s_add_u32 s46, s42, 3538944\n\t" \
    "s_addc_u32 s47, s43, 0\n\t" \
    "s_add_u32 m0, s82, 110592\n\t" \
    "s_nop 0\n\t" \
    "global_load_lds_dwordx4 v200, s[46:47]\n\t" \
    "s_add_u32 s46, s42, 3670016\n\t" \
    "s_addc_u32 s47, s43, 0\n\t" \
    "s_add_u32 m0, s82, 114688\n\t" \
    "s_nop 0\n\t" \
    "global_load_lds_dwordx4 v200, s[46:47]\n\t" \
    "s_add_u32 s46, s42, 3801088\n\t" \
    "s_addc_u32 s47, s43, 0\n\t" \
    "s_add_u32 m0, s82, 118784\n\t" \
    "s_nop 0\n\t" \
    "global_load_lds_dwordx4 v200, s[46:47]\n\t" \
    "s_add_u32 s46, s42, 3932160\n\t" \
    "s_addc_u32 s47, s43, 0\n\t" \
    "s_add_u32 m0, s82, 122880\n\t" \
    "s_nop 0\n\t" \
    "global_load_lds_dwordx4 v200, s[46:47]\n\t" \
    "s_add_u32 s46, s42, 4063232\n\t" \
    "s_addc_u32 s47, s43, 0\n\t" \
    "s_add_u32 m0, s82, 126976\n\t" \
    "s_nop 0\n\t" \
    "global_load_lds_dwordx4 v200, s[46:47]\n\t" \
    "s_add_u32 s46, s42, 4194304\n\t" \
    "s_addc_u32 s47, s43, 0\n\t" \
    "s_add_u32 m0, s82, 131072\n\t" \
    "s_nop 0\n\t" \
    "global_load_lds_dwordx4 v200, s[46:47]\n\t" \
    "s_add_u32 s46, s42, 4325376\n\t" \
    "s_addc_u32 s47, s43, 0\n\t" \
    "s_add_u32 m0, s82, 135168\n\t" \
    "s_nop 0\n\t" \
    "global_load_lds_dwordx4 v200, s[46:47]\n\t" \
    "s_add_u32 s46, s42, 4456448\n\t" \
    "s_addc_u32 s47, s43, 0\n\t" \
    "s_add_u32 m0, s82, 139264\n\t" \
    "s_nop 0\n\t" \
    "global_load_lds_dwordx4 v200, s[46:47]\n\t" \
    "s_add_u32 s46, s42, 4587520\n\t" \
    "s_addc_u32 s47, s43, 0\n\t" \
    "s_add_u32 m0, s82, 143360\n\t" \
    "s_nop 0\n\t" \
    "global_load_lds_dwordx4 v200, s[46:47]\n\t" \
    "s_mov_b32 s77, 3019898880\n\t" \
    "s_mov_b32 s78, 3054198966\n\t" \
    "s_waitcnt lgkmcnt(0)\n\t" \
    "s_mov_b32 s70, 116\n\t" \
    "qk_E1_%=:\n\t" \
    "s_memtime s[56:57]\n\t" \
    "s_memrealtime s[2:3]\n\t" \
    "s_nop 0\n\t" \
    "s_memrealtime s[4:5]\n\t" \
    "s_nop 0\n\t" \
    "s_memrealtime s[6:7]\n\t" \
    "s_nop 0\n\t" \
    "s_memrealtime s[8:9]\n\t" \
    "s_nop 0\n\t" \
    "s_memrealtime s[10:11]\n\t" \
    "s_nop 0\n\t" \
    "s_memrealtime s[12:13]\n\t" \
    "s_nop 0\n\t" \
    "s_memrealtime s[14:15]\n\t" \
    "s_nop 0\n\t" \
    "s_memrealtime s[16:17]\n\t" \
    "s_nop 0\n\t" \
    "s_memrealtime s[18:19]\n\t" \
    "s_nop 0\n\t" \
    "s_memrealtime s[20:21]\n\t" \
    "s_nop 0\n\t" \
    "s_memrealtime s[22:23]\n\t" \
    "s_nop 0\n\t" \
    "s_memrealtime s[24:25]\n\t" \
    "s_nop 0\n\t" \
    "s_memrealtime s[26:27]\n\t" \
    "s_nop 0\n\t" \
    "s_memrealtime s[28:29]\n\t" \
    "s_memtime s[54:55]\n\t" \
    "s_add_u32 s70, s70, 8\n\t" \
    "s_waitcnt lgkmcnt(0)\n\t" \
    "s_sub_u32 s69, s54, s56\n\t" \
    "s_cmp_gt_u32 s69, s70\n\t" \
    "s_cbranch_scc1 qk_E1_%=\n\t" \
    "s_add_u32 s2, s2, s4\n\t" \
    "s_add_u32 s6, s6, s8\n\t" \
    "s_add_u32 s10, s10, s12\n\t" \
    "s_add_u32 s14, s14, s16\n\t" \
    "s_add_u32 s18, s18, s20\n\t" \
    "s_add_u32 s22, s22, s24\n\t" \
    "s_add_u32 s2, s2, s6\n\t" \
    "s_add_u32 s10, s10, s14\n\t" \
    "s_add_u32 s18, s18, s22\n\t" \
    "s_add_u32 s2, s2, s26\n\t" \
    "s_add_u32 s10, s10, s18\n\t" \
    "s_add_u32 s2, s2, s10\n\t" \
    "s_mul_i32 s70, s28, 13\n\t" \
    "s_sub_u32 s70, s70, s2\n\t" \
    "s_min_u32 s70, s70, 52\n\t" \
    "s_mul_i32 s69, s69, s70\n\t" \
    "s_mul_hi_u32 s69, s69, 76695844\n\t" \
    "s_mov_b32 s75, s73\n\t" \
    "s_mov_b32 s76, s74\n\t" \
    "s_add_u32 s73, s56, s69\n\t" \
    "s_lshl_b32 s74, s28, 4\n\t" \
    "s_waitcnt vmcnt(32) lgkmcnt(0)\n\t" \
    "s_barrier\n\t" \
    "ds_read_b64_tr_b16 a[0:1], v201 offset:0\n\t" \
    "ds_read_b64_tr_b16 a[2:3], v201 offset:1024\n\t" \
    "ds_read_b64_tr_b16 a[4:5], v201 offset:8192\n\t" \
    "ds_read_b64_tr_b16 a[6:7], v201 offset:9216\n\t" \
    "ds_read_b64_tr_b16 a[72:73], v202 offset:0\n\t" \
    "ds_read_b64_tr_b16 a[74:75], v202 offset:1024\n\t" \
    "ds_read_b64_tr_b16 a[76:77], v202 offset:8192\n\t" \
    "ds_read_b64_tr_b16 a[78:79], v202 offset:9216\n\t" \
    "s_waitcnt vmcnt(28) lgkmcnt(0)\n\t" \
    "s_barrier\n\t" \
    "ds_read_b64_tr_b16 a[8:9], v201 offset:16384\n\t" \
    "ds_read_b64_tr_b16 a[10:11], v201 offset:17408\n\t" \
    "ds_read_b64_tr_b16 a[12:13], v201 offset:24576\n\t" \
    "ds_read_b64_tr_b16 a[14:15], v201 offset:25600\n\t" \
    "ds_read_b64_tr_b16 a[80:81], v202 offset:16384\n\t" \
    "ds_read_b64_tr_b16 a[82:83], v202 offset:17408\n\t" \
    "ds_read_b64_tr_b16 a[84:85], v202 offset:24576\n\t" \
    "ds_read_b64_tr_b16 a[86:87], v202 offset:25600\n\t" \
    "s_add_u32 s46, s44, 0\n\t" \
    "s_addc_u32 s47, s45, 0\n\t" \
    "s_add_u32 m0, s82, 0\n\t" \
    "s_nop 0\n\t" \
    "global_load_lds_dwordx4 v200, s[46:47]\n\t" \
    "s_add_u32 s46, s44, 131072\n\t" \
    "s_addc_u32 s47, s45, 0\n\t" \
    "s_add_u32 m0, s82, 4096\n\t" \
    "s_nop 0\n\t" \
    "global_load_lds_dwordx4 v200, s[46:47]\n\t" \
    "s_add_u32 s46, s44, 262144\n\t" \
    "s_addc_u32 s47, s45, 0\n\t" \
    "s_add_u32 m0, s82, 8192\n\t" \
    "s_nop 0\n\t" \
    "global_load_lds_dwordx4 v200, s[46:47]\n\t" \
    "s_add_u32 s46, s44, 393216\n\t" \
    "s_addc_u32 s47, s45, 0\n\t" \
    "s_add_u32 m0, s82, 12288\n\t" \
    "s_nop 0\n\t" \
    "global_load_lds_dwordx4 v200, s[46:47]\n\t" \
    "s_waitcnt vmcnt(28) lgkmcnt(0)\n\t" \
    "s_barrier\n\t" \
    "ds_read_b64_tr_b16 a[16:17], v201 offset:32768\n\t" \
    "ds_read_b64_tr_b16 a[18:19], v201 offset:33792\n\t" \
    "ds_read_b64_tr_b16 a[20:21], v201 offset:40960\n\t" \
    "ds_read_b64_tr_b16 a[22:23], v201 offset:41984\n\t" \
    "ds_read_b64_tr_b16 a[88:89], v202 offset:32768\n\t" \
    "ds_read_b64_tr_b16 a[90:91], v202 offset:33792\n\t" \
    "ds_read_b64_tr_b16 a[92:93], v202 offset:40960\n\t" \
    "ds_read_b64_tr_b16 a[94:95], v202 offset:41984\n\t" \
    "s_add_u32 s46, s44, 524288\n\t" \
    "s_addc_u32 s47, s45, 0\n\t" \
    "s_add_u32 m0, s82, 16384\n\t" \
    "s_nop 0\n\t" \
    "global_load_lds_dwordx4 v200, s[46:47]\n\t" \
    "s_add_u32 s46, s44, 655360\n\t" \
    "s_addc_u32 s47, s45, 0\n\t" \
    "s_add_u32 m0, s82, 20480\n\t" \
    "s_nop 0\n\t" \
    "global_load_lds_dwordx4 v200, s[46:47]\n\t" \
    "s_add_u32 s46, s44, 786432\n\t" \
    "s_addc_u32 s47, s45, 0\n\t" \
    "s_add_u32 m0, s82, 24576\n\t" \
    "s_nop 0\n\t" \
    "global_load_lds_dwordx4 v200, s[46:47]\n\t" \
    "s_add_u32 s46, s44, 917504\n\t" \
    "s_addc_u32 s47, s45, 0\n\t" \
    "s_add_u32 m0, s82, 28672\n\t" \
    "s_nop 0\n\t" \
    "global_load_lds_dwordx4 v200, s[46:47]\n\t" \
    "s_waitcnt vmcnt(28) lgkmcnt(0)\n\t" \
    "s_barrier\n\t" \
    "ds_read_b64_tr_b16 a[24:25], v201 offset:49152\n\t" \
    "ds_read_b64_tr_b16 a[26:27], v201 offset:50176\n\t" \
    "ds_read_b64_tr_b16 a[28:29], v201 offset:57344\n\t" \
    "ds_read_b64_tr_b16 a[30:31], v201 offset:58368\n\t" \
    "ds_read_b64_tr_b16 a[96:97], v202 offset:49152\n\t" \
    "ds_read_b64_tr_b16 a[98:99], v202 offset:50176\n\t" \
    "ds_read_b64_tr_b16 a[100:101], v202 offset:57344\n\t" \
    "ds_read_b64_tr_b16 a[102:103], v202 offset:58368\n\t" \
    "s_add_u32 s46, s44, 1048576\n\t" \
    "s_addc_u32 s47, s45, 0\n\t" \
    "s_add_u32 m0, s82, 32768\n\t" \
    "s_nop 0\n\t" \
    "global_load_lds_dwordx4 v200, s[46:47]\n\t" \
    "s_add_u32 s46, s44, 1179648\n\t" \
    "s_addc_u32 s47, s45, 0\n\t" \
    "s_add_u32 m0, s82, 36864\n\t" \
    "s_nop 0\n\t" \
    "global_load_lds_dwordx4 v200, s[46:47]\n\t" \
    "s_add_u32 s46, s44, 1310720\n\t" \
    "s_addc_u32 s47, s45, 0\n\t" \
    "s_add_u32 m0, s82, 40960\n\t" \
    "s_nop 0\n\t" \
    "global_load_lds_dwordx4 v200, s[46:47]\n\t" \
    "s_add_u32 s46, s44, 1441792\n\t" \
    "s_addc_u32 s47, s45, 0\n\t" \
    "s_add_u32 m0, s82, 45056\n\t" \
    "s_nop 0\n\t" \
    "global_load_lds_dwordx4 v200, s[46:47]\n\t" \
    "s_waitcnt vmcnt(28) lgkmcnt(0)\n\t" \
    "s_barrier\n\t" \
    "v_add_u32 v201, 0x10000, v201\n\t" \
    "v_add_u32 v202, 0x10000, v202\n\t" \
    "ds_read_b64_tr_b16 a[32:33], v201 offset:0\n\t" \
    "ds_read_b64_tr_b16 a[34:35], v201 offset:1024\n\t" \
    "ds_read_b64_tr_b16 a[36:37], v201 offset:8192\n\t" \
    "ds_read_b64_tr_b16 a[38:39], v201 offset:9216\n\t" \
    "ds_read_b64_tr_b16 a[104:105], v202 offset:0\n\t" \
    "ds_read_b64_tr_b16 a[106:107], v202 offset:1024\n\t" \
    "ds_read_b64_tr_b16 a[108:109], v202 offset:8192\n\t" \
    "ds_read_b64_tr_b16 a[110:111], v202 offset:9216\n\t" \
    "s_add_u32 s46, s44, 1572864\n\t" \
    "s_addc_u32 s47, s45, 0\n\t" \
    "s_add_u32 m0, s82, 49152\n\t" \
    "s_nop 0\n\t" \
    "global_load_lds_dwordx4 v200, s[46:47]\n\t" \
    "s_add_u32 s46, s44, 1703936\n\t" \
    "s_addc_u32 s47, s45, 0\n\t" \
    "s_add_u32 m0, s82, 53248\n\t" \
    "s_nop 0\n\t" \
    "global_load_lds_dwordx4 v200, s[46:47]\n\t" \
    "s_add_u32 s46, s44, 1835008\n\t" \
    "s_addc_u32 s47, s45, 0\n\t" \
    "s_add_u32 m0, s82, 57344\n\t" \
    "s_nop 0\n\t" \
    "global_load_lds_dwordx4 v200, s[46:47]\n\t" \
    "s_add_u32 s46, s44, 1966080\n\t" \
    "s_addc_u32 s47, s45, 0\n\t" \
    "s_add_u32 m0, s82, 61440\n\t" \
    "s_nop 0\n\t" \
    "global_load_lds_dwordx4 v200, s[46:47]\n\t" \
    "s_waitcnt vmcnt(28) lgkmcnt(0)\n\t" \
    "s_barrier\n\t" \
    "ds_read_b64_tr_b16 a[40:41], v201 offset:16384\n\t" \
    "ds_read_b64_tr_b16 a[42:43], v201 offset:17408\n\t" \
    "ds_read_b64_tr_b16 a[44:45], v201 offset:24576\n\t" \
    "ds_read_b64_tr_b16 a[46:47], v201 offset:25600\n\t" \
    "ds_read_b64_tr_b16 a[112:113], v202 offset:16384\n\t" \
    "ds_read_b64_tr_b16 a[114:115], v202 offset:17408\n\t" \
    "ds_read_b64_tr_b16 a[116:117], v202 offset:24576\n\t" \
    "ds_read_b64_tr_b16 a[118:119], v202 offset:25600\n\t" \
    "s_add_u32 s46, s44, 2097152\n\t" \
    "s_addc_u32 s47, s45, 0\n\t" \
    "s_add_u32 m0, s82, 65536\n\t" \
    "s_nop 0\n\t" \
    "global_load_lds_dwordx4 v200, s[46:47]\n\t" \
    "s_add_u32 s46, s44, 2228224\n\t" \
    "s_addc_u32 s47, s45, 0\n\t" \
    "s_add_u32 m0, s82, 69632\n\t" \
    "s_nop 0\n\t" \
    "global_load_lds_dwordx4 v200, s[46:47]\n\t" \
    "s_add_u32 s46, s44, 2359296\n\t" \
    "s_addc_u32 s47, s45, 0\n\t" \
    "s_add_u32 m0, s82, 73728\n\t" \
    "s_nop 0\n\t" \
    "global_load_lds_dwordx4 v200, s[46:47]\n\t" \
    "s_add_u32 s46, s44, 2490368\n\t" \
    "s_addc_u32 s47, s45, 0\n\t" \
    "s_add_u32 m0, s82, 77824\n\t" \
    "s_nop 0\n\t" \
    "global_load_lds_dwordx4 v200, s[46:47]\n\t" \
    "s_waitcnt vmcnt(28) lgkmcnt(0)\n\t" \
    "s_barrier\n\t" \
    "ds_read_b64_tr_b16 a[48:49], v201 offset:32768\n\t" \
    "ds_read_b64_tr_b16 a[50:51], v201 offset:33792\n\t" \
    "ds_read_b64_tr_b16 a[52:53], v201 offset:40960\n\t" \
    "ds_read_b64_tr_b16 a[54:55], v201 offset:41984\n\t" \
    "ds_read_b64_tr_b16 a[120:121], v202 offset:32768\n\t" \
    "ds_read_b64_tr_b16 a[122:123], v202 offset:33792\n\t" \
    "ds_read_b64_tr_b16 a[124:125], v202 offset:40960\n\t" \
    "ds_read_b64_tr_b16 a[126:127], v202 offset:41984\n\t" \
    "s_add_u32 s46, s44, 2621440\n\t" \
    "s_addc_u32 s47, s45, 0\n\t" \
    "s_add_u32 m0, s82, 81920\n\t" \
    "s_nop 0\n\t" \
    "global_load_lds_dwordx4 v200, s[46:47]\n\t" \
    "s_add_u32 s46, s44, 2752512\n\t" \
    "s_addc_u32 s47, s45, 0\n\t" \
    "s_add_u32 m0, s82, 86016\n\t" \
    "s_nop 0\n\t" \
    "global_load_lds_dwordx4 v200, s[46:47]\n\t" \
    "s_add_u32 s46, s44, 2883584\n\t" \
    "s_addc_u32 s47, s45, 0\n\t" \
    "s_add_u32 m0, s82, 90112\n\t" \
    "s_nop 0\n\t" \
    "global_load_lds_dwordx4 v200, s[46:47]\n\t" \
    "s_add_u32 s46, s44, 3014656\n\t" \
    "s_addc_u32 s47, s45, 0\n\t" \
    "s_add_u32 m0, s82, 94208\n\t" \
    "s_nop 0\n\t" \
    "global_load_lds_dwordx4 v200, s[46:47]\n\t" \
    "s_waitcnt vmcnt(28) lgkmcnt(0)\n\t" \
    "s_barrier\n\t" \
    "ds_read_b64_tr_b16 a[56:57], v201 offset:49152\n\t" \
    "ds_read_b64_tr_b16 a[58:59], v201 offset:50176\n\t" \
    "ds_read_b64_tr_b16 a[60:61], v201 offset:57344\n\t" \
    "ds_read_b64_tr_b16 a[62:63], v201 offset:58368\n\t" \
    "ds_read_b64_tr_b16 a[128:129], v202 offset:49152\n\t" \
    "ds_read_b64_tr_b16 a[130:131], v202 offset:50176\n\t" \
    "ds_read_b64_tr_b16 a[132:133], v202 offset:57344\n\t" \
    "ds_read_b64_tr_b16 a[134:135], v202 offset:58368\n\t" \
    "s_add_u32 s46, s44, 3145728\n\t" \
    "s_addc_u32 s47, s45, 0\n\t" \
    "s_add_u32 m0, s82, 98304\n\t" \
    "s_nop 0\n\t" \
    "global_load_lds_dwordx4 v200, s[46:47]\n\t" \
    "s_add_u32 s46, s44, 3276800\n\t" \
    "s_addc_u32 s47, s45, 0\n\t" \
    "s_add_u32 m0, s82, 102400\n\t" \
    "s_nop 0\n\t" \
    "global_load_lds_dwordx4 v200, s[46:47]\n\t" \
    "s_add_u32 s46, s44, 3407872\n\t" \
    "s_addc_u32 s47, s45, 0\n\t" \
    "s_add_u32 m0, s82, 106496\n\t" \
    "s_nop 0\n\t" \
    "global_load_lds_dwordx4 v200, s[46:47]\n\t" \
    "s_add_u32 s46, s44, 3538944\n\t" \
    "s_addc_u32 s47, s45, 0\n\t" \
    "s_add_u32 m0, s82, 110592\n\t" \
    "s_nop 0\n\t" \
    "global_load_lds_dwordx4 v200, s[46:47]\n\t" \
    "v_mbcnt_lo_u32_b32 v160, -1, 0\n\t" \
    "v_mbcnt_hi_u32_b32 v160, -1, v160\n\t" \
    "v_lshlrev_b32 v160, 6, v160\n\t" \
    "v_add_u32 v160, 6225920, v160\n\t" \
    "v_mov_b32 v161, 6234112\n\t" \
    "global_load_dword v161, v161, s[36:37] sc1\n\t" \
    "global_load_dwordx2 v[162:163], v160, s[36:37] sc1\n\t" \
    "global_load_dword v164, v160, s[36:37] offset:2048 sc1\n\t" \
    "s_waitcnt vmcnt(31) lgkmcnt(0)\n\t" \
    "s_barrier\n\t" \
    "v_add_u32 v201, 0x10000, v201\n\t" \
    "v_add_u32 v202, 0x10000, v202\n\t" \
    "ds_read_b64_tr_b16 a[64:65], v201 offset:0\n\t" \
    "ds_read_b64_tr_b16 a[66:67], v201 offset:1024\n\t" \
    "ds_read_b64_tr_b16 a[68:69], v201 offset:8192\n\t" \
    "ds_read_b64_tr_b16 a[70:71], v201 offset:9216\n\t" \
    "ds_read_b64_tr_b16 a[136:137], v202 offset:0\n\t" \
    "ds_read_b64_tr_b16 a[138:139], v202 offset:1024\n\t" \
    "ds_read_b64_tr_b16 a[140:141], v202 offset:8192\n\t" \
    "ds_read_b64_tr_b16 a[142:143], v202 offset:9216\n\t" \
    "v_add_u32 v201, 0xfffe0000, v201\n\t" \
    "v_add_u32 v202, 0xfffe0000, v202\n\t" \
    "s_add_u32 s46, s44, 3670016\n\t" \
    "s_addc_u32 s47, s45, 0\n\t" \
    "s_add_u32 m0, s82, 114688\n\t" \
    "s_nop 0\n\t" \
    "global_load_lds_dwordx4 v200, s[46:47]\n\t" \
    "s_add_u32 s46, s44, 3801088\n\t" \
    "s_addc_u32 s47, s45, 0\n\t" \
    "s_add_u32 m0, s82, 118784\n\t" \
    "s_nop 0\n\t" \
    "global_load_lds_dwordx4 v200, s[46:47]\n\t" \
    "s_add_u32 s46, s44, 3932160\n\t" \
    "s_addc_u32 s47, s45, 0\n\t" \
    "s_add_u32 m0, s82, 122880\n\t" \
    "s_nop 0\n\t" \
    "global_load_lds_dwordx4 v200, s[46:47]\n\t" \
    "s_add_u32 s46, s44, 4063232\n\t" \
    "s_addc_u32 s47, s45, 0\n\t" \
    "s_add_u32 m0, s82, 126976\n\t" \
    "s_nop 0\n\t" \
    "global_load_lds_dwordx4 v200, s[46:47]\n\t" \
    "s_waitcnt lgkmcnt(0)\n\t" \
    "s_barrier\n\t" \
    "s_add_u32 s46, s44, 4194304\n\t" \
    "s_addc_u32 s47, s45, 0\n\t" \
    "s_add_u32 m0, s82, 131072\n\t" \
    "s_nop 0\n\t" \
    "global_load_lds_dwordx4 v200, s[46:47]\n\t" \
    "s_add_u32 s46, s44, 4325376\n\t" \
    "s_addc_u32 s47, s45, 0\n\t" \
    "s_add_u32 m0, s82, 135168\n\t" \
    "s_nop 0\n\t" \
    "global_load_lds_dwordx4 v200, s[46:47]\n\t" \
    "s_add_u32 s46, s44, 4456448\n\t" \
    "s_addc_u32 s47, s45, 0\n\t" \
    "s_add_u32 m0, s82, 139264\n\t" \
    "s_nop 0\n\t" \
    "global_load_lds_dwordx4 v200, s[46:47]\n\t" \
    "s_add_u32 s46, s44, 4587520\n\t" \
    "s_addc_u32 s47, s45, 0\n\t" \
    "s_add_u32 m0, s82, 143360\n\t" \
    "s_nop 0\n\t" \
    "global_load_lds_dwordx4 v200, s[46:47]\n\t" \
    "s_waitcnt vmcnt(35) lgkmcnt(0)\n\t" \
    "s_barrier\n\t" \
    "ds_read_b64_tr_b16 a[144:145], v201 offset:0\n\t" \
    "ds_read_b64_tr_b16 a[146:147], v201 offset:1024\n\t" \
    "ds_read_b64_tr_b16 a[148:149], v201 offset:8192\n\t" \
    "ds_read_b64_tr_b16 a[150:151], v201 offset:9216\n\t" \
    "ds_read_b64_tr_b16 a[216:217], v202 offset:0\n\t" \
    "ds_read_b64_tr_b16 a[218:219], v202 offset:1024\n\t" \
    "ds_read_b64_tr_b16 a[220:221], v202 offset:8192\n\t" \
    "ds_read_b64_tr_b16 a[222:223], v202 offset:9216\n\t" \
    "s_waitcnt vmcnt(31) lgkmcnt(0)\n\t" \
    "s_barrier\n\t" \
    "ds_read_b64_tr_b16 a[152:153], v201 offset:16384\n\t" \
    "ds_read_b64_tr_b16 a[154:155], v201 offset:17408\n\t" \
    "ds_read_b64_tr_b16 a[156:157], v201 offset:24576\n\t" \
    "ds_read_b64_tr_b16 a[158:159], v201 offset:25600\n\t" \
    "ds_read_b64_tr_b16 a[224:225], v202 offset:16384\n\t" \
    "ds_read_b64_tr_b16 a[226:227], v202 offset:17408\n\t" \
    "ds_read_b64_tr_b16 a[228:229], v202 offset:24576\n\t" \
    "ds_read_b64_tr_b16 a[230:231], v202 offset:25600\n\t" \
    "s_waitcnt vmcnt(27) lgkmcnt(0)\n\t" \
    "s_barrier\n\t" \
    "ds_read_b64_tr_b16 a[160:161], v201 offset:32768\n\t" \
    "ds_read_b64_tr_b16 a[162:163], v201 offset:33792\n\t" \
    "ds_read_b64_tr_b16 a[164:165], v201 offset:40960\n\t" \
    "ds_read_b64_tr_b16 a[166:167], v201 offset:41984\n\t" \
    "ds_read_b64_tr_b16 a[232:233], v202 offset:32768\n\t" \
    "ds_read_b64_tr_b16 a[234:235], v202 offset:33792\n\t" \
    "ds_read_b64_tr_b16 a[236:237], v202 offset:40960\n\t" \
    "ds_read_b64_tr_b16 a[238:239], v202 offset:41984\n\t" \
    "s_waitcnt vmcnt(23) lgkmcnt(0)\n\t" \
    "s_barrier\n\t" \
    "ds_read_b64_tr_b16 a[168:169], v201 offset:49152\n\t" \
    "ds_read_b64_tr_b16 a[170:171], v201 offset:50176\n\t" \
    "ds_read_b64_tr_b16 a[172:173], v201 offset:57344\n\t" \
    "ds_read_b64_tr_b16 a[174:175], v201 offset:58368\n\t" \
    "ds_read_b64_tr_b16 a[240:241], v202 offset:49152\n\t" \
    "ds_read_b64_tr_b16 a[242:243], v202 offset:50176\n\t" \
    "ds_read_b64_tr_b16 a[244:245], v202 offset:57344\n\t" \
    "ds_read_b64_tr_b16 a[246:247], v202 offset:58368\n\t" \
    "s_waitcnt vmcnt(19) lgkmcnt(0)\n\t" \
    "s_barrier\n\t" \
    "v_add_u32 v201, 0x10000, v201\n\t" \
    "v_add_u32 v202, 0x10000, v202\n\t" \
    "ds_read_b64_tr_b16 a[176:177], v201 offset:0\n\t" \
    "ds_read_b64_tr_b16 a[178:179], v201 offset:1024\n\t" \
    "ds_read_b64_tr_b16 a[180:181], v201 offset:8192\n\t" \
    "ds_read_b64_tr_b16 a[182:183], v201 offset:9216\n\t" \
    "ds_read_b64_tr_b16 a[248:249], v202 offset:0\n\t" \
    "ds_read_b64_tr_b16 a[250:251], v202 offset:1024\n\t" \
    "ds_read_b64_tr_b16 a[252:253], v202 offset:8192\n\t" \
    "ds_read_b64_tr_b16 a[254:255], v202 offset:9216\n\t" \
    "s_waitcnt vmcnt(15) lgkmcnt(0)\n\t" \
    "s_barrier\n\t" \
    "ds_read_b64_tr_b16 a[184:185], v201 offset:16384\n\t" \
    "ds_read_b64_tr_b16 a[186:187], v201 offset:17408\n\t" \
    "ds_read_b64_tr_b16 a[188:189], v201 offset:24576\n\t" \
    "ds_read_b64_tr_b16 a[190:191], v201 offset:25600\n\t" \
    "ds_read_b64_tr_b16 v[224:225], v202 offset:16384\n\t" \
    "ds_read_b64_tr_b16 v[226:227], v202 offset:17408\n\t" \
    "ds_read_b64_tr_b16 v[228:229], v202 offset:24576\n\t" \
    "ds_read_b64_tr_b16 v[230:231], v202 offset:25600\n\t" \
    "s_waitcnt vmcnt(11) lgkmcnt(0)\n\t" \
    "s_barrier\n\t" \
    "ds_read_b64_tr_b16 a[192:193], v201 offset:32768\n\t" \
    "ds_read_b64_tr_b16 a[194:195], v201 offset:33792\n\t" \
    "ds_read_b64_tr_b16 a[196:197], v201 offset:40960\n\t" \
    "ds_read_b64_tr_b16 a[198:199], v201 offset:41984\n\t" \
    "ds_read_b64_tr_b16 v[232:233], v202 offset:32768\n\t" \
    "ds_read_b64_tr_b16 v[234:235], v202 offset:33792\n\t" \
    "ds_read_b64_tr_b16 v[236:237], v202 offset:40960\n\t" \
    "ds_read_b64_tr_b16 v[238:239], v202 offset:41984\n\t" \
    "s_waitcnt vmcnt(8)\n\t" \
    "qk_C2_%=:\n\t" \
    "v_cmp_ne_u32 s[54:55], v162, v164\n\t" \
    "v_sub_u32 v162, v162, v161\n\t" \
    "v_cmp_ne_u32 vcc, 1, v162\n\t" \
    "s_nop 1\n\t" \
    "s_or_b64 vcc, vcc, s[54:55]\n\t" \
    "s_cbranch_scc0 qk_Cok3_%=\n\t" \
    "s_cmp_eq_u32 s68, 0\n\t" \
    "s_cbranch_scc1 qk_F5_%=\n\t" \
    "s_sub_u32 s68, s68, 1\n\t" \
    "s_sleep 6\n\t" \
    "s_branch qk_Cag4_%=\n\t" \
    "qk_F5_%=:\n\t" \
    "v_mov_b32 v164, 5767424\n\t" \
    "v_mov_b32 v165, 4\n\t" \
    "global_store_dword v164, v165, s[36:37] sc1\n\t" \
    "s_waitcnt vmcnt(0)\n\t" \
    "s_branch qk_Cok3_%=\n\t" \
    "qk_Cag4_%=:\n\t" \
    "global_load_dwordx2 v[162:163], v160, s[36:37] sc1\n\t" \
    "global_load_dword v164, v160, s[36:37] offset:2048 sc1\n\t" \
    "s_waitcnt vmcnt(0)\n\t" \
    "s_branch qk_C2_%=\n\t" \
    "qk_Cok3_%=:\n\t" \
    "v_readlane_b32 s69, v163, 2\n\t" \
    "v_readlane_b32 s70, v163, 3\n\t" \
    "s_nop 1\n\t" \
    "s_cmp_lt_u32 s70, s69\n\t" \
    "s_cselect_b32 s69, 0, 8192\n\t" \
    "s_add_u32 s100, s36, s69\n\t" \
    "s_addc_u32 s101, s37, 0\n\t" \
    "s_cmp_lg_u32 s66, 0\n\t" \
    "s_cbranch_scc1 qk_Ca6_%=\n\t" \
    "v_mov_b32 v160, 5768192\n\t" \
    "s_mov_b64 exec, 1\n\t" \
    "global_atomic_add v160, v162, s[36:37] sc1\n\t" \
    "s_mov_b64 exec, -1\n\t" \
    "qk_Ca6_%=:\n\t" \
    "v_mov_b32_dpp v160, v163 quad_perm:[1,0,3,2] row_mask:0xf bank_mask:0xf\n\t" \
    "v_cmp_lt_u32 s[44:45], v160, v163\n\t" \
    "s_nop 1\n\t" \
    "s_bitcmp1_b32 s44, 0\n\t" \
    "s_cselect_b32 s62, 8192, 0\n\t" \
    "s_bitcmp1_b32 s44, 2\n\t" \
    "s_cselect_b32 s63, 8192, 0\n\t" \
    "s_or_b32 s63, s63, 0x200000\n\t" \
    "v_min_u32 v160, v160, v163\n\t" \
    "v_cmp_lt_u32 vcc, 660, v160\n\t" \
    "s_nop 1\n\t" \
    "s_bcnt1_i32_b64 s69, vcc\n\t" \
    "s_cmp_gt_u32 s69, 16\n\t" \
    "s_cselect_b32 s69, 1, 0\n\t" \
    "s_mul_i32 s70, s69, 160\n\t" \
    "s_add_u32 s86, s86, s70\n\t" \
    "s_mul_i32 s70, s69, 536\n\t" \
    "s_add_u32 s87, s87, s70\n\t" \
    "s_mul_i32 s70, s69, 256\n\t" \
    "s_add_u32 s88, s88, s70\n\t" \
    "global_load_dword v25, v24, s[100:101] sc1\n\t" \
    "s_waitcnt vmcnt(5) lgkmcnt(0)\n\t" \
    "s_barrier\n\t" \
    "ds_read_b64_tr_b16 a[200:201], v201 offset:49152\n\t" \
    "ds_read_b64_tr_b16 a[202:203], v201 offset:50176\n\t" \
    "ds_read_b64_tr_b16 a[204:205], v201 offset:57344\n\t" \
    "ds_read_b64_tr_b16 a[206:207], v201 offset:58368\n\t" \
    "ds_read_b64_tr_b16 v[240:241], v202 offset:49152\n\t" \
    "ds_read_b64_tr_b16 v[242:243], v202 offset:50176\n\t" \
    "ds_read_b64_tr_b16 v[244:245], v202 offset:57344\n\t" \
    "ds_read_b64_tr_b16 v[246:247], v202 offset:58368\n\t" \
    "s_waitcnt vmcnt(1) lgkmcnt(0)\n\t" \
    "s_barrier\n\t" \
    "v_add_u32 v201, 0x10000, v201\n\t" \
    "v_add_u32 v202, 0x10000, v202\n\t" \
    "ds_read_b64_tr_b16 a[208:209], v201 offset:0\n\t" \
    "ds_read_b64_tr_b16 a[210:211], v201 offset:1024\n\t" \
    "ds_read_b64_tr_b16 a[212:213], v201 offset:8192\n\t" \
    "ds_read_b64_tr_b16 a[214:215], v201 offset:9216\n\t" \
    "ds_read_b64_tr_b16 v[248:249], v202 offset:0\n\t" \
    "ds_read_b64_tr_b16 v[250:251], v202 offset:1024\n\t" \
    "ds_read_b64_tr_b16 v[252:253], v202 offset:8192\n\t" \
    "ds_read_b64_tr_b16 v[254:255], v202 offset:9216\n\t" \
    "v_add_u32 v201, 0xfffe0000, v201\n\t" \
    "v_add_u32 v202, 0xfffe0000, v202\n\t" \
    "s_waitcnt lgkmcnt(0)\n\t" \
    "s_barrier\n\t" \
    "s_mul_i32 s70, s67, 7\n\t" \
    "s_add_u32 s69, s65, 0\n\t" \
    "s_xor_b32 s69, s69, s70\n\t" \
    "s_and_b32 s69, s69, 63\n\t" \
    "s_lshl_b32 s69, s69, 13\n\t" \
    "s_mul_i32 s70, s67, 524288\n\t" \
    "s_add_u32 s69, s69, s70\n\t" \
    "s_add_u32 s69, s69, 23068672\n\t" \
    "s_add_u32 s38, s36, s69\n\t" \
    "s_addc_u32 s39, s37, 0\n\t" \
    "v_mov_b32 v26, 0x3db504f3\n\t" \
    "v_mov_b32 v28, 0x3fb8aa3b\n\t" \
    "v_mov_b32 v27, 0x3db504f3\n\t" \
    "v_mov_b32 v29, 0x3fb8aa3b\n\t" \
    "v_xor_b32 v180, 65536, v18\n\t" \
    "v_xor_b32 v181, 131072, v18\n\t" \
    "v_xor_b32 v182, 196608, v18\n\t" \
    "v_add_u32 v34, 16384, v1\n\t" \
    "v_subrev_u32 v35, 16384, v2\n\t" \
    "s_mov_b32 s48, 0xaaaaaaaa\n\t" \
    "s_mov_b32 s49, 0xaaaaaaaa\n\t" \
    "s_mov_b32 s58, 0x00ff00ff\n\t" \
    "s_mov_b32 s59, 0x00ff00ff\n\t" \
    "v_mov_b32 v31, -1\n\t" \
    "s_mov_b32 s91, -1\n\t" \
    "ds_write_b32 v12, v31 offset:0\n\t" \
    "ds_write_b32 v12, v31 offset:64\n\t" \
    "s_waitcnt vmcnt(0)\n\t" \
    "s_cmp_gt_u32 s66, 1\n\t" \
    "s_cbranch_scc1 qk_nk7_%=\n\t" \
    "v_readfirstlane_b32 s83, v25\n\t" \
    "s_nop 1\n\t" \
    "qk_WF8_%=:\n\t" \
    "s_cmp_ge_i32 s83, s65\n\t" \
    "s_cbranch_scc1 qk_WFok9_%=\n\t" \
    "global_load_dword v160, v24, s[100:101] sc1\n\t" \
    "s_waitcnt vmcnt(0)\n\t" \
    "v_readfirstlane_b32 s83, v160\n\t" \
    "s_nop 1\n\t" \
    "s_cmp_ge_i32 s83, s65\n\t" \
    "s_cbranch_scc1 qk_WFok9_%=\n\t" \
    "s_cmp_eq_u32 s68, 0\n\t" \
    "s_cbranch_scc1 qk_F10_%=\n\t" \
    "s_sub_u32 s68, s68, 1\n\t" \
    "s_sleep 2\n\t" \
    "s_branch qk_WF8_%=\n\t" \
    "qk_F10_%=:\n\t" \
    "v_mov_b32 v160, 5767424\n\t" \
    "v_mov_b32 v161, 8\n\t" \
    "global_store_dword v160, v161, s[36:37] sc1\n\t" \
    "s_waitcnt vmcnt(0)\n\t" \
    "s_branch qk_WFok9_%=\n\t" \
    "qk_WFok9_%=:\n\t" \
    "global_load_dwordx4 v[100:103], v0, s[38:39] offset:0 sc1\n\t" \
    "global_load_dwordx4 v[104:107], v0, s[38:39] offset:2048 sc1\n\t" \
    "global_load_dwordx4 v[108:111], v15, s[38:39] offset:0 sc1\n\t" \
    "global_load_dwordx4 v[112:115], v15, s[38:39] offset:2048 sc1\n\t" \
    "global_load_dwordx4 v[116:119], v0, s[38:39] offset:1024 sc1\n\t" \
    "global_load_dwordx4 v[120:123], v0, s[38:39] offset:3072 sc1\n\t" \
    "global_load_dwordx4 v[124:127], v15, s[38:39] offset:1024 sc1\n\t" \
    "global_load_dwordx4 v[128:131], v15, s[38:39] offset:3072 sc1\n\t" \
    "qk_nk7_%=:\n\t" \
    "v_mov_b32 v33, v21\n\t" \
    "v_readlane_b32 s69, v21, 63\n\t" \
    "s_nop 1\n\t" \
    "v_mov_b32 v21, s69\n\t" \
    "s_mov_b32 s79, s74\n\t" \
    "s_mov_b32 s80, s74\n\t" \
    "s_cmp_eq_u32 s66, 1\n\t" \
    "s_cbranch_scc1 qk_loop12_%=\n\t" \
    "s_cmp_eq_u32 s66, 2\n\t" \
    "s_cbranch_scc1 qk_loop13_%=\n\t" \
    "s_cmp_eq_u32 s66, 3\n\t" \
    "s_cbranch_scc1 qk_loop14_%=\n\t" \
    "qk_loop11_%=:\n\t" \
    "v_mov_b32 v72, 0\n\t" \
    "v_mov_b32 v73, 0\n\t" \
    "v_mov_b32 v74, 0\n\t" \
    "v_mov_b32 v75, 0\n\t" \
    "v_mov_b32 v76, 0\n\t" \
    "v_mov_b32 v77, 0\n\t" \
    "v_mov_b32 v78, 0\n\t" \
    "v_mov_b32 v79, 0\n\t" \
    "v_mov_b32 v80, 0\n\t" \
    "v_mov_b32 v81, 0\n\t" \
    "v_mov_b32 v82, 0\n\t" \
    "v_mov_b32 v83, 0\n\t" \
    "v_mov_b32 v84, 0\n\t" \
    "v_mov_b32 v85, 0\n\t" \
    "v_mov_b32 v86, 0\n\t" \
    "v_mov_b32 v87, 0\n\t" \
    "v_mov_b32 v88, 0\n\t" \
    "v_mov_b32 v89, 0\n\t" \
    "v_mov_b32 v90, 0\n\t" \
    "v_mov_b32 v91, 0\n\t" \
    "v_mov_b32 v92, 0\n\t" \
    "v_mov_b32 v93, 0\n\t" \
    "v_mov_b32 v94, 0\n\t" \
    "v_mov_b32 v95, 0\n\t" \
    "v_mov_b32 v96, 0\n\t" \
    "v_mov_b32 v97, 0\n\t" \
    "v_mov_b32 v98, 0\n\t" \
    "v_mov_b32 v99, 0\n\t" \
    "s_add_u32 s69, s65, 0\n\t" \
    "s_and_b32 s69, s69, 7\n\t" \
    "s_lshl_b32 s69, s69, 18\n\t" \
    "s_add_u32 s69, s69, s62\n\t" \
    "s_add_u32 s46, s36, s69\n\t" \
    "s_addc_u32 s47, s37, 0\n\t" \
    "s_sub_u32 s70, s79, s74\n\t" \
    "s_max_i32 s70, s70, 0\n\t" \
    "s_min_i32 s70, s70, 16000\n\t" \
    "s_lshl_b32 s70, s70, 1\n\t" \
    "s_mul_hi_u32 s70, s70, s77\n\t" \
    "s_add_u32 s70, s70, s73\n\t" \
    ".p2align 6\n\t" \
    "s_nop 0\n\t" \
    "s_nop 0\n\t" \
    "s_nop 0\n\t" \
    "s_nop 0\n\t" \
    "s_nop 0\n\t" \
    "s_nop 0\n\t" \
    "qk_go16_%=:\n\t" \
    "s_getpc_b64 s[30:31]\n\t" \
    "qk_Wp18_%=:\n\t" \
    "s_add_u32 s30, s30, qk_We19_%=-qk_Wp18_%=\n\t" \
    "s_addc_u32 s31, s31, 0\n\t" \
    "s_sub_u32 s70, s70, 96\n\t" \
    "qk_W17_%=:\n\t" \
    "s_memtime s[56:57]\n\t" \
    "s_waitcnt lgkmcnt(0)\n\t" \
    "s_sub_u32 s69, s70, s56\n\t" \
    "s_cmp_gt_i32 s69, 80\n\t" \
    "s_cbranch_scc1 qk_W17_%=\n\t" \
    "s_max_i32 s69, s69, 0\n\t" \
    "s_and_b32 s69, s69, -4\n\t" \
    "s_sub_u32 s30, s30, s69\n\t" \
    "s_subb_u32 s31, s31, 0\n\t" \
    "s_setpc_b64 s[30:31]\n\t" \
    "s_nop 0\n\t" \
    "s_nop 0\n\t" \
    "s_nop 0\n\t" \
    "s_nop 0\n\t" \
    "s_nop 0\n\t" \
    "s_nop 0\n\t" \
    "s_nop 0\n\t" \
    "s_nop 0\n\t" \
    "s_nop 0\n\t" \
    "s_nop 0\n\t" \
    "s_nop 0\n\t" \
    "s_nop 0\n\t" \
    "s_nop 0\n\t" \
    "s_nop 0\n\t" \
    "s_nop 0\n\t" \
    "s_nop 0\n\t" \
    "s_nop 0\n\t" \
    "s_nop 0\n\t" \
    "s_nop 0\n\t" \
    "s_nop 0\n\t" \
    "qk_We19_%=:\n\t" \
    "qk_G20_%=:\n\t" \
    "global_load_dwordx4 v[36:39], v1, s[46:47] sc1\n\t" \
    "global_load_dword v161, v2, s[46:47] sc1\n\t" \
    "global_load_dwordx4 v[40:43], v23, s[46:47] sc1\n\t" \
    "s_waitcnt vmcnt(2)\n\t" \
    "qk_Garr25_%=:\n\t" \
    "ds_write_b128 v3, v[36:39]\n\t" \
    "v_smfmac_f32_16x16x64_bf16 v[72:75], v[36:39], a[0:7], v10\n\t" \
    "s_waitcnt vmcnt(1)\n\t" \
    "ds_write_b32 v22, v161 offset:19456\n\t" \
    "v_smfmac_f32_16x16x64_bf16 v[76:79], v[36:39], a[72:79], v10\n\t" \
    "s_waitcnt vmcnt(0)\n\t" \
    "ds_write_b128 v3, v[40:43] offset:1024\n\t" \
    "v_max3_u32 v160, v36, v40, v161\n\t" \
    "v_cmp_eq_u32 vcc, -1, v160\n\t" \
    "v_smfmac_f32_16x16x64_bf16 v[80:83], v[36:39], a[144:151], v10\n\t" \
    "v_smfmac_f32_16x16x64_bf16 v[84:87], v[36:39], a[216:223], v10\n\t" \
    "v_smfmac_f32_16x16x64_bf16 v[72:75], v[40:43], a[8:15], v10\n\t" \
    "s_cbranch_vccnz qk_Gbad21_%=\n\t" \
    "qk_Gc22_%=:\n\t" \
    "s_waitcnt lgkmcnt(0)\n\t" \
    "s_barrier\n\t" \
    "ds_read_b128 v[48:51], v4\n\t" \
    "ds_read_b128 v[52:55], v5\n\t" \
    "v_smfmac_f32_16x16x64_bf16 v[76:79], v[40:43], a[80:87], v10\n\t" \
    "ds_read_b128 v[56:59], v6\n\t" \
    "ds_read_b128 v[60:63], v7\n\t" \
    "v_smfmac_f32_16x16x64_bf16 v[80:83], v[40:43], a[152:159], v10\n\t" \
    "ds_read_b128 v[64:67], v8\n\t" \
    "v_smfmac_f32_16x16x64_bf16 v[84:87], v[40:43], a[224:231], v10\n\t" \
    "ds_read_b128 v[68:71], v9\n\t" \
    "s_waitcnt lgkmcnt(5)\n\t" \
    "v_smfmac_f32_16x16x64_bf16 v[72:75], v[48:51], a[16:23], v10\n\t" \
    "ds_read_b128 v[44:47], v0 offset:19456\n\t" \
    "s_waitcnt lgkmcnt(5)\n\t" \
    "v_smfmac_f32_16x16x64_bf16 v[72:75], v[52:55], a[24:31], v10\n\t" \
    "s_waitcnt lgkmcnt(4)\n\t" \
    "v_smfmac_f32_16x16x64_bf16 v[72:75], v[56:59], a[32:39], v10\n\t" \
    "s_waitcnt lgkmcnt(3)\n\t" \
    "v_smfmac_f32_16x16x64_bf16 v[72:75], v[60:63], a[40:47], v10\n\t" \
    "s_waitcnt lgkmcnt(2)\n\t" \
    "v_smfmac_f32_16x16x64_bf16 v[72:75], v[64:67], a[48:55], v10\n\t" \
    "s_waitcnt lgkmcnt(1)\n\t" \
    "v_smfmac_f32_16x16x64_bf16 v[72:75], v[68:71], a[56:63], v10\n\t" \
    "s_waitcnt lgkmcnt(0)\n\t" \
    "v_smfmac_f32_16x16x64_bf16 v[72:75], v[44:47], a[64:71], v10\n\t" \
    "s_nop 2\n\t" \
    "v_smfmac_f32_16x16x64_bf16 v[76:79], v[48:51], a[88:95], v10\n\t" \
    "s_nop 2\n\t" \
    "v_smfmac_f32_16x16x64_bf16 v[76:79], v[52:55], a[96:103], v10\n\t" \
    "v_add_f32 v72, v72, v74\n\t" \
    "v_smfmac_f32_16x16x64_bf16 v[76:79], v[56:59], a[104:111], v10\n\t" \
    "v_add_f32 v73, v73, v75\n\t" \
    "v_smfmac_f32_16x16x64_bf16 v[76:79], v[60:63], a[112:119], v10\n\t" \
    "v_cvt_pk_bf16_f32 v156, v72, v73\n\t" \
    "v_smfmac_f32_16x16x64_bf16 v[76:79], v[64:67], a[120:127], v10\n\t" \
    "ds_write_b32 v12, v156 offset:0\n\t" \
    "v_smfmac_f32_16x16x64_bf16 v[76:79], v[68:71], a[128:135], v10\n\t" \
    "v_smfmac_f32_16x16x64_bf16 v[76:79], v[44:47], a[136:143], v10\n\t" \
    "s_nop 2\n\t" \
    "v_smfmac_f32_16x16x64_bf16 v[80:83], v[48:51], a[160:167], v10\n\t" \
    "s_nop 2\n\t" \
    "v_smfmac_f32_16x16x64_bf16 v[80:83], v[52:55], a[168:175], v10\n\t" \
    "v_add_f32 v76, v76, v78\n\t" \
    "v_smfmac_f32_16x16x64_bf16 v[80:83], v[56:59], a[176:183], v10\n\t" \
    "v_add_f32 v77, v77, v79\n\t" \
    "v_smfmac_f32_16x16x64_bf16 v[80:83], v[60:63], a[184:191], v10\n\t" \
    "v_cvt_pk_bf16_f32 v157, v76, v77\n\t" \
    "v_smfmac_f32_16x16x64_bf16 v[80:83], v[64:67], a[192:199], v10\n\t" \
    "ds_write_b32 v12, v157 offset:64\n\t" \
    "v_smfmac_f32_16x16x64_bf16 v[80:83], v[68:71], a[200:207], v10\n\t" \
    "v_smfmac_f32_16x16x64_bf16 v[80:83], v[44:47], a[208:215], v10\n\t" \
    "s_nop 2\n\t" \
    "v_smfmac_f32_16x16x64_bf16 v[84:87], v[48:51], a[232:239], v10\n\t" \
    "s_nop 2\n\t" \
    "v_smfmac_f32_16x16x64_bf16 v[84:87], v[52:55], a[240:247], v10\n\t" \
    "v_add_f32 v80, v80, v82\n\t" \
    "v_smfmac_f32_16x16x64_bf16 v[84:87], v[56:59], a[248:255], v10\n\t" \
    "ds_read_b64_tr_b16 v[132:133], v13 offset:0\n\t" \
    "v_add_f32 v81, v81, v83\n\t" \
    "v_smfmac_f32_16x16x64_bf16 v[84:87], v[60:63], v[224:231], v10\n\t" \
    "ds_read_b64_tr_b16 v[134:135], v13 offset:128\n\t" \
    "v_cvt_pk_bf16_f32 v158, v80, v81\n\t" \
    "v_smfmac_f32_16x16x64_bf16 v[84:87], v[64:67], v[232:239], v10\n\t" \
    "ds_write_b32 v16, v158 offset:0\n\t" \
    "v_smfmac_f32_16x16x64_bf16 v[84:87], v[68:71], v[240:247], v10\n\t" \
    "ds_read_b64_tr_b16 v[136:137], v13 offset:1024\n\t" \
    "ds_read_b64_tr_b16 v[138:139], v13 offset:1152\n\t" \
    "v_smfmac_f32_16x16x64_bf16 v[84:87], v[44:47], v[248:255], v10\n\t" \
    "s_waitcnt lgkmcnt(3)\n\t" \
    "v_smfmac_f32_16x16x64_bf16 v[88:91], v[132:135], v[100:107], v10\n\t" \
    "s_nop 4\n\t" \
    "v_cmp_u_f32 vcc, v72, v73\n\t" \
    "v_pk_add_f32 v[84:85], v[84:85], v[86:87]\n\t" \
    "v_cvt_pk_bf16_f32 v159, v84, v85\n\t" \
    "ds_write_b32 v16, v159 offset:256\n\t" \
    "v_smfmac_f32_16x16x64_bf16 v[92:95], v[132:135], v[116:123], v10\n\t" \
    "s_waitcnt lgkmcnt(1)\n\t" \
    "v_smfmac_f32_16x16x64_bf16 v[88:91], v[136:139], v[108:115], v10\n\t" \
    "s_cbranch_vccnz qk_Gslow23_%=\n\t" \
    "qk_Gback24_%=:\n\t" \
    "v_smfmac_f32_16x16x64_bf16 v[92:95], v[136:139], v[124:131], v10\n\t" \
    "s_waitcnt lgkmcnt(0)\n\t" \
    "s_barrier\n\t" \
    "ds_read_b64_tr_b16 v[140:141], v17 offset:0\n\t" \
    "ds_read_b64_tr_b16 v[142:143], v17 offset:256\n\t" \
    "ds_read_b64_tr_b16 v[144:145], v17 offset:512\n\t" \
    "ds_read_b64_tr_b16 v[146:147], v17 offset:768\n\t" \
    "ds_read_b64_tr_b16 v[148:149], v17 offset:1024\n\t" \
    "ds_read_b64_tr_b16 v[150:151], v17 offset:1280\n\t" \
    "ds_read_b64_tr_b16 v[152:153], v17 offset:1536\n\t" \
    "ds_read_b64_tr_b16 v[154:155], v17 offset:1792\n\t" \
    "qk_qb27_%=:\n\t" \
    "v_add_f32 v168, v88, v90\n\t" \
    "v_add_f32 v169, v92, v94\n\t" \
    "v_cmp_u_f32 vcc, v168, v169\n\t" \
    "v_cvt_pk_bf16_f32 v168, 0, v168\n\t" \
    "v_cvt_pk_bf16_f32 v169, 0, v169\n\t" \
    "s_waitcnt lgkmcnt(4)\n\t" \
    "v_smfmac_f32_16x16x64_bf16 v[96:99], v[132:135], v[140:147], v10\n\t" \
    "s_waitcnt lgkmcnt(0)\n\t" \
    "v_smfmac_f32_16x16x64_bf16 v[96:99], v[136:139], v[148:155], v10\n\t" \
    "s_cbranch_vccnz qk_qs26_%=\n\t" \
    "qk_qb228_%=:\n\t" \
    "v_mul_f32 v168, v168, v26\n\t" \
    "v_mul_f32 v169, v169, v26\n\t" \
    "v_cvt_pk_bf16_f32 v168, 0, v168\n\t" \
    "v_cvt_pk_bf16_f32 v169, 0, v169\n\t" \
    "v_max_f32 v178, v168, v169\n\t" \
    "v_mul_f32 v168, v168, v28\n\t" \
    "v_mul_f32 v169, v169, v28\n\t" \
    "v_max_f32_dpp v178, v178, v178 row_mirror row_mask:0xf bank_mask:0xf\n\t" \
    "v_add_f32 v172, v96, v98\n\t" \
    "v_cvt_pk_bf16_f32 v172, 0, v172\n\t" \
    "v_mul_f32 v172, v172, v26\n\t" \
    "v_cvt_pk_bf16_f32 v172, 0, v172\n\t" \
    "v_max_f32 v173, v178, v172\n\t" \
    "v_mul_f32 v172, v172, v28\n\t" \
    "s_nop 0\n\t" \
    "v_max_f32_dpp v173, v173, v173 quad_perm:[1,0,3,2] row_mask:0xf bank_mask:0xf\n\t" \
    "s_nop 1\n\t" \
    "v_max_f32_dpp v173, v173, v173 quad_perm:[2,3,0,1] row_mask:0xf bank_mask:0xf\n\t" \
    "s_nop 1\n\t" \
    "v_max_f32_dpp v173, v173, v173 row_half_mirror row_mask:0xf bank_mask:0xf\n\t" \
    "v_fma_f32 v172, -v173, v28, v172\n\t" \
    "v_fma_f32 v168, -v173, v28, v168\n\t" \
    "v_fma_f32 v169, -v173, v28, v169\n\t" \
    "v_exp_f32 v172, v172\n\t" \
    "v_exp_f32 v168, v168\n\t" \
    "v_exp_f32 v169, v169\n\t" \
    "v_fma_f32 v173, v172, 0.5, v168\n\t" \
    "v_add_f32 v173, v173, v169\n\t" \
    "v_mov_b32_dpp v174, v172 quad_perm:[0,0,2,2] row_mask:0xf bank_mask:0xf\n\t" \
    "v_mov_b32_dpp v175, v172 quad_perm:[1,1,3,3] row_mask:0xf bank_mask:0xf\n\t" \
    "v_add_f32_dpp v173, v173, v173 quad_perm:[1,0,3,2] row_mask:0xf bank_mask:0xf\n\t" \
    "v_mov_b32_dpp v176, v168 quad_perm:[1,0,3,2] row_mask:0xf bank_mask:0xf\n\t" \
    "v_mov_b32_dpp v177, v169 quad_perm:[1,0,3,2] row_mask:0xf bank_mask:0xf\n\t" \
    "v_add_f32_dpp v173, v173, v173 quad_perm:[2,3,0,1] row_mask:0xf bank_mask:0xf\n\t" \
    "v_cndmask_b32 v168, v168, v177, s[48:49]\n\t" \
    "v_cndmask_b32 v169, v176, v169, s[48:49]\n\t" \
    "v_add_f32_dpp v173, v173, v173 row_half_mirror row_mask:0xf bank_mask:0xf\n\t" \
    "s_nop 1\n\t" \
    "v_add_f32_dpp v173, v173, v173 row_mirror row_mask:0xf bank_mask:0xf\n\t" \
    "v_rcp_f32 v173, v173\n\t" \
    "s_nop 0\n\t" \
    "v_pk_mul_f32 v[168:169], v[168:169], v[172:173] op_sel:[0,1] op_sel_hi:[1,1]\n\t" \
    "v_cvt_pk_bf16_f32 v176, v168, v169\n\t" \
    "global_store_dword v18, v176, s[46:47] sc1\n\t" \
    "global_store_dword v180, v176, s[46:47] sc1\n\t" \
    "global_store_dword v181, v176, s[46:47] sc1\n\t" \
    "global_store_dword v182, v176, s[46:47] sc1\n\t" \
    "v_pk_mul_f32 v[174:175], v[174:175], v[172:173] op_sel:[0,1] op_sel_hi:[1,1]\n\t" \
    "v_cvt_pk_bf16_f32 v177, v174, v175\n\t" \
    "global_store_dword v19, v177, s[46:47] sc1\n\t" \
    "s_cmp_eq_u32 s65, 63\n\t" \
    "s_cbranch_scc1 qk_last15_%=\n\t" \
    "v_readfirstlane_b32 s69, v25\n\t" \
    "s_add_u32 s65, s65, 1\n\t" \
    "s_max_i32 s83, s83, s69\n\t" \
    "s_cmp_gt_u32 s65, 7\n\t" \
    "s_cselect_b32 s83, 99, s83\n\t" \
    "v_mov_b32 v72, 0\n\t" \
    "v_mov_b32 v73, 0\n\t" \
    "v_mov_b32 v74, 0\n\t" \
    "v_mov_b32 v75, 0\n\t" \
    "v_mov_b32 v76, 0\n\t" \
    "v_mov_b32 v77, 0\n\t" \
    "v_mov_b32 v78, 0\n\t" \
    "v_mov_b32 v79, 0\n\t" \
    "v_mov_b32 v80, 0\n\t" \
    "v_mov_b32 v81, 0\n\t" \
    "v_mov_b32 v82, 0\n\t" \
    "v_mov_b32 v83, 0\n\t" \
    "v_mov_b32 v84, 0\n\t" \
    "v_mov_b32 v85, 0\n\t" \
    "v_mov_b32 v86, 0\n\t" \
    "v_mov_b32 v87, 0\n\t" \
    "v_mov_b32 v88, 0\n\t" \
    "v_mov_b32 v89, 0\n\t" \
    "v_mov_b32 v90, 0\n\t" \
    "v_mov_b32 v91, 0\n\t" \
    "v_mov_b32 v92, 0\n\t" \
    "v_mov_b32 v93, 0\n\t" \
    "v_mov_b32 v94, 0\n\t" \
    "v_mov_b32 v95, 0\n\t" \
    "v_mov_b32 v96, 0\n\t" \
    "v_mov_b32 v97, 0\n\t" \
    "v_mov_b32 v98, 0\n\t" \
    "v_mov_b32 v99, 0\n\t" \
    "s_add_u32 s69, s65, 0\n\t" \
    "s_and_b32 s69, s69, 7\n\t" \
    "s_lshl_b32 s69, s69, 18\n\t" \
    "s_add_u32 s69, s69, s62\n\t" \
    "s_add_u32 s46, s36, s69\n\t" \
    "s_addc_u32 s47, s37, 0\n\t" \
    "s_mul_i32 s70, s67, 7\n\t" \
    "s_add_u32 s69, s65, 0\n\t" \
    "s_xor_b32 s69, s69, s70\n\t" \
    "s_and_b32 s69, s69, 63\n\t" \
    "s_lshl_b32 s69, s69, 13\n\t" \
    "s_mul_i32 s70, s67, 524288\n\t" \
    "s_add_u32 s69, s69, s70\n\t" \
    "s_add_u32 s69, s69, 23068672\n\t" \
    "s_add_u32 s38, s36, s69\n\t" \
    "s_addc_u32 s39, s37, 0\n\t" \
    "qk_WF29_%=:\n\t" \
    "s_cmp_ge_i32 s83, s65\n\t" \
    "s_cbranch_scc1 qk_WFok30_%=\n\t" \
    "global_load_dword v160, v24, s[100:101] sc1\n\t" \
    "s_waitcnt vmcnt(0)\n\t" \
    "v_readfirstlane_b32 s83, v160\n\t" \
    "s_nop 1\n\t" \
    "s_cmp_ge_i32 s83, s65\n\t" \
    "s_cbranch_scc1 qk_WFok30_%=\n\t" \
    "s_cmp_eq_u32 s68, 0\n\t" \
    "s_cbranch_scc1 qk_F31_%=\n\t" \
    "s_sub_u32 s68, s68, 1\n\t" \
    "s_sleep 2\n\t" \
    "s_branch qk_WF29_%=\n\t" \
    "qk_F31_%=:\n\t" \
    "v_mov_b32 v160, 5767424\n\t" \
    "v_mov_b32 v161, 9\n\t" \
    "global_store_dword v160, v161, s[36:37] sc1\n\t" \
    "s_waitcnt vmcnt(0)\n\t" \
    "s_branch qk_WFok30_%=\n\t" \
    "qk_WFok30_%=:\n\t" \
    "global_load_dwordx4 v[100:103], v0, s[38:39] offset:0 sc1\n\t" \
    "global_load_dwordx4 v[104:107], v0, s[38:39] offset:2048 sc1\n\t" \
    "global_load_dwordx4 v[108:111], v15, s[38:39] offset:0 sc1\n\t" \
    "global_load_dwordx4 v[112:115], v15, s[38:39] offset:2048 sc1\n\t" \
    "global_load_dwordx4 v[116:119], v0, s[38:39] offset:1024 sc1\n\t" \
    "global_load_dwordx4 v[120:123], v0, s[38:39] offset:3072 sc1\n\t" \
    "global_load_dwordx4 v[124:127], v15, s[38:39] offset:1024 sc1\n\t" \
    "global_load_dwordx4 v[128:131], v15, s[38:39] offset:3072 sc1\n\t" \
    "s_cmp_gt_i32 s83, s65\n\t" \
    "s_cbranch_scc1 qk_nf32_%=\n\t" \
    "global_load_dword v25, v24, s[100:101] sc1\n\t" \
    "qk_nf32_%=:\n\t" \
    "s_barrier\n\t" \
    "ds_read_b32 v168, v11 offset:20480\n\t" \
    "s_waitcnt lgkmcnt(0)\n\t" \
    "v_readfirstlane_b32 s70, v168\n\t" \
    "ds_write_b32 v12, v31 offset:0\n\t" \
    "ds_write_b32 v12, v31 offset:64\n\t" \
    "s_branch qk_go16_%=\n\t" \
    "qk_Gbad21_%=:\n\t" \
    "s_waitcnt vmcnt(0) lgkmcnt(0)\n\t" \
    "s_nop 7\n\t" \
    "v_mov_b32 v72, 0\n\t" \
    "v_mov_b32 v73, 0\n\t" \
    "v_mov_b32 v74, 0\n\t" \
    "v_mov_b32 v75, 0\n\t" \
    "v_mov_b32 v76, 0\n\t" \
    "v_mov_b32 v77, 0\n\t" \
    "v_mov_b32 v78, 0\n\t" \
    "v_mov_b32 v79, 0\n\t" \
    "v_mov_b32 v80, 0\n\t" \
    "v_mov_b32 v81, 0\n\t" \
    "v_mov_b32 v82, 0\n\t" \
    "v_mov_b32 v83, 0\n\t" \
    "v_mov_b32 v84, 0\n\t" \
    "v_mov_b32 v85, 0\n\t" \
    "v_mov_b32 v86, 0\n\t" \
    "v_mov_b32 v87, 0\n\t" \
    "s_cmp_eq_u32 s68, 0\n\t" \
    "s_cbranch_scc1 qk_F33_%=\n\t" \
    "s_sub_u32 s68, s68, 1\n\t" \
    "s_branch qk_G20_%=\n\t" \
    "qk_F33_%=:\n\t" \
    "v_mov_b32 v160, 5767424\n\t" \
    "v_mov_b32 v161, 1\n\t" \
    "global_store_dword v160, v161, s[36:37] sc1\n\t" \
    "s_waitcnt vmcnt(0)\n\t" \
    "s_branch qk_Gc22_%=\n\t" \
    "qk_Gslow23_%=:\n\t" \
    "v_max_u32 v160, v36, v37\n\t" \
    "v_max3_u32 v160, v160, v38, v39\n\t" \
    "v_max3_u32 v160, v160, v40, v41\n\t" \
    "v_max3_u32 v160, v160, v42, v43\n\t" \
    "v_max3_u32 v160, v160, v44, v45\n\t" \
    "v_max3_u32 v160, v160, v46, v47\n\t" \
    "v_max3_u32 v160, v160, v48, v49\n\t" \
    "v_max3_u32 v160, v160, v50, v51\n\t" \
    "v_max3_u32 v160, v160, v52, v53\n\t" \
    "v_max3_u32 v160, v160, v54, v55\n\t" \
    "v_max3_u32 v160, v160, v56, v57\n\t" \
    "v_max3_u32 v160, v160, v58, v59\n\t" \
    "v_max3_u32 v160, v160, v60, v61\n\t" \
    "v_max3_u32 v160, v160, v62, v63\n\t" \
    "v_max3_u32 v160, v160, v64, v65\n\t" \
    "v_max3_u32 v160, v160, v66, v67\n\t" \
    "v_max3_u32 v160, v160, v68, v69\n\t" \
    "v_max3_u32 v160, v160, v70, v71\n\t" \
    "v_cmp_eq_u32 vcc, -1, v160\n\t" \
    "s_cbranch_vccz qk_Gback24_%=\n\t" \
    "s_waitcnt lgkmcnt(0)\n\t" \
    "v_sub_u32 v160, v1, v3\n\t" \
    "s_nop 0\n\t" \
    "v_readfirstlane_b32 s69, v160\n\t" \
    "v_add_u32 v163, 20480, v0\n\t" \
    "s_nop 7\n\t" \
    "s_add_u32 s30, s46, s69\n\t" \
    "s_addc_u32 s31, s47, 0\n\t" \
    "qk_Gag34_%=:\n\t" \
    "global_load_dwordx4 v[36:39], v1, s[46:47] sc1\n\t" \
    "global_load_dwordx4 v[40:43], v23, s[46:47] sc1\n\t" \
    "global_load_dwordx4 v[44:47], v163, s[30:31] sc1\n\t" \
    "global_load_dwordx4 v[48:51], v4, s[30:31] sc1\n\t" \
    "global_load_dwordx4 v[52:55], v5, s[30:31] sc1\n\t" \
    "global_load_dwordx4 v[56:59], v6, s[30:31] sc1\n\t" \
    "global_load_dwordx4 v[60:63], v7, s[30:31] sc1\n\t" \
    "global_load_dwordx4 v[64:67], v8, s[30:31] sc1\n\t" \
    "global_load_dwordx4 v[68:71], v9, s[30:31] sc1\n\t" \
    "s_waitcnt vmcnt(0)\n\t" \
    "v_max_u32 v160, v36, v37\n\t" \
    "v_max3_u32 v160, v160, v38, v39\n\t" \
    "v_max3_u32 v160, v160, v40, v41\n\t" \
    "v_max3_u32 v160, v160, v42, v43\n\t" \
    "v_max3_u32 v160, v160, v44, v45\n\t" \
    "v_max3_u32 v160, v160, v46, v47\n\t" \
    "v_max3_u32 v160, v160, v48, v49\n\t" \
    "v_max3_u32 v160, v160, v50, v51\n\t" \
    "v_max3_u32 v160, v160, v52, v53\n\t" \
    "v_max3_u32 v160, v160, v54, v55\n\t" \
    "v_max3_u32 v160, v160, v56, v57\n\t" \
    "v_max3_u32 v160, v160, v58, v59\n\t" \
    "v_max3_u32 v160, v160, v60, v61\n\t" \
    "v_max3_u32 v160, v160, v62, v63\n\t" \
    "v_max3_u32 v160, v160, v64, v65\n\t" \
    "v_max3_u32 v160, v160, v66, v67\n\t" \
    "v_max3_u32 v160, v160, v68, v69\n\t" \
    "v_max3_u32 v160, v160, v70, v71\n\t" \
    "v_cmp_eq_u32 vcc, -1, v160\n\t" \
    "s_cbranch_vccz qk_Gredo35_%=\n\t" \
    "s_cmp_eq_u32 s68, 0\n\t" \
    "s_cbranch_scc1 qk_F36_%=\n\t" \
    "s_sub_u32 s68, s68, 1\n\t" \
    "s_sleep 1\n\t" \
    "s_branch qk_Gag34_%=\n\t" \
    "qk_F36_%=:\n\t" \
    "v_mov_b32 v160, 5767424\n\t" \
    "v_mov_b32 v161, 2\n\t" \
    "global_store_dword v160, v161, s[36:37] sc1\n\t" \
    "s_waitcnt vmcnt(0)\n\t" \
    "s_branch qk_Gredo35_%=\n\t" \
    "qk_Gredo35_%=:\n\t" \
    "v_mov_b32 v72, 0\n\t" \
    "v_mov_b32 v73, 0\n\t" \
    "v_mov_b32 v74, 0\n\t" \
    "v_mov_b32 v75, 0\n\t" \
    "v_mov_b32 v76, 0\n\t" \
    "v_mov_b32 v77, 0\n\t" \
    "v_mov_b32 v78, 0\n\t" \
    "v_mov_b32 v79, 0\n\t" \
    "v_mov_b32 v80, 0\n\t" \
    "v_mov_b32 v81, 0\n\t" \
    "v_mov_b32 v82, 0\n\t" \
    "v_mov_b32 v83, 0\n\t" \
    "v_mov_b32 v84, 0\n\t" \
    "v_mov_b32 v85, 0\n\t" \
    "v_mov_b32 v86, 0\n\t" \
    "v_mov_b32 v87, 0\n\t" \
    "v_smfmac_f32_16x16x64_bf16 v[72:75], v[36:39], a[0:7], v10\n\t" \
    "v_smfmac_f32_16x16x64_bf16 v[72:75], v[40:43], a[8:15], v10\n\t" \
    "v_smfmac_f32_16x16x64_bf16 v[72:75], v[48:51], a[16:23], v10\n\t" \
    "v_smfmac_f32_16x16x64_bf16 v[72:75], v[52:55], a[24:31], v10\n\t" \
    "v_smfmac_f32_16x16x64_bf16 v[72:75], v[56:59], a[32:39], v10\n\t" \
    "v_smfmac_f32_16x16x64_bf16 v[72:75], v[60:63], a[40:47], v10\n\t" \
    "v_smfmac_f32_16x16x64_bf16 v[72:75], v[64:67], a[48:55], v10\n\t" \
    "v_smfmac_f32_16x16x64_bf16 v[72:75], v[68:71], a[56:63], v10\n\t" \
    "v_smfmac_f32_16x16x64_bf16 v[72:75], v[44:47], a[64:71], v10\n\t" \
    "v_smfmac_f32_16x16x64_bf16 v[76:79], v[36:39], a[72:79], v10\n\t" \
    "v_smfmac_f32_16x16x64_bf16 v[76:79], v[40:43], a[80:87], v10\n\t" \
    "v_smfmac_f32_16x16x64_bf16 v[76:79], v[48:51], a[88:95], v10\n\t" \
    "v_smfmac_f32_16x16x64_bf16 v[76:79], v[52:55], a[96:103], v10\n\t" \
    "v_smfmac_f32_16x16x64_bf16 v[76:79], v[56:59], a[104:111], v10\n\t" \
    "v_smfmac_f32_16x16x64_bf16 v[76:79], v[60:63], a[112:119], v10\n\t" \
    "v_smfmac_f32_16x16x64_bf16 v[76:79], v[64:67], a[120:127], v10\n\t" \
    "v_smfmac_f32_16x16x64_bf16 v[76:79], v[68:71], a[128:135], v10\n\t" \
    "v_smfmac_f32_16x16x64_bf16 v[76:79], v[44:47], a[136:143], v10\n\t" \
    "v_smfmac_f32_16x16x64_bf16 v[80:83], v[36:39], a[144:151], v10\n\t" \
    "v_smfmac_f32_16x16x64_bf16 v[80:83], v[40:43], a[152:159], v10\n\t" \
    "v_smfmac_f32_16x16x64_bf16 v[80:83], v[48:51], a[160:167], v10\n\t" \
    "v_smfmac_f32_16x16x64_bf16 v[80:83], v[52:55], a[168:175], v10\n\t" \
    "v_smfmac_f32_16x16x64_bf16 v[80:83], v[56:59], a[176:183], v10\n\t" \
    "v_smfmac_f32_16x16x64_bf16 v[80:83], v[60:63], a[184:191], v10\n\t" \
    "v_smfmac_f32_16x16x64_bf16 v[80:83], v[64:67], a[192:199], v10\n\t" \
    "v_smfmac_f32_16x16x64_bf16 v[80:83], v[68:71], a[200:207], v10\n\t" \
    "v_smfmac_f32_16x16x64_bf16 v[80:83], v[44:47], a[208:215], v10\n\t" \
    "v_smfmac_f32_16x16x64_bf16 v[84:87], v[36:39], a[216:223], v10\n\t" \
    "v_smfmac_f32_16x16x64_bf16 v[84:87], v[40:43], a[224:231], v10\n\t" \
    "v_smfmac_f32_16x16x64_bf16 v[84:87], v[48:51], a[232:239], v10\n\t" \
    "v_smfmac_f32_16x16x64_bf16 v[84:87], v[52:55], a[240:247], v10\n\t" \
    "v_smfmac_f32_16x16x64_bf16 v[84:87], v[56:59], a[248:255], v10\n\t" \
    "v_smfmac_f32_16x16x64_bf16 v[84:87], v[60:63], v[224:231], v10\n\t" \
    "v_smfmac_f32_16x16x64_bf16 v[84:87], v[64:67], v[232:239], v10\n\t" \
    "v_smfmac_f32_16x16x64_bf16 v[84:87], v[68:71], v[240:247], v10\n\t" \
    "v_smfmac_f32_16x16x64_bf16 v[84:87], v[44:47], v[248:255], v10\n\t" \
    "v_add_f32 v72, v72, v74\n\t" \
    "v_add_f32 v73, v73, v75\n\t" \
    "v_cvt_pk_bf16_f32 v156, v72, v73\n\t" \
    "ds_write_b32 v12, v156 offset:0\n\t" \
    "v_add_f32 v76, v76, v78\n\t" \
    "v_add_f32 v77, v77, v79\n\t" \
    "v_cvt_pk_bf16_f32 v157, v76, v77\n\t" \
    "ds_write_b32 v12, v157 offset:64\n\t" \
    "v_add_f32 v80, v80, v82\n\t" \
    "v_add_f32 v81, v81, v83\n\t" \
    "v_cvt_pk_bf16_f32 v158, v80, v81\n\t" \
    "ds_write_b32 v16, v158 offset:0\n\t" \
    "v_pk_add_f32 v[84:85], v[84:85], v[86:87]\n\t" \
    "v_cvt_pk_bf16_f32 v159, v84, v85\n\t" \
    "ds_write_b32 v16, v159 offset:256\n\t" \
    "s_branch qk_Gback24_%=\n\t" \
    "qk_qs26_%=:\n\t" \
    "s_cmp_eq_u32 s91, s65\n\t" \
    "s_cbranch_scc1 qk_qb228_%=\n\t" \
    "s_mov_b32 s91, s65\n\t" \
    "s_nop 7\n\t" \
    "ds_read_b64_tr_b16 v[132:133], v13 offset:0\n\t" \
    "ds_read_b64_tr_b16 v[134:135], v13 offset:128\n\t" \
    "ds_read_b64_tr_b16 v[136:137], v13 offset:1024\n\t" \
    "ds_read_b64_tr_b16 v[138:139], v13 offset:1152\n\t" \
    "v_mov_b32 v88, 0\n\t" \
    "v_mov_b32 v89, 0\n\t" \
    "v_mov_b32 v90, 0\n\t" \
    "v_mov_b32 v91, 0\n\t" \
    "v_mov_b32 v92, 0\n\t" \
    "v_mov_b32 v93, 0\n\t" \
    "v_mov_b32 v94, 0\n\t" \
    "v_mov_b32 v95, 0\n\t" \
    "v_mov_b32 v96, 0\n\t" \
    "v_mov_b32 v97, 0\n\t" \
    "v_mov_b32 v98, 0\n\t" \
    "v_mov_b32 v99, 0\n\t" \
    "s_waitcnt lgkmcnt(0)\n\t" \
    "v_smfmac_f32_16x16x64_bf16 v[88:91], v[132:135], v[100:107], v10\n\t" \
    "v_smfmac_f32_16x16x64_bf16 v[92:95], v[132:135], v[116:123], v10\n\t" \
    "v_smfmac_f32_16x16x64_bf16 v[88:91], v[136:139], v[108:115], v10\n\t" \
    "v_smfmac_f32_16x16x64_bf16 v[92:95], v[136:139], v[124:131], v10\n\t" \
    "s_nop 7\n\t" \
    "s_branch qk_qb27_%=\n\t" \
    "qk_last15_%=:\n\t" \
    "s_endpgm\n\t" \
    "qk_loop12_%=:\n\t" \
    "v_mov_b32 v72, 0\n\t" \
    "v_mov_b32 v73, 0\n\t" \
    "v_mov_b32 v74, 0\n\t" \
    "v_mov_b32 v75, 0\n\t" \
    "v_mov_b32 v76, 0\n\t" \
    "v_mov_b32 v77, 0\n\t" \
    "v_mov_b32 v78, 0\n\t" \
    "v_mov_b32 v79, 0\n\t" \
    "v_mov_b32 v80, 0\n\t" \
    "v_mov_b32 v81, 0\n\t" \
    "v_mov_b32 v82, 0\n\t" \
    "v_mov_b32 v83, 0\n\t" \
    "v_mov_b32 v84, 0\n\t" \
    "v_mov_b32 v85, 0\n\t" \
    "v_mov_b32 v86, 0\n\t" \
    "v_mov_b32 v87, 0\n\t" \
    "v_mov_b32 v88, 0\n\t" \
    "v_mov_b32 v89, 0\n\t" \
    "v_mov_b32 v90, 0\n\t" \
    "v_mov_b32 v91, 0\n\t" \
    "v_mov_b32 v92, 0\n\t" \
    "v_mov_b32 v93, 0\n\t" \
    "v_mov_b32 v94, 0\n\t" \
    "v_mov_b32 v95, 0\n\t" \
    "v_mov_b32 v96, 0\n\t" \
    "v_mov_b32 v97, 0\n\t" \
    "v_mov_b32 v98, 0\n\t" \
    "v_mov_b32 v99, 0\n\t" \
    "s_add_u32 s69, s65, 0\n\t" \
    "s_and_b32 s69, s69, 7\n\t" \
    "s_lshl_b32 s69, s69, 18\n\t" \
    "s_add_u32 s69, s69, s62\n\t" \
    "s_add_u32 s46, s36, s69\n\t" \
    "s_addc_u32 s47, s37, 0\n\t" \
    "s_sub_u32 s70, s79, s74\n\t" \
    "s_max_i32 s70, s70, 0\n\t" \
    "s_min_i32 s70, s70, 16000\n\t" \
    "s_lshl_b32 s70, s70, 1\n\t" \
    "s_mul_hi_u32 s70, s70, s77\n\t" \
    "s_add_u32 s70, s70, s73\n\t" \
    ".p2align 6\n\t" \
    "s_nop 0\n\t" \
    "s_nop 0\n\t" \
    "s_nop 0\n\t" \
    "s_nop 0\n\t" \
    "s_nop 0\n\t" \
    "s_nop 0\n\t" \
    "qk_go38_%=:\n\t" \
    "s_getpc_b64 s[30:31]\n\t" \
    "qk_Wp40_%=:\n\t" \
    "s_add_u32 s30, s30, qk_We41_%=-qk_Wp40_%=\n\t" \
    "s_addc_u32 s31, s31, 0\n\t" \
    "s_sub_u32 s70, s70, 96\n\t" \
    "qk_W39_%=:\n\t" \
    "s_memtime s[56:57]\n\t" \
    "s_waitcnt lgkmcnt(0)\n\t" \
    "s_sub_u32 s69, s70, s56\n\t" \
    "s_cmp_gt_i32 s69, 80\n\t" \
    "s_cbranch_scc1 qk_W39_%=\n\t" \
    "s_max_i32 s69, s69, 0\n\t" \
    "s_and_b32 s69, s69, -4\n\t" \
    "s_sub_u32 s30, s30, s69\n\t" \
    "s_subb_u32 s31, s31, 0\n\t" \
    "s_setpc_b64 s[30:31]\n\t" \
    "s_nop 0\n\t" \
    "s_nop 0\n\t" \
    "s_nop 0\n\t" \
    "s_nop 0\n\t" \
    "s_nop 0\n\t" \
    "s_nop 0\n\t" \
    "s_nop 0\n\t" \
    "s_nop 0\n\t" \
    "s_nop 0\n\t" \
    "s_nop 0\n\t" \
    "s_nop 0\n\t" \
    "s_nop 0\n\t" \
    "s_nop 0\n\t" \
    "s_nop 0\n\t" \
    "s_nop 0\n\t" \
    "s_nop 0\n\t" \
    "s_nop 0\n\t" \
    "s_nop 0\n\t" \
    "s_nop 0\n\t" \
    "s_nop 0\n\t" \
    "qk_We41_%=:\n\t" \
    "qk_G42_%=:\n\t" \
    "global_load_dwordx4 v[36:39], v1, s[46:47] sc1\n\t" \
    "global_load_dword v161, v2, s[46:47] sc1\n\t" \
    "global_load_dwordx4 v[40:43], v23, s[46:47] sc1\n\t" \
    "s_waitcnt vmcnt(2)\n\t" \
    "qk_Garr47_%=:\n\t" \
    "ds_write_b128 v3, v[36:39]\n\t" \
    "v_smfmac_f32_16x16x64_bf16 v[72:75], v[36:39], a[16:23], v10\n\t" \
    "s_waitcnt vmcnt(1)\n\t" \
    "ds_write_b32 v22, v161 offset:19712\n\t" \
    "v_smfmac_f32_16x16x64_bf16 v[76:79], v[36:39], a[88:95], v10\n\t" \
    "s_waitcnt vmcnt(0)\n\t" \
    "ds_write_b128 v3, v[40:43] offset:1024\n\t" \
    "v_max3_u32 v160, v36, v40, v161\n\t" \
    "v_cmp_eq_u32 vcc, -1, v160\n\t" \
    "v_smfmac_f32_16x16x64_bf16 v[80:83], v[36:39], a[160:167], v10\n\t" \
    "v_smfmac_f32_16x16x64_bf16 v[84:87], v[36:39], a[232:239], v10\n\t" \
    "v_smfmac_f32_16x16x64_bf16 v[72:75], v[40:43], a[24:31], v10\n\t" \
    "s_cbranch_vccnz qk_Gbad43_%=\n\t" \
    "qk_Gc44_%=:\n\t" \
    "s_waitcnt lgkmcnt(0)\n\t" \
    "s_barrier\n\t" \
    "ds_read_b128 v[48:51], v4\n\t" \
    "ds_read_b128 v[52:55], v5\n\t" \
    "v_smfmac_f32_16x16x64_bf16 v[76:79], v[40:43], a[96:103], v10\n\t" \
    "ds_read_b128 v[56:59], v6\n\t" \
    "ds_read_b128 v[60:63], v7\n\t" \
    "v_smfmac_f32_16x16x64_bf16 v[80:83], v[40:43], a[168:175], v10\n\t" \
    "ds_read_b128 v[64:67], v8\n\t" \
    "v_smfmac_f32_16x16x64_bf16 v[84:87], v[40:43], a[240:247], v10\n\t" \
    "ds_read_b128 v[68:71], v9\n\t" \
    "s_waitcnt lgkmcnt(5)\n\t" \
    "v_smfmac_f32_16x16x64_bf16 v[72:75], v[48:51], a[32:39], v10\n\t" \
    "ds_read_b128 v[44:47], v0 offset:19456\n\t" \
    "s_waitcnt lgkmcnt(5)\n\t" \
    "v_smfmac_f32_16x16x64_bf16 v[72:75], v[52:55], a[40:47], v10\n\t" \
    "s_waitcnt lgkmcnt(4)\n\t" \
    "v_smfmac_f32_16x16x64_bf16 v[72:75], v[56:59], a[48:55], v10\n\t" \
    "s_waitcnt lgkmcnt(3)\n\t" \
    "v_smfmac_f32_16x16x64_bf16 v[72:75], v[60:63], a[56:63], v10\n\t" \
    "s_waitcnt lgkmcnt(2)\n\t" \
    "v_smfmac_f32_16x16x64_bf16 v[72:75], v[64:67], a[0:7], v10\n\t" \
    "s_waitcnt lgkmcnt(1)\n\t" \
    "v_smfmac_f32_16x16x64_bf16 v[72:75], v[68:71], a[8:15], v10\n\t" \
    "s_waitcnt lgkmcnt(0)\n\t" \
    "v_smfmac_f32_16x16x64_bf16 v[72:75], v[44:47], a[64:71], v10\n\t" \
    "s_nop 2\n\t" \
    "v_smfmac_f32_16x16x64_bf16 v[76:79], v[48:51], a[104:111], v10\n\t" \
    "s_nop 2\n\t" \
    "v_smfmac_f32_16x16x64_bf16 v[76:79], v[52:55], a[112:119], v10\n\t" \
    "v_add_f32 v72, v72, v74\n\t" \
    "v_smfmac_f32_16x16x64_bf16 v[76:79], v[56:59], a[120:127], v10\n\t" \
    "v_add_f32 v73, v73, v75\n\t" \
    "v_smfmac_f32_16x16x64_bf16 v[76:79], v[60:63], a[128:135], v10\n\t" \
    "v_cvt_pk_bf16_f32 v156, v72, v73\n\t" \
    "v_smfmac_f32_16x16x64_bf16 v[76:79], v[64:67], a[72:79], v10\n\t" \
    "ds_write_b32 v12, v156 offset:0\n\t" \
    "v_smfmac_f32_16x16x64_bf16 v[76:79], v[68:71], a[80:87], v10\n\t" \
    "v_smfmac_f32_16x16x64_bf16 v[76:79], v[44:47], a[136:143], v10\n\t" \
    "s_nop 2\n\t" \
    "v_smfmac_f32_16x16x64_bf16 v[80:83], v[48:51], a[176:183], v10\n\t" \
    "s_nop 2\n\t" \
    "v_smfmac_f32_16x16x64_bf16 v[80:83], v[52:55], a[184:191], v10\n\t" \
    "v_add_f32 v76, v76, v78\n\t" \
    "v_smfmac_f32_16x16x64_bf16 v[80:83], v[56:59], a[192:199], v10\n\t" \
    "v_add_f32 v77, v77, v79\n\t" \
    "v_smfmac_f32_16x16x64_bf16 v[80:83], v[60:63], a[200:207], v10\n\t" \
    "v_cvt_pk_bf16_f32 v157, v76, v77\n\t" \
    "v_smfmac_f32_16x16x64_bf16 v[80:83], v[64:67], a[144:151], v10\n\t" \
    "ds_write_b32 v12, v157 offset:64\n\t" \
    "v_smfmac_f32_16x16x64_bf16 v[80:83], v[68:71], a[152:159], v10\n\t" \
    "v_smfmac_f32_16x16x64_bf16 v[80:83], v[44:47], a[208:215], v10\n\t" \
    "s_nop 2\n\t" \
    "v_smfmac_f32_16x16x64_bf16 v[84:87], v[48:51], a[248:255], v10\n\t" \
    "s_nop 2\n\t" \
    "v_smfmac_f32_16x16x64_bf16 v[84:87], v[52:55], v[224:231], v10\n\t" \
    "v_add_f32 v80, v80, v82\n\t" \
    "v_smfmac_f32_16x16x64_bf16 v[84:87], v[56:59], v[232:239], v10\n\t" \
    "ds_read_b64_tr_b16 v[132:133], v13 offset:0\n\t" \
    "v_add_f32 v81, v81, v83\n\t" \
    "v_smfmac_f32_16x16x64_bf16 v[84:87], v[60:63], v[240:247], v10\n\t" \
    "ds_read_b64_tr_b16 v[134:135], v13 offset:128\n\t" \
    "v_cvt_pk_bf16_f32 v158, v80, v81\n\t" \
    "v_smfmac_f32_16x16x64_bf16 v[84:87], v[64:67], a[216:223], v10\n\t" \
    "ds_write_b32 v16, v158 offset:0\n\t" \
    "v_smfmac_f32_16x16x64_bf16 v[84:87], v[68:71], a[224:231], v10\n\t" \
    "ds_read_b64_tr_b16 v[136:137], v13 offset:1024\n\t" \
    "ds_read_b64_tr_b16 v[138:139], v13 offset:1152\n\t" \
    "v_smfmac_f32_16x16x64_bf16 v[84:87], v[44:47], v[248:255], v10\n\t" \
    "s_waitcnt lgkmcnt(3)\n\t" \
    "v_smfmac_f32_16x16x64_bf16 v[88:91], v[132:135], v[100:107], v10\n\t" \
    "s_nop 4\n\t" \
    "v_cmp_u_f32 vcc, v72, v73\n\t" \
    "v_pk_add_f32 v[84:85], v[84:85], v[86:87]\n\t" \
    "v_cvt_pk_bf16_f32 v159, v84, v85\n\t" \
    "ds_write_b32 v16, v159 offset:256\n\t" \
    "v_smfmac_f32_16x16x64_bf16 v[92:95], v[132:135], v[116:123], v10\n\t" \
    "s_waitcnt lgkmcnt(1)\n\t" \
    "v_smfmac_f32_16x16x64_bf16 v[88:91], v[136:139], v[108:115], v10\n\t" \
    "s_cbranch_vccnz qk_Gslow45_%=\n\t" \
    "qk_Gback46_%=:\n\t" \
    "v_smfmac_f32_16x16x64_bf16 v[92:95], v[136:139], v[124:131], v10\n\t" \
    "s_waitcnt lgkmcnt(0)\n\t" \
    "s_barrier\n\t" \
    "ds_read_b64_tr_b16 v[140:141], v17 offset:0\n\t" \
    "ds_read_b64_tr_b16 v[142:143], v17 offset:256\n\t" \
    "ds_read_b64_tr_b16 v[144:145], v17 offset:512\n\t" \
    "ds_read_b64_tr_b16 v[146:147], v17 offset:768\n\t" \
    "ds_read_b64_tr_b16 v[148:149], v17 offset:1024\n\t" \
    "ds_read_b64_tr_b16 v[150:151], v17 offset:1280\n\t" \
    "ds_read_b64_tr_b16 v[152:153], v17 offset:1536\n\t" \
    "ds_read_b64_tr_b16 v[154:155], v17 offset:1792\n\t" \
    "qk_qb49_%=:\n\t" \
    "v_add_f32 v168, v89, v91\n\t" \
    "v_add_f32 v169, v93, v95\n\t" \
    "v_cmp_u_f32 vcc, v168, v169\n\t" \
    "v_cvt_pk_bf16_f32 v168, 0, v168\n\t" \
    "v_cvt_pk_bf16_f32 v169, 0, v169\n\t" \
    "s_waitcnt lgkmcnt(4)\n\t" \
    "v_smfmac_f32_16x16x64_bf16 v[96:99], v[132:135], v[140:147], v10\n\t" \
    "s_waitcnt lgkmcnt(0)\n\t" \
    "v_smfmac_f32_16x16x64_bf16 v[96:99], v[136:139], v[148:155], v10\n\t" \
    "s_cbranch_vccnz qk_qs48_%=\n\t" \
    "qk_qb250_%=:\n\t" \
    "v_mul_f32 v168, v168, v26\n\t" \
    "v_mul_f32 v169, v169, v26\n\t" \
    "v_cvt_pk_bf16_f32 v168, 0, v168\n\t" \
    "v_cvt_pk_bf16_f32 v169, 0, v169\n\t" \
    "v_max_f32 v178, v168, v169\n\t" \
    "v_mul_f32 v168, v168, v28\n\t" \
    "v_mul_f32 v169, v169, v28\n\t" \
    "v_max_f32_dpp v178, v178, v178 row_mirror row_mask:0xf bank_mask:0xf\n\t" \
    "v_add_f32 v172, v97, v99\n\t" \
    "v_cvt_pk_bf16_f32 v172, 0, v172\n\t" \
    "v_mul_f32 v172, v172, v26\n\t" \
    "v_cvt_pk_bf16_f32 v172, 0, v172\n\t" \
    "v_max_f32 v173, v178, v172\n\t" \
    "v_mul_f32 v172, v172, v28\n\t" \
    "s_nop 0\n\t" \
    "v_max_f32_dpp v173, v173, v173 quad_perm:[1,0,3,2] row_mask:0xf bank_mask:0xf\n\t" \
    "s_nop 1\n\t" \
    "v_max_f32_dpp v173, v173, v173 quad_perm:[2,3,0,1] row_mask:0xf bank_mask:0xf\n\t" \
    "s_nop 1\n\t" \
    "v_max_f32_dpp v173, v173, v173 row_half_mirror row_mask:0xf bank_mask:0xf\n\t" \
    "v_fma_f32 v172, -v173, v28, v172\n\t" \
    "v_fma_f32 v168, -v173, v28, v168\n\t" \
    "v_fma_f32 v169, -v173, v28, v169\n\t" \
    "v_exp_f32 v172, v172\n\t" \
    "v_exp_f32 v168, v168\n\t" \
    "v_exp_f32 v169, v169\n\t" \
    "v_fma_f32 v173, v172, 0.5, v168\n\t" \
    "v_add_f32 v173, v173, v169\n\t" \
    "v_mov_b32_dpp v174, v172 quad_perm:[0,0,2,2] row_mask:0xf bank_mask:0xf\n\t" \
    "v_mov_b32_dpp v175, v172 quad_perm:[1,1,3,3] row_mask:0xf bank_mask:0xf\n\t" \
    "v_add_f32_dpp v173, v173, v173 quad_perm:[1,0,3,2] row_mask:0xf bank_mask:0xf\n\t" \
    "v_mov_b32_dpp v176, v168 quad_perm:[1,0,3,2] row_mask:0xf bank_mask:0xf\n\t" \
    "v_mov_b32_dpp v177, v169 quad_perm:[1,0,3,2] row_mask:0xf bank_mask:0xf\n\t" \
    "v_add_f32_dpp v173, v173, v173 quad_perm:[2,3,0,1] row_mask:0xf bank_mask:0xf\n\t" \
    "v_cndmask_b32 v168, v168, v177, s[48:49]\n\t" \
    "v_cndmask_b32 v169, v176, v169, s[48:49]\n\t" \
    "v_add_f32_dpp v173, v173, v173 row_half_mirror row_mask:0xf bank_mask:0xf\n\t" \
    "s_nop 1\n\t" \
    "v_add_f32_dpp v173, v173, v173 row_mirror row_mask:0xf bank_mask:0xf\n\t" \
    "v_rcp_f32 v173, v173\n\t" \
    "s_nop 0\n\t" \
    "v_pk_mul_f32 v[168:169], v[168:169], v[172:173] op_sel:[0,1] op_sel_hi:[1,1]\n\t" \
    "v_cvt_pk_bf16_f32 v176, v168, v169\n\t" \
    "global_store_dword v18, v176, s[46:47] sc1\n\t" \
    "global_store_dword v180, v176, s[46:47] sc1\n\t" \
    "global_store_dword v181, v176, s[46:47] sc1\n\t" \
    "global_store_dword v182, v176, s[46:47] sc1\n\t" \
    "v_pk_mul_f32 v[174:175], v[174:175], v[172:173] op_sel:[0,1] op_sel_hi:[1,1]\n\t" \
    "v_cvt_pk_bf16_f32 v177, v174, v175\n\t" \
    "global_store_dword v19, v177, s[46:47] sc1\n\t" \
    "s_cmp_eq_u32 s65, 63\n\t" \
    "s_cbranch_scc1 qk_last37_%=\n\t" \
    "v_readfirstlane_b32 s69, v25\n\t" \
    "s_add_u32 s65, s65, 1\n\t" \
    "s_max_i32 s83, s83, s69\n\t" \
    "s_cmp_gt_u32 s65, 7\n\t" \
    "s_cselect_b32 s83, 99, s83\n\t" \
    "v_mov_b32 v72, 0\n\t" \
    "v_mov_b32 v73, 0\n\t" \
    "v_mov_b32 v74, 0\n\t" \
    "v_mov_b32 v75, 0\n\t" \
    "v_mov_b32 v76, 0\n\t" \
    "v_mov_b32 v77, 0\n\t" \
    "v_mov_b32 v78, 0\n\t" \
    "v_mov_b32 v79, 0\n\t" \
    "v_mov_b32 v80, 0\n\t" \
    "v_mov_b32 v81, 0\n\t" \
    "v_mov_b32 v82, 0\n\t" \
    "v_mov_b32 v83, 0\n\t" \
    "v_mov_b32 v84, 0\n\t" \
    "v_mov_b32 v85, 0\n\t" \
    "v_mov_b32 v86, 0\n\t" \
    "v_mov_b32 v87, 0\n\t" \
    "v_mov_b32 v88, 0\n\t" \
    "v_mov_b32 v89, 0\n\t" \
    "v_mov_b32 v90, 0\n\t" \
    "v_mov_b32 v91, 0\n\t" \
    "v_mov_b32 v92, 0\n\t" \
    "v_mov_b32 v93, 0\n\t" \
    "v_mov_b32 v94, 0\n\t" \
    "v_mov_b32 v95, 0\n\t" \
    "v_mov_b32 v96, 0\n\t" \
    "v_mov_b32 v97, 0\n\t" \
    "v_mov_b32 v98, 0\n\t" \
    "v_mov_b32 v99, 0\n\t" \
    "s_add_u32 s69, s65, 0\n\t" \
    "s_and_b32 s69, s69, 7\n\t" \
    "s_lshl_b32 s69, s69, 18\n\t" \
    "s_add_u32 s69, s69, s62\n\t" \
    "s_add_u32 s46, s36, s69\n\t" \
    "s_addc_u32 s47, s37, 0\n\t" \
    "s_mul_i32 s70, s67, 7\n\t" \
    "s_add_u32 s69, s65, 0\n\t" \
    "s_xor_b32 s69, s69, s70\n\t" \
    "s_and_b32 s69, s69, 63\n\t" \
    "s_lshl_b32 s69, s69, 13\n\t" \
    "s_mul_i32 s70, s67, 524288\n\t" \
    "s_add_u32 s69, s69, s70\n\t" \
    "s_add_u32 s69, s69, 23068672\n\t" \
    "s_add_u32 s38, s36, s69\n\t" \
    "s_addc_u32 s39, s37, 0\n\t" \
    "qk_WF51_%=:\n\t" \
    "s_cmp_ge_i32 s83, s65\n\t" \
    "s_cbranch_scc1 qk_WFok52_%=\n\t" \
    "global_load_dword v160, v24, s[100:101] sc1\n\t" \
    "s_waitcnt vmcnt(0)\n\t" \
    "v_readfirstlane_b32 s83, v160\n\t" \
    "s_nop 1\n\t" \
    "s_cmp_ge_i32 s83, s65\n\t" \
    "s_cbranch_scc1 qk_WFok52_%=\n\t" \
    "s_cmp_eq_u32 s68, 0\n\t" \
    "s_cbranch_scc1 qk_F53_%=\n\t" \
    "s_sub_u32 s68, s68, 1\n\t" \
    "s_sleep 2\n\t" \
    "s_branch qk_WF51_%=\n\t" \
    "qk_F53_%=:\n\t" \
    "v_mov_b32 v160, 5767424\n\t" \
    "v_mov_b32 v161, 9\n\t" \
    "global_store_dword v160, v161, s[36:37] sc1\n\t" \
    "s_waitcnt vmcnt(0)\n\t" \
    "s_branch qk_WFok52_%=\n\t" \
    "qk_WFok52_%=:\n\t" \
    "global_load_dwordx4 v[100:103], v0, s[38:39] offset:0 sc1\n\t" \
    "global_load_dwordx4 v[104:107], v0, s[38:39] offset:2048 sc1\n\t" \
    "global_load_dwordx4 v[108:111], v15, s[38:39] offset:0 sc1\n\t" \
    "global_load_dwordx4 v[112:115], v15, s[38:39] offset:2048 sc1\n\t" \
    "global_load_dwordx4 v[116:119], v0, s[38:39] offset:1024 sc1\n\t" \
    "global_load_dwordx4 v[120:123], v0, s[38:39] offset:3072 sc1\n\t" \
    "global_load_dwordx4 v[124:127], v15, s[38:39] offset:1024 sc1\n\t" \
    "global_load_dwordx4 v[128:131], v15, s[38:39] offset:3072 sc1\n\t" \
    "s_cmp_gt_i32 s83, s65\n\t" \
    "s_cbranch_scc1 qk_nf54_%=\n\t" \
    "global_load_dword v25, v24, s[100:101] sc1\n\t" \
    "qk_nf54_%=:\n\t" \
    "s_barrier\n\t" \
    "ds_read_b32 v168, v11 offset:20480\n\t" \
    "s_waitcnt lgkmcnt(0)\n\t" \
    "v_readfirstlane_b32 s70, v168\n\t" \
    "ds_write_b32 v12, v31 offset:0\n\t" \
    "ds_write_b32 v12, v31 offset:64\n\t" \
    "s_branch qk_go38_%=\n\t" \
    "qk_Gbad43_%=:\n\t" \
    "s_waitcnt vmcnt(0) lgkmcnt(0)\n\t" \
    "s_nop 7\n\t" \
    "v_mov_b32 v72, 0\n\t" \
    "v_mov_b32 v73, 0\n\t" \
    "v_mov_b32 v74, 0\n\t" \
    "v_mov_b32 v75, 0\n\t" \
    "v_mov_b32 v76, 0\n\t" \
    "v_mov_b32 v77, 0\n\t" \
    "v_mov_b32 v78, 0\n\t" \
    "v_mov_b32 v79, 0\n\t" \
    "v_mov_b32 v80, 0\n\t" \
    "v_mov_b32 v81, 0\n\t" \
    "v_mov_b32 v82, 0\n\t" \
    "v_mov_b32 v83, 0\n\t" \
    "v_mov_b32 v84, 0\n\t" \
    "v_mov_b32 v85, 0\n\t" \
    "v_mov_b32 v86, 0\n\t" \
    "v_mov_b32 v87, 0\n\t" \
    "s_cmp_eq_u32 s68, 0\n\t" \
    "s_cbranch_scc1 qk_F55_%=\n\t" \
    "s_sub_u32 s68, s68, 1\n\t" \
    "s_branch qk_G42_%=\n\t" \
    "qk_F55_%=:\n\t" \
    "v_mov_b32 v160, 5767424\n\t" \
    "v_mov_b32 v161, 1\n\t" \
    "global_store_dword v160, v161, s[36:37] sc1\n\t" \
    "s_waitcnt vmcnt(0)\n\t" \
    "s_branch qk_Gc44_%=\n\t" \
    "qk_Gslow45_%=:\n\t" \
    "v_max_u32 v160, v36, v37\n\t" \
    "v_max3_u32 v160, v160, v38, v39\n\t" \
    "v_max3_u32 v160, v160, v40, v41\n\t" \
    "v_max3_u32 v160, v160, v42, v43\n\t" \
    "v_max3_u32 v160, v160, v44, v45\n\t" \
    "v_max3_u32 v160, v160, v46, v47\n\t" \
    "v_max3_u32 v160, v160, v48, v49\n\t" \
    "v_max3_u32 v160, v160, v50, v51\n\t" \
    "v_max3_u32 v160, v160, v52, v53\n\t" \
    "v_max3_u32 v160, v160, v54, v55\n\t" \
    "v_max3_u32 v160, v160, v56, v57\n\t" \
    "v_max3_u32 v160, v160, v58, v59\n\t" \
    "v_max3_u32 v160, v160, v60, v61\n\t" \
    "v_max3_u32 v160, v160, v62, v63\n\t" \
    "v_max3_u32 v160, v160, v64, v65\n\t" \
    "v_max3_u32 v160, v160, v66, v67\n\t" \
    "v_max3_u32 v160, v160, v68, v69\n\t" \
    "v_max3_u32 v160, v160, v70, v71\n\t" \
    "v_cmp_eq_u32 vcc, -1, v160\n\t" \
    "s_cbranch_vccz qk_Gback46_%=\n\t" \
    "s_waitcnt lgkmcnt(0)\n\t" \
    "v_sub_u32 v160, v1, v3\n\t" \
    "s_nop 0\n\t" \
    "v_readfirstlane_b32 s69, v160\n\t" \
    "v_add_u32 v163, 20480, v0\n\t" \
    "s_nop 7\n\t" \
    "s_add_u32 s30, s46, s69\n\t" \
    "s_addc_u32 s31, s47, 0\n\t" \
    "qk_Gag56_%=:\n\t" \
    "global_load_dwordx4 v[36:39], v1, s[46:47] sc1\n\t" \
    "global_load_dwordx4 v[40:43], v23, s[46:47] sc1\n\t" \
    "global_load_dwordx4 v[44:47], v163, s[30:31] sc1\n\t" \
    "global_load_dwordx4 v[48:51], v4, s[30:31] sc1\n\t" \
    "global_load_dwordx4 v[52:55], v5, s[30:31] sc1\n\t" \
    "global_load_dwordx4 v[56:59], v6, s[30:31] sc1\n\t" \
    "global_load_dwordx4 v[60:63], v7, s[30:31] sc1\n\t" \
    "global_load_dwordx4 v[64:67], v8, s[30:31] sc1\n\t" \
    "global_load_dwordx4 v[68:71], v9, s[30:31] sc1\n\t" \
    "s_waitcnt vmcnt(0)\n\t" \
    "v_max_u32 v160, v36, v37\n\t" \
    "v_max3_u32 v160, v160, v38, v39\n\t" \
    "v_max3_u32 v160, v160, v40, v41\n\t" \
    "v_max3_u32 v160, v160, v42, v43\n\t" \
    "v_max3_u32 v160, v160, v44, v45\n\t" \
    "v_max3_u32 v160, v160, v46, v47\n\t" \
    "v_max3_u32 v160, v160, v48, v49\n\t" \
    "v_max3_u32 v160, v160, v50, v51\n\t" \
    "v_max3_u32 v160, v160, v52, v53\n\t" \
    "v_max3_u32 v160, v160, v54, v55\n\t" \
    "v_max3_u32 v160, v160, v56, v57\n\t" \
    "v_max3_u32 v160, v160, v58, v59\n\t" \
    "v_max3_u32 v160, v160, v60, v61\n\t" \
    "v_max3_u32 v160, v160, v62, v63\n\t" \
    "v_max3_u32 v160, v160, v64, v65\n\t" \
    "v_max3_u32 v160, v160, v66, v67\n\t" \
    "v_max3_u32 v160, v160, v68, v69\n\t" \
    "v_max3_u32 v160, v160, v70, v71\n\t" \
    "v_cmp_eq_u32 vcc, -1, v160\n\t" \
    "s_cbranch_vccz qk_Gredo57_%=\n\t" \
    "s_cmp_eq_u32 s68, 0\n\t" \
    "s_cbranch_scc1 qk_F58_%=\n\t" \
    "s_sub_u32 s68, s68, 1\n\t" \
    "s_sleep 1\n\t" \
    "s_branch qk_Gag56_%=\n\t" \
    "qk_F58_%=:\n\t" \
    "v_mov_b32 v160, 5767424\n\t" \
    "v_mov_b32 v161, 2\n\t" \
    "global_store_dword v160, v161, s[36:37] sc1\n\t" \
    "s_waitcnt vmcnt(0)\n\t" \
    "s_branch qk_Gredo57_%=\n\t" \
    "qk_Gredo57_%=:\n\t" \
    "v_mov_b32 v72, 0\n\t" \
    "v_mov_b32 v73, 0\n\t" \
    "v_mov_b32 v74, 0\n\t" \
    "v_mov_b32 v75, 0\n\t" \
    "v_mov_b32 v76, 0\n\t" \
    "v_mov_b32 v77, 0\n\t" \
    "v_mov_b32 v78, 0\n\t" \
    "v_mov_b32 v79, 0\n\t" \
    "v_mov_b32 v80, 0\n\t" \
    "v_mov_b32 v81, 0\n\t" \
    "v_mov_b32 v82, 0\n\t" \
    "v_mov_b32 v83, 0\n\t" \
    "v_mov_b32 v84, 0\n\t" \
    "v_mov_b32 v85, 0\n\t" \
    "v_mov_b32 v86, 0\n\t" \
    "v_mov_b32 v87, 0\n\t" \
    "v_smfmac_f32_16x16x64_bf16 v[72:75], v[36:39], a[16:23], v10\n\t" \
    "v_smfmac_f32_16x16x64_bf16 v[72:75], v[40:43], a[24:31], v10\n\t" \
    "v_smfmac_f32_16x16x64_bf16 v[72:75], v[48:51], a[32:39], v10\n\t" \
    "v_smfmac_f32_16x16x64_bf16 v[72:75], v[52:55], a[40:47], v10\n\t" \
    "v_smfmac_f32_16x16x64_bf16 v[72:75], v[56:59], a[48:55], v10\n\t" \
    "v_smfmac_f32_16x16x64_bf16 v[72:75], v[60:63], a[56:63], v10\n\t" \
    "v_smfmac_f32_16x16x64_bf16 v[72:75], v[64:67], a[0:7], v10\n\t" \
    "v_smfmac_f32_16x16x64_bf16 v[72:75], v[68:71], a[8:15], v10\n\t" \
    "v_smfmac_f32_16x16x64_bf16 v[72:75], v[44:47], a[64:71], v10\n\t" \
    "v_smfmac_f32_16x16x64_bf16 v[76:79], v[36:39], a[88:95], v10\n\t" \
    "v_smfmac_f32_16x16x64_bf16 v[76:79], v[40:43], a[96:103], v10\n\t" \
    "v_smfmac_f32_16x16x64_bf16 v[76:79], v[48:51], a[104:111], v10\n\t" \
    "v_smfmac_f32_16x16x64_bf16 v[76:79], v[52:55], a[112:119], v10\n\t" \
    "v_smfmac_f32_16x16x64_bf16 v[76:79], v[56:59], a[120:127], v10\n\t" \
    "v_smfmac_f32_16x16x64_bf16 v[76:79], v[60:63], a[128:135], v10\n\t" \
    "v_smfmac_f32_16x16x64_bf16 v[76:79], v[64:67], a[72:79], v10\n\t" \
    "v_smfmac_f32_16x16x64_bf16 v[76:79], v[68:71], a[80:87], v10\n\t" \
    "v_smfmac_f32_16x16x64_bf16 v[76:79], v[44:47], a[136:143], v10\n\t" \
    "v_smfmac_f32_16x16x64_bf16 v[80:83], v[36:39], a[160:167], v10\n\t" \
    "v_smfmac_f32_16x16x64_bf16 v[80:83], v[40:43], a[168:175], v10\n\t" \
    "v_smfmac_f32_16x16x64_bf16 v[80:83], v[48:51], a[176:183], v10\n\t" \
    "v_smfmac_f32_16x16x64_bf16 v[80:83], v[52:55], a[184:191], v10\n\t" \
    "v_smfmac_f32_16x16x64_bf16 v[80:83], v[56:59], a[192:199], v10\n\t" \
    "v_smfmac_f32_16x16x64_bf16 v[80:83], v[60:63], a[200:207], v10\n\t" \
    "v_smfmac_f32_16x16x64_bf16 v[80:83], v[64:67], a[144:151], v10\n\t" \
    "v_smfmac_f32_16x16x64_bf16 v[80:83], v[68:71], a[152:159], v10\n\t" \
    "v_smfmac_f32_16x16x64_bf16 v[80:83], v[44:47], a[208:215], v10\n\t" \
    "v_smfmac_f32_16x16x64_bf16 v[84:87], v[36:39], a[232:239], v10\n\t" \
    "v_smfmac_f32_16x16x64_bf16 v[84:87], v[40:43], a[240:247], v10\n\t" \
    "v_smfmac_f32_16x16x64_bf16 v[84:87], v[48:51], a[248:255], v10\n\t" \
    "v_smfmac_f32_16x16x64_bf16 v[84:87], v[52:55], v[224:231], v10\n\t" \
    "v_smfmac_f32_16x16x64_bf16 v[84:87], v[56:59], v[232:239], v10\n\t" \
    "v_smfmac_f32_16x16x64_bf16 v[84:87], v[60:63], v[240:247], v10\n\t" \
    "v_smfmac_f32_16x16x64_bf16 v[84:87], v[64:67], a[216:223], v10\n\t" \
    "v_smfmac_f32_16x16x64_bf16 v[84:87], v[68:71], a[224:231], v10\n\t" \
    "v_smfmac_f32_16x16x64_bf16 v[84:87], v[44:47], v[248:255], v10\n\t" \
    "v_add_f32 v72, v72, v74\n\t" \
    "v_add_f32 v73, v73, v75\n\t" \
    "v_cvt_pk_bf16_f32 v156, v72, v73\n\t" \
    "ds_write_b32 v12, v156 offset:0\n\t" \
    "v_add_f32 v76, v76, v78\n\t" \
    "v_add_f32 v77, v77, v79\n\t" \
    "v_cvt_pk_bf16_f32 v157, v76, v77\n\t" \
    "ds_write_b32 v12, v157 offset:64\n\t" \
    "v_add_f32 v80, v80, v82\n\t" \
    "v_add_f32 v81, v81, v83\n\t" \
    "v_cvt_pk_bf16_f32 v158, v80, v81\n\t" \
    "ds_write_b32 v16, v158 offset:0\n\t" \
    "v_pk_add_f32 v[84:85], v[84:85], v[86:87]\n\t" \
    "v_cvt_pk_bf16_f32 v159, v84, v85\n\t" \
    "ds_write_b32 v16, v159 offset:256\n\t" \
    "s_branch qk_Gback46_%=\n\t" \
    "qk_qs48_%=:\n\t" \
    "s_cmp_eq_u32 s91, s65\n\t" \
    "s_cbranch_scc1 qk_qb250_%=\n\t" \
    "s_mov_b32 s91, s65\n\t" \
    "s_nop 7\n\t" \
    "ds_read_b64_tr_b16 v[132:133], v13 offset:0\n\t" \
    "ds_read_b64_tr_b16 v[134:135], v13 offset:128\n\t" \
    "ds_read_b64_tr_b16 v[136:137], v13 offset:1024\n\t" \
    "ds_read_b64_tr_b16 v[138:139], v13 offset:1152\n\t" \
    "v_mov_b32 v88, 0\n\t" \
    "v_mov_b32 v89, 0\n\t" \
    "v_mov_b32 v90, 0\n\t" \
    "v_mov_b32 v91, 0\n\t" \
    "v_mov_b32 v92, 0\n\t" \
    "v_mov_b32 v93, 0\n\t" \
    "v_mov_b32 v94, 0\n\t" \
    "v_mov_b32 v95, 0\n\t" \
    "v_mov_b32 v96, 0\n\t" \
    "v_mov_b32 v97, 0\n\t" \
    "v_mov_b32 v98, 0\n\t" \
    "v_mov_b32 v99, 0\n\t" \
    "s_waitcnt lgkmcnt(0)\n\t" \
    "v_smfmac_f32_16x16x64_bf16 v[88:91], v[132:135], v[100:107], v10\n\t" \
    "v_smfmac_f32_16x16x64_bf16 v[92:95], v[132:135], v[116:123], v10\n\t" \
    "v_smfmac_f32_16x16x64_bf16 v[88:91], v[136:139], v[108:115], v10\n\t" \
    "v_smfmac_f32_16x16x64_bf16 v[92:95], v[136:139], v[124:131], v10\n\t" \
    "s_nop 7\n\t" \
    "s_branch qk_qb49_%=\n\t" \
    "qk_last37_%=:\n\t" \
    "s_endpgm\n\t" \
    "qk_loop13_%=:\n\t" \
    "v_mov_b32 v72, 0\n\t" \
    "v_mov_b32 v73, 0\n\t" \
    "v_mov_b32 v74, 0\n\t" \
    "v_mov_b32 v75, 0\n\t" \
    "v_mov_b32 v76, 0\n\t" \
    "v_mov_b32 v77, 0\n\t" \
    "v_mov_b32 v78, 0\n\t" \
    "v_mov_b32 v79, 0\n\t" \
    "v_mov_b32 v80, 0\n\t" \
    "v_mov_b32 v81, 0\n\t" \
    "v_mov_b32 v82, 0\n\t" \
    "v_mov_b32 v83, 0\n\t" \
    "v_mov_b32 v84, 0\n\t" \
    "v_mov_b32 v85, 0\n\t" \
    "v_mov_b32 v86, 0\n\t" \
    "v_mov_b32 v87, 0\n\t" \
    "s_add_u32 s69, s65, 0\n\t" \
    "s_and_b32 s69, s69, 7\n\t" \
    "s_lshl_b32 s69, s69, 18\n\t" \
    "s_add_u32 s69, s69, s62\n\t" \
    "s_add_u32 s46, s36, s69\n\t" \
    "s_addc_u32 s47, s37, 0\n\t" \
    "s_mul_i32 s70, s67, 7\n\t" \
    "s_add_u32 s69, s65, 0\n\t" \
    "s_xor_b32 s69, s69, s70\n\t" \
    "s_and_b32 s69, s69, 63\n\t" \
    "s_lshl_b32 s69, s69, 12\n\t" \
    "s_mul_i32 s70, s67, 262144\n\t" \
    "s_add_u32 s69, s69, s70\n\t" \
    "s_add_u32 s69, s69, 39845888\n\t" \
    "s_add_u32 s40, s36, s69\n\t" \
    "s_addc_u32 s41, s37, 0\n\t" \
    "s_sub_u32 s91, s65, 1\n\t" \
    "v_mov_b32 v25, s91\n\t" \
    "s_sub_u32 s70, s79, s74\n\t" \
    "s_max_i32 s70, s70, 0\n\t" \
    "s_min_i32 s70, s70, 16000\n\t" \
    "s_lshl_b32 s70, s70, 1\n\t" \
    "s_mul_hi_u32 s70, s70, s77\n\t" \
    "s_add_u32 s70, s70, s73\n\t" \
    ".p2align 6\n\t" \
    "s_nop 0\n\t" \
    "s_nop 0\n\t" \
    "s_nop 0\n\t" \
    "s_nop 0\n\t" \
    "s_nop 0\n\t" \
    "s_nop 0\n\t" \
    "qk_go60_%=:\n\t" \
    "s_getpc_b64 s[30:31]\n\t" \
    "qk_Wp64_%=:\n\t" \
    "s_add_u32 s30, s30, qk_We65_%=-qk_Wp64_%=\n\t" \
    "s_addc_u32 s31, s31, 0\n\t" \
    "s_sub_u32 s70, s70, 96\n\t" \
    "qk_W63_%=:\n\t" \
    "s_memtime s[56:57]\n\t" \
    "s_waitcnt lgkmcnt(0)\n\t" \
    "s_sub_u32 s69, s70, s56\n\t" \
    "s_cmp_gt_i32 s69, 80\n\t" \
    "s_cbranch_scc1 qk_W63_%=\n\t" \
    "s_max_i32 s69, s69, 0\n\t" \
    "s_and_b32 s69, s69, -4\n\t" \
    "s_sub_u32 s30, s30, s69\n\t" \
    "s_subb_u32 s31, s31, 0\n\t" \
    "s_setpc_b64 s[30:31]\n\t" \
    "s_nop 0\n\t" \
    "s_nop 0\n\t" \
    "s_nop 0\n\t" \
    "s_nop 0\n\t" \
    "s_nop 0\n\t" \
    "s_nop 0\n\t" \
    "s_nop 0\n\t" \
    "s_nop 0\n\t" \
    "s_nop 0\n\t" \
    "s_nop 0\n\t" \
    "s_nop 0\n\t" \
    "s_nop 0\n\t" \
    "s_nop 0\n\t" \
    "s_nop 0\n\t" \
    "s_nop 0\n\t" \
    "s_nop 0\n\t" \
    "s_nop 0\n\t" \
    "s_nop 0\n\t" \
    "s_nop 0\n\t" \
    "s_nop 0\n\t" \
    "qk_We65_%=:\n\t" \
    "qk_G66_%=:\n\t" \
    "global_load_dwordx4 v[36:39], v1, s[46:47] sc1\n\t" \
    "global_load_dword v161, v2, s[46:47] sc1\n\t" \
    "global_load_dwordx4 v[40:43], v23, s[46:47] sc1\n\t" \
    "s_waitcnt vmcnt(2)\n\t" \
    "qk_Garr71_%=:\n\t" \
    "ds_write_b128 v3, v[36:39]\n\t" \
    "v_smfmac_f32_16x16x64_bf16 v[72:75], v[36:39], a[32:39], v10\n\t" \
    "s_waitcnt vmcnt(1)\n\t" \
    "ds_write_b32 v22, v161 offset:19968\n\t" \
    "v_smfmac_f32_16x16x64_bf16 v[76:79], v[36:39], a[104:111], v10\n\t" \
    "s_waitcnt vmcnt(0)\n\t" \
    "ds_write_b128 v3, v[40:43] offset:1024\n\t" \
    "v_max3_u32 v160, v36, v40, v161\n\t" \
    "v_cmp_eq_u32 vcc, -1, v160\n\t" \
    "v_smfmac_f32_16x16x64_bf16 v[80:83], v[36:39], a[176:183], v10\n\t" \
    "v_smfmac_f32_16x16x64_bf16 v[84:87], v[36:39], a[248:255], v10\n\t" \
    "v_smfmac_f32_16x16x64_bf16 v[72:75], v[40:43], a[40:47], v10\n\t" \
    "s_cbranch_vccnz qk_Gbad67_%=\n\t" \
    "qk_Gc68_%=:\n\t" \
    "s_cmp_ge_u32 s91, 57\n\t" \
    "s_cbranch_scc1 qk_kf61_%=\n\t" \
    "qk_kb62_%=:\n\t" \
    "s_waitcnt lgkmcnt(0)\n\t" \
    "s_barrier\n\t" \
    "ds_read_b128 v[48:51], v4\n\t" \
    "ds_read_b128 v[52:55], v5\n\t" \
    "v_smfmac_f32_16x16x64_bf16 v[76:79], v[40:43], a[112:119], v10\n\t" \
    "ds_read_b128 v[56:59], v6\n\t" \
    "ds_read_b128 v[60:63], v7\n\t" \
    "v_smfmac_f32_16x16x64_bf16 v[80:83], v[40:43], a[184:191], v10\n\t" \
    "ds_read_b128 v[64:67], v8\n\t" \
    "v_smfmac_f32_16x16x64_bf16 v[84:87], v[40:43], v[224:231], v10\n\t" \
    "ds_read_b128 v[68:71], v9\n\t" \
    "s_waitcnt lgkmcnt(5)\n\t" \
    "v_smfmac_f32_16x16x64_bf16 v[72:75], v[48:51], a[48:55], v10\n\t" \
    "ds_read_b128 v[44:47], v0 offset:19456\n\t" \
    "s_waitcnt lgkmcnt(5)\n\t" \
    "v_smfmac_f32_16x16x64_bf16 v[72:75], v[52:55], a[56:63], v10\n\t" \
    "s_waitcnt lgkmcnt(4)\n\t" \
    "v_smfmac_f32_16x16x64_bf16 v[72:75], v[56:59], a[0:7], v10\n\t" \
    "s_waitcnt lgkmcnt(3)\n\t" \
    "v_smfmac_f32_16x16x64_bf16 v[72:75], v[60:63], a[8:15], v10\n\t" \
    "s_waitcnt lgkmcnt(2)\n\t" \
    "v_smfmac_f32_16x16x64_bf16 v[72:75], v[64:67], a[16:23], v10\n\t" \
    "s_waitcnt lgkmcnt(1)\n\t" \
    "v_smfmac_f32_16x16x64_bf16 v[72:75], v[68:71], a[24:31], v10\n\t" \
    "s_waitcnt lgkmcnt(0)\n\t" \
    "v_smfmac_f32_16x16x64_bf16 v[72:75], v[44:47], a[64:71], v10\n\t" \
    "s_nop 2\n\t" \
    "v_smfmac_f32_16x16x64_bf16 v[76:79], v[48:51], a[120:127], v10\n\t" \
    "s_nop 2\n\t" \
    "v_smfmac_f32_16x16x64_bf16 v[76:79], v[52:55], a[128:135], v10\n\t" \
    "v_add_f32 v72, v72, v74\n\t" \
    "v_smfmac_f32_16x16x64_bf16 v[76:79], v[56:59], a[72:79], v10\n\t" \
    "v_add_f32 v73, v73, v75\n\t" \
    "v_smfmac_f32_16x16x64_bf16 v[76:79], v[60:63], a[80:87], v10\n\t" \
    "v_cvt_pk_bf16_f32 v156, v72, v73\n\t" \
    "v_smfmac_f32_16x16x64_bf16 v[76:79], v[64:67], a[88:95], v10\n\t" \
    "ds_write_b32 v12, v156 offset:0\n\t" \
    "v_smfmac_f32_16x16x64_bf16 v[76:79], v[68:71], a[96:103], v10\n\t" \
    "v_smfmac_f32_16x16x64_bf16 v[76:79], v[44:47], a[136:143], v10\n\t" \
    "s_nop 2\n\t" \
    "v_smfmac_f32_16x16x64_bf16 v[80:83], v[48:51], a[192:199], v10\n\t" \
    "s_nop 2\n\t" \
    "v_smfmac_f32_16x16x64_bf16 v[80:83], v[52:55], a[200:207], v10\n\t" \
    "v_add_f32 v76, v76, v78\n\t" \
    "v_smfmac_f32_16x16x64_bf16 v[80:83], v[56:59], a[144:151], v10\n\t" \
    "v_add_f32 v77, v77, v79\n\t" \
    "v_smfmac_f32_16x16x64_bf16 v[80:83], v[60:63], a[152:159], v10\n\t" \
    "v_cvt_pk_bf16_f32 v157, v76, v77\n\t" \
    "v_smfmac_f32_16x16x64_bf16 v[80:83], v[64:67], a[160:167], v10\n\t" \
    "ds_write_b32 v12, v157 offset:64\n\t" \
    "v_smfmac_f32_16x16x64_bf16 v[80:83], v[68:71], a[168:175], v10\n\t" \
    "v_smfmac_f32_16x16x64_bf16 v[80:83], v[44:47], a[208:215], v10\n\t" \
    "s_nop 2\n\t" \
    "v_smfmac_f32_16x16x64_bf16 v[84:87], v[48:51], v[232:239], v10\n\t" \
    "s_nop 2\n\t" \
    "v_smfmac_f32_16x16x64_bf16 v[84:87], v[52:55], v[240:247], v10\n\t" \
    "v_add_f32 v80, v80, v82\n\t" \
    "v_smfmac_f32_16x16x64_bf16 v[84:87], v[56:59], a[216:223], v10\n\t" \
    "v_add_f32 v81, v81, v83\n\t" \
    "v_smfmac_f32_16x16x64_bf16 v[84:87], v[60:63], a[224:231], v10\n\t" \
    "v_cvt_pk_bf16_f32 v158, v80, v81\n\t" \
    "v_smfmac_f32_16x16x64_bf16 v[84:87], v[64:67], a[232:239], v10\n\t" \
    "ds_write_b32 v16, v158 offset:0\n\t" \
    "v_smfmac_f32_16x16x64_bf16 v[84:87], v[68:71], a[240:247], v10\n\t" \
    "v_smfmac_f32_16x16x64_bf16 v[84:87], v[44:47], v[248:255], v10\n\t" \
    "s_nop 6\n\t" \
    "v_cmp_u_f32 vcc, v72, v73\n\t" \
    "v_pk_add_f32 v[84:85], v[84:85], v[86:87]\n\t" \
    "v_cvt_pk_bf16_f32 v159, v84, v85\n\t" \
    "ds_write_b32 v16, v159 offset:256\n\t" \
    "s_cbranch_vccnz qk_Gslow69_%=\n\t" \
    "qk_Gback70_%=:\n\t" \
    "s_waitcnt lgkmcnt(0)\n\t" \
    "s_barrier\n\t" \
    "s_sleep 1\n\t" \
    "ds_read_b64_tr_b16 v[140:141], v17 offset:0\n\t" \
    "ds_read_b64_tr_b16 v[142:143], v17 offset:256\n\t" \
    "ds_read_b64_tr_b16 v[144:145], v17 offset:512\n\t" \
    "ds_read_b64_tr_b16 v[146:147], v17 offset:768\n\t" \
    "ds_read_b64_tr_b16 v[148:149], v17 offset:1024\n\t" \
    "ds_read_b64_tr_b16 v[150:151], v17 offset:1280\n\t" \
    "ds_read_b64_tr_b16 v[152:153], v17 offset:1536\n\t" \
    "ds_read_b64_tr_b16 v[154:155], v17 offset:1792\n\t" \
    "s_waitcnt lgkmcnt(0)\n\t" \
    "s_cmp_eq_u32 s65, 63\n\t" \
    "s_cbranch_scc1 qk_last59_%=\n\t" \
    "s_cmp_ge_u32 s65, 58\n\t" \
    "s_cbranch_scc1 qk_nsl72_%=\n\t" \
    "s_sleep 6\n\t" \
    "global_store_dword v14, v25, s[100:101] sc1\n\t" \
    "qk_nsl72_%=:\n\t" \
    "s_mov_b64 exec, s[58:59]\n\t" \
    "global_store_dwordx4 v0, v[140:143], s[40:41] offset:0 sc1\n\t" \
    "global_store_dwordx4 v0, v[144:147], s[40:41] offset:1024 sc1\n\t" \
    "global_store_dwordx4 v0, v[148:151], s[40:41] offset:2048 sc1\n\t" \
    "global_store_dwordx4 v0, v[152:155], s[40:41] offset:3072 sc1\n\t" \
    "s_mov_b64 exec, -1\n\t" \
    "s_add_u32 s65, s65, 1\n\t" \
    "v_mov_b32 v72, 0\n\t" \
    "v_mov_b32 v73, 0\n\t" \
    "v_mov_b32 v74, 0\n\t" \
    "v_mov_b32 v75, 0\n\t" \
    "v_mov_b32 v76, 0\n\t" \
    "v_mov_b32 v77, 0\n\t" \
    "v_mov_b32 v78, 0\n\t" \
    "v_mov_b32 v79, 0\n\t" \
    "v_mov_b32 v80, 0\n\t" \
    "v_mov_b32 v81, 0\n\t" \
    "v_mov_b32 v82, 0\n\t" \
    "v_mov_b32 v83, 0\n\t" \
    "v_mov_b32 v84, 0\n\t" \
    "v_mov_b32 v85, 0\n\t" \
    "v_mov_b32 v86, 0\n\t" \
    "v_mov_b32 v87, 0\n\t" \
    "s_add_u32 s69, s65, 0\n\t" \
    "s_and_b32 s69, s69, 7\n\t" \
    "s_lshl_b32 s69, s69, 18\n\t" \
    "s_add_u32 s69, s69, s62\n\t" \
    "s_add_u32 s46, s36, s69\n\t" \
    "s_addc_u32 s47, s37, 0\n\t" \
    "s_mul_i32 s70, s67, 7\n\t" \
    "s_add_u32 s69, s65, 0\n\t" \
    "s_xor_b32 s69, s69, s70\n\t" \
    "s_and_b32 s69, s69, 63\n\t" \
    "s_lshl_b32 s69, s69, 12\n\t" \
    "s_mul_i32 s70, s67, 262144\n\t" \
    "s_add_u32 s69, s69, s70\n\t" \
    "s_add_u32 s69, s69, 39845888\n\t" \
    "s_add_u32 s40, s36, s69\n\t" \
    "s_addc_u32 s41, s37, 0\n\t" \
    "s_sub_u32 s91, s65, 1\n\t" \
    "v_mov_b32 v25, s91\n\t" \
    "s_barrier\n\t" \
    "ds_read_b32 v168, v11 offset:20480\n\t" \
    "s_waitcnt lgkmcnt(0)\n\t" \
    "v_readfirstlane_b32 s70, v168\n\t" \
    "ds_write_b32 v12, v31 offset:0\n\t" \
    "ds_write_b32 v12, v31 offset:64\n\t" \
    "s_branch qk_go60_%=\n\t" \
    "qk_Gbad67_%=:\n\t" \
    "s_waitcnt vmcnt(0) lgkmcnt(0)\n\t" \
    "s_nop 7\n\t" \
    "v_mov_b32 v72, 0\n\t" \
    "v_mov_b32 v73, 0\n\t" \
    "v_mov_b32 v74, 0\n\t" \
    "v_mov_b32 v75, 0\n\t" \
    "v_mov_b32 v76, 0\n\t" \
    "v_mov_b32 v77, 0\n\t" \
    "v_mov_b32 v78, 0\n\t" \
    "v_mov_b32 v79, 0\n\t" \
    "v_mov_b32 v80, 0\n\t" \
    "v_mov_b32 v81, 0\n\t" \
    "v_mov_b32 v82, 0\n\t" \
    "v_mov_b32 v83, 0\n\t" \
    "v_mov_b32 v84, 0\n\t" \
    "v_mov_b32 v85, 0\n\t" \
    "v_mov_b32 v86, 0\n\t" \
    "v_mov_b32 v87, 0\n\t" \
    "s_cmp_eq_u32 s68, 0\n\t" \
    "s_cbranch_scc1 qk_F73_%=\n\t" \
    "s_sub_u32 s68, s68, 1\n\t" \
    "s_branch qk_G66_%=\n\t" \
    "qk_F73_%=:\n\t" \
    "v_mov_b32 v160, 5767424\n\t" \
    "v_mov_b32 v161, 1\n\t" \
    "global_store_dword v160, v161, s[36:37] sc1\n\t" \
    "s_waitcnt vmcnt(0)\n\t" \
    "s_branch qk_Gc68_%=\n\t" \
    "qk_Gslow69_%=:\n\t" \
    "v_max_u32 v160, v36, v37\n\t" \
    "v_max3_u32 v160, v160, v38, v39\n\t" \
    "v_max3_u32 v160, v160, v40, v41\n\t" \
    "v_max3_u32 v160, v160, v42, v43\n\t" \
    "v_max3_u32 v160, v160, v44, v45\n\t" \
    "v_max3_u32 v160, v160, v46, v47\n\t" \
    "v_max3_u32 v160, v160, v48, v49\n\t" \
    "v_max3_u32 v160, v160, v50, v51\n\t" \
    "v_max3_u32 v160, v160, v52, v53\n\t" \
    "v_max3_u32 v160, v160, v54, v55\n\t" \
    "v_max3_u32 v160, v160, v56, v57\n\t" \
    "v_max3_u32 v160, v160, v58, v59\n\t" \
    "v_max3_u32 v160, v160, v60, v61\n\t" \
    "v_max3_u32 v160, v160, v62, v63\n\t" \
    "v_max3_u32 v160, v160, v64, v65\n\t" \
    "v_max3_u32 v160, v160, v66, v67\n\t" \
    "v_max3_u32 v160, v160, v68, v69\n\t" \
    "v_max3_u32 v160, v160, v70, v71\n\t" \
    "v_cmp_eq_u32 vcc, -1, v160\n\t" \
    "s_cbranch_vccz qk_Gback70_%=\n\t" \
    "s_waitcnt lgkmcnt(0)\n\t" \
    "v_sub_u32 v160, v1, v3\n\t" \
    "s_nop 0\n\t" \
    "v_readfirstlane_b32 s69, v160\n\t" \
    "v_add_u32 v163, 20480, v0\n\t" \
    "s_nop 7\n\t" \
    "s_add_u32 s30, s46, s69\n\t" \
    "s_addc_u32 s31, s47, 0\n\t" \
    "qk_Gag74_%=:\n\t" \
    "global_load_dwordx4 v[36:39], v1, s[46:47] sc1\n\t" \
    "global_load_dwordx4 v[40:43], v23, s[46:47] sc1\n\t" \
    "global_load_dwordx4 v[44:47], v163, s[30:31] sc1\n\t" \
    "global_load_dwordx4 v[48:51], v4, s[30:31] sc1\n\t" \
    "global_load_dwordx4 v[52:55], v5, s[30:31] sc1\n\t" \
    "global_load_dwordx4 v[56:59], v6, s[30:31] sc1\n\t" \
    "global_load_dwordx4 v[60:63], v7, s[30:31] sc1\n\t" \
    "global_load_dwordx4 v[64:67], v8, s[30:31] sc1\n\t" \
    "global_load_dwordx4 v[68:71], v9, s[30:31] sc1\n\t" \
    "s_waitcnt vmcnt(0)\n\t" \
    "v_max_u32 v160, v36, v37\n\t" \
    "v_max3_u32 v160, v160, v38, v39\n\t" \
    "v_max3_u32 v160, v160, v40, v41\n\t" \
    "v_max3_u32 v160, v160, v42, v43\n\t" \
    "v_max3_u32 v160, v160, v44, v45\n\t" \
    "v_max3_u32 v160, v160, v46, v47\n\t" \
    "v_max3_u32 v160, v160, v48, v49\n\t" \
    "v_max3_u32 v160, v160, v50, v51\n\t" \
    "v_max3_u32 v160, v160, v52, v53\n\t" \
    "v_max3_u32 v160, v160, v54, v55\n\t" \
    "v_max3_u32 v160, v160, v56, v57\n\t" \
    "v_max3_u32 v160, v160, v58, v59\n\t" \
    "v_max3_u32 v160, v160, v60, v61\n\t" \
    "v_max3_u32 v160, v160, v62, v63\n\t" \
    "v_max3_u32 v160, v160, v64, v65\n\t" \
    "v_max3_u32 v160, v160, v66, v67\n\t" \
    "v_max3_u32 v160, v160, v68, v69\n\t" \
    "v_max3_u32 v160, v160, v70, v71\n\t" \
    "v_cmp_eq_u32 vcc, -1, v160\n\t" \
    "s_cbranch_vccz qk_Gredo75_%=\n\t" \
    "s_cmp_eq_u32 s68, 0\n\t" \
    "s_cbranch_scc1 qk_F76_%=\n\t" \
    "s_sub_u32 s68, s68, 1\n\t" \
    "s_sleep 1\n\t" \
    "s_branch qk_Gag74_%=\n\t" \
    "qk_F76_%=:\n\t" \
    "v_mov_b32 v160, 5767424\n\t" \
    "v_mov_b32 v161, 2\n\t" \
    "global_store_dword v160, v161, s[36:37] sc1\n\t" \
    "s_waitcnt vmcnt(0)\n\t" \
    "s_branch qk_Gredo75_%=\n\t" \
    "qk_Gredo75_%=:\n\t" \
    "v_mov_b32 v72, 0\n\t" \
    "v_mov_b32 v73, 0\n\t" \
    "v_mov_b32 v74, 0\n\t" \
    "v_mov_b32 v75, 0\n\t" \
    "v_mov_b32 v76, 0\n\t" \
    "v_mov_b32 v77, 0\n\t" \
    "v_mov_b32 v78, 0\n\t" \
    "v_mov_b32 v79, 0\n\t" \
    "v_mov_b32 v80, 0\n\t" \
    "v_mov_b32 v81, 0\n\t" \
    "v_mov_b32 v82, 0\n\t" \
    "v_mov_b32 v83, 0\n\t" \
    "v_mov_b32 v84, 0\n\t" \
    "v_mov_b32 v85, 0\n\t" \
    "v_mov_b32 v86, 0\n\t" \
    "v_mov_b32 v87, 0\n\t" \
    "v_smfmac_f32_16x16x64_bf16 v[72:75], v[36:39], a[32:39], v10\n\t" \
    "v_smfmac_f32_16x16x64_bf16 v[72:75], v[40:43], a[40:47], v10\n\t" \
    "v_smfmac_f32_16x16x64_bf16 v[72:75], v[48:51], a[48:55], v10\n\t" \
    "v_smfmac_f32_16x16x64_bf16 v[72:75], v[52:55], a[56:63], v10\n\t" \
    "v_smfmac_f32_16x16x64_bf16 v[72:75], v[56:59], a[0:7], v10\n\t" \
    "v_smfmac_f32_16x16x64_bf16 v[72:75], v[60:63], a[8:15], v10\n\t" \
    "v_smfmac_f32_16x16x64_bf16 v[72:75], v[64:67], a[16:23], v10\n\t" \
    "v_smfmac_f32_16x16x64_bf16 v[72:75], v[68:71], a[24:31], v10\n\t" \
    "v_smfmac_f32_16x16x64_bf16 v[72:75], v[44:47], a[64:71], v10\n\t" \
    "v_smfmac_f32_16x16x64_bf16 v[76:79], v[36:39], a[104:111], v10\n\t" \
    "v_smfmac_f32_16x16x64_bf16 v[76:79], v[40:43], a[112:119], v10\n\t" \
    "v_smfmac_f32_16x16x64_bf16 v[76:79], v[48:51], a[120:127], v10\n\t" \
    "v_smfmac_f32_16x16x64_bf16 v[76:79], v[52:55], a[128:135], v10\n\t" \
    "v_smfmac_f32_16x16x64_bf16 v[76:79], v[56:59], a[72:79], v10\n\t" \
    "v_smfmac_f32_16x16x64_bf16 v[76:79], v[60:63], a[80:87], v10\n\t" \
    "v_smfmac_f32_16x16x64_bf16 v[76:79], v[64:67], a[88:95], v10\n\t" \
    "v_smfmac_f32_16x16x64_bf16 v[76:79], v[68:71], a[96:103], v10\n\t" \
    "v_smfmac_f32_16x16x64_bf16 v[76:79], v[44:47], a[136:143], v10\n\t" \
    "v_smfmac_f32_16x16x64_bf16 v[80:83], v[36:39], a[176:183], v10\n\t" \
    "v_smfmac_f32_16x16x64_bf16 v[80:83], v[40:43], a[184:191], v10\n\t" \
    "v_smfmac_f32_16x16x64_bf16 v[80:83], v[48:51], a[192:199], v10\n\t" \
    "v_smfmac_f32_16x16x64_bf16 v[80:83], v[52:55], a[200:207], v10\n\t" \
    "v_smfmac_f32_16x16x64_bf16 v[80:83], v[56:59], a[144:151], v10\n\t" \
    "v_smfmac_f32_16x16x64_bf16 v[80:83], v[60:63], a[152:159], v10\n\t" \
    "v_smfmac_f32_16x16x64_bf16 v[80:83], v[64:67], a[160:167], v10\n\t" \
    "v_smfmac_f32_16x16x64_bf16 v[80:83], v[68:71], a[168:175], v10\n\t" \
    "v_smfmac_f32_16x16x64_bf16 v[80:83], v[44:47], a[208:215], v10\n\t" \
    "v_smfmac_f32_16x16x64_bf16 v[84:87], v[36:39], a[248:255], v10\n\t" \
    "v_smfmac_f32_16x16x64_bf16 v[84:87], v[40:43], v[224:231], v10\n\t" \
    "v_smfmac_f32_16x16x64_bf16 v[84:87], v[48:51], v[232:239], v10\n\t" \
    "v_smfmac_f32_16x16x64_bf16 v[84:87], v[52:55], v[240:247], v10\n\t" \
    "v_smfmac_f32_16x16x64_bf16 v[84:87], v[56:59], a[216:223], v10\n\t" \
    "v_smfmac_f32_16x16x64_bf16 v[84:87], v[60:63], a[224:231], v10\n\t" \
    "v_smfmac_f32_16x16x64_bf16 v[84:87], v[64:67], a[232:239], v10\n\t" \
    "v_smfmac_f32_16x16x64_bf16 v[84:87], v[68:71], a[240:247], v10\n\t" \
    "v_smfmac_f32_16x16x64_bf16 v[84:87], v[44:47], v[248:255], v10\n\t" \
    "v_add_f32 v72, v72, v74\n\t" \
    "v_add_f32 v73, v73, v75\n\t" \
    "v_cvt_pk_bf16_f32 v156, v72, v73\n\t" \
    "ds_write_b32 v12, v156 offset:0\n\t" \
    "v_add_f32 v76, v76, v78\n\t" \
    "v_add_f32 v77, v77, v79\n\t" \
    "v_cvt_pk_bf16_f32 v157, v76, v77\n\t" \
    "ds_write_b32 v12, v157 offset:64\n\t" \
    "v_add_f32 v80, v80, v82\n\t" \
    "v_add_f32 v81, v81, v83\n\t" \
    "v_cvt_pk_bf16_f32 v158, v80, v81\n\t" \
    "ds_write_b32 v16, v158 offset:0\n\t" \
    "v_pk_add_f32 v[84:85], v[84:85], v[86:87]\n\t" \
    "v_cvt_pk_bf16_f32 v159, v84, v85\n\t" \
    "ds_write_b32 v16, v159 offset:256\n\t" \
    "s_branch qk_Gback70_%=\n\t" \
    "qk_kf61_%=:\n\t" \
    "global_store_dword v14, v25, s[100:101] sc1\n\t" \
    "s_branch qk_kb62_%=\n\t" \
    "qk_last59_%=:\n\t" \
    "s_mov_b64 exec, s[58:59]\n\t" \
    "global_store_dwordx4 v0, v[140:143], s[40:41] offset:0 sc1\n\t" \
    "global_store_dwordx4 v0, v[144:147], s[40:41] offset:1024 sc1\n\t" \
    "global_store_dwordx4 v0, v[148:151], s[40:41] offset:2048 sc1\n\t" \
    "global_store_dwordx4 v0, v[152:155], s[40:41] offset:3072 sc1\n\t" \
    "s_mov_b64 exec, -1\n\t" \
    "s_endpgm\n\t" \
    "qk_loop14_%=:\n\t" \
    "s_mov_b32 s38, 0x1b8d00\n\t" \
    "s_mov_b32 s39, 0x0\n\t" \
    "v_mov_b32 v72, 0\n\t" \
    "v_mov_b32 v73, 0\n\t" \
    "v_mov_b32 v74, 0\n\t" \
    "v_mov_b32 v75, 0\n\t" \
    "v_mov_b32 v76, 0\n\t" \
    "v_mov_b32 v77, 0\n\t" \
    "v_mov_b32 v78, 0\n\t" \
    "v_mov_b32 v79, 0\n\t" \
    "v_mov_b32 v80, 0\n\t" \
    "v_mov_b32 v81, 0\n\t" \
    "v_mov_b32 v82, 0\n\t" \
    "v_mov_b32 v83, 0\n\t" \
    "v_mov_b32 v84, 0\n\t" \
    "v_mov_b32 v85, 0\n\t" \
    "v_mov_b32 v86, 0\n\t" \
    "v_mov_b32 v87, 0\n\t" \
    "s_add_u32 s69, s65, 0\n\t" \
    "s_and_b32 s69, s69, 7\n\t" \
    "s_lshl_b32 s69, s69, 18\n\t" \
    "s_add_u32 s69, s69, s62\n\t" \
    "s_add_u32 s46, s36, s69\n\t" \
    "s_addc_u32 s47, s37, 0\n\t" \
    "s_lshr_b64 s[38:39], s[38:39], 4\n\t" \
    "s_and_b32 s97, s38, 15\n\t" \
    "s_lshl_b32 s97, s97, 6\n\t" \
    "s_and_b32 s69, s65, 7\n\t" \
    "s_lshl_b32 s69, s69, 18\n\t" \
    "s_add_u32 s69, s69, s63\n\t" \
    "s_add_u32 s52, s36, s69\n\t" \
    "s_addc_u32 s53, s37, 0\n\t" \
    "s_sub_u32 s70, s79, s74\n\t" \
    "s_max_i32 s70, s70, 0\n\t" \
    "s_min_i32 s70, s70, 16000\n\t" \
    "s_lshl_b32 s70, s70, 1\n\t" \
    "s_mul_hi_u32 s70, s70, s77\n\t" \
    "s_add_u32 s70, s70, s73\n\t" \
    ".p2align 6\n\t" \
    "s_nop 0\n\t" \
    "s_nop 0\n\t" \
    "s_nop 0\n\t" \
    "s_nop 0\n\t" \
    "s_nop 0\n\t" \
    "s_nop 0\n\t" \
    "qk_go78_%=:\n\t" \
    "s_getpc_b64 s[30:31]\n\t" \
    "qk_Wp80_%=:\n\t" \
    "s_add_u32 s30, s30, qk_We81_%=-qk_Wp80_%=\n\t" \
    "s_addc_u32 s31, s31, 0\n\t" \
    "s_sub_u32 s70, s70, 96\n\t" \
    "qk_W79_%=:\n\t" \
    "s_memtime s[56:57]\n\t" \
    "s_waitcnt lgkmcnt(0)\n\t" \
    "s_sub_u32 s69, s70, s56\n\t" \
    "s_cmp_gt_i32 s69, 80\n\t" \
    "s_cbranch_scc1 qk_W79_%=\n\t" \
    "s_max_i32 s69, s69, 0\n\t" \
    "s_and_b32 s69, s69, -4\n\t" \
    "s_sub_u32 s30, s30, s69\n\t" \
    "s_subb_u32 s31, s31, 0\n\t" \
    "s_setpc_b64 s[30:31]\n\t" \
    "s_nop 0\n\t" \
    "s_nop 0\n\t" \
    "s_nop 0\n\t" \
    "s_nop 0\n\t" \
    "s_nop 0\n\t" \
    "s_nop 0\n\t" \
    "s_nop 0\n\t" \
    "s_nop 0\n\t" \
    "s_nop 0\n\t" \
    "s_nop 0\n\t" \
    "s_nop 0\n\t" \
    "s_nop 0\n\t" \
    "s_nop 0\n\t" \
    "s_nop 0\n\t" \
    "s_nop 0\n\t" \
    "s_nop 0\n\t" \
    "s_nop 0\n\t" \
    "s_nop 0\n\t" \
    "s_nop 0\n\t" \
    "s_nop 0\n\t" \
    "qk_We81_%=:\n\t" \
    "qk_G82_%=:\n\t" \
    "global_load_dwordx4 v[36:39], v1, s[46:47] sc1\n\t" \
    "global_load_dword v161, v2, s[46:47] sc1\n\t" \
    "global_load_dwordx4 v[40:43], v23, s[46:47] sc1\n\t" \
    "s_mul_hi_u32 s69, s84, s78\n\t" \
    "s_add_u32 s33, s74, s69\n\t" \
    "s_add_u32 s33, s33, s97\n\t" \
    "s_lshr_b32 s69, s90, 16\n\t" \
    "s_add_u32 s34, s33, s69\n\t" \
    "s_and_b32 s69, s90, 0xffff\n\t" \
    "s_sub_u32 s35, s80, s74\n\t" \
    "s_sub_u32 s35, s35, s69\n\t" \
    "s_add_u32 s99, s35, 240\n\t" \
    "s_add_u32 s98, s33, 448\n\t" \
    "s_sub_u32 s96, s79, s74\n\t" \
    "s_cmp_le_u32 s65, 0\n\t" \
    "s_cselect_b32 s69, 112, 0\n\t" \
    "s_add_u32 s69, s69, 448\n\t" \
    "s_add_u32 s96, s96, s69\n\t" \
    "s_max_i32 s35, s35, s96\n\t" \
    "s_add_u32 s99, s35, 716\n\t" \
    "s_mul_i32 s69, s35, 10\n\t" \
    "s_ashr_i32 s69, s69, 4\n\t" \
    "s_sub_u32 s69, s33, s69\n\t" \
    "v_lshrrev_b32_e64 v163, 1, s69\n\t" \
    "s_waitcnt vmcnt(2)\n\t" \
    "qk_Garr87_%=:\n\t" \
    "ds_write_b128 v3, v[36:39]\n\t" \
    "v_smfmac_f32_16x16x64_bf16 v[72:75], v[36:39], a[48:55], v10\n\t" \
    "s_waitcnt vmcnt(1)\n\t" \
    "ds_write_b32 v22, v161 offset:20224\n\t" \
    "v_smfmac_f32_16x16x64_bf16 v[76:79], v[36:39], a[120:127], v10\n\t" \
    "s_waitcnt vmcnt(0)\n\t" \
    "ds_write_b128 v3, v[40:43] offset:1024\n\t" \
    "v_max3_u32 v160, v36, v40, v161\n\t" \
    "v_cmp_eq_u32 vcc, -1, v160\n\t" \
    "v_smfmac_f32_16x16x64_bf16 v[80:83], v[36:39], a[192:199], v10\n\t" \
    "v_smfmac_f32_16x16x64_bf16 v[84:87], v[36:39], v[232:239], v10\n\t" \
    "v_smfmac_f32_16x16x64_bf16 v[72:75], v[40:43], a[56:63], v10\n\t" \
    "s_cbranch_vccnz qk_Gbad83_%=\n\t" \
    "qk_Gc84_%=:\n\t" \
    "s_waitcnt lgkmcnt(0)\n\t" \
    "s_barrier\n\t" \
    "s_memtime s[56:57]\n\t" \
    "ds_read_b128 v[48:51], v4\n\t" \
    "ds_read_b128 v[52:55], v5\n\t" \
    "v_smfmac_f32_16x16x64_bf16 v[76:79], v[40:43], a[128:135], v10\n\t" \
    "ds_read_b128 v[56:59], v6\n\t" \
    "ds_read_b128 v[60:63], v7\n\t" \
    "v_smfmac_f32_16x16x64_bf16 v[80:83], v[40:43], a[200:207], v10\n\t" \
    "ds_read_b128 v[64:67], v8\n\t" \
    "ds_read_b128 v[68:71], v9\n\t" \
    "v_smfmac_f32_16x16x64_bf16 v[84:87], v[40:43], v[240:247], v10\n\t" \
    "ds_read_b128 v[44:47], v0 offset:19456\n\t" \
    "s_waitcnt lgkmcnt(6)\n\t" \
    "v_smfmac_f32_16x16x64_bf16 v[72:75], v[48:51], a[0:7], v10\n\t" \
    "s_sub_u32 s69, s56, s73\n\t" \
    "s_mul_hi_u32 s69, s69, s78\n\t" \
    "s_waitcnt lgkmcnt(5)\n\t" \
    "v_smfmac_f32_16x16x64_bf16 v[72:75], v[52:55], a[8:15], v10\n\t" \
    "s_max_i32 s69, s69, s96\n\t" \
    "s_max_i32 s70, s69, s35\n\t" \
    "s_waitcnt lgkmcnt(4)\n\t" \
    "v_smfmac_f32_16x16x64_bf16 v[72:75], v[56:59], a[16:23], v10\n\t" \
    "s_min_i32 s70, s70, s99\n\t" \
    "s_mul_i32 s70, s70, 10\n\t" \
    "s_waitcnt lgkmcnt(3)\n\t" \
    "v_smfmac_f32_16x16x64_bf16 v[72:75], v[60:63], a[24:31], v10\n\t" \
    "s_lshl4_add_u32 s70, s69, s70\n\t" \
    "s_ashr_i32 s70, s70, 5\n\t" \
    "v_add_lshl_u32 v162, s70, v163, 1\n\t" \
    "s_waitcnt lgkmcnt(2)\n\t" \
    "v_smfmac_f32_16x16x64_bf16 v[72:75], v[64:67], a[32:39], v10\n\t" \
    "global_store_dword v21, v162, s[52:53] sc1\n\t" \
    "s_waitcnt lgkmcnt(1)\n\t" \
    "v_smfmac_f32_16x16x64_bf16 v[72:75], v[68:71], a[40:47], v10\n\t" \
    "s_waitcnt lgkmcnt(0)\n\t" \
    "v_smfmac_f32_16x16x64_bf16 v[72:75], v[44:47], a[64:71], v10\n\t" \
    "s_nop 2\n\t" \
    "v_smfmac_f32_16x16x64_bf16 v[76:79], v[48:51], a[72:79], v10\n\t" \
    "s_nop 2\n\t" \
    "v_smfmac_f32_16x16x64_bf16 v[76:79], v[52:55], a[80:87], v10\n\t" \
    "v_add_f32 v72, v72, v74\n\t" \
    "v_smfmac_f32_16x16x64_bf16 v[76:79], v[56:59], a[88:95], v10\n\t" \
    "v_add_f32 v73, v73, v75\n\t" \
    "v_smfmac_f32_16x16x64_bf16 v[76:79], v[60:63], a[96:103], v10\n\t" \
    "v_cvt_pk_bf16_f32 v156, v72, v73\n\t" \
    "v_smfmac_f32_16x16x64_bf16 v[76:79], v[64:67], a[104:111], v10\n\t" \
    "ds_write_b32 v12, v156 offset:0\n\t" \
    "v_smfmac_f32_16x16x64_bf16 v[76:79], v[68:71], a[112:119], v10\n\t" \
    "v_smfmac_f32_16x16x64_bf16 v[76:79], v[44:47], a[136:143], v10\n\t" \
    "s_nop 2\n\t" \
    "v_smfmac_f32_16x16x64_bf16 v[80:83], v[48:51], a[144:151], v10\n\t" \
    "s_nop 2\n\t" \
    "v_smfmac_f32_16x16x64_bf16 v[80:83], v[52:55], a[152:159], v10\n\t" \
    "v_add_f32 v76, v76, v78\n\t" \
    "v_smfmac_f32_16x16x64_bf16 v[80:83], v[56:59], a[160:167], v10\n\t" \
    "v_add_f32 v77, v77, v79\n\t" \
    "v_smfmac_f32_16x16x64_bf16 v[80:83], v[60:63], a[168:175], v10\n\t" \
    "v_cvt_pk_bf16_f32 v157, v76, v77\n\t" \
    "v_smfmac_f32_16x16x64_bf16 v[80:83], v[64:67], a[176:183], v10\n\t" \
    "ds_write_b32 v12, v157 offset:64\n\t" \
    "v_smfmac_f32_16x16x64_bf16 v[80:83], v[68:71], a[184:191], v10\n\t" \
    "v_smfmac_f32_16x16x64_bf16 v[80:83], v[44:47], a[208:215], v10\n\t" \
    "s_nop 2\n\t" \
    "v_smfmac_f32_16x16x64_bf16 v[84:87], v[48:51], a[216:223], v10\n\t" \
    "s_nop 2\n\t" \
    "v_smfmac_f32_16x16x64_bf16 v[84:87], v[52:55], a[224:231], v10\n\t" \
    "v_add_f32 v80, v80, v82\n\t" \
    "v_smfmac_f32_16x16x64_bf16 v[84:87], v[56:59], a[232:239], v10\n\t" \
    "v_add_f32 v81, v81, v83\n\t" \
    "v_smfmac_f32_16x16x64_bf16 v[84:87], v[60:63], a[240:247], v10\n\t" \
    "v_cvt_pk_bf16_f32 v158, v80, v81\n\t" \
    "v_smfmac_f32_16x16x64_bf16 v[84:87], v[64:67], a[248:255], v10\n\t" \
    "ds_write_b32 v16, v158 offset:0\n\t" \
    "v_smfmac_f32_16x16x64_bf16 v[84:87], v[68:71], v[224:231], v10\n\t" \
    "v_smfmac_f32_16x16x64_bf16 v[84:87], v[44:47], v[248:255], v10\n\t" \
    "s_nop 6\n\t" \
    "v_cmp_u_f32 vcc, v72, v73\n\t" \
    "v_pk_add_f32 v[84:85], v[84:85], v[86:87]\n\t" \
    "v_cvt_pk_bf16_f32 v159, v84, v85\n\t" \
    "ds_write_b32 v16, v159 offset:256\n\t" \
    "s_cbranch_vccnz qk_Gslow85_%=\n\t" \
    "qk_Gback86_%=:\n\t" \
    "s_waitcnt lgkmcnt(0)\n\t" \
    "s_barrier\n\t" \
    "s_cmp_eq_u32 s65, 63\n\t" \
    "s_cbranch_scc1 qk_last77_%=\n\t" \
    "s_cmp_lg_u32 s65, 0\n\t" \
    "s_cbranch_scc1 qk_ng88_%=\n\t" \
    "v_mov_b32 v168, 5769216\n\t" \
    "v_mov_b32 v169, 1\n\t" \
    "s_mov_b64 exec, 1\n\t" \
    "global_atomic_add v170, v168, v169, s[36:37] sc0 sc1\n\t" \
    "s_mov_b64 exec, -1\n\t" \
    "v_mbcnt_lo_u32_b32 v168, -1, 0\n\t" \
    "v_and_b32 v169, 3, v168\n\t" \
    "v_lshlrev_b32 v169, 16, v169\n\t" \
    "v_bfe_u32 v171, v168, 2, 2\n\t" \
    "v_lshl_add_u32 v169, v171, 11, v169\n\t" \
    "v_bfe_u32 v171, v168, 4, 1\n\t" \
    "v_lshl_add_u32 v169, v171, 14, v169\n\t" \
    "v_add_u32 v169, s63, v169\n\t" \
    "s_waitcnt vmcnt(0)\n\t" \
    "v_readfirstlane_b32 s69, v170\n\t" \
    "s_nop 1\n\t" \
    "s_cmp_lg_u32 s69, 126\n\t" \
    "s_cbranch_scc1 qk_go89_%=\n\t" \
    "s_memrealtime s[56:57]\n\t" \
    "s_waitcnt lgkmcnt(0)\n\t" \
    "s_lshl_b32 s69, s56, 4\n\t" \
    "s_add_u32 s69, s69, 2960\n\t" \
    "s_add_u32 s70, s56, 24\n\t" \
    "s_lshr_b32 s70, s70, 4\n\t" \
    "v_lshrrev_b32 v171, 2, v168\n\t" \
    "v_add_u32 v171, s70, v171\n\t" \
    "v_and_b32 v171, 63, v171\n\t" \
    "v_lshlrev_b32 v171, 6, v171\n\t" \
    "v_and_b32 v170, 3, v168\n\t" \
    "v_lshl_add_u32 v171, v170, 12, v171\n\t" \
    "v_add_u32 v171, 5783552, v171\n\t" \
    "v_mov_b32 v168, s69\n\t" \
    "s_mov_b32 exec_hi, 0\n\t" \
    "global_store_dword v171, v168, s[36:37] sc1\n\t" \
    "global_store_dword v169, v168, s[36:37] sc1\n\t" \
    "s_mov_b32 exec_hi, -1\n\t" \
    "qk_go89_%=:\n\t" \
    "v_mov_b32 v21, v33\n\t" \
    "qk_ng88_%=:\n\t" \
    "s_sub_u32 s70, s80, s74\n\t" \
    "s_max_i32 s70, s70, 0\n\t" \
    "s_min_i32 s70, s70, 16000\n\t" \
    "s_lshl_b32 s70, s70, 1\n\t" \
    "s_mul_hi_u32 s70, s70, s77\n\t" \
    "s_add_u32 s70, s70, s73\n\t" \
    "qk_W90_%=:\n\t" \
    "s_memtime s[56:57]\n\t" \
    "s_waitcnt lgkmcnt(0)\n\t" \
    "s_sub_u32 s69, s70, s56\n\t" \
    "s_cmp_gt_i32 s69, 24\n\t" \
    "s_cbranch_scc1 qk_W90_%=\n\t" \
    "global_load_dword v160, v20, s[52:53] sc1\n\t" \
    "s_waitcnt lgkmcnt(0)\n\t" \
    "s_mov_b32 s70, 116\n\t" \
    "qk_E94_%=:\n\t" \
    "s_memtime s[56:57]\n\t" \
    "s_memrealtime s[2:3]\n\t" \
    "s_nop 0\n\t" \
    "s_memrealtime s[4:5]\n\t" \
    "s_nop 0\n\t" \
    "s_memrealtime s[6:7]\n\t" \
    "s_nop 0\n\t" \
    "s_memrealtime s[8:9]\n\t" \
    "s_nop 0\n\t" \
    "s_memrealtime s[10:11]\n\t" \
    "s_nop 0\n\t" \
    "s_memrealtime s[12:13]\n\t" \
    "s_nop 0\n\t" \
    "s_memrealtime s[14:15]\n\t" \
    "s_nop 0\n\t" \
    "s_memrealtime s[16:17]\n\t" \
    "s_nop 0\n\t" \
    "s_memrealtime s[18:19]\n\t" \
    "s_nop 0\n\t" \
    "s_memrealtime s[20:21]\n\t" \
    "s_nop 0\n\t" \
    "s_memrealtime s[22:23]\n\t" \
    "s_nop 0\n\t" \
    "s_memrealtime s[24:25]\n\t" \
    "s_nop 0\n\t" \
    "s_memrealtime s[26:27]\n\t" \
    "s_nop 0\n\t" \
    "s_memrealtime s[28:29]\n\t" \
    "s_memtime s[54:55]\n\t" \
    "s_add_u32 s70, s70, 8\n\t" \
    "s_waitcnt lgkmcnt(0)\n\t" \
    "s_sub_u32 s69, s54, s56\n\t" \
    "s_cmp_gt_u32 s69, s70\n\t" \
    "s_cbranch_scc1 qk_E94_%=\n\t" \
    "s_add_u32 s2, s2, s4\n\t" \
    "s_add_u32 s6, s6, s8\n\t" \
    "s_add_u32 s10, s10, s12\n\t" \
    "s_add_u32 s14, s14, s16\n\t" \
    "s_add_u32 s18, s18, s20\n\t" \
    "s_add_u32 s22, s22, s24\n\t" \
    "s_add_u32 s2, s2, s6\n\t" \
    "s_add_u32 s10, s10, s14\n\t" \
    "s_add_u32 s18, s18, s22\n\t" \
    "s_add_u32 s2, s2, s26\n\t" \
    "s_add_u32 s10, s10, s18\n\t" \
    "s_add_u32 s2, s2, s10\n\t" \
    "s_mul_i32 s70, s28, 13\n\t" \
    "s_sub_u32 s70, s70, s2\n\t" \
    "s_min_u32 s70, s70, 52\n\t" \
    "s_mul_i32 s69, s69, s70\n\t" \
    "s_mul_hi_u32 s69, s69, 76695844\n\t" \
    "s_mov_b32 s75, s73\n\t" \
    "s_mov_b32 s76, s74\n\t" \
    "s_add_u32 s73, s56, s69\n\t" \
    "s_lshl_b32 s74, s28, 4\n\t" \
    "s_sub_u32 s69, s73, s75\n\t" \
    "s_sub_u32 s70, s74, s76\n\t" \
    "s_sub_u32 s71, s70, 1600\n\t" \
    "s_cmp_lt_u32 s71, 400000\n\t" \
    "s_cbranch_scc0 qk_K95_%=\n\t" \
    "v_cvt_f32_u32 v168, s69\n\t" \
    "v_cvt_f32_u32 v169, s70\n\t" \
    "v_rcp_f32 v170, v168\n\t" \
    "v_rcp_f32 v171, v169\n\t" \
    "v_mul_f32 v170, v170, v169\n\t" \
    "v_mul_f32 v171, v171, v168\n\t" \
    "v_mul_f32 v170, 0x4f800000, v170\n\t" \
    "v_mul_f32 v171, 0x4f000000, v171\n\t" \
    "v_cvt_u32_f32 v170, v170\n\t" \
    "v_cvt_u32_f32 v171, v171\n\t" \
    "v_readfirstlane_b32 s69, v170\n\t" \
    "v_readfirstlane_b32 s70, v171\n\t" \
    "s_nop 1\n\t" \
    "s_min_u32 s71, s65, 1\n\t" \
    "s_sub_u32 s69, s69, s78\n\t" \
    "s_ashr_i32 s69, s69, s71\n\t" \
    "s_add_u32 s78, s78, s69\n\t" \
    "s_sub_u32 s70, s70, s77\n\t" \
    "s_ashr_i32 s70, s70, s71\n\t" \
    "s_add_u32 s77, s77, s70\n\t" \
    "qk_K95_%=:\n\t" \
    "s_add_u32 s65, s65, 1\n\t" \
    "v_mov_b32 v72, 0\n\t" \
    "v_mov_b32 v73, 0\n\t" \
    "v_mov_b32 v74, 0\n\t" \
    "v_mov_b32 v75, 0\n\t" \
    "v_mov_b32 v76, 0\n\t" \
    "v_mov_b32 v77, 0\n\t" \
    "v_mov_b32 v78, 0\n\t" \
    "v_mov_b32 v79, 0\n\t" \
    "v_mov_b32 v80, 0\n\t" \
    "v_mov_b32 v81, 0\n\t" \
    "v_mov_b32 v82, 0\n\t" \
    "v_mov_b32 v83, 0\n\t" \
    "v_mov_b32 v84, 0\n\t" \
    "v_mov_b32 v85, 0\n\t" \
    "v_mov_b32 v86, 0\n\t" \
    "v_mov_b32 v87, 0\n\t" \
    "s_add_u32 s69, s65, 0\n\t" \
    "s_and_b32 s69, s69, 7\n\t" \
    "s_lshl_b32 s69, s69, 18\n\t" \
    "s_add_u32 s69, s69, s62\n\t" \
    "s_add_u32 s46, s36, s69\n\t" \
    "s_addc_u32 s47, s37, 0\n\t" \
    "s_lshr_b64 s[38:39], s[38:39], 4\n\t" \
    "s_and_b32 s97, s38, 15\n\t" \
    "s_lshl_b32 s97, s97, 6\n\t" \
    "s_waitcnt vmcnt(0)\n\t" \
    "v_cmp_eq_u32 vcc, -1, v160\n\t" \
    "s_cbranch_vccz qk_Hok92_%=\n\t" \
    "qk_H91_%=:\n\t" \
    "global_load_dword v160, v20, s[52:53] sc1\n\t" \
    "s_waitcnt vmcnt(0)\n\t" \
    "v_cmp_eq_u32 vcc, -1, v160\n\t" \
    "s_cbranch_vccz qk_Hc93_%=\n\t" \
    "s_cmp_eq_u32 s68, 0\n\t" \
    "s_cbranch_scc1 qk_F96_%=\n\t" \
    "s_sub_u32 s68, s68, 1\n\t" \
    "s_sleep 2\n\t" \
    "s_branch qk_H91_%=\n\t" \
    "qk_F96_%=:\n\t" \
    "v_mov_b32 v161, 5767424\n\t" \
    "v_mov_b32 v162, 5\n\t" \
    "global_store_dword v161, v162, s[36:37] sc1\n\t" \
    "s_waitcnt vmcnt(0)\n\t" \
    "s_branch qk_Hc93_%=\n\t" \
    "qk_Hc93_%=:\n\t" \
    "s_memtime s[56:57]\n\t" \
    "s_waitcnt lgkmcnt(0)\n\t" \
    "s_sub_u32 s69, s56, s73\n\t" \
    "s_cmp_lt_u32 s69, 3000\n\t" \
    "s_cbranch_scc1 qk_St97_%=\n\t" \
    "s_waitcnt lgkmcnt(0)\n\t" \
    "s_mov_b32 s70, 116\n\t" \
    "qk_E98_%=:\n\t" \
    "s_memtime s[56:57]\n\t" \
    "s_memrealtime s[2:3]\n\t" \
    "s_nop 0\n\t" \
    "s_memrealtime s[4:5]\n\t" \
    "s_nop 0\n\t" \
    "s_memrealtime s[6:7]\n\t" \
    "s_nop 0\n\t" \
    "s_memrealtime s[8:9]\n\t" \
    "s_nop 0\n\t" \
    "s_memrealtime s[10:11]\n\t" \
    "s_nop 0\n\t" \
    "s_memrealtime s[12:13]\n\t" \
    "s_nop 0\n\t" \
    "s_memrealtime s[14:15]\n\t" \
    "s_nop 0\n\t" \
    "s_memrealtime s[16:17]\n\t" \
    "s_nop 0\n\t" \
    "s_memrealtime s[18:19]\n\t" \
    "s_nop 0\n\t" \
    "s_memrealtime s[20:21]\n\t" \
    "s_nop 0\n\t" \
    "s_memrealtime s[22:23]\n\t" \
    "s_nop 0\n\t" \
    "s_memrealtime s[24:25]\n\t" \
    "s_nop 0\n\t" \
    "s_memrealtime s[26:27]\n\t" \
    "s_nop 0\n\t" \
    "s_memrealtime s[28:29]\n\t" \
    "s_memtime s[54:55]\n\t" \
    "s_add_u32 s70, s70, 8\n\t" \
    "s_waitcnt lgkmcnt(0)\n\t" \
    "s_sub_u32 s69, s54, s56\n\t" \
    "s_cmp_gt_u32 s69, s70\n\t" \
    "s_cbranch_scc1 qk_E98_%=\n\t" \
    "s_add_u32 s2, s2, s4\n\t" \
    "s_add_u32 s6, s6, s8\n\t" \
    "s_add_u32 s10, s10, s12\n\t" \
    "s_add_u32 s14, s14, s16\n\t" \
    "s_add_u32 s18, s18, s20\n\t" \
    "s_add_u32 s22, s22, s24\n\t" \
    "s_add_u32 s2, s2, s6\n\t" \
    "s_add_u32 s10, s10, s14\n\t" \
    "s_add_u32 s18, s18, s22\n\t" \
    "s_add_u32 s2, s2, s26\n\t" \
    "s_add_u32 s10, s10, s18\n\t" \
    "s_add_u32 s2, s2, s10\n\t" \
    "s_mul_i32 s70, s28, 13\n\t" \
    "s_sub_u32 s70, s70, s2\n\t" \
    "s_min_u32 s70, s70, 52\n\t" \
    "s_mul_i32 s69, s69, s70\n\t" \
    "s_mul_hi_u32 s69, s69, 76695844\n\t" \
    "s_mov_b32 s75, s73\n\t" \
    "s_mov_b32 s76, s74\n\t" \
    "s_add_u32 s73, s56, s69\n\t" \
    "s_lshl_b32 s74, s28, 4\n\t" \
    "qk_St97_%=:\n\t" \
    "qk_Hok92_%=:\n\t" \
    "v_max_u32_dpp v160, v160, v160 row_ror:1 row_mask:0xf bank_mask:0xf\n\t" \
    "s_nop 1\n\t" \
    "v_max_u32_dpp v160, v160, v160 row_ror:2 row_mask:0xf bank_mask:0xf\n\t" \
    "s_nop 1\n\t" \
    "v_max_u32_dpp v160, v160, v160 row_ror:4 row_mask:0xf bank_mask:0xf\n\t" \
    "s_nop 1\n\t" \
    "v_max_u32_dpp v160, v160, v160 row_ror:8 row_mask:0xf bank_mask:0xf\n\t" \
    "s_nop 0\n\t" \
    "v_readlane_b32 s69, v160, 0\n\t" \
    "v_readlane_b32 s71, v160, 16\n\t" \
    "s_nop 1\n\t" \
    "s_max_u32 s81, s69, s71\n\t" \
    "s_mul_hi_u32 s69, s85, s78\n\t" \
    "s_add_u32 s79, s81, s69\n\t" \
    "s_add_u32 s79, s79, s87\n\t" \
    "s_cmp_lt_u32 s65, 6\n\t" \
    "s_cselect_b32 s69, 48, 0\n\t" \
    "s_add_u32 s79, s79, s69\n\t" \
    "s_add_u32 s80, s79, s88\n\t" \
    "s_sub_u32 s70, s79, s74\n\t" \
    "s_max_i32 s70, s70, 0\n\t" \
    "s_min_i32 s70, s70, 16000\n\t" \
    "s_lshl_b32 s70, s70, 1\n\t" \
    "s_mul_hi_u32 s70, s70, s77\n\t" \
    "s_add_u32 s70, s70, s73\n\t" \
    "v_mov_b32 v168, s70\n\t" \
    "ds_write_b32 v11, v168 offset:20480\n\t" \
    "s_and_b32 s69, s65, 7\n\t" \
    "s_lshl_b32 s69, s69, 18\n\t" \
    "s_add_u32 s69, s69, s63\n\t" \
    "s_add_u32 s52, s36, s69\n\t" \
    "s_addc_u32 s53, s37, 0\n\t" \
    "s_waitcnt lgkmcnt(0)\n\t" \
    "s_barrier\n\t" \
    "ds_write_b32 v12, v31 offset:0\n\t" \
    "ds_write_b32 v12, v31 offset:64\n\t" \
    "s_branch qk_go78_%=\n\t" \
    "qk_Gbad83_%=:\n\t" \
    "s_waitcnt vmcnt(0) lgkmcnt(0)\n\t" \
    "s_nop 7\n\t" \
    "v_mov_b32 v72, 0\n\t" \
    "v_mov_b32 v73, 0\n\t" \
    "v_mov_b32 v74, 0\n\t" \
    "v_mov_b32 v75, 0\n\t" \
    "v_mov_b32 v76, 0\n\t" \
    "v_mov_b32 v77, 0\n\t" \
    "v_mov_b32 v78, 0\n\t" \
    "v_mov_b32 v79, 0\n\t" \
    "v_mov_b32 v80, 0\n\t" \
    "v_mov_b32 v81, 0\n\t" \
    "v_mov_b32 v82, 0\n\t" \
    "v_mov_b32 v83, 0\n\t" \
    "v_mov_b32 v84, 0\n\t" \
    "v_mov_b32 v85, 0\n\t" \
    "v_mov_b32 v86, 0\n\t" \
    "v_mov_b32 v87, 0\n\t" \
    "s_cmp_eq_u32 s68, 0\n\t" \
    "s_cbranch_scc1 qk_F99_%=\n\t" \
    "s_sub_u32 s68, s68, 1\n\t" \
    "s_branch qk_G82_%=\n\t" \
    "qk_F99_%=:\n\t" \
    "v_mov_b32 v160, 5767424\n\t" \
    "v_mov_b32 v161, 1\n\t" \
    "global_store_dword v160, v161, s[36:37] sc1\n\t" \
    "s_waitcnt vmcnt(0)\n\t" \
    "s_branch qk_Gc84_%=\n\t" \
    "qk_Gslow85_%=:\n\t" \
    "v_max_u32 v160, v36, v37\n\t" \
    "v_max3_u32 v160, v160, v38, v39\n\t" \
    "v_max3_u32 v160, v160, v40, v41\n\t" \
    "v_max3_u32 v160, v160, v42, v43\n\t" \
    "v_max3_u32 v160, v160, v44, v45\n\t" \
    "v_max3_u32 v160, v160, v46, v47\n\t" \
    "v_max3_u32 v160, v160, v48, v49\n\t" \
    "v_max3_u32 v160, v160, v50, v51\n\t" \
    "v_max3_u32 v160, v160, v52, v53\n\t" \
    "v_max3_u32 v160, v160, v54, v55\n\t" \
    "v_max3_u32 v160, v160, v56, v57\n\t" \
    "v_max3_u32 v160, v160, v58, v59\n\t" \
    "v_max3_u32 v160, v160, v60, v61\n\t" \
    "v_max3_u32 v160, v160, v62, v63\n\t" \
    "v_max3_u32 v160, v160, v64, v65\n\t" \
    "v_max3_u32 v160, v160, v66, v67\n\t" \
    "v_max3_u32 v160, v160, v68, v69\n\t" \
    "v_max3_u32 v160, v160, v70, v71\n\t" \
    "v_cmp_eq_u32 vcc, -1, v160\n\t" \
    "s_cbranch_vccz qk_Gback86_%=\n\t" \
    "s_waitcnt lgkmcnt(0)\n\t" \
    "v_sub_u32 v160, v1, v3\n\t" \
    "s_nop 0\n\t" \
    "v_readfirstlane_b32 s69, v160\n\t" \
    "v_add_u32 v163, 20480, v0\n\t" \
    "s_nop 7\n\t" \
    "s_add_u32 s30, s46, s69\n\t" \
    "s_addc_u32 s31, s47, 0\n\t" \
    "qk_Gag100_%=:\n\t" \
    "global_load_dwordx4 v[36:39], v1, s[46:47] sc1\n\t" \
    "global_load_dwordx4 v[40:43], v23, s[46:47] sc1\n\t" \
    "global_load_dwordx4 v[44:47], v163, s[30:31] sc1\n\t" \
    "global_load_dwordx4 v[48:51], v4, s[30:31] sc1\n\t" \
    "global_load_dwordx4 v[52:55], v5, s[30:31] sc1\n\t" \
    "global_load_dwordx4 v[56:59], v6, s[30:31] sc1\n\t" \
    "global_load_dwordx4 v[60:63], v7, s[30:31] sc1\n\t" \
    "global_load_dwordx4 v[64:67], v8, s[30:31] sc1\n\t" \
    "global_load_dwordx4 v[68:71], v9, s[30:31] sc1\n\t" \
    "s_waitcnt vmcnt(0)\n\t" \
    "v_max_u32 v160, v36, v37\n\t" \
    "v_max3_u32 v160, v160, v38, v39\n\t" \
    "v_max3_u32 v160, v160, v40, v41\n\t" \
    "v_max3_u32 v160, v160, v42, v43\n\t" \
    "v_max3_u32 v160, v160, v44, v45\n\t" \
    "v_max3_u32 v160, v160, v46, v47\n\t" \
    "v_max3_u32 v160, v160, v48, v49\n\t" \
    "v_max3_u32 v160, v160, v50, v51\n\t" \
    "v_max3_u32 v160, v160, v52, v53\n\t" \
    "v_max3_u32 v160, v160, v54, v55\n\t" \
    "v_max3_u32 v160, v160, v56, v57\n\t" \
    "v_max3_u32 v160, v160, v58, v59\n\t" \
    "v_max3_u32 v160, v160, v60, v61\n\t" \
    "v_max3_u32 v160, v160, v62, v63\n\t" \
    "v_max3_u32 v160, v160, v64, v65\n\t" \
    "v_max3_u32 v160, v160, v66, v67\n\t" \
    "v_max3_u32 v160, v160, v68, v69\n\t" \
    "v_max3_u32 v160, v160, v70, v71\n\t" \
    "v_cmp_eq_u32 vcc, -1, v160\n\t" \
    "s_cbranch_vccz qk_Gredo101_%=\n\t" \
    "s_cmp_eq_u32 s68, 0\n\t" \
    "s_cbranch_scc1 qk_F102_%=\n\t" \
    "s_sub_u32 s68, s68, 1\n\t" \
    "s_sleep 1\n\t" \
    "s_branch qk_Gag100_%=\n\t" \
    "qk_F102_%=:\n\t" \
    "v_mov_b32 v160, 5767424\n\t" \
    "v_mov_b32 v161, 2\n\t" \
    "global_store_dword v160, v161, s[36:37] sc1\n\t" \
    "s_waitcnt vmcnt(0)\n\t" \
    "s_branch qk_Gredo101_%=\n\t" \
    "qk_Gredo101_%=:\n\t" \
    "v_mov_b32 v72, 0\n\t" \
    "v_mov_b32 v73, 0\n\t" \
    "v_mov_b32 v74, 0\n\t" \
    "v_mov_b32 v75, 0\n\t" \
    "v_mov_b32 v76, 0\n\t" \
    "v_mov_b32 v77, 0\n\t" \
    "v_mov_b32 v78, 0\n\t" \
    "v_mov_b32 v79, 0\n\t" \
    "v_mov_b32 v80, 0\n\t" \
    "v_mov_b32 v81, 0\n\t" \
    "v_mov_b32 v82, 0\n\t" \
    "v_mov_b32 v83, 0\n\t" \
    "v_mov_b32 v84, 0\n\t" \
    "v_mov_b32 v85, 0\n\t" \
    "v_mov_b32 v86, 0\n\t" \
    "v_mov_b32 v87, 0\n\t" \
    "v_smfmac_f32_16x16x64_bf16 v[72:75], v[36:39], a[48:55], v10\n\t" \
    "v_smfmac_f32_16x16x64_bf16 v[72:75], v[40:43], a[56:63], v10\n\t" \
    "v_smfmac_f32_16x16x64_bf16 v[72:75], v[48:51], a[0:7], v10\n\t" \
    "v_smfmac_f32_16x16x64_bf16 v[72:75], v[52:55], a[8:15], v10\n\t" \
    "v_smfmac_f32_16x16x64_bf16 v[72:75], v[56:59], a[16:23], v10\n\t" \
    "v_smfmac_f32_16x16x64_bf16 v[72:75], v[60:63], a[24:31], v10\n\t" \
    "v_smfmac_f32_16x16x64_bf16 v[72:75], v[64:67], a[32:39], v10\n\t" \
    "v_smfmac_f32_16x16x64_bf16 v[72:75], v[68:71], a[40:47], v10\n\t" \
    "v_smfmac_f32_16x16x64_bf16 v[72:75], v[44:47], a[64:71], v10\n\t" \
    "v_smfmac_f32_16x16x64_bf16 v[76:79], v[36:39], a[120:127], v10\n\t" \
    "v_smfmac_f32_16x16x64_bf16 v[76:79], v[40:43], a[128:135], v10\n\t" \
    "v_smfmac_f32_16x16x64_bf16 v[76:79], v[48:51], a[72:79], v10\n\t" \
    "v_smfmac_f32_16x16x64_bf16 v[76:79], v[52:55], a[80:87], v10\n\t" \
    "v_smfmac_f32_16x16x64_bf16 v[76:79], v[56:59], a[88:95], v10\n\t" \
    "v_smfmac_f32_16x16x64_bf16 v[76:79], v[60:63], a[96:103], v10\n\t" \
    "v_smfmac_f32_16x16x64_bf16 v[76:79], v[64:67], a[104:111], v10\n\t" \
    "v_smfmac_f32_16x16x64_bf16 v[76:79], v[68:71], a[112:119], v10\n\t" \
    "v_smfmac_f32_16x16x64_bf16 v[76:79], v[44:47], a[136:143], v10\n\t" \
    "v_smfmac_f32_16x16x64_bf16 v[80:83], v[36:39], a[192:199], v10\n\t" \
    "v_smfmac_f32_16x16x64_bf16 v[80:83], v[40:43], a[200:207], v10\n\t" \
    "v_smfmac_f32_16x16x64_bf16 v[80:83], v[48:51], a[144:151], v10\n\t" \
    "v_smfmac_f32_16x16x64_bf16 v[80:83], v[52:55], a[152:159], v10\n\t" \
    "v_smfmac_f32_16x16x64_bf16 v[80:83], v[56:59], a[160:167], v10\n\t" \
    "v_smfmac_f32_16x16x64_bf16 v[80:83], v[60:63], a[168:175], v10\n\t" \
    "v_smfmac_f32_16x16x64_bf16 v[80:83], v[64:67], a[176:183], v10\n\t" \
    "v_smfmac_f32_16x16x64_bf16 v[80:83], v[68:71], a[184:191], v10\n\t" \
    "v_smfmac_f32_16x16x64_bf16 v[80:83], v[44:47], a[208:215], v10\n\t" \
    "v_smfmac_f32_16x16x64_bf16 v[84:87], v[36:39], v[232:239], v10\n\t" \
    "v_smfmac_f32_16x16x64_bf16 v[84:87], v[40:43], v[240:247], v10\n\t" \
    "v_smfmac_f32_16x16x64_bf16 v[84:87], v[48:51], a[216:223], v10\n\t" \
    "v_smfmac_f32_16x16x64_bf16 v[84:87], v[52:55], a[224:231], v10\n\t" \
    "v_smfmac_f32_16x16x64_bf16 v[84:87], v[56:59], a[232:239], v10\n\t" \
    "v_smfmac_f32_16x16x64_bf16 v[84:87], v[60:63], a[240:247], v10\n\t" \
    "v_smfmac_f32_16x16x64_bf16 v[84:87], v[64:67], a[248:255], v10\n\t" \
    "v_smfmac_f32_16x16x64_bf16 v[84:87], v[68:71], v[224:231], v10\n\t" \
    "v_smfmac_f32_16x16x64_bf16 v[84:87], v[44:47], v[248:255], v10\n\t" \
    "v_add_f32 v72, v72, v74\n\t" \
    "v_add_f32 v73, v73, v75\n\t" \
    "v_cvt_pk_bf16_f32 v156, v72, v73\n\t" \
    "ds_write_b32 v12, v156 offset:0\n\t" \
    "v_add_f32 v76, v76, v78\n\t" \
    "v_add_f32 v77, v77, v79\n\t" \
    "v_cvt_pk_bf16_f32 v157, v76, v77\n\t" \
    "ds_write_b32 v12, v157 offset:64\n\t" \
    "v_add_f32 v80, v80, v82\n\t" \
    "v_add_f32 v81, v81, v83\n\t" \
    "v_cvt_pk_bf16_f32 v158, v80, v81\n\t" \
    "ds_write_b32 v16, v158 offset:0\n\t" \
    "v_pk_add_f32 v[84:85], v[84:85], v[86:87]\n\t" \
    "v_cvt_pk_bf16_f32 v159, v84, v85\n\t" \
    "ds_write_b32 v16, v159 offset:256\n\t" \
    "s_branch qk_Gback86_%=\n\t" \
    "qk_last77_%=:\n\t" \
    "s_endpgm\n\t" \
    "qk_last_%=:\n\t" \
    ""
#define ASM_V \
    "s_load_dwordx2 s[42:43], %3, 56\n\t" \
    "s_load_dwordx2 s[44:45], %3, 64\n\t" \
    "s_load_dwordx2 s[36:37], %3, 0x58\n\t" \
    "s_load_dwordx2 s[50:51], %3, 0x48\n\t" \
    "s_load_dwordx8 s[84:91], %3, 0\n\t" \
    "s_mov_b32 s66, %4\n\t" \
    "s_mov_b32 s67, %5\n\t" \
    "s_mov_b32 s68, 262144\n\t" \
    "s_mov_b32 s72, 0\n\t" \
    "s_mov_b32 s83, -1\n\t" \
    "s_mov_b32 s65, 0\n\t" \
    "s_lshl_b32 s82, s66, 10\n\t" \
    "s_waitcnt lgkmcnt(0)\n\t" \
    "s_add_u32 s46, s42, 2097152\n\t" \
    "s_addc_u32 s47, s43, 0\n\t" \
    "s_add_u32 m0, s82, 65536\n\t" \
    "s_nop 0\n\t" \
    "global_load_lds_dwordx4 v200, s[46:47]\n\t" \
    "s_add_u32 s46, s42, 2228224\n\t" \
    "s_addc_u32 s47, s43, 0\n\t" \
    "s_add_u32 m0, s82, 69632\n\t" \
    "s_nop 0\n\t" \
    "global_load_lds_dwordx4 v200, s[46:47]\n\t" \
    "s_add_u32 s46, s42, 2359296\n\t" \
    "s_addc_u32 s47, s43, 0\n\t" \
    "s_add_u32 m0, s82, 73728\n\t" \
    "s_nop 0\n\t" \
    "global_load_lds_dwordx4 v200, s[46:47]\n\t" \
    "s_add_u32 s46, s42, 2490368\n\t" \
    "s_addc_u32 s47, s43, 0\n\t" \
    "s_add_u32 m0, s82, 77824\n\t" \
    "s_nop 0\n\t" \
    "global_load_lds_dwordx4 v200, s[46:47]\n\t" \
    "s_add_u32 s46, s42, 2621440\n\t" \
    "s_addc_u32 s47, s43, 0\n\t" \
    "s_add_u32 m0, s82, 81920\n\t" \
    "s_nop 0\n\t" \
    "global_load_lds_dwordx4 v200, s[46:47]\n\t" \
    "s_add_u32 s46, s42, 2752512\n\t" \
    "s_addc_u32 s47, s43, 0\n\t" \
    "s_add_u32 m0, s82, 86016\n\t" \
    "s_nop 0\n\t" \
    "global_load_lds_dwordx4 v200, s[46:47]\n\t" \
    "s_add_u32 s46, s42, 2883584\n\t" \
    "s_addc_u32 s47, s43, 0\n\t" \
    "s_add_u32 m0, s82, 90112\n\t" \
    "s_nop 0\n\t" \
    "global_load_lds_dwordx4 v200, s[46:47]\n\t" \
    "s_add_u32 s46, s42, 3014656\n\t" \
    "s_addc_u32 s47, s43, 0\n\t" \
    "s_add_u32 m0, s82, 94208\n\t" \
    "s_nop 0\n\t" \
    "global_load_lds_dwordx4 v200, s[46:47]\n\t" \
    "s_add_u32 s46, s42, 3145728\n\t" \
    "s_addc_u32 s47, s43, 0\n\t" \
    "s_add_u32 m0, s82, 98304\n\t" \
    "s_nop 0\n\t" \
    "global_load_lds_dwordx4 v200, s[46:47]\n\t" \
    "s_add_u32 s46, s42, 3276800\n\t" \
    "s_addc_u32 s47, s43, 0\n\t" \
    "s_add_u32 m0, s82, 102400\n\t" \
    "s_nop 0\n\t" \
    "global_load_lds_dwordx4 v200, s[46:47]\n\t" \
    "s_add_u32 s46, s42, 3407872\n\t" \
    "s_addc_u32 s47, s43, 0\n\t" \
    "s_add_u32 m0, s82, 106496\n\t" \
    "s_nop 0\n\t" \
    "global_load_lds_dwordx4 v200, s[46:47]\n\t" \
    "s_add_u32 s46, s42, 3538944\n\t" \
    "s_addc_u32 s47, s43, 0\n\t" \
    "s_add_u32 m0, s82, 110592\n\t" \
    "s_nop 0\n\t" \
    "global_load_lds_dwordx4 v200, s[46:47]\n\t" \
    "s_add_u32 s46, s42, 3670016\n\t" \
    "s_addc_u32 s47, s43, 0\n\t" \
    "s_add_u32 m0, s82, 114688\n\t" \
    "s_nop 0\n\t" \
    "global_load_lds_dwordx4 v200, s[46:47]\n\t" \
    "s_add_u32 s46, s42, 3801088\n\t" \
    "s_addc_u32 s47, s43, 0\n\t" \
    "s_add_u32 m0, s82, 118784\n\t" \
    "s_nop 0\n\t" \
    "global_load_lds_dwordx4 v200, s[46:47]\n\t" \
    "s_add_u32 s46, s42, 3932160\n\t" \
    "s_addc_u32 s47, s43, 0\n\t" \
    "s_add_u32 m0, s82, 122880\n\t" \
    "s_nop 0\n\t" \
    "global_load_lds_dwordx4 v200, s[46:47]\n\t" \
    "s_add_u32 s46, s42, 4063232\n\t" \
    "s_addc_u32 s47, s43, 0\n\t" \
    "s_add_u32 m0, s82, 126976\n\t" \
    "s_nop 0\n\t" \
    "global_load_lds_dwordx4 v200, s[46:47]\n\t" \
    "s_add_u32 s46, s42, 4194304\n\t" \
    "s_addc_u32 s47, s43, 0\n\t" \
    "s_add_u32 m0, s82, 131072\n\t" \
    "s_nop 0\n\t" \
    "global_load_lds_dwordx4 v200, s[46:47]\n\t" \
    "s_add_u32 s46, s42, 4325376\n\t" \
    "s_addc_u32 s47, s43, 0\n\t" \
    "s_add_u32 m0, s82, 135168\n\t" \
    "s_nop 0\n\t" \
    "global_load_lds_dwordx4 v200, s[46:47]\n\t" \
    "s_add_u32 s46, s42, 4456448\n\t" \
    "s_addc_u32 s47, s43, 0\n\t" \
    "s_add_u32 m0, s82, 139264\n\t" \
    "s_nop 0\n\t" \
    "global_load_lds_dwordx4 v200, s[46:47]\n\t" \
    "s_add_u32 s46, s42, 4587520\n\t" \
    "s_addc_u32 s47, s43, 0\n\t" \
    "s_add_u32 m0, s82, 143360\n\t" \
    "s_nop 0\n\t" \
    "global_load_lds_dwordx4 v200, s[46:47]\n\t" \
    "s_mov_b32 s77, 3019898880\n\t" \
    "s_mov_b32 s78, 3054198966\n\t" \
    "s_waitcnt lgkmcnt(0)\n\t" \
    "s_mov_b32 s70, 116\n\t" \
    "vv_E1_%=:\n\t" \
    "s_memtime s[56:57]\n\t" \
    "s_memrealtime s[2:3]\n\t" \
    "s_nop 0\n\t" \
    "s_memrealtime s[4:5]\n\t" \
    "s_nop 0\n\t" \
    "s_memrealtime s[6:7]\n\t" \
    "s_nop 0\n\t" \
    "s_memrealtime s[8:9]\n\t" \
    "s_nop 0\n\t" \
    "s_memrealtime s[10:11]\n\t" \
    "s_nop 0\n\t" \
    "s_memrealtime s[12:13]\n\t" \
    "s_nop 0\n\t" \
    "s_memrealtime s[14:15]\n\t" \
    "s_nop 0\n\t" \
    "s_memrealtime s[16:17]\n\t" \
    "s_nop 0\n\t" \
    "s_memrealtime s[18:19]\n\t" \
    "s_nop 0\n\t" \
    "s_memrealtime s[20:21]\n\t" \
    "s_nop 0\n\t" \
    "s_memrealtime s[22:23]\n\t" \
    "s_nop 0\n\t" \
    "s_memrealtime s[24:25]\n\t" \
    "s_nop 0\n\t" \
    "s_memrealtime s[26:27]\n\t" \
    "s_nop 0\n\t" \
    "s_memrealtime s[28:29]\n\t" \
    "s_memtime s[54:55]\n\t" \
    "s_add_u32 s70, s70, 8\n\t" \
    "s_waitcnt lgkmcnt(0)\n\t" \
    "s_sub_u32 s69, s54, s56\n\t" \
    "s_cmp_gt_u32 s69, s70\n\t" \
    "s_cbranch_scc1 vv_E1_%=\n\t" \
    "s_add_u32 s2, s2, s4\n\t" \
    "s_add_u32 s6, s6, s8\n\t" \
    "s_add_u32 s10, s10, s12\n\t" \
    "s_add_u32 s14, s14, s16\n\t" \
    "s_add_u32 s18, s18, s20\n\t" \
    "s_add_u32 s22, s22, s24\n\t" \
    "s_add_u32 s2, s2, s6\n\t" \
    "s_add_u32 s10, s10, s14\n\t" \
    "s_add_u32 s18, s18, s22\n\t" \
    "s_add_u32 s2, s2, s26\n\t" \
    "s_add_u32 s10, s10, s18\n\t" \
    "s_add_u32 s2, s2, s10\n\t" \
    "s_mul_i32 s70, s28, 13\n\t" \
    "s_sub_u32 s70, s70, s2\n\t" \
    "s_min_u32 s70, s70, 52\n\t" \
    "s_mul_i32 s69, s69, s70\n\t" \
    "s_mul_hi_u32 s69, s69, 76695844\n\t" \
    "s_mov_b32 s75, s73\n\t" \
    "s_mov_b32 s76, s74\n\t" \
    "s_add_u32 s73, s56, s69\n\t" \
    "s_lshl_b32 s74, s28, 4\n\t" \
    "s_waitcnt vmcnt(32) lgkmcnt(0)\n\t" \
    "s_barrier\n\t" \
    "ds_read_b64_tr_b16 a[0:1], v201 offset:0\n\t" \
    "ds_read_b64_tr_b16 a[2:3], v201 offset:1024\n\t" \
    "ds_read_b64_tr_b16 a[4:5], v201 offset:8192\n\t" \
    "ds_read_b64_tr_b16 a[6:7], v201 offset:9216\n\t" \
    "ds_read_b64_tr_b16 a[72:73], v202 offset:0\n\t" \
    "ds_read_b64_tr_b16 a[74:75], v202 offset:1024\n\t" \
    "ds_read_b64_tr_b16 a[76:77], v202 offset:8192\n\t" \
    "ds_read_b64_tr_b16 a[78:79], v202 offset:9216\n\t" \
    "s_waitcnt vmcnt(28) lgkmcnt(0)\n\t" \
    "s_barrier\n\t" \
    "ds_read_b64_tr_b16 a[8:9], v201 offset:16384\n\t" \
    "ds_read_b64_tr_b16 a[10:11], v201 offset:17408\n\t" \
    "ds_read_b64_tr_b16 a[12:13], v201 offset:24576\n\t" \
    "ds_read_b64_tr_b16 a[14:15], v201 offset:25600\n\t" \
    "ds_read_b64_tr_b16 a[80:81], v202 offset:16384\n\t" \
    "ds_read_b64_tr_b16 a[82:83], v202 offset:17408\n\t" \
    "ds_read_b64_tr_b16 a[84:85], v202 offset:24576\n\t" \
    "ds_read_b64_tr_b16 a[86:87], v202 offset:25600\n\t" \
    "s_waitcnt vmcnt(24) lgkmcnt(0)\n\t" \
    "s_barrier\n\t" \
    "ds_read_b64_tr_b16 a[16:17], v201 offset:32768\n\t" \
    "ds_read_b64_tr_b16 a[18:19], v201 offset:33792\n\t" \
    "ds_read_b64_tr_b16 a[20:21], v201 offset:40960\n\t" \
    "ds_read_b64_tr_b16 a[22:23], v201 offset:41984\n\t" \
    "ds_read_b64_tr_b16 a[88:89], v202 offset:32768\n\t" \
    "ds_read_b64_tr_b16 a[90:91], v202 offset:33792\n\t" \
    "ds_read_b64_tr_b16 a[92:93], v202 offset:40960\n\t" \
    "ds_read_b64_tr_b16 a[94:95], v202 offset:41984\n\t" \
    "s_waitcnt vmcnt(20) lgkmcnt(0)\n\t" \
    "s_barrier\n\t" \
    "ds_read_b64_tr_b16 a[24:25], v201 offset:49152\n\t" \
    "ds_read_b64_tr_b16 a[26:27], v201 offset:50176\n\t" \
    "ds_read_b64_tr_b16 a[28:29], v201 offset:57344\n\t" \
    "ds_read_b64_tr_b16 a[30:31], v201 offset:58368\n\t" \
    "ds_read_b64_tr_b16 a[96:97], v202 offset:49152\n\t" \
    "ds_read_b64_tr_b16 a[98:99], v202 offset:50176\n\t" \
    "ds_read_b64_tr_b16 a[100:101], v202 offset:57344\n\t" \
    "ds_read_b64_tr_b16 a[102:103], v202 offset:58368\n\t" \
    "s_add_u32 s46, s44, 0\n\t" \
    "s_addc_u32 s47, s45, 0\n\t" \
    "s_mul_i32 s69, s82, 9\n\t" \
    "s_add_u32 m0, s69, 0\n\t" \
    "s_nop 0\n\t" \
    "global_load_lds_dwordx4 v203, s[46:47]\n\t" \
    "s_add_u32 m0, s69, 1024\n\t" \
    "s_nop 0\n\t" \
    "global_load_lds_dwordx4 v204, s[46:47]\n\t" \
    "s_add_u32 m0, s69, 2048\n\t" \
    "s_nop 0\n\t" \
    "global_load_lds_dwordx4 v205, s[46:47]\n\t" \
    "s_add_u32 m0, s69, 3072\n\t" \
    "s_nop 0\n\t" \
    "global_load_lds_dwordx4 v206, s[46:47]\n\t" \
    "s_add_u32 m0, s69, 4096\n\t" \
    "s_nop 0\n\t" \
    "global_load_lds_dwordx4 v207, s[46:47]\n\t" \
    "s_add_u32 m0, s69, 5120\n\t" \
    "s_nop 0\n\t" \
    "global_load_lds_dwordx4 v208, s[46:47]\n\t" \
    "s_add_u32 m0, s69, 6144\n\t" \
    "s_nop 0\n\t" \
    "global_load_lds_dwordx4 v209, s[46:47]\n\t" \
    "s_add_u32 m0, s69, 7168\n\t" \
    "s_nop 0\n\t" \
    "global_load_lds_dwordx4 v210, s[46:47]\n\t" \
    "s_add_u32 m0, s69, 8192\n\t" \
    "s_nop 0\n\t" \
    "global_load_lds_dwordx4 v211, s[46:47]\n\t" \
    "s_waitcnt vmcnt(25) lgkmcnt(0)\n\t" \
    "s_barrier\n\t" \
    "v_add_u32 v201, 0x10000, v201\n\t" \
    "v_add_u32 v202, 0x10000, v202\n\t" \
    "ds_read_b64_tr_b16 a[32:33], v201 offset:0\n\t" \
    "ds_read_b64_tr_b16 a[34:35], v201 offset:1024\n\t" \
    "ds_read_b64_tr_b16 a[36:37], v201 offset:8192\n\t" \
    "ds_read_b64_tr_b16 a[38:39], v201 offset:9216\n\t" \
    "ds_read_b64_tr_b16 a[104:105], v202 offset:0\n\t" \
    "ds_read_b64_tr_b16 a[106:107], v202 offset:1024\n\t" \
    "ds_read_b64_tr_b16 a[108:109], v202 offset:8192\n\t" \
    "ds_read_b64_tr_b16 a[110:111], v202 offset:9216\n\t" \
    "s_waitcnt vmcnt(21) lgkmcnt(0)\n\t" \
    "s_barrier\n\t" \
    "ds_read_b64_tr_b16 a[40:41], v201 offset:16384\n\t" \
    "ds_read_b64_tr_b16 a[42:43], v201 offset:17408\n\t" \
    "ds_read_b64_tr_b16 a[44:45], v201 offset:24576\n\t" \
    "ds_read_b64_tr_b16 a[46:47], v201 offset:25600\n\t" \
    "ds_read_b64_tr_b16 a[112:113], v202 offset:16384\n\t" \
    "ds_read_b64_tr_b16 a[114:115], v202 offset:17408\n\t" \
    "ds_read_b64_tr_b16 a[116:117], v202 offset:24576\n\t" \
    "ds_read_b64_tr_b16 a[118:119], v202 offset:25600\n\t" \
    "s_add_u32 s46, s44, 36864\n\t" \
    "s_addc_u32 s47, s45, 0\n\t" \
    "s_mul_i32 s69, s82, 9\n\t" \
    "s_add_u32 m0, s69, 36864\n\t" \
    "s_nop 0\n\t" \
    "global_load_lds_dwordx4 v203, s[46:47]\n\t" \
    "s_add_u32 m0, s69, 37888\n\t" \
    "s_nop 0\n\t" \
    "global_load_lds_dwordx4 v204, s[46:47]\n\t" \
    "s_add_u32 m0, s69, 38912\n\t" \
    "s_nop 0\n\t" \
    "global_load_lds_dwordx4 v205, s[46:47]\n\t" \
    "s_add_u32 m0, s69, 39936\n\t" \
    "s_nop 0\n\t" \
    "global_load_lds_dwordx4 v206, s[46:47]\n\t" \
    "s_add_u32 m0, s69, 40960\n\t" \
    "s_nop 0\n\t" \
    "global_load_lds_dwordx4 v207, s[46:47]\n\t" \
    "s_add_u32 m0, s69, 41984\n\t" \
    "s_nop 0\n\t" \
    "global_load_lds_dwordx4 v208, s[46:47]\n\t" \
    "s_add_u32 m0, s69, 43008\n\t" \
    "s_nop 0\n\t" \
    "global_load_lds_dwordx4 v209, s[46:47]\n\t" \
    "s_add_u32 m0, s69, 44032\n\t" \
    "s_nop 0\n\t" \
    "global_load_lds_dwordx4 v210, s[46:47]\n\t" \
    "s_add_u32 m0, s69, 45056\n\t" \
    "s_nop 0\n\t" \
    "global_load_lds_dwordx4 v211, s[46:47]\n\t" \
    "s_waitcnt vmcnt(26) lgkmcnt(0)\n\t" \
    "s_barrier\n\t" \
    "ds_read_b64_tr_b16 a[48:49], v201 offset:32768\n\t" \
    "ds_read_b64_tr_b16 a[50:51], v201 offset:33792\n\t" \
    "ds_read_b64_tr_b16 a[52:53], v201 offset:40960\n\t" \
    "ds_read_b64_tr_b16 a[54:55], v201 offset:41984\n\t" \
    "ds_read_b64_tr_b16 a[120:121], v202 offset:32768\n\t" \
    "ds_read_b64_tr_b16 a[122:123], v202 offset:33792\n\t" \
    "ds_read_b64_tr_b16 a[124:125], v202 offset:40960\n\t" \
    "ds_read_b64_tr_b16 a[126:127], v202 offset:41984\n\t" \
    "s_waitcnt vmcnt(22) lgkmcnt(0)\n\t" \
    "s_barrier\n\t" \
    "ds_read_b64_tr_b16 a[56:57], v201 offset:49152\n\t" \
    "ds_read_b64_tr_b16 a[58:59], v201 offset:50176\n\t" \
    "ds_read_b64_tr_b16 a[60:61], v201 offset:57344\n\t" \
    "ds_read_b64_tr_b16 a[62:63], v201 offset:58368\n\t" \
    "ds_read_b64_tr_b16 a[128:129], v202 offset:49152\n\t" \
    "ds_read_b64_tr_b16 a[130:131], v202 offset:50176\n\t" \
    "ds_read_b64_tr_b16 a[132:133], v202 offset:57344\n\t" \
    "ds_read_b64_tr_b16 a[134:135], v202 offset:58368\n\t" \
    "s_add_u32 s46, s44, 73728\n\t" \
    "s_addc_u32 s47, s45, 0\n\t" \
    "s_mul_i32 s69, s82, 9\n\t" \
    "s_add_u32 m0, s69, 73728\n\t" \
    "s_nop 0\n\t" \
    "global_load_lds_dwordx4 v203, s[46:47]\n\t" \
    "s_add_u32 m0, s69, 74752\n\t" \
    "s_nop 0\n\t" \
    "global_load_lds_dwordx4 v204, s[46:47]\n\t" \
    "s_add_u32 m0, s69, 75776\n\t" \
    "s_nop 0\n\t" \
    "global_load_lds_dwordx4 v205, s[46:47]\n\t" \
    "s_add_u32 m0, s69, 76800\n\t" \
    "s_nop 0\n\t" \
    "global_load_lds_dwordx4 v206, s[46:47]\n\t" \
    "s_add_u32 m0, s69, 77824\n\t" \
    "s_nop 0\n\t" \
    "global_load_lds_dwordx4 v207, s[46:47]\n\t" \
    "s_add_u32 m0, s69, 78848\n\t" \
    "s_nop 0\n\t" \
    "global_load_lds_dwordx4 v208, s[46:47]\n\t" \
    "s_add_u32 m0, s69, 79872\n\t" \
    "s_nop 0\n\t" \
    "global_load_lds_dwordx4 v209, s[46:47]\n\t" \
    "s_add_u32 m0, s69, 80896\n\t" \
    "s_nop 0\n\t" \
    "global_load_lds_dwordx4 v210, s[46:47]\n\t" \
    "s_add_u32 m0, s69, 81920\n\t" \
    "s_nop 0\n\t" \
    "global_load_lds_dwordx4 v211, s[46:47]\n\t" \
    "v_mbcnt_lo_u32_b32 v140, -1, 0\n\t" \
    "v_mbcnt_hi_u32_b32 v140, -1, v140\n\t" \
    "v_lshlrev_b32 v140, 6, v140\n\t" \
    "v_add_u32 v140, 6225920, v140\n\t" \
    "v_mov_b32 v141, 6234112\n\t" \
    "global_load_dword v141, v141, s[36:37] sc1\n\t" \
    "global_load_dwordx2 v[142:143], v140, s[36:37] sc1\n\t" \
    "global_load_dword v144, v140, s[36:37] offset:2048 sc1\n\t" \
    "s_waitcnt vmcnt(30) lgkmcnt(0)\n\t" \
    "s_barrier\n\t" \
    "v_add_u32 v201, 0x10000, v201\n\t" \
    "v_add_u32 v202, 0x10000, v202\n\t" \
    "ds_read_b64_tr_b16 a[64:65], v201 offset:0\n\t" \
    "ds_read_b64_tr_b16 a[66:67], v201 offset:1024\n\t" \
    "ds_read_b64_tr_b16 a[68:69], v201 offset:8192\n\t" \
    "ds_read_b64_tr_b16 a[70:71], v201 offset:9216\n\t" \
    "ds_read_b64_tr_b16 a[136:137], v202 offset:0\n\t" \
    "ds_read_b64_tr_b16 a[138:139], v202 offset:1024\n\t" \
    "ds_read_b64_tr_b16 a[140:141], v202 offset:8192\n\t" \
    "ds_read_b64_tr_b16 a[142:143], v202 offset:9216\n\t" \
    "v_add_u32 v201, 0xfffe0000, v201\n\t" \
    "v_add_u32 v202, 0xfffe0000, v202\n\t" \
    "s_waitcnt lgkmcnt(0)\n\t" \
    "s_barrier\n\t" \
    "s_add_u32 s46, s44, 110592\n\t" \
    "s_addc_u32 s47, s45, 0\n\t" \
    "s_mul_i32 s69, s82, 9\n\t" \
    "s_add_u32 m0, s69, 110592\n\t" \
    "s_nop 0\n\t" \
    "global_load_lds_dwordx4 v203, s[46:47]\n\t" \
    "s_add_u32 m0, s69, 111616\n\t" \
    "s_nop 0\n\t" \
    "global_load_lds_dwordx4 v204, s[46:47]\n\t" \
    "s_add_u32 m0, s69, 112640\n\t" \
    "s_nop 0\n\t" \
    "global_load_lds_dwordx4 v205, s[46:47]\n\t" \
    "s_add_u32 m0, s69, 113664\n\t" \
    "s_nop 0\n\t" \
    "global_load_lds_dwordx4 v206, s[46:47]\n\t" \
    "s_add_u32 m0, s69, 114688\n\t" \
    "s_nop 0\n\t" \
    "global_load_lds_dwordx4 v207, s[46:47]\n\t" \
    "s_add_u32 m0, s69, 115712\n\t" \
    "s_nop 0\n\t" \
    "global_load_lds_dwordx4 v208, s[46:47]\n\t" \
    "s_add_u32 m0, s69, 116736\n\t" \
    "s_nop 0\n\t" \
    "global_load_lds_dwordx4 v209, s[46:47]\n\t" \
    "s_add_u32 m0, s69, 117760\n\t" \
    "s_nop 0\n\t" \
    "global_load_lds_dwordx4 v210, s[46:47]\n\t" \
    "s_add_u32 m0, s69, 118784\n\t" \
    "s_nop 0\n\t" \
    "global_load_lds_dwordx4 v211, s[46:47]\n\t" \
    "s_waitcnt vmcnt(21) lgkmcnt(0)\n\t" \
    "s_barrier\n\t" \
    "ds_read_b64_tr_b16 a[144:145], v212 offset:0\n\t" \
    "ds_read_b64_tr_b16 a[146:147], v212 offset:2304\n\t" \
    "ds_read_b64_tr_b16 a[148:149], v212 offset:18432\n\t" \
    "ds_read_b64_tr_b16 a[150:151], v212 offset:20736\n\t" \
    "s_waitcnt lgkmcnt(6)\n\t" \
    "ds_read_b64_tr_b16 a[176:177], v213 offset:0\n\t" \
    "ds_read_b64_tr_b16 a[178:179], v213 offset:2304\n\t" \
    "ds_read_b64_tr_b16 a[180:181], v213 offset:18432\n\t" \
    "ds_read_b64_tr_b16 a[182:183], v213 offset:20736\n\t" \
    "s_waitcnt lgkmcnt(6)\n\t" \
    "ds_read_b64_tr_b16 a[208:209], v212 offset:128\n\t" \
    "ds_read_b64_tr_b16 a[210:211], v212 offset:2432\n\t" \
    "ds_read_b64_tr_b16 a[212:213], v212 offset:18560\n\t" \
    "ds_read_b64_tr_b16 a[214:215], v212 offset:20864\n\t" \
    "s_waitcnt lgkmcnt(6)\n\t" \
    "ds_read_b64_tr_b16 a[240:241], v213 offset:128\n\t" \
    "ds_read_b64_tr_b16 a[242:243], v213 offset:2432\n\t" \
    "ds_read_b64_tr_b16 a[244:245], v213 offset:18560\n\t" \
    "ds_read_b64_tr_b16 a[246:247], v213 offset:20864\n\t" \
    "s_waitcnt lgkmcnt(6)\n\t" \
    "ds_read_b64_tr_b16 a[152:153], v214 offset:0\n\t" \
    "ds_read_b64_tr_b16 a[154:155], v214 offset:2304\n\t" \
    "ds_read_b64_tr_b16 a[156:157], v214 offset:18432\n\t" \
    "ds_read_b64_tr_b16 a[158:159], v214 offset:20736\n\t" \
    "s_waitcnt lgkmcnt(6)\n\t" \
    "ds_read_b64_tr_b16 a[184:185], v215 offset:0\n\t" \
    "ds_read_b64_tr_b16 a[186:187], v215 offset:2304\n\t" \
    "ds_read_b64_tr_b16 a[188:189], v215 offset:18432\n\t" \
    "ds_read_b64_tr_b16 a[190:191], v215 offset:20736\n\t" \
    "s_waitcnt lgkmcnt(6)\n\t" \
    "ds_read_b64_tr_b16 a[216:217], v214 offset:128\n\t" \
    "ds_read_b64_tr_b16 a[218:219], v214 offset:2432\n\t" \
    "ds_read_b64_tr_b16 a[220:221], v214 offset:18560\n\t" \
    "ds_read_b64_tr_b16 a[222:223], v214 offset:20864\n\t" \
    "s_waitcnt lgkmcnt(6)\n\t" \
    "ds_read_b64_tr_b16 a[248:249], v215 offset:128\n\t" \
    "ds_read_b64_tr_b16 a[250:251], v215 offset:2432\n\t" \
    "ds_read_b64_tr_b16 a[252:253], v215 offset:18560\n\t" \
    "ds_read_b64_tr_b16 a[254:255], v215 offset:20864\n\t" \
    "s_waitcnt lgkmcnt(6)\n\t" \
    "ds_read_b64_tr_b16 v[240:241], v24 offset:0\n\t" \
    "ds_read_b64_tr_b16 v[242:243], v24 offset:2304\n\t" \
    "ds_read_b64_tr_b16 v[244:245], v24 offset:18432\n\t" \
    "ds_read_b64_tr_b16 v[246:247], v24 offset:20736\n\t" \
    "v_add_u32 v212, 0x12000, v212\n\t" \
    "v_add_u32 v213, 0x12000, v213\n\t" \
    "v_add_u32 v214, 0x12000, v214\n\t" \
    "v_add_u32 v215, 0x12000, v215\n\t" \
    "v_add_u32 v24, 0x12000, v24\n\t" \
    "s_waitcnt vmcnt(9)\n\t" \
    "vv_C2_%=:\n\t" \
    "v_cmp_ne_u32 s[54:55], v142, v144\n\t" \
    "v_sub_u32 v142, v142, v141\n\t" \
    "v_cmp_ne_u32 vcc, 1, v142\n\t" \
    "s_nop 1\n\t" \
    "s_or_b64 vcc, vcc, s[54:55]\n\t" \
    "s_cbranch_scc0 vv_Cok3_%=\n\t" \
    "s_cmp_eq_u32 s68, 0\n\t" \
    "s_cbranch_scc1 vv_F5_%=\n\t" \
    "s_sub_u32 s68, s68, 1\n\t" \
    "s_sleep 6\n\t" \
    "s_branch vv_Cag4_%=\n\t" \
    "vv_F5_%=:\n\t" \
    "v_mov_b32 v144, 5767424\n\t" \
    "v_mov_b32 v145, 4\n\t" \
    "global_store_dword v144, v145, s[36:37] sc1\n\t" \
    "s_waitcnt vmcnt(0)\n\t" \
    "s_branch vv_Cok3_%=\n\t" \
    "vv_Cag4_%=:\n\t" \
    "global_load_dwordx2 v[142:143], v140, s[36:37] sc1\n\t" \
    "global_load_dword v144, v140, s[36:37] offset:2048 sc1\n\t" \
    "s_waitcnt vmcnt(0)\n\t" \
    "s_branch vv_C2_%=\n\t" \
    "vv_Cok3_%=:\n\t" \
    "v_readlane_b32 s69, v143, 2\n\t" \
    "v_readlane_b32 s70, v143, 3\n\t" \
    "s_nop 1\n\t" \
    "s_cmp_lt_u32 s70, s69\n\t" \
    "s_cselect_b32 s69, 0, 8192\n\t" \
    "s_add_u32 s100, s36, s69\n\t" \
    "s_addc_u32 s101, s37, 0\n\t" \
    "s_cmp_lg_u32 s66, 0\n\t" \
    "s_cbranch_scc1 vv_Ca6_%=\n\t" \
    "v_mov_b32 v140, 5768192\n\t" \
    "s_mov_b64 exec, 1\n\t" \
    "global_atomic_add v140, v142, s[36:37] sc1\n\t" \
    "s_mov_b64 exec, -1\n\t" \
    "vv_Ca6_%=:\n\t" \
    "v_mov_b32_dpp v140, v143 quad_perm:[1,0,3,2] row_mask:0xf bank_mask:0xf\n\t" \
    "v_cmp_lt_u32 s[44:45], v140, v143\n\t" \
    "s_nop 1\n\t" \
    "s_bitcmp1_b32 s44, 0\n\t" \
    "s_cselect_b32 s62, 8192, 0\n\t" \
    "s_bitcmp1_b32 s44, 2\n\t" \
    "s_cselect_b32 s63, 8192, 0\n\t" \
    "s_or_b32 s63, s63, 0x200000\n\t" \
    "v_min_u32 v140, v140, v143\n\t" \
    "v_cmp_lt_u32 vcc, 660, v140\n\t" \
    "s_nop 1\n\t" \
    "s_bcnt1_i32_b64 s69, vcc\n\t" \
    "s_cmp_gt_u32 s69, 16\n\t" \
    "s_cselect_b32 s69, 1, 0\n\t" \
    "s_mul_i32 s70, s69, 160\n\t" \
    "s_add_u32 s86, s86, s70\n\t" \
    "s_mul_i32 s70, s69, 536\n\t" \
    "s_add_u32 s87, s87, s70\n\t" \
    "s_mul_i32 s70, s69, 256\n\t" \
    "s_add_u32 s88, s88, s70\n\t" \
    "s_waitcnt vmcnt(0) lgkmcnt(0)\n\t" \
    "s_barrier\n\t" \
    "ds_read_b64_tr_b16 a[160:161], v212 offset:0\n\t" \
    "ds_read_b64_tr_b16 a[162:163], v212 offset:2304\n\t" \
    "ds_read_b64_tr_b16 a[164:165], v212 offset:18432\n\t" \
    "ds_read_b64_tr_b16 a[166:167], v212 offset:20736\n\t" \
    "s_waitcnt lgkmcnt(6)\n\t" \
    "ds_read_b64_tr_b16 a[192:193], v213 offset:0\n\t" \
    "ds_read_b64_tr_b16 a[194:195], v213 offset:2304\n\t" \
    "ds_read_b64_tr_b16 a[196:197], v213 offset:18432\n\t" \
    "ds_read_b64_tr_b16 a[198:199], v213 offset:20736\n\t" \
    "s_waitcnt lgkmcnt(6)\n\t" \
    "ds_read_b64_tr_b16 a[224:225], v212 offset:128\n\t" \
    "ds_read_b64_tr_b16 a[226:227], v212 offset:2432\n\t" \
    "ds_read_b64_tr_b16 a[228:229], v212 offset:18560\n\t" \
    "ds_read_b64_tr_b16 a[230:231], v212 offset:20864\n\t" \
    "s_waitcnt lgkmcnt(6)\n\t" \
    "ds_read_b64_tr_b16 v[224:225], v213 offset:128\n\t" \
    "ds_read_b64_tr_b16 v[226:227], v213 offset:2432\n\t" \
    "ds_read_b64_tr_b16 v[228:229], v213 offset:18560\n\t" \
    "ds_read_b64_tr_b16 v[230:231], v213 offset:20864\n\t" \
    "s_waitcnt lgkmcnt(6)\n\t" \
    "ds_read_b64_tr_b16 a[168:169], v214 offset:0\n\t" \
    "ds_read_b64_tr_b16 a[170:171], v214 offset:2304\n\t" \
    "ds_read_b64_tr_b16 a[172:173], v214 offset:18432\n\t" \
    "ds_read_b64_tr_b16 a[174:175], v214 offset:20736\n\t" \
    "s_waitcnt lgkmcnt(6)\n\t" \
    "ds_read_b64_tr_b16 a[200:201], v215 offset:0\n\t" \
    "ds_read_b64_tr_b16 a[202:203], v215 offset:2304\n\t" \
    "ds_read_b64_tr_b16 a[204:205], v215 offset:18432\n\t" \
    "ds_read_b64_tr_b16 a[206:207], v215 offset:20736\n\t" \
    "s_waitcnt lgkmcnt(6)\n\t" \
    "ds_read_b64_tr_b16 a[232:233], v214 offset:128\n\t" \
    "ds_read_b64_tr_b16 a[234:235], v214 offset:2432\n\t" \
    "ds_read_b64_tr_b16 a[236:237], v214 offset:18560\n\t" \
    "ds_read_b64_tr_b16 a[238:239], v214 offset:20864\n\t" \
    "s_waitcnt lgkmcnt(6)\n\t" \
    "ds_read_b64_tr_b16 v[232:233], v215 offset:128\n\t" \
    "ds_read_b64_tr_b16 v[234:235], v215 offset:2432\n\t" \
    "ds_read_b64_tr_b16 v[236:237], v215 offset:18560\n\t" \
    "ds_read_b64_tr_b16 v[238:239], v215 offset:20864\n\t" \
    "s_waitcnt lgkmcnt(6)\n\t" \
    "ds_read_b64_tr_b16 v[248:249], v24 offset:0\n\t" \
    "ds_read_b64_tr_b16 v[250:251], v24 offset:2304\n\t" \
    "ds_read_b64_tr_b16 v[252:253], v24 offset:18432\n\t" \
    "ds_read_b64_tr_b16 v[254:255], v24 offset:20736\n\t" \
    "v_add_u32 v212, 0x12000, v212\n\t" \
    "v_add_u32 v213, 0x12000, v213\n\t" \
    "v_add_u32 v214, 0x12000, v214\n\t" \
    "v_add_u32 v215, 0x12000, v215\n\t" \
    "v_add_u32 v24, 0x12000, v24\n\t" \
    "s_waitcnt lgkmcnt(0)\n\t" \
    "s_barrier\n\t" \
    "s_mov_b32 s79, s74\n\t" \
    "s_sub_u32 s80, s74, 32000\n\t" \
    "v_mov_b32 v72, 0\n\t" \
    "v_mov_b32 v73, 0\n\t" \
    "v_mov_b32 v74, 0\n\t" \
    "v_mov_b32 v75, 0\n\t" \
    "v_mov_b32 v76, 0\n\t" \
    "v_mov_b32 v77, 0\n\t" \
    "v_mov_b32 v78, 0\n\t" \
    "v_mov_b32 v79, 0\n\t" \
    "v_mov_b32 v108, 0\n\t" \
    "v_mov_b32 v109, 0\n\t" \
    "v_mov_b32 v110, 0\n\t" \
    "v_mov_b32 v111, 0\n\t" \
    "v_mov_b32 v112, 0\n\t" \
    "v_mov_b32 v113, 0\n\t" \
    "v_mov_b32 v114, 0\n\t" \
    "v_mov_b32 v115, 0\n\t" \
    "v_mov_b32 v116, 0\n\t" \
    "v_mov_b32 v117, 0\n\t" \
    "v_mov_b32 v118, 0\n\t" \
    "v_mov_b32 v119, 0\n\t" \
    "v_mov_b32 v120, 0\n\t" \
    "v_mov_b32 v121, 0\n\t" \
    "v_mov_b32 v122, 0\n\t" \
    "v_mov_b32 v123, 0\n\t" \
    "v_mov_b32 v124, 0\n\t" \
    "v_mov_b32 v125, 0\n\t" \
    "v_mov_b32 v126, 0\n\t" \
    "v_mov_b32 v127, 0\n\t" \
    "s_add_u32 s69, s65, 0\n\t" \
    "s_and_b32 s69, s69, 7\n\t" \
    "s_lshl_b32 s69, s69, 18\n\t" \
    "s_add_u32 s69, s69, s62\n\t" \
    "s_add_u32 s46, s36, s69\n\t" \
    "s_addc_u32 s47, s37, 0\n\t" \
    "s_add_u32 s70, s65, 0\n\t" \
    "s_mul_i32 s69, s70, 171\n\t" \
    "s_lshr_b32 s69, s69, 9\n\t" \
    "s_mul_i32 s70, s70, 589824\n\t" \
    "s_mul_i32 s71, s69, 327680\n\t" \
    "s_add_u32 s70, s70, s71\n\t" \
    "s_lshl1_add_u32 s69, s69, 4\n\t" \
    "s_bitcmp1_b64 s[44:45], s69\n\t" \
    "s_cselect_b32 s69, 8192, 0\n\t" \
    "s_add_u32 s69, s69, s70\n\t" \
    "s_add_u32 s69, s69, 123731968\n\t" \
    "s_add_u32 s38, s36, s69\n\t" \
    "s_addc_u32 s39, s37, 0\n\t" \
    "s_and_b32 s69, s69, 0xffe00000\n\t" \
    "s_add_u32 s69, s69, 1823744\n\t" \
    "v_mov_b32 v17, s69\n\t" \
    "global_load_dword v17, v17, s[36:37]\n\t" \
    "s_and_b32 s69, s65, 7\n\t" \
    "s_lshl_b32 s69, s69, 18\n\t" \
    "s_add_u32 s69, s69, s63\n\t" \
    "s_add_u32 s52, s36, s69\n\t" \
    "s_addc_u32 s53, s37, 0\n\t" \
    "v_add_u32 v34, 16384, v1\n\t" \
    "v_subrev_u32 v35, 16384, v2\n\t" \
    "v_mov_b32 v100, 0\n\t" \
    "v_mov_b32 v101, 0\n\t" \
    "v_mov_b32 v102, 0\n\t" \
    "v_mov_b32 v103, 0\n\t" \
    "s_sub_u32 s70, s79, s74\n\t" \
    "s_max_i32 s70, s70, 0\n\t" \
    "s_min_i32 s70, s70, 16000\n\t" \
    "s_lshl_b32 s70, s70, 1\n\t" \
    "s_mul_hi_u32 s70, s70, s77\n\t" \
    "s_add_u32 s70, s70, s73\n\t" \
    "s_cmp_eq_u32 s66, 1\n\t" \
    "s_cbranch_scc1 vv_loop10_%=\n\t" \
    "s_cmp_eq_u32 s66, 2\n\t" \
    "s_cbranch_scc1 vv_loop11_%=\n\t" \
    "s_cmp_eq_u32 s66, 3\n\t" \
    "s_cbranch_scc1 vv_loop12_%=\n\t" \
    ".p2align 6\n\t" \
    "s_nop 0\n\t" \
    "s_nop 0\n\t" \
    "s_nop 0\n\t" \
    "s_nop 0\n\t" \
    "s_nop 0\n\t" \
    "s_nop 0\n\t" \
    "vv_loop9_%=:\n\t" \
    "s_getpc_b64 s[30:31]\n\t" \
    "vv_Wp15_%=:\n\t" \
    "s_add_u32 s30, s30, vv_We16_%=-vv_Wp15_%=\n\t" \
    "s_addc_u32 s31, s31, 0\n\t" \
    "s_sub_u32 s70, s70, -54\n\t" \
    "vv_W14_%=:\n\t" \
    "s_memtime s[56:57]\n\t" \
    "s_waitcnt lgkmcnt(0)\n\t" \
    "s_sub_u32 s69, s70, s56\n\t" \
    "s_cmp_gt_i32 s69, 80\n\t" \
    "s_cbranch_scc1 vv_W14_%=\n\t" \
    "s_max_i32 s69, s69, 0\n\t" \
    "s_and_b32 s69, s69, -4\n\t" \
    "s_sub_u32 s30, s30, s69\n\t" \
    "s_subb_u32 s31, s31, 0\n\t" \
    "s_setpc_b64 s[30:31]\n\t" \
    "s_nop 0\n\t" \
    "s_nop 0\n\t" \
    "s_nop 0\n\t" \
    "s_nop 0\n\t" \
    "s_nop 0\n\t" \
    "s_nop 0\n\t" \
    "s_nop 0\n\t" \
    "s_nop 0\n\t" \
    "s_nop 0\n\t" \
    "s_nop 0\n\t" \
    "s_nop 0\n\t" \
    "s_nop 0\n\t" \
    "s_nop 0\n\t" \
    "s_nop 0\n\t" \
    "s_nop 0\n\t" \
    "s_nop 0\n\t" \
    "s_nop 0\n\t" \
    "s_nop 0\n\t" \
    "s_nop 0\n\t" \
    "s_nop 0\n\t" \
    "vv_We16_%=:\n\t" \
    "vv_G17_%=:\n\t" \
    "global_load_dwordx4 v[36:39], v1, s[46:47] sc1\n\t" \
    "global_load_dword v141, v2, s[46:47] sc1\n\t" \
    "global_load_dwordx4 v[40:43], v23, s[46:47] sc1\n\t" \
    "s_waitcnt vmcnt(2)\n\t" \
    "vv_Garr22_%=:\n\t" \
    "ds_write_b128 v3, v[36:39]\n\t" \
    "v_smfmac_f32_16x16x64_bf16 v[72:75], v[36:39], a[0:7], v10\n\t" \
    "s_waitcnt vmcnt(1)\n\t" \
    "ds_write_b32 v22, v141 offset:19456\n\t" \
    "s_waitcnt vmcnt(0)\n\t" \
    "ds_write_b128 v3, v[40:43] offset:1024\n\t" \
    "v_max3_u32 v140, v36, v40, v141\n\t" \
    "v_cmp_eq_u32 vcc, -1, v140\n\t" \
    "s_cbranch_vccnz vv_Gbad18_%=\n\t" \
    "vv_Gc19_%=:\n\t" \
    "s_waitcnt lgkmcnt(0)\n\t" \
    "s_barrier\n\t" \
    "ds_read_b128 v[48:51], v4\n\t" \
    "ds_read_b128 v[52:55], v5\n\t" \
    "v_smfmac_f32_16x16x64_bf16 v[76:79], v[36:39], a[72:79], v10\n\t" \
    "ds_read_b128 v[56:59], v6\n\t" \
    "ds_read_b128 v[60:63], v7\n\t" \
    "v_smfmac_f32_16x16x64_bf16 v[72:75], v[40:43], a[8:15], v10\n\t" \
    "ds_read_b128 v[64:67], v8\n\t" \
    "v_smfmac_f32_16x16x64_bf16 v[76:79], v[40:43], a[80:87], v10\n\t" \
    "ds_read_b128 v[68:71], v9\n\t" \
    "s_waitcnt lgkmcnt(5)\n\t" \
    "v_smfmac_f32_16x16x64_bf16 v[72:75], v[48:51], a[16:23], v10\n\t" \
    "ds_read_b128 v[44:47], v0 offset:19456\n\t" \
    "s_waitcnt lgkmcnt(5)\n\t" \
    "v_smfmac_f32_16x16x64_bf16 v[72:75], v[52:55], a[24:31], v10\n\t" \
    "s_waitcnt lgkmcnt(4)\n\t" \
    "v_smfmac_f32_16x16x64_bf16 v[72:75], v[56:59], a[32:39], v10\n\t" \
    "s_waitcnt lgkmcnt(3)\n\t" \
    "v_smfmac_f32_16x16x64_bf16 v[72:75], v[60:63], a[40:47], v10\n\t" \
    "s_waitcnt lgkmcnt(2)\n\t" \
    "v_smfmac_f32_16x16x64_bf16 v[72:75], v[64:67], a[48:55], v10\n\t" \
    "s_waitcnt lgkmcnt(1)\n\t" \
    "v_smfmac_f32_16x16x64_bf16 v[72:75], v[68:71], a[56:63], v10\n\t" \
    "s_waitcnt lgkmcnt(0)\n\t" \
    "v_smfmac_f32_16x16x64_bf16 v[72:75], v[44:47], a[64:71], v10\n\t" \
    "s_nop 2\n\t" \
    "v_smfmac_f32_16x16x64_bf16 v[76:79], v[48:51], a[88:95], v10\n\t" \
    "s_nop 2\n\t" \
    "v_smfmac_f32_16x16x64_bf16 v[76:79], v[52:55], a[96:103], v10\n\t" \
    "v_add_f32 v72, v72, v74\n\t" \
    "v_smfmac_f32_16x16x64_bf16 v[76:79], v[56:59], a[104:111], v10\n\t" \
    "v_add_f32 v73, v73, v75\n\t" \
    "v_smfmac_f32_16x16x64_bf16 v[76:79], v[60:63], a[112:119], v10\n\t" \
    "v_cvt_pk_bf16_f32 v80, v72, v73\n\t" \
    "v_smfmac_f32_16x16x64_bf16 v[76:79], v[64:67], a[120:127], v10\n\t" \
    "ds_write_b32 v12, v80 offset:0\n\t" \
    "v_smfmac_f32_16x16x64_bf16 v[76:79], v[68:71], a[128:135], v10\n\t" \
    "v_smfmac_f32_16x16x64_bf16 v[76:79], v[44:47], a[136:143], v10\n\t" \
    "s_nop 6\n\t" \
    "v_cmp_u_f32 vcc, v72, v73\n\t" \
    "v_pk_add_f32 v[76:77], v[76:77], v[78:79]\n\t" \
    "v_cvt_pk_bf16_f32 v81, v76, v77\n\t" \
    "ds_write_b32 v12, v81 offset:256\n\t" \
    "s_cbranch_vccnz vv_Gslow20_%=\n\t" \
    "vv_Gback21_%=:\n\t" \
    "s_waitcnt lgkmcnt(0)\n\t" \
    "s_barrier\n\t" \
    "ds_read_b64_tr_b16 v[84:85], v13 offset:0\n\t" \
    "v_smfmac_f32_16x16x64_bf16 v[100:103], v[36:39], a[0:7], v10\n\t" \
    "ds_read_b64_tr_b16 v[86:87], v13 offset:64\n\t" \
    "ds_read_b64_tr_b16 v[92:93], v13 offset:1152\n\t" \
    "v_smfmac_f32_16x16x64_bf16 v[100:103], v[36:39], a[0:7], v10\n\t" \
    "ds_read_b64_tr_b16 v[94:95], v13 offset:1216\n\t" \
    "ds_read_b64_tr_b16 v[88:89], v25 offset:0\n\t" \
    "v_smfmac_f32_16x16x64_bf16 v[100:103], v[36:39], a[0:7], v10\n\t" \
    "ds_read_b64_tr_b16 v[90:91], v25 offset:64\n\t" \
    "s_waitcnt lgkmcnt(4)\n\t" \
    "v_smfmac_f32_16x16x64_bf16 v[124:127], v[84:87], v[240:247], v19\n\t" \
    "ds_read_b64_tr_b16 v[96:97], v25 offset:1152\n\t" \
    "ds_read_b64_tr_b16 v[98:99], v25 offset:1216\n\t" \
    "s_waitcnt lgkmcnt(4)\n\t" \
    "v_smfmac_f32_16x16x64_bf16 v[124:127], v[92:95], v[248:255], v19\n\t" \
    "s_nop 2\n\t" \
    "v_smfmac_f32_16x16x64_bf16 v[108:111], v[84:87], a[144:151], v19\n\t" \
    "s_nop 2\n\t" \
    "v_smfmac_f32_16x16x64_bf16 v[108:111], v[92:95], a[160:167], v19\n\t" \
    "s_nop 1\n\t" \
    "v_permlane32_swap_b32 v124, v126\n\t" \
    "v_smfmac_f32_16x16x64_bf16 v[112:115], v[84:87], a[176:183], v19\n\t" \
    "s_nop 1\n\t" \
    "v_permlane32_swap_b32 v125, v127\n\t" \
    "v_smfmac_f32_16x16x64_bf16 v[112:115], v[92:95], a[192:199], v19\n\t" \
    "s_nop 0\n\t" \
    "v_add_f32 v124, v124, v126\n\t" \
    "s_waitcnt lgkmcnt(2)\n\t" \
    "v_smfmac_f32_16x16x64_bf16 v[108:111], v[88:91], a[152:159], v19\n\t" \
    "s_nop 1\n\t" \
    "v_add_f32 v125, v125, v127\n\t" \
    "v_smfmac_f32_16x16x64_bf16 v[112:115], v[88:91], a[184:191], v19\n\t" \
    "s_nop 0\n\t" \
    "v_cvt_pk_bf16_f32 v136, v124, v125\n\t" \
    "s_waitcnt lgkmcnt(0)\n\t" \
    "v_smfmac_f32_16x16x64_bf16 v[108:111], v[96:99], a[168:175], v19\n\t" \
    "s_nop 1\n\t" \
    "v_cvt_pk_bf16_f32 v137, v124, v125\n\t" \
    "v_smfmac_f32_16x16x64_bf16 v[112:115], v[96:99], a[200:207], v19\n\t" \
    "s_nop 1\n\t" \
    "v_permlane32_swap_b32 v136, v137\n\t" \
    "v_smfmac_f32_16x16x64_bf16 v[116:119], v[84:87], a[208:215], v19\n\t" \
    "s_mov_b32 exec_hi, 0\n\t" \
    "global_store_dwordx2 v16, v[136:137], s[38:39] sc1\n\t" \
    "s_mov_b32 exec_hi, -1\n\t" \
    "v_cvt_pk_bf16_f32 v128, v108, v109\n\t" \
    "v_smfmac_f32_16x16x64_bf16 v[116:119], v[88:91], a[216:223], v19\n\t" \
    "s_nop 1\n\t" \
    "v_cvt_pk_bf16_f32 v129, v110, v111\n\t" \
    "v_smfmac_f32_16x16x64_bf16 v[116:119], v[92:95], a[224:231], v19\n\t" \
    "s_nop 1\n\t" \
    "v_cvt_pk_bf16_f32 v130, v112, v113\n\t" \
    "v_smfmac_f32_16x16x64_bf16 v[116:119], v[96:99], a[232:239], v19\n\t" \
    "s_nop 1\n\t" \
    "v_cvt_pk_bf16_f32 v131, v114, v115\n\t" \
    "v_smfmac_f32_16x16x64_bf16 v[120:123], v[84:87], a[240:247], v19\n\t" \
    "s_nop 1\n\t" \
    "v_permlane16_swap_b32 v128, v130\n\t" \
    "v_smfmac_f32_16x16x64_bf16 v[120:123], v[88:91], a[248:255], v19\n\t" \
    "s_nop 1\n\t" \
    "v_permlane16_swap_b32 v129, v131\n\t" \
    "v_smfmac_f32_16x16x64_bf16 v[120:123], v[92:95], v[224:231], v19\n\t" \
    "s_nop 0\n\t" \
    "global_store_dwordx4 v14, v[128:131], s[38:39] sc1\n\t" \
    "v_cvt_pk_bf16_f32 v132, v116, v117\n\t" \
    "v_smfmac_f32_16x16x64_bf16 v[120:123], v[96:99], v[232:239], v19\n\t" \
    "s_nop 1\n\t" \
    "v_cvt_pk_bf16_f32 v133, v118, v119\n\t" \
    "s_nop 4\n\t" \
    "v_cvt_pk_bf16_f32 v134, v120, v121\n\t" \
    "v_cvt_pk_bf16_f32 v135, v122, v123\n\t" \
    "s_nop 0\n\t" \
    "v_permlane16_swap_b32 v132, v134\n\t" \
    "v_permlane16_swap_b32 v133, v135\n\t" \
    "global_store_dwordx4 v15, v[132:135], s[38:39] sc1\n\t" \
    "s_cmp_eq_u32 s65, 63\n\t" \
    "s_cbranch_scc1 vv_end13_%=\n\t" \
    "s_cmp_lg_u32 s66, 0\n\t" \
    "s_cbranch_scc1 vv_SF23_%=\n\t" \
    "s_lshl_b32 s69, s67, 7\n\t" \
    "s_add_u32 s69, s69, 3182592\n\t" \
    "s_add_u32 s70, s65, -1\n\t" \
    "v_mov_b32 v142, s69\n\t" \
    "v_mov_b32 v143, s70\n\t" \
    "s_mov_b64 exec, 1\n\t" \
    "global_store_dword v142, v143, s[100:101] sc1\n\t" \
    "s_mov_b64 exec, -1\n\t" \
    "vv_SF23_%=:\n\t" \
    "s_add_u32 s65, s65, 1\n\t" \
    "v_mov_b32 v72, 0\n\t" \
    "v_mov_b32 v73, 0\n\t" \
    "v_mov_b32 v74, 0\n\t" \
    "v_mov_b32 v75, 0\n\t" \
    "v_mov_b32 v76, 0\n\t" \
    "v_mov_b32 v77, 0\n\t" \
    "v_mov_b32 v78, 0\n\t" \
    "v_mov_b32 v79, 0\n\t" \
    "v_mov_b32 v108, 0\n\t" \
    "v_mov_b32 v109, 0\n\t" \
    "v_mov_b32 v110, 0\n\t" \
    "v_mov_b32 v111, 0\n\t" \
    "v_mov_b32 v112, 0\n\t" \
    "v_mov_b32 v113, 0\n\t" \
    "v_mov_b32 v114, 0\n\t" \
    "v_mov_b32 v115, 0\n\t" \
    "v_mov_b32 v116, 0\n\t" \
    "v_mov_b32 v117, 0\n\t" \
    "v_mov_b32 v118, 0\n\t" \
    "v_mov_b32 v119, 0\n\t" \
    "v_mov_b32 v120, 0\n\t" \
    "v_mov_b32 v121, 0\n\t" \
    "v_mov_b32 v122, 0\n\t" \
    "v_mov_b32 v123, 0\n\t" \
    "v_mov_b32 v124, 0\n\t" \
    "v_mov_b32 v125, 0\n\t" \
    "v_mov_b32 v126, 0\n\t" \
    "v_mov_b32 v127, 0\n\t" \
    "s_add_u32 s69, s65, 0\n\t" \
    "s_and_b32 s69, s69, 7\n\t" \
    "s_lshl_b32 s69, s69, 18\n\t" \
    "s_add_u32 s69, s69, s62\n\t" \
    "s_add_u32 s46, s36, s69\n\t" \
    "s_addc_u32 s47, s37, 0\n\t" \
    "s_add_u32 s70, s65, 0\n\t" \
    "s_mul_i32 s69, s70, 171\n\t" \
    "s_lshr_b32 s69, s69, 9\n\t" \
    "s_mul_i32 s70, s70, 589824\n\t" \
    "s_mul_i32 s71, s69, 327680\n\t" \
    "s_add_u32 s70, s70, s71\n\t" \
    "s_lshl1_add_u32 s69, s69, 4\n\t" \
    "s_bitcmp1_b64 s[44:45], s69\n\t" \
    "s_cselect_b32 s69, 8192, 0\n\t" \
    "s_add_u32 s69, s69, s70\n\t" \
    "s_add_u32 s69, s69, 123731968\n\t" \
    "s_add_u32 s38, s36, s69\n\t" \
    "s_addc_u32 s39, s37, 0\n\t" \
    "s_and_b32 s69, s69, 0xffe00000\n\t" \
    "s_add_u32 s69, s69, 1823744\n\t" \
    "v_mov_b32 v17, s69\n\t" \
    "global_load_dword v17, v17, s[36:37]\n\t" \
    "s_and_b32 s69, s65, 7\n\t" \
    "s_lshl_b32 s69, s69, 18\n\t" \
    "s_add_u32 s69, s69, s63\n\t" \
    "s_add_u32 s52, s36, s69\n\t" \
    "s_addc_u32 s53, s37, 0\n\t" \
    "s_barrier\n\t" \
    "ds_read_b32 v160, v11 offset:20480\n\t" \
    "s_waitcnt lgkmcnt(0)\n\t" \
    "v_readfirstlane_b32 s70, v160\n\t" \
    "s_branch vv_loop9_%=\n\t" \
    "vv_Gbad18_%=:\n\t" \
    "s_waitcnt vmcnt(0) lgkmcnt(0)\n\t" \
    "s_nop 7\n\t" \
    "v_mov_b32 v72, 0\n\t" \
    "v_mov_b32 v73, 0\n\t" \
    "v_mov_b32 v74, 0\n\t" \
    "v_mov_b32 v75, 0\n\t" \
    "v_mov_b32 v76, 0\n\t" \
    "v_mov_b32 v77, 0\n\t" \
    "v_mov_b32 v78, 0\n\t" \
    "v_mov_b32 v79, 0\n\t" \
    "s_cmp_eq_u32 s68, 0\n\t" \
    "s_cbranch_scc1 vv_F24_%=\n\t" \
    "s_sub_u32 s68, s68, 1\n\t" \
    "s_branch vv_G17_%=\n\t" \
    "vv_F24_%=:\n\t" \
    "v_mov_b32 v140, 5767424\n\t" \
    "v_mov_b32 v141, 1\n\t" \
    "global_store_dword v140, v141, s[36:37] sc1\n\t" \
    "s_waitcnt vmcnt(0)\n\t" \
    "s_branch vv_Gc19_%=\n\t" \
    "vv_Gslow20_%=:\n\t" \
    "v_max_u32 v140, v36, v37\n\t" \
    "v_max3_u32 v140, v140, v38, v39\n\t" \
    "v_max3_u32 v140, v140, v40, v41\n\t" \
    "v_max3_u32 v140, v140, v42, v43\n\t" \
    "v_max3_u32 v140, v140, v44, v45\n\t" \
    "v_max3_u32 v140, v140, v46, v47\n\t" \
    "v_max3_u32 v140, v140, v48, v49\n\t" \
    "v_max3_u32 v140, v140, v50, v51\n\t" \
    "v_max3_u32 v140, v140, v52, v53\n\t" \
    "v_max3_u32 v140, v140, v54, v55\n\t" \
    "v_max3_u32 v140, v140, v56, v57\n\t" \
    "v_max3_u32 v140, v140, v58, v59\n\t" \
    "v_max3_u32 v140, v140, v60, v61\n\t" \
    "v_max3_u32 v140, v140, v62, v63\n\t" \
    "v_max3_u32 v140, v140, v64, v65\n\t" \
    "v_max3_u32 v140, v140, v66, v67\n\t" \
    "v_max3_u32 v140, v140, v68, v69\n\t" \
    "v_max3_u32 v140, v140, v70, v71\n\t" \
    "v_cmp_eq_u32 vcc, -1, v140\n\t" \
    "s_cbranch_vccz vv_Gback21_%=\n\t" \
    "s_waitcnt lgkmcnt(0)\n\t" \
    "v_sub_u32 v140, v1, v3\n\t" \
    "s_nop 0\n\t" \
    "v_readfirstlane_b32 s69, v140\n\t" \
    "v_add_u32 v143, 20480, v0\n\t" \
    "s_nop 7\n\t" \
    "s_add_u32 s30, s46, s69\n\t" \
    "s_addc_u32 s31, s47, 0\n\t" \
    "vv_Gag25_%=:\n\t" \
    "global_load_dwordx4 v[36:39], v1, s[46:47] sc1\n\t" \
    "global_load_dwordx4 v[40:43], v23, s[46:47] sc1\n\t" \
    "global_load_dwordx4 v[44:47], v143, s[30:31] sc1\n\t" \
    "global_load_dwordx4 v[48:51], v4, s[30:31] sc1\n\t" \
    "global_load_dwordx4 v[52:55], v5, s[30:31] sc1\n\t" \
    "global_load_dwordx4 v[56:59], v6, s[30:31] sc1\n\t" \
    "global_load_dwordx4 v[60:63], v7, s[30:31] sc1\n\t" \
    "global_load_dwordx4 v[64:67], v8, s[30:31] sc1\n\t" \
    "global_load_dwordx4 v[68:71], v9, s[30:31] sc1\n\t" \
    "s_waitcnt vmcnt(0)\n\t" \
    "v_max_u32 v140, v36, v37\n\t" \
    "v_max3_u32 v140, v140, v38, v39\n\t" \
    "v_max3_u32 v140, v140, v40, v41\n\t" \
    "v_max3_u32 v140, v140, v42, v43\n\t" \
    "v_max3_u32 v140, v140, v44, v45\n\t" \
    "v_max3_u32 v140, v140, v46, v47\n\t" \
    "v_max3_u32 v140, v140, v48, v49\n\t" \
    "v_max3_u32 v140, v140, v50, v51\n\t" \
    "v_max3_u32 v140, v140, v52, v53\n\t" \
    "v_max3_u32 v140, v140, v54, v55\n\t" \
    "v_max3_u32 v140, v140, v56, v57\n\t" \
    "v_max3_u32 v140, v140, v58, v59\n\t" \
    "v_max3_u32 v140, v140, v60, v61\n\t" \
    "v_max3_u32 v140, v140, v62, v63\n\t" \
    "v_max3_u32 v140, v140, v64, v65\n\t" \
    "v_max3_u32 v140, v140, v66, v67\n\t" \
    "v_max3_u32 v140, v140, v68, v69\n\t" \
    "v_max3_u32 v140, v140, v70, v71\n\t" \
    "v_cmp_eq_u32 vcc, -1, v140\n\t" \
    "s_cbranch_vccz vv_Gredo26_%=\n\t" \
    "s_cmp_eq_u32 s68, 0\n\t" \
    "s_cbranch_scc1 vv_F27_%=\n\t" \
    "s_sub_u32 s68, s68, 1\n\t" \
    "s_sleep 1\n\t" \
    "s_branch vv_Gag25_%=\n\t" \
    "vv_F27_%=:\n\t" \
    "v_mov_b32 v140, 5767424\n\t" \
    "v_mov_b32 v141, 2\n\t" \
    "global_store_dword v140, v141, s[36:37] sc1\n\t" \
    "s_waitcnt vmcnt(0)\n\t" \
    "s_branch vv_Gredo26_%=\n\t" \
    "vv_Gredo26_%=:\n\t" \
    "v_mov_b32 v72, 0\n\t" \
    "v_mov_b32 v73, 0\n\t" \
    "v_mov_b32 v74, 0\n\t" \
    "v_mov_b32 v75, 0\n\t" \
    "v_mov_b32 v76, 0\n\t" \
    "v_mov_b32 v77, 0\n\t" \
    "v_mov_b32 v78, 0\n\t" \
    "v_mov_b32 v79, 0\n\t" \
    "v_smfmac_f32_16x16x64_bf16 v[72:75], v[36:39], a[0:7], v10\n\t" \
    "v_smfmac_f32_16x16x64_bf16 v[72:75], v[40:43], a[8:15], v10\n\t" \
    "v_smfmac_f32_16x16x64_bf16 v[72:75], v[48:51], a[16:23], v10\n\t" \
    "v_smfmac_f32_16x16x64_bf16 v[72:75], v[52:55], a[24:31], v10\n\t" \
    "v_smfmac_f32_16x16x64_bf16 v[72:75], v[56:59], a[32:39], v10\n\t" \
    "v_smfmac_f32_16x16x64_bf16 v[72:75], v[60:63], a[40:47], v10\n\t" \
    "v_smfmac_f32_16x16x64_bf16 v[72:75], v[64:67], a[48:55], v10\n\t" \
    "v_smfmac_f32_16x16x64_bf16 v[72:75], v[68:71], a[56:63], v10\n\t" \
    "v_smfmac_f32_16x16x64_bf16 v[72:75], v[44:47], a[64:71], v10\n\t" \
    "v_smfmac_f32_16x16x64_bf16 v[76:79], v[36:39], a[72:79], v10\n\t" \
    "v_smfmac_f32_16x16x64_bf16 v[76:79], v[40:43], a[80:87], v10\n\t" \
    "v_smfmac_f32_16x16x64_bf16 v[76:79], v[48:51], a[88:95], v10\n\t" \
    "v_smfmac_f32_16x16x64_bf16 v[76:79], v[52:55], a[96:103], v10\n\t" \
    "v_smfmac_f32_16x16x64_bf16 v[76:79], v[56:59], a[104:111], v10\n\t" \
    "v_smfmac_f32_16x16x64_bf16 v[76:79], v[60:63], a[112:119], v10\n\t" \
    "v_smfmac_f32_16x16x64_bf16 v[76:79], v[64:67], a[120:127], v10\n\t" \
    "v_smfmac_f32_16x16x64_bf16 v[76:79], v[68:71], a[128:135], v10\n\t" \
    "v_smfmac_f32_16x16x64_bf16 v[76:79], v[44:47], a[136:143], v10\n\t" \
    "v_add_f32 v72, v72, v74\n\t" \
    "v_add_f32 v73, v73, v75\n\t" \
    "v_cvt_pk_bf16_f32 v80, v72, v73\n\t" \
    "ds_write_b32 v12, v80 offset:0\n\t" \
    "s_nop 3\n\t" \
    "v_pk_add_f32 v[76:77], v[76:77], v[78:79]\n\t" \
    "v_cvt_pk_bf16_f32 v81, v76, v77\n\t" \
    "ds_write_b32 v12, v81 offset:256\n\t" \
    "s_branch vv_Gback21_%=\n\t" \
    "vv_end13_%=:\n\t" \
    "s_endpgm\n\t" \
    ".p2align 6\n\t" \
    "s_nop 0\n\t" \
    "s_nop 0\n\t" \
    "s_nop 0\n\t" \
    "s_nop 0\n\t" \
    "s_nop 0\n\t" \
    "s_nop 0\n\t" \
    "vv_loop10_%=:\n\t" \
    "s_getpc_b64 s[30:31]\n\t" \
    "vv_Wp30_%=:\n\t" \
    "s_add_u32 s30, s30, vv_We31_%=-vv_Wp30_%=\n\t" \
    "s_addc_u32 s31, s31, 0\n\t" \
    "s_sub_u32 s70, s70, -54\n\t" \
    "vv_W29_%=:\n\t" \
    "s_memtime s[56:57]\n\t" \
    "s_waitcnt lgkmcnt(0)\n\t" \
    "s_sub_u32 s69, s70, s56\n\t" \
    "s_cmp_gt_i32 s69, 80\n\t" \
    "s_cbranch_scc1 vv_W29_%=\n\t" \
    "s_max_i32 s69, s69, 0\n\t" \
    "s_and_b32 s69, s69, -4\n\t" \
    "s_sub_u32 s30, s30, s69\n\t" \
    "s_subb_u32 s31, s31, 0\n\t" \
    "s_setpc_b64 s[30:31]\n\t" \
    "s_nop 0\n\t" \
    "s_nop 0\n\t" \
    "s_nop 0\n\t" \
    "s_nop 0\n\t" \
    "s_nop 0\n\t" \
    "s_nop 0\n\t" \
    "s_nop 0\n\t" \
    "s_nop 0\n\t" \
    "s_nop 0\n\t" \
    "s_nop 0\n\t" \
    "s_nop 0\n\t" \
    "s_nop 0\n\t" \
    "s_nop 0\n\t" \
    "s_nop 0\n\t" \
    "s_nop 0\n\t" \
    "s_nop 0\n\t" \
    "s_nop 0\n\t" \
    "s_nop 0\n\t" \
    "s_nop 0\n\t" \
    "s_nop 0\n\t" \
    "vv_We31_%=:\n\t" \
    "vv_G32_%=:\n\t" \
    "global_load_dwordx4 v[36:39], v1, s[46:47] sc1\n\t" \
    "global_load_dword v141, v2, s[46:47] sc1\n\t" \
    "global_load_dwordx4 v[40:43], v23, s[46:47] sc1\n\t" \
    "s_waitcnt vmcnt(2)\n\t" \
    "vv_Garr37_%=:\n\t" \
    "ds_write_b128 v3, v[36:39]\n\t" \
    "v_smfmac_f32_16x16x64_bf16 v[72:75], v[36:39], a[16:23], v10\n\t" \
    "s_waitcnt vmcnt(1)\n\t" \
    "ds_write_b32 v22, v141 offset:19712\n\t" \
    "s_waitcnt vmcnt(0)\n\t" \
    "ds_write_b128 v3, v[40:43] offset:1024\n\t" \
    "v_max3_u32 v140, v36, v40, v141\n\t" \
    "v_cmp_eq_u32 vcc, -1, v140\n\t" \
    "s_cbranch_vccnz vv_Gbad33_%=\n\t" \
    "vv_Gc34_%=:\n\t" \
    "s_waitcnt lgkmcnt(0)\n\t" \
    "s_barrier\n\t" \
    "ds_read_b128 v[48:51], v4\n\t" \
    "ds_read_b128 v[52:55], v5\n\t" \
    "v_smfmac_f32_16x16x64_bf16 v[76:79], v[36:39], a[88:95], v10\n\t" \
    "ds_read_b128 v[56:59], v6\n\t" \
    "ds_read_b128 v[60:63], v7\n\t" \
    "v_smfmac_f32_16x16x64_bf16 v[72:75], v[40:43], a[24:31], v10\n\t" \
    "ds_read_b128 v[64:67], v8\n\t" \
    "v_smfmac_f32_16x16x64_bf16 v[76:79], v[40:43], a[96:103], v10\n\t" \
    "ds_read_b128 v[68:71], v9\n\t" \
    "s_waitcnt lgkmcnt(5)\n\t" \
    "v_smfmac_f32_16x16x64_bf16 v[72:75], v[48:51], a[32:39], v10\n\t" \
    "ds_read_b128 v[44:47], v0 offset:19456\n\t" \
    "s_waitcnt lgkmcnt(5)\n\t" \
    "v_smfmac_f32_16x16x64_bf16 v[72:75], v[52:55], a[40:47], v10\n\t" \
    "s_waitcnt lgkmcnt(4)\n\t" \
    "v_smfmac_f32_16x16x64_bf16 v[72:75], v[56:59], a[48:55], v10\n\t" \
    "s_waitcnt lgkmcnt(3)\n\t" \
    "v_smfmac_f32_16x16x64_bf16 v[72:75], v[60:63], a[56:63], v10\n\t" \
    "s_waitcnt lgkmcnt(2)\n\t" \
    "v_smfmac_f32_16x16x64_bf16 v[72:75], v[64:67], a[0:7], v10\n\t" \
    "s_waitcnt lgkmcnt(1)\n\t" \
    "v_smfmac_f32_16x16x64_bf16 v[72:75], v[68:71], a[8:15], v10\n\t" \
    "s_waitcnt lgkmcnt(0)\n\t" \
    "v_smfmac_f32_16x16x64_bf16 v[72:75], v[44:47], a[64:71], v10\n\t" \
    "s_nop 2\n\t" \
    "v_smfmac_f32_16x16x64_bf16 v[76:79], v[48:51], a[104:111], v10\n\t" \
    "s_nop 2\n\t" \
    "v_smfmac_f32_16x16x64_bf16 v[76:79], v[52:55], a[112:119], v10\n\t" \
    "v_add_f32 v72, v72, v74\n\t" \
    "v_smfmac_f32_16x16x64_bf16 v[76:79], v[56:59], a[120:127], v10\n\t" \
    "v_add_f32 v73, v73, v75\n\t" \
    "v_smfmac_f32_16x16x64_bf16 v[76:79], v[60:63], a[128:135], v10\n\t" \
    "v_cvt_pk_bf16_f32 v80, v72, v73\n\t" \
    "v_smfmac_f32_16x16x64_bf16 v[76:79], v[64:67], a[72:79], v10\n\t" \
    "ds_write_b32 v12, v80 offset:0\n\t" \
    "v_smfmac_f32_16x16x64_bf16 v[76:79], v[68:71], a[80:87], v10\n\t" \
    "v_smfmac_f32_16x16x64_bf16 v[76:79], v[44:47], a[136:143], v10\n\t" \
    "s_nop 6\n\t" \
    "v_cmp_u_f32 vcc, v72, v73\n\t" \
    "v_pk_add_f32 v[76:77], v[76:77], v[78:79]\n\t" \
    "v_cvt_pk_bf16_f32 v81, v76, v77\n\t" \
    "ds_write_b32 v12, v81 offset:256\n\t" \
    "s_cbranch_vccnz vv_Gslow35_%=\n\t" \
    "vv_Gback36_%=:\n\t" \
    "s_waitcnt lgkmcnt(0)\n\t" \
    "s_barrier\n\t" \
    "ds_read_b64_tr_b16 v[84:85], v13 offset:0\n\t" \
    "v_smfmac_f32_16x16x64_bf16 v[100:103], v[36:39], a[0:7], v10\n\t" \
    "ds_read_b64_tr_b16 v[86:87], v13 offset:64\n\t" \
    "ds_read_b64_tr_b16 v[92:93], v13 offset:1152\n\t" \
    "v_smfmac_f32_16x16x64_bf16 v[100:103], v[36:39], a[0:7], v10\n\t" \
    "ds_read_b64_tr_b16 v[94:95], v13 offset:1216\n\t" \
    "ds_read_b64_tr_b16 v[88:89], v25 offset:0\n\t" \
    "v_smfmac_f32_16x16x64_bf16 v[100:103], v[36:39], a[0:7], v10\n\t" \
    "ds_read_b64_tr_b16 v[90:91], v25 offset:64\n\t" \
    "s_waitcnt lgkmcnt(4)\n\t" \
    "v_smfmac_f32_16x16x64_bf16 v[124:127], v[84:87], v[240:247], v19\n\t" \
    "ds_read_b64_tr_b16 v[96:97], v25 offset:1152\n\t" \
    "ds_read_b64_tr_b16 v[98:99], v25 offset:1216\n\t" \
    "s_waitcnt lgkmcnt(4)\n\t" \
    "v_smfmac_f32_16x16x64_bf16 v[124:127], v[92:95], v[248:255], v19\n\t" \
    "s_nop 2\n\t" \
    "v_smfmac_f32_16x16x64_bf16 v[108:111], v[84:87], a[144:151], v19\n\t" \
    "s_nop 2\n\t" \
    "v_smfmac_f32_16x16x64_bf16 v[108:111], v[92:95], a[160:167], v19\n\t" \
    "s_nop 1\n\t" \
    "v_permlane32_swap_b32 v124, v126\n\t" \
    "v_smfmac_f32_16x16x64_bf16 v[112:115], v[84:87], a[176:183], v19\n\t" \
    "s_nop 1\n\t" \
    "v_permlane32_swap_b32 v125, v127\n\t" \
    "v_smfmac_f32_16x16x64_bf16 v[112:115], v[92:95], a[192:199], v19\n\t" \
    "s_nop 0\n\t" \
    "v_add_f32 v124, v124, v126\n\t" \
    "s_waitcnt lgkmcnt(2)\n\t" \
    "v_smfmac_f32_16x16x64_bf16 v[108:111], v[88:91], a[152:159], v19\n\t" \
    "s_nop 1\n\t" \
    "v_add_f32 v125, v125, v127\n\t" \
    "v_smfmac_f32_16x16x64_bf16 v[112:115], v[88:91], a[184:191], v19\n\t" \
    "s_nop 0\n\t" \
    "v_cvt_pk_bf16_f32 v136, v124, v125\n\t" \
    "s_waitcnt lgkmcnt(0)\n\t" \
    "v_smfmac_f32_16x16x64_bf16 v[108:111], v[96:99], a[168:175], v19\n\t" \
    "s_nop 1\n\t" \
    "v_cvt_pk_bf16_f32 v137, v124, v125\n\t" \
    "v_smfmac_f32_16x16x64_bf16 v[112:115], v[96:99], a[200:207], v19\n\t" \
    "s_nop 1\n\t" \
    "v_permlane32_swap_b32 v136, v137\n\t" \
    "v_smfmac_f32_16x16x64_bf16 v[116:119], v[84:87], a[208:215], v19\n\t" \
    "s_mov_b32 exec_hi, 0\n\t" \
    "global_store_dwordx2 v16, v[136:137], s[38:39] sc1\n\t" \
    "s_mov_b32 exec_hi, -1\n\t" \
    "v_cvt_pk_bf16_f32 v128, v108, v109\n\t" \
    "v_smfmac_f32_16x16x64_bf16 v[116:119], v[88:91], a[216:223], v19\n\t" \
    "s_nop 1\n\t" \
    "v_cvt_pk_bf16_f32 v129, v110, v111\n\t" \
    "v_smfmac_f32_16x16x64_bf16 v[116:119], v[92:95], a[224:231], v19\n\t" \
    "s_nop 1\n\t" \
    "v_cvt_pk_bf16_f32 v130, v112, v113\n\t" \
    "v_smfmac_f32_16x16x64_bf16 v[116:119], v[96:99], a[232:239], v19\n\t" \
    "s_nop 1\n\t" \
    "v_cvt_pk_bf16_f32 v131, v114, v115\n\t" \
    "v_smfmac_f32_16x16x64_bf16 v[120:123], v[84:87], a[240:247], v19\n\t" \
    "s_nop 1\n\t" \
    "v_permlane16_swap_b32 v128, v130\n\t" \
    "v_smfmac_f32_16x16x64_bf16 v[120:123], v[88:91], a[248:255], v19\n\t" \
    "s_nop 1\n\t" \
    "v_permlane16_swap_b32 v129, v131\n\t" \
    "v_smfmac_f32_16x16x64_bf16 v[120:123], v[92:95], v[224:231], v19\n\t" \
    "s_nop 0\n\t" \
    "global_store_dwordx4 v14, v[128:131], s[38:39] sc1\n\t" \
    "v_cvt_pk_bf16_f32 v132, v116, v117\n\t" \
    "v_smfmac_f32_16x16x64_bf16 v[120:123], v[96:99], v[232:239], v19\n\t" \
    "s_nop 1\n\t" \
    "v_cvt_pk_bf16_f32 v133, v118, v119\n\t" \
    "s_nop 4\n\t" \
    "v_cvt_pk_bf16_f32 v134, v120, v121\n\t" \
    "v_cvt_pk_bf16_f32 v135, v122, v123\n\t" \
    "s_nop 0\n\t" \
    "v_permlane16_swap_b32 v132, v134\n\t" \
    "v_permlane16_swap_b32 v133, v135\n\t" \
    "global_store_dwordx4 v15, v[132:135], s[38:39] sc1\n\t" \
    "s_cmp_eq_u32 s65, 63\n\t" \
    "s_cbranch_scc1 vv_end28_%=\n\t" \
    "s_add_u32 s65, s65, 1\n\t" \
    "v_mov_b32 v72, 0\n\t" \
    "v_mov_b32 v73, 0\n\t" \
    "v_mov_b32 v74, 0\n\t" \
    "v_mov_b32 v75, 0\n\t" \
    "v_mov_b32 v76, 0\n\t" \
    "v_mov_b32 v77, 0\n\t" \
    "v_mov_b32 v78, 0\n\t" \
    "v_mov_b32 v79, 0\n\t" \
    "v_mov_b32 v108, 0\n\t" \
    "v_mov_b32 v109, 0\n\t" \
    "v_mov_b32 v110, 0\n\t" \
    "v_mov_b32 v111, 0\n\t" \
    "v_mov_b32 v112, 0\n\t" \
    "v_mov_b32 v113, 0\n\t" \
    "v_mov_b32 v114, 0\n\t" \
    "v_mov_b32 v115, 0\n\t" \
    "v_mov_b32 v116, 0\n\t" \
    "v_mov_b32 v117, 0\n\t" \
    "v_mov_b32 v118, 0\n\t" \
    "v_mov_b32 v119, 0\n\t" \
    "v_mov_b32 v120, 0\n\t" \
    "v_mov_b32 v121, 0\n\t" \
    "v_mov_b32 v122, 0\n\t" \
    "v_mov_b32 v123, 0\n\t" \
    "v_mov_b32 v124, 0\n\t" \
    "v_mov_b32 v125, 0\n\t" \
    "v_mov_b32 v126, 0\n\t" \
    "v_mov_b32 v127, 0\n\t" \
    "s_add_u32 s69, s65, 0\n\t" \
    "s_and_b32 s69, s69, 7\n\t" \
    "s_lshl_b32 s69, s69, 18\n\t" \
    "s_add_u32 s69, s69, s62\n\t" \
    "s_add_u32 s46, s36, s69\n\t" \
    "s_addc_u32 s47, s37, 0\n\t" \
    "s_add_u32 s70, s65, 0\n\t" \
    "s_mul_i32 s69, s70, 171\n\t" \
    "s_lshr_b32 s69, s69, 9\n\t" \
    "s_mul_i32 s70, s70, 589824\n\t" \
    "s_mul_i32 s71, s69, 327680\n\t" \
    "s_add_u32 s70, s70, s71\n\t" \
    "s_lshl1_add_u32 s69, s69, 4\n\t" \
    "s_bitcmp1_b64 s[44:45], s69\n\t" \
    "s_cselect_b32 s69, 8192, 0\n\t" \
    "s_add_u32 s69, s69, s70\n\t" \
    "s_add_u32 s69, s69, 123731968\n\t" \
    "s_add_u32 s38, s36, s69\n\t" \
    "s_addc_u32 s39, s37, 0\n\t" \
    "s_and_b32 s69, s69, 0xffe00000\n\t" \
    "s_add_u32 s69, s69, 1823744\n\t" \
    "v_mov_b32 v17, s69\n\t" \
    "global_load_dword v17, v17, s[36:37]\n\t" \
    "s_and_b32 s69, s65, 7\n\t" \
    "s_lshl_b32 s69, s69, 18\n\t" \
    "s_add_u32 s69, s69, s63\n\t" \
    "s_add_u32 s52, s36, s69\n\t" \
    "s_addc_u32 s53, s37, 0\n\t" \
    "s_barrier\n\t" \
    "ds_read_b32 v160, v11 offset:20480\n\t" \
    "s_waitcnt lgkmcnt(0)\n\t" \
    "v_readfirstlane_b32 s70, v160\n\t" \
    "s_branch vv_loop10_%=\n\t" \
    "vv_Gbad33_%=:\n\t" \
    "s_waitcnt vmcnt(0) lgkmcnt(0)\n\t" \
    "s_nop 7\n\t" \
    "v_mov_b32 v72, 0\n\t" \
    "v_mov_b32 v73, 0\n\t" \
    "v_mov_b32 v74, 0\n\t" \
    "v_mov_b32 v75, 0\n\t" \
    "v_mov_b32 v76, 0\n\t" \
    "v_mov_b32 v77, 0\n\t" \
    "v_mov_b32 v78, 0\n\t" \
    "v_mov_b32 v79, 0\n\t" \
    "s_cmp_eq_u32 s68, 0\n\t" \
    "s_cbranch_scc1 vv_F38_%=\n\t" \
    "s_sub_u32 s68, s68, 1\n\t" \
    "s_branch vv_G32_%=\n\t" \
    "vv_F38_%=:\n\t" \
    "v_mov_b32 v140, 5767424\n\t" \
    "v_mov_b32 v141, 1\n\t" \
    "global_store_dword v140, v141, s[36:37] sc1\n\t" \
    "s_waitcnt vmcnt(0)\n\t" \
    "s_branch vv_Gc34_%=\n\t" \
    "vv_Gslow35_%=:\n\t" \
    "v_max_u32 v140, v36, v37\n\t" \
    "v_max3_u32 v140, v140, v38, v39\n\t" \
    "v_max3_u32 v140, v140, v40, v41\n\t" \
    "v_max3_u32 v140, v140, v42, v43\n\t" \
    "v_max3_u32 v140, v140, v44, v45\n\t" \
    "v_max3_u32 v140, v140, v46, v47\n\t" \
    "v_max3_u32 v140, v140, v48, v49\n\t" \
    "v_max3_u32 v140, v140, v50, v51\n\t" \
    "v_max3_u32 v140, v140, v52, v53\n\t" \
    "v_max3_u32 v140, v140, v54, v55\n\t" \
    "v_max3_u32 v140, v140, v56, v57\n\t" \
    "v_max3_u32 v140, v140, v58, v59\n\t" \
    "v_max3_u32 v140, v140, v60, v61\n\t" \
    "v_max3_u32 v140, v140, v62, v63\n\t" \
    "v_max3_u32 v140, v140, v64, v65\n\t" \
    "v_max3_u32 v140, v140, v66, v67\n\t" \
    "v_max3_u32 v140, v140, v68, v69\n\t" \
    "v_max3_u32 v140, v140, v70, v71\n\t" \
    "v_cmp_eq_u32 vcc, -1, v140\n\t" \
    "s_cbranch_vccz vv_Gback36_%=\n\t" \
    "s_waitcnt lgkmcnt(0)\n\t" \
    "v_sub_u32 v140, v1, v3\n\t" \
    "s_nop 0\n\t" \
    "v_readfirstlane_b32 s69, v140\n\t" \
    "v_add_u32 v143, 20480, v0\n\t" \
    "s_nop 7\n\t" \
    "s_add_u32 s30, s46, s69\n\t" \
    "s_addc_u32 s31, s47, 0\n\t" \
    "vv_Gag39_%=:\n\t" \
    "global_load_dwordx4 v[36:39], v1, s[46:47] sc1\n\t" \
    "global_load_dwordx4 v[40:43], v23, s[46:47] sc1\n\t" \
    "global_load_dwordx4 v[44:47], v143, s[30:31] sc1\n\t" \
    "global_load_dwordx4 v[48:51], v4, s[30:31] sc1\n\t" \
    "global_load_dwordx4 v[52:55], v5, s[30:31] sc1\n\t" \
    "global_load_dwordx4 v[56:59], v6, s[30:31] sc1\n\t" \
    "global_load_dwordx4 v[60:63], v7, s[30:31] sc1\n\t" \
    "global_load_dwordx4 v[64:67], v8, s[30:31] sc1\n\t" \
    "global_load_dwordx4 v[68:71], v9, s[30:31] sc1\n\t" \
    "s_waitcnt vmcnt(0)\n\t" \
    "v_max_u32 v140, v36, v37\n\t" \
    "v_max3_u32 v140, v140, v38, v39\n\t" \
    "v_max3_u32 v140, v140, v40, v41\n\t" \
    "v_max3_u32 v140, v140, v42, v43\n\t" \
    "v_max3_u32 v140, v140, v44, v45\n\t" \
    "v_max3_u32 v140, v140, v46, v47\n\t" \
    "v_max3_u32 v140, v140, v48, v49\n\t" \
    "v_max3_u32 v140, v140, v50, v51\n\t" \
    "v_max3_u32 v140, v140, v52, v53\n\t" \
    "v_max3_u32 v140, v140, v54, v55\n\t" \
    "v_max3_u32 v140, v140, v56, v57\n\t" \
    "v_max3_u32 v140, v140, v58, v59\n\t" \
    "v_max3_u32 v140, v140, v60, v61\n\t" \
    "v_max3_u32 v140, v140, v62, v63\n\t" \
    "v_max3_u32 v140, v140, v64, v65\n\t" \
    "v_max3_u32 v140, v140, v66, v67\n\t" \
    "v_max3_u32 v140, v140, v68, v69\n\t" \
    "v_max3_u32 v140, v140, v70, v71\n\t" \
    "v_cmp_eq_u32 vcc, -1, v140\n\t" \
    "s_cbranch_vccz vv_Gredo40_%=\n\t" \
    "s_cmp_eq_u32 s68, 0\n\t" \
    "s_cbranch_scc1 vv_F41_%=\n\t" \
    "s_sub_u32 s68, s68, 1\n\t" \
    "s_sleep 1\n\t" \
    "s_branch vv_Gag39_%=\n\t" \
    "vv_F41_%=:\n\t" \
    "v_mov_b32 v140, 5767424\n\t" \
    "v_mov_b32 v141, 2\n\t" \
    "global_store_dword v140, v141, s[36:37] sc1\n\t" \
    "s_waitcnt vmcnt(0)\n\t" \
    "s_branch vv_Gredo40_%=\n\t" \
    "vv_Gredo40_%=:\n\t" \
    "v_mov_b32 v72, 0\n\t" \
    "v_mov_b32 v73, 0\n\t" \
    "v_mov_b32 v74, 0\n\t" \
    "v_mov_b32 v75, 0\n\t" \
    "v_mov_b32 v76, 0\n\t" \
    "v_mov_b32 v77, 0\n\t" \
    "v_mov_b32 v78, 0\n\t" \
    "v_mov_b32 v79, 0\n\t" \
    "v_smfmac_f32_16x16x64_bf16 v[72:75], v[36:39], a[16:23], v10\n\t" \
    "v_smfmac_f32_16x16x64_bf16 v[72:75], v[40:43], a[24:31], v10\n\t" \
    "v_smfmac_f32_16x16x64_bf16 v[72:75], v[48:51], a[32:39], v10\n\t" \
    "v_smfmac_f32_16x16x64_bf16 v[72:75], v[52:55], a[40:47], v10\n\t" \
    "v_smfmac_f32_16x16x64_bf16 v[72:75], v[56:59], a[48:55], v10\n\t" \
    "v_smfmac_f32_16x16x64_bf16 v[72:75], v[60:63], a[56:63], v10\n\t" \
    "v_smfmac_f32_16x16x64_bf16 v[72:75], v[64:67], a[0:7], v10\n\t" \
    "v_smfmac_f32_16x16x64_bf16 v[72:75], v[68:71], a[8:15], v10\n\t" \
    "v_smfmac_f32_16x16x64_bf16 v[72:75], v[44:47], a[64:71], v10\n\t" \
    "v_smfmac_f32_16x16x64_bf16 v[76:79], v[36:39], a[88:95], v10\n\t" \
    "v_smfmac_f32_16x16x64_bf16 v[76:79], v[40:43], a[96:103], v10\n\t" \
    "v_smfmac_f32_16x16x64_bf16 v[76:79], v[48:51], a[104:111], v10\n\t" \
    "v_smfmac_f32_16x16x64_bf16 v[76:79], v[52:55], a[112:119], v10\n\t" \
    "v_smfmac_f32_16x16x64_bf16 v[76:79], v[56:59], a[120:127], v10\n\t" \
    "v_smfmac_f32_16x16x64_bf16 v[76:79], v[60:63], a[128:135], v10\n\t" \
    "v_smfmac_f32_16x16x64_bf16 v[76:79], v[64:67], a[72:79], v10\n\t" \
    "v_smfmac_f32_16x16x64_bf16 v[76:79], v[68:71], a[80:87], v10\n\t" \
    "v_smfmac_f32_16x16x64_bf16 v[76:79], v[44:47], a[136:143], v10\n\t" \
    "v_add_f32 v72, v72, v74\n\t" \
    "v_add_f32 v73, v73, v75\n\t" \
    "v_cvt_pk_bf16_f32 v80, v72, v73\n\t" \
    "ds_write_b32 v12, v80 offset:0\n\t" \
    "s_nop 3\n\t" \
    "v_pk_add_f32 v[76:77], v[76:77], v[78:79]\n\t" \
    "v_cvt_pk_bf16_f32 v81, v76, v77\n\t" \
    "ds_write_b32 v12, v81 offset:256\n\t" \
    "s_branch vv_Gback36_%=\n\t" \
    "vv_end28_%=:\n\t" \
    "s_endpgm\n\t" \
    ".p2align 6\n\t" \
    "s_nop 0\n\t" \
    "s_nop 0\n\t" \
    "s_nop 0\n\t" \
    "s_nop 0\n\t" \
    "s_nop 0\n\t" \
    "s_nop 0\n\t" \
    "vv_loop11_%=:\n\t" \
    "s_getpc_b64 s[30:31]\n\t" \
    "vv_Wp44_%=:\n\t" \
    "s_add_u32 s30, s30, vv_We45_%=-vv_Wp44_%=\n\t" \
    "s_addc_u32 s31, s31, 0\n\t" \
    "s_sub_u32 s70, s70, -54\n\t" \
    "vv_W43_%=:\n\t" \
    "s_memtime s[56:57]\n\t" \
    "s_waitcnt lgkmcnt(0)\n\t" \
    "s_sub_u32 s69, s70, s56\n\t" \
    "s_cmp_gt_i32 s69, 80\n\t" \
    "s_cbranch_scc1 vv_W43_%=\n\t" \
    "s_max_i32 s69, s69, 0\n\t" \
    "s_and_b32 s69, s69, -4\n\t" \
    "s_sub_u32 s30, s30, s69\n\t" \
    "s_subb_u32 s31, s31, 0\n\t" \
    "s_setpc_b64 s[30:31]\n\t" \
    "s_nop 0\n\t" \
    "s_nop 0\n\t" \
    "s_nop 0\n\t" \
    "s_nop 0\n\t" \
    "s_nop 0\n\t" \
    "s_nop 0\n\t" \
    "s_nop 0\n\t" \
    "s_nop 0\n\t" \
    "s_nop 0\n\t" \
    "s_nop 0\n\t" \
    "s_nop 0\n\t" \
    "s_nop 0\n\t" \
    "s_nop 0\n\t" \
    "s_nop 0\n\t" \
    "s_nop 0\n\t" \
    "s_nop 0\n\t" \
    "s_nop 0\n\t" \
    "s_nop 0\n\t" \
    "s_nop 0\n\t" \
    "s_nop 0\n\t" \
    "vv_We45_%=:\n\t" \
    "vv_G46_%=:\n\t" \
    "global_load_dwordx4 v[36:39], v1, s[46:47] sc1\n\t" \
    "global_load_dword v141, v2, s[46:47] sc1\n\t" \
    "global_load_dwordx4 v[40:43], v23, s[46:47] sc1\n\t" \
    "s_waitcnt vmcnt(2)\n\t" \
    "vv_Garr51_%=:\n\t" \
    "ds_write_b128 v3, v[36:39]\n\t" \
    "v_smfmac_f32_16x16x64_bf16 v[72:75], v[36:39], a[32:39], v10\n\t" \
    "s_waitcnt vmcnt(1)\n\t" \
    "ds_write_b32 v22, v141 offset:19968\n\t" \
    "s_waitcnt vmcnt(0)\n\t" \
    "ds_write_b128 v3, v[40:43] offset:1024\n\t" \
    "v_max3_u32 v140, v36, v40, v141\n\t" \
    "v_cmp_eq_u32 vcc, -1, v140\n\t" \
    "s_cbranch_vccnz vv_Gbad47_%=\n\t" \
    "vv_Gc48_%=:\n\t" \
    "s_waitcnt lgkmcnt(0)\n\t" \
    "s_barrier\n\t" \
    "ds_read_b128 v[48:51], v4\n\t" \
    "ds_read_b128 v[52:55], v5\n\t" \
    "v_smfmac_f32_16x16x64_bf16 v[76:79], v[36:39], a[104:111], v10\n\t" \
    "ds_read_b128 v[56:59], v6\n\t" \
    "ds_read_b128 v[60:63], v7\n\t" \
    "v_smfmac_f32_16x16x64_bf16 v[72:75], v[40:43], a[40:47], v10\n\t" \
    "ds_read_b128 v[64:67], v8\n\t" \
    "v_smfmac_f32_16x16x64_bf16 v[76:79], v[40:43], a[112:119], v10\n\t" \
    "ds_read_b128 v[68:71], v9\n\t" \
    "s_waitcnt lgkmcnt(5)\n\t" \
    "v_smfmac_f32_16x16x64_bf16 v[72:75], v[48:51], a[48:55], v10\n\t" \
    "ds_read_b128 v[44:47], v0 offset:19456\n\t" \
    "s_waitcnt lgkmcnt(5)\n\t" \
    "v_smfmac_f32_16x16x64_bf16 v[72:75], v[52:55], a[56:63], v10\n\t" \
    "s_waitcnt lgkmcnt(4)\n\t" \
    "v_smfmac_f32_16x16x64_bf16 v[72:75], v[56:59], a[0:7], v10\n\t" \
    "s_waitcnt lgkmcnt(3)\n\t" \
    "v_smfmac_f32_16x16x64_bf16 v[72:75], v[60:63], a[8:15], v10\n\t" \
    "s_waitcnt lgkmcnt(2)\n\t" \
    "v_smfmac_f32_16x16x64_bf16 v[72:75], v[64:67], a[16:23], v10\n\t" \
    "s_waitcnt lgkmcnt(1)\n\t" \
    "v_smfmac_f32_16x16x64_bf16 v[72:75], v[68:71], a[24:31], v10\n\t" \
    "s_waitcnt lgkmcnt(0)\n\t" \
    "v_smfmac_f32_16x16x64_bf16 v[72:75], v[44:47], a[64:71], v10\n\t" \
    "s_nop 2\n\t" \
    "v_smfmac_f32_16x16x64_bf16 v[76:79], v[48:51], a[120:127], v10\n\t" \
    "s_nop 2\n\t" \
    "v_smfmac_f32_16x16x64_bf16 v[76:79], v[52:55], a[128:135], v10\n\t" \
    "v_add_f32 v72, v72, v74\n\t" \
    "v_smfmac_f32_16x16x64_bf16 v[76:79], v[56:59], a[72:79], v10\n\t" \
    "v_add_f32 v73, v73, v75\n\t" \
    "v_smfmac_f32_16x16x64_bf16 v[76:79], v[60:63], a[80:87], v10\n\t" \
    "v_cvt_pk_bf16_f32 v80, v72, v73\n\t" \
    "v_smfmac_f32_16x16x64_bf16 v[76:79], v[64:67], a[88:95], v10\n\t" \
    "ds_write_b32 v12, v80 offset:0\n\t" \
    "v_smfmac_f32_16x16x64_bf16 v[76:79], v[68:71], a[96:103], v10\n\t" \
    "v_smfmac_f32_16x16x64_bf16 v[76:79], v[44:47], a[136:143], v10\n\t" \
    "s_nop 6\n\t" \
    "v_cmp_u_f32 vcc, v72, v73\n\t" \
    "v_pk_add_f32 v[76:77], v[76:77], v[78:79]\n\t" \
    "v_cvt_pk_bf16_f32 v81, v76, v77\n\t" \
    "ds_write_b32 v12, v81 offset:256\n\t" \
    "s_cbranch_vccnz vv_Gslow49_%=\n\t" \
    "vv_Gback50_%=:\n\t" \
    "s_waitcnt lgkmcnt(0)\n\t" \
    "s_barrier\n\t" \
    "ds_read_b64_tr_b16 v[84:85], v13 offset:0\n\t" \
    "v_smfmac_f32_16x16x64_bf16 v[100:103], v[36:39], a[0:7], v10\n\t" \
    "ds_read_b64_tr_b16 v[86:87], v13 offset:64\n\t" \
    "ds_read_b64_tr_b16 v[92:93], v13 offset:1152\n\t" \
    "v_smfmac_f32_16x16x64_bf16 v[100:103], v[36:39], a[0:7], v10\n\t" \
    "ds_read_b64_tr_b16 v[94:95], v13 offset:1216\n\t" \
    "ds_read_b64_tr_b16 v[88:89], v25 offset:0\n\t" \
    "v_smfmac_f32_16x16x64_bf16 v[100:103], v[36:39], a[0:7], v10\n\t" \
    "ds_read_b64_tr_b16 v[90:91], v25 offset:64\n\t" \
    "s_waitcnt lgkmcnt(4)\n\t" \
    "v_smfmac_f32_16x16x64_bf16 v[124:127], v[84:87], v[240:247], v19\n\t" \
    "ds_read_b64_tr_b16 v[96:97], v25 offset:1152\n\t" \
    "ds_read_b64_tr_b16 v[98:99], v25 offset:1216\n\t" \
    "s_waitcnt lgkmcnt(4)\n\t" \
    "v_smfmac_f32_16x16x64_bf16 v[124:127], v[92:95], v[248:255], v19\n\t" \
    "s_nop 2\n\t" \
    "v_smfmac_f32_16x16x64_bf16 v[108:111], v[84:87], a[144:151], v19\n\t" \
    "s_nop 2\n\t" \
    "v_smfmac_f32_16x16x64_bf16 v[108:111], v[92:95], a[160:167], v19\n\t" \
    "s_nop 1\n\t" \
    "v_permlane32_swap_b32 v124, v126\n\t" \
    "v_smfmac_f32_16x16x64_bf16 v[112:115], v[84:87], a[176:183], v19\n\t" \
    "s_nop 1\n\t" \
    "v_permlane32_swap_b32 v125, v127\n\t" \
    "v_smfmac_f32_16x16x64_bf16 v[112:115], v[92:95], a[192:199], v19\n\t" \
    "s_nop 0\n\t" \
    "v_add_f32 v124, v124, v126\n\t" \
    "s_waitcnt lgkmcnt(2)\n\t" \
    "v_smfmac_f32_16x16x64_bf16 v[108:111], v[88:91], a[152:159], v19\n\t" \
    "s_nop 1\n\t" \
    "v_add_f32 v125, v125, v127\n\t" \
    "v_smfmac_f32_16x16x64_bf16 v[112:115], v[88:91], a[184:191], v19\n\t" \
    "s_nop 0\n\t" \
    "v_cvt_pk_bf16_f32 v136, v124, v125\n\t" \
    "s_waitcnt lgkmcnt(0)\n\t" \
    "v_smfmac_f32_16x16x64_bf16 v[108:111], v[96:99], a[168:175], v19\n\t" \
    "s_nop 1\n\t" \
    "v_cvt_pk_bf16_f32 v137, v124, v125\n\t" \
    "v_smfmac_f32_16x16x64_bf16 v[112:115], v[96:99], a[200:207], v19\n\t" \
    "s_nop 1\n\t" \
    "v_permlane32_swap_b32 v136, v137\n\t" \
    "v_smfmac_f32_16x16x64_bf16 v[116:119], v[84:87], a[208:215], v19\n\t" \
    "s_mov_b32 exec_hi, 0\n\t" \
    "global_store_dwordx2 v16, v[136:137], s[38:39] sc1\n\t" \
    "s_mov_b32 exec_hi, -1\n\t" \
    "v_cvt_pk_bf16_f32 v128, v108, v109\n\t" \
    "v_smfmac_f32_16x16x64_bf16 v[116:119], v[88:91], a[216:223], v19\n\t" \
    "s_nop 1\n\t" \
    "v_cvt_pk_bf16_f32 v129, v110, v111\n\t" \
    "v_smfmac_f32_16x16x64_bf16 v[116:119], v[92:95], a[224:231], v19\n\t" \
    "s_nop 1\n\t" \
    "v_cvt_pk_bf16_f32 v130, v112, v113\n\t" \
    "v_smfmac_f32_16x16x64_bf16 v[116:119], v[96:99], a[232:239], v19\n\t" \
    "s_nop 1\n\t" \
    "v_cvt_pk_bf16_f32 v131, v114, v115\n\t" \
    "v_smfmac_f32_16x16x64_bf16 v[120:123], v[84:87], a[240:247], v19\n\t" \
    "s_nop 1\n\t" \
    "v_permlane16_swap_b32 v128, v130\n\t" \
    "v_smfmac_f32_16x16x64_bf16 v[120:123], v[88:91], a[248:255], v19\n\t" \
    "s_nop 1\n\t" \
    "v_permlane16_swap_b32 v129, v131\n\t" \
    "v_smfmac_f32_16x16x64_bf16 v[120:123], v[92:95], v[224:231], v19\n\t" \
    "s_nop 0\n\t" \
    "global_store_dwordx4 v14, v[128:131], s[38:39] sc1\n\t" \
    "v_cvt_pk_bf16_f32 v132, v116, v117\n\t" \
    "v_smfmac_f32_16x16x64_bf16 v[120:123], v[96:99], v[232:239], v19\n\t" \
    "s_nop 1\n\t" \
    "v_cvt_pk_bf16_f32 v133, v118, v119\n\t" \
    "s_nop 4\n\t" \
    "v_cvt_pk_bf16_f32 v134, v120, v121\n\t" \
    "v_cvt_pk_bf16_f32 v135, v122, v123\n\t" \
    "s_nop 0\n\t" \
    "v_permlane16_swap_b32 v132, v134\n\t" \
    "v_permlane16_swap_b32 v133, v135\n\t" \
    "global_store_dwordx4 v15, v[132:135], s[38:39] sc1\n\t" \
    "s_cmp_eq_u32 s65, 63\n\t" \
    "s_cbranch_scc1 vv_end42_%=\n\t" \
    "s_add_u32 s65, s65, 1\n\t" \
    "v_mov_b32 v72, 0\n\t" \
    "v_mov_b32 v73, 0\n\t" \
    "v_mov_b32 v74, 0\n\t" \
    "v_mov_b32 v75, 0\n\t" \
    "v_mov_b32 v76, 0\n\t" \
    "v_mov_b32 v77, 0\n\t" \
    "v_mov_b32 v78, 0\n\t" \
    "v_mov_b32 v79, 0\n\t" \
    "v_mov_b32 v108, 0\n\t" \
    "v_mov_b32 v109, 0\n\t" \
    "v_mov_b32 v110, 0\n\t" \
    "v_mov_b32 v111, 0\n\t" \
    "v_mov_b32 v112, 0\n\t" \
    "v_mov_b32 v113, 0\n\t" \
    "v_mov_b32 v114, 0\n\t" \
    "v_mov_b32 v115, 0\n\t" \
    "v_mov_b32 v116, 0\n\t" \
    "v_mov_b32 v117, 0\n\t" \
    "v_mov_b32 v118, 0\n\t" \
    "v_mov_b32 v119, 0\n\t" \
    "v_mov_b32 v120, 0\n\t" \
    "v_mov_b32 v121, 0\n\t" \
    "v_mov_b32 v122, 0\n\t" \
    "v_mov_b32 v123, 0\n\t" \
    "v_mov_b32 v124, 0\n\t" \
    "v_mov_b32 v125, 0\n\t" \
    "v_mov_b32 v126, 0\n\t" \
    "v_mov_b32 v127, 0\n\t" \
    "s_add_u32 s69, s65, 0\n\t" \
    "s_and_b32 s69, s69, 7\n\t" \
    "s_lshl_b32 s69, s69, 18\n\t" \
    "s_add_u32 s69, s69, s62\n\t" \
    "s_add_u32 s46, s36, s69\n\t" \
    "s_addc_u32 s47, s37, 0\n\t" \
    "s_add_u32 s70, s65, 0\n\t" \
    "s_mul_i32 s69, s70, 171\n\t" \
    "s_lshr_b32 s69, s69, 9\n\t" \
    "s_mul_i32 s70, s70, 589824\n\t" \
    "s_mul_i32 s71, s69, 327680\n\t" \
    "s_add_u32 s70, s70, s71\n\t" \
    "s_lshl1_add_u32 s69, s69, 4\n\t" \
    "s_bitcmp1_b64 s[44:45], s69\n\t" \
    "s_cselect_b32 s69, 8192, 0\n\t" \
    "s_add_u32 s69, s69, s70\n\t" \
    "s_add_u32 s69, s69, 123731968\n\t" \
    "s_add_u32 s38, s36, s69\n\t" \
    "s_addc_u32 s39, s37, 0\n\t" \
    "s_and_b32 s69, s69, 0xffe00000\n\t" \
    "s_add_u32 s69, s69, 1823744\n\t" \
    "v_mov_b32 v17, s69\n\t" \
    "global_load_dword v17, v17, s[36:37]\n\t" \
    "s_and_b32 s69, s65, 7\n\t" \
    "s_lshl_b32 s69, s69, 18\n\t" \
    "s_add_u32 s69, s69, s63\n\t" \
    "s_add_u32 s52, s36, s69\n\t" \
    "s_addc_u32 s53, s37, 0\n\t" \
    "s_barrier\n\t" \
    "ds_read_b32 v160, v11 offset:20480\n\t" \
    "s_waitcnt lgkmcnt(0)\n\t" \
    "v_readfirstlane_b32 s70, v160\n\t" \
    "s_branch vv_loop11_%=\n\t" \
    "vv_Gbad47_%=:\n\t" \
    "s_waitcnt vmcnt(0) lgkmcnt(0)\n\t" \
    "s_nop 7\n\t" \
    "v_mov_b32 v72, 0\n\t" \
    "v_mov_b32 v73, 0\n\t" \
    "v_mov_b32 v74, 0\n\t" \
    "v_mov_b32 v75, 0\n\t" \
    "v_mov_b32 v76, 0\n\t" \
    "v_mov_b32 v77, 0\n\t" \
    "v_mov_b32 v78, 0\n\t" \
    "v_mov_b32 v79, 0\n\t" \
    "s_cmp_eq_u32 s68, 0\n\t" \
    "s_cbranch_scc1 vv_F52_%=\n\t" \
    "s_sub_u32 s68, s68, 1\n\t" \
    "s_branch vv_G46_%=\n\t" \
    "vv_F52_%=:\n\t" \
    "v_mov_b32 v140, 5767424\n\t" \
    "v_mov_b32 v141, 1\n\t" \
    "global_store_dword v140, v141, s[36:37] sc1\n\t" \
    "s_waitcnt vmcnt(0)\n\t" \
    "s_branch vv_Gc48_%=\n\t" \
    "vv_Gslow49_%=:\n\t" \
    "v_max_u32 v140, v36, v37\n\t" \
    "v_max3_u32 v140, v140, v38, v39\n\t" \
    "v_max3_u32 v140, v140, v40, v41\n\t" \
    "v_max3_u32 v140, v140, v42, v43\n\t" \
    "v_max3_u32 v140, v140, v44, v45\n\t" \
    "v_max3_u32 v140, v140, v46, v47\n\t" \
    "v_max3_u32 v140, v140, v48, v49\n\t" \
    "v_max3_u32 v140, v140, v50, v51\n\t" \
    "v_max3_u32 v140, v140, v52, v53\n\t" \
    "v_max3_u32 v140, v140, v54, v55\n\t" \
    "v_max3_u32 v140, v140, v56, v57\n\t" \
    "v_max3_u32 v140, v140, v58, v59\n\t" \
    "v_max3_u32 v140, v140, v60, v61\n\t" \
    "v_max3_u32 v140, v140, v62, v63\n\t" \
    "v_max3_u32 v140, v140, v64, v65\n\t" \
    "v_max3_u32 v140, v140, v66, v67\n\t" \
    "v_max3_u32 v140, v140, v68, v69\n\t" \
    "v_max3_u32 v140, v140, v70, v71\n\t" \
    "v_cmp_eq_u32 vcc, -1, v140\n\t" \
    "s_cbranch_vccz vv_Gback50_%=\n\t" \
    "s_waitcnt lgkmcnt(0)\n\t" \
    "v_sub_u32 v140, v1, v3\n\t" \
    "s_nop 0\n\t" \
    "v_readfirstlane_b32 s69, v140\n\t" \
    "v_add_u32 v143, 20480, v0\n\t" \
    "s_nop 7\n\t" \
    "s_add_u32 s30, s46, s69\n\t" \
    "s_addc_u32 s31, s47, 0\n\t" \
    "vv_Gag53_%=:\n\t" \
    "global_load_dwordx4 v[36:39], v1, s[46:47] sc1\n\t" \
    "global_load_dwordx4 v[40:43], v23, s[46:47] sc1\n\t" \
    "global_load_dwordx4 v[44:47], v143, s[30:31] sc1\n\t" \
    "global_load_dwordx4 v[48:51], v4, s[30:31] sc1\n\t" \
    "global_load_dwordx4 v[52:55], v5, s[30:31] sc1\n\t" \
    "global_load_dwordx4 v[56:59], v6, s[30:31] sc1\n\t" \
    "global_load_dwordx4 v[60:63], v7, s[30:31] sc1\n\t" \
    "global_load_dwordx4 v[64:67], v8, s[30:31] sc1\n\t" \
    "global_load_dwordx4 v[68:71], v9, s[30:31] sc1\n\t" \
    "s_waitcnt vmcnt(0)\n\t" \
    "v_max_u32 v140, v36, v37\n\t" \
    "v_max3_u32 v140, v140, v38, v39\n\t" \
    "v_max3_u32 v140, v140, v40, v41\n\t" \
    "v_max3_u32 v140, v140, v42, v43\n\t" \
    "v_max3_u32 v140, v140, v44, v45\n\t" \
    "v_max3_u32 v140, v140, v46, v47\n\t" \
    "v_max3_u32 v140, v140, v48, v49\n\t" \
    "v_max3_u32 v140, v140, v50, v51\n\t" \
    "v_max3_u32 v140, v140, v52, v53\n\t" \
    "v_max3_u32 v140, v140, v54, v55\n\t" \
    "v_max3_u32 v140, v140, v56, v57\n\t" \
    "v_max3_u32 v140, v140, v58, v59\n\t" \
    "v_max3_u32 v140, v140, v60, v61\n\t" \
    "v_max3_u32 v140, v140, v62, v63\n\t" \
    "v_max3_u32 v140, v140, v64, v65\n\t" \
    "v_max3_u32 v140, v140, v66, v67\n\t" \
    "v_max3_u32 v140, v140, v68, v69\n\t" \
    "v_max3_u32 v140, v140, v70, v71\n\t" \
    "v_cmp_eq_u32 vcc, -1, v140\n\t" \
    "s_cbranch_vccz vv_Gredo54_%=\n\t" \
    "s_cmp_eq_u32 s68, 0\n\t" \
    "s_cbranch_scc1 vv_F55_%=\n\t" \
    "s_sub_u32 s68, s68, 1\n\t" \
    "s_sleep 1\n\t" \
    "s_branch vv_Gag53_%=\n\t" \
    "vv_F55_%=:\n\t" \
    "v_mov_b32 v140, 5767424\n\t" \
    "v_mov_b32 v141, 2\n\t" \
    "global_store_dword v140, v141, s[36:37] sc1\n\t" \
    "s_waitcnt vmcnt(0)\n\t" \
    "s_branch vv_Gredo54_%=\n\t" \
    "vv_Gredo54_%=:\n\t" \
    "v_mov_b32 v72, 0\n\t" \
    "v_mov_b32 v73, 0\n\t" \
    "v_mov_b32 v74, 0\n\t" \
    "v_mov_b32 v75, 0\n\t" \
    "v_mov_b32 v76, 0\n\t" \
    "v_mov_b32 v77, 0\n\t" \
    "v_mov_b32 v78, 0\n\t" \
    "v_mov_b32 v79, 0\n\t" \
    "v_smfmac_f32_16x16x64_bf16 v[72:75], v[36:39], a[32:39], v10\n\t" \
    "v_smfmac_f32_16x16x64_bf16 v[72:75], v[40:43], a[40:47], v10\n\t" \
    "v_smfmac_f32_16x16x64_bf16 v[72:75], v[48:51], a[48:55], v10\n\t" \
    "v_smfmac_f32_16x16x64_bf16 v[72:75], v[52:55], a[56:63], v10\n\t" \
    "v_smfmac_f32_16x16x64_bf16 v[72:75], v[56:59], a[0:7], v10\n\t" \
    "v_smfmac_f32_16x16x64_bf16 v[72:75], v[60:63], a[8:15], v10\n\t" \
    "v_smfmac_f32_16x16x64_bf16 v[72:75], v[64:67], a[16:23], v10\n\t" \
    "v_smfmac_f32_16x16x64_bf16 v[72:75], v[68:71], a[24:31], v10\n\t" \
    "v_smfmac_f32_16x16x64_bf16 v[72:75], v[44:47], a[64:71], v10\n\t" \
    "v_smfmac_f32_16x16x64_bf16 v[76:79], v[36:39], a[104:111], v10\n\t" \
    "v_smfmac_f32_16x16x64_bf16 v[76:79], v[40:43], a[112:119], v10\n\t" \
    "v_smfmac_f32_16x16x64_bf16 v[76:79], v[48:51], a[120:127], v10\n\t" \
    "v_smfmac_f32_16x16x64_bf16 v[76:79], v[52:55], a[128:135], v10\n\t" \
    "v_smfmac_f32_16x16x64_bf16 v[76:79], v[56:59], a[72:79], v10\n\t" \
    "v_smfmac_f32_16x16x64_bf16 v[76:79], v[60:63], a[80:87], v10\n\t" \
    "v_smfmac_f32_16x16x64_bf16 v[76:79], v[64:67], a[88:95], v10\n\t" \
    "v_smfmac_f32_16x16x64_bf16 v[76:79], v[68:71], a[96:103], v10\n\t" \
    "v_smfmac_f32_16x16x64_bf16 v[76:79], v[44:47], a[136:143], v10\n\t" \
    "v_add_f32 v72, v72, v74\n\t" \
    "v_add_f32 v73, v73, v75\n\t" \
    "v_cvt_pk_bf16_f32 v80, v72, v73\n\t" \
    "ds_write_b32 v12, v80 offset:0\n\t" \
    "s_nop 3\n\t" \
    "v_pk_add_f32 v[76:77], v[76:77], v[78:79]\n\t" \
    "v_cvt_pk_bf16_f32 v81, v76, v77\n\t" \
    "ds_write_b32 v12, v81 offset:256\n\t" \
    "s_branch vv_Gback50_%=\n\t" \
    "vv_end42_%=:\n\t" \
    "s_endpgm\n\t" \
    ".p2align 6\n\t" \
    "s_nop 0\n\t" \
    "s_nop 0\n\t" \
    "s_nop 0\n\t" \
    "s_nop 0\n\t" \
    "s_nop 0\n\t" \
    "s_nop 0\n\t" \
    "vv_loop12_%=:\n\t" \
    "s_getpc_b64 s[30:31]\n\t" \
    "vv_Wp58_%=:\n\t" \
    "s_add_u32 s30, s30, vv_We59_%=-vv_Wp58_%=\n\t" \
    "s_addc_u32 s31, s31, 0\n\t" \
    "s_sub_u32 s70, s70, -54\n\t" \
    "vv_W57_%=:\n\t" \
    "s_memtime s[56:57]\n\t" \
    "s_waitcnt lgkmcnt(0)\n\t" \
    "s_sub_u32 s69, s70, s56\n\t" \
    "s_cmp_gt_i32 s69, 80\n\t" \
    "s_cbranch_scc1 vv_W57_%=\n\t" \
    "s_max_i32 s69, s69, 0\n\t" \
    "s_and_b32 s69, s69, -4\n\t" \
    "s_sub_u32 s30, s30, s69\n\t" \
    "s_subb_u32 s31, s31, 0\n\t" \
    "s_setpc_b64 s[30:31]\n\t" \
    "s_nop 0\n\t" \
    "s_nop 0\n\t" \
    "s_nop 0\n\t" \
    "s_nop 0\n\t" \
    "s_nop 0\n\t" \
    "s_nop 0\n\t" \
    "s_nop 0\n\t" \
    "s_nop 0\n\t" \
    "s_nop 0\n\t" \
    "s_nop 0\n\t" \
    "s_nop 0\n\t" \
    "s_nop 0\n\t" \
    "s_nop 0\n\t" \
    "s_nop 0\n\t" \
    "s_nop 0\n\t" \
    "s_nop 0\n\t" \
    "s_nop 0\n\t" \
    "s_nop 0\n\t" \
    "s_nop 0\n\t" \
    "s_nop 0\n\t" \
    "vv_We59_%=:\n\t" \
    "vv_G60_%=:\n\t" \
    "global_load_dwordx4 v[36:39], v1, s[46:47] sc1\n\t" \
    "global_load_dword v141, v2, s[46:47] sc1\n\t" \
    "global_load_dwordx4 v[40:43], v23, s[46:47] sc1\n\t" \
    "s_waitcnt vmcnt(2)\n\t" \
    "vv_Garr65_%=:\n\t" \
    "ds_write_b128 v3, v[36:39]\n\t" \
    "v_smfmac_f32_16x16x64_bf16 v[72:75], v[36:39], a[48:55], v10\n\t" \
    "s_waitcnt vmcnt(1)\n\t" \
    "ds_write_b32 v22, v141 offset:20224\n\t" \
    "s_waitcnt vmcnt(0)\n\t" \
    "ds_write_b128 v3, v[40:43] offset:1024\n\t" \
    "v_max3_u32 v140, v36, v40, v141\n\t" \
    "v_cmp_eq_u32 vcc, -1, v140\n\t" \
    "s_cbranch_vccnz vv_Gbad61_%=\n\t" \
    "vv_Gc62_%=:\n\t" \
    "s_waitcnt lgkmcnt(0)\n\t" \
    "s_barrier\n\t" \
    "ds_read_b128 v[48:51], v4\n\t" \
    "ds_read_b128 v[52:55], v5\n\t" \
    "v_smfmac_f32_16x16x64_bf16 v[76:79], v[36:39], a[120:127], v10\n\t" \
    "ds_read_b128 v[56:59], v6\n\t" \
    "ds_read_b128 v[60:63], v7\n\t" \
    "v_smfmac_f32_16x16x64_bf16 v[72:75], v[40:43], a[56:63], v10\n\t" \
    "ds_read_b128 v[64:67], v8\n\t" \
    "v_smfmac_f32_16x16x64_bf16 v[76:79], v[40:43], a[128:135], v10\n\t" \
    "ds_read_b128 v[68:71], v9\n\t" \
    "s_waitcnt lgkmcnt(5)\n\t" \
    "v_smfmac_f32_16x16x64_bf16 v[72:75], v[48:51], a[0:7], v10\n\t" \
    "ds_read_b128 v[44:47], v0 offset:19456\n\t" \
    "s_waitcnt lgkmcnt(5)\n\t" \
    "v_smfmac_f32_16x16x64_bf16 v[72:75], v[52:55], a[8:15], v10\n\t" \
    "s_waitcnt lgkmcnt(4)\n\t" \
    "v_smfmac_f32_16x16x64_bf16 v[72:75], v[56:59], a[16:23], v10\n\t" \
    "s_waitcnt lgkmcnt(3)\n\t" \
    "v_smfmac_f32_16x16x64_bf16 v[72:75], v[60:63], a[24:31], v10\n\t" \
    "s_waitcnt lgkmcnt(2)\n\t" \
    "v_smfmac_f32_16x16x64_bf16 v[72:75], v[64:67], a[32:39], v10\n\t" \
    "s_waitcnt lgkmcnt(1)\n\t" \
    "v_smfmac_f32_16x16x64_bf16 v[72:75], v[68:71], a[40:47], v10\n\t" \
    "s_waitcnt lgkmcnt(0)\n\t" \
    "v_smfmac_f32_16x16x64_bf16 v[72:75], v[44:47], a[64:71], v10\n\t" \
    "s_nop 2\n\t" \
    "v_smfmac_f32_16x16x64_bf16 v[76:79], v[48:51], a[72:79], v10\n\t" \
    "s_nop 2\n\t" \
    "v_smfmac_f32_16x16x64_bf16 v[76:79], v[52:55], a[80:87], v10\n\t" \
    "v_add_f32 v72, v72, v74\n\t" \
    "v_smfmac_f32_16x16x64_bf16 v[76:79], v[56:59], a[88:95], v10\n\t" \
    "v_add_f32 v73, v73, v75\n\t" \
    "v_smfmac_f32_16x16x64_bf16 v[76:79], v[60:63], a[96:103], v10\n\t" \
    "v_cvt_pk_bf16_f32 v80, v72, v73\n\t" \
    "v_smfmac_f32_16x16x64_bf16 v[76:79], v[64:67], a[104:111], v10\n\t" \
    "ds_write_b32 v12, v80 offset:0\n\t" \
    "v_smfmac_f32_16x16x64_bf16 v[76:79], v[68:71], a[112:119], v10\n\t" \
    "v_smfmac_f32_16x16x64_bf16 v[76:79], v[44:47], a[136:143], v10\n\t" \
    "s_nop 6\n\t" \
    "v_cmp_u_f32 vcc, v72, v73\n\t" \
    "v_pk_add_f32 v[76:77], v[76:77], v[78:79]\n\t" \
    "v_cvt_pk_bf16_f32 v81, v76, v77\n\t" \
    "ds_write_b32 v12, v81 offset:256\n\t" \
    "s_cbranch_vccnz vv_Gslow63_%=\n\t" \
    "vv_Gback64_%=:\n\t" \
    "s_waitcnt lgkmcnt(0)\n\t" \
    "s_barrier\n\t" \
    "ds_read_b64_tr_b16 v[84:85], v13 offset:0\n\t" \
    "v_smfmac_f32_16x16x64_bf16 v[100:103], v[36:39], a[0:7], v10\n\t" \
    "ds_read_b64_tr_b16 v[86:87], v13 offset:64\n\t" \
    "ds_read_b64_tr_b16 v[92:93], v13 offset:1152\n\t" \
    "v_smfmac_f32_16x16x64_bf16 v[100:103], v[36:39], a[0:7], v10\n\t" \
    "ds_read_b64_tr_b16 v[94:95], v13 offset:1216\n\t" \
    "ds_read_b64_tr_b16 v[88:89], v25 offset:0\n\t" \
    "v_smfmac_f32_16x16x64_bf16 v[100:103], v[36:39], a[0:7], v10\n\t" \
    "ds_read_b64_tr_b16 v[90:91], v25 offset:64\n\t" \
    "s_waitcnt lgkmcnt(4)\n\t" \
    "v_smfmac_f32_16x16x64_bf16 v[124:127], v[84:87], v[240:247], v19\n\t" \
    "ds_read_b64_tr_b16 v[96:97], v25 offset:1152\n\t" \
    "ds_read_b64_tr_b16 v[98:99], v25 offset:1216\n\t" \
    "s_waitcnt lgkmcnt(4)\n\t" \
    "v_smfmac_f32_16x16x64_bf16 v[124:127], v[92:95], v[248:255], v19\n\t" \
    "s_nop 2\n\t" \
    "v_smfmac_f32_16x16x64_bf16 v[108:111], v[84:87], a[144:151], v19\n\t" \
    "s_nop 2\n\t" \
    "v_smfmac_f32_16x16x64_bf16 v[108:111], v[92:95], a[160:167], v19\n\t" \
    "s_nop 1\n\t" \
    "v_permlane32_swap_b32 v124, v126\n\t" \
    "v_smfmac_f32_16x16x64_bf16 v[112:115], v[84:87], a[176:183], v19\n\t" \
    "s_nop 1\n\t" \
    "v_permlane32_swap_b32 v125, v127\n\t" \
    "v_smfmac_f32_16x16x64_bf16 v[112:115], v[92:95], a[192:199], v19\n\t" \
    "s_nop 0\n\t" \
    "v_add_f32 v124, v124, v126\n\t" \
    "s_waitcnt lgkmcnt(2)\n\t" \
    "v_smfmac_f32_16x16x64_bf16 v[108:111], v[88:91], a[152:159], v19\n\t" \
    "s_nop 1\n\t" \
    "v_add_f32 v125, v125, v127\n\t" \
    "v_smfmac_f32_16x16x64_bf16 v[112:115], v[88:91], a[184:191], v19\n\t" \
    "s_nop 0\n\t" \
    "v_cvt_pk_bf16_f32 v136, v124, v125\n\t" \
    "s_waitcnt lgkmcnt(0)\n\t" \
    "v_smfmac_f32_16x16x64_bf16 v[108:111], v[96:99], a[168:175], v19\n\t" \
    "s_nop 1\n\t" \
    "v_cvt_pk_bf16_f32 v137, v124, v125\n\t" \
    "v_smfmac_f32_16x16x64_bf16 v[112:115], v[96:99], a[200:207], v19\n\t" \
    "s_nop 1\n\t" \
    "v_permlane32_swap_b32 v136, v137\n\t" \
    "v_smfmac_f32_16x16x64_bf16 v[116:119], v[84:87], a[208:215], v19\n\t" \
    "s_mov_b32 exec_hi, 0\n\t" \
    "global_store_dwordx2 v16, v[136:137], s[38:39] sc1\n\t" \
    "s_mov_b32 exec_hi, -1\n\t" \
    "v_cvt_pk_bf16_f32 v128, v108, v109\n\t" \
    "v_smfmac_f32_16x16x64_bf16 v[116:119], v[88:91], a[216:223], v19\n\t" \
    "s_nop 1\n\t" \
    "v_cvt_pk_bf16_f32 v129, v110, v111\n\t" \
    "v_smfmac_f32_16x16x64_bf16 v[116:119], v[92:95], a[224:231], v19\n\t" \
    "s_nop 1\n\t" \
    "v_cvt_pk_bf16_f32 v130, v112, v113\n\t" \
    "v_smfmac_f32_16x16x64_bf16 v[116:119], v[96:99], a[232:239], v19\n\t" \
    "s_nop 1\n\t" \
    "v_cvt_pk_bf16_f32 v131, v114, v115\n\t" \
    "v_smfmac_f32_16x16x64_bf16 v[120:123], v[84:87], a[240:247], v19\n\t" \
    "s_nop 1\n\t" \
    "v_permlane16_swap_b32 v128, v130\n\t" \
    "v_smfmac_f32_16x16x64_bf16 v[120:123], v[88:91], a[248:255], v19\n\t" \
    "s_nop 1\n\t" \
    "v_permlane16_swap_b32 v129, v131\n\t" \
    "v_smfmac_f32_16x16x64_bf16 v[120:123], v[92:95], v[224:231], v19\n\t" \
    "s_nop 0\n\t" \
    "global_store_dwordx4 v14, v[128:131], s[38:39] sc1\n\t" \
    "v_cvt_pk_bf16_f32 v132, v116, v117\n\t" \
    "v_smfmac_f32_16x16x64_bf16 v[120:123], v[96:99], v[232:239], v19\n\t" \
    "s_nop 1\n\t" \
    "v_cvt_pk_bf16_f32 v133, v118, v119\n\t" \
    "s_nop 4\n\t" \
    "v_cvt_pk_bf16_f32 v134, v120, v121\n\t" \
    "v_cvt_pk_bf16_f32 v135, v122, v123\n\t" \
    "s_nop 0\n\t" \
    "v_permlane16_swap_b32 v132, v134\n\t" \
    "v_permlane16_swap_b32 v133, v135\n\t" \
    "global_store_dwordx4 v15, v[132:135], s[38:39] sc1\n\t" \
    "global_load_dword v140, v20, s[52:53] sc1\n\t" \
    "s_cmp_eq_u32 s65, 63\n\t" \
    "s_cbranch_scc1 vv_end56_%=\n\t" \
    "s_cmp_lg_u32 s65, 0\n\t" \
    "s_cbranch_scc1 vv_ng66_%=\n\t" \
    "v_mov_b32 v160, 5769216\n\t" \
    "v_mov_b32 v161, 1\n\t" \
    "s_mov_b64 exec, 1\n\t" \
    "global_atomic_add v162, v160, v161, s[36:37] sc0 sc1\n\t" \
    "s_mov_b64 exec, -1\n\t" \
    "v_mbcnt_lo_u32_b32 v160, -1, 0\n\t" \
    "v_and_b32 v161, 3, v160\n\t" \
    "v_lshlrev_b32 v161, 16, v161\n\t" \
    "v_bfe_u32 v163, v160, 2, 2\n\t" \
    "v_lshl_add_u32 v161, v163, 11, v161\n\t" \
    "v_bfe_u32 v163, v160, 4, 1\n\t" \
    "v_lshl_add_u32 v161, v163, 14, v161\n\t" \
    "v_add_u32 v161, s63, v161\n\t" \
    "s_waitcnt vmcnt(0)\n\t" \
    "v_readfirstlane_b32 s69, v162\n\t" \
    "s_nop 1\n\t" \
    "s_cmp_lg_u32 s69, 126\n\t" \
    "s_cbranch_scc1 vv_go67_%=\n\t" \
    "s_memrealtime s[56:57]\n\t" \
    "s_waitcnt lgkmcnt(0)\n\t" \
    "s_lshl_b32 s69, s56, 4\n\t" \
    "s_add_u32 s69, s69, 2960\n\t" \
    "s_add_u32 s70, s56, 24\n\t" \
    "s_lshr_b32 s70, s70, 4\n\t" \
    "v_lshrrev_b32 v163, 2, v160\n\t" \
    "v_add_u32 v163, s70, v163\n\t" \
    "v_and_b32 v163, 63, v163\n\t" \
    "v_lshlrev_b32 v163, 6, v163\n\t" \
    "v_and_b32 v162, 3, v160\n\t" \
    "v_lshl_add_u32 v163, v162, 12, v163\n\t" \
    "v_add_u32 v163, 5783552, v163\n\t" \
    "v_mov_b32 v160, s69\n\t" \
    "s_mov_b32 exec_hi, 0\n\t" \
    "global_store_dword v163, v160, s[36:37] sc1\n\t" \
    "global_store_dword v161, v160, s[36:37] sc1\n\t" \
    "s_mov_b32 exec_hi, -1\n\t" \
    "vv_go67_%=:\n\t" \
    "vv_ng66_%=:\n\t" \
    "s_waitcnt lgkmcnt(0)\n\t" \
    "s_mov_b32 s70, 116\n\t" \
    "vv_E71_%=:\n\t" \
    "s_memtime s[56:57]\n\t" \
    "s_memrealtime s[2:3]\n\t" \
    "s_nop 0\n\t" \
    "s_memrealtime s[4:5]\n\t" \
    "s_nop 0\n\t" \
    "s_memrealtime s[6:7]\n\t" \
    "s_nop 0\n\t" \
    "s_memrealtime s[8:9]\n\t" \
    "s_nop 0\n\t" \
    "s_memrealtime s[10:11]\n\t" \
    "s_nop 0\n\t" \
    "s_memrealtime s[12:13]\n\t" \
    "s_nop 0\n\t" \
    "s_memrealtime s[14:15]\n\t" \
    "s_nop 0\n\t" \
    "s_memrealtime s[16:17]\n\t" \
    "s_nop 0\n\t" \
    "s_memrealtime s[18:19]\n\t" \
    "s_nop 0\n\t" \
    "s_memrealtime s[20:21]\n\t" \
    "s_nop 0\n\t" \
    "s_memrealtime s[22:23]\n\t" \
    "s_nop 0\n\t" \
    "s_memrealtime s[24:25]\n\t" \
    "s_nop 0\n\t" \
    "s_memrealtime s[26:27]\n\t" \
    "s_nop 0\n\t" \
    "s_memrealtime s[28:29]\n\t" \
    "s_memtime s[54:55]\n\t" \
    "s_add_u32 s70, s70, 8\n\t" \
    "s_waitcnt lgkmcnt(0)\n\t" \
    "s_sub_u32 s69, s54, s56\n\t" \
    "s_cmp_gt_u32 s69, s70\n\t" \
    "s_cbranch_scc1 vv_E71_%=\n\t" \
    "s_add_u32 s2, s2, s4\n\t" \
    "s_add_u32 s6, s6, s8\n\t" \
    "s_add_u32 s10, s10, s12\n\t" \
    "s_add_u32 s14, s14, s16\n\t" \
    "s_add_u32 s18, s18, s20\n\t" \
    "s_add_u32 s22, s22, s24\n\t" \
    "s_add_u32 s2, s2, s6\n\t" \
    "s_add_u32 s10, s10, s14\n\t" \
    "s_add_u32 s18, s18, s22\n\t" \
    "s_add_u32 s2, s2, s26\n\t" \
    "s_add_u32 s10, s10, s18\n\t" \
    "s_add_u32 s2, s2, s10\n\t" \
    "s_mul_i32 s70, s28, 13\n\t" \
    "s_sub_u32 s70, s70, s2\n\t" \
    "s_min_u32 s70, s70, 52\n\t" \
    "s_mul_i32 s69, s69, s70\n\t" \
    "s_mul_hi_u32 s69, s69, 76695844\n\t" \
    "s_mov_b32 s75, s73\n\t" \
    "s_mov_b32 s76, s74\n\t" \
    "s_add_u32 s73, s56, s69\n\t" \
    "s_lshl_b32 s74, s28, 4\n\t" \
    "s_sub_u32 s69, s73, s75\n\t" \
    "s_sub_u32 s70, s74, s76\n\t" \
    "s_sub_u32 s71, s70, 1600\n\t" \
    "s_cmp_lt_u32 s71, 400000\n\t" \
    "s_cbranch_scc0 vv_K72_%=\n\t" \
    "v_cvt_f32_u32 v160, s69\n\t" \
    "v_cvt_f32_u32 v161, s70\n\t" \
    "v_rcp_f32 v162, v160\n\t" \
    "v_rcp_f32 v163, v161\n\t" \
    "v_mul_f32 v162, v162, v161\n\t" \
    "v_mul_f32 v163, v163, v160\n\t" \
    "v_mul_f32 v162, 0x4f800000, v162\n\t" \
    "v_mul_f32 v163, 0x4f000000, v163\n\t" \
    "v_cvt_u32_f32 v162, v162\n\t" \
    "v_cvt_u32_f32 v163, v163\n\t" \
    "v_readfirstlane_b32 s69, v162\n\t" \
    "v_readfirstlane_b32 s70, v163\n\t" \
    "s_nop 1\n\t" \
    "s_min_u32 s71, s65, 1\n\t" \
    "s_sub_u32 s69, s69, s78\n\t" \
    "s_ashr_i32 s69, s69, s71\n\t" \
    "s_add_u32 s78, s78, s69\n\t" \
    "s_sub_u32 s70, s70, s77\n\t" \
    "s_ashr_i32 s70, s70, s71\n\t" \
    "s_add_u32 s77, s77, s70\n\t" \
    "vv_K72_%=:\n\t" \
    "s_add_u32 s65, s65, 1\n\t" \
    "v_mov_b32 v72, 0\n\t" \
    "v_mov_b32 v73, 0\n\t" \
    "v_mov_b32 v74, 0\n\t" \
    "v_mov_b32 v75, 0\n\t" \
    "v_mov_b32 v76, 0\n\t" \
    "v_mov_b32 v77, 0\n\t" \
    "v_mov_b32 v78, 0\n\t" \
    "v_mov_b32 v79, 0\n\t" \
    "v_mov_b32 v108, 0\n\t" \
    "v_mov_b32 v109, 0\n\t" \
    "v_mov_b32 v110, 0\n\t" \
    "v_mov_b32 v111, 0\n\t" \
    "v_mov_b32 v112, 0\n\t" \
    "v_mov_b32 v113, 0\n\t" \
    "v_mov_b32 v114, 0\n\t" \
    "v_mov_b32 v115, 0\n\t" \
    "v_mov_b32 v116, 0\n\t" \
    "v_mov_b32 v117, 0\n\t" \
    "v_mov_b32 v118, 0\n\t" \
    "v_mov_b32 v119, 0\n\t" \
    "v_mov_b32 v120, 0\n\t" \
    "v_mov_b32 v121, 0\n\t" \
    "v_mov_b32 v122, 0\n\t" \
    "v_mov_b32 v123, 0\n\t" \
    "v_mov_b32 v124, 0\n\t" \
    "v_mov_b32 v125, 0\n\t" \
    "v_mov_b32 v126, 0\n\t" \
    "v_mov_b32 v127, 0\n\t" \
    "s_add_u32 s69, s65, 0\n\t" \
    "s_and_b32 s69, s69, 7\n\t" \
    "s_lshl_b32 s69, s69, 18\n\t" \
    "s_add_u32 s69, s69, s62\n\t" \
    "s_add_u32 s46, s36, s69\n\t" \
    "s_addc_u32 s47, s37, 0\n\t" \
    "s_add_u32 s70, s65, 0\n\t" \
    "s_mul_i32 s69, s70, 171\n\t" \
    "s_lshr_b32 s69, s69, 9\n\t" \
    "s_mul_i32 s70, s70, 589824\n\t" \
    "s_mul_i32 s71, s69, 327680\n\t" \
    "s_add_u32 s70, s70, s71\n\t" \
    "s_lshl1_add_u32 s69, s69, 4\n\t" \
    "s_bitcmp1_b64 s[44:45], s69\n\t" \
    "s_cselect_b32 s69, 8192, 0\n\t" \
    "s_add_u32 s69, s69, s70\n\t" \
    "s_add_u32 s69, s69, 123731968\n\t" \
    "s_add_u32 s38, s36, s69\n\t" \
    "s_addc_u32 s39, s37, 0\n\t" \
    "s_and_b32 s69, s69, 0xffe00000\n\t" \
    "s_add_u32 s69, s69, 1823744\n\t" \
    "v_mov_b32 v17, s69\n\t" \
    "global_load_dword v17, v17, s[36:37]\n\t" \
    "s_waitcnt vmcnt(0)\n\t" \
    "v_cmp_eq_u32 vcc, -1, v140\n\t" \
    "s_cbranch_vccz vv_Hok69_%=\n\t" \
    "vv_H68_%=:\n\t" \
    "global_load_dword v140, v20, s[52:53] sc1\n\t" \
    "s_waitcnt vmcnt(0)\n\t" \
    "v_cmp_eq_u32 vcc, -1, v140\n\t" \
    "s_cbranch_vccz vv_Hc70_%=\n\t" \
    "s_cmp_eq_u32 s68, 0\n\t" \
    "s_cbranch_scc1 vv_F73_%=\n\t" \
    "s_sub_u32 s68, s68, 1\n\t" \
    "s_sleep 2\n\t" \
    "s_branch vv_H68_%=\n\t" \
    "vv_F73_%=:\n\t" \
    "v_mov_b32 v141, 5767424\n\t" \
    "v_mov_b32 v142, 6\n\t" \
    "global_store_dword v141, v142, s[36:37] sc1\n\t" \
    "s_waitcnt vmcnt(0)\n\t" \
    "s_branch vv_Hc70_%=\n\t" \
    "vv_Hc70_%=:\n\t" \
    "s_memtime s[56:57]\n\t" \
    "s_waitcnt lgkmcnt(0)\n\t" \
    "s_sub_u32 s69, s56, s73\n\t" \
    "s_cmp_lt_u32 s69, 3000\n\t" \
    "s_cbranch_scc1 vv_St74_%=\n\t" \
    "s_waitcnt lgkmcnt(0)\n\t" \
    "s_mov_b32 s70, 116\n\t" \
    "vv_E75_%=:\n\t" \
    "s_memtime s[56:57]\n\t" \
    "s_memrealtime s[2:3]\n\t" \
    "s_nop 0\n\t" \
    "s_memrealtime s[4:5]\n\t" \
    "s_nop 0\n\t" \
    "s_memrealtime s[6:7]\n\t" \
    "s_nop 0\n\t" \
    "s_memrealtime s[8:9]\n\t" \
    "s_nop 0\n\t" \
    "s_memrealtime s[10:11]\n\t" \
    "s_nop 0\n\t" \
    "s_memrealtime s[12:13]\n\t" \
    "s_nop 0\n\t" \
    "s_memrealtime s[14:15]\n\t" \
    "s_nop 0\n\t" \
    "s_memrealtime s[16:17]\n\t" \
    "s_nop 0\n\t" \
    "s_memrealtime s[18:19]\n\t" \
    "s_nop 0\n\t" \
    "s_memrealtime s[20:21]\n\t" \
    "s_nop 0\n\t" \
    "s_memrealtime s[22:23]\n\t" \
    "s_nop 0\n\t" \
    "s_memrealtime s[24:25]\n\t" \
    "s_nop 0\n\t" \
    "s_memrealtime s[26:27]\n\t" \
    "s_nop 0\n\t" \
    "s_memrealtime s[28:29]\n\t" \
    "s_memtime s[54:55]\n\t" \
    "s_add_u32 s70, s70, 8\n\t" \
    "s_waitcnt lgkmcnt(0)\n\t" \
    "s_sub_u32 s69, s54, s56\n\t" \
    "s_cmp_gt_u32 s69, s70\n\t" \
    "s_cbranch_scc1 vv_E75_%=\n\t" \
    "s_add_u32 s2, s2, s4\n\t" \
    "s_add_u32 s6, s6, s8\n\t" \
    "s_add_u32 s10, s10, s12\n\t" \
    "s_add_u32 s14, s14, s16\n\t" \
    "s_add_u32 s18, s18, s20\n\t" \
    "s_add_u32 s22, s22, s24\n\t" \
    "s_add_u32 s2, s2, s6\n\t" \
    "s_add_u32 s10, s10, s14\n\t" \
    "s_add_u32 s18, s18, s22\n\t" \
    "s_add_u32 s2, s2, s26\n\t" \
    "s_add_u32 s10, s10, s18\n\t" \
    "s_add_u32 s2, s2, s10\n\t" \
    "s_mul_i32 s70, s28, 13\n\t" \
    "s_sub_u32 s70, s70, s2\n\t" \
    "s_min_u32 s70, s70, 52\n\t" \
    "s_mul_i32 s69, s69, s70\n\t" \
    "s_mul_hi_u32 s69, s69, 76695844\n\t" \
    "s_mov_b32 s75, s73\n\t" \
    "s_mov_b32 s76, s74\n\t" \
    "s_add_u32 s73, s56, s69\n\t" \
    "s_lshl_b32 s74, s28, 4\n\t" \
    "vv_St74_%=:\n\t" \
    "vv_Hok69_%=:\n\t" \
    "v_max_u32_dpp v140, v140, v140 row_ror:1 row_mask:0xf bank_mask:0xf\n\t" \
    "s_nop 1\n\t" \
    "v_max_u32_dpp v140, v140, v140 row_ror:2 row_mask:0xf bank_mask:0xf\n\t" \
    "s_nop 1\n\t" \
    "v_max_u32_dpp v140, v140, v140 row_ror:4 row_mask:0xf bank_mask:0xf\n\t" \
    "s_nop 1\n\t" \
    "v_max_u32_dpp v140, v140, v140 row_ror:8 row_mask:0xf bank_mask:0xf\n\t" \
    "s_nop 0\n\t" \
    "v_readlane_b32 s69, v140, 0\n\t" \
    "v_readlane_b32 s71, v140, 16\n\t" \
    "s_nop 1\n\t" \
    "s_max_u32 s81, s69, s71\n\t" \
    "s_mul_hi_u32 s69, s85, s78\n\t" \
    "s_add_u32 s79, s81, s69\n\t" \
    "s_add_u32 s79, s79, s87\n\t" \
    "s_cmp_lt_u32 s65, 6\n\t" \
    "s_cselect_b32 s69, 48, 0\n\t" \
    "s_add_u32 s79, s79, s69\n\t" \
    "s_add_u32 s80, s79, s88\n\t" \
    "s_sub_u32 s70, s79, s74\n\t" \
    "s_max_i32 s70, s70, 0\n\t" \
    "s_min_i32 s70, s70, 16000\n\t" \
    "s_lshl_b32 s70, s70, 1\n\t" \
    "s_mul_hi_u32 s70, s70, s77\n\t" \
    "s_add_u32 s70, s70, s73\n\t" \
    "v_mov_b32 v160, s70\n\t" \
    "ds_write_b32 v11, v160 offset:20480\n\t" \
    "s_and_b32 s69, s65, 7\n\t" \
    "s_lshl_b32 s69, s69, 18\n\t" \
    "s_add_u32 s69, s69, s63\n\t" \
    "s_add_u32 s52, s36, s69\n\t" \
    "s_addc_u32 s53, s37, 0\n\t" \
    "s_waitcnt lgkmcnt(0)\n\t" \
    "s_barrier\n\t" \
    "s_branch vv_loop12_%=\n\t" \
    "vv_Gbad61_%=:\n\t" \
    "s_waitcnt vmcnt(0) lgkmcnt(0)\n\t" \
    "s_nop 7\n\t" \
    "v_mov_b32 v72, 0\n\t" \
    "v_mov_b32 v73, 0\n\t" \
    "v_mov_b32 v74, 0\n\t" \
    "v_mov_b32 v75, 0\n\t" \
    "v_mov_b32 v76, 0\n\t" \
    "v_mov_b32 v77, 0\n\t" \
    "v_mov_b32 v78, 0\n\t" \
    "v_mov_b32 v79, 0\n\t" \
    "s_cmp_eq_u32 s68, 0\n\t" \
    "s_cbranch_scc1 vv_F76_%=\n\t" \
    "s_sub_u32 s68, s68, 1\n\t" \
    "s_branch vv_G60_%=\n\t" \
    "vv_F76_%=:\n\t" \
    "v_mov_b32 v140, 5767424\n\t" \
    "v_mov_b32 v141, 1\n\t" \
    "global_store_dword v140, v141, s[36:37] sc1\n\t" \
    "s_waitcnt vmcnt(0)\n\t" \
    "s_branch vv_Gc62_%=\n\t" \
    "vv_Gslow63_%=:\n\t" \
    "v_max_u32 v140, v36, v37\n\t" \
    "v_max3_u32 v140, v140, v38, v39\n\t" \
    "v_max3_u32 v140, v140, v40, v41\n\t" \
    "v_max3_u32 v140, v140, v42, v43\n\t" \
    "v_max3_u32 v140, v140, v44, v45\n\t" \
    "v_max3_u32 v140, v140, v46, v47\n\t" \
    "v_max3_u32 v140, v140, v48, v49\n\t" \
    "v_max3_u32 v140, v140, v50, v51\n\t" \
    "v_max3_u32 v140, v140, v52, v53\n\t" \
    "v_max3_u32 v140, v140, v54, v55\n\t" \
    "v_max3_u32 v140, v140, v56, v57\n\t" \
    "v_max3_u32 v140, v140, v58, v59\n\t" \
    "v_max3_u32 v140, v140, v60, v61\n\t" \
    "v_max3_u32 v140, v140, v62, v63\n\t" \
    "v_max3_u32 v140, v140, v64, v65\n\t" \
    "v_max3_u32 v140, v140, v66, v67\n\t" \
    "v_max3_u32 v140, v140, v68, v69\n\t" \
    "v_max3_u32 v140, v140, v70, v71\n\t" \
    "v_cmp_eq_u32 vcc, -1, v140\n\t" \
    "s_cbranch_vccz vv_Gback64_%=\n\t" \
    "s_waitcnt lgkmcnt(0)\n\t" \
    "v_sub_u32 v140, v1, v3\n\t" \
    "s_nop 0\n\t" \
    "v_readfirstlane_b32 s69, v140\n\t" \
    "v_add_u32 v143, 20480, v0\n\t" \
    "s_nop 7\n\t" \
    "s_add_u32 s30, s46, s69\n\t" \
    "s_addc_u32 s31, s47, 0\n\t" \
    "vv_Gag77_%=:\n\t" \
    "global_load_dwordx4 v[36:39], v1, s[46:47] sc1\n\t" \
    "global_load_dwordx4 v[40:43], v23, s[46:47] sc1\n\t" \
    "global_load_dwordx4 v[44:47], v143, s[30:31] sc1\n\t" \
    "global_load_dwordx4 v[48:51], v4, s[30:31] sc1\n\t" \
    "global_load_dwordx4 v[52:55], v5, s[30:31] sc1\n\t" \
    "global_load_dwordx4 v[56:59], v6, s[30:31] sc1\n\t" \
    "global_load_dwordx4 v[60:63], v7, s[30:31] sc1\n\t" \
    "global_load_dwordx4 v[64:67], v8, s[30:31] sc1\n\t" \
    "global_load_dwordx4 v[68:71], v9, s[30:31] sc1\n\t" \
    "s_waitcnt vmcnt(0)\n\t" \
    "v_max_u32 v140, v36, v37\n\t" \
    "v_max3_u32 v140, v140, v38, v39\n\t" \
    "v_max3_u32 v140, v140, v40, v41\n\t" \
    "v_max3_u32 v140, v140, v42, v43\n\t" \
    "v_max3_u32 v140, v140, v44, v45\n\t" \
    "v_max3_u32 v140, v140, v46, v47\n\t" \
    "v_max3_u32 v140, v140, v48, v49\n\t" \
    "v_max3_u32 v140, v140, v50, v51\n\t" \
    "v_max3_u32 v140, v140, v52, v53\n\t" \
    "v_max3_u32 v140, v140, v54, v55\n\t" \
    "v_max3_u32 v140, v140, v56, v57\n\t" \
    "v_max3_u32 v140, v140, v58, v59\n\t" \
    "v_max3_u32 v140, v140, v60, v61\n\t" \
    "v_max3_u32 v140, v140, v62, v63\n\t" \
    "v_max3_u32 v140, v140, v64, v65\n\t" \
    "v_max3_u32 v140, v140, v66, v67\n\t" \
    "v_max3_u32 v140, v140, v68, v69\n\t" \
    "v_max3_u32 v140, v140, v70, v71\n\t" \
    "v_cmp_eq_u32 vcc, -1, v140\n\t" \
    "s_cbranch_vccz vv_Gredo78_%=\n\t" \
    "s_cmp_eq_u32 s68, 0\n\t" \
    "s_cbranch_scc1 vv_F79_%=\n\t" \
    "s_sub_u32 s68, s68, 1\n\t" \
    "s_sleep 1\n\t" \
    "s_branch vv_Gag77_%=\n\t" \
    "vv_F79_%=:\n\t" \
    "v_mov_b32 v140, 5767424\n\t" \
    "v_mov_b32 v141, 2\n\t" \
    "global_store_dword v140, v141, s[36:37] sc1\n\t" \
    "s_waitcnt vmcnt(0)\n\t" \
    "s_branch vv_Gredo78_%=\n\t" \
    "vv_Gredo78_%=:\n\t" \
    "v_mov_b32 v72, 0\n\t" \
    "v_mov_b32 v73, 0\n\t" \
    "v_mov_b32 v74, 0\n\t" \
    "v_mov_b32 v75, 0\n\t" \
    "v_mov_b32 v76, 0\n\t" \
    "v_mov_b32 v77, 0\n\t" \
    "v_mov_b32 v78, 0\n\t" \
    "v_mov_b32 v79, 0\n\t" \
    "v_smfmac_f32_16x16x64_bf16 v[72:75], v[36:39], a[48:55], v10\n\t" \
    "v_smfmac_f32_16x16x64_bf16 v[72:75], v[40:43], a[56:63], v10\n\t" \
    "v_smfmac_f32_16x16x64_bf16 v[72:75], v[48:51], a[0:7], v10\n\t" \
    "v_smfmac_f32_16x16x64_bf16 v[72:75], v[52:55], a[8:15], v10\n\t" \
    "v_smfmac_f32_16x16x64_bf16 v[72:75], v[56:59], a[16:23], v10\n\t" \
    "v_smfmac_f32_16x16x64_bf16 v[72:75], v[60:63], a[24:31], v10\n\t" \
    "v_smfmac_f32_16x16x64_bf16 v[72:75], v[64:67], a[32:39], v10\n\t" \
    "v_smfmac_f32_16x16x64_bf16 v[72:75], v[68:71], a[40:47], v10\n\t" \
    "v_smfmac_f32_16x16x64_bf16 v[72:75], v[44:47], a[64:71], v10\n\t" \
    "v_smfmac_f32_16x16x64_bf16 v[76:79], v[36:39], a[120:127], v10\n\t" \
    "v_smfmac_f32_16x16x64_bf16 v[76:79], v[40:43], a[128:135], v10\n\t" \
    "v_smfmac_f32_16x16x64_bf16 v[76:79], v[48:51], a[72:79], v10\n\t" \
    "v_smfmac_f32_16x16x64_bf16 v[76:79], v[52:55], a[80:87], v10\n\t" \
    "v_smfmac_f32_16x16x64_bf16 v[76:79], v[56:59], a[88:95], v10\n\t" \
    "v_smfmac_f32_16x16x64_bf16 v[76:79], v[60:63], a[96:103], v10\n\t" \
    "v_smfmac_f32_16x16x64_bf16 v[76:79], v[64:67], a[104:111], v10\n\t" \
    "v_smfmac_f32_16x16x64_bf16 v[76:79], v[68:71], a[112:119], v10\n\t" \
    "v_smfmac_f32_16x16x64_bf16 v[76:79], v[44:47], a[136:143], v10\n\t" \
    "v_add_f32 v72, v72, v74\n\t" \
    "v_add_f32 v73, v73, v75\n\t" \
    "v_cvt_pk_bf16_f32 v80, v72, v73\n\t" \
    "ds_write_b32 v12, v80 offset:0\n\t" \
    "s_nop 3\n\t" \
    "v_pk_add_f32 v[76:77], v[76:77], v[78:79]\n\t" \
    "v_cvt_pk_bf16_f32 v81, v76, v77\n\t" \
    "ds_write_b32 v12, v81 offset:256\n\t" \
    "s_branch vv_Gback64_%=\n\t" \
    "vv_end56_%=:\n\t" \
    "s_endpgm\n\t" \
    "vv_last_%=:\n\t" \
    ""
#define ASM_R \
    "s_mov_b64 s[36:37], %0\n\t" \
    "s_mov_b64 s[42:43], %1\n\t" \
    "s_mov_b64 s[44:45], %2\n\t" \
    "s_mov_b32 s66, %3\n\t" \
    "s_mov_b32 s67, %4\n\t" \
    "s_mov_b64 s[50:51], %5\n\t" \
    "s_mov_b32 s82, %7\n\t" \
    "s_load_dwordx8 s[84:91], %6, 0\n\t" \
    "v_mbcnt_lo_u32_b32 v223, -1, 0\n\t" \
    "v_mbcnt_hi_u32_b32 v223, -1, v223\n\t" \
    "s_getpc_b64 s[46:47]\n\t" \
    "rr_pc_%=:\n\t" \
    "s_lshl_b32 s69, s66, 13\n\t" \
    "v_lshl_add_u32 v222, v223, 7, s69\n\t" \
    "v_min_u32 v222, rr_last_%=-rr_pc_%=-4, v222\n\t" \
    "global_load_dword v222, v222, s[46:47]\n\t" \
    "v_lshlrev_b32 v223, 2, v223\n\t" \
    "v_add_u32 v223, s82, v223\n\t" \
    "ds_read_b32 v0, v223 offset:0\n\t" \
    "ds_read_b32 v1, v223 offset:256\n\t" \
    "ds_read_b32 v2, v223 offset:512\n\t" \
    "ds_read_b32 v3, v223 offset:768\n\t" \
    "ds_read_b32 v4, v223 offset:1024\n\t" \
    "ds_read_b32 v5, v223 offset:1280\n\t" \
    "ds_read_b32 v6, v223 offset:1536\n\t" \
    "ds_read_b32 v7, v223 offset:1792\n\t" \
    "ds_read_b32 v8, v223 offset:2048\n\t" \
    "ds_read_b32 v9, v223 offset:2304\n\t" \
    "ds_read_b32 v10, v223 offset:2560\n\t" \
    "ds_read_b32 v11, v223 offset:2816\n\t" \
    "s_waitcnt lgkmcnt(0)\n\t" \
    "ds_read_b32 v12, v223 offset:3072\n\t" \
    "ds_read_b32 v13, v223 offset:3328\n\t" \
    "ds_read_b32 v14, v223 offset:3584\n\t" \
    "ds_read_b32 v15, v223 offset:3840\n\t" \
    "ds_read_b32 v16, v223 offset:4096\n\t" \
    "ds_read_b32 v17, v223 offset:4352\n\t" \
    "ds_read_b32 v18, v223 offset:4608\n\t" \
    "ds_read_b32 v19, v223 offset:4864\n\t" \
    "ds_read_b32 v20, v223 offset:5120\n\t" \
    "ds_read_b32 v21, v223 offset:5376\n\t" \
    "ds_read_b32 v22, v223 offset:5632\n\t" \
    "ds_read_b32 v23, v223 offset:5888\n\t" \
    "s_waitcnt lgkmcnt(0)\n\t" \
    "ds_read_b32 v24, v223 offset:6144\n\t" \
    "ds_read_b32 v25, v223 offset:6400\n\t" \
    "ds_read_b32 v26, v223 offset:6656\n\t" \
    "ds_read_b32 v27, v223 offset:6912\n\t" \
    "ds_read_b32 v28, v223 offset:7168\n\t" \
    "ds_read_b32 v29, v223 offset:7424\n\t" \
    "ds_read_b32 v30, v223 offset:7680\n\t" \
    "ds_read_b32 v31, v223 offset:7936\n\t" \
    "ds_read_b32 v32, v223 offset:8192\n\t" \
    "ds_read_b32 v33, v223 offset:8448\n\t" \
    "ds_read_b32 v34, v223 offset:8704\n\t" \
    "ds_read_b32 v35, v223 offset:8960\n\t" \
    "s_waitcnt lgkmcnt(0)\n\t" \
    "ds_read_b32 v36, v223 offset:9216\n\t" \
    "ds_read_b32 v37, v223 offset:9472\n\t" \
    "ds_read_b32 v38, v223 offset:9728\n\t" \
    "ds_read_b32 v39, v223 offset:9984\n\t" \
    "ds_read_b32 v40, v223 offset:10240\n\t" \
    "ds_read_b32 v41, v223 offset:10496\n\t" \
    "ds_read_b32 v42, v223 offset:10752\n\t" \
    "ds_read_b32 v43, v223 offset:11008\n\t" \
    "ds_read_b32 v44, v223 offset:11264\n\t" \
    "ds_read_b32 v45, v223 offset:11520\n\t" \
    "ds_read_b32 v183, v223 offset:11776\n\t" \
    "ds_read_b32 v184, v223 offset:12032\n\t" \
    "s_waitcnt lgkmcnt(0)\n\t" \
    "ds_read_b32 v185, v223 offset:12288\n\t" \
    "ds_read_b32 v186, v223 offset:12544\n\t" \
    "ds_read_b32 v187, v223 offset:12800\n\t" \
    "ds_read_b32 v188, v223 offset:13056\n\t" \
    "s_waitcnt lgkmcnt(0)\n\t" \
    "s_mov_b32 s68, 262144\n\t" \
    "s_mov_b32 s72, 0\n\t" \
    "s_mov_b32 s83, -1\n\t" \
    "s_mov_b32 s65, 0\n\t" \
    "v_xor_b32 v180, 65536, v2\n\t" \
    "v_xor_b32 v181, 131072, v2\n\t" \
    "v_xor_b32 v182, 196608, v2\n\t" \
    "v_xor_b32 v204, 0x204000, v180\n\t" \
    "v_xor_b32 v205, 0x204000, v181\n\t" \
    "v_xor_b32 v206, 0x204000, v182\n\t" \
    "v_xor_b32 v207, 0x204000, v2\n\t" \
    "v_add_u32 v189, 4096, v7\n\t" \
    "v_add_u32 v190, 16384, v7\n\t" \
    "v_add_u32 v191, 20480, v7\n\t" \
    "s_mul_i32 s69, s67, 57\n\t" \
    "s_lshr_b32 s69, s69, 9\n\t" \
    "s_mul_i32 s69, s69, 9\n\t" \
    "s_sub_u32 s69, s67, s69\n\t" \
    "s_cmp_lt_u32 s69, 4\n\t" \
    "s_cselect_b32 s70, 21, 16\n\t" \
    "s_cmp_eq_u32 s69, 8\n\t" \
    "s_cselect_b32 s70, 26, s70\n\t" \
    "s_lshr_b32 s97, s91, s70\n\t" \
    "s_and_b32 s97, s97, 31\n\t" \
    "s_min_u32 s97, s97, 0\n\t" \
    "s_lshl_b32 s97, s97, 4\n\t" \
    "s_mov_b32 s77, 3019898880\n\t" \
    "s_mov_b32 s78, 3054198966\n\t" \
    "s_waitcnt lgkmcnt(0)\n\t" \
    "s_mov_b32 s70, 116\n\t" \
    "rr_E1_%=:\n\t" \
    "s_memtime s[56:57]\n\t" \
    "s_memrealtime s[2:3]\n\t" \
    "s_nop 0\n\t" \
    "s_memrealtime s[4:5]\n\t" \
    "s_nop 0\n\t" \
    "s_memrealtime s[6:7]\n\t" \
    "s_nop 0\n\t" \
    "s_memrealtime s[8:9]\n\t" \
    "s_nop 0\n\t" \
    "s_memrealtime s[10:11]\n\t" \
    "s_nop 0\n\t" \
    "s_memrealtime s[12:13]\n\t" \
    "s_nop 0\n\t" \
    "s_memrealtime s[14:15]\n\t" \
    "s_nop 0\n\t" \
    "s_memrealtime s[16:17]\n\t" \
    "s_nop 0\n\t" \
    "s_memrealtime s[18:19]\n\t" \
    "s_nop 0\n\t" \
    "s_memrealtime s[20:21]\n\t" \
    "s_nop 0\n\t" \
    "s_memrealtime s[22:23]\n\t" \
    "s_nop 0\n\t" \
    "s_memrealtime s[24:25]\n\t" \
    "s_nop 0\n\t" \
    "s_memrealtime s[26:27]\n\t" \
    "s_nop 0\n\t" \
    "s_memrealtime s[28:29]\n\t" \
    "s_memtime s[54:55]\n\t" \
    "s_add_u32 s70, s70, 8\n\t" \
    "s_waitcnt lgkmcnt(0)\n\t" \
    "s_sub_u32 s69, s54, s56\n\t" \
    "s_cmp_gt_u32 s69, s70\n\t" \
    "s_cbranch_scc1 rr_E1_%=\n\t" \
    "s_add_u32 s2, s2, s4\n\t" \
    "s_add_u32 s6, s6, s8\n\t" \
    "s_add_u32 s10, s10, s12\n\t" \
    "s_add_u32 s14, s14, s16\n\t" \
    "s_add_u32 s18, s18, s20\n\t" \
    "s_add_u32 s22, s22, s24\n\t" \
    "s_add_u32 s2, s2, s6\n\t" \
    "s_add_u32 s10, s10, s14\n\t" \
    "s_add_u32 s18, s18, s22\n\t" \
    "s_add_u32 s2, s2, s26\n\t" \
    "s_add_u32 s10, s10, s18\n\t" \
    "s_add_u32 s2, s2, s10\n\t" \
    "s_mul_i32 s70, s28, 13\n\t" \
    "s_sub_u32 s70, s70, s2\n\t" \
    "s_min_u32 s70, s70, 52\n\t" \
    "s_mul_i32 s69, s69, s70\n\t" \
    "s_mul_hi_u32 s69, s69, 76695844\n\t" \
    "s_mov_b32 s75, s73\n\t" \
    "s_mov_b32 s76, s74\n\t" \
    "s_add_u32 s73, s56, s69\n\t" \
    "s_lshl_b32 s74, s28, 4\n\t" \
    "v_mov_b32 v172, 0xd800\n\t" \
    "v_mov_b32 v173, 0x20d800\n\t" \
    "global_load_dword v172, v172, s[36:37]\n\t" \
    "global_load_dword v173, v173, s[36:37]\n\t" \
    "s_mov_b32 s62, 0\n\t" \
    "s_mov_b32 s63, 0x200000\n\t" \
    "s_mov_b64 s[44:45], 0\n\t" \
    "v_mov_b32 v170, 0\n\t" \
    "s_mov_b32 s65, s66\n\t" \
    "s_lshl_b32 s58, s67, 14\n\t" \
    "s_sub_u32 s80, s74, 32000\n\t" \
    "s_mov_b32 s64, 1\n\t" \
    "s_mov_b32 s65, 5\n\t" \
    "s_mov_b32 s83, 99\n\t" \
    "s_add_u32 s70, s65, 59\n\t" \
    "s_mul_i32 s69, s70, 171\n\t" \
    "s_lshr_b32 s69, s69, 9\n\t" \
    "s_mul_i32 s70, s70, 589824\n\t" \
    "s_mul_i32 s71, s69, 327680\n\t" \
    "s_add_u32 s70, s70, s71\n\t" \
    "s_lshl1_add_u32 s69, s69, 4\n\t" \
    "s_bitcmp1_b64 s[44:45], s69\n\t" \
    "s_cselect_b32 s69, 8192, 0\n\t" \
    "s_add_u32 s69, s69, s70\n\t" \
    "s_add_u32 s69, s69, 123731968\n\t" \
    "s_add_u32 s69, s69, s58\n\t" \
    "s_add_u32 s40, s36, s69\n\t" \
    "s_addc_u32 s41, s37, 0\n\t" \
    "v_mov_b32 v192, 0\n\t" \
    "s_mov_b64 exec, 1\n\t" \
    "s_branch rr_act3_%=\n\t" \
    "rr_g214_%=:\n\t" \
    "s_mov_b64 exec, -1\n\t" \
    "s_waitcnt vmcnt(0)\n\t" \
    "v_mbcnt_lo_u32_b32 v164, -1, 0\n\t" \
    "v_mbcnt_hi_u32_b32 v164, -1, v164\n\t" \
    "v_lshlrev_b32 v164, 6, v164\n\t" \
    "v_add_u32 v164, 6225920, v164\n\t" \
    "v_mov_b32 v165, 6234112\n\t" \
    "global_load_dword v165, v165, s[36:37] sc1\n\t" \
    "global_load_dwordx2 v[166:167], v164, s[36:37] sc1\n\t" \
    "global_load_dword v168, v164, s[36:37] offset:2048 sc1\n\t" \
    "s_waitcnt vmcnt(0)\n\t" \
    "rr_C15_%=:\n\t" \
    "v_cmp_ne_u32 s[54:55], v166, v168\n\t" \
    "v_sub_u32 v166, v166, v165\n\t" \
    "v_cmp_ne_u32 vcc, 1, v166\n\t" \
    "s_nop 1\n\t" \
    "s_or_b64 vcc, vcc, s[54:55]\n\t" \
    "s_cbranch_scc0 rr_Cok16_%=\n\t" \
    "s_cmp_eq_u32 s68, 0\n\t" \
    "s_cbranch_scc1 rr_F18_%=\n\t" \
    "s_sub_u32 s68, s68, 1\n\t" \
    "s_sleep 6\n\t" \
    "s_branch rr_Cag17_%=\n\t" \
    "rr_F18_%=:\n\t" \
    "v_mov_b32 v168, 5767424\n\t" \
    "v_mov_b32 v169, 4\n\t" \
    "global_store_dword v168, v169, s[36:37] sc1\n\t" \
    "s_waitcnt vmcnt(0)\n\t" \
    "s_branch rr_Cok16_%=\n\t" \
    "rr_Cag17_%=:\n\t" \
    "global_load_dwordx2 v[166:167], v164, s[36:37] sc1\n\t" \
    "global_load_dword v168, v164, s[36:37] offset:2048 sc1\n\t" \
    "s_waitcnt vmcnt(0)\n\t" \
    "s_branch rr_C15_%=\n\t" \
    "rr_Cok16_%=:\n\t" \
    "v_readlane_b32 s69, v167, 2\n\t" \
    "v_readlane_b32 s70, v167, 3\n\t" \
    "s_nop 1\n\t" \
    "s_cmp_lt_u32 s70, s69\n\t" \
    "s_cselect_b32 s69, 0, 8192\n\t" \
    "s_add_u32 s100, s36, s69\n\t" \
    "s_addc_u32 s101, s37, 0\n\t" \
    "s_cmp_lg_u32 s66, 0\n\t" \
    "s_cbranch_scc1 rr_Ca19_%=\n\t" \
    "v_mov_b32 v164, 5768192\n\t" \
    "s_mov_b64 exec, 1\n\t" \
    "global_atomic_add v164, v166, s[36:37] sc1\n\t" \
    "s_mov_b64 exec, -1\n\t" \
    "rr_Ca19_%=:\n\t" \
    "v_mov_b32_dpp v164, v167 quad_perm:[1,0,3,2] row_mask:0xf bank_mask:0xf\n\t" \
    "v_cmp_lt_u32 s[44:45], v164, v167\n\t" \
    "s_nop 1\n\t" \
    "s_bitcmp1_b32 s44, 0\n\t" \
    "s_cselect_b32 s62, 8192, 0\n\t" \
    "s_bitcmp1_b32 s44, 2\n\t" \
    "s_cselect_b32 s63, 8192, 0\n\t" \
    "s_or_b32 s63, s63, 0x200000\n\t" \
    "v_min_u32 v164, v164, v167\n\t" \
    "v_cmp_lt_u32 vcc, 660, v164\n\t" \
    "s_nop 1\n\t" \
    "s_bcnt1_i32_b64 s69, vcc\n\t" \
    "s_cmp_gt_u32 s69, 16\n\t" \
    "s_cselect_b32 s69, 1, 0\n\t" \
    "s_mul_i32 s70, s69, 160\n\t" \
    "s_add_u32 s86, s86, s70\n\t" \
    "s_mul_i32 s70, s69, 536\n\t" \
    "s_add_u32 s87, s87, s70\n\t" \
    "s_mul_i32 s70, s69, 256\n\t" \
    "s_add_u32 s88, s88, s70\n\t" \
    "v_mov_b32 v172, -1\n\t" \
    "v_mov_b32 v173, -1\n\t" \
    "v_mov_b32 v174, -1\n\t" \
    "v_mov_b32 v175, -1\n\t" \
    "v_mov_b32 v176, -1\n\t" \
    "s_sub_u32 s69, s63, s62\n\t" \
    "s_sub_u32 s69, s69, 0x200000\n\t" \
    "v_bfe_u32 v164, v186, 21, 1\n\t" \
    "v_mad_i32_i24 v186, v164, s69, v186\n\t" \
    "v_bfe_u32 v164, v187, 21, 1\n\t" \
    "v_mad_i32_i24 v187, v164, s69, v187\n\t" \
    "v_bfe_u32 v164, v188, 21, 1\n\t" \
    "v_mad_i32_i24 v188, v164, s69, v188\n\t" \
    "v_add_u32 v204, s69, v204\n\t" \
    "v_add_u32 v205, s69, v205\n\t" \
    "v_add_u32 v206, s69, v206\n\t" \
    "v_add_u32 v207, s69, v207\n\t" \
    "s_mov_b32 s64, 0\n\t" \
    "s_mov_b32 s65, s66\n\t" \
    "s_mov_b32 s83, 1\n\t" \
    "s_mov_b32 s77, 3154116608\n\t" \
    "rr_pf9_%=:\n\t" \
    "s_cmp_gt_u32 s65, 8\n\t" \
    "s_cselect_b32 s83, 99, s83\n\t" \
    "v_cmp_gt_i32 vcc, s65, v176\n\t" \
    "s_nop 1\n\t" \
    "s_max_i32 s69, s83, s65\n\t" \
    "s_cmp_eq_u64 vcc, 0\n\t" \
    "s_cselect_b32 s83, s69, s83\n\t" \
    "s_lshl_b32 s69, s65, 11\n\t" \
    "s_add_u32 s69, s69, 5795840\n\t" \
    "v_and_b32 v169, 0x7c0, v7\n\t" \
    "v_add_u32 v169, s69, v169\n\t" \
    "s_cmp_lg_u32 s65, 2\n\t" \
    "s_cbranch_scc1 rr_n2s20_%=\n\t" \
    "s_sleep 10\n\t" \
    "rr_n2s20_%=:\n\t" \
    "rr_WF21_%=:\n\t" \
    "s_cmp_ge_i32 s83, s65\n\t" \
    "s_cbranch_scc1 rr_WFok22_%=\n\t" \
    "global_load_dword v164, v169, s[36:37] sc1\n\t" \
    "s_waitcnt vmcnt(0)\n\t" \
    "v_min_i32_dpp v164, v164, v164 row_ror:1 row_mask:0xf bank_mask:0xf\n\t" \
    "s_nop 1\n\t" \
    "v_min_i32_dpp v164, v164, v164 row_ror:2 row_mask:0xf bank_mask:0xf\n\t" \
    "s_nop 1\n\t" \
    "v_min_i32_dpp v164, v164, v164 row_ror:4 row_mask:0xf bank_mask:0xf\n\t" \
    "s_nop 1\n\t" \
    "v_min_i32_dpp v164, v164, v164 row_ror:8 row_mask:0xf bank_mask:0xf\n\t" \
    "s_nop 0\n\t" \
    "v_readlane_b32 s69, v164, 0\n\t" \
    "v_readlane_b32 s70, v164, 16\n\t" \
    "s_nop 1\n\t" \
    "s_min_i32 s83, s69, s70\n\t" \
    "s_cmp_ge_i32 s83, s65\n\t" \
    "s_cbranch_scc1 rr_WFok22_%=\n\t" \
    "s_cmp_eq_u32 s68, 0\n\t" \
    "s_cbranch_scc1 rr_F23_%=\n\t" \
    "s_sub_u32 s68, s68, 1\n\t" \
    "s_sleep 2\n\t" \
    "s_branch rr_WF21_%=\n\t" \
    "rr_F23_%=:\n\t" \
    "v_mov_b32 v164, 5767424\n\t" \
    "v_mov_b32 v165, 10\n\t" \
    "global_store_dword v164, v165, s[36:37] sc1\n\t" \
    "s_waitcnt vmcnt(0)\n\t" \
    "s_branch rr_WFok22_%=\n\t" \
    "rr_WFok22_%=:\n\t" \
    "s_mul_i32 s70, s67, 7\n\t" \
    "s_add_u32 s69, s65, 0\n\t" \
    "s_xor_b32 s69, s69, s70\n\t" \
    "s_and_b32 s69, s69, 63\n\t" \
    "s_lshl_b32 s69, s69, 15\n\t" \
    "s_mul_i32 s70, s67, 2097152\n\t" \
    "s_add_u32 s69, s69, s70\n\t" \
    "s_add_u32 s69, s69, 48234496\n\t" \
    "s_add_u32 s38, s36, s69\n\t" \
    "s_addc_u32 s39, s37, 0\n\t" \
    "s_add_u32 s70, s65, 0\n\t" \
    "s_mul_i32 s69, s70, 171\n\t" \
    "s_lshr_b32 s69, s69, 9\n\t" \
    "s_mul_i32 s70, s70, 589824\n\t" \
    "s_mul_i32 s71, s69, 327680\n\t" \
    "s_add_u32 s70, s70, s71\n\t" \
    "s_lshl1_add_u32 s69, s69, 4\n\t" \
    "s_bitcmp1_b64 s[44:45], s69\n\t" \
    "s_cselect_b32 s69, 8192, 0\n\t" \
    "s_add_u32 s69, s69, s70\n\t" \
    "s_add_u32 s69, s69, 123731968\n\t" \
    "s_add_u32 s69, s69, s58\n\t" \
    "s_add_u32 s40, s36, s69\n\t" \
    "s_addc_u32 s41, s37, 0\n\t" \
    "s_and_b32 s69, s69, 0xffe00000\n\t" \
    "s_add_u32 s69, s69, 1823744\n\t" \
    "v_mov_b32 v168, s69\n\t" \
    "global_load_dword v168, v168, s[36:37]\n\t" \
    "s_cmp_gt_u32 s65, 61\n\t" \
    "s_cbranch_scc1 rr_idle2_%=\n\t" \
    "s_add_u32 s69, s65, 2\n\t" \
    "s_and_b32 s69, s69, 7\n\t" \
    "s_lshl_b32 s69, s69, 18\n\t" \
    "s_add_u32 s69, s69, s62\n\t" \
    "s_add_u32 s46, s36, s69\n\t" \
    "s_addc_u32 s47, s37, 0\n\t" \
    "global_store_dwordx4 v183, v[172:175], s[46:47] sc1\n\t" \
    "global_store_dwordx4 v184, v[172:175], s[46:47] sc1\n\t" \
    "global_store_dwordx4 v185, v[172:175], s[46:47] sc1\n\t" \
    "global_store_dwordx4 v186, v[172:175], s[46:47] sc1\n\t" \
    "global_store_dwordx4 v187, v[172:175], s[46:47] sc1\n\t" \
    "rr_idle2_%=:\n\t" \
    "v_mov_b32 v200, 0\n\t" \
    "s_cmp_lg_u32 s67, 35\n\t" \
    "s_cbranch_scc1 rr_cke24_%=\n\t" \
    "s_cmp_lt_u32 s65, 7\n\t" \
    "s_cbranch_scc1 rr_cke24_%=\n\t" \
    "s_lshl_b32 s69, s65, 7\n\t" \
    "s_add_u32 s69, s69, 5775360\n\t" \
    "v_mov_b32 v201, s69\n\t" \
    "global_load_dword v200, v201, s[36:37] sc1\n\t" \
    "rr_cke24_%=:\n\t" \
    "s_cmp_gt_u32 s65, 62\n\t" \
    "s_cbranch_scc1 rr_n225_%=\n\t" \
    "s_cmp_lt_u32 s65, 2\n\t" \
    "s_cbranch_scc1 rr_nw226_%=\n\t" \
    "s_mul_hi_u32 s69, s85, s78\n\t" \
    "s_add_u32 s79, s81, s69\n\t" \
    "s_add_u32 s79, s79, s87\n\t" \
    "s_cmp_lt_u32 s65, 7\n\t" \
    "s_cselect_b32 s69, 48, 0\n\t" \
    "s_add_u32 s79, s79, s69\n\t" \
    "s_add_u32 s80, s79, s88\n\t" \
    "s_cmp_gt_u32 s65, 4\n\t" \
    "s_cselect_b32 s71, 240, 64\n\t" \
    "s_add_u32 s71, s71, s79\n\t" \
    "s_sub_u32 s70, s71, s74\n\t" \
    "s_max_i32 s70, s70, 0\n\t" \
    "s_min_i32 s70, s70, 16000\n\t" \
    "s_lshl_b32 s70, s70, 1\n\t" \
    "s_mul_hi_u32 s70, s70, s77\n\t" \
    "s_add_u32 s70, s70, s73\n\t" \
    "rr_W27_%=:\n\t" \
    "s_memtime s[56:57]\n\t" \
    "s_waitcnt lgkmcnt(0)\n\t" \
    "s_sub_u32 s69, s70, s56\n\t" \
    "s_cmp_gt_i32 s69, 24\n\t" \
    "s_cbranch_scc1 rr_W27_%=\n\t" \
    "s_branch rr_g113_%=\n\t" \
    "rr_nw226_%=:\n\t" \
    "s_mov_b32 s96, 0xaaaaaaaa\n\t" \
    "v_mov_b32 v166, -1\n\t" \
    "v_readfirstlane_b32 s71, v7\n\t" \
    "s_nop 1\n\t" \
    "s_lshr_b32 s71, s71, 4\n\t" \
    "s_add_u32 s71, s71, 5783552\n\t" \
    "rr_g012_%=:\n\t" \
    "s_memrealtime s[54:55]\n\t" \
    "s_waitcnt lgkmcnt(0)\n\t" \
    "s_bfe_u32 s69, s54, 0x60004\n\t" \
    "s_lshl_b32 s69, s69, 6\n\t" \
    "s_add_u32 s69, s69, s71\n\t" \
    "v_mov_b32 v165, s69\n\t" \
    "global_load_dword v169, v165, s[36:37] sc1\n\t" \
    "s_and_b32 s69, s68, 63\n\t" \
    "s_cbranch_scc1 rr_gf28_%=\n\t" \
    "global_load_dword v166, v6, s[100:101] sc1\n\t" \
    "rr_gf28_%=:\n\t" \
    "s_waitcnt vmcnt(0)\n\t" \
    "v_cmp_ne_u32 s[30:31], -1, v169\n\t" \
    "v_cmp_le_i32 vcc, s65, v166\n\t" \
    "s_nop 1\n\t" \
    "s_lshr_b32 s69, vcc_lo, 1\n\t" \
    "s_and_b32 s69, s69, vcc_lo\n\t" \
    "s_or_b32 s69, s69, s30\n\t" \
    "s_andn2_b32 s69, s69, s96\n\t" \
    "s_cbranch_scc0 rr_gs29_%=\n\t" \
    "s_or_b32 s96, s96, s69\n\t" \
    "s_cmp_eq_u32 s66, 0\n\t" \
    "s_cbranch_scc1 rr_w030_%=\n\t" \
    "s_sleep 8\n\t" \
    "rr_w030_%=:\n\t" \
    "s_bitcmp0_b32 s69, 0\n\t" \
    "s_cbranch_scc1 rr_gk31_%=\n\t" \
    "global_load_dwordx4 a[0:3], v26, s[38:39] offset:0 sc1\n\t" \
    "global_load_dwordx4 a[4:7], v26, s[38:39] offset:1024 sc1\n\t" \
    "rr_gk31_%=:\n\t" \
    "s_bitcmp0_b32 s69, 2\n\t" \
    "s_cbranch_scc1 rr_gk32_%=\n\t" \
    "global_load_dwordx4 a[8:11], v27, s[38:39] offset:0 sc1\n\t" \
    "global_load_dwordx4 a[12:15], v27, s[38:39] offset:1024 sc1\n\t" \
    "rr_gk32_%=:\n\t" \
    "s_bitcmp0_b32 s69, 4\n\t" \
    "s_cbranch_scc1 rr_gk33_%=\n\t" \
    "global_load_dwordx4 a[16:19], v28, s[38:39] offset:0 sc1\n\t" \
    "global_load_dwordx4 a[20:23], v28, s[38:39] offset:1024 sc1\n\t" \
    "rr_gk33_%=:\n\t" \
    "s_bitcmp0_b32 s69, 6\n\t" \
    "s_cbranch_scc1 rr_gk34_%=\n\t" \
    "global_load_dwordx4 a[24:27], v29, s[38:39] offset:0 sc1\n\t" \
    "global_load_dwordx4 a[28:31], v29, s[38:39] offset:1024 sc1\n\t" \
    "rr_gk34_%=:\n\t" \
    "s_bitcmp0_b32 s69, 8\n\t" \
    "s_cbranch_scc1 rr_gk35_%=\n\t" \
    "global_load_dwordx4 a[32:35], v30, s[38:39] offset:0 sc1\n\t" \
    "global_load_dwordx4 a[36:39], v30, s[38:39] offset:1024 sc1\n\t" \
    "rr_gk35_%=:\n\t" \
    "s_bitcmp0_b32 s69, 10\n\t" \
    "s_cbranch_scc1 rr_gk36_%=\n\t" \
    "global_load_dwordx4 a[40:43], v31, s[38:39] offset:0 sc1\n\t" \
    "global_load_dwordx4 a[44:47], v31, s[38:39] offset:1024 sc1\n\t" \
    "rr_gk36_%=:\n\t" \
    "s_bitcmp0_b32 s69, 12\n\t" \
    "s_cbranch_scc1 rr_gk37_%=\n\t" \
    "global_load_dwordx4 a[48:51], v32, s[38:39] offset:0 sc1\n\t" \
    "global_load_dwordx4 a[52:55], v32, s[38:39] offset:1024 sc1\n\t" \
    "rr_gk37_%=:\n\t" \
    "s_bitcmp0_b32 s69, 14\n\t" \
    "s_cbranch_scc1 rr_gk38_%=\n\t" \
    "global_load_dwordx4 a[56:59], v33, s[38:39] offset:0 sc1\n\t" \
    "global_load_dwordx4 a[60:63], v33, s[38:39] offset:1024 sc1\n\t" \
    "rr_gk38_%=:\n\t" \
    "s_bitcmp0_b32 s69, 16\n\t" \
    "s_cbranch_scc1 rr_gk39_%=\n\t" \
    "global_load_dwordx4 a[64:67], v34, s[38:39] offset:0 sc1\n\t" \
    "global_load_dwordx4 a[68:71], v34, s[38:39] offset:1024 sc1\n\t" \
    "rr_gk39_%=:\n\t" \
    "s_bitcmp0_b32 s69, 18\n\t" \
    "s_cbranch_scc1 rr_gk40_%=\n\t" \
    "global_load_dwordx4 a[72:75], v35, s[38:39] offset:0 sc1\n\t" \
    "global_load_dwordx4 a[76:79], v35, s[38:39] offset:1024 sc1\n\t" \
    "rr_gk40_%=:\n\t" \
    "s_bitcmp0_b32 s69, 20\n\t" \
    "s_cbranch_scc1 rr_gk41_%=\n\t" \
    "global_load_dwordx4 a[80:83], v36, s[38:39] offset:0 sc1\n\t" \
    "global_load_dwordx4 a[84:87], v36, s[38:39] offset:1024 sc1\n\t" \
    "rr_gk41_%=:\n\t" \
    "s_bitcmp0_b32 s69, 22\n\t" \
    "s_cbranch_scc1 rr_gk42_%=\n\t" \
    "global_load_dwordx4 a[88:91], v37, s[38:39] offset:0 sc1\n\t" \
    "global_load_dwordx4 a[92:95], v37, s[38:39] offset:1024 sc1\n\t" \
    "rr_gk42_%=:\n\t" \
    "s_bitcmp0_b32 s69, 24\n\t" \
    "s_cbranch_scc1 rr_gk43_%=\n\t" \
    "global_load_dwordx4 a[96:99], v38, s[38:39] offset:0 sc1\n\t" \
    "global_load_dwordx4 a[100:103], v38, s[38:39] offset:1024 sc1\n\t" \
    "rr_gk43_%=:\n\t" \
    "s_bitcmp0_b32 s69, 26\n\t" \
    "s_cbranch_scc1 rr_gk44_%=\n\t" \
    "global_load_dwordx4 a[104:107], v39, s[38:39] offset:0 sc1\n\t" \
    "global_load_dwordx4 a[108:111], v39, s[38:39] offset:1024 sc1\n\t" \
    "rr_gk44_%=:\n\t" \
    "s_bitcmp0_b32 s69, 28\n\t" \
    "s_cbranch_scc1 rr_gk45_%=\n\t" \
    "global_load_dwordx4 a[112:115], v40, s[38:39] offset:0 sc1\n\t" \
    "global_load_dwordx4 a[116:119], v40, s[38:39] offset:1024 sc1\n\t" \
    "rr_gk45_%=:\n\t" \
    "s_bitcmp0_b32 s69, 30\n\t" \
    "s_cbranch_scc1 rr_gk46_%=\n\t" \
    "global_load_dwordx4 a[120:123], v41, s[38:39] offset:0 sc1\n\t" \
    "global_load_dwordx4 a[124:127], v41, s[38:39] offset:1024 sc1\n\t" \
    "rr_gk46_%=:\n\t" \
    "s_cmp_eq_u32 s96, -1\n\t" \
    "s_cbranch_scc1 rr_n225_%=\n\t" \
    "rr_gs29_%=:\n\t" \
    "s_cmp_eq_u32 s68, 0\n\t" \
    "s_cbranch_scc1 rr_F47_%=\n\t" \
    "s_sub_u32 s68, s68, 1\n\t" \
    "s_sleep 1\n\t" \
    "s_branch rr_g012_%=\n\t" \
    "rr_F47_%=:\n\t" \
    "v_mov_b32 v167, 5767424\n\t" \
    "v_mov_b32 v168, 16\n\t" \
    "global_store_dword v167, v168, s[36:37] sc1\n\t" \
    "s_waitcnt vmcnt(0)\n\t" \
    "s_branch rr_n225_%=\n\t" \
    "rr_g113_%=:\n\t" \
    "global_load_dwordx4 a[0:3], v26, s[38:39] offset:0 sc1\n\t" \
    "global_load_dwordx4 a[4:7], v26, s[38:39] offset:1024 sc1\n\t" \
    "global_load_dwordx4 a[8:11], v27, s[38:39] offset:0 sc1\n\t" \
    "global_load_dwordx4 a[12:15], v27, s[38:39] offset:1024 sc1\n\t" \
    "global_load_dwordx4 a[16:19], v28, s[38:39] offset:0 sc1\n\t" \
    "global_load_dwordx4 a[20:23], v28, s[38:39] offset:1024 sc1\n\t" \
    "global_load_dwordx4 a[24:27], v29, s[38:39] offset:0 sc1\n\t" \
    "global_load_dwordx4 a[28:31], v29, s[38:39] offset:1024 sc1\n\t" \
    "global_load_dwordx4 a[32:35], v30, s[38:39] offset:0 sc1\n\t" \
    "global_load_dwordx4 a[36:39], v30, s[38:39] offset:1024 sc1\n\t" \
    "global_load_dwordx4 a[40:43], v31, s[38:39] offset:0 sc1\n\t" \
    "global_load_dwordx4 a[44:47], v31, s[38:39] offset:1024 sc1\n\t" \
    "global_load_dwordx4 a[48:51], v32, s[38:39] offset:0 sc1\n\t" \
    "global_load_dwordx4 a[52:55], v32, s[38:39] offset:1024 sc1\n\t" \
    "global_load_dwordx4 a[56:59], v33, s[38:39] offset:0 sc1\n\t" \
    "global_load_dwordx4 a[60:63], v33, s[38:39] offset:1024 sc1\n\t" \
    "global_load_dwordx4 a[64:67], v34, s[38:39] offset:0 sc1\n\t" \
    "global_load_dwordx4 a[68:71], v34, s[38:39] offset:1024 sc1\n\t" \
    "global_load_dwordx4 a[72:75], v35, s[38:39] offset:0 sc1\n\t" \
    "global_load_dwordx4 a[76:79], v35, s[38:39] offset:1024 sc1\n\t" \
    "global_load_dwordx4 a[80:83], v36, s[38:39] offset:0 sc1\n\t" \
    "global_load_dwordx4 a[84:87], v36, s[38:39] offset:1024 sc1\n\t" \
    "global_load_dwordx4 a[88:91], v37, s[38:39] offset:0 sc1\n\t" \
    "global_load_dwordx4 a[92:95], v37, s[38:39] offset:1024 sc1\n\t" \
    "global_load_dwordx4 a[96:99], v38, s[38:39] offset:0 sc1\n\t" \
    "global_load_dwordx4 a[100:103], v38, s[38:39] offset:1024 sc1\n\t" \
    "global_load_dwordx4 a[104:107], v39, s[38:39] offset:0 sc1\n\t" \
    "global_load_dwordx4 a[108:111], v39, s[38:39] offset:1024 sc1\n\t" \
    "global_load_dwordx4 a[112:115], v40, s[38:39] offset:0 sc1\n\t" \
    "global_load_dwordx4 a[116:119], v40, s[38:39] offset:1024 sc1\n\t" \
    "global_load_dwordx4 a[120:123], v41, s[38:39] offset:0 sc1\n\t" \
    "global_load_dwordx4 a[124:127], v41, s[38:39] offset:1024 sc1\n\t" \
    "rr_n225_%=:\n\t" \
    "s_cmp_eq_u32 s65, 0\n\t" \
    "s_cbranch_scc1 rr_act3_%=\n\t" \
    "s_waitcnt vmcnt(0)\n\t" \
    "rr_ckl49_%=:\n\t" \
    "v_cmp_eq_u32 vcc, -1, v200\n\t" \
    "s_cbranch_vccz rr_ck48_%=\n\t" \
    "global_load_dword v200, v201, s[36:37] sc1\n\t" \
    "s_waitcnt vmcnt(0)\n\t" \
    "s_cmp_eq_u32 s68, 0\n\t" \
    "s_cbranch_scc1 rr_F50_%=\n\t" \
    "s_sub_u32 s68, s68, 1\n\t" \
    "s_sleep 1\n\t" \
    "s_branch rr_ckl49_%=\n\t" \
    "rr_F50_%=:\n\t" \
    "v_mov_b32 v166, 5767424\n\t" \
    "v_mov_b32 v167, 15\n\t" \
    "global_store_dword v166, v167, s[36:37] sc1\n\t" \
    "s_waitcnt vmcnt(0)\n\t" \
    "s_branch rr_ck48_%=\n\t" \
    "rr_ck48_%=:\n\t" \
    "s_barrier\n\t" \
    "s_cmp_gt_u32 s65, 62\n\t" \
    "s_cbranch_scc1 rr_end4_%=\n\t" \
    "ds_read_b32 v164, v9\n\t" \
    "s_waitcnt lgkmcnt(0)\n\t" \
    "v_readfirstlane_b32 s81, v164\n\t" \
    "s_nop 1\n\t" \
    "s_cmp_lg_u32 s65, 1\n\t" \
    "s_cbranch_scc1 rr_n151_%=\n\t" \
    "s_waitcnt lgkmcnt(0)\n\t" \
    "s_mov_b32 s70, 116\n\t" \
    "rr_E52_%=:\n\t" \
    "s_memtime s[56:57]\n\t" \
    "s_memrealtime s[2:3]\n\t" \
    "s_nop 0\n\t" \
    "s_memrealtime s[4:5]\n\t" \
    "s_nop 0\n\t" \
    "s_memrealtime s[6:7]\n\t" \
    "s_nop 0\n\t" \
    "s_memrealtime s[8:9]\n\t" \
    "s_nop 0\n\t" \
    "s_memrealtime s[10:11]\n\t" \
    "s_nop 0\n\t" \
    "s_memrealtime s[12:13]\n\t" \
    "s_nop 0\n\t" \
    "s_memrealtime s[14:15]\n\t" \
    "s_nop 0\n\t" \
    "s_memrealtime s[16:17]\n\t" \
    "s_nop 0\n\t" \
    "s_memrealtime s[18:19]\n\t" \
    "s_nop 0\n\t" \
    "s_memrealtime s[20:21]\n\t" \
    "s_nop 0\n\t" \
    "s_memrealtime s[22:23]\n\t" \
    "s_nop 0\n\t" \
    "s_memrealtime s[24:25]\n\t" \
    "s_nop 0\n\t" \
    "s_memrealtime s[26:27]\n\t" \
    "s_nop 0\n\t" \
    "s_memrealtime s[28:29]\n\t" \
    "s_memtime s[54:55]\n\t" \
    "s_add_u32 s70, s70, 8\n\t" \
    "s_waitcnt lgkmcnt(0)\n\t" \
    "s_sub_u32 s69, s54, s56\n\t" \
    "s_cmp_gt_u32 s69, s70\n\t" \
    "s_cbranch_scc1 rr_E52_%=\n\t" \
    "s_add_u32 s2, s2, s4\n\t" \
    "s_add_u32 s6, s6, s8\n\t" \
    "s_add_u32 s10, s10, s12\n\t" \
    "s_add_u32 s14, s14, s16\n\t" \
    "s_add_u32 s18, s18, s20\n\t" \
    "s_add_u32 s22, s22, s24\n\t" \
    "s_add_u32 s2, s2, s6\n\t" \
    "s_add_u32 s10, s10, s14\n\t" \
    "s_add_u32 s18, s18, s22\n\t" \
    "s_add_u32 s2, s2, s26\n\t" \
    "s_add_u32 s10, s10, s18\n\t" \
    "s_add_u32 s2, s2, s10\n\t" \
    "s_mul_i32 s70, s28, 13\n\t" \
    "s_sub_u32 s70, s70, s2\n\t" \
    "s_min_u32 s70, s70, 52\n\t" \
    "s_mul_i32 s69, s69, s70\n\t" \
    "s_mul_hi_u32 s69, s69, 76695844\n\t" \
    "s_mov_b32 s75, s73\n\t" \
    "s_mov_b32 s76, s74\n\t" \
    "s_add_u32 s73, s56, s69\n\t" \
    "s_lshl_b32 s74, s28, 4\n\t" \
    "rr_n151_%=:\n\t" \
    "s_mul_hi_u32 s69, s85, s78\n\t" \
    "s_add_u32 s79, s81, s69\n\t" \
    "s_add_u32 s79, s79, s87\n\t" \
    "s_cmp_lt_u32 s65, 6\n\t" \
    "s_cselect_b32 s69, 48, 0\n\t" \
    "s_add_u32 s79, s79, s69\n\t" \
    "s_add_u32 s80, s79, s88\n\t" \
    "s_sub_u32 s70, s79, s74\n\t" \
    "s_max_i32 s70, s70, 0\n\t" \
    "s_min_i32 s70, s70, 16000\n\t" \
    "s_lshl_b32 s70, s70, 1\n\t" \
    "s_mul_hi_u32 s70, s70, s77\n\t" \
    "s_add_u32 s70, s70, s73\n\t" \
    "rr_W53_%=:\n\t" \
    "s_memtime s[56:57]\n\t" \
    "s_waitcnt lgkmcnt(0)\n\t" \
    "s_sub_u32 s69, s70, s56\n\t" \
    "s_cmp_gt_i32 s69, 24\n\t" \
    "s_cbranch_scc1 rr_W53_%=\n\t" \
    "rr_act3_%=:\n\t" \
    "s_add_u32 s69, s65, 0\n\t" \
    "s_and_b32 s69, s69, 7\n\t" \
    "s_lshl_b32 s69, s69, 18\n\t" \
    "s_add_u32 s69, s69, s62\n\t" \
    "s_add_u32 s46, s36, s69\n\t" \
    "s_addc_u32 s47, s37, 0\n\t" \
    "s_add_u32 s69, s65, 1\n\t" \
    "s_and_b32 s69, s69, 7\n\t" \
    "s_lshl_b32 s69, s69, 18\n\t" \
    "s_add_u32 s69, s69, s62\n\t" \
    "s_add_u32 s48, s36, s69\n\t" \
    "s_addc_u32 s49, s37, 0\n\t" \
    "s_and_b32 s69, s65, 7\n\t" \
    "s_lshl_b32 s69, s69, 18\n\t" \
    "s_add_u32 s69, s69, s63\n\t" \
    "s_add_u32 s52, s36, s69\n\t" \
    "s_addc_u32 s53, s37, 0\n\t" \
    "s_sub_u32 s69, s65, 2\n\t" \
    "s_cmp_ge_u32 s69, 5\n\t" \
    "s_cbranch_scc1 rr_nf54_%=\n\t" \
    "s_cmp_lg_u32 s64, 0\n\t" \
    "s_cbranch_scc1 rr_nf54_%=\n\t" \
    "v_add_u32 v169, 4096, v169\n\t" \
    "global_load_dword v176, v169, s[36:37] sc1\n\t" \
    "rr_nf54_%=:\n\t" \
    "s_waitcnt lgkmcnt(0)\n\t" \
    "s_mov_b32 s70, 116\n\t" \
    "rr_E55_%=:\n\t" \
    "s_memtime s[56:57]\n\t" \
    "s_memrealtime s[2:3]\n\t" \
    "s_nop 0\n\t" \
    "s_memrealtime s[4:5]\n\t" \
    "s_nop 0\n\t" \
    "s_memrealtime s[6:7]\n\t" \
    "s_nop 0\n\t" \
    "s_memrealtime s[8:9]\n\t" \
    "s_nop 0\n\t" \
    "s_memrealtime s[10:11]\n\t" \
    "s_nop 0\n\t" \
    "s_memrealtime s[12:13]\n\t" \
    "s_nop 0\n\t" \
    "s_memrealtime s[14:15]\n\t" \
    "s_nop 0\n\t" \
    "s_memrealtime s[16:17]\n\t" \
    "s_nop 0\n\t" \
    "s_memrealtime s[18:19]\n\t" \
    "s_nop 0\n\t" \
    "s_memrealtime s[20:21]\n\t" \
    "s_nop 0\n\t" \
    "s_memrealtime s[22:23]\n\t" \
    "s_nop 0\n\t" \
    "s_memrealtime s[24:25]\n\t" \
    "s_nop 0\n\t" \
    "s_memrealtime s[26:27]\n\t" \
    "s_nop 0\n\t" \
    "s_memrealtime s[28:29]\n\t" \
    "s_memtime s[54:55]\n\t" \
    "s_add_u32 s70, s70, 8\n\t" \
    "s_waitcnt lgkmcnt(0)\n\t" \
    "s_sub_u32 s69, s54, s56\n\t" \
    "s_cmp_gt_u32 s69, s70\n\t" \
    "s_cbranch_scc1 rr_E55_%=\n\t" \
    "s_add_u32 s2, s2, s4\n\t" \
    "s_add_u32 s6, s6, s8\n\t" \
    "s_add_u32 s10, s10, s12\n\t" \
    "s_add_u32 s14, s14, s16\n\t" \
    "s_add_u32 s18, s18, s20\n\t" \
    "s_add_u32 s22, s22, s24\n\t" \
    "s_add_u32 s2, s2, s6\n\t" \
    "s_add_u32 s10, s10, s14\n\t" \
    "s_add_u32 s18, s18, s22\n\t" \
    "s_add_u32 s2, s2, s26\n\t" \
    "s_add_u32 s10, s10, s18\n\t" \
    "s_add_u32 s2, s2, s10\n\t" \
    "s_mul_i32 s70, s28, 13\n\t" \
    "s_sub_u32 s70, s70, s2\n\t" \
    "s_min_u32 s70, s70, 52\n\t" \
    "s_mul_i32 s69, s69, s70\n\t" \
    "s_mul_hi_u32 s69, s69, 76695844\n\t" \
    "s_mov_b32 s75, s73\n\t" \
    "s_mov_b32 s76, s74\n\t" \
    "s_add_u32 s73, s56, s69\n\t" \
    "s_lshl_b32 s74, s28, 4\n\t" \
    "s_sub_u32 s69, s73, s75\n\t" \
    "s_sub_u32 s70, s74, s76\n\t" \
    "s_sub_u32 s71, s70, 1600\n\t" \
    "s_cmp_lt_u32 s71, 400000\n\t" \
    "s_cbranch_scc0 rr_K56_%=\n\t" \
    "v_cvt_f32_u32 v164, s69\n\t" \
    "v_cvt_f32_u32 v165, s70\n\t" \
    "v_rcp_f32 v166, v164\n\t" \
    "v_rcp_f32 v167, v165\n\t" \
    "v_mul_f32 v166, v166, v165\n\t" \
    "v_mul_f32 v167, v167, v164\n\t" \
    "v_mul_f32 v166, 0x4f800000, v166\n\t" \
    "v_mul_f32 v167, 0x4f000000, v167\n\t" \
    "v_cvt_u32_f32 v166, v166\n\t" \
    "v_cvt_u32_f32 v167, v167\n\t" \
    "v_readfirstlane_b32 s69, v166\n\t" \
    "v_readfirstlane_b32 s70, v167\n\t" \
    "s_nop 1\n\t" \
    "s_lshr_b32 s71, s65, 1\n\t" \
    "s_min_u32 s71, s71, 1\n\t" \
    "s_sub_u32 s69, s69, s78\n\t" \
    "s_ashr_i32 s69, s69, s71\n\t" \
    "s_add_u32 s78, s78, s69\n\t" \
    "s_sub_u32 s70, s70, s77\n\t" \
    "s_ashr_i32 s70, s70, s71\n\t" \
    "s_add_u32 s77, s77, s70\n\t" \
    "rr_K56_%=:\n\t" \
    "s_mov_b32 s91, s64\n\t" \
    "s_cmp_lg_u32 s64, 0\n\t" \
    "s_cselect_b32 s77, 0, s77\n\t" \
    "v_mov_b32 v160, 0\n\t" \
    "v_mov_b32 v161, 0\n\t" \
    "v_mov_b32 v162, 0\n\t" \
    "v_mov_b32 v163, 0\n\t" \
    ".p2align 6\n\t" \
    "s_nop 0\n\t" \
    "s_nop 0\n\t" \
    "s_sub_u32 s70, s80, s74\n\t" \
    "s_max_i32 s70, s70, 0\n\t" \
    "s_min_i32 s70, s70, 16000\n\t" \
    "s_lshl_b32 s70, s70, 1\n\t" \
    "s_mul_hi_u32 s70, s70, s77\n\t" \
    "s_add_u32 s70, s70, s73\n\t" \
    "s_getpc_b64 s[30:31]\n\t" \
    "rr_Wp59_%=:\n\t" \
    "s_add_u32 s30, s30, rr_We60_%=-rr_Wp59_%=\n\t" \
    "s_addc_u32 s31, s31, 0\n\t" \
    "s_sub_u32 s70, s70, 96\n\t" \
    "rr_W58_%=:\n\t" \
    "s_memtime s[56:57]\n\t" \
    "s_waitcnt lgkmcnt(0)\n\t" \
    "s_sub_u32 s69, s70, s56\n\t" \
    "s_cmp_gt_i32 s69, 80\n\t" \
    "s_cbranch_scc1 rr_W58_%=\n\t" \
    "s_max_i32 s69, s69, 0\n\t" \
    "s_and_b32 s69, s69, -4\n\t" \
    "s_sub_u32 s30, s30, s69\n\t" \
    "s_subb_u32 s31, s31, 0\n\t" \
    "s_setpc_b64 s[30:31]\n\t" \
    "s_nop 0\n\t" \
    "s_nop 0\n\t" \
    "s_nop 0\n\t" \
    "s_nop 0\n\t" \
    "s_nop 0\n\t" \
    "s_nop 0\n\t" \
    "s_nop 0\n\t" \
    "s_nop 0\n\t" \
    "s_nop 0\n\t" \
    "s_nop 0\n\t" \
    "s_nop 0\n\t" \
    "s_nop 0\n\t" \
    "s_nop 0\n\t" \
    "s_nop 0\n\t" \
    "s_nop 0\n\t" \
    "s_nop 0\n\t" \
    "s_nop 0\n\t" \
    "s_nop 0\n\t" \
    "s_nop 0\n\t" \
    "s_nop 0\n\t" \
    "rr_We60_%=:\n\t" \
    "global_load_dword v192, v7, s[52:53] offset:0 sc1\n\t" \
    "s_sub_u32 s71, s86, s74\n\t" \
    "s_lshl_b32 s71, s71, 2\n\t" \
    "s_lshr_b32 s33, s77, 1\n\t" \
    "s_sub_u32 s34, s73, 176\n\t" \
    "s_mov_b32 s96, 0\n\t" \
    "s_getpc_b64 s[30:31]\n\t" \
    "rr_rpc64_%=:\n\t" \
    "s_add_u32 s50, s30, rr_pgo67_%=-rr_rpc64_%=\n\t" \
    "s_addc_u32 s51, s31, 0\n\t" \
    "s_add_u32 s30, s30, rr_again5_%=-rr_rpc64_%=\n\t" \
    "s_addc_u32 s31, s31, 0\n\t" \
    "s_nop 7\n\t" \
    "s_nop 7\n\t" \
    "s_nop 7\n\t" \
    "s_nop 7\n\t" \
    "s_nop 3\n\t" \
    "global_load_dword v193, v7, s[52:53] offset:2048 sc1\n\t" \
    "s_nop 7\n\t" \
    "s_nop 7\n\t" \
    "s_nop 7\n\t" \
    "s_nop 7\n\t" \
    "s_nop 7\n\t" \
    "s_nop 7\n\t" \
    "global_load_dword v194, v189, s[52:53] offset:0 sc1\n\t" \
    "s_waitcnt vmcnt(2)\n\t" \
    "rr_hok61_%=:\n\t" \
    "s_memtime s[54:55]\n\t" \
    "v_max_u32_dpp v192, v192, v192 row_ror:1 row_mask:0xf bank_mask:0xf\n\t" \
    "s_nop 1\n\t" \
    "v_max_u32_dpp v192, v192, v192 row_ror:2 row_mask:0xf bank_mask:0xf\n\t" \
    "s_nop 1\n\t" \
    "v_max_u32_dpp v192, v192, v192 row_ror:4 row_mask:0xf bank_mask:0xf\n\t" \
    "s_nop 1\n\t" \
    "v_max_u32_dpp v192, v192, v192 row_ror:8 row_mask:0xf bank_mask:0xf\n\t" \
    "s_nop 0\n\t" \
    "v_readlane_b32 s69, v192, 0\n\t" \
    "v_readlane_b32 s70, v192, 16\n\t" \
    "s_waitcnt lgkmcnt(0)\n\t" \
    "rr_hmax70_%=:\n\t" \
    "s_sub_u32 s56, s34, s54\n\t" \
    "s_max_u32 s81, s69, s70\n\t" \
    "s_cmp_eq_u32 s81, -1\n\t" \
    "s_cbranch_scc1 rr_hlate62_%=\n\t" \
    "s_lshl2_add_u32 s70, s81, s71\n\t" \
    "s_mul_hi_i32 s70, s70, s33\n\t" \
    "s_add_u32 s69, s70, s56\n\t" \
    "s_max_i32 s69, s69, 0\n\t" \
    "s_cmp_gt_i32 s69, 80\n\t" \
    "s_cbranch_scc1 rr_rpoll65_%=\n\t" \
    "rr_rback66_%=:\n\t" \
    "s_and_b32 s69, s69, -4\n\t" \
    "s_sub_u32 s30, s30, s69\n\t" \
    "s_subb_u32 s31, s31, 0\n\t" \
    "s_setpc_b64 s[30:31]\n\t" \
    "rr_rpoll65_%=:\n\t" \
    "s_min_i32 s70, s70, 24000\n\t" \
    "s_add_u32 s70, s70, s73\n\t" \
    "s_sub_u32 s70, s70, 124\n\t" \
    "s_sub_u32 s70, s70, s96\n\t" \
    "rr_wl63_%=:\n\t" \
    "s_memtime s[56:57]\n\t" \
    "s_waitcnt lgkmcnt(0)\n\t" \
    "s_sub_u32 s69, s70, s56\n\t" \
    "s_cmp_gt_i32 s69, 80\n\t" \
    "s_cbranch_scc1 rr_wl63_%=\n\t" \
    "s_max_i32 s69, s69, 0\n\t" \
    "s_branch rr_rback66_%=\n\t" \
    "s_nop 0\n\t" \
    "s_nop 0\n\t" \
    "s_nop 0\n\t" \
    "s_nop 0\n\t" \
    "s_nop 0\n\t" \
    "s_nop 0\n\t" \
    "s_nop 0\n\t" \
    "s_nop 0\n\t" \
    "s_nop 0\n\t" \
    "s_nop 0\n\t" \
    "s_nop 0\n\t" \
    "s_nop 0\n\t" \
    "s_nop 0\n\t" \
    "s_nop 0\n\t" \
    "s_nop 0\n\t" \
    "s_nop 0\n\t" \
    "s_nop 0\n\t" \
    "s_nop 0\n\t" \
    "s_nop 0\n\t" \
    "s_nop 0\n\t" \
    "rr_again5_%=:\n\t" \
    "rr_pgo67_%=:\n\t" \
    "global_load_dwordx4 v[48:51], v10, s[46:47] sc1\n\t" \
    "global_load_dwordx4 v[52:55], v11, s[46:47] sc1\n\t" \
    "global_load_dwordx4 v[56:59], v12, s[46:47] sc1\n\t" \
    "global_load_dwordx4 v[60:63], v13, s[46:47] sc1\n\t" \
    "global_load_dwordx4 v[64:67], v14, s[46:47] sc1\n\t" \
    "global_load_dwordx4 v[68:71], v15, s[46:47] sc1\n\t" \
    "global_load_dwordx4 v[72:75], v16, s[46:47] sc1\n\t" \
    "global_load_dwordx4 v[76:79], v17, s[46:47] sc1\n\t" \
    "global_load_dwordx4 v[80:83], v18, s[46:47] sc1\n\t" \
    "global_load_dwordx4 v[84:87], v19, s[46:47] sc1\n\t" \
    "global_load_dwordx4 v[88:91], v20, s[46:47] sc1\n\t" \
    "global_load_dwordx4 v[92:95], v21, s[46:47] sc1\n\t" \
    "global_load_dwordx4 v[96:99], v22, s[46:47] sc1\n\t" \
    "global_load_dwordx4 v[100:103], v23, s[46:47] sc1\n\t" \
    "global_load_dwordx4 v[104:107], v24, s[46:47] sc1\n\t" \
    "global_load_dwordx4 v[108:111], v25, s[46:47] sc1\n\t" \
    "global_load_dwordx4 v[112:115], v42, s[46:47] sc1\n\t" \
    "global_load_dwordx4 v[116:119], v43, s[46:47] sc1\n\t" \
    "global_load_dwordx4 v[120:123], v44, s[46:47] sc1\n\t" \
    "global_load_dwordx4 v[124:127], v45, s[46:47] sc1\n\t" \
    "global_load_dwordx4 v[128:131], v0, s[40:41] offset:0 sc1\n\t" \
    "global_load_dwordx4 v[132:135], v0, s[40:41] offset:1024 sc1\n\t" \
    "global_load_dwordx4 v[136:139], v0, s[40:41] offset:2048 sc1\n\t" \
    "global_load_dwordx4 v[140:143], v0, s[40:41] offset:3072 sc1\n\t" \
    "global_load_dwordx4 v[144:147], v1, s[40:41] offset:0 sc1\n\t" \
    "global_load_dwordx4 v[148:151], v1, s[40:41] offset:1024 sc1\n\t" \
    "global_load_dwordx4 v[152:155], v1, s[40:41] offset:2048 sc1\n\t" \
    "global_load_dwordx4 v[156:159], v1, s[40:41] offset:3072 sc1\n\t" \
    "s_cmp_lg_u32 s91, 0\n\t" \
    "s_cbranch_scc1 rr_nob8_%=\n\t" \
    "rr_hbk71_%=:\n\t" \
    "v_mov_b32 v164, s81\n\t" \
    "ds_write_b32 v9, v164\n\t" \
    "s_waitcnt lgkmcnt(0)\n\t" \
    "s_barrier\n\t" \
    "rr_nob8_%=:\n\t" \
    "s_waitcnt vmcnt(27)\n\t" \
    "v_smfmac_f32_16x16x64_bf16 v[160:163], v[48:51], a[0:7], v4\n\t" \
    "s_waitcnt vmcnt(26)\n\t" \
    "v_smfmac_f32_16x16x64_bf16 v[160:163], v[52:55], a[8:15], v4\n\t" \
    "s_waitcnt vmcnt(25)\n\t" \
    "v_smfmac_f32_16x16x64_bf16 v[160:163], v[56:59], a[16:23], v4\n\t" \
    "s_waitcnt vmcnt(24)\n\t" \
    "v_smfmac_f32_16x16x64_bf16 v[160:163], v[60:63], a[24:31], v4\n\t" \
    "s_waitcnt vmcnt(23)\n\t" \
    "v_smfmac_f32_16x16x64_bf16 v[160:163], v[64:67], a[32:39], v4\n\t" \
    "s_waitcnt vmcnt(22)\n\t" \
    "v_smfmac_f32_16x16x64_bf16 v[160:163], v[68:71], a[40:47], v4\n\t" \
    "s_waitcnt vmcnt(21)\n\t" \
    "v_smfmac_f32_16x16x64_bf16 v[160:163], v[72:75], a[48:55], v4\n\t" \
    "s_waitcnt vmcnt(20)\n\t" \
    "v_smfmac_f32_16x16x64_bf16 v[160:163], v[76:79], a[56:63], v4\n\t" \
    "s_waitcnt vmcnt(19)\n\t" \
    "v_smfmac_f32_16x16x64_bf16 v[160:163], v[80:83], a[64:71], v4\n\t" \
    "s_waitcnt vmcnt(18)\n\t" \
    "v_smfmac_f32_16x16x64_bf16 v[160:163], v[84:87], a[72:79], v4\n\t" \
    "s_waitcnt vmcnt(17)\n\t" \
    "v_smfmac_f32_16x16x64_bf16 v[160:163], v[88:91], a[80:87], v4\n\t" \
    "s_waitcnt vmcnt(16)\n\t" \
    "v_smfmac_f32_16x16x64_bf16 v[160:163], v[92:95], a[88:95], v4\n\t" \
    "s_waitcnt vmcnt(15)\n\t" \
    "v_smfmac_f32_16x16x64_bf16 v[160:163], v[96:99], a[96:103], v4\n\t" \
    "s_waitcnt vmcnt(14)\n\t" \
    "v_smfmac_f32_16x16x64_bf16 v[160:163], v[100:103], a[104:111], v4\n\t" \
    "s_waitcnt vmcnt(13)\n\t" \
    "v_smfmac_f32_16x16x64_bf16 v[160:163], v[104:107], a[112:119], v4\n\t" \
    "s_waitcnt vmcnt(12)\n\t" \
    "v_smfmac_f32_16x16x64_bf16 v[160:163], v[108:111], a[120:127], v4\n\t" \
    "s_waitcnt vmcnt(6)\n\t" \
    "v_smfmac_f32_16x16x64_bf16 v[160:163], v[112:115], v[128:135], v4\n\t" \
    "s_waitcnt vmcnt(4)\n\t" \
    "v_smfmac_f32_16x16x64_bf16 v[160:163], v[116:119], v[136:143], v4\n\t" \
    "s_waitcnt vmcnt(2)\n\t" \
    "v_smfmac_f32_16x16x64_bf16 v[160:163], v[120:123], v[144:151], v4\n\t" \
    "s_waitcnt vmcnt(0)\n\t" \
    "v_smfmac_f32_16x16x64_bf16 v[160:163], v[124:127], v[152:159], v4\n\t" \
    "s_nop 7\n\t" \
    "v_pk_add_f32 v[160:161], v[160:161], v[162:163]\n\t" \
    "v_add_f32 v166, v160, v161\n\t" \
    "v_cvt_pk_bf16_f32 v164, v160, v161\n\t" \
    "s_nop 0\n\t" \
    "v_add_f32_dpp v166, v166, v166 quad_perm:[1,0,3,2] row_mask:0xf bank_mask:0xf\n\t" \
    "v_mov_b32_dpp v165, v164 quad_perm:[1,0,3,2] row_mask:0xf bank_mask:0xf\n\t" \
    "v_cmp_u_f32 vcc, v166, v166\n\t" \
    "v_perm_b32 v167, v165, v164, v5\n\t" \
    "s_nop 0\n\t" \
    "v_cndmask_b32 v164, v167, v172, vcc\n\t" \
    "rr_cont7_%=:\n\t" \
    "global_store_dword v180, v164, s[48:49] sc1\n\t" \
    "global_store_dword v181, v164, s[48:49] sc1\n\t" \
    "global_store_dword v182, v164, s[48:49] sc1\n\t" \
    "global_store_dword v2, v164, s[48:49] sc1\n\t" \
    "s_cmp_lg_u32 s64, 0\n\t" \
    "s_cbranch_scc1 rr_g214_%=\n\t" \
    "s_cbranch_vccnz rr_bad6_%=\n\t" \
    "s_cmp_eq_u32 s65, 62\n\t" \
    "s_cbranch_scc1 rr_end4_%=\n\t" \
    "s_add_u32 s65, s65, 2\n\t" \
    "s_branch rr_pf9_%=\n\t" \
    "rr_bad6_%=:\n\t" \
    "s_cmp_lg_u32 s64, 0\n\t" \
    "s_cbranch_scc1 rr_cont7_%=\n\t" \
    "s_waitcnt lgkmcnt(0)\n\t" \
    "v_max_u32 v165, v48, v49\n\t" \
    "v_max3_u32 v165, v165, v50, v51\n\t" \
    "v_max3_u32 v165, v165, v52, v53\n\t" \
    "v_max3_u32 v165, v165, v54, v55\n\t" \
    "v_max3_u32 v165, v165, v56, v57\n\t" \
    "v_max3_u32 v165, v165, v58, v59\n\t" \
    "v_max3_u32 v165, v165, v60, v61\n\t" \
    "v_max3_u32 v165, v165, v62, v63\n\t" \
    "v_max3_u32 v165, v165, v64, v65\n\t" \
    "v_max3_u32 v165, v165, v66, v67\n\t" \
    "v_max3_u32 v165, v165, v68, v69\n\t" \
    "v_max3_u32 v165, v165, v70, v71\n\t" \
    "v_max3_u32 v165, v165, v72, v73\n\t" \
    "v_max3_u32 v165, v165, v74, v75\n\t" \
    "v_max3_u32 v165, v165, v76, v77\n\t" \
    "v_max3_u32 v165, v165, v78, v79\n\t" \
    "v_max3_u32 v165, v165, v80, v81\n\t" \
    "v_max3_u32 v165, v165, v82, v83\n\t" \
    "v_max3_u32 v165, v165, v84, v85\n\t" \
    "v_max3_u32 v165, v165, v86, v87\n\t" \
    "v_max3_u32 v165, v165, v88, v89\n\t" \
    "v_max3_u32 v165, v165, v90, v91\n\t" \
    "v_max3_u32 v165, v165, v92, v93\n\t" \
    "v_max3_u32 v165, v165, v94, v95\n\t" \
    "v_max3_u32 v165, v165, v96, v97\n\t" \
    "v_max3_u32 v165, v165, v98, v99\n\t" \
    "v_max3_u32 v165, v165, v100, v101\n\t" \
    "v_max3_u32 v165, v165, v102, v103\n\t" \
    "v_max3_u32 v165, v165, v104, v105\n\t" \
    "v_max3_u32 v165, v165, v106, v107\n\t" \
    "v_max3_u32 v165, v165, v108, v109\n\t" \
    "v_max3_u32 v165, v165, v110, v111\n\t" \
    "v_max3_u32 v165, v165, v112, v113\n\t" \
    "v_max3_u32 v165, v165, v114, v115\n\t" \
    "v_max3_u32 v165, v165, v116, v117\n\t" \
    "v_max3_u32 v165, v165, v118, v119\n\t" \
    "v_max3_u32 v165, v165, v120, v121\n\t" \
    "v_max3_u32 v165, v165, v122, v123\n\t" \
    "v_max3_u32 v165, v165, v124, v125\n\t" \
    "v_max3_u32 v165, v165, v126, v127\n\t" \
    "v_max_u32 v166, v128, v129\n\t" \
    "v_max3_u32 v166, v166, v130, v131\n\t" \
    "v_max3_u32 v166, v166, v132, v133\n\t" \
    "v_max3_u32 v166, v166, v134, v135\n\t" \
    "v_max3_u32 v166, v166, v136, v137\n\t" \
    "v_max3_u32 v166, v166, v138, v139\n\t" \
    "v_max3_u32 v166, v166, v140, v141\n\t" \
    "v_max3_u32 v166, v166, v142, v143\n\t" \
    "v_max3_u32 v166, v166, v144, v145\n\t" \
    "v_max3_u32 v166, v166, v146, v147\n\t" \
    "v_max3_u32 v166, v166, v148, v149\n\t" \
    "v_max3_u32 v166, v166, v150, v151\n\t" \
    "v_max3_u32 v166, v166, v152, v153\n\t" \
    "v_max3_u32 v166, v166, v154, v155\n\t" \
    "v_max3_u32 v166, v166, v156, v157\n\t" \
    "v_max3_u32 v166, v166, v158, v159\n\t" \
    "v_max_u32 v165, v165, v166\n\t" \
    "v_mov_b32 v164, v167\n\t" \
    "v_cmp_eq_u32 vcc, -1, v165\n\t" \
    "s_cbranch_vccz rr_cont7_%=\n\t" \
    "s_mov_b64 vcc, 0\n\t" \
    "s_mov_b32 s91, 1\n\t" \
    "s_mov_b32 s96, 0\n\t" \
    "v_mov_b32 v160, 0\n\t" \
    "v_mov_b32 v161, 0\n\t" \
    "v_mov_b32 v162, 0\n\t" \
    "v_mov_b32 v163, 0\n\t" \
    "s_cmp_eq_u32 s68, 0\n\t" \
    "s_cbranch_scc1 rr_F74_%=\n\t" \
    "s_sub_u32 s68, s68, 1\n\t" \
    "s_branch rr_again5_%=\n\t" \
    "rr_F74_%=:\n\t" \
    "v_mov_b32 v166, 5767424\n\t" \
    "v_mov_b32 v167, 3\n\t" \
    "global_store_dword v166, v167, s[36:37] sc1\n\t" \
    "s_waitcnt vmcnt(0)\n\t" \
    "s_branch rr_cont7_%=\n\t" \
    "rr_hlate62_%=:\n\t" \
    "s_cmp_lg_u32 s64, 0\n\t" \
    "s_cbranch_scc1 rr_hpd73_%=\n\t" \
    "global_load_dword v195, v189, s[52:53] offset:2048 sc1\n\t" \
    "s_waitcnt vmcnt(2)\n\t" \
    "v_mov_b32 v192, v193\n\t" \
    "v_cmp_eq_u32 vcc, -1, v193\n\t" \
    "s_cbranch_vccz rr_hok61_%=\n\t" \
    "global_load_dword v196, v190, s[52:53] offset:0 sc1\n\t" \
    "s_waitcnt vmcnt(2)\n\t" \
    "v_mov_b32 v192, v194\n\t" \
    "v_cmp_eq_u32 vcc, -1, v194\n\t" \
    "s_cbranch_vccz rr_hok61_%=\n\t" \
    "global_load_dword v197, v190, s[52:53] offset:2048 sc1\n\t" \
    "s_waitcnt vmcnt(2)\n\t" \
    "v_mov_b32 v192, v195\n\t" \
    "v_cmp_eq_u32 vcc, -1, v195\n\t" \
    "s_cbranch_vccz rr_hok61_%=\n\t" \
    "s_waitcnt vmcnt(1)\n\t" \
    "v_mov_b32 v192, v196\n\t" \
    "v_cmp_eq_u32 vcc, -1, v196\n\t" \
    "s_cbranch_vccz rr_hok61_%=\n\t" \
    "s_waitcnt vmcnt(0)\n\t" \
    "v_mov_b32 v192, v197\n\t" \
    "v_cmp_eq_u32 vcc, -1, v197\n\t" \
    "s_cbranch_vccz rr_hok61_%=\n\t" \
    "rr_hp72_%=:\n\t" \
    "global_load_dword v192, v191, s[52:53] offset:0 sc1\n\t" \
    "s_waitcnt vmcnt(0)\n\t" \
    "v_cmp_eq_u32 vcc, -1, v192\n\t" \
    "s_cbranch_vccz rr_hpd73_%=\n\t" \
    "s_cmp_eq_u32 s68, 0\n\t" \
    "s_cbranch_scc1 rr_F75_%=\n\t" \
    "s_sub_u32 s68, s68, 1\n\t" \
    "s_sleep 1\n\t" \
    "s_branch rr_hp72_%=\n\t" \
    "rr_F75_%=:\n\t" \
    "v_mov_b32 v166, 5767424\n\t" \
    "v_mov_b32 v167, 7\n\t" \
    "global_store_dword v166, v167, s[36:37] sc1\n\t" \
    "s_waitcnt vmcnt(0)\n\t" \
    "s_branch rr_hpd73_%=\n\t" \
    "rr_hpd73_%=:\n\t" \
    "v_and_b32 v192, -2, v192\n\t" \
    "s_waitcnt lgkmcnt(0)\n\t" \
    "s_mov_b32 s70, 116\n\t" \
    "rr_E76_%=:\n\t" \
    "s_memtime s[56:57]\n\t" \
    "s_memrealtime s[2:3]\n\t" \
    "s_nop 0\n\t" \
    "s_memrealtime s[4:5]\n\t" \
    "s_nop 0\n\t" \
    "s_memrealtime s[6:7]\n\t" \
    "s_nop 0\n\t" \
    "s_memrealtime s[8:9]\n\t" \
    "s_nop 0\n\t" \
    "s_memrealtime s[10:11]\n\t" \
    "s_nop 0\n\t" \
    "s_memrealtime s[12:13]\n\t" \
    "s_nop 0\n\t" \
    "s_memrealtime s[14:15]\n\t" \
    "s_nop 0\n\t" \
    "s_memrealtime s[16:17]\n\t" \
    "s_nop 0\n\t" \
    "s_memrealtime s[18:19]\n\t" \
    "s_nop 0\n\t" \
    "s_memrealtime s[20:21]\n\t" \
    "s_nop 0\n\t" \
    "s_memrealtime s[22:23]\n\t" \
    "s_nop 0\n\t" \
    "s_memrealtime s[24:25]\n\t" \
    "s_nop 0\n\t" \
    "s_memrealtime s[26:27]\n\t" \
    "s_nop 0\n\t" \
    "s_memrealtime s[28:29]\n\t" \
    "s_memtime s[54:55]\n\t" \
    "s_add_u32 s70, s70, 8\n\t" \
    "s_waitcnt lgkmcnt(0)\n\t" \
    "s_sub_u32 s69, s54, s56\n\t" \
    "s_cmp_gt_u32 s69, s70\n\t" \
    "s_cbranch_scc1 rr_E76_%=\n\t" \
    "s_add_u32 s2, s2, s4\n\t" \
    "s_add_u32 s6, s6, s8\n\t" \
    "s_add_u32 s10, s10, s12\n\t" \
    "s_add_u32 s14, s14, s16\n\t" \
    "s_add_u32 s18, s18, s20\n\t" \
    "s_add_u32 s22, s22, s24\n\t" \
    "s_add_u32 s2, s2, s6\n\t" \
    "s_add_u32 s10, s10, s14\n\t" \
    "s_add_u32 s18, s18, s22\n\t" \
    "s_add_u32 s2, s2, s26\n\t" \
    "s_add_u32 s10, s10, s18\n\t" \
    "s_add_u32 s2, s2, s10\n\t" \
    "s_mul_i32 s70, s28, 13\n\t" \
    "s_sub_u32 s70, s70, s2\n\t" \
    "s_min_u32 s70, s70, 52\n\t" \
    "s_mul_i32 s69, s69, s70\n\t" \
    "s_mul_hi_u32 s69, s69, 76695844\n\t" \
    "s_mov_b32 s75, s73\n\t" \
    "s_mov_b32 s76, s74\n\t" \
    "s_add_u32 s73, s56, s69\n\t" \
    "s_lshl_b32 s74, s28, 4\n\t" \
    "s_sub_u32 s71, s86, s74\n\t" \
    "s_lshl_b32 s71, s71, 2\n\t" \
    "s_lshr_b32 s33, s77, 1\n\t" \
    "s_sub_u32 s34, s73, 176\n\t" \
    "s_branch rr_hok61_%=\n\t" \
    "rr_end4_%=:\n\t" \
    "s_endpgm\n\t" \
    "rr_last_%=:\n\t" \
    ""
#define ASM_H \
    "s_mov_b64 s[36:37], %0\n\t" \
    "s_mov_b64 s[42:43], %1\n\t" \
    "s_mov_b64 s[44:45], %2\n\t" \
    "s_mov_b32 s66, %3\n\t" \
    "s_mov_b32 s67, %4\n\t" \
    "s_mov_b64 s[50:51], %5\n\t" \
    "s_mov_b32 s82, %7\n\t" \
    "s_load_dwordx8 s[84:91], %6, 0\n\t" \
    "v_mbcnt_lo_u32_b32 v223, -1, 0\n\t" \
    "v_mbcnt_hi_u32_b32 v223, -1, v223\n\t" \
    "s_getpc_b64 s[46:47]\n\t" \
    "hh_pc_%=:\n\t" \
    "s_lshl_b32 s69, s66, 13\n\t" \
    "v_lshl_add_u32 v222, v223, 7, s69\n\t" \
    "v_min_u32 v222, hh_last_%=-hh_pc_%=-4, v222\n\t" \
    "global_load_dword v222, v222, s[46:47]\n\t" \
    "v_lshlrev_b32 v223, 2, v223\n\t" \
    "v_add_u32 v223, s82, v223\n\t" \
    "ds_read_b32 v0, v223 offset:0\n\t" \
    "ds_read_b32 v1, v223 offset:256\n\t" \
    "ds_read_b32 v2, v223 offset:512\n\t" \
    "ds_read_b32 v3, v223 offset:768\n\t" \
    "ds_read_b32 v4, v223 offset:1024\n\t" \
    "ds_read_b32 v5, v223 offset:1280\n\t" \
    "ds_read_b32 v6, v223 offset:1536\n\t" \
    "ds_read_b32 v7, v223 offset:1792\n\t" \
    "ds_read_b32 v8, v223 offset:2048\n\t" \
    "ds_read_b32 v9, v223 offset:2304\n\t" \
    "ds_read_b32 v10, v223 offset:2560\n\t" \
    "ds_read_b32 v11, v223 offset:2816\n\t" \
    "s_waitcnt lgkmcnt(0)\n\t" \
    "ds_read_b32 v12, v223 offset:3072\n\t" \
    "ds_read_b32 v13, v223 offset:3328\n\t" \
    "ds_read_b32 v14, v223 offset:3584\n\t" \
    "ds_read_b32 v15, v223 offset:3840\n\t" \
    "ds_read_b32 v16, v223 offset:4096\n\t" \
    "ds_read_b32 v17, v223 offset:4352\n\t" \
    "s_waitcnt lgkmcnt(0)\n\t" \
    "s_mov_b32 s68, 262144\n\t" \
    "s_mov_b32 s72, 0\n\t" \
    "s_mov_b32 s83, -1\n\t" \
    "s_mov_b32 s65, 0\n\t" \
    "s_mov_b32 s77, 3019898880\n\t" \
    "s_mov_b32 s78, 3054198966\n\t" \
    "s_waitcnt lgkmcnt(0)\n\t" \
    "s_mov_b32 s70, 116\n\t" \
    "hh_E1_%=:\n\t" \
    "s_memtime s[56:57]\n\t" \
    "s_memrealtime s[2:3]\n\t" \
    "s_nop 0\n\t" \
    "s_memrealtime s[4:5]\n\t" \
    "s_nop 0\n\t" \
    "s_memrealtime s[6:7]\n\t" \
    "s_nop 0\n\t" \
    "s_memrealtime s[8:9]\n\t" \
    "s_nop 0\n\t" \
    "s_memrealtime s[10:11]\n\t" \
    "s_nop 0\n\t" \
    "s_memrealtime s[12:13]\n\t" \
    "s_nop 0\n\t" \
    "s_memrealtime s[14:15]\n\t" \
    "s_nop 0\n\t" \
    "s_memrealtime s[16:17]\n\t" \
    "s_nop 0\n\t" \
    "s_memrealtime s[18:19]\n\t" \
    "s_nop 0\n\t" \
    "s_memrealtime s[20:21]\n\t" \
    "s_nop 0\n\t" \
    "s_memrealtime s[22:23]\n\t" \
    "s_nop 0\n\t" \
    "s_memrealtime s[24:25]\n\t" \
    "s_nop 0\n\t" \
    "s_memrealtime s[26:27]\n\t" \
    "s_nop 0\n\t" \
    "s_memrealtime s[28:29]\n\t" \
    "s_memtime s[54:55]\n\t" \
    "s_add_u32 s70, s70, 8\n\t" \
    "s_waitcnt lgkmcnt(0)\n\t" \
    "s_sub_u32 s69, s54, s56\n\t" \
    "s_cmp_gt_u32 s69, s70\n\t" \
    "s_cbranch_scc1 hh_E1_%=\n\t" \
    "s_add_u32 s2, s2, s4\n\t" \
    "s_add_u32 s6, s6, s8\n\t" \
    "s_add_u32 s10, s10, s12\n\t" \
    "s_add_u32 s14, s14, s16\n\t" \
    "s_add_u32 s18, s18, s20\n\t" \
    "s_add_u32 s22, s22, s24\n\t" \
    "s_add_u32 s2, s2, s6\n\t" \
    "s_add_u32 s10, s10, s14\n\t" \
    "s_add_u32 s18, s18, s22\n\t" \
    "s_add_u32 s2, s2, s26\n\t" \
    "s_add_u32 s10, s10, s18\n\t" \
    "s_add_u32 s2, s2, s10\n\t" \
    "s_mul_i32 s70, s28, 13\n\t" \
    "s_sub_u32 s70, s70, s2\n\t" \
    "s_min_u32 s70, s70, 52\n\t" \
    "s_mul_i32 s69, s69, s70\n\t" \
    "s_mul_hi_u32 s69, s69, 76695844\n\t" \
    "s_mov_b32 s75, s73\n\t" \
    "s_mov_b32 s76, s74\n\t" \
    "s_add_u32 s73, s56, s69\n\t" \
    "s_lshl_b32 s74, s28, 4\n\t" \
    "v_mbcnt_lo_u32_b32 v30, -1, 0\n\t" \
    "v_mbcnt_hi_u32_b32 v30, -1, v30\n\t" \
    "v_lshlrev_b32 v30, 6, v30\n\t" \
    "v_add_u32 v30, 6225920, v30\n\t" \
    "v_mov_b32 v31, 6234112\n\t" \
    "global_load_dword v31, v31, s[36:37] sc1\n\t" \
    "global_load_dwordx2 v[32:33], v30, s[36:37] sc1\n\t" \
    "global_load_dword v34, v30, s[36:37] offset:2048 sc1\n\t" \
    "s_waitcnt vmcnt(0)\n\t" \
    "hh_C2_%=:\n\t" \
    "v_cmp_ne_u32 s[54:55], v32, v34\n\t" \
    "v_sub_u32 v32, v32, v31\n\t" \
    "v_cmp_ne_u32 vcc, 1, v32\n\t" \
    "s_nop 1\n\t" \
    "s_or_b64 vcc, vcc, s[54:55]\n\t" \
    "s_cbranch_scc0 hh_Cok3_%=\n\t" \
    "s_cmp_eq_u32 s68, 0\n\t" \
    "s_cbranch_scc1 hh_F5_%=\n\t" \
    "s_sub_u32 s68, s68, 1\n\t" \
    "s_sleep 6\n\t" \
    "s_branch hh_Cag4_%=\n\t" \
    "hh_F5_%=:\n\t" \
    "v_mov_b32 v34, 5767424\n\t" \
    "v_mov_b32 v35, 4\n\t" \
    "global_store_dword v34, v35, s[36:37] sc1\n\t" \
    "s_waitcnt vmcnt(0)\n\t" \
    "s_branch hh_Cok3_%=\n\t" \
    "hh_Cag4_%=:\n\t" \
    "global_load_dwordx2 v[32:33], v30, s[36:37] sc1\n\t" \
    "global_load_dword v34, v30, s[36:37] offset:2048 sc1\n\t" \
    "s_waitcnt vmcnt(0)\n\t" \
    "s_branch hh_C2_%=\n\t" \
    "hh_Cok3_%=:\n\t" \
    "v_readlane_b32 s69, v33, 2\n\t" \
    "v_readlane_b32 s70, v33, 3\n\t" \
    "s_nop 1\n\t" \
    "s_cmp_lt_u32 s70, s69\n\t" \
    "s_cselect_b32 s69, 0, 8192\n\t" \
    "s_add_u32 s100, s36, s69\n\t" \
    "s_addc_u32 s101, s37, 0\n\t" \
    "v_mov_b32_dpp v30, v33 quad_perm:[1,0,3,2] row_mask:0xf bank_mask:0xf\n\t" \
    "v_cmp_lt_u32 s[44:45], v30, v33\n\t" \
    "s_nop 1\n\t" \
    "s_bitcmp1_b32 s44, 0\n\t" \
    "s_cselect_b32 s62, 8192, 0\n\t" \
    "s_bitcmp1_b32 s44, 2\n\t" \
    "s_cselect_b32 s63, 8192, 0\n\t" \
    "s_or_b32 s63, s63, 0x200000\n\t" \
    "v_min_u32 v30, v30, v33\n\t" \
    "v_cmp_lt_u32 vcc, 660, v30\n\t" \
    "s_nop 1\n\t" \
    "s_bcnt1_i32_b64 s69, vcc\n\t" \
    "s_cmp_gt_u32 s69, 16\n\t" \
    "s_cselect_b32 s69, 1, 0\n\t" \
    "s_mul_i32 s70, s69, 160\n\t" \
    "s_add_u32 s86, s86, s70\n\t" \
    "s_mul_i32 s70, s69, 536\n\t" \
    "s_add_u32 s87, s87, s70\n\t" \
    "s_mul_i32 s70, s69, 256\n\t" \
    "s_add_u32 s88, s88, s70\n\t" \
    "s_waitcnt lgkmcnt(0)\n\t" \
    "s_mov_b32 s70, 116\n\t" \
    "hh_E6_%=:\n\t" \
    "s_memtime s[56:57]\n\t" \
    "s_memrealtime s[2:3]\n\t" \
    "s_nop 0\n\t" \
    "s_memrealtime s[4:5]\n\t" \
    "s_nop 0\n\t" \
    "s_memrealtime s[6:7]\n\t" \
    "s_nop 0\n\t" \
    "s_memrealtime s[8:9]\n\t" \
    "s_nop 0\n\t" \
    "s_memrealtime s[10:11]\n\t" \
    "s_nop 0\n\t" \
    "s_memrealtime s[12:13]\n\t" \
    "s_nop 0\n\t" \
    "s_memrealtime s[14:15]\n\t" \
    "s_nop 0\n\t" \
    "s_memrealtime s[16:17]\n\t" \
    "s_nop 0\n\t" \
    "s_memrealtime s[18:19]\n\t" \
    "s_nop 0\n\t" \
    "s_memrealtime s[20:21]\n\t" \
    "s_nop 0\n\t" \
    "s_memrealtime s[22:23]\n\t" \
    "s_nop 0\n\t" \
    "s_memrealtime s[24:25]\n\t" \
    "s_nop 0\n\t" \
    "s_memrealtime s[26:27]\n\t" \
    "s_nop 0\n\t" \
    "s_memrealtime s[28:29]\n\t" \
    "s_memtime s[54:55]\n\t" \
    "s_add_u32 s70, s70, 8\n\t" \
    "s_waitcnt lgkmcnt(0)\n\t" \
    "s_sub_u32 s69, s54, s56\n\t" \
    "s_cmp_gt_u32 s69, s70\n\t" \
    "s_cbranch_scc1 hh_E6_%=\n\t" \
    "s_add_u32 s2, s2, s4\n\t" \
    "s_add_u32 s6, s6, s8\n\t" \
    "s_add_u32 s10, s10, s12\n\t" \
    "s_add_u32 s14, s14, s16\n\t" \
    "s_add_u32 s18, s18, s20\n\t" \
    "s_add_u32 s22, s22, s24\n\t" \
    "s_add_u32 s2, s2, s6\n\t" \
    "s_add_u32 s10, s10, s14\n\t" \
    "s_add_u32 s18, s18, s22\n\t" \
    "s_add_u32 s2, s2, s26\n\t" \
    "s_add_u32 s10, s10, s18\n\t" \
    "s_add_u32 s2, s2, s10\n\t" \
    "s_mul_i32 s70, s28, 13\n\t" \
    "s_sub_u32 s70, s70, s2\n\t" \
    "s_min_u32 s70, s70, 52\n\t" \
    "s_mul_i32 s69, s69, s70\n\t" \
    "s_mul_hi_u32 s69, s69, 76695844\n\t" \
    "s_mov_b32 s75, s73\n\t" \
    "s_mov_b32 s76, s74\n\t" \
    "s_add_u32 s73, s56, s69\n\t" \
    "s_lshl_b32 s74, s28, 4\n\t" \
    "s_sub_u32 s69, s73, s75\n\t" \
    "s_sub_u32 s70, s74, s76\n\t" \
    "s_sub_u32 s71, s70, 1600\n\t" \
    "s_cmp_lt_u32 s71, 400000\n\t" \
    "s_cbranch_scc0 hh_K7_%=\n\t" \
    "v_cvt_f32_u32 v30, s69\n\t" \
    "v_cvt_f32_u32 v31, s70\n\t" \
    "v_rcp_f32 v32, v30\n\t" \
    "v_rcp_f32 v33, v31\n\t" \
    "v_mul_f32 v32, v32, v31\n\t" \
    "v_mul_f32 v33, v33, v30\n\t" \
    "v_mul_f32 v32, 0x4f800000, v32\n\t" \
    "v_mul_f32 v33, 0x4f000000, v33\n\t" \
    "v_cvt_u32_f32 v32, v32\n\t" \
    "v_cvt_u32_f32 v33, v33\n\t" \
    "v_readfirstlane_b32 s69, v32\n\t" \
    "v_readfirstlane_b32 s70, v33\n\t" \
    "s_nop 1\n\t" \
    "s_mov_b32 s78, s69\n\t" \
    "s_mov_b32 s77, s70\n\t" \
    "hh_K7_%=:\n\t" \
    "s_sub_u32 s80, s74, 32000\n\t" \
    "v_mov_b32 v38, -1\n\t" \
    "v_mov_b32 v39, -1\n\t" \
    "s_sub_u32 s69, s100, s36\n\t" \
    "v_cmp_eq_u32 vcc, 0, v3\n\t" \
    "v_mov_b32 v30, s69\n\t" \
    "s_nop 0\n\t" \
    "v_cndmask_b32 v30, 0, v30, vcc\n\t" \
    "v_add_u32 v2, v2, v30\n\t" \
    "s_mov_b32 m0, 155648\n\t" \
    "s_mul_i32 s96, s67, 16\n\t" \
    "s_add_u32 s96, s96, 64\n\t" \
    "hh_loop8_%=:\n\t" \
    "s_and_b32 s69, s65, 7\n\t" \
    "s_lshl_b32 s69, s69, 18\n\t" \
    "s_add_u32 s69, s69, s63\n\t" \
    "s_add_u32 s52, s36, s69\n\t" \
    "s_addc_u32 s53, s37, 0\n\t" \
    "s_add_u32 s71, s80, 608\n\t" \
    "s_sub_u32 s70, s71, s74\n\t" \
    "s_max_i32 s70, s70, 0\n\t" \
    "s_min_i32 s70, s70, 16000\n\t" \
    "s_lshl_b32 s70, s70, 1\n\t" \
    "s_mul_hi_u32 s70, s70, s77\n\t" \
    "s_add_u32 s70, s70, s73\n\t" \
    "hh_W11_%=:\n\t" \
    "s_memtime s[56:57]\n\t" \
    "s_waitcnt lgkmcnt(0)\n\t" \
    "s_sub_u32 s69, s70, s56\n\t" \
    "s_cmp_gt_i32 s69, 24\n\t" \
    "s_cbranch_scc1 hh_W11_%=\n\t" \
    "global_load_dword v30, v1, s[52:53] sc1\n\t" \
    "s_cmp_lt_u32 s65, 7\n\t" \
    "s_cselect_b32 s69, -2, 63\n\t" \
    "v_mov_b32 v38, s69\n\t" \
    "s_sub_u32 s69, s65, 2\n\t" \
    "s_cmp_ge_u32 s69, 5\n\t" \
    "s_cbranch_scc1 hh_ne12_%=\n\t" \
    "v_mad_u32_u24 v39, s65, v3, v2\n\t" \
    "global_load_dword v38, v39, s[36:37] sc1\n\t" \
    "hh_ne12_%=:\n\t" \
    "s_waitcnt lgkmcnt(0)\n\t" \
    "s_mov_b32 s70, 116\n\t" \
    "hh_E16_%=:\n\t" \
    "s_memtime s[56:57]\n\t" \
    "s_memrealtime s[2:3]\n\t" \
    "s_nop 0\n\t" \
    "s_memrealtime s[4:5]\n\t" \
    "s_nop 0\n\t" \
    "s_memrealtime s[6:7]\n\t" \
    "s_nop 0\n\t" \
    "s_memrealtime s[8:9]\n\t" \
    "s_nop 0\n\t" \
    "s_memrealtime s[10:11]\n\t" \
    "s_nop 0\n\t" \
    "s_memrealtime s[12:13]\n\t" \
    "s_nop 0\n\t" \
    "s_memrealtime s[14:15]\n\t" \
    "s_nop 0\n\t" \
    "s_memrealtime s[16:17]\n\t" \
    "s_nop 0\n\t" \
    "s_memrealtime s[18:19]\n\t" \
    "s_nop 0\n\t" \
    "s_memrealtime s[20:21]\n\t" \
    "s_nop 0\n\t" \
    "s_memrealtime s[22:23]\n\t" \
    "s_nop 0\n\t" \
    "s_memrealtime s[24:25]\n\t" \
    "s_nop 0\n\t" \
    "s_memrealtime s[26:27]\n\t" \
    "s_nop 0\n\t" \
    "s_memrealtime s[28:29]\n\t" \
    "s_memtime s[54:55]\n\t" \
    "s_add_u32 s70, s70, 8\n\t" \
    "s_waitcnt lgkmcnt(0)\n\t" \
    "s_sub_u32 s69, s54, s56\n\t" \
    "s_cmp_gt_u32 s69, s70\n\t" \
    "s_cbranch_scc1 hh_E16_%=\n\t" \
    "s_add_u32 s2, s2, s4\n\t" \
    "s_add_u32 s6, s6, s8\n\t" \
    "s_add_u32 s10, s10, s12\n\t" \
    "s_add_u32 s14, s14, s16\n\t" \
    "s_add_u32 s18, s18, s20\n\t" \
    "s_add_u32 s22, s22, s24\n\t" \
    "s_add_u32 s2, s2, s6\n\t" \
    "s_add_u32 s10, s10, s14\n\t" \
    "s_add_u32 s18, s18, s22\n\t" \
    "s_add_u32 s2, s2, s26\n\t" \
    "s_add_u32 s10, s10, s18\n\t" \
    "s_add_u32 s2, s2, s10\n\t" \
    "s_mul_i32 s70, s28, 13\n\t" \
    "s_sub_u32 s70, s70, s2\n\t" \
    "s_min_u32 s70, s70, 52\n\t" \
    "s_mul_i32 s69, s69, s70\n\t" \
    "s_mul_hi_u32 s69, s69, 76695844\n\t" \
    "s_mov_b32 s75, s73\n\t" \
    "s_mov_b32 s76, s74\n\t" \
    "s_add_u32 s73, s56, s69\n\t" \
    "s_lshl_b32 s74, s28, 4\n\t" \
    "s_sub_u32 s69, s73, s75\n\t" \
    "s_sub_u32 s70, s74, s76\n\t" \
    "s_sub_u32 s71, s70, 1600\n\t" \
    "s_cmp_lt_u32 s71, 400000\n\t" \
    "s_cbranch_scc0 hh_K17_%=\n\t" \
    "v_cvt_f32_u32 v34, s69\n\t" \
    "v_cvt_f32_u32 v35, s70\n\t" \
    "v_rcp_f32 v36, v34\n\t" \
    "v_rcp_f32 v37, v35\n\t" \
    "v_mul_f32 v36, v36, v35\n\t" \
    "v_mul_f32 v37, v37, v34\n\t" \
    "v_mul_f32 v36, 0x4f800000, v36\n\t" \
    "v_mul_f32 v37, 0x4f000000, v37\n\t" \
    "v_cvt_u32_f32 v36, v36\n\t" \
    "v_cvt_u32_f32 v37, v37\n\t" \
    "v_readfirstlane_b32 s69, v36\n\t" \
    "v_readfirstlane_b32 s70, v37\n\t" \
    "s_nop 1\n\t" \
    "s_sub_u32 s69, s69, s78\n\t" \
    "s_ashr_i32 s69, s69, 1\n\t" \
    "s_add_u32 s78, s78, s69\n\t" \
    "s_sub_u32 s70, s70, s77\n\t" \
    "s_ashr_i32 s70, s70, 1\n\t" \
    "s_add_u32 s77, s77, s70\n\t" \
    "hh_K17_%=:\n\t" \
    "s_waitcnt vmcnt(0)\n\t" \
    "v_cmp_eq_u32 vcc, -1, v30\n\t" \
    "s_cbranch_vccz hh_Hok14_%=\n\t" \
    "hh_H13_%=:\n\t" \
    "global_load_dword v30, v1, s[52:53] sc1\n\t" \
    "s_waitcnt vmcnt(0)\n\t" \
    "v_cmp_eq_u32 vcc, -1, v30\n\t" \
    "s_cbranch_vccz hh_Hc15_%=\n\t" \
    "s_cmp_eq_u32 s68, 0\n\t" \
    "s_cbranch_scc1 hh_F18_%=\n\t" \
    "s_sub_u32 s68, s68, 1\n\t" \
    "s_sleep 2\n\t" \
    "s_branch hh_H13_%=\n\t" \
    "hh_F18_%=:\n\t" \
    "v_mov_b32 v31, 5767424\n\t" \
    "v_mov_b32 v32, 14\n\t" \
    "global_store_dword v31, v32, s[36:37] sc1\n\t" \
    "s_waitcnt vmcnt(0)\n\t" \
    "s_branch hh_Hc15_%=\n\t" \
    "hh_Hc15_%=:\n\t" \
    "s_waitcnt lgkmcnt(0)\n\t" \
    "s_mov_b32 s70, 116\n\t" \
    "hh_E19_%=:\n\t" \
    "s_memtime s[56:57]\n\t" \
    "s_memrealtime s[2:3]\n\t" \
    "s_nop 0\n\t" \
    "s_memrealtime s[4:5]\n\t" \
    "s_nop 0\n\t" \
    "s_memrealtime s[6:7]\n\t" \
    "s_nop 0\n\t" \
    "s_memrealtime s[8:9]\n\t" \
    "s_nop 0\n\t" \
    "s_memrealtime s[10:11]\n\t" \
    "s_nop 0\n\t" \
    "s_memrealtime s[12:13]\n\t" \
    "s_nop 0\n\t" \
    "s_memrealtime s[14:15]\n\t" \
    "s_nop 0\n\t" \
    "s_memrealtime s[16:17]\n\t" \
    "s_nop 0\n\t" \
    "s_memrealtime s[18:19]\n\t" \
    "s_nop 0\n\t" \
    "s_memrealtime s[20:21]\n\t" \
    "s_nop 0\n\t" \
    "s_memrealtime s[22:23]\n\t" \
    "s_nop 0\n\t" \
    "s_memrealtime s[24:25]\n\t" \
    "s_nop 0\n\t" \
    "s_memrealtime s[26:27]\n\t" \
    "s_nop 0\n\t" \
    "s_memrealtime s[28:29]\n\t" \
    "s_memtime s[54:55]\n\t" \
    "s_add_u32 s70, s70, 8\n\t" \
    "s_waitcnt lgkmcnt(0)\n\t" \
    "s_sub_u32 s69, s54, s56\n\t" \
    "s_cmp_gt_u32 s69, s70\n\t" \
    "s_cbranch_scc1 hh_E19_%=\n\t" \
    "s_add_u32 s2, s2, s4\n\t" \
    "s_add_u32 s6, s6, s8\n\t" \
    "s_add_u32 s10, s10, s12\n\t" \
    "s_add_u32 s14, s14, s16\n\t" \
    "s_add_u32 s18, s18, s20\n\t" \
    "s_add_u32 s22, s22, s24\n\t" \
    "s_add_u32 s2, s2, s6\n\t" \
    "s_add_u32 s10, s10, s14\n\t" \
    "s_add_u32 s18, s18, s22\n\t" \
    "s_add_u32 s2, s2, s26\n\t" \
    "s_add_u32 s10, s10, s18\n\t" \
    "s_add_u32 s2, s2, s10\n\t" \
    "s_mul_i32 s70, s28, 13\n\t" \
    "s_sub_u32 s70, s70, s2\n\t" \
    "s_min_u32 s70, s70, 52\n\t" \
    "s_mul_i32 s69, s69, s70\n\t" \
    "s_mul_hi_u32 s69, s69, 76695844\n\t" \
    "s_mov_b32 s75, s73\n\t" \
    "s_mov_b32 s76, s74\n\t" \
    "s_add_u32 s73, s56, s69\n\t" \
    "s_lshl_b32 s74, s28, 4\n\t" \
    "s_sub_u32 s69, s73, s75\n\t" \
    "s_sub_u32 s70, s74, s76\n\t" \
    "s_sub_u32 s71, s70, 1600\n\t" \
    "s_cmp_lt_u32 s71, 400000\n\t" \
    "s_cbranch_scc0 hh_K20_%=\n\t" \
    "v_cvt_f32_u32 v34, s69\n\t" \
    "v_cvt_f32_u32 v35, s70\n\t" \
    "v_rcp_f32 v36, v34\n\t" \
    "v_rcp_f32 v37, v35\n\t" \
    "v_mul_f32 v36, v36, v35\n\t" \
    "v_mul_f32 v37, v37, v34\n\t" \
    "v_mul_f32 v36, 0x4f800000, v36\n\t" \
    "v_mul_f32 v37, 0x4f000000, v37\n\t" \
    "v_cvt_u32_f32 v36, v36\n\t" \
    "v_cvt_u32_f32 v37, v37\n\t" \
    "v_readfirstlane_b32 s69, v36\n\t" \
    "v_readfirstlane_b32 s70, v37\n\t" \
    "s_nop 1\n\t" \
    "s_sub_u32 s69, s69, s78\n\t" \
    "s_ashr_i32 s69, s69, 1\n\t" \
    "s_add_u32 s78, s78, s69\n\t" \
    "s_sub_u32 s70, s70, s77\n\t" \
    "s_ashr_i32 s70, s70, 1\n\t" \
    "s_add_u32 s77, s77, s70\n\t" \
    "hh_K20_%=:\n\t" \
    "hh_Hok14_%=:\n\t" \
    "v_max_u32_dpp v30, v30, v30 row_ror:1 row_mask:0xf bank_mask:0xf\n\t" \
    "s_nop 1\n\t" \
    "v_max_u32_dpp v30, v30, v30 row_ror:2 row_mask:0xf bank_mask:0xf\n\t" \
    "s_nop 1\n\t" \
    "v_max_u32_dpp v30, v30, v30 row_ror:4 row_mask:0xf bank_mask:0xf\n\t" \
    "s_nop 1\n\t" \
    "v_max_u32_dpp v30, v30, v30 row_ror:8 row_mask:0xf bank_mask:0xf\n\t" \
    "s_nop 0\n\t" \
    "v_readlane_b32 s69, v30, 0\n\t" \
    "v_readlane_b32 s71, v30, 16\n\t" \
    "s_nop 1\n\t" \
    "s_max_u32 s81, s69, s71\n\t" \
    "s_mul_hi_u32 s69, s85, s78\n\t" \
    "s_add_u32 s79, s81, s69\n\t" \
    "s_add_u32 s79, s79, s87\n\t" \
    "s_cmp_lt_u32 s65, 5\n\t" \
    "s_cselect_b32 s69, 48, 0\n\t" \
    "s_add_u32 s79, s79, s69\n\t" \
    "s_add_u32 s80, s79, s88\n\t" \
    "s_add_u32 s65, s65, 1\n\t" \
    "s_cmp_gt_u32 s65, 62\n\t" \
    "s_cbranch_scc1 hh_end10_%=\n\t" \
    "v_lshrrev_b32 v39, 11, v3\n\t" \
    "v_sub_u32 v38, v38, v39\n\t" \
    "s_min_u32 s69, s65, 32\n\t" \
    "v_cmp_ge_i32 vcc, s69, v38\n\t" \
    "s_cbranch_vccnz hh_loop8_%=\n\t" \
    "v_lshrrev_b32 v20, 15, v11\n\t" \
    "v_add3_u32 v20, v20, s65, 1\n\t" \
    "v_min_u32 v20, 63, v20\n\t" \
    "v_mul_u32_u24 v20, v20, v11\n\t" \
    "v_xor_b32 v20, v20, v4\n\t" \
    "v_lshrrev_b32 v21, 15, v12\n\t" \
    "v_add3_u32 v21, v21, s65, 1\n\t" \
    "v_min_u32 v21, 63, v21\n\t" \
    "v_mul_u32_u24 v21, v21, v12\n\t" \
    "v_xor_b32 v21, v21, v5\n\t" \
    "v_lshrrev_b32 v22, 15, v13\n\t" \
    "v_add3_u32 v22, v22, s65, 1\n\t" \
    "v_min_u32 v22, 63, v22\n\t" \
    "v_mul_u32_u24 v22, v22, v13\n\t" \
    "v_xor_b32 v22, v22, v6\n\t" \
    "v_lshrrev_b32 v23, 15, v14\n\t" \
    "v_add3_u32 v23, v23, s65, 1\n\t" \
    "v_min_u32 v23, 63, v23\n\t" \
    "v_mul_u32_u24 v23, v23, v14\n\t" \
    "v_xor_b32 v23, v23, v7\n\t" \
    "v_lshrrev_b32 v24, 15, v15\n\t" \
    "v_add3_u32 v24, v24, s65, 1\n\t" \
    "v_min_u32 v24, 63, v24\n\t" \
    "v_mul_u32_u24 v24, v24, v15\n\t" \
    "v_xor_b32 v24, v24, v8\n\t" \
    "v_lshrrev_b32 v25, 15, v16\n\t" \
    "v_add3_u32 v25, v25, s65, 1\n\t" \
    "v_min_u32 v25, 63, v25\n\t" \
    "v_mul_u32_u24 v25, v25, v16\n\t" \
    "v_xor_b32 v25, v25, v9\n\t" \
    "v_lshrrev_b32 v26, 15, v17\n\t" \
    "v_add3_u32 v26, v26, s65, 1\n\t" \
    "v_min_u32 v26, 63, v26\n\t" \
    "v_mul_u32_u24 v26, v26, v17\n\t" \
    "v_xor_b32 v26, v26, v10\n\t" \
    "s_add_u32 s71, s79, s96\n\t" \
    "s_sub_u32 s70, s71, s74\n\t" \
    "s_max_i32 s70, s70, 0\n\t" \
    "s_min_i32 s70, s70, 16000\n\t" \
    "s_lshl_b32 s70, s70, 1\n\t" \
    "s_mul_hi_u32 s70, s70, s77\n\t" \
    "s_add_u32 s70, s70, s73\n\t" \
    "s_getpc_b64 s[30:31]\n\t" \
    "hh_Wp23_%=:\n\t" \
    "s_add_u32 s30, s30, hh_We24_%=-hh_Wp23_%=\n\t" \
    "s_addc_u32 s31, s31, 0\n\t" \
    "s_sub_u32 s70, s70, 96\n\t" \
    "hh_W22_%=:\n\t" \
    "s_memtime s[56:57]\n\t" \
    "s_waitcnt lgkmcnt(0)\n\t" \
    "s_sub_u32 s69, s70, s56\n\t" \
    "s_cmp_gt_i32 s69, 80\n\t" \
    "s_cbranch_scc1 hh_W22_%=\n\t" \
    "s_max_i32 s69, s69, 0\n\t" \
    "s_and_b32 s69, s69, -4\n\t" \
    "s_sub_u32 s30, s30, s69\n\t" \
    "s_subb_u32 s31, s31, 0\n\t" \
    "s_setpc_b64 s[30:31]\n\t" \
    "s_nop 0\n\t" \
    "s_nop 0\n\t" \
    "s_nop 0\n\t" \
    "s_nop 0\n\t" \
    "s_nop 0\n\t" \
    "s_nop 0\n\t" \
    "s_nop 0\n\t" \
    "s_nop 0\n\t" \
    "s_nop 0\n\t" \
    "s_nop 0\n\t" \
    "s_nop 0\n\t" \
    "s_nop 0\n\t" \
    "s_nop 0\n\t" \
    "s_nop 0\n\t" \
    "s_nop 0\n\t" \
    "s_nop 0\n\t" \
    "s_nop 0\n\t" \
    "s_nop 0\n\t" \
    "s_nop 0\n\t" \
    "s_nop 0\n\t" \
    "hh_We24_%=:\n\t" \
    "global_load_lds_dword v26, s[36:37] sc1\n\t" \
    "s_sleep 2\n\t" \
    "s_nop 5\n\t" \
    "global_load_lds_dword v25, s[36:37] sc1\n\t" \
    "s_sleep 2\n\t" \
    "s_nop 5\n\t" \
    "global_load_lds_dword v24, s[36:37] sc1\n\t" \
    "s_sleep 2\n\t" \
    "s_nop 5\n\t" \
    "global_load_lds_dword v23, s[36:37] sc1\n\t" \
    "s_sleep 2\n\t" \
    "s_nop 5\n\t" \
    "global_load_lds_dword v22, s[36:37] sc1\n\t" \
    "s_sleep 2\n\t" \
    "s_nop 5\n\t" \
    "global_load_lds_dword v21, s[36:37] sc1\n\t" \
    "s_sleep 2\n\t" \
    "s_nop 5\n\t" \
    "global_load_lds_dword v20, s[36:37] sc1\n\t" \
    "s_branch hh_loop8_%=\n\t" \
    "hh_end10_%=:\n\t" \
    "s_waitcnt vmcnt(0)\n\t" \
    "s_endpgm\n\t" \
    "hh_last_%=:\n\t" \
    ""
#define ASM_GX \
    "s_load_dwordx2 s[42:43], %3, 48\n\t" \
    "s_load_dwordx2 s[44:45], %3, 40\n\t" \
    "s_load_dwordx2 s[36:37], %3, 0x58\n\t" \
    "s_load_dwordx2 s[50:51], %3, 0x48\n\t" \
    "s_mov_b32 s66, %4\n\t" \
    "s_mov_b32 s67, %5\n\t" \
    "s_mov_b32 s68, 262144\n\t" \
    "s_mov_b32 s72, 0\n\t" \
    "s_mov_b32 s83, -1\n\t" \
    "s_mov_b32 s65, 0\n\t" \
    "s_lshl_b32 s82, s66, 10\n\t" \
    "s_waitcnt lgkmcnt(0)\n\t" \
    "s_mul_i32 s89, s66, 9216\n\t" \
    "s_mov_b32 s90, 0\n\t" \
    "s_add_u32 s46, s42, 2097152\n\t" \
    "s_addc_u32 s47, s43, 0\n\t" \
    "s_add_u32 m0, s82, 65536\n\t" \
    "s_nop 0\n\t" \
    "global_load_lds_dwordx4 v200, s[46:47]\n\t" \
    "s_add_u32 s46, s42, 2228224\n\t" \
    "s_addc_u32 s47, s43, 0\n\t" \
    "s_add_u32 m0, s82, 69632\n\t" \
    "s_nop 0\n\t" \
    "global_load_lds_dwordx4 v200, s[46:47]\n\t" \
    "s_add_u32 s46, s42, 2359296\n\t" \
    "s_addc_u32 s47, s43, 0\n\t" \
    "s_add_u32 m0, s82, 73728\n\t" \
    "s_nop 0\n\t" \
    "global_load_lds_dwordx4 v200, s[46:47]\n\t" \
    "s_add_u32 s46, s42, 2490368\n\t" \
    "s_addc_u32 s47, s43, 0\n\t" \
    "s_add_u32 m0, s82, 77824\n\t" \
    "s_nop 0\n\t" \
    "global_load_lds_dwordx4 v200, s[46:47]\n\t" \
    "s_add_u32 s46, s42, 2621440\n\t" \
    "s_addc_u32 s47, s43, 0\n\t" \
    "s_add_u32 m0, s82, 81920\n\t" \
    "s_nop 0\n\t" \
    "global_load_lds_dwordx4 v200, s[46:47]\n\t" \
    "s_add_u32 s46, s42, 2752512\n\t" \
    "s_addc_u32 s47, s43, 0\n\t" \
    "s_add_u32 m0, s82, 86016\n\t" \
    "s_nop 0\n\t" \
    "global_load_lds_dwordx4 v200, s[46:47]\n\t" \
    "s_add_u32 s46, s42, 2883584\n\t" \
    "s_addc_u32 s47, s43, 0\n\t" \
    "s_add_u32 m0, s82, 90112\n\t" \
    "s_nop 0\n\t" \
    "global_load_lds_dwordx4 v200, s[46:47]\n\t" \
    "s_add_u32 s46, s42, 3014656\n\t" \
    "s_addc_u32 s47, s43, 0\n\t" \
    "s_add_u32 m0, s82, 94208\n\t" \
    "s_nop 0\n\t" \
    "global_load_lds_dwordx4 v200, s[46:47]\n\t" \
    "s_add_u32 s46, s42, 3145728\n\t" \
    "s_addc_u32 s47, s43, 0\n\t" \
    "s_add_u32 m0, s82, 98304\n\t" \
    "s_nop 0\n\t" \
    "global_load_lds_dwordx4 v200, s[46:47]\n\t" \
    "s_add_u32 s46, s42, 3276800\n\t" \
    "s_addc_u32 s47, s43, 0\n\t" \
    "s_add_u32 m0, s82, 102400\n\t" \
    "s_nop 0\n\t" \
    "global_load_lds_dwordx4 v200, s[46:47]\n\t" \
    "s_add_u32 s46, s42, 3407872\n\t" \
    "s_addc_u32 s47, s43, 0\n\t" \
    "s_add_u32 m0, s82, 106496\n\t" \
    "s_nop 0\n\t" \
    "global_load_lds_dwordx4 v200, s[46:47]\n\t" \
    "s_add_u32 s46, s42, 3538944\n\t" \
    "s_addc_u32 s47, s43, 0\n\t" \
    "s_add_u32 m0, s82, 110592\n\t" \
    "s_nop 0\n\t" \
    "global_load_lds_dwordx4 v200, s[46:47]\n\t" \
    "s_add_u32 s46, s42, 3670016\n\t" \
    "s_addc_u32 s47, s43, 0\n\t" \
    "s_add_u32 m0, s82, 114688\n\t" \
    "s_nop 0\n\t" \
    "global_load_lds_dwordx4 v200, s[46:47]\n\t" \
    "s_add_u32 s46, s42, 3801088\n\t" \
    "s_addc_u32 s47, s43, 0\n\t" \
    "s_add_u32 m0, s82, 118784\n\t" \
    "s_nop 0\n\t" \
    "global_load_lds_dwordx4 v200, s[46:47]\n\t" \
    "s_add_u32 s46, s42, 3932160\n\t" \
    "s_addc_u32 s47, s43, 0\n\t" \
    "s_add_u32 m0, s82, 122880\n\t" \
    "s_nop 0\n\t" \
    "global_load_lds_dwordx4 v200, s[46:47]\n\t" \
    "s_add_u32 s46, s42, 4063232\n\t" \
    "s_addc_u32 s47, s43, 0\n\t" \
    "s_add_u32 m0, s82, 126976\n\t" \
    "s_nop 0\n\t" \
    "global_load_lds_dwordx4 v200, s[46:47]\n\t" \
    "s_add_u32 s46, s42, 4194304\n\t" \
    "s_addc_u32 s47, s43, 0\n\t" \
    "s_add_u32 m0, s82, 131072\n\t" \
    "s_nop 0\n\t" \
    "global_load_lds_dwordx4 v200, s[46:47]\n\t" \
    "s_add_u32 s46, s42, 4325376\n\t" \
    "s_addc_u32 s47, s43, 0\n\t" \
    "s_add_u32 m0, s82, 135168\n\t" \
    "s_nop 0\n\t" \
    "global_load_lds_dwordx4 v200, s[46:47]\n\t" \
    "s_add_u32 s46, s42, 4456448\n\t" \
    "s_addc_u32 s47, s43, 0\n\t" \
    "s_add_u32 m0, s82, 139264\n\t" \
    "s_nop 0\n\t" \
    "global_load_lds_dwordx4 v200, s[46:47]\n\t" \
    "s_add_u32 s46, s42, 4587520\n\t" \
    "s_addc_u32 s47, s43, 0\n\t" \
    "s_add_u32 m0, s82, 143360\n\t" \
    "s_nop 0\n\t" \
    "global_load_lds_dwordx4 v200, s[46:47]\n\t" \
    "global_load_dwordx4 v[120:123], v203, s[50:51]\n\t" \
    "global_load_dwordx4 v[124:127], v204, s[50:51]\n\t" \
    "global_load_dwordx4 v[128:131], v205, s[50:51]\n\t" \
    "global_load_dwordx4 v[132:135], v206, s[50:51]\n\t" \
    "global_load_dwordx4 v[136:139], v207, s[50:51]\n\t" \
    "global_load_dwordx4 v[140:143], v208, s[50:51]\n\t" \
    "global_load_dwordx4 v[144:147], v209, s[50:51]\n\t" \
    "global_load_dwordx4 v[148:151], v210, s[50:51]\n\t" \
    "global_load_dwordx4 v[152:155], v211, s[50:51]\n\t" \
    "s_add_u32 s52, s50, 36864\n\t" \
    "s_addc_u32 s53, s51, 0\n\t" \
    "global_load_dwordx4 v[72:75], v203, s[52:53]\n\t" \
    "global_load_dwordx4 v[76:79], v204, s[52:53]\n\t" \
    "global_load_dwordx4 v[80:83], v205, s[52:53]\n\t" \
    "global_load_dwordx4 v[84:87], v206, s[52:53]\n\t" \
    "global_load_dwordx4 v[88:91], v207, s[52:53]\n\t" \
    "global_load_dwordx4 v[92:95], v208, s[52:53]\n\t" \
    "global_load_dwordx4 v[96:99], v209, s[52:53]\n\t" \
    "global_load_dwordx4 v[100:103], v210, s[52:53]\n\t" \
    "global_load_dwordx4 v[104:107], v211, s[52:53]\n\t" \
    "s_waitcnt vmcnt(50) lgkmcnt(0)\n\t" \
    "s_barrier\n\t" \
    "ds_read_b64_tr_b16 a[144:145], v201 offset:0\n\t" \
    "ds_read_b64_tr_b16 a[146:147], v201 offset:1024\n\t" \
    "ds_read_b64_tr_b16 a[148:149], v201 offset:8192\n\t" \
    "ds_read_b64_tr_b16 a[150:151], v201 offset:9216\n\t" \
    "ds_read_b64_tr_b16 a[216:217], v202 offset:0\n\t" \
    "ds_read_b64_tr_b16 a[218:219], v202 offset:1024\n\t" \
    "ds_read_b64_tr_b16 a[220:221], v202 offset:8192\n\t" \
    "ds_read_b64_tr_b16 a[222:223], v202 offset:9216\n\t" \
    "s_waitcnt vmcnt(46) lgkmcnt(0)\n\t" \
    "s_barrier\n\t" \
    "ds_read_b64_tr_b16 a[152:153], v201 offset:16384\n\t" \
    "ds_read_b64_tr_b16 a[154:155], v201 offset:17408\n\t" \
    "ds_read_b64_tr_b16 a[156:157], v201 offset:24576\n\t" \
    "ds_read_b64_tr_b16 a[158:159], v201 offset:25600\n\t" \
    "ds_read_b64_tr_b16 a[224:225], v202 offset:16384\n\t" \
    "ds_read_b64_tr_b16 a[226:227], v202 offset:17408\n\t" \
    "ds_read_b64_tr_b16 a[228:229], v202 offset:24576\n\t" \
    "ds_read_b64_tr_b16 a[230:231], v202 offset:25600\n\t" \
    "s_waitcnt vmcnt(42) lgkmcnt(0)\n\t" \
    "s_barrier\n\t" \
    "ds_read_b64_tr_b16 a[160:161], v201 offset:32768\n\t" \
    "ds_read_b64_tr_b16 a[162:163], v201 offset:33792\n\t" \
    "ds_read_b64_tr_b16 a[164:165], v201 offset:40960\n\t" \
    "ds_read_b64_tr_b16 a[166:167], v201 offset:41984\n\t" \
    "ds_read_b64_tr_b16 a[232:233], v202 offset:32768\n\t" \
    "ds_read_b64_tr_b16 a[234:235], v202 offset:33792\n\t" \
    "ds_read_b64_tr_b16 a[236:237], v202 offset:40960\n\t" \
    "ds_read_b64_tr_b16 a[238:239], v202 offset:41984\n\t" \
    "s_waitcnt vmcnt(38) lgkmcnt(0)\n\t" \
    "s_barrier\n\t" \
    "ds_read_b64_tr_b16 a[168:169], v201 offset:49152\n\t" \
    "ds_read_b64_tr_b16 a[170:171], v201 offset:50176\n\t" \
    "ds_read_b64_tr_b16 a[172:173], v201 offset:57344\n\t" \
    "ds_read_b64_tr_b16 a[174:175], v201 offset:58368\n\t" \
    "ds_read_b64_tr_b16 a[240:241], v202 offset:49152\n\t" \
    "ds_read_b64_tr_b16 a[242:243], v202 offset:50176\n\t" \
    "ds_read_b64_tr_b16 a[244:245], v202 offset:57344\n\t" \
    "ds_read_b64_tr_b16 a[246:247], v202 offset:58368\n\t" \
    "v_mbcnt_lo_u32_b32 v164, -1, 0\n\t" \
    "v_mbcnt_hi_u32_b32 v164, -1, v164\n\t" \
    "v_lshlrev_b32 v164, 6, v164\n\t" \
    "v_add_u32 v164, 6225920, v164\n\t" \
    "v_mov_b32 v165, 6234112\n\t" \
    "global_load_dword v165, v165, s[36:37] sc1\n\t" \
    "global_load_dwordx2 v[166:167], v164, s[36:37] sc1\n\t" \
    "global_load_dword v168, v164, s[36:37] offset:2048 sc1\n\t" \
    "s_waitcnt vmcnt(37) lgkmcnt(0)\n\t" \
    "s_barrier\n\t" \
    "v_add_u32 v201, 0x10000, v201\n\t" \
    "v_add_u32 v202, 0x10000, v202\n\t" \
    "ds_read_b64_tr_b16 a[176:177], v201 offset:0\n\t" \
    "ds_read_b64_tr_b16 a[178:179], v201 offset:1024\n\t" \
    "ds_read_b64_tr_b16 a[180:181], v201 offset:8192\n\t" \
    "ds_read_b64_tr_b16 a[182:183], v201 offset:9216\n\t" \
    "ds_read_b64_tr_b16 a[248:249], v202 offset:0\n\t" \
    "ds_read_b64_tr_b16 a[250:251], v202 offset:1024\n\t" \
    "ds_read_b64_tr_b16 a[252:253], v202 offset:8192\n\t" \
    "ds_read_b64_tr_b16 a[254:255], v202 offset:9216\n\t" \
    "s_waitcnt vmcnt(33) lgkmcnt(0)\n\t" \
    "s_barrier\n\t" \
    "ds_read_b64_tr_b16 a[184:185], v201 offset:16384\n\t" \
    "ds_read_b64_tr_b16 a[186:187], v201 offset:17408\n\t" \
    "ds_read_b64_tr_b16 a[188:189], v201 offset:24576\n\t" \
    "ds_read_b64_tr_b16 a[190:191], v201 offset:25600\n\t" \
    "ds_read_b64_tr_b16 v[224:225], v202 offset:16384\n\t" \
    "ds_read_b64_tr_b16 v[226:227], v202 offset:17408\n\t" \
    "ds_read_b64_tr_b16 v[228:229], v202 offset:24576\n\t" \
    "ds_read_b64_tr_b16 v[230:231], v202 offset:25600\n\t" \
    "s_waitcnt vmcnt(29) lgkmcnt(0)\n\t" \
    "s_barrier\n\t" \
    "ds_read_b64_tr_b16 a[192:193], v201 offset:32768\n\t" \
    "ds_read_b64_tr_b16 a[194:195], v201 offset:33792\n\t" \
    "ds_read_b64_tr_b16 a[196:197], v201 offset:40960\n\t" \
    "ds_read_b64_tr_b16 a[198:199], v201 offset:41984\n\t" \
    "ds_read_b64_tr_b16 v[232:233], v202 offset:32768\n\t" \
    "ds_read_b64_tr_b16 v[234:235], v202 offset:33792\n\t" \
    "ds_read_b64_tr_b16 v[236:237], v202 offset:40960\n\t" \
    "ds_read_b64_tr_b16 v[238:239], v202 offset:41984\n\t" \
    "s_waitcnt vmcnt(25) lgkmcnt(0)\n\t" \
    "s_barrier\n\t" \
    "ds_read_b64_tr_b16 a[200:201], v201 offset:49152\n\t" \
    "ds_read_b64_tr_b16 a[202:203], v201 offset:50176\n\t" \
    "ds_read_b64_tr_b16 a[204:205], v201 offset:57344\n\t" \
    "ds_read_b64_tr_b16 a[206:207], v201 offset:58368\n\t" \
    "ds_read_b64_tr_b16 v[240:241], v202 offset:49152\n\t" \
    "ds_read_b64_tr_b16 v[242:243], v202 offset:50176\n\t" \
    "ds_read_b64_tr_b16 v[244:245], v202 offset:57344\n\t" \
    "ds_read_b64_tr_b16 v[246:247], v202 offset:58368\n\t" \
    "s_waitcnt vmcnt(21) lgkmcnt(0)\n\t" \
    "s_barrier\n\t" \
    "v_add_u32 v201, 0x10000, v201\n\t" \
    "v_add_u32 v202, 0x10000, v202\n\t" \
    "ds_read_b64_tr_b16 a[208:209], v201 offset:0\n\t" \
    "ds_read_b64_tr_b16 a[210:211], v201 offset:1024\n\t" \
    "ds_read_b64_tr_b16 a[212:213], v201 offset:8192\n\t" \
    "ds_read_b64_tr_b16 a[214:215], v201 offset:9216\n\t" \
    "ds_read_b64_tr_b16 v[248:249], v202 offset:0\n\t" \
    "ds_read_b64_tr_b16 v[250:251], v202 offset:1024\n\t" \
    "ds_read_b64_tr_b16 v[252:253], v202 offset:8192\n\t" \
    "ds_read_b64_tr_b16 v[254:255], v202 offset:9216\n\t" \
    "v_add_u32 v201, 0xfffe0000, v201\n\t" \
    "v_add_u32 v202, 0xfffe0000, v202\n\t" \
    "s_waitcnt lgkmcnt(0)\n\t" \
    "s_barrier\n\t" \
    "s_waitcnt vmcnt(0)\n\t" \
    "ds_write_b128 v3, v[120:123] offset:0\n\t" \
    "ds_write_b128 v3, v[124:127] offset:1024\n\t" \
    "ds_write_b128 v3, v[128:131] offset:2048\n\t" \
    "ds_write_b128 v3, v[132:135] offset:3072\n\t" \
    "ds_write_b128 v3, v[136:139] offset:4096\n\t" \
    "ds_write_b128 v3, v[140:143] offset:5120\n\t" \
    "ds_write_b128 v3, v[144:147] offset:6144\n\t" \
    "ds_write_b128 v3, v[148:151] offset:7168\n\t" \
    "ds_write_b128 v3, v[152:155] offset:8192\n\t" \
    "ds_write_b128 v3, v[72:75] offset:36864\n\t" \
    "ds_write_b128 v3, v[76:79] offset:37888\n\t" \
    "ds_write_b128 v3, v[80:83] offset:38912\n\t" \
    "ds_write_b128 v3, v[84:87] offset:39936\n\t" \
    "ds_write_b128 v3, v[88:91] offset:40960\n\t" \
    "ds_write_b128 v3, v[92:95] offset:41984\n\t" \
    "ds_write_b128 v3, v[96:99] offset:43008\n\t" \
    "ds_write_b128 v3, v[100:103] offset:44032\n\t" \
    "ds_write_b128 v3, v[104:107] offset:45056\n\t" \
    "gx_C1_%=:\n\t" \
    "v_cmp_ne_u32 s[54:55], v166, v168\n\t" \
    "v_sub_u32 v166, v166, v165\n\t" \
    "v_cmp_ne_u32 vcc, 1, v166\n\t" \
    "s_nop 1\n\t" \
    "s_or_b64 vcc, vcc, s[54:55]\n\t" \
    "s_cbranch_scc0 gx_Cok2_%=\n\t" \
    "s_cmp_eq_u32 s68, 0\n\t" \
    "s_cbranch_scc1 gx_F4_%=\n\t" \
    "s_sub_u32 s68, s68, 1\n\t" \
    "s_sleep 6\n\t" \
    "s_branch gx_Cag3_%=\n\t" \
    "gx_F4_%=:\n\t" \
    "v_mov_b32 v168, 5767424\n\t" \
    "v_mov_b32 v169, 4\n\t" \
    "global_store_dword v168, v169, s[36:37] sc1\n\t" \
    "s_waitcnt vmcnt(0)\n\t" \
    "s_branch gx_Cok2_%=\n\t" \
    "gx_Cag3_%=:\n\t" \
    "global_load_dwordx2 v[166:167], v164, s[36:37] sc1\n\t" \
    "global_load_dword v168, v164, s[36:37] offset:2048 sc1\n\t" \
    "s_waitcnt vmcnt(0)\n\t" \
    "s_branch gx_C1_%=\n\t" \
    "gx_Cok2_%=:\n\t" \
    "v_readlane_b32 s69, v167, 2\n\t" \
    "v_readlane_b32 s70, v167, 3\n\t" \
    "s_nop 1\n\t" \
    "s_cmp_lt_u32 s70, s69\n\t" \
    "s_cselect_b32 s69, 0, 8192\n\t" \
    "s_add_u32 s100, s36, s69\n\t" \
    "s_addc_u32 s101, s37, 0\n\t" \
    "s_cmp_lg_u32 s66, 0\n\t" \
    "s_cbranch_scc1 gx_Ca5_%=\n\t" \
    "v_mov_b32 v164, 5768192\n\t" \
    "s_mov_b64 exec, 1\n\t" \
    "global_atomic_add v164, v166, s[36:37] sc1\n\t" \
    "s_mov_b64 exec, -1\n\t" \
    "gx_Ca5_%=:\n\t" \
    "s_waitcnt lgkmcnt(0)\n\t" \
    "s_barrier\n\t" \
    "ds_read_b128 v[24:27], v1 offset:0\n\t" \
    "ds_read_b128 v[28:31], v1 offset:18432\n\t" \
    "ds_read_b128 v[32:35], v2 offset:0\n\t" \
    "ds_read_b128 v[36:39], v2 offset:18432\n\t" \
    "ds_read_b128 v[40:43], v1 offset:128\n\t" \
    "ds_read_b128 v[44:47], v1 offset:18560\n\t" \
    "s_add_u32 s91, s89, 0x12000\n\t" \
    "s_add_u32 s85, s91, s89\n\t" \
    "s_add_u32 s52, s50, 73728\n\t" \
    "s_addc_u32 s53, s51, 0\n\t" \
    "s_mul_i32 s69, s67, 8192\n\t" \
    "s_add_u32 s69, s69, 175112192\n\t" \
    "s_add_u32 s42, s36, s69\n\t" \
    "s_addc_u32 s43, s37, 0\n\t" \
    "s_mov_b64 s[40:41], s[42:43]\n\t" \
    "gx_l16_%=:\n\t" \
    "s_waitcnt lgkmcnt(5)\n\t" \
    "v_mfma_f32_16x16x32_bf16 v[88:91], a[144:147], v[24:27], 0\n\t" \
    "ds_read_b128 v[48:51], v2 offset:128\n\t" \
    "s_waitcnt lgkmcnt(5)\n\t" \
    "v_mfma_f32_16x16x32_bf16 v[92:95], a[144:147], v[28:31], 0\n\t" \
    "ds_read_b128 v[52:55], v2 offset:18560\n\t" \
    "v_mfma_f32_16x16x32_bf16 v[96:99], a[216:219], v[24:27], 0\n\t" \
    "s_add_u32 m0, s91, 0\n\t" \
    "v_mfma_f32_16x16x32_bf16 v[100:103], a[216:219], v[28:31], 0\n\t" \
    "global_load_lds_dwordx4 v203, s[52:53]\n\t" \
    "s_waitcnt lgkmcnt(5)\n\t" \
    "v_mfma_f32_16x16x32_bf16 v[88:91], a[148:151], v[32:35], v[88:91]\n\t" \
    "ds_read_b128 v[56:59], v1 offset:256\n\t" \
    "s_waitcnt lgkmcnt(5)\n\t" \
    "v_mfma_f32_16x16x32_bf16 v[92:95], a[148:151], v[36:39], v[92:95]\n\t" \
    "ds_read_b128 v[60:63], v1 offset:18688\n\t" \
    "v_mfma_f32_16x16x32_bf16 v[96:99], a[220:223], v[32:35], v[96:99]\n\t" \
    "s_add_u32 m0, s91, 1024\n\t" \
    "v_mfma_f32_16x16x32_bf16 v[100:103], a[220:223], v[36:39], v[100:103]\n\t" \
    "global_load_lds_dwordx4 v204, s[52:53]\n\t" \
    "s_waitcnt lgkmcnt(5)\n\t" \
    "v_mfma_f32_16x16x32_bf16 v[88:91], a[152:155], v[40:43], v[88:91]\n\t" \
    "ds_read_b128 v[64:67], v2 offset:256\n\t" \
    "s_waitcnt lgkmcnt(5)\n\t" \
    "v_mfma_f32_16x16x32_bf16 v[92:95], a[152:155], v[44:47], v[92:95]\n\t" \
    "ds_read_b128 v[68:71], v2 offset:18688\n\t" \
    "v_cvt_pk_bf16_f32 v104, v72, v73\n\t" \
    "v_mfma_f32_16x16x32_bf16 v[96:99], a[224:227], v[40:43], v[96:99]\n\t" \
    "s_add_u32 m0, s91, 2048\n\t" \
    "v_cvt_pk_bf16_f32 v105, v74, v75\n\t" \
    "v_mfma_f32_16x16x32_bf16 v[100:103], a[224:227], v[44:47], v[100:103]\n\t" \
    "global_load_lds_dwordx4 v205, s[52:53]\n\t" \
    "s_waitcnt lgkmcnt(5)\n\t" \
    "v_mfma_f32_16x16x32_bf16 v[88:91], a[156:159], v[48:51], v[88:91]\n\t" \
    "ds_read_b128 v[24:27], v1 offset:384\n\t" \
    "s_waitcnt lgkmcnt(5)\n\t" \
    "v_mfma_f32_16x16x32_bf16 v[92:95], a[156:159], v[52:55], v[92:95]\n\t" \
    "ds_read_b128 v[28:31], v1 offset:18816\n\t" \
    "v_cvt_pk_bf16_f32 v106, v80, v81\n\t" \
    "v_mfma_f32_16x16x32_bf16 v[96:99], a[228:231], v[48:51], v[96:99]\n\t" \
    "s_add_u32 m0, s91, 3072\n\t" \
    "v_cvt_pk_bf16_f32 v107, v82, v83\n\t" \
    "v_mfma_f32_16x16x32_bf16 v[100:103], a[228:231], v[52:55], v[100:103]\n\t" \
    "global_load_lds_dwordx4 v206, s[52:53]\n\t" \
    "s_waitcnt lgkmcnt(5)\n\t" \
    "v_mfma_f32_16x16x32_bf16 v[88:91], a[160:163], v[56:59], v[88:91]\n\t" \
    "ds_read_b128 v[32:35], v2 offset:384\n\t" \
    "s_waitcnt lgkmcnt(5)\n\t" \
    "v_mfma_f32_16x16x32_bf16 v[92:95], a[160:163], v[60:63], v[92:95]\n\t" \
    "ds_read_b128 v[36:39], v2 offset:18816\n\t" \
    "global_store_dwordx4 v8, v[104:107], s[40:41] offset:0 sc1\n\t" \
    "v_mfma_f32_16x16x32_bf16 v[96:99], a[232:235], v[56:59], v[96:99]\n\t" \
    "s_add_u32 m0, s91, 4096\n\t" \
    "v_cvt_pk_bf16_f32 v108, v76, v77\n\t" \
    "v_mfma_f32_16x16x32_bf16 v[100:103], a[232:235], v[60:63], v[100:103]\n\t" \
    "global_load_lds_dwordx4 v207, s[52:53]\n\t" \
    "s_waitcnt lgkmcnt(5)\n\t" \
    "v_mfma_f32_16x16x32_bf16 v[88:91], a[164:167], v[64:67], v[88:91]\n\t" \
    "ds_read_b128 v[40:43], v1 offset:512\n\t" \
    "s_waitcnt lgkmcnt(5)\n\t" \
    "v_mfma_f32_16x16x32_bf16 v[92:95], a[164:167], v[68:71], v[92:95]\n\t" \
    "ds_read_b128 v[44:47], v1 offset:18944\n\t" \
    "v_cvt_pk_bf16_f32 v109, v78, v79\n\t" \
    "v_mfma_f32_16x16x32_bf16 v[96:99], a[236:239], v[64:67], v[96:99]\n\t" \
    "s_add_u32 m0, s91, 5120\n\t" \
    "v_cvt_pk_bf16_f32 v110, v84, v85\n\t" \
    "v_mfma_f32_16x16x32_bf16 v[100:103], a[236:239], v[68:71], v[100:103]\n\t" \
    "global_load_lds_dwordx4 v208, s[52:53]\n\t" \
    "s_waitcnt lgkmcnt(5)\n\t" \
    "v_mfma_f32_16x16x32_bf16 v[88:91], a[168:171], v[24:27], v[88:91]\n\t" \
    "ds_read_b128 v[48:51], v2 offset:512\n\t" \
    "s_waitcnt lgkmcnt(5)\n\t" \
    "v_mfma_f32_16x16x32_bf16 v[92:95], a[168:171], v[28:31], v[92:95]\n\t" \
    "ds_read_b128 v[52:55], v2 offset:18944\n\t" \
    "v_cvt_pk_bf16_f32 v111, v86, v87\n\t" \
    "v_mfma_f32_16x16x32_bf16 v[96:99], a[240:243], v[24:27], v[96:99]\n\t" \
    "s_add_u32 m0, s91, 6144\n\t" \
    "global_store_dwordx4 v8, v[108:111], s[40:41] offset:1024 sc1\n\t" \
    "v_mfma_f32_16x16x32_bf16 v[100:103], a[240:243], v[28:31], v[100:103]\n\t" \
    "global_load_lds_dwordx4 v209, s[52:53]\n\t" \
    "s_waitcnt lgkmcnt(5)\n\t" \
    "v_mfma_f32_16x16x32_bf16 v[88:91], a[172:175], v[32:35], v[88:91]\n\t" \
    "ds_read_b128 v[56:59], v1 offset:640\n\t" \
    "s_waitcnt lgkmcnt(5)\n\t" \
    "v_mfma_f32_16x16x32_bf16 v[92:95], a[172:175], v[36:39], v[92:95]\n\t" \
    "ds_read_b128 v[60:63], v1 offset:19072\n\t" \
    "v_mfma_f32_16x16x32_bf16 v[96:99], a[244:247], v[32:35], v[96:99]\n\t" \
    "s_add_u32 m0, s91, 7168\n\t" \
    "v_mfma_f32_16x16x32_bf16 v[100:103], a[244:247], v[36:39], v[100:103]\n\t" \
    "global_load_lds_dwordx4 v210, s[52:53]\n\t" \
    "s_waitcnt lgkmcnt(5)\n\t" \
    "v_mfma_f32_16x16x32_bf16 v[88:91], a[176:179], v[40:43], v[88:91]\n\t" \
    "ds_read_b128 v[64:67], v2 offset:640\n\t" \
    "s_waitcnt lgkmcnt(5)\n\t" \
    "v_mfma_f32_16x16x32_bf16 v[92:95], a[176:179], v[44:47], v[92:95]\n\t" \
    "ds_read_b128 v[68:71], v2 offset:19072\n\t" \
    "v_mfma_f32_16x16x32_bf16 v[96:99], a[248:251], v[40:43], v[96:99]\n\t" \
    "s_add_u32 m0, s91, 8192\n\t" \
    "v_mfma_f32_16x16x32_bf16 v[100:103], a[248:251], v[44:47], v[100:103]\n\t" \
    "global_load_lds_dwordx4 v211, s[52:53]\n\t" \
    "s_waitcnt lgkmcnt(5)\n\t" \
    "v_mfma_f32_16x16x32_bf16 v[88:91], a[180:183], v[48:51], v[88:91]\n\t" \
    "ds_read_b128 v[24:27], v1 offset:768\n\t" \
    "s_waitcnt lgkmcnt(5)\n\t" \
    "v_mfma_f32_16x16x32_bf16 v[92:95], a[180:183], v[52:55], v[92:95]\n\t" \
    "ds_read_b128 v[28:31], v1 offset:19200\n\t" \
    "v_mfma_f32_16x16x32_bf16 v[96:99], a[252:255], v[48:51], v[96:99]\n\t" \
    "v_mfma_f32_16x16x32_bf16 v[100:103], a[252:255], v[52:55], v[100:103]\n\t" \
    "s_waitcnt lgkmcnt(5)\n\t" \
    "v_mfma_f32_16x16x32_bf16 v[88:91], a[184:187], v[56:59], v[88:91]\n\t" \
    "ds_read_b128 v[32:35], v2 offset:768\n\t" \
    "s_waitcnt lgkmcnt(5)\n\t" \
    "v_mfma_f32_16x16x32_bf16 v[92:95], a[184:187], v[60:63], v[92:95]\n\t" \
    "ds_read_b128 v[36:39], v2 offset:19200\n\t" \
    "v_mfma_f32_16x16x32_bf16 v[96:99], v[224:227], v[56:59], v[96:99]\n\t" \
    "s_waitcnt vmcnt(11)\n\t" \
    "s_barrier\n\t" \
    "v_mfma_f32_16x16x32_bf16 v[100:103], v[224:227], v[60:63], v[100:103]\n\t" \
    "s_cmp_lg_u32 s66, 0\n\t" \
    "s_cbranch_scc1 gx_SF8_%=\n\t" \
    "s_lshl_b32 s69, s67, 7\n\t" \
    "s_add_u32 s69, s69, 3149824\n\t" \
    "s_add_u32 s70, s65, -2\n\t" \
    "v_mov_b32 v156, s69\n\t" \
    "v_mov_b32 v157, s70\n\t" \
    "s_mov_b64 exec, 1\n\t" \
    "global_store_dword v156, v157, s[100:101] sc1\n\t" \
    "s_mov_b64 exec, -1\n\t" \
    "gx_SF8_%=:\n\t" \
    "s_waitcnt lgkmcnt(5)\n\t" \
    "v_mfma_f32_16x16x32_bf16 v[88:91], a[188:191], v[64:67], v[88:91]\n\t" \
    "ds_read_b128 v[40:43], v1 offset:896\n\t" \
    "s_waitcnt lgkmcnt(5)\n\t" \
    "v_mfma_f32_16x16x32_bf16 v[92:95], a[188:191], v[68:71], v[92:95]\n\t" \
    "ds_read_b128 v[44:47], v1 offset:19328\n\t" \
    "v_mfma_f32_16x16x32_bf16 v[96:99], v[228:231], v[64:67], v[96:99]\n\t" \
    "v_add_u32 v22, 0x9000, v1\n\t" \
    "v_add_u32 v23, 0x9000, v2\n\t" \
    "v_mfma_f32_16x16x32_bf16 v[100:103], v[228:231], v[68:71], v[100:103]\n\t" \
    "s_add_u32 s48, s52, 36864\n\t" \
    "s_addc_u32 s49, s53, 0\n\t" \
    "s_waitcnt lgkmcnt(5)\n\t" \
    "v_mfma_f32_16x16x32_bf16 v[88:91], a[192:195], v[24:27], v[88:91]\n\t" \
    "ds_read_b128 v[48:51], v2 offset:896\n\t" \
    "s_waitcnt lgkmcnt(5)\n\t" \
    "v_mfma_f32_16x16x32_bf16 v[92:95], a[192:195], v[28:31], v[92:95]\n\t" \
    "ds_read_b128 v[52:55], v2 offset:19328\n\t" \
    "v_mfma_f32_16x16x32_bf16 v[96:99], v[232:235], v[24:27], v[96:99]\n\t" \
    "s_mul_i32 s70, s67, 7\n\t" \
    "s_add_u32 s69, s65, 0\n\t" \
    "v_mfma_f32_16x16x32_bf16 v[100:103], v[232:235], v[28:31], v[100:103]\n\t" \
    "s_xor_b32 s69, s69, s70\n\t" \
    "s_and_b32 s69, s69, 63\n\t" \
    "s_waitcnt lgkmcnt(5)\n\t" \
    "v_mfma_f32_16x16x32_bf16 v[88:91], a[196:199], v[32:35], v[88:91]\n\t" \
    "ds_read_b128 v[56:59], v1 offset:1024\n\t" \
    "s_waitcnt lgkmcnt(5)\n\t" \
    "v_mfma_f32_16x16x32_bf16 v[92:95], a[196:199], v[36:39], v[92:95]\n\t" \
    "ds_read_b128 v[60:63], v1 offset:19456\n\t" \
    "v_mfma_f32_16x16x32_bf16 v[96:99], v[236:239], v[32:35], v[96:99]\n\t" \
    "s_lshl_b32 s69, s69, 13\n\t" \
    "s_mul_i32 s70, s67, 524288\n\t" \
    "v_mfma_f32_16x16x32_bf16 v[100:103], v[236:239], v[36:39], v[100:103]\n\t" \
    "s_add_u32 s69, s69, s70\n\t" \
    "s_add_u32 s69, s69, 23068672\n\t" \
    "s_waitcnt lgkmcnt(5)\n\t" \
    "v_mfma_f32_16x16x32_bf16 v[88:91], a[200:203], v[40:43], v[88:91]\n\t" \
    "ds_read_b128 v[64:67], v2 offset:1024\n\t" \
    "s_waitcnt lgkmcnt(5)\n\t" \
    "v_mfma_f32_16x16x32_bf16 v[92:95], a[200:203], v[44:47], v[92:95]\n\t" \
    "ds_read_b128 v[68:71], v2 offset:19456\n\t" \
    "v_mfma_f32_16x16x32_bf16 v[96:99], v[240:243], v[40:43], v[96:99]\n\t" \
    "s_add_u32 s38, s36, s69\n\t" \
    "s_addc_u32 s39, s37, 0\n\t" \
    "v_mfma_f32_16x16x32_bf16 v[100:103], v[240:243], v[44:47], v[100:103]\n\t" \
    "s_waitcnt lgkmcnt(5)\n\t" \
    "v_mfma_f32_16x16x32_bf16 v[88:91], a[204:207], v[48:51], v[88:91]\n\t" \
    "ds_read_b128 v[24:27], v22 offset:0\n\t" \
    "s_waitcnt lgkmcnt(5)\n\t" \
    "v_mfma_f32_16x16x32_bf16 v[92:95], a[204:207], v[52:55], v[92:95]\n\t" \
    "ds_read_b128 v[28:31], v22 offset:18432\n\t" \
    "v_mfma_f32_16x16x32_bf16 v[96:99], v[244:247], v[48:51], v[96:99]\n\t" \
    "v_mfma_f32_16x16x32_bf16 v[100:103], v[244:247], v[52:55], v[100:103]\n\t" \
    "s_waitcnt lgkmcnt(5)\n\t" \
    "v_mfma_f32_16x16x32_bf16 v[88:91], a[208:211], v[56:59], v[88:91]\n\t" \
    "ds_read_b128 v[32:35], v23 offset:0\n\t" \
    "s_waitcnt lgkmcnt(5)\n\t" \
    "v_mfma_f32_16x16x32_bf16 v[92:95], a[208:211], v[60:63], v[92:95]\n\t" \
    "ds_read_b128 v[36:39], v23 offset:18432\n\t" \
    "v_mfma_f32_16x16x32_bf16 v[96:99], v[248:251], v[56:59], v[96:99]\n\t" \
    "v_mfma_f32_16x16x32_bf16 v[100:103], v[248:251], v[60:63], v[100:103]\n\t" \
    "s_waitcnt lgkmcnt(5)\n\t" \
    "v_mfma_f32_16x16x32_bf16 v[88:91], a[212:215], v[64:67], v[88:91]\n\t" \
    "ds_read_b128 v[40:43], v22 offset:128\n\t" \
    "s_waitcnt lgkmcnt(5)\n\t" \
    "v_mfma_f32_16x16x32_bf16 v[92:95], a[212:215], v[68:71], v[92:95]\n\t" \
    "ds_read_b128 v[44:47], v22 offset:18560\n\t" \
    "v_mfma_f32_16x16x32_bf16 v[96:99], v[252:255], v[64:67], v[96:99]\n\t" \
    "v_mfma_f32_16x16x32_bf16 v[100:103], v[252:255], v[68:71], v[100:103]\n\t" \
    "s_cmp_lg_u32 s65, 0\n\t" \
    "s_cbranch_scc1 gx_nf09_%=\n\t" \
    "v_cvt_pk_bf16_f32 v104, v88, v89\n\t" \
    "v_cvt_pk_bf16_f32 v105, v90, v91\n\t" \
    "s_nop 2\n\t" \
    "v_cvt_pk_bf16_f32 v106, v96, v97\n\t" \
    "v_cvt_pk_bf16_f32 v107, v98, v99\n\t" \
    "global_store_dwordx4 v8, v[104:107], s[38:39] offset:0 sc1\n\t" \
    "v_cvt_pk_bf16_f32 v108, v92, v93\n\t" \
    "v_cvt_pk_bf16_f32 v109, v94, v95\n\t" \
    "v_cvt_pk_bf16_f32 v110, v100, v101\n\t" \
    "v_cvt_pk_bf16_f32 v111, v102, v103\n\t" \
    "global_store_dwordx4 v8, v[108:111], s[38:39] offset:1024 sc1\n\t" \
    "s_waitcnt vmcnt(0)\n\t" \
    "s_barrier\n\t" \
    "s_cmp_lg_u32 s66, 0\n\t" \
    "s_cbranch_scc1 gx_SF10_%=\n\t" \
    "s_lshl_b32 s69, s67, 7\n\t" \
    "s_add_u32 s69, s69, 3149824\n\t" \
    "s_add_u32 s70, s65, 0\n\t" \
    "v_mov_b32 v156, s69\n\t" \
    "v_mov_b32 v157, s70\n\t" \
    "s_mov_b64 exec, 1\n\t" \
    "global_store_dword v156, v157, s[100:101] sc1\n\t" \
    "s_mov_b64 exec, -1\n\t" \
    "gx_SF10_%=:\n\t" \
    "gx_nf09_%=:\n\t" \
    "s_waitcnt lgkmcnt(5)\n\t" \
    "v_mfma_f32_16x16x32_bf16 v[72:75], a[144:147], v[24:27], 0\n\t" \
    "ds_read_b128 v[48:51], v23 offset:128\n\t" \
    "s_waitcnt lgkmcnt(5)\n\t" \
    "v_mfma_f32_16x16x32_bf16 v[76:79], a[144:147], v[28:31], 0\n\t" \
    "ds_read_b128 v[52:55], v23 offset:18560\n\t" \
    "v_mfma_f32_16x16x32_bf16 v[80:83], a[216:219], v[24:27], 0\n\t" \
    "s_add_u32 m0, s91, 36864\n\t" \
    "v_mfma_f32_16x16x32_bf16 v[84:87], a[216:219], v[28:31], 0\n\t" \
    "global_load_lds_dwordx4 v203, s[48:49]\n\t" \
    "s_waitcnt lgkmcnt(5)\n\t" \
    "v_mfma_f32_16x16x32_bf16 v[72:75], a[148:151], v[32:35], v[72:75]\n\t" \
    "ds_read_b128 v[56:59], v22 offset:256\n\t" \
    "s_waitcnt lgkmcnt(5)\n\t" \
    "v_mfma_f32_16x16x32_bf16 v[76:79], a[148:151], v[36:39], v[76:79]\n\t" \
    "ds_read_b128 v[60:63], v22 offset:18688\n\t" \
    "v_mfma_f32_16x16x32_bf16 v[80:83], a[220:223], v[32:35], v[80:83]\n\t" \
    "s_add_u32 m0, s91, 37888\n\t" \
    "v_mfma_f32_16x16x32_bf16 v[84:87], a[220:223], v[36:39], v[84:87]\n\t" \
    "global_load_lds_dwordx4 v204, s[48:49]\n\t" \
    "s_waitcnt lgkmcnt(5)\n\t" \
    "v_mfma_f32_16x16x32_bf16 v[72:75], a[152:155], v[40:43], v[72:75]\n\t" \
    "ds_read_b128 v[64:67], v23 offset:256\n\t" \
    "s_waitcnt lgkmcnt(5)\n\t" \
    "v_mfma_f32_16x16x32_bf16 v[76:79], a[152:155], v[44:47], v[76:79]\n\t" \
    "ds_read_b128 v[68:71], v23 offset:18688\n\t" \
    "v_cvt_pk_bf16_f32 v104, v88, v89\n\t" \
    "v_mfma_f32_16x16x32_bf16 v[80:83], a[224:227], v[40:43], v[80:83]\n\t" \
    "s_add_u32 m0, s91, 38912\n\t" \
    "v_cvt_pk_bf16_f32 v105, v90, v91\n\t" \
    "v_mfma_f32_16x16x32_bf16 v[84:87], a[224:227], v[44:47], v[84:87]\n\t" \
    "global_load_lds_dwordx4 v205, s[48:49]\n\t" \
    "s_waitcnt lgkmcnt(5)\n\t" \
    "v_mfma_f32_16x16x32_bf16 v[72:75], a[156:159], v[48:51], v[72:75]\n\t" \
    "ds_read_b128 v[24:27], v22 offset:384\n\t" \
    "s_waitcnt lgkmcnt(5)\n\t" \
    "v_mfma_f32_16x16x32_bf16 v[76:79], a[156:159], v[52:55], v[76:79]\n\t" \
    "ds_read_b128 v[28:31], v22 offset:18816\n\t" \
    "v_cvt_pk_bf16_f32 v106, v96, v97\n\t" \
    "v_mfma_f32_16x16x32_bf16 v[80:83], a[228:231], v[48:51], v[80:83]\n\t" \
    "s_add_u32 m0, s91, 39936\n\t" \
    "v_cvt_pk_bf16_f32 v107, v98, v99\n\t" \
    "v_mfma_f32_16x16x32_bf16 v[84:87], a[228:231], v[52:55], v[84:87]\n\t" \
    "global_load_lds_dwordx4 v206, s[48:49]\n\t" \
    "s_waitcnt lgkmcnt(5)\n\t" \
    "v_mfma_f32_16x16x32_bf16 v[72:75], a[160:163], v[56:59], v[72:75]\n\t" \
    "ds_read_b128 v[32:35], v23 offset:384\n\t" \
    "s_waitcnt lgkmcnt(5)\n\t" \
    "v_mfma_f32_16x16x32_bf16 v[76:79], a[160:163], v[60:63], v[76:79]\n\t" \
    "ds_read_b128 v[36:39], v23 offset:18816\n\t" \
    "global_store_dwordx4 v8, v[104:107], s[38:39] offset:0 sc1\n\t" \
    "v_mfma_f32_16x16x32_bf16 v[80:83], a[232:235], v[56:59], v[80:83]\n\t" \
    "s_add_u32 m0, s91, 40960\n\t" \
    "v_cvt_pk_bf16_f32 v108, v92, v93\n\t" \
    "v_mfma_f32_16x16x32_bf16 v[84:87], a[232:235], v[60:63], v[84:87]\n\t" \
    "global_load_lds_dwordx4 v207, s[48:49]\n\t" \
    "s_waitcnt lgkmcnt(5)\n\t" \
    "v_mfma_f32_16x16x32_bf16 v[72:75], a[164:167], v[64:67], v[72:75]\n\t" \
    "ds_read_b128 v[40:43], v22 offset:512\n\t" \
    "s_waitcnt lgkmcnt(5)\n\t" \
    "v_mfma_f32_16x16x32_bf16 v[76:79], a[164:167], v[68:71], v[76:79]\n\t" \
    "ds_read_b128 v[44:47], v22 offset:18944\n\t" \
    "v_cvt_pk_bf16_f32 v109, v94, v95\n\t" \
    "v_mfma_f32_16x16x32_bf16 v[80:83], a[236:239], v[64:67], v[80:83]\n\t" \
    "s_add_u32 m0, s91, 41984\n\t" \
    "v_cvt_pk_bf16_f32 v110, v100, v101\n\t" \
    "v_mfma_f32_16x16x32_bf16 v[84:87], a[236:239], v[68:71], v[84:87]\n\t" \
    "global_load_lds_dwordx4 v208, s[48:49]\n\t" \
    "s_waitcnt lgkmcnt(5)\n\t" \
    "v_mfma_f32_16x16x32_bf16 v[72:75], a[168:171], v[24:27], v[72:75]\n\t" \
    "ds_read_b128 v[48:51], v23 offset:512\n\t" \
    "s_waitcnt lgkmcnt(5)\n\t" \
    "v_mfma_f32_16x16x32_bf16 v[76:79], a[168:171], v[28:31], v[76:79]\n\t" \
    "ds_read_b128 v[52:55], v23 offset:18944\n\t" \
    "v_cvt_pk_bf16_f32 v111, v102, v103\n\t" \
    "v_mfma_f32_16x16x32_bf16 v[80:83], a[240:243], v[24:27], v[80:83]\n\t" \
    "s_add_u32 m0, s91, 43008\n\t" \
    "global_store_dwordx4 v8, v[108:111], s[38:39] offset:1024 sc1\n\t" \
    "v_mfma_f32_16x16x32_bf16 v[84:87], a[240:243], v[28:31], v[84:87]\n\t" \
    "global_load_lds_dwordx4 v209, s[48:49]\n\t" \
    "s_waitcnt lgkmcnt(5)\n\t" \
    "v_mfma_f32_16x16x32_bf16 v[72:75], a[172:175], v[32:35], v[72:75]\n\t" \
    "ds_read_b128 v[56:59], v22 offset:640\n\t" \
    "s_waitcnt lgkmcnt(5)\n\t" \
    "v_mfma_f32_16x16x32_bf16 v[76:79], a[172:175], v[36:39], v[76:79]\n\t" \
    "ds_read_b128 v[60:63], v22 offset:19072\n\t" \
    "v_mfma_f32_16x16x32_bf16 v[80:83], a[244:247], v[32:35], v[80:83]\n\t" \
    "s_add_u32 m0, s91, 44032\n\t" \
    "v_mfma_f32_16x16x32_bf16 v[84:87], a[244:247], v[36:39], v[84:87]\n\t" \
    "global_load_lds_dwordx4 v210, s[48:49]\n\t" \
    "s_waitcnt lgkmcnt(5)\n\t" \
    "v_mfma_f32_16x16x32_bf16 v[72:75], a[176:179], v[40:43], v[72:75]\n\t" \
    "ds_read_b128 v[64:67], v23 offset:640\n\t" \
    "s_waitcnt lgkmcnt(5)\n\t" \
    "v_mfma_f32_16x16x32_bf16 v[76:79], a[176:179], v[44:47], v[76:79]\n\t" \
    "ds_read_b128 v[68:71], v23 offset:19072\n\t" \
    "v_mfma_f32_16x16x32_bf16 v[80:83], a[248:251], v[40:43], v[80:83]\n\t" \
    "s_add_u32 m0, s91, 45056\n\t" \
    "v_mfma_f32_16x16x32_bf16 v[84:87], a[248:251], v[44:47], v[84:87]\n\t" \
    "global_load_lds_dwordx4 v211, s[48:49]\n\t" \
    "s_waitcnt lgkmcnt(5)\n\t" \
    "v_mfma_f32_16x16x32_bf16 v[72:75], a[180:183], v[48:51], v[72:75]\n\t" \
    "ds_read_b128 v[24:27], v22 offset:768\n\t" \
    "s_waitcnt lgkmcnt(5)\n\t" \
    "v_mfma_f32_16x16x32_bf16 v[76:79], a[180:183], v[52:55], v[76:79]\n\t" \
    "ds_read_b128 v[28:31], v22 offset:19200\n\t" \
    "v_mfma_f32_16x16x32_bf16 v[80:83], a[252:255], v[48:51], v[80:83]\n\t" \
    "v_mfma_f32_16x16x32_bf16 v[84:87], a[252:255], v[52:55], v[84:87]\n\t" \
    "s_waitcnt lgkmcnt(5)\n\t" \
    "v_mfma_f32_16x16x32_bf16 v[72:75], a[184:187], v[56:59], v[72:75]\n\t" \
    "ds_read_b128 v[32:35], v23 offset:768\n\t" \
    "s_waitcnt lgkmcnt(5)\n\t" \
    "v_mfma_f32_16x16x32_bf16 v[76:79], a[184:187], v[60:63], v[76:79]\n\t" \
    "ds_read_b128 v[36:39], v23 offset:19200\n\t" \
    "v_mfma_f32_16x16x32_bf16 v[80:83], v[224:227], v[56:59], v[80:83]\n\t" \
    "s_waitcnt vmcnt(11)\n\t" \
    "s_barrier\n\t" \
    "v_mfma_f32_16x16x32_bf16 v[84:87], v[224:227], v[60:63], v[84:87]\n\t" \
    "s_max_u32 s71, s65, 1\n\t" \
    "s_cmp_lg_u32 s66, 0\n\t" \
    "s_cbranch_scc1 gx_SF11_%=\n\t" \
    "s_lshl_b32 s69, s67, 7\n\t" \
    "s_add_u32 s69, s69, 3149824\n\t" \
    "s_add_u32 s70, s71, -1\n\t" \
    "v_mov_b32 v156, s69\n\t" \
    "v_mov_b32 v157, s70\n\t" \
    "s_mov_b64 exec, 1\n\t" \
    "global_store_dword v156, v157, s[100:101] sc1\n\t" \
    "s_mov_b64 exec, -1\n\t" \
    "gx_SF11_%=:\n\t" \
    "s_waitcnt lgkmcnt(5)\n\t" \
    "v_mfma_f32_16x16x32_bf16 v[72:75], a[188:191], v[64:67], v[72:75]\n\t" \
    "ds_read_b128 v[40:43], v22 offset:896\n\t" \
    "s_waitcnt lgkmcnt(5)\n\t" \
    "v_mfma_f32_16x16x32_bf16 v[76:79], a[188:191], v[68:71], v[76:79]\n\t" \
    "ds_read_b128 v[44:47], v22 offset:19328\n\t" \
    "v_mfma_f32_16x16x32_bf16 v[80:83], v[228:231], v[64:67], v[80:83]\n\t" \
    "s_xor_b32 s90, s90, 0x12000\n\t" \
    "v_add_u32 v1, s90, v0\n\t" \
    "v_mfma_f32_16x16x32_bf16 v[84:87], v[228:231], v[68:71], v[84:87]\n\t" \
    "v_xor_b32 v2, 64, v1\n\t" \
    "s_add_u32 s52, s52, 73728\n\t" \
    "s_waitcnt lgkmcnt(5)\n\t" \
    "v_mfma_f32_16x16x32_bf16 v[72:75], a[192:195], v[24:27], v[72:75]\n\t" \
    "ds_read_b128 v[48:51], v23 offset:896\n\t" \
    "s_waitcnt lgkmcnt(5)\n\t" \
    "v_mfma_f32_16x16x32_bf16 v[76:79], a[192:195], v[28:31], v[76:79]\n\t" \
    "ds_read_b128 v[52:55], v23 offset:19328\n\t" \
    "v_mfma_f32_16x16x32_bf16 v[80:83], v[232:235], v[24:27], v[80:83]\n\t" \
    "s_addc_u32 s53, s53, 0\n\t" \
    "s_mul_i32 s70, s67, 7\n\t" \
    "v_mfma_f32_16x16x32_bf16 v[84:87], v[232:235], v[28:31], v[84:87]\n\t" \
    "s_add_u32 s69, s65, 1\n\t" \
    "s_xor_b32 s69, s69, s70\n\t" \
    "s_waitcnt lgkmcnt(5)\n\t" \
    "v_mfma_f32_16x16x32_bf16 v[72:75], a[196:199], v[32:35], v[72:75]\n\t" \
    "ds_read_b128 v[56:59], v22 offset:1024\n\t" \
    "s_waitcnt lgkmcnt(5)\n\t" \
    "v_mfma_f32_16x16x32_bf16 v[76:79], a[196:199], v[36:39], v[76:79]\n\t" \
    "ds_read_b128 v[60:63], v22 offset:19456\n\t" \
    "v_mfma_f32_16x16x32_bf16 v[80:83], v[236:239], v[32:35], v[80:83]\n\t" \
    "s_and_b32 s69, s69, 63\n\t" \
    "s_lshl_b32 s69, s69, 13\n\t" \
    "v_mfma_f32_16x16x32_bf16 v[84:87], v[236:239], v[36:39], v[84:87]\n\t" \
    "s_mul_i32 s70, s67, 524288\n\t" \
    "s_add_u32 s69, s69, s70\n\t" \
    "s_waitcnt lgkmcnt(5)\n\t" \
    "v_mfma_f32_16x16x32_bf16 v[72:75], a[200:203], v[40:43], v[72:75]\n\t" \
    "ds_read_b128 v[64:67], v23 offset:1024\n\t" \
    "s_waitcnt lgkmcnt(5)\n\t" \
    "v_mfma_f32_16x16x32_bf16 v[76:79], a[200:203], v[44:47], v[76:79]\n\t" \
    "ds_read_b128 v[68:71], v23 offset:19456\n\t" \
    "v_mfma_f32_16x16x32_bf16 v[80:83], v[240:243], v[40:43], v[80:83]\n\t" \
    "s_add_u32 s69, s69, 23068672\n\t" \
    "s_add_u32 s40, s36, s69\n\t" \
    "v_mfma_f32_16x16x32_bf16 v[84:87], v[240:243], v[44:47], v[84:87]\n\t" \
    "s_addc_u32 s41, s37, 0\n\t" \
    "s_sub_u32 s91, s85, s91\n\t" \
    "s_waitcnt lgkmcnt(5)\n\t" \
    "v_mfma_f32_16x16x32_bf16 v[72:75], a[204:207], v[48:51], v[72:75]\n\t" \
    "ds_read_b128 v[24:27], v1 offset:0\n\t" \
    "s_waitcnt lgkmcnt(5)\n\t" \
    "v_mfma_f32_16x16x32_bf16 v[76:79], a[204:207], v[52:55], v[76:79]\n\t" \
    "ds_read_b128 v[28:31], v1 offset:18432\n\t" \
    "v_mfma_f32_16x16x32_bf16 v[80:83], v[244:247], v[48:51], v[80:83]\n\t" \
    "v_mfma_f32_16x16x32_bf16 v[84:87], v[244:247], v[52:55], v[84:87]\n\t" \
    "s_waitcnt lgkmcnt(5)\n\t" \
    "v_mfma_f32_16x16x32_bf16 v[72:75], a[208:211], v[56:59], v[72:75]\n\t" \
    "ds_read_b128 v[32:35], v2 offset:0\n\t" \
    "s_waitcnt lgkmcnt(5)\n\t" \
    "v_mfma_f32_16x16x32_bf16 v[76:79], a[208:211], v[60:63], v[76:79]\n\t" \
    "ds_read_b128 v[36:39], v2 offset:18432\n\t" \
    "v_mfma_f32_16x16x32_bf16 v[80:83], v[248:251], v[56:59], v[80:83]\n\t" \
    "v_mfma_f32_16x16x32_bf16 v[84:87], v[248:251], v[60:63], v[84:87]\n\t" \
    "s_waitcnt lgkmcnt(5)\n\t" \
    "v_mfma_f32_16x16x32_bf16 v[72:75], a[212:215], v[64:67], v[72:75]\n\t" \
    "ds_read_b128 v[40:43], v1 offset:128\n\t" \
    "s_waitcnt lgkmcnt(5)\n\t" \
    "v_mfma_f32_16x16x32_bf16 v[76:79], a[212:215], v[68:71], v[76:79]\n\t" \
    "ds_read_b128 v[44:47], v1 offset:18560\n\t" \
    "v_mfma_f32_16x16x32_bf16 v[80:83], v[252:255], v[64:67], v[80:83]\n\t" \
    "v_mfma_f32_16x16x32_bf16 v[84:87], v[252:255], v[68:71], v[84:87]\n\t" \
    "s_lshl_b32 s69, s65, 6\n\t" \
    "s_lshl_b32 s70, s66, 5\n\t" \
    "s_add_u32 s69, s69, s70\n\t" \
    "v_lshrrev_b32 v156, 3, v6\n\t" \
    "v_add_u32 v156, s69, v156\n\t" \
    "v_min_u32 v156, 575, v156\n\t" \
    "v_and_b32 v157, 4, v6\n\t" \
    "v_lshlrev_b32 v157, 5, v157\n\t" \
    "v_lshl_add_u32 v156, v156, 13, v157\n\t" \
    "s_lshl_b32 s69, s67, 8\n\t" \
    "v_add_u32 v156, s69, v156\n\t" \
    "s_mov_b32 m0, 155648\n\t" \
    "s_nop 0\n\t" \
    "global_load_lds_dword v156, s[44:45]\n\t" \
    "s_add_u32 s65, s65, 2\n\t" \
    "s_cmp_lt_u32 s65, 8\n\t" \
    "s_cbranch_scc1 gx_l16_%=\n\t" \
    "v_cvt_pk_bf16_f32 v104, v72, v73\n\t" \
    "v_cvt_pk_bf16_f32 v105, v74, v75\n\t" \
    "v_cvt_pk_bf16_f32 v106, v80, v81\n\t" \
    "v_cvt_pk_bf16_f32 v107, v82, v83\n\t" \
    "global_store_dwordx4 v8, v[104:107], s[40:41] offset:0 sc1\n\t" \
    "v_cvt_pk_bf16_f32 v108, v76, v77\n\t" \
    "v_cvt_pk_bf16_f32 v109, v78, v79\n\t" \
    "v_cvt_pk_bf16_f32 v110, v84, v85\n\t" \
    "v_cvt_pk_bf16_f32 v111, v86, v87\n\t" \
    "global_store_dwordx4 v8, v[108:111], s[40:41] offset:1024 sc1\n\t" \
    "s_waitcnt vmcnt(0) lgkmcnt(0)\n\t" \
    "s_barrier\n\t" \
    "s_cmp_lg_u32 s66, 0\n\t" \
    "s_cbranch_scc1 gx_SF12_%=\n\t" \
    "s_lshl_b32 s69, s67, 7\n\t" \
    "s_add_u32 s69, s69, 3149824\n\t" \
    "s_add_u32 s70, s65, -1\n\t" \
    "v_mov_b32 v156, s69\n\t" \
    "v_mov_b32 v157, s70\n\t" \
    "s_mov_b64 exec, 1\n\t" \
    "global_store_dword v156, v157, s[100:101] sc1\n\t" \
    "s_mov_b64 exec, -1\n\t" \
    "gx_SF12_%=:\n\t" \
    "s_add_u32 s46, s44, 0\n\t" \
    "s_addc_u32 s47, s45, 0\n\t" \
    "s_add_u32 m0, s82, 0\n\t" \
    "s_nop 0\n\t" \
    "global_load_lds_dwordx4 v200, s[46:47]\n\t" \
    "s_add_u32 s46, s44, 131072\n\t" \
    "s_addc_u32 s47, s45, 0\n\t" \
    "s_add_u32 m0, s82, 4096\n\t" \
    "s_nop 0\n\t" \
    "global_load_lds_dwordx4 v200, s[46:47]\n\t" \
    "s_add_u32 s46, s44, 262144\n\t" \
    "s_addc_u32 s47, s45, 0\n\t" \
    "s_add_u32 m0, s82, 8192\n\t" \
    "s_nop 0\n\t" \
    "global_load_lds_dwordx4 v200, s[46:47]\n\t" \
    "s_add_u32 s46, s44, 393216\n\t" \
    "s_addc_u32 s47, s45, 0\n\t" \
    "s_add_u32 m0, s82, 12288\n\t" \
    "s_nop 0\n\t" \
    "global_load_lds_dwordx4 v200, s[46:47]\n\t" \
    "s_add_u32 s46, s44, 524288\n\t" \
    "s_addc_u32 s47, s45, 0\n\t" \
    "s_add_u32 m0, s82, 16384\n\t" \
    "s_nop 0\n\t" \
    "global_load_lds_dwordx4 v200, s[46:47]\n\t" \
    "s_add_u32 s46, s44, 655360\n\t" \
    "s_addc_u32 s47, s45, 0\n\t" \
    "s_add_u32 m0, s82, 20480\n\t" \
    "s_nop 0\n\t" \
    "global_load_lds_dwordx4 v200, s[46:47]\n\t" \
    "s_add_u32 s46, s44, 786432\n\t" \
    "s_addc_u32 s47, s45, 0\n\t" \
    "s_add_u32 m0, s82, 24576\n\t" \
    "s_nop 0\n\t" \
    "global_load_lds_dwordx4 v200, s[46:47]\n\t" \
    "s_add_u32 s46, s44, 917504\n\t" \
    "s_addc_u32 s47, s45, 0\n\t" \
    "s_add_u32 m0, s82, 28672\n\t" \
    "s_nop 0\n\t" \
    "global_load_lds_dwordx4 v200, s[46:47]\n\t" \
    "s_add_u32 s46, s44, 1048576\n\t" \
    "s_addc_u32 s47, s45, 0\n\t" \
    "s_add_u32 m0, s82, 32768\n\t" \
    "s_nop 0\n\t" \
    "global_load_lds_dwordx4 v200, s[46:47]\n\t" \
    "s_add_u32 s46, s44, 1179648\n\t" \
    "s_addc_u32 s47, s45, 0\n\t" \
    "s_add_u32 m0, s82, 36864\n\t" \
    "s_nop 0\n\t" \
    "global_load_lds_dwordx4 v200, s[46:47]\n\t" \
    "s_add_u32 s46, s44, 1310720\n\t" \
    "s_addc_u32 s47, s45, 0\n\t" \
    "s_add_u32 m0, s82, 40960\n\t" \
    "s_nop 0\n\t" \
    "global_load_lds_dwordx4 v200, s[46:47]\n\t" \
    "s_add_u32 s46, s44, 1441792\n\t" \
    "s_addc_u32 s47, s45, 0\n\t" \
    "s_add_u32 m0, s82, 45056\n\t" \
    "s_nop 0\n\t" \
    "global_load_lds_dwordx4 v200, s[46:47]\n\t" \
    "s_add_u32 s46, s44, 1572864\n\t" \
    "s_addc_u32 s47, s45, 0\n\t" \
    "s_add_u32 m0, s82, 49152\n\t" \
    "s_nop 0\n\t" \
    "global_load_lds_dwordx4 v200, s[46:47]\n\t" \
    "s_add_u32 s46, s44, 1703936\n\t" \
    "s_addc_u32 s47, s45, 0\n\t" \
    "s_add_u32 m0, s82, 53248\n\t" \
    "s_nop 0\n\t" \
    "global_load_lds_dwordx4 v200, s[46:47]\n\t" \
    "s_add_u32 s46, s44, 1835008\n\t" \
    "s_addc_u32 s47, s45, 0\n\t" \
    "s_add_u32 m0, s82, 57344\n\t" \
    "s_nop 0\n\t" \
    "global_load_lds_dwordx4 v200, s[46:47]\n\t" \
    "s_add_u32 s46, s44, 1966080\n\t" \
    "s_addc_u32 s47, s45, 0\n\t" \
    "s_add_u32 m0, s82, 61440\n\t" \
    "s_nop 0\n\t" \
    "global_load_lds_dwordx4 v200, s[46:47]\n\t" \
    "s_add_u32 s46, s44, 2097152\n\t" \
    "s_addc_u32 s47, s45, 0\n\t" \
    "s_add_u32 m0, s82, 65536\n\t" \
    "s_nop 0\n\t" \
    "global_load_lds_dwordx4 v200, s[46:47]\n\t" \
    "s_add_u32 s46, s44, 2228224\n\t" \
    "s_addc_u32 s47, s45, 0\n\t" \
    "s_add_u32 m0, s82, 69632\n\t" \
    "s_nop 0\n\t" \
    "global_load_lds_dwordx4 v200, s[46:47]\n\t" \
    "s_add_u32 s46, s44, 2359296\n\t" \
    "s_addc_u32 s47, s45, 0\n\t" \
    "s_add_u32 m0, s82, 73728\n\t" \
    "s_nop 0\n\t" \
    "global_load_lds_dwordx4 v200, s[46:47]\n\t" \
    "s_add_u32 s46, s44, 2490368\n\t" \
    "s_addc_u32 s47, s45, 0\n\t" \
    "s_add_u32 m0, s82, 77824\n\t" \
    "s_nop 0\n\t" \
    "global_load_lds_dwordx4 v200, s[46:47]\n\t" \
    "s_add_u32 s46, s44, 2621440\n\t" \
    "s_addc_u32 s47, s45, 0\n\t" \
    "s_add_u32 m0, s82, 81920\n\t" \
    "s_nop 0\n\t" \
    "global_load_lds_dwordx4 v200, s[46:47]\n\t" \
    "s_add_u32 s46, s44, 2752512\n\t" \
    "s_addc_u32 s47, s45, 0\n\t" \
    "s_add_u32 m0, s82, 86016\n\t" \
    "s_nop 0\n\t" \
    "global_load_lds_dwordx4 v200, s[46:47]\n\t" \
    "s_add_u32 s46, s44, 2883584\n\t" \
    "s_addc_u32 s47, s45, 0\n\t" \
    "s_add_u32 m0, s82, 90112\n\t" \
    "s_nop 0\n\t" \
    "global_load_lds_dwordx4 v200, s[46:47]\n\t" \
    "s_add_u32 s46, s44, 3014656\n\t" \
    "s_addc_u32 s47, s45, 0\n\t" \
    "s_add_u32 m0, s82, 94208\n\t" \
    "s_nop 0\n\t" \
    "global_load_lds_dwordx4 v200, s[46:47]\n\t" \
    "s_add_u32 s46, s44, 3145728\n\t" \
    "s_addc_u32 s47, s45, 0\n\t" \
    "s_add_u32 m0, s82, 98304\n\t" \
    "s_nop 0\n\t" \
    "global_load_lds_dwordx4 v200, s[46:47]\n\t" \
    "s_add_u32 s46, s44, 3276800\n\t" \
    "s_addc_u32 s47, s45, 0\n\t" \
    "s_add_u32 m0, s82, 102400\n\t" \
    "s_nop 0\n\t" \
    "global_load_lds_dwordx4 v200, s[46:47]\n\t" \
    "s_add_u32 s46, s44, 3407872\n\t" \
    "s_addc_u32 s47, s45, 0\n\t" \
    "s_add_u32 m0, s82, 106496\n\t" \
    "s_nop 0\n\t" \
    "global_load_lds_dwordx4 v200, s[46:47]\n\t" \
    "s_add_u32 s46, s44, 3538944\n\t" \
    "s_addc_u32 s47, s45, 0\n\t" \
    "s_add_u32 m0, s82, 110592\n\t" \
    "s_nop 0\n\t" \
    "global_load_lds_dwordx4 v200, s[46:47]\n\t" \
    "s_add_u32 s46, s44, 3670016\n\t" \
    "s_addc_u32 s47, s45, 0\n\t" \
    "s_add_u32 m0, s82, 114688\n\t" \
    "s_nop 0\n\t" \
    "global_load_lds_dwordx4 v200, s[46:47]\n\t" \
    "s_add_u32 s46, s44, 3801088\n\t" \
    "s_addc_u32 s47, s45, 0\n\t" \
    "s_add_u32 m0, s82, 118784\n\t" \
    "s_nop 0\n\t" \
    "global_load_lds_dwordx4 v200, s[46:47]\n\t" \
    "s_add_u32 s46, s44, 3932160\n\t" \
    "s_addc_u32 s47, s45, 0\n\t" \
    "s_add_u32 m0, s82, 122880\n\t" \
    "s_nop 0\n\t" \
    "global_load_lds_dwordx4 v200, s[46:47]\n\t" \
    "s_add_u32 s46, s44, 4063232\n\t" \
    "s_addc_u32 s47, s45, 0\n\t" \
    "s_add_u32 m0, s82, 126976\n\t" \
    "s_nop 0\n\t" \
    "global_load_lds_dwordx4 v200, s[46:47]\n\t" \
    "s_add_u32 s46, s44, 4194304\n\t" \
    "s_addc_u32 s47, s45, 0\n\t" \
    "s_add_u32 m0, s82, 131072\n\t" \
    "s_nop 0\n\t" \
    "global_load_lds_dwordx4 v200, s[46:47]\n\t" \
    "s_add_u32 s46, s44, 4325376\n\t" \
    "s_addc_u32 s47, s45, 0\n\t" \
    "s_add_u32 m0, s82, 135168\n\t" \
    "s_nop 0\n\t" \
    "global_load_lds_dwordx4 v200, s[46:47]\n\t" \
    "s_add_u32 s46, s44, 4456448\n\t" \
    "s_addc_u32 s47, s45, 0\n\t" \
    "s_add_u32 m0, s82, 139264\n\t" \
    "s_nop 0\n\t" \
    "global_load_lds_dwordx4 v200, s[46:47]\n\t" \
    "s_add_u32 s46, s44, 4587520\n\t" \
    "s_addc_u32 s47, s45, 0\n\t" \
    "s_add_u32 m0, s82, 143360\n\t" \
    "s_nop 0\n\t" \
    "global_load_lds_dwordx4 v200, s[46:47]\n\t" \
    "global_load_dwordx4 v[120:123], v203, s[50:51]\n\t" \
    "global_load_dwordx4 v[124:127], v204, s[50:51]\n\t" \
    "global_load_dwordx4 v[128:131], v205, s[50:51]\n\t" \
    "global_load_dwordx4 v[132:135], v206, s[50:51]\n\t" \
    "global_load_dwordx4 v[136:139], v207, s[50:51]\n\t" \
    "global_load_dwordx4 v[140:143], v208, s[50:51]\n\t" \
    "global_load_dwordx4 v[144:147], v209, s[50:51]\n\t" \
    "global_load_dwordx4 v[148:151], v210, s[50:51]\n\t" \
    "global_load_dwordx4 v[152:155], v211, s[50:51]\n\t" \
    "s_add_u32 s52, s50, 294912\n\t" \
    "s_addc_u32 s53, s51, 0\n\t" \
    "global_load_dwordx4 v[72:75], v203, s[52:53]\n\t" \
    "global_load_dwordx4 v[76:79], v204, s[52:53]\n\t" \
    "global_load_dwordx4 v[80:83], v205, s[52:53]\n\t" \
    "global_load_dwordx4 v[84:87], v206, s[52:53]\n\t" \
    "global_load_dwordx4 v[88:91], v207, s[52:53]\n\t" \
    "global_load_dwordx4 v[92:95], v208, s[52:53]\n\t" \
    "global_load_dwordx4 v[96:99], v209, s[52:53]\n\t" \
    "global_load_dwordx4 v[100:103], v210, s[52:53]\n\t" \
    "global_load_dwordx4 v[104:107], v211, s[52:53]\n\t" \
    "s_waitcnt vmcnt(50) lgkmcnt(0)\n\t" \
    "s_barrier\n\t" \
    "ds_read_b64_tr_b16 a[0:1], v201 offset:0\n\t" \
    "ds_read_b64_tr_b16 a[2:3], v201 offset:1024\n\t" \
    "ds_read_b64_tr_b16 a[4:5], v201 offset:8192\n\t" \
    "ds_read_b64_tr_b16 a[6:7], v201 offset:9216\n\t" \
    "ds_read_b64_tr_b16 a[72:73], v202 offset:0\n\t" \
    "ds_read_b64_tr_b16 a[74:75], v202 offset:1024\n\t" \
    "ds_read_b64_tr_b16 a[76:77], v202 offset:8192\n\t" \
    "ds_read_b64_tr_b16 a[78:79], v202 offset:9216\n\t" \
    "s_waitcnt vmcnt(46) lgkmcnt(0)\n\t" \
    "s_barrier\n\t" \
    "ds_read_b64_tr_b16 a[8:9], v201 offset:16384\n\t" \
    "ds_read_b64_tr_b16 a[10:11], v201 offset:17408\n\t" \
    "ds_read_b64_tr_b16 a[12:13], v201 offset:24576\n\t" \
    "ds_read_b64_tr_b16 a[14:15], v201 offset:25600\n\t" \
    "ds_read_b64_tr_b16 a[80:81], v202 offset:16384\n\t" \
    "ds_read_b64_tr_b16 a[82:83], v202 offset:17408\n\t" \
    "ds_read_b64_tr_b16 a[84:85], v202 offset:24576\n\t" \
    "ds_read_b64_tr_b16 a[86:87], v202 offset:25600\n\t" \
    "s_waitcnt vmcnt(42) lgkmcnt(0)\n\t" \
    "s_barrier\n\t" \
    "ds_read_b64_tr_b16 a[16:17], v201 offset:32768\n\t" \
    "ds_read_b64_tr_b16 a[18:19], v201 offset:33792\n\t" \
    "ds_read_b64_tr_b16 a[20:21], v201 offset:40960\n\t" \
    "ds_read_b64_tr_b16 a[22:23], v201 offset:41984\n\t" \
    "ds_read_b64_tr_b16 a[88:89], v202 offset:32768\n\t" \
    "ds_read_b64_tr_b16 a[90:91], v202 offset:33792\n\t" \
    "ds_read_b64_tr_b16 a[92:93], v202 offset:40960\n\t" \
    "ds_read_b64_tr_b16 a[94:95], v202 offset:41984\n\t" \
    "s_waitcnt vmcnt(38) lgkmcnt(0)\n\t" \
    "s_barrier\n\t" \
    "ds_read_b64_tr_b16 a[24:25], v201 offset:49152\n\t" \
    "ds_read_b64_tr_b16 a[26:27], v201 offset:50176\n\t" \
    "ds_read_b64_tr_b16 a[28:29], v201 offset:57344\n\t" \
    "ds_read_b64_tr_b16 a[30:31], v201 offset:58368\n\t" \
    "ds_read_b64_tr_b16 a[96:97], v202 offset:49152\n\t" \
    "ds_read_b64_tr_b16 a[98:99], v202 offset:50176\n\t" \
    "ds_read_b64_tr_b16 a[100:101], v202 offset:57344\n\t" \
    "ds_read_b64_tr_b16 a[102:103], v202 offset:58368\n\t" \
    "s_waitcnt vmcnt(34) lgkmcnt(0)\n\t" \
    "s_barrier\n\t" \
    "v_add_u32 v201, 0x10000, v201\n\t" \
    "v_add_u32 v202, 0x10000, v202\n\t" \
    "ds_read_b64_tr_b16 a[32:33], v201 offset:0\n\t" \
    "ds_read_b64_tr_b16 a[34:35], v201 offset:1024\n\t" \
    "ds_read_b64_tr_b16 a[36:37], v201 offset:8192\n\t" \
    "ds_read_b64_tr_b16 a[38:39], v201 offset:9216\n\t" \
    "ds_read_b64_tr_b16 a[104:105], v202 offset:0\n\t" \
    "ds_read_b64_tr_b16 a[106:107], v202 offset:1024\n\t" \
    "ds_read_b64_tr_b16 a[108:109], v202 offset:8192\n\t" \
    "ds_read_b64_tr_b16 a[110:111], v202 offset:9216\n\t" \
    "s_waitcnt vmcnt(30) lgkmcnt(0)\n\t" \
    "s_barrier\n\t" \
    "ds_read_b64_tr_b16 a[40:41], v201 offset:16384\n\t" \
    "ds_read_b64_tr_b16 a[42:43], v201 offset:17408\n\t" \
    "ds_read_b64_tr_b16 a[44:45], v201 offset:24576\n\t" \
    "ds_read_b64_tr_b16 a[46:47], v201 offset:25600\n\t" \
    "ds_read_b64_tr_b16 a[112:113], v202 offset:16384\n\t" \
    "ds_read_b64_tr_b16 a[114:115], v202 offset:17408\n\t" \
    "ds_read_b64_tr_b16 a[116:117], v202 offset:24576\n\t" \
    "ds_read_b64_tr_b16 a[118:119], v202 offset:25600\n\t" \
    "s_waitcnt vmcnt(26) lgkmcnt(0)\n\t" \
    "s_barrier\n\t" \
    "ds_read_b64_tr_b16 a[48:49], v201 offset:32768\n\t" \
    "ds_read_b64_tr_b16 a[50:51], v201 offset:33792\n\t" \
    "ds_read_b64_tr_b16 a[52:53], v201 offset:40960\n\t" \
    "ds_read_b64_tr_b16 a[54:55], v201 offset:41984\n\t" \
    "ds_read_b64_tr_b16 a[120:121], v202 offset:32768\n\t" \
    "ds_read_b64_tr_b16 a[122:123], v202 offset:33792\n\t" \
    "ds_read_b64_tr_b16 a[124:125], v202 offset:40960\n\t" \
    "ds_read_b64_tr_b16 a[126:127], v202 offset:41984\n\t" \
    "s_waitcnt vmcnt(22) lgkmcnt(0)\n\t" \
    "s_barrier\n\t" \
    "ds_read_b64_tr_b16 a[56:57], v201 offset:49152\n\t" \
    "ds_read_b64_tr_b16 a[58:59], v201 offset:50176\n\t" \
    "ds_read_b64_tr_b16 a[60:61], v201 offset:57344\n\t" \
    "ds_read_b64_tr_b16 a[62:63], v201 offset:58368\n\t" \
    "ds_read_b64_tr_b16 a[128:129], v202 offset:49152\n\t" \
    "ds_read_b64_tr_b16 a[130:131], v202 offset:50176\n\t" \
    "ds_read_b64_tr_b16 a[132:133], v202 offset:57344\n\t" \
    "ds_read_b64_tr_b16 a[134:135], v202 offset:58368\n\t" \
    "s_waitcnt vmcnt(18) lgkmcnt(0)\n\t" \
    "s_barrier\n\t" \
    "v_add_u32 v201, 0x10000, v201\n\t" \
    "v_add_u32 v202, 0x10000, v202\n\t" \
    "ds_read_b64_tr_b16 a[64:65], v201 offset:0\n\t" \
    "ds_read_b64_tr_b16 a[66:67], v201 offset:1024\n\t" \
    "ds_read_b64_tr_b16 a[68:69], v201 offset:8192\n\t" \
    "ds_read_b64_tr_b16 a[70:71], v201 offset:9216\n\t" \
    "ds_read_b64_tr_b16 a[136:137], v202 offset:0\n\t" \
    "ds_read_b64_tr_b16 a[138:139], v202 offset:1024\n\t" \
    "ds_read_b64_tr_b16 a[140:141], v202 offset:8192\n\t" \
    "ds_read_b64_tr_b16 a[142:143], v202 offset:9216\n\t" \
    "v_add_u32 v201, 0xfffe0000, v201\n\t" \
    "v_add_u32 v202, 0xfffe0000, v202\n\t" \
    "s_waitcnt lgkmcnt(0)\n\t" \
    "s_barrier\n\t" \
    "s_waitcnt vmcnt(0)\n\t" \
    "ds_write_b128 v3, v[120:123] offset:0\n\t" \
    "ds_write_b128 v3, v[124:127] offset:1024\n\t" \
    "ds_write_b128 v3, v[128:131] offset:2048\n\t" \
    "ds_write_b128 v3, v[132:135] offset:3072\n\t" \
    "ds_write_b128 v3, v[136:139] offset:4096\n\t" \
    "ds_write_b128 v3, v[140:143] offset:5120\n\t" \
    "ds_write_b128 v3, v[144:147] offset:6144\n\t" \
    "ds_write_b128 v3, v[148:151] offset:7168\n\t" \
    "ds_write_b128 v3, v[152:155] offset:8192\n\t" \
    "ds_write_b128 v3, v[72:75] offset:36864\n\t" \
    "ds_write_b128 v3, v[76:79] offset:37888\n\t" \
    "ds_write_b128 v3, v[80:83] offset:38912\n\t" \
    "ds_write_b128 v3, v[84:87] offset:39936\n\t" \
    "ds_write_b128 v3, v[88:91] offset:40960\n\t" \
    "ds_write_b128 v3, v[92:95] offset:41984\n\t" \
    "ds_write_b128 v3, v[96:99] offset:43008\n\t" \
    "ds_write_b128 v3, v[100:103] offset:44032\n\t" \
    "ds_write_b128 v3, v[104:107] offset:45056\n\t" \
    "s_mov_b32 s65, 0\n\t" \
    "v_mov_b32 v1, v0\n\t" \
    "v_xor_b32 v2, 64, v0\n\t" \
    "s_mov_b32 s90, 0\n\t" \
    "s_add_u32 s91, s89, 0x12000\n\t" \
    "s_add_u32 s85, s91, s89\n\t" \
    "s_add_u32 s52, s50, 36864\n\t" \
    "s_addc_u32 s53, s51, 0\n\t" \
    "s_mul_i32 s69, s67, 8192\n\t" \
    "s_add_u32 s69, s69, 175112192\n\t" \
    "s_add_u32 s42, s36, s69\n\t" \
    "s_addc_u32 s43, s37, 0\n\t" \
    "s_mov_b64 s[40:41], s[42:43]\n\t" \
    "s_waitcnt lgkmcnt(0)\n\t" \
    "s_barrier\n\t" \
    "ds_read_b128 v[24:27], v1 offset:0\n\t" \
    "ds_read_b128 v[28:31], v1 offset:18432\n\t" \
    "ds_read_b128 v[32:35], v2 offset:0\n\t" \
    "ds_read_b128 v[36:39], v2 offset:18432\n\t" \
    "ds_read_b128 v[40:43], v1 offset:128\n\t" \
    "ds_read_b128 v[44:47], v1 offset:18560\n\t" \
    ".p2align 6\n\t" \
    "s_nop 0\n\t" \
    "s_nop 0\n\t" \
    "gx_l27_%=:\n\t" \
    "s_waitcnt lgkmcnt(5)\n\t" \
    "v_mfma_f32_16x16x32_bf16 v[88:91], a[0:3], v[24:27], 0\n\t" \
    "ds_read_b128 v[48:51], v2 offset:128\n\t" \
    "s_waitcnt lgkmcnt(5)\n\t" \
    "v_mfma_f32_16x16x32_bf16 v[92:95], a[0:3], v[28:31], 0\n\t" \
    "ds_read_b128 v[52:55], v2 offset:18560\n\t" \
    "v_mfma_f32_16x16x32_bf16 v[96:99], a[72:75], v[24:27], 0\n\t" \
    "s_add_u32 m0, s91, 0\n\t" \
    "v_mfma_f32_16x16x32_bf16 v[100:103], a[72:75], v[28:31], 0\n\t" \
    "global_load_lds_dwordx4 v203, s[52:53]\n\t" \
    "s_waitcnt lgkmcnt(5)\n\t" \
    "v_mfma_f32_16x16x32_bf16 v[88:91], a[4:7], v[32:35], v[88:91]\n\t" \
    "ds_read_b128 v[56:59], v1 offset:256\n\t" \
    "s_waitcnt lgkmcnt(5)\n\t" \
    "v_mfma_f32_16x16x32_bf16 v[92:95], a[4:7], v[36:39], v[92:95]\n\t" \
    "ds_read_b128 v[60:63], v1 offset:18688\n\t" \
    "v_mfma_f32_16x16x32_bf16 v[96:99], a[76:79], v[32:35], v[96:99]\n\t" \
    "s_add_u32 m0, s91, 1024\n\t" \
    "v_mfma_f32_16x16x32_bf16 v[100:103], a[76:79], v[36:39], v[100:103]\n\t" \
    "global_load_lds_dwordx4 v204, s[52:53]\n\t" \
    "s_waitcnt lgkmcnt(5)\n\t" \
    "v_mfma_f32_16x16x32_bf16 v[88:91], a[8:11], v[40:43], v[88:91]\n\t" \
    "ds_read_b128 v[64:67], v2 offset:256\n\t" \
    "s_waitcnt lgkmcnt(5)\n\t" \
    "v_mfma_f32_16x16x32_bf16 v[92:95], a[8:11], v[44:47], v[92:95]\n\t" \
    "ds_read_b128 v[68:71], v2 offset:18688\n\t" \
    "v_cvt_pk_bf16_f32 v104, v72, v73\n\t" \
    "v_mfma_f32_16x16x32_bf16 v[96:99], a[80:83], v[40:43], v[96:99]\n\t" \
    "s_add_u32 m0, s91, 2048\n\t" \
    "v_cvt_pk_bf16_f32 v105, v74, v75\n\t" \
    "v_mfma_f32_16x16x32_bf16 v[100:103], a[80:83], v[44:47], v[100:103]\n\t" \
    "global_load_lds_dwordx4 v205, s[52:53]\n\t" \
    "s_waitcnt lgkmcnt(5)\n\t" \
    "v_mfma_f32_16x16x32_bf16 v[88:91], a[12:15], v[48:51], v[88:91]\n\t" \
    "ds_read_b128 v[24:27], v1 offset:384\n\t" \
    "s_waitcnt lgkmcnt(5)\n\t" \
    "v_mfma_f32_16x16x32_bf16 v[92:95], a[12:15], v[52:55], v[92:95]\n\t" \
    "ds_read_b128 v[28:31], v1 offset:18816\n\t" \
    "v_cvt_pk_bf16_f32 v106, v80, v81\n\t" \
    "v_mfma_f32_16x16x32_bf16 v[96:99], a[84:87], v[48:51], v[96:99]\n\t" \
    "s_add_u32 m0, s91, 3072\n\t" \
    "v_cvt_pk_bf16_f32 v107, v82, v83\n\t" \
    "v_mfma_f32_16x16x32_bf16 v[100:103], a[84:87], v[52:55], v[100:103]\n\t" \
    "global_load_lds_dwordx4 v206, s[52:53]\n\t" \
    "s_waitcnt lgkmcnt(5)\n\t" \
    "v_mfma_f32_16x16x32_bf16 v[88:91], a[16:19], v[56:59], v[88:91]\n\t" \
    "ds_read_b128 v[32:35], v2 offset:384\n\t" \
    "s_waitcnt lgkmcnt(5)\n\t" \
    "v_mfma_f32_16x16x32_bf16 v[92:95], a[16:19], v[60:63], v[92:95]\n\t" \
    "ds_read_b128 v[36:39], v2 offset:18816\n\t" \
    "global_store_dwordx4 v8, v[104:107], s[40:41] offset:0 sc1\n\t" \
    "v_mfma_f32_16x16x32_bf16 v[96:99], a[88:91], v[56:59], v[96:99]\n\t" \
    "s_add_u32 m0, s91, 4096\n\t" \
    "v_cvt_pk_bf16_f32 v108, v76, v77\n\t" \
    "v_mfma_f32_16x16x32_bf16 v[100:103], a[88:91], v[60:63], v[100:103]\n\t" \
    "global_load_lds_dwordx4 v207, s[52:53]\n\t" \
    "s_waitcnt lgkmcnt(5)\n\t" \
    "v_mfma_f32_16x16x32_bf16 v[88:91], a[20:23], v[64:67], v[88:91]\n\t" \
    "ds_read_b128 v[40:43], v1 offset:512\n\t" \
    "s_waitcnt lgkmcnt(5)\n\t" \
    "v_mfma_f32_16x16x32_bf16 v[92:95], a[20:23], v[68:71], v[92:95]\n\t" \
    "ds_read_b128 v[44:47], v1 offset:18944\n\t" \
    "v_cvt_pk_bf16_f32 v109, v78, v79\n\t" \
    "v_mfma_f32_16x16x32_bf16 v[96:99], a[92:95], v[64:67], v[96:99]\n\t" \
    "s_add_u32 m0, s91, 5120\n\t" \
    "v_cvt_pk_bf16_f32 v110, v84, v85\n\t" \
    "v_mfma_f32_16x16x32_bf16 v[100:103], a[92:95], v[68:71], v[100:103]\n\t" \
    "global_load_lds_dwordx4 v208, s[52:53]\n\t" \
    "s_waitcnt lgkmcnt(5)\n\t" \
    "v_mfma_f32_16x16x32_bf16 v[88:91], a[24:27], v[24:27], v[88:91]\n\t" \
    "ds_read_b128 v[48:51], v2 offset:512\n\t" \
    "s_waitcnt lgkmcnt(5)\n\t" \
    "v_mfma_f32_16x16x32_bf16 v[92:95], a[24:27], v[28:31], v[92:95]\n\t" \
    "ds_read_b128 v[52:55], v2 offset:18944\n\t" \
    "v_cvt_pk_bf16_f32 v111, v86, v87\n\t" \
    "v_mfma_f32_16x16x32_bf16 v[96:99], a[96:99], v[24:27], v[96:99]\n\t" \
    "s_add_u32 m0, s91, 6144\n\t" \
    "global_store_dwordx4 v8, v[108:111], s[40:41] offset:1024 sc1\n\t" \
    "v_mfma_f32_16x16x32_bf16 v[100:103], a[96:99], v[28:31], v[100:103]\n\t" \
    "global_load_lds_dwordx4 v209, s[52:53]\n\t" \
    "s_waitcnt lgkmcnt(5)\n\t" \
    "v_mfma_f32_16x16x32_bf16 v[88:91], a[28:31], v[32:35], v[88:91]\n\t" \
    "ds_read_b128 v[56:59], v1 offset:640\n\t" \
    "s_waitcnt lgkmcnt(5)\n\t" \
    "v_mfma_f32_16x16x32_bf16 v[92:95], a[28:31], v[36:39], v[92:95]\n\t" \
    "ds_read_b128 v[60:63], v1 offset:19072\n\t" \
    "v_mfma_f32_16x16x32_bf16 v[96:99], a[100:103], v[32:35], v[96:99]\n\t" \
    "s_add_u32 m0, s91, 7168\n\t" \
    "v_mfma_f32_16x16x32_bf16 v[100:103], a[100:103], v[36:39], v[100:103]\n\t" \
    "global_load_lds_dwordx4 v210, s[52:53]\n\t" \
    "s_waitcnt lgkmcnt(5)\n\t" \
    "v_mfma_f32_16x16x32_bf16 v[88:91], a[32:35], v[40:43], v[88:91]\n\t" \
    "ds_read_b128 v[64:67], v2 offset:640\n\t" \
    "s_waitcnt lgkmcnt(5)\n\t" \
    "v_mfma_f32_16x16x32_bf16 v[92:95], a[32:35], v[44:47], v[92:95]\n\t" \
    "ds_read_b128 v[68:71], v2 offset:19072\n\t" \
    "v_mfma_f32_16x16x32_bf16 v[96:99], a[104:107], v[40:43], v[96:99]\n\t" \
    "s_add_u32 m0, s91, 8192\n\t" \
    "v_mfma_f32_16x16x32_bf16 v[100:103], a[104:107], v[44:47], v[100:103]\n\t" \
    "global_load_lds_dwordx4 v211, s[52:53]\n\t" \
    "s_waitcnt lgkmcnt(5)\n\t" \
    "v_mfma_f32_16x16x32_bf16 v[88:91], a[36:39], v[48:51], v[88:91]\n\t" \
    "ds_read_b128 v[24:27], v1 offset:768\n\t" \
    "s_waitcnt lgkmcnt(5)\n\t" \
    "v_mfma_f32_16x16x32_bf16 v[92:95], a[36:39], v[52:55], v[92:95]\n\t" \
    "ds_read_b128 v[28:31], v1 offset:19200\n\t" \
    "v_mfma_f32_16x16x32_bf16 v[96:99], a[108:111], v[48:51], v[96:99]\n\t" \
    "v_mfma_f32_16x16x32_bf16 v[100:103], a[108:111], v[52:55], v[100:103]\n\t" \
    "s_waitcnt lgkmcnt(5)\n\t" \
    "v_mfma_f32_16x16x32_bf16 v[88:91], a[40:43], v[56:59], v[88:91]\n\t" \
    "ds_read_b128 v[32:35], v2 offset:768\n\t" \
    "s_waitcnt lgkmcnt(5)\n\t" \
    "v_mfma_f32_16x16x32_bf16 v[92:95], a[40:43], v[60:63], v[92:95]\n\t" \
    "ds_read_b128 v[36:39], v2 offset:19200\n\t" \
    "v_mfma_f32_16x16x32_bf16 v[96:99], a[112:115], v[56:59], v[96:99]\n\t" \
    "s_waitcnt vmcnt(11)\n\t" \
    "s_barrier\n\t" \
    "v_mfma_f32_16x16x32_bf16 v[100:103], a[112:115], v[60:63], v[100:103]\n\t" \
    "s_cmp_lg_u32 s66, 0\n\t" \
    "s_cbranch_scc1 gx_SF13_%=\n\t" \
    "s_lshl_b32 s69, s67, 7\n\t" \
    "s_add_u32 s69, s69, 3211264\n\t" \
    "s_add_u32 s70, s65, -1\n\t" \
    "v_mov_b32 v156, s69\n\t" \
    "v_mov_b32 v157, s70\n\t" \
    "s_mov_b64 exec, 1\n\t" \
    "global_store_dword v156, v157, s[100:101] sc1\n\t" \
    "s_mov_b64 exec, -1\n\t" \
    "gx_SF13_%=:\n\t" \
    "s_waitcnt lgkmcnt(5)\n\t" \
    "v_mfma_f32_16x16x32_bf16 v[88:91], a[44:47], v[64:67], v[88:91]\n\t" \
    "ds_read_b128 v[40:43], v1 offset:896\n\t" \
    "s_waitcnt lgkmcnt(5)\n\t" \
    "v_mfma_f32_16x16x32_bf16 v[92:95], a[44:47], v[68:71], v[92:95]\n\t" \
    "ds_read_b128 v[44:47], v1 offset:19328\n\t" \
    "v_mfma_f32_16x16x32_bf16 v[96:99], a[116:119], v[64:67], v[96:99]\n\t" \
    "v_add_u32 v22, 0x9000, v1\n\t" \
    "v_add_u32 v23, 0x9000, v2\n\t" \
    "v_mfma_f32_16x16x32_bf16 v[100:103], a[116:119], v[68:71], v[100:103]\n\t" \
    "s_cmp_lt_u32 s65, 55\n\t" \
    "s_cselect_b32 s84, 9, 2\n\t" \
    "s_waitcnt lgkmcnt(5)\n\t" \
    "v_mfma_f32_16x16x32_bf16 v[88:91], a[48:51], v[24:27], v[88:91]\n\t" \
    "ds_read_b128 v[48:51], v2 offset:896\n\t" \
    "s_waitcnt lgkmcnt(5)\n\t" \
    "v_mfma_f32_16x16x32_bf16 v[92:95], a[48:51], v[28:31], v[92:95]\n\t" \
    "ds_read_b128 v[52:55], v2 offset:19328\n\t" \
    "v_mfma_f32_16x16x32_bf16 v[96:99], a[120:123], v[24:27], v[96:99]\n\t" \
    "s_add_u32 s84, s65, s84\n\t" \
    "s_min_u32 s69, s84, 63\n\t" \
    "v_mfma_f32_16x16x32_bf16 v[100:103], a[120:123], v[28:31], v[100:103]\n\t" \
    "s_mul_i32 s69, s69, 36864\n\t" \
    "s_add_u32 s48, s50, s69\n\t" \
    "s_waitcnt lgkmcnt(5)\n\t" \
    "v_mfma_f32_16x16x32_bf16 v[88:91], a[52:55], v[32:35], v[88:91]\n\t" \
    "ds_read_b128 v[56:59], v1 offset:1024\n\t" \
    "s_waitcnt lgkmcnt(5)\n\t" \
    "v_mfma_f32_16x16x32_bf16 v[92:95], a[52:55], v[36:39], v[92:95]\n\t" \
    "ds_read_b128 v[60:63], v1 offset:19456\n\t" \
    "v_mfma_f32_16x16x32_bf16 v[96:99], a[124:127], v[32:35], v[96:99]\n\t" \
    "s_addc_u32 s49, s51, 0\n\t" \
    "s_mul_i32 s70, s67, 7\n\t" \
    "v_mfma_f32_16x16x32_bf16 v[100:103], a[124:127], v[36:39], v[100:103]\n\t" \
    "s_add_u32 s69, s65, 0\n\t" \
    "s_xor_b32 s69, s69, s70\n\t" \
    "s_waitcnt lgkmcnt(5)\n\t" \
    "v_mfma_f32_16x16x32_bf16 v[88:91], a[56:59], v[40:43], v[88:91]\n\t" \
    "ds_read_b128 v[64:67], v2 offset:1024\n\t" \
    "s_waitcnt lgkmcnt(5)\n\t" \
    "v_mfma_f32_16x16x32_bf16 v[92:95], a[56:59], v[44:47], v[92:95]\n\t" \
    "ds_read_b128 v[68:71], v2 offset:19456\n\t" \
    "v_mfma_f32_16x16x32_bf16 v[96:99], a[128:131], v[40:43], v[96:99]\n\t" \
    "s_and_b32 s69, s69, 63\n\t" \
    "s_lshl_b32 s69, s69, 13\n\t" \
    "v_mfma_f32_16x16x32_bf16 v[100:103], a[128:131], v[44:47], v[100:103]\n\t" \
    "s_mul_i32 s70, s67, 524288\n\t" \
    "s_add_u32 s69, s69, s70\n\t" \
    "s_waitcnt lgkmcnt(5)\n\t" \
    "v_mfma_f32_16x16x32_bf16 v[88:91], a[60:63], v[48:51], v[88:91]\n\t" \
    "ds_read_b128 v[24:27], v22 offset:0\n\t" \
    "s_waitcnt lgkmcnt(5)\n\t" \
    "v_mfma_f32_16x16x32_bf16 v[92:95], a[60:63], v[52:55], v[92:95]\n\t" \
    "ds_read_b128 v[28:31], v22 offset:18432\n\t" \
    "v_mfma_f32_16x16x32_bf16 v[96:99], a[132:135], v[48:51], v[96:99]\n\t" \
    "s_add_u32 s69, s69, 6291456\n\t" \
    "s_add_u32 s38, s36, s69\n\t" \
    "v_mfma_f32_16x16x32_bf16 v[100:103], a[132:135], v[52:55], v[100:103]\n\t" \
    "s_addc_u32 s39, s37, 0\n\t" \
    "s_waitcnt lgkmcnt(5)\n\t" \
    "v_mfma_f32_16x16x32_bf16 v[88:91], a[64:67], v[56:59], v[88:91]\n\t" \
    "ds_read_b128 v[32:35], v23 offset:0\n\t" \
    "s_waitcnt lgkmcnt(5)\n\t" \
    "v_mfma_f32_16x16x32_bf16 v[92:95], a[64:67], v[60:63], v[92:95]\n\t" \
    "ds_read_b128 v[36:39], v23 offset:18432\n\t" \
    "v_mfma_f32_16x16x32_bf16 v[96:99], a[136:139], v[56:59], v[96:99]\n\t" \
    "v_mfma_f32_16x16x32_bf16 v[100:103], a[136:139], v[60:63], v[100:103]\n\t" \
    "s_waitcnt lgkmcnt(5)\n\t" \
    "v_mfma_f32_16x16x32_bf16 v[88:91], a[68:71], v[64:67], v[88:91]\n\t" \
    "ds_read_b128 v[40:43], v22 offset:128\n\t" \
    "s_waitcnt lgkmcnt(5)\n\t" \
    "v_mfma_f32_16x16x32_bf16 v[92:95], a[68:71], v[68:71], v[92:95]\n\t" \
    "ds_read_b128 v[44:47], v22 offset:18560\n\t" \
    "v_mfma_f32_16x16x32_bf16 v[96:99], a[140:143], v[64:67], v[96:99]\n\t" \
    "v_mfma_f32_16x16x32_bf16 v[100:103], a[140:143], v[68:71], v[100:103]\n\t" \
    "s_waitcnt lgkmcnt(5)\n\t" \
    "v_mfma_f32_16x16x32_bf16 v[72:75], a[144:147], v[24:27], 0\n\t" \
    "ds_read_b128 v[48:51], v23 offset:128\n\t" \
    "s_waitcnt lgkmcnt(5)\n\t" \
    "v_mfma_f32_16x16x32_bf16 v[76:79], a[144:147], v[28:31], 0\n\t" \
    "ds_read_b128 v[52:55], v23 offset:18560\n\t" \
    "v_mfma_f32_16x16x32_bf16 v[80:83], a[216:219], v[24:27], 0\n\t" \
    "s_add_u32 m0, s91, 36864\n\t" \
    "v_mfma_f32_16x16x32_bf16 v[84:87], a[216:219], v[28:31], 0\n\t" \
    "global_load_lds_dwordx4 v203, s[48:49]\n\t" \
    "s_waitcnt lgkmcnt(5)\n\t" \
    "v_mfma_f32_16x16x32_bf16 v[72:75], a[148:151], v[32:35], v[72:75]\n\t" \
    "ds_read_b128 v[56:59], v22 offset:256\n\t" \
    "s_waitcnt lgkmcnt(5)\n\t" \
    "v_mfma_f32_16x16x32_bf16 v[76:79], a[148:151], v[36:39], v[76:79]\n\t" \
    "ds_read_b128 v[60:63], v22 offset:18688\n\t" \
    "v_mfma_f32_16x16x32_bf16 v[80:83], a[220:223], v[32:35], v[80:83]\n\t" \
    "s_add_u32 m0, s91, 37888\n\t" \
    "v_mfma_f32_16x16x32_bf16 v[84:87], a[220:223], v[36:39], v[84:87]\n\t" \
    "global_load_lds_dwordx4 v204, s[48:49]\n\t" \
    "s_waitcnt lgkmcnt(5)\n\t" \
    "v_mfma_f32_16x16x32_bf16 v[72:75], a[152:155], v[40:43], v[72:75]\n\t" \
    "ds_read_b128 v[64:67], v23 offset:256\n\t" \
    "s_waitcnt lgkmcnt(5)\n\t" \
    "v_mfma_f32_16x16x32_bf16 v[76:79], a[152:155], v[44:47], v[76:79]\n\t" \
    "ds_read_b128 v[68:71], v23 offset:18688\n\t" \
    "v_cvt_pk_bf16_f32 v104, v88, v89\n\t" \
    "v_mfma_f32_16x16x32_bf16 v[80:83], a[224:227], v[40:43], v[80:83]\n\t" \
    "s_add_u32 m0, s91, 38912\n\t" \
    "v_cvt_pk_bf16_f32 v105, v90, v91\n\t" \
    "v_mfma_f32_16x16x32_bf16 v[84:87], a[224:227], v[44:47], v[84:87]\n\t" \
    "global_load_lds_dwordx4 v205, s[48:49]\n\t" \
    "s_waitcnt lgkmcnt(5)\n\t" \
    "v_mfma_f32_16x16x32_bf16 v[72:75], a[156:159], v[48:51], v[72:75]\n\t" \
    "ds_read_b128 v[24:27], v22 offset:384\n\t" \
    "s_waitcnt lgkmcnt(5)\n\t" \
    "v_mfma_f32_16x16x32_bf16 v[76:79], a[156:159], v[52:55], v[76:79]\n\t" \
    "ds_read_b128 v[28:31], v22 offset:18816\n\t" \
    "v_cvt_pk_bf16_f32 v106, v96, v97\n\t" \
    "v_mfma_f32_16x16x32_bf16 v[80:83], a[228:231], v[48:51], v[80:83]\n\t" \
    "s_add_u32 m0, s91, 39936\n\t" \
    "v_cvt_pk_bf16_f32 v107, v98, v99\n\t" \
    "v_mfma_f32_16x16x32_bf16 v[84:87], a[228:231], v[52:55], v[84:87]\n\t" \
    "global_load_lds_dwordx4 v206, s[48:49]\n\t" \
    "s_waitcnt lgkmcnt(5)\n\t" \
    "v_mfma_f32_16x16x32_bf16 v[72:75], a[160:163], v[56:59], v[72:75]\n\t" \
    "ds_read_b128 v[32:35], v23 offset:384\n\t" \
    "s_waitcnt lgkmcnt(5)\n\t" \
    "v_mfma_f32_16x16x32_bf16 v[76:79], a[160:163], v[60:63], v[76:79]\n\t" \
    "ds_read_b128 v[36:39], v23 offset:18816\n\t" \
    "global_store_dwordx4 v8, v[104:107], s[38:39] offset:0 sc1\n\t" \
    "v_mfma_f32_16x16x32_bf16 v[80:83], a[232:235], v[56:59], v[80:83]\n\t" \
    "s_add_u32 m0, s91, 40960\n\t" \
    "v_cvt_pk_bf16_f32 v108, v92, v93\n\t" \
    "v_mfma_f32_16x16x32_bf16 v[84:87], a[232:235], v[60:63], v[84:87]\n\t" \
    "global_load_lds_dwordx4 v207, s[48:49]\n\t" \
    "s_waitcnt lgkmcnt(5)\n\t" \
    "v_mfma_f32_16x16x32_bf16 v[72:75], a[164:167], v[64:67], v[72:75]\n\t" \
    "ds_read_b128 v[40:43], v22 offset:512\n\t" \
    "s_waitcnt lgkmcnt(5)\n\t" \
    "v_mfma_f32_16x16x32_bf16 v[76:79], a[164:167], v[68:71], v[76:79]\n\t" \
    "ds_read_b128 v[44:47], v22 offset:18944\n\t" \
    "v_cvt_pk_bf16_f32 v109, v94, v95\n\t" \
    "v_mfma_f32_16x16x32_bf16 v[80:83], a[236:239], v[64:67], v[80:83]\n\t" \
    "s_add_u32 m0, s91, 41984\n\t" \
    "v_cvt_pk_bf16_f32 v110, v100, v101\n\t" \
    "v_mfma_f32_16x16x32_bf16 v[84:87], a[236:239], v[68:71], v[84:87]\n\t" \
    "global_load_lds_dwordx4 v208, s[48:49]\n\t" \
    "s_waitcnt lgkmcnt(5)\n\t" \
    "v_mfma_f32_16x16x32_bf16 v[72:75], a[168:171], v[24:27], v[72:75]\n\t" \
    "ds_read_b128 v[48:51], v23 offset:512\n\t" \
    "s_waitcnt lgkmcnt(5)\n\t" \
    "v_mfma_f32_16x16x32_bf16 v[76:79], a[168:171], v[28:31], v[76:79]\n\t" \
    "ds_read_b128 v[52:55], v23 offset:18944\n\t" \
    "v_cvt_pk_bf16_f32 v111, v102, v103\n\t" \
    "v_mfma_f32_16x16x32_bf16 v[80:83], a[240:243], v[24:27], v[80:83]\n\t" \
    "s_add_u32 m0, s91, 43008\n\t" \
    "global_store_dwordx4 v8, v[108:111], s[38:39] offset:1024 sc1\n\t" \
    "v_mfma_f32_16x16x32_bf16 v[84:87], a[240:243], v[28:31], v[84:87]\n\t" \
    "global_load_lds_dwordx4 v209, s[48:49]\n\t" \
    "s_waitcnt lgkmcnt(5)\n\t" \
    "v_mfma_f32_16x16x32_bf16 v[72:75], a[172:175], v[32:35], v[72:75]\n\t" \
    "ds_read_b128 v[56:59], v22 offset:640\n\t" \
    "s_waitcnt lgkmcnt(5)\n\t" \
    "v_mfma_f32_16x16x32_bf16 v[76:79], a[172:175], v[36:39], v[76:79]\n\t" \
    "ds_read_b128 v[60:63], v22 offset:19072\n\t" \
    "v_mfma_f32_16x16x32_bf16 v[80:83], a[244:247], v[32:35], v[80:83]\n\t" \
    "s_add_u32 m0, s91, 44032\n\t" \
    "v_mfma_f32_16x16x32_bf16 v[84:87], a[244:247], v[36:39], v[84:87]\n\t" \
    "global_load_lds_dwordx4 v210, s[48:49]\n\t" \
    "s_waitcnt lgkmcnt(5)\n\t" \
    "v_mfma_f32_16x16x32_bf16 v[72:75], a[176:179], v[40:43], v[72:75]\n\t" \
    "ds_read_b128 v[64:67], v23 offset:640\n\t" \
    "s_waitcnt lgkmcnt(5)\n\t" \
    "v_mfma_f32_16x16x32_bf16 v[76:79], a[176:179], v[44:47], v[76:79]\n\t" \
    "ds_read_b128 v[68:71], v23 offset:19072\n\t" \
    "v_mfma_f32_16x16x32_bf16 v[80:83], a[248:251], v[40:43], v[80:83]\n\t" \
    "s_add_u32 m0, s91, 45056\n\t" \
    "v_mfma_f32_16x16x32_bf16 v[84:87], a[248:251], v[44:47], v[84:87]\n\t" \
    "global_load_lds_dwordx4 v211, s[48:49]\n\t" \
    "s_waitcnt lgkmcnt(5)\n\t" \
    "v_mfma_f32_16x16x32_bf16 v[72:75], a[180:183], v[48:51], v[72:75]\n\t" \
    "ds_read_b128 v[24:27], v22 offset:768\n\t" \
    "s_waitcnt lgkmcnt(5)\n\t" \
    "v_mfma_f32_16x16x32_bf16 v[76:79], a[180:183], v[52:55], v[76:79]\n\t" \
    "ds_read_b128 v[28:31], v22 offset:19200\n\t" \
    "v_mfma_f32_16x16x32_bf16 v[80:83], a[252:255], v[48:51], v[80:83]\n\t" \
    "v_mfma_f32_16x16x32_bf16 v[84:87], a[252:255], v[52:55], v[84:87]\n\t" \
    "s_waitcnt lgkmcnt(5)\n\t" \
    "v_mfma_f32_16x16x32_bf16 v[72:75], a[184:187], v[56:59], v[72:75]\n\t" \
    "ds_read_b128 v[32:35], v23 offset:768\n\t" \
    "s_waitcnt lgkmcnt(5)\n\t" \
    "v_mfma_f32_16x16x32_bf16 v[76:79], a[184:187], v[60:63], v[76:79]\n\t" \
    "ds_read_b128 v[36:39], v23 offset:19200\n\t" \
    "v_mfma_f32_16x16x32_bf16 v[80:83], v[224:227], v[56:59], v[80:83]\n\t" \
    "s_waitcnt vmcnt(11)\n\t" \
    "s_barrier\n\t" \
    "v_mfma_f32_16x16x32_bf16 v[84:87], v[224:227], v[60:63], v[84:87]\n\t" \
    "s_add_u32 s71, s65, 7\n\t" \
    "s_cmp_lg_u32 s66, 0\n\t" \
    "s_cbranch_scc1 gx_SF14_%=\n\t" \
    "s_lshl_b32 s69, s67, 7\n\t" \
    "s_add_u32 s69, s69, 3149824\n\t" \
    "s_add_u32 s70, s71, 0\n\t" \
    "v_mov_b32 v156, s69\n\t" \
    "v_mov_b32 v157, s70\n\t" \
    "s_mov_b64 exec, 1\n\t" \
    "global_store_dword v156, v157, s[100:101] sc1\n\t" \
    "s_mov_b64 exec, -1\n\t" \
    "gx_SF14_%=:\n\t" \
    "s_waitcnt lgkmcnt(5)\n\t" \
    "v_mfma_f32_16x16x32_bf16 v[72:75], a[188:191], v[64:67], v[72:75]\n\t" \
    "ds_read_b128 v[40:43], v22 offset:896\n\t" \
    "s_waitcnt lgkmcnt(5)\n\t" \
    "v_mfma_f32_16x16x32_bf16 v[76:79], a[188:191], v[68:71], v[76:79]\n\t" \
    "ds_read_b128 v[44:47], v22 offset:19328\n\t" \
    "v_mfma_f32_16x16x32_bf16 v[80:83], v[228:231], v[64:67], v[80:83]\n\t" \
    "s_xor_b32 s90, s90, 0x12000\n\t" \
    "v_add_u32 v1, s90, v0\n\t" \
    "v_mfma_f32_16x16x32_bf16 v[84:87], v[228:231], v[68:71], v[84:87]\n\t" \
    "v_xor_b32 v2, 64, v1\n\t" \
    "s_add_u32 s84, s65, 2\n\t" \
    "s_waitcnt lgkmcnt(5)\n\t" \
    "v_mfma_f32_16x16x32_bf16 v[72:75], a[192:195], v[24:27], v[72:75]\n\t" \
    "ds_read_b128 v[48:51], v23 offset:896\n\t" \
    "s_waitcnt lgkmcnt(5)\n\t" \
    "v_mfma_f32_16x16x32_bf16 v[76:79], a[192:195], v[28:31], v[76:79]\n\t" \
    "ds_read_b128 v[52:55], v23 offset:19328\n\t" \
    "v_mfma_f32_16x16x32_bf16 v[80:83], v[232:235], v[24:27], v[80:83]\n\t" \
    "s_min_u32 s69, s84, 63\n\t" \
    "s_mul_i32 s69, s69, 36864\n\t" \
    "v_mfma_f32_16x16x32_bf16 v[84:87], v[232:235], v[28:31], v[84:87]\n\t" \
    "s_add_u32 s52, s50, s69\n\t" \
    "s_addc_u32 s53, s51, 0\n\t" \
    "s_waitcnt lgkmcnt(5)\n\t" \
    "v_mfma_f32_16x16x32_bf16 v[72:75], a[196:199], v[32:35], v[72:75]\n\t" \
    "ds_read_b128 v[56:59], v22 offset:1024\n\t" \
    "s_waitcnt lgkmcnt(5)\n\t" \
    "v_mfma_f32_16x16x32_bf16 v[76:79], a[196:199], v[36:39], v[76:79]\n\t" \
    "ds_read_b128 v[60:63], v22 offset:19456\n\t" \
    "v_mfma_f32_16x16x32_bf16 v[80:83], v[236:239], v[32:35], v[80:83]\n\t" \
    "s_mul_i32 s70, s67, 7\n\t" \
    "s_add_u32 s69, s65, 8\n\t" \
    "v_mfma_f32_16x16x32_bf16 v[84:87], v[236:239], v[36:39], v[84:87]\n\t" \
    "s_xor_b32 s69, s69, s70\n\t" \
    "s_and_b32 s69, s69, 63\n\t" \
    "s_waitcnt lgkmcnt(5)\n\t" \
    "v_mfma_f32_16x16x32_bf16 v[72:75], a[200:203], v[40:43], v[72:75]\n\t" \
    "ds_read_b128 v[64:67], v23 offset:1024\n\t" \
    "s_waitcnt lgkmcnt(5)\n\t" \
    "v_mfma_f32_16x16x32_bf16 v[76:79], a[200:203], v[44:47], v[76:79]\n\t" \
    "ds_read_b128 v[68:71], v23 offset:19456\n\t" \
    "v_mfma_f32_16x16x32_bf16 v[80:83], v[240:243], v[40:43], v[80:83]\n\t" \
    "s_lshl_b32 s69, s69, 13\n\t" \
    "s_mul_i32 s70, s67, 524288\n\t" \
    "v_mfma_f32_16x16x32_bf16 v[84:87], v[240:243], v[44:47], v[84:87]\n\t" \
    "s_add_u32 s69, s69, s70\n\t" \
    "s_add_u32 s69, s69, 23068672\n\t" \
    "s_waitcnt lgkmcnt(5)\n\t" \
    "v_mfma_f32_16x16x32_bf16 v[72:75], a[204:207], v[48:51], v[72:75]\n\t" \
    "ds_read_b128 v[24:27], v1 offset:0\n\t" \
    "s_waitcnt lgkmcnt(5)\n\t" \
    "v_mfma_f32_16x16x32_bf16 v[76:79], a[204:207], v[52:55], v[76:79]\n\t" \
    "ds_read_b128 v[28:31], v1 offset:18432\n\t" \
    "v_mfma_f32_16x16x32_bf16 v[80:83], v[244:247], v[48:51], v[80:83]\n\t" \
    "s_add_u32 s40, s36, s69\n\t" \
    "s_addc_u32 s41, s37, 0\n\t" \
    "v_mfma_f32_16x16x32_bf16 v[84:87], v[244:247], v[52:55], v[84:87]\n\t" \
    "s_sub_u32 s91, s85, s91\n\t" \
    "s_add_u32 s65, s65, 1\n\t" \
    "s_waitcnt lgkmcnt(5)\n\t" \
    "v_mfma_f32_16x16x32_bf16 v[72:75], a[208:211], v[56:59], v[72:75]\n\t" \
    "ds_read_b128 v[32:35], v2 offset:0\n\t" \
    "s_waitcnt lgkmcnt(5)\n\t" \
    "v_mfma_f32_16x16x32_bf16 v[76:79], a[208:211], v[60:63], v[76:79]\n\t" \
    "ds_read_b128 v[36:39], v2 offset:18432\n\t" \
    "v_mfma_f32_16x16x32_bf16 v[80:83], v[248:251], v[56:59], v[80:83]\n\t" \
    "v_mfma_f32_16x16x32_bf16 v[84:87], v[248:251], v[60:63], v[84:87]\n\t" \
    "s_waitcnt lgkmcnt(5)\n\t" \
    "v_mfma_f32_16x16x32_bf16 v[72:75], a[212:215], v[64:67], v[72:75]\n\t" \
    "ds_read_b128 v[40:43], v1 offset:128\n\t" \
    "s_waitcnt lgkmcnt(5)\n\t" \
    "v_mfma_f32_16x16x32_bf16 v[76:79], a[212:215], v[68:71], v[76:79]\n\t" \
    "ds_read_b128 v[44:47], v1 offset:18560\n\t" \
    "v_mfma_f32_16x16x32_bf16 v[80:83], v[252:255], v[64:67], v[80:83]\n\t" \
    "v_mfma_f32_16x16x32_bf16 v[84:87], v[252:255], v[68:71], v[84:87]\n\t" \
    "s_cmp_lt_u32 s65, 56\n\t" \
    "s_cbranch_scc1 gx_l27_%=\n\t" \
    "v_cvt_pk_bf16_f32 v104, v72, v73\n\t" \
    "v_cvt_pk_bf16_f32 v105, v74, v75\n\t" \
    "s_nop 2\n\t" \
    "v_cvt_pk_bf16_f32 v106, v80, v81\n\t" \
    "v_cvt_pk_bf16_f32 v107, v82, v83\n\t" \
    "global_store_dwordx4 v8, v[104:107], s[40:41] offset:0 sc1\n\t" \
    "v_cvt_pk_bf16_f32 v108, v76, v77\n\t" \
    "v_cvt_pk_bf16_f32 v109, v78, v79\n\t" \
    "v_cvt_pk_bf16_f32 v110, v84, v85\n\t" \
    "v_cvt_pk_bf16_f32 v111, v86, v87\n\t" \
    "global_store_dwordx4 v8, v[108:111], s[40:41] offset:1024 sc1\n\t" \
    "gx_l315_%=:\n\t" \
    "s_add_u32 s84, s65, 2\n\t" \
    "s_min_u32 s69, s84, 63\n\t" \
    "s_mul_i32 s69, s69, 36864\n\t" \
    "s_add_u32 s52, s50, s69\n\t" \
    "s_addc_u32 s53, s51, 0\n\t" \
    "s_add_u32 s90, s90, 36864\n\t" \
    "s_cmp_eq_u32 s90, 147456\n\t" \
    "s_cselect_b32 s90, 0, s90\n\t" \
    "s_add_u32 s91, s90, 36864\n\t" \
    "s_cmp_eq_u32 s91, 147456\n\t" \
    "s_cselect_b32 s91, 0, s91\n\t" \
    "s_add_u32 s91, s91, s89\n\t" \
    "v_add_u32 v22, s90, v0\n\t" \
    "v_xor_b32 v23, 64, v22\n\t" \
    "s_mul_i32 s70, s67, 7\n\t" \
    "s_add_u32 s69, s65, 0\n\t" \
    "s_xor_b32 s69, s69, s70\n\t" \
    "s_and_b32 s69, s69, 63\n\t" \
    "s_lshl_b32 s69, s69, 13\n\t" \
    "s_mul_i32 s70, s67, 524288\n\t" \
    "s_add_u32 s69, s69, s70\n\t" \
    "s_add_u32 s69, s69, 6291456\n\t" \
    "s_add_u32 s38, s36, s69\n\t" \
    "s_addc_u32 s39, s37, 0\n\t" \
    "s_waitcnt lgkmcnt(5)\n\t" \
    "v_mfma_f32_16x16x32_bf16 v[88:91], a[0:3], v[24:27], 0\n\t" \
    "ds_read_b128 v[48:51], v2 offset:128\n\t" \
    "s_waitcnt lgkmcnt(5)\n\t" \
    "v_mfma_f32_16x16x32_bf16 v[92:95], a[0:3], v[28:31], 0\n\t" \
    "ds_read_b128 v[52:55], v2 offset:18560\n\t" \
    "v_mfma_f32_16x16x32_bf16 v[96:99], a[72:75], v[24:27], 0\n\t" \
    "s_add_u32 m0, s91, 0\n\t" \
    "v_mfma_f32_16x16x32_bf16 v[100:103], a[72:75], v[28:31], 0\n\t" \
    "global_load_lds_dwordx4 v203, s[52:53]\n\t" \
    "s_waitcnt lgkmcnt(5)\n\t" \
    "v_mfma_f32_16x16x32_bf16 v[88:91], a[4:7], v[32:35], v[88:91]\n\t" \
    "ds_read_b128 v[56:59], v1 offset:256\n\t" \
    "s_waitcnt lgkmcnt(5)\n\t" \
    "v_mfma_f32_16x16x32_bf16 v[92:95], a[4:7], v[36:39], v[92:95]\n\t" \
    "ds_read_b128 v[60:63], v1 offset:18688\n\t" \
    "v_mfma_f32_16x16x32_bf16 v[96:99], a[76:79], v[32:35], v[96:99]\n\t" \
    "s_add_u32 m0, s91, 1024\n\t" \
    "v_mfma_f32_16x16x32_bf16 v[100:103], a[76:79], v[36:39], v[100:103]\n\t" \
    "global_load_lds_dwordx4 v204, s[52:53]\n\t" \
    "s_waitcnt lgkmcnt(5)\n\t" \
    "v_mfma_f32_16x16x32_bf16 v[88:91], a[8:11], v[40:43], v[88:91]\n\t" \
    "ds_read_b128 v[64:67], v2 offset:256\n\t" \
    "s_waitcnt lgkmcnt(5)\n\t" \
    "v_mfma_f32_16x16x32_bf16 v[92:95], a[8:11], v[44:47], v[92:95]\n\t" \
    "ds_read_b128 v[68:71], v2 offset:18688\n\t" \
    "v_mfma_f32_16x16x32_bf16 v[96:99], a[80:83], v[40:43], v[96:99]\n\t" \
    "s_add_u32 m0, s91, 2048\n\t" \
    "v_mfma_f32_16x16x32_bf16 v[100:103], a[80:83], v[44:47], v[100:103]\n\t" \
    "global_load_lds_dwordx4 v205, s[52:53]\n\t" \
    "s_waitcnt lgkmcnt(5)\n\t" \
    "v_mfma_f32_16x16x32_bf16 v[88:91], a[12:15], v[48:51], v[88:91]\n\t" \
    "ds_read_b128 v[24:27], v1 offset:384\n\t" \
    "s_waitcnt lgkmcnt(5)\n\t" \
    "v_mfma_f32_16x16x32_bf16 v[92:95], a[12:15], v[52:55], v[92:95]\n\t" \
    "ds_read_b128 v[28:31], v1 offset:18816\n\t" \
    "v_mfma_f32_16x16x32_bf16 v[96:99], a[84:87], v[48:51], v[96:99]\n\t" \
    "s_add_u32 m0, s91, 3072\n\t" \
    "v_mfma_f32_16x16x32_bf16 v[100:103], a[84:87], v[52:55], v[100:103]\n\t" \
    "global_load_lds_dwordx4 v206, s[52:53]\n\t" \
    "s_waitcnt lgkmcnt(5)\n\t" \
    "v_mfma_f32_16x16x32_bf16 v[88:91], a[16:19], v[56:59], v[88:91]\n\t" \
    "ds_read_b128 v[32:35], v2 offset:384\n\t" \
    "s_waitcnt lgkmcnt(5)\n\t" \
    "v_mfma_f32_16x16x32_bf16 v[92:95], a[16:19], v[60:63], v[92:95]\n\t" \
    "ds_read_b128 v[36:39], v2 offset:18816\n\t" \
    "v_mfma_f32_16x16x32_bf16 v[96:99], a[88:91], v[56:59], v[96:99]\n\t" \
    "s_add_u32 m0, s91, 4096\n\t" \
    "v_mfma_f32_16x16x32_bf16 v[100:103], a[88:91], v[60:63], v[100:103]\n\t" \
    "global_load_lds_dwordx4 v207, s[52:53]\n\t" \
    "s_waitcnt lgkmcnt(5)\n\t" \
    "v_mfma_f32_16x16x32_bf16 v[88:91], a[20:23], v[64:67], v[88:91]\n\t" \
    "ds_read_b128 v[40:43], v1 offset:512\n\t" \
    "s_waitcnt lgkmcnt(5)\n\t" \
    "v_mfma_f32_16x16x32_bf16 v[92:95], a[20:23], v[68:71], v[92:95]\n\t" \
    "ds_read_b128 v[44:47], v1 offset:18944\n\t" \
    "v_mfma_f32_16x16x32_bf16 v[96:99], a[92:95], v[64:67], v[96:99]\n\t" \
    "s_add_u32 m0, s91, 5120\n\t" \
    "v_mfma_f32_16x16x32_bf16 v[100:103], a[92:95], v[68:71], v[100:103]\n\t" \
    "global_load_lds_dwordx4 v208, s[52:53]\n\t" \
    "s_waitcnt lgkmcnt(5)\n\t" \
    "v_mfma_f32_16x16x32_bf16 v[88:91], a[24:27], v[24:27], v[88:91]\n\t" \
    "ds_read_b128 v[48:51], v2 offset:512\n\t" \
    "s_waitcnt lgkmcnt(5)\n\t" \
    "v_mfma_f32_16x16x32_bf16 v[92:95], a[24:27], v[28:31], v[92:95]\n\t" \
    "ds_read_b128 v[52:55], v2 offset:18944\n\t" \
    "v_mfma_f32_16x16x32_bf16 v[96:99], a[96:99], v[24:27], v[96:99]\n\t" \
    "s_add_u32 m0, s91, 6144\n\t" \
    "v_mfma_f32_16x16x32_bf16 v[100:103], a[96:99], v[28:31], v[100:103]\n\t" \
    "global_load_lds_dwordx4 v209, s[52:53]\n\t" \
    "s_waitcnt lgkmcnt(5)\n\t" \
    "v_mfma_f32_16x16x32_bf16 v[88:91], a[28:31], v[32:35], v[88:91]\n\t" \
    "ds_read_b128 v[56:59], v1 offset:640\n\t" \
    "s_waitcnt lgkmcnt(5)\n\t" \
    "v_mfma_f32_16x16x32_bf16 v[92:95], a[28:31], v[36:39], v[92:95]\n\t" \
    "ds_read_b128 v[60:63], v1 offset:19072\n\t" \
    "v_mfma_f32_16x16x32_bf16 v[96:99], a[100:103], v[32:35], v[96:99]\n\t" \
    "s_add_u32 m0, s91, 7168\n\t" \
    "v_mfma_f32_16x16x32_bf16 v[100:103], a[100:103], v[36:39], v[100:103]\n\t" \
    "global_load_lds_dwordx4 v210, s[52:53]\n\t" \
    "s_waitcnt lgkmcnt(5)\n\t" \
    "v_mfma_f32_16x16x32_bf16 v[88:91], a[32:35], v[40:43], v[88:91]\n\t" \
    "ds_read_b128 v[64:67], v2 offset:640\n\t" \
    "s_waitcnt lgkmcnt(5)\n\t" \
    "v_mfma_f32_16x16x32_bf16 v[92:95], a[32:35], v[44:47], v[92:95]\n\t" \
    "ds_read_b128 v[68:71], v2 offset:19072\n\t" \
    "v_mfma_f32_16x16x32_bf16 v[96:99], a[104:107], v[40:43], v[96:99]\n\t" \
    "s_add_u32 m0, s91, 8192\n\t" \
    "v_mfma_f32_16x16x32_bf16 v[100:103], a[104:107], v[44:47], v[100:103]\n\t" \
    "global_load_lds_dwordx4 v211, s[52:53]\n\t" \
    "s_waitcnt lgkmcnt(5)\n\t" \
    "v_mfma_f32_16x16x32_bf16 v[88:91], a[36:39], v[48:51], v[88:91]\n\t" \
    "ds_read_b128 v[24:27], v1 offset:768\n\t" \
    "s_waitcnt lgkmcnt(5)\n\t" \
    "v_mfma_f32_16x16x32_bf16 v[92:95], a[36:39], v[52:55], v[92:95]\n\t" \
    "ds_read_b128 v[28:31], v1 offset:19200\n\t" \
    "v_mfma_f32_16x16x32_bf16 v[96:99], a[108:111], v[48:51], v[96:99]\n\t" \
    "v_mfma_f32_16x16x32_bf16 v[100:103], a[108:111], v[52:55], v[100:103]\n\t" \
    "s_waitcnt lgkmcnt(5)\n\t" \
    "v_mfma_f32_16x16x32_bf16 v[88:91], a[40:43], v[56:59], v[88:91]\n\t" \
    "ds_read_b128 v[32:35], v2 offset:768\n\t" \
    "s_waitcnt lgkmcnt(5)\n\t" \
    "v_mfma_f32_16x16x32_bf16 v[92:95], a[40:43], v[60:63], v[92:95]\n\t" \
    "ds_read_b128 v[36:39], v2 offset:19200\n\t" \
    "v_mfma_f32_16x16x32_bf16 v[96:99], a[112:115], v[56:59], v[96:99]\n\t" \
    "s_waitcnt vmcnt(9)\n\t" \
    "s_barrier\n\t" \
    "v_mfma_f32_16x16x32_bf16 v[100:103], a[112:115], v[60:63], v[100:103]\n\t" \
    "s_cmp_lg_u32 s66, 0\n\t" \
    "s_cbranch_scc1 gx_SF16_%=\n\t" \
    "s_lshl_b32 s69, s67, 7\n\t" \
    "s_add_u32 s69, s69, 3211264\n\t" \
    "s_add_u32 s70, s65, -1\n\t" \
    "v_mov_b32 v156, s69\n\t" \
    "v_mov_b32 v157, s70\n\t" \
    "s_mov_b64 exec, 1\n\t" \
    "global_store_dword v156, v157, s[100:101] sc1\n\t" \
    "s_mov_b64 exec, -1\n\t" \
    "gx_SF16_%=:\n\t" \
    "s_cmp_lg_u32 s66, 1\n\t" \
    "s_cbranch_scc1 gx_SF17_%=\n\t" \
    "s_lshl_b32 s69, s67, 7\n\t" \
    "s_add_u32 s69, s69, 3149824\n\t" \
    "s_add_u32 s70, s65, 40\n\t" \
    "v_mov_b32 v156, s69\n\t" \
    "v_mov_b32 v157, s70\n\t" \
    "s_mov_b64 exec, 1\n\t" \
    "global_store_dword v156, v157, s[100:101] sc1\n\t" \
    "s_mov_b64 exec, -1\n\t" \
    "gx_SF17_%=:\n\t" \
    "s_waitcnt lgkmcnt(5)\n\t" \
    "v_mfma_f32_16x16x32_bf16 v[88:91], a[44:47], v[64:67], v[88:91]\n\t" \
    "ds_read_b128 v[40:43], v1 offset:896\n\t" \
    "s_waitcnt lgkmcnt(5)\n\t" \
    "v_mfma_f32_16x16x32_bf16 v[92:95], a[44:47], v[68:71], v[92:95]\n\t" \
    "ds_read_b128 v[44:47], v1 offset:19328\n\t" \
    "v_mfma_f32_16x16x32_bf16 v[96:99], a[116:119], v[64:67], v[96:99]\n\t" \
    "v_mfma_f32_16x16x32_bf16 v[100:103], a[116:119], v[68:71], v[100:103]\n\t" \
    "s_waitcnt lgkmcnt(5)\n\t" \
    "v_mfma_f32_16x16x32_bf16 v[88:91], a[48:51], v[24:27], v[88:91]\n\t" \
    "ds_read_b128 v[48:51], v2 offset:896\n\t" \
    "s_waitcnt lgkmcnt(5)\n\t" \
    "v_mfma_f32_16x16x32_bf16 v[92:95], a[48:51], v[28:31], v[92:95]\n\t" \
    "ds_read_b128 v[52:55], v2 offset:19328\n\t" \
    "v_mfma_f32_16x16x32_bf16 v[96:99], a[120:123], v[24:27], v[96:99]\n\t" \
    "v_mfma_f32_16x16x32_bf16 v[100:103], a[120:123], v[28:31], v[100:103]\n\t" \
    "s_waitcnt lgkmcnt(5)\n\t" \
    "v_mfma_f32_16x16x32_bf16 v[88:91], a[52:55], v[32:35], v[88:91]\n\t" \
    "ds_read_b128 v[56:59], v1 offset:1024\n\t" \
    "s_waitcnt lgkmcnt(5)\n\t" \
    "v_mfma_f32_16x16x32_bf16 v[92:95], a[52:55], v[36:39], v[92:95]\n\t" \
    "ds_read_b128 v[60:63], v1 offset:19456\n\t" \
    "v_mfma_f32_16x16x32_bf16 v[96:99], a[124:127], v[32:35], v[96:99]\n\t" \
    "v_mfma_f32_16x16x32_bf16 v[100:103], a[124:127], v[36:39], v[100:103]\n\t" \
    "s_waitcnt lgkmcnt(5)\n\t" \
    "v_mfma_f32_16x16x32_bf16 v[88:91], a[56:59], v[40:43], v[88:91]\n\t" \
    "ds_read_b128 v[64:67], v2 offset:1024\n\t" \
    "s_waitcnt lgkmcnt(5)\n\t" \
    "v_mfma_f32_16x16x32_bf16 v[92:95], a[56:59], v[44:47], v[92:95]\n\t" \
    "ds_read_b128 v[68:71], v2 offset:19456\n\t" \
    "v_mfma_f32_16x16x32_bf16 v[96:99], a[128:131], v[40:43], v[96:99]\n\t" \
    "v_mfma_f32_16x16x32_bf16 v[100:103], a[128:131], v[44:47], v[100:103]\n\t" \
    "s_waitcnt lgkmcnt(5)\n\t" \
    "v_mfma_f32_16x16x32_bf16 v[88:91], a[60:63], v[48:51], v[88:91]\n\t" \
    "ds_read_b128 v[24:27], v22 offset:0\n\t" \
    "s_waitcnt lgkmcnt(5)\n\t" \
    "v_mfma_f32_16x16x32_bf16 v[92:95], a[60:63], v[52:55], v[92:95]\n\t" \
    "ds_read_b128 v[28:31], v22 offset:18432\n\t" \
    "v_mfma_f32_16x16x32_bf16 v[96:99], a[132:135], v[48:51], v[96:99]\n\t" \
    "v_mfma_f32_16x16x32_bf16 v[100:103], a[132:135], v[52:55], v[100:103]\n\t" \
    "s_waitcnt lgkmcnt(5)\n\t" \
    "v_mfma_f32_16x16x32_bf16 v[88:91], a[64:67], v[56:59], v[88:91]\n\t" \
    "ds_read_b128 v[32:35], v23 offset:0\n\t" \
    "s_waitcnt lgkmcnt(5)\n\t" \
    "v_mfma_f32_16x16x32_bf16 v[92:95], a[64:67], v[60:63], v[92:95]\n\t" \
    "ds_read_b128 v[36:39], v23 offset:18432\n\t" \
    "v_mfma_f32_16x16x32_bf16 v[96:99], a[136:139], v[56:59], v[96:99]\n\t" \
    "v_mfma_f32_16x16x32_bf16 v[100:103], a[136:139], v[60:63], v[100:103]\n\t" \
    "s_waitcnt lgkmcnt(5)\n\t" \
    "v_mfma_f32_16x16x32_bf16 v[88:91], a[68:71], v[64:67], v[88:91]\n\t" \
    "ds_read_b128 v[40:43], v22 offset:128\n\t" \
    "s_waitcnt lgkmcnt(5)\n\t" \
    "v_mfma_f32_16x16x32_bf16 v[92:95], a[68:71], v[68:71], v[92:95]\n\t" \
    "ds_read_b128 v[44:47], v22 offset:18560\n\t" \
    "v_mfma_f32_16x16x32_bf16 v[96:99], a[140:143], v[64:67], v[96:99]\n\t" \
    "v_mfma_f32_16x16x32_bf16 v[100:103], a[140:143], v[68:71], v[100:103]\n\t" \
    "v_mov_b32 v1, v22\n\t" \
    "v_mov_b32 v2, v23\n\t" \
    "v_cvt_pk_bf16_f32 v104, v88, v89\n\t" \
    "v_cvt_pk_bf16_f32 v105, v90, v91\n\t" \
    "s_nop 2\n\t" \
    "v_cvt_pk_bf16_f32 v106, v96, v97\n\t" \
    "v_cvt_pk_bf16_f32 v107, v98, v99\n\t" \
    "global_store_dwordx4 v8, v[104:107], s[38:39] offset:0 sc1\n\t" \
    "v_cvt_pk_bf16_f32 v108, v92, v93\n\t" \
    "v_cvt_pk_bf16_f32 v109, v94, v95\n\t" \
    "v_cvt_pk_bf16_f32 v110, v100, v101\n\t" \
    "v_cvt_pk_bf16_f32 v111, v102, v103\n\t" \
    "global_store_dwordx4 v8, v[108:111], s[38:39] offset:1024 sc1\n\t" \
    "s_add_u32 s65, s65, 1\n\t" \
    "s_cmp_lt_u32 s65, 64\n\t" \
    "s_cbranch_scc1 gx_l315_%=\n\t" \
    "s_waitcnt vmcnt(0) lgkmcnt(0)\n\t" \
    "s_barrier\n\t" \
    "s_cmp_lg_u32 s66, 0\n\t" \
    "s_cbranch_scc1 gx_SF18_%=\n\t" \
    "s_lshl_b32 s69, s67, 7\n\t" \
    "s_add_u32 s69, s69, 3211264\n\t" \
    "s_add_u32 s70, s65, -1\n\t" \
    "v_mov_b32 v156, s69\n\t" \
    "v_mov_b32 v157, s70\n\t" \
    "s_mov_b64 exec, 1\n\t" \
    "global_store_dword v156, v157, s[100:101] sc1\n\t" \
    "s_mov_b64 exec, -1\n\t" \
    "gx_SF18_%=:\n\t" \
    "gx_last_%=:\n\t" \
    ""
#define ASM_GV \
    "s_load_dwordx2 s[42:43], %3, 56\n\t" \
    "s_load_dwordx2 s[44:45], %3, 64\n\t" \
    "s_load_dwordx2 s[36:37], %3, 0x58\n\t" \
    "s_load_dwordx2 s[50:51], %3, 0x48\n\t" \
    "s_mov_b32 s66, %4\n\t" \
    "s_mov_b32 s67, %5\n\t" \
    "s_mov_b32 s68, 262144\n\t" \
    "s_mov_b32 s72, 0\n\t" \
    "s_mov_b32 s83, -1\n\t" \
    "s_mov_b32 s65, 0\n\t" \
    "s_lshl_b32 s82, s66, 10\n\t" \
    "s_waitcnt lgkmcnt(0)\n\t" \
    "s_lshr_b32 s65, s67, 5\n\t" \
    "s_cselect_b32 s88, 24, 64\n\t" \
    "s_cmp_lt_u32 s65, 24\n\t" \
    "s_addc_u32 s84, s65, 1\n\t" \
    "s_cmp_lt_u32 s84, 24\n\t" \
    "s_addc_u32 s85, s84, 1\n\t" \
    "s_mov_b32 s86, -1\n\t" \
    "s_mov_b32 s87, -1\n\t" \
    "s_add_u32 s38, s36, 48234496\n\t" \
    "s_addc_u32 s39, s37, 0\n\t" \
    "s_and_b32 s69, s67, 31\n\t" \
    "s_mul_i32 s69, s69, 147456\n\t" \
    "s_add_u32 s44, s44, s69\n\t" \
    "s_addc_u32 s45, s45, 0\n\t" \
    "s_mul_i32 s89, s66, 9216\n\t" \
    "s_mov_b32 s90, 0\n\t" \
    "v_mov_b32 v196, -1\n\t" \
    "v_mov_b32 v197, -1\n\t" \
    "v_mov_b32 v198, -1\n\t" \
    "v_mov_b32 v199, -1\n\t" \
    "s_add_u32 s46, s42, 2097152\n\t" \
    "s_addc_u32 s47, s43, 0\n\t" \
    "s_add_u32 m0, s82, 65536\n\t" \
    "s_nop 0\n\t" \
    "global_load_lds_dwordx4 v200, s[46:47]\n\t" \
    "s_add_u32 s46, s42, 2228224\n\t" \
    "s_addc_u32 s47, s43, 0\n\t" \
    "s_add_u32 m0, s82, 69632\n\t" \
    "s_nop 0\n\t" \
    "global_load_lds_dwordx4 v200, s[46:47]\n\t" \
    "s_add_u32 s46, s42, 2359296\n\t" \
    "s_addc_u32 s47, s43, 0\n\t" \
    "s_add_u32 m0, s82, 73728\n\t" \
    "s_nop 0\n\t" \
    "global_load_lds_dwordx4 v200, s[46:47]\n\t" \
    "s_add_u32 s46, s42, 2490368\n\t" \
    "s_addc_u32 s47, s43, 0\n\t" \
    "s_add_u32 m0, s82, 77824\n\t" \
    "s_nop 0\n\t" \
    "global_load_lds_dwordx4 v200, s[46:47]\n\t" \
    "s_add_u32 s46, s42, 2621440\n\t" \
    "s_addc_u32 s47, s43, 0\n\t" \
    "s_add_u32 m0, s82, 81920\n\t" \
    "s_nop 0\n\t" \
    "global_load_lds_dwordx4 v200, s[46:47]\n\t" \
    "s_add_u32 s46, s42, 2752512\n\t" \
    "s_addc_u32 s47, s43, 0\n\t" \
    "s_add_u32 m0, s82, 86016\n\t" \
    "s_nop 0\n\t" \
    "global_load_lds_dwordx4 v200, s[46:47]\n\t" \
    "s_add_u32 s46, s42, 2883584\n\t" \
    "s_addc_u32 s47, s43, 0\n\t" \
    "s_add_u32 m0, s82, 90112\n\t" \
    "s_nop 0\n\t" \
    "global_load_lds_dwordx4 v200, s[46:47]\n\t" \
    "s_add_u32 s46, s42, 3014656\n\t" \
    "s_addc_u32 s47, s43, 0\n\t" \
    "s_add_u32 m0, s82, 94208\n\t" \
    "s_nop 0\n\t" \
    "global_load_lds_dwordx4 v200, s[46:47]\n\t" \
    "s_add_u32 s46, s42, 3145728\n\t" \
    "s_addc_u32 s47, s43, 0\n\t" \
    "s_add_u32 m0, s82, 98304\n\t" \
    "s_nop 0\n\t" \
    "global_load_lds_dwordx4 v200, s[46:47]\n\t" \
    "s_add_u32 s46, s42, 3276800\n\t" \
    "s_addc_u32 s47, s43, 0\n\t" \
    "s_add_u32 m0, s82, 102400\n\t" \
    "s_nop 0\n\t" \
    "global_load_lds_dwordx4 v200, s[46:47]\n\t" \
    "s_add_u32 s46, s42, 3407872\n\t" \
    "s_addc_u32 s47, s43, 0\n\t" \
    "s_add_u32 m0, s82, 106496\n\t" \
    "s_nop 0\n\t" \
    "global_load_lds_dwordx4 v200, s[46:47]\n\t" \
    "s_add_u32 s46, s42, 3538944\n\t" \
    "s_addc_u32 s47, s43, 0\n\t" \
    "s_add_u32 m0, s82, 110592\n\t" \
    "s_nop 0\n\t" \
    "global_load_lds_dwordx4 v200, s[46:47]\n\t" \
    "s_add_u32 s46, s42, 3670016\n\t" \
    "s_addc_u32 s47, s43, 0\n\t" \
    "s_add_u32 m0, s82, 114688\n\t" \
    "s_nop 0\n\t" \
    "global_load_lds_dwordx4 v200, s[46:47]\n\t" \
    "s_add_u32 s46, s42, 3801088\n\t" \
    "s_addc_u32 s47, s43, 0\n\t" \
    "s_add_u32 m0, s82, 118784\n\t" \
    "s_nop 0\n\t" \
    "global_load_lds_dwordx4 v200, s[46:47]\n\t" \
    "s_add_u32 s46, s42, 3932160\n\t" \
    "s_addc_u32 s47, s43, 0\n\t" \
    "s_add_u32 m0, s82, 122880\n\t" \
    "s_nop 0\n\t" \
    "global_load_lds_dwordx4 v200, s[46:47]\n\t" \
    "s_add_u32 s46, s42, 4063232\n\t" \
    "s_addc_u32 s47, s43, 0\n\t" \
    "s_add_u32 m0, s82, 126976\n\t" \
    "s_nop 0\n\t" \
    "global_load_lds_dwordx4 v200, s[46:47]\n\t" \
    "s_add_u32 s46, s42, 4194304\n\t" \
    "s_addc_u32 s47, s43, 0\n\t" \
    "s_add_u32 m0, s82, 131072\n\t" \
    "s_nop 0\n\t" \
    "global_load_lds_dwordx4 v200, s[46:47]\n\t" \
    "s_add_u32 s46, s42, 4325376\n\t" \
    "s_addc_u32 s47, s43, 0\n\t" \
    "s_add_u32 m0, s82, 135168\n\t" \
    "s_nop 0\n\t" \
    "global_load_lds_dwordx4 v200, s[46:47]\n\t" \
    "s_add_u32 s46, s42, 4456448\n\t" \
    "s_addc_u32 s47, s43, 0\n\t" \
    "s_add_u32 m0, s82, 139264\n\t" \
    "s_nop 0\n\t" \
    "global_load_lds_dwordx4 v200, s[46:47]\n\t" \
    "s_add_u32 s46, s42, 4587520\n\t" \
    "s_addc_u32 s47, s43, 0\n\t" \
    "s_add_u32 m0, s82, 143360\n\t" \
    "s_nop 0\n\t" \
    "global_load_lds_dwordx4 v200, s[46:47]\n\t" \
    "s_waitcnt vmcnt(32) lgkmcnt(0)\n\t" \
    "s_barrier\n\t" \
    "ds_read_b64_tr_b16 a[0:1], v201 offset:0\n\t" \
    "ds_read_b64_tr_b16 a[2:3], v201 offset:1024\n\t" \
    "ds_read_b64_tr_b16 a[4:5], v201 offset:8192\n\t" \
    "ds_read_b64_tr_b16 a[6:7], v201 offset:9216\n\t" \
    "ds_read_b64_tr_b16 a[72:73], v202 offset:0\n\t" \
    "ds_read_b64_tr_b16 a[74:75], v202 offset:1024\n\t" \
    "ds_read_b64_tr_b16 a[76:77], v202 offset:8192\n\t" \
    "ds_read_b64_tr_b16 a[78:79], v202 offset:9216\n\t" \
    "s_waitcnt vmcnt(28) lgkmcnt(0)\n\t" \
    "s_barrier\n\t" \
    "ds_read_b64_tr_b16 a[8:9], v201 offset:16384\n\t" \
    "ds_read_b64_tr_b16 a[10:11], v201 offset:17408\n\t" \
    "ds_read_b64_tr_b16 a[12:13], v201 offset:24576\n\t" \
    "ds_read_b64_tr_b16 a[14:15], v201 offset:25600\n\t" \
    "ds_read_b64_tr_b16 a[80:81], v202 offset:16384\n\t" \
    "ds_read_b64_tr_b16 a[82:83], v202 offset:17408\n\t" \
    "ds_read_b64_tr_b16 a[84:85], v202 offset:24576\n\t" \
    "ds_read_b64_tr_b16 a[86:87], v202 offset:25600\n\t" \
    "s_waitcnt vmcnt(24) lgkmcnt(0)\n\t" \
    "s_barrier\n\t" \
    "ds_read_b64_tr_b16 a[16:17], v201 offset:32768\n\t" \
    "ds_read_b64_tr_b16 a[18:19], v201 offset:33792\n\t" \
    "ds_read_b64_tr_b16 a[20:21], v201 offset:40960\n\t" \
    "ds_read_b64_tr_b16 a[22:23], v201 offset:41984\n\t" \
    "ds_read_b64_tr_b16 a[88:89], v202 offset:32768\n\t" \
    "ds_read_b64_tr_b16 a[90:91], v202 offset:33792\n\t" \
    "ds_read_b64_tr_b16 a[92:93], v202 offset:40960\n\t" \
    "ds_read_b64_tr_b16 a[94:95], v202 offset:41984\n\t" \
    "s_waitcnt vmcnt(20) lgkmcnt(0)\n\t" \
    "s_barrier\n\t" \
    "ds_read_b64_tr_b16 a[24:25], v201 offset:49152\n\t" \
    "ds_read_b64_tr_b16 a[26:27], v201 offset:50176\n\t" \
    "ds_read_b64_tr_b16 a[28:29], v201 offset:57344\n\t" \
    "ds_read_b64_tr_b16 a[30:31], v201 offset:58368\n\t" \
    "ds_read_b64_tr_b16 a[96:97], v202 offset:49152\n\t" \
    "ds_read_b64_tr_b16 a[98:99], v202 offset:50176\n\t" \
    "ds_read_b64_tr_b16 a[100:101], v202 offset:57344\n\t" \
    "ds_read_b64_tr_b16 a[102:103], v202 offset:58368\n\t" \
    "s_add_u32 s46, s44, 0\n\t" \
    "s_addc_u32 s47, s45, 0\n\t" \
    "s_mul_i32 s69, s82, 9\n\t" \
    "s_add_u32 m0, s69, 0\n\t" \
    "s_nop 0\n\t" \
    "global_load_lds_dwordx4 v203, s[46:47]\n\t" \
    "s_add_u32 m0, s69, 1024\n\t" \
    "s_nop 0\n\t" \
    "global_load_lds_dwordx4 v204, s[46:47]\n\t" \
    "s_add_u32 m0, s69, 2048\n\t" \
    "s_nop 0\n\t" \
    "global_load_lds_dwordx4 v205, s[46:47]\n\t" \
    "s_add_u32 m0, s69, 3072\n\t" \
    "s_nop 0\n\t" \
    "global_load_lds_dwordx4 v206, s[46:47]\n\t" \
    "s_add_u32 m0, s69, 4096\n\t" \
    "s_nop 0\n\t" \
    "global_load_lds_dwordx4 v207, s[46:47]\n\t" \
    "s_add_u32 m0, s69, 5120\n\t" \
    "s_nop 0\n\t" \
    "global_load_lds_dwordx4 v208, s[46:47]\n\t" \
    "s_add_u32 m0, s69, 6144\n\t" \
    "s_nop 0\n\t" \
    "global_load_lds_dwordx4 v209, s[46:47]\n\t" \
    "s_add_u32 m0, s69, 7168\n\t" \
    "s_nop 0\n\t" \
    "global_load_lds_dwordx4 v210, s[46:47]\n\t" \
    "s_add_u32 m0, s69, 8192\n\t" \
    "s_nop 0\n\t" \
    "global_load_lds_dwordx4 v211, s[46:47]\n\t" \
    "s_min_u32 s69, s65, 63\n\t" \
    "s_mul_i32 s69, s69, 36864\n\t" \
    "s_add_u32 s52, s50, s69\n\t" \
    "s_addc_u32 s53, s51, 0\n\t" \
    "global_load_dwordx4 v[120:123], v203, s[52:53]\n\t" \
    "global_load_dwordx4 v[124:127], v204, s[52:53]\n\t" \
    "global_load_dwordx4 v[128:131], v205, s[52:53]\n\t" \
    "global_load_dwordx4 v[132:135], v206, s[52:53]\n\t" \
    "global_load_dwordx4 v[136:139], v207, s[52:53]\n\t" \
    "global_load_dwordx4 v[140:143], v208, s[52:53]\n\t" \
    "global_load_dwordx4 v[144:147], v209, s[52:53]\n\t" \
    "global_load_dwordx4 v[148:151], v210, s[52:53]\n\t" \
    "global_load_dwordx4 v[152:155], v211, s[52:53]\n\t" \
    "s_waitcnt vmcnt(34) lgkmcnt(0)\n\t" \
    "s_barrier\n\t" \
    "v_add_u32 v201, 0x10000, v201\n\t" \
    "v_add_u32 v202, 0x10000, v202\n\t" \
    "ds_read_b64_tr_b16 a[32:33], v201 offset:0\n\t" \
    "ds_read_b64_tr_b16 a[34:35], v201 offset:1024\n\t" \
    "ds_read_b64_tr_b16 a[36:37], v201 offset:8192\n\t" \
    "ds_read_b64_tr_b16 a[38:39], v201 offset:9216\n\t" \
    "ds_read_b64_tr_b16 a[104:105], v202 offset:0\n\t" \
    "ds_read_b64_tr_b16 a[106:107], v202 offset:1024\n\t" \
    "ds_read_b64_tr_b16 a[108:109], v202 offset:8192\n\t" \
    "ds_read_b64_tr_b16 a[110:111], v202 offset:9216\n\t" \
    "s_waitcnt vmcnt(30) lgkmcnt(0)\n\t" \
    "s_barrier\n\t" \
    "ds_read_b64_tr_b16 a[40:41], v201 offset:16384\n\t" \
    "ds_read_b64_tr_b16 a[42:43], v201 offset:17408\n\t" \
    "ds_read_b64_tr_b16 a[44:45], v201 offset:24576\n\t" \
    "ds_read_b64_tr_b16 a[46:47], v201 offset:25600\n\t" \
    "ds_read_b64_tr_b16 a[112:113], v202 offset:16384\n\t" \
    "ds_read_b64_tr_b16 a[114:115], v202 offset:17408\n\t" \
    "ds_read_b64_tr_b16 a[116:117], v202 offset:24576\n\t" \
    "ds_read_b64_tr_b16 a[118:119], v202 offset:25600\n\t" \
    "s_add_u32 s46, s44, 36864\n\t" \
    "s_addc_u32 s47, s45, 0\n\t" \
    "s_mul_i32 s69, s82, 9\n\t" \
    "s_add_u32 m0, s69, 36864\n\t" \
    "s_nop 0\n\t" \
    "global_load_lds_dwordx4 v203, s[46:47]\n\t" \
    "s_add_u32 m0, s69, 37888\n\t" \
    "s_nop 0\n\t" \
    "global_load_lds_dwordx4 v204, s[46:47]\n\t" \
    "s_add_u32 m0, s69, 38912\n\t" \
    "s_nop 0\n\t" \
    "global_load_lds_dwordx4 v205, s[46:47]\n\t" \
    "s_add_u32 m0, s69, 39936\n\t" \
    "s_nop 0\n\t" \
    "global_load_lds_dwordx4 v206, s[46:47]\n\t" \
    "s_add_u32 m0, s69, 40960\n\t" \
    "s_nop 0\n\t" \
    "global_load_lds_dwordx4 v207, s[46:47]\n\t" \
    "s_add_u32 m0, s69, 41984\n\t" \
    "s_nop 0\n\t" \
    "global_load_lds_dwordx4 v208, s[46:47]\n\t" \
    "s_add_u32 m0, s69, 43008\n\t" \
    "s_nop 0\n\t" \
    "global_load_lds_dwordx4 v209, s[46:47]\n\t" \
    "s_add_u32 m0, s69, 44032\n\t" \
    "s_nop 0\n\t" \
    "global_load_lds_dwordx4 v210, s[46:47]\n\t" \
    "s_add_u32 m0, s69, 45056\n\t" \
    "s_nop 0\n\t" \
    "global_load_lds_dwordx4 v211, s[46:47]\n\t" \
    "s_waitcnt vmcnt(35) lgkmcnt(0)\n\t" \
    "s_barrier\n\t" \
    "ds_read_b64_tr_b16 a[48:49], v201 offset:32768\n\t" \
    "ds_read_b64_tr_b16 a[50:51], v201 offset:33792\n\t" \
    "ds_read_b64_tr_b16 a[52:53], v201 offset:40960\n\t" \
    "ds_read_b64_tr_b16 a[54:55], v201 offset:41984\n\t" \
    "ds_read_b64_tr_b16 a[120:121], v202 offset:32768\n\t" \
    "ds_read_b64_tr_b16 a[122:123], v202 offset:33792\n\t" \
    "ds_read_b64_tr_b16 a[124:125], v202 offset:40960\n\t" \
    "ds_read_b64_tr_b16 a[126:127], v202 offset:41984\n\t" \
    "s_waitcnt vmcnt(31) lgkmcnt(0)\n\t" \
    "s_barrier\n\t" \
    "ds_read_b64_tr_b16 a[56:57], v201 offset:49152\n\t" \
    "ds_read_b64_tr_b16 a[58:59], v201 offset:50176\n\t" \
    "ds_read_b64_tr_b16 a[60:61], v201 offset:57344\n\t" \
    "ds_read_b64_tr_b16 a[62:63], v201 offset:58368\n\t" \
    "ds_read_b64_tr_b16 a[128:129], v202 offset:49152\n\t" \
    "ds_read_b64_tr_b16 a[130:131], v202 offset:50176\n\t" \
    "ds_read_b64_tr_b16 a[132:133], v202 offset:57344\n\t" \
    "ds_read_b64_tr_b16 a[134:135], v202 offset:58368\n\t" \
    "s_add_u32 s46, s44, 73728\n\t" \
    "s_addc_u32 s47, s45, 0\n\t" \
    "s_mul_i32 s69, s82, 9\n\t" \
    "s_add_u32 m0, s69, 73728\n\t" \
    "s_nop 0\n\t" \
    "global_load_lds_dwordx4 v203, s[46:47]\n\t" \
    "s_add_u32 m0, s69, 74752\n\t" \
    "s_nop 0\n\t" \
    "global_load_lds_dwordx4 v204, s[46:47]\n\t" \
    "s_add_u32 m0, s69, 75776\n\t" \
    "s_nop 0\n\t" \
    "global_load_lds_dwordx4 v205, s[46:47]\n\t" \
    "s_add_u32 m0, s69, 76800\n\t" \
    "s_nop 0\n\t" \
    "global_load_lds_dwordx4 v206, s[46:47]\n\t" \
    "s_add_u32 m0, s69, 77824\n\t" \
    "s_nop 0\n\t" \
    "global_load_lds_dwordx4 v207, s[46:47]\n\t" \
    "s_add_u32 m0, s69, 78848\n\t" \
    "s_nop 0\n\t" \
    "global_load_lds_dwordx4 v208, s[46:47]\n\t" \
    "s_add_u32 m0, s69, 79872\n\t" \
    "s_nop 0\n\t" \
    "global_load_lds_dwordx4 v209, s[46:47]\n\t" \
    "s_add_u32 m0, s69, 80896\n\t" \
    "s_nop 0\n\t" \
    "global_load_lds_dwordx4 v210, s[46:47]\n\t" \
    "s_add_u32 m0, s69, 81920\n\t" \
    "s_nop 0\n\t" \
    "global_load_lds_dwordx4 v211, s[46:47]\n\t" \
    "v_mbcnt_lo_u32_b32 v164, -1, 0\n\t" \
    "v_mbcnt_hi_u32_b32 v164, -1, v164\n\t" \
    "v_lshlrev_b32 v164, 6, v164\n\t" \
    "v_add_u32 v164, 6225920, v164\n\t" \
    "v_mov_b32 v165, 6234112\n\t" \
    "global_load_dword v165, v165, s[36:37] sc1\n\t" \
    "global_load_dwordx2 v[166:167], v164, s[36:37] sc1\n\t" \
    "global_load_dword v168, v164, s[36:37] offset:2048 sc1\n\t" \
    "s_waitcnt vmcnt(39) lgkmcnt(0)\n\t" \
    "s_barrier\n\t" \
    "v_add_u32 v201, 0x10000, v201\n\t" \
    "v_add_u32 v202, 0x10000, v202\n\t" \
    "ds_read_b64_tr_b16 a[64:65], v201 offset:0\n\t" \
    "ds_read_b64_tr_b16 a[66:67], v201 offset:1024\n\t" \
    "ds_read_b64_tr_b16 a[68:69], v201 offset:8192\n\t" \
    "ds_read_b64_tr_b16 a[70:71], v201 offset:9216\n\t" \
    "ds_read_b64_tr_b16 a[136:137], v202 offset:0\n\t" \
    "ds_read_b64_tr_b16 a[138:139], v202 offset:1024\n\t" \
    "ds_read_b64_tr_b16 a[140:141], v202 offset:8192\n\t" \
    "ds_read_b64_tr_b16 a[142:143], v202 offset:9216\n\t" \
    "v_add_u32 v201, 0xfffe0000, v201\n\t" \
    "v_add_u32 v202, 0xfffe0000, v202\n\t" \
    "gv_jn2_%=:\n\t" \
    "s_waitcnt lgkmcnt(0)\n\t" \
    "s_barrier\n\t" \
    "s_add_u32 s46, s44, 110592\n\t" \
    "s_addc_u32 s47, s45, 0\n\t" \
    "s_mul_i32 s69, s82, 9\n\t" \
    "s_add_u32 m0, s69, 110592\n\t" \
    "s_nop 0\n\t" \
    "global_load_lds_dwordx4 v203, s[46:47]\n\t" \
    "s_add_u32 m0, s69, 111616\n\t" \
    "s_nop 0\n\t" \
    "global_load_lds_dwordx4 v204, s[46:47]\n\t" \
    "s_add_u32 m0, s69, 112640\n\t" \
    "s_nop 0\n\t" \
    "global_load_lds_dwordx4 v205, s[46:47]\n\t" \
    "s_add_u32 m0, s69, 113664\n\t" \
    "s_nop 0\n\t" \
    "global_load_lds_dwordx4 v206, s[46:47]\n\t" \
    "s_add_u32 m0, s69, 114688\n\t" \
    "s_nop 0\n\t" \
    "global_load_lds_dwordx4 v207, s[46:47]\n\t" \
    "s_add_u32 m0, s69, 115712\n\t" \
    "s_nop 0\n\t" \
    "global_load_lds_dwordx4 v208, s[46:47]\n\t" \
    "s_add_u32 m0, s69, 116736\n\t" \
    "s_nop 0\n\t" \
    "global_load_lds_dwordx4 v209, s[46:47]\n\t" \
    "s_add_u32 m0, s69, 117760\n\t" \
    "s_nop 0\n\t" \
    "global_load_lds_dwordx4 v210, s[46:47]\n\t" \
    "s_add_u32 m0, s69, 118784\n\t" \
    "s_nop 0\n\t" \
    "global_load_lds_dwordx4 v211, s[46:47]\n\t" \
    "s_waitcnt vmcnt(39) lgkmcnt(0)\n\t" \
    "s_barrier\n\t" \
    "ds_read_b64_tr_b16 a[144:145], v212 offset:0\n\t" \
    "ds_read_b64_tr_b16 a[146:147], v212 offset:18432\n\t" \
    "ds_read_b64_tr_b16 a[160:161], v213 offset:0\n\t" \
    "ds_read_b64_tr_b16 a[162:163], v213 offset:18432\n\t" \
    "ds_read_b64_tr_b16 a[176:177], v214 offset:0\n\t" \
    "ds_read_b64_tr_b16 a[178:179], v214 offset:18432\n\t" \
    "s_waitcnt lgkmcnt(6)\n\t" \
    "ds_read_b64_tr_b16 a[192:193], v215 offset:0\n\t" \
    "ds_read_b64_tr_b16 a[194:195], v215 offset:18432\n\t" \
    "ds_read_b64_tr_b16 a[208:209], v212 offset:128\n\t" \
    "ds_read_b64_tr_b16 a[210:211], v212 offset:18560\n\t" \
    "ds_read_b64_tr_b16 a[224:225], v213 offset:128\n\t" \
    "ds_read_b64_tr_b16 a[226:227], v213 offset:18560\n\t" \
    "s_waitcnt lgkmcnt(6)\n\t" \
    "ds_read_b64_tr_b16 a[240:241], v214 offset:128\n\t" \
    "ds_read_b64_tr_b16 a[242:243], v214 offset:18560\n\t" \
    "ds_read_b64_tr_b16 v[224:225], v215 offset:128\n\t" \
    "ds_read_b64_tr_b16 v[226:227], v215 offset:18560\n\t" \
    "ds_read_b64_tr_b16 v[240:241], v212 offset:256\n\t" \
    "ds_read_b64_tr_b16 v[242:243], v212 offset:18688\n\t" \
    "s_waitcnt lgkmcnt(6)\n\t" \
    "v_add_u32 v212, 0x9000, v212\n\t" \
    "v_add_u32 v213, 0x9000, v213\n\t" \
    "v_add_u32 v214, 0x9000, v214\n\t" \
    "v_add_u32 v215, 0x9000, v215\n\t" \
    "s_waitcnt vmcnt(21) lgkmcnt(0)\n\t" \
    "s_barrier\n\t" \
    "ds_read_b64_tr_b16 a[148:149], v212 offset:0\n\t" \
    "ds_read_b64_tr_b16 a[150:151], v212 offset:18432\n\t" \
    "ds_read_b64_tr_b16 a[164:165], v213 offset:0\n\t" \
    "ds_read_b64_tr_b16 a[166:167], v213 offset:18432\n\t" \
    "ds_read_b64_tr_b16 a[180:181], v214 offset:0\n\t" \
    "ds_read_b64_tr_b16 a[182:183], v214 offset:18432\n\t" \
    "s_waitcnt lgkmcnt(6)\n\t" \
    "ds_read_b64_tr_b16 a[196:197], v215 offset:0\n\t" \
    "ds_read_b64_tr_b16 a[198:199], v215 offset:18432\n\t" \
    "ds_read_b64_tr_b16 a[212:213], v212 offset:128\n\t" \
    "ds_read_b64_tr_b16 a[214:215], v212 offset:18560\n\t" \
    "ds_read_b64_tr_b16 a[228:229], v213 offset:128\n\t" \
    "ds_read_b64_tr_b16 a[230:231], v213 offset:18560\n\t" \
    "s_waitcnt lgkmcnt(6)\n\t" \
    "ds_read_b64_tr_b16 a[244:245], v214 offset:128\n\t" \
    "ds_read_b64_tr_b16 a[246:247], v214 offset:18560\n\t" \
    "ds_read_b64_tr_b16 v[228:229], v215 offset:128\n\t" \
    "ds_read_b64_tr_b16 v[230:231], v215 offset:18560\n\t" \
    "ds_read_b64_tr_b16 v[244:245], v212 offset:256\n\t" \
    "ds_read_b64_tr_b16 v[246:247], v212 offset:18688\n\t" \
    "s_waitcnt lgkmcnt(6)\n\t" \
    "v_add_u32 v212, 0x9000, v212\n\t" \
    "v_add_u32 v213, 0x9000, v213\n\t" \
    "v_add_u32 v214, 0x9000, v214\n\t" \
    "v_add_u32 v215, 0x9000, v215\n\t" \
    "ds_write_b128 v3, v[120:123] offset:0\n\t" \
    "ds_write_b128 v3, v[124:127] offset:1024\n\t" \
    "ds_write_b128 v3, v[128:131] offset:2048\n\t" \
    "ds_write_b128 v3, v[132:135] offset:3072\n\t" \
    "ds_write_b128 v3, v[136:139] offset:4096\n\t" \
    "ds_write_b128 v3, v[140:143] offset:5120\n\t" \
    "ds_write_b128 v3, v[144:147] offset:6144\n\t" \
    "ds_write_b128 v3, v[148:151] offset:7168\n\t" \
    "ds_write_b128 v3, v[152:155] offset:8192\n\t" \
    "s_waitcnt vmcnt(12) lgkmcnt(0)\n\t" \
    "s_barrier\n\t" \
    "ds_read_b64_tr_b16 a[152:153], v212 offset:0\n\t" \
    "ds_read_b64_tr_b16 a[154:155], v212 offset:18432\n\t" \
    "ds_read_b64_tr_b16 a[168:169], v213 offset:0\n\t" \
    "ds_read_b64_tr_b16 a[170:171], v213 offset:18432\n\t" \
    "ds_read_b64_tr_b16 a[184:185], v214 offset:0\n\t" \
    "ds_read_b64_tr_b16 a[186:187], v214 offset:18432\n\t" \
    "s_waitcnt lgkmcnt(6)\n\t" \
    "ds_read_b64_tr_b16 a[200:201], v215 offset:0\n\t" \
    "ds_read_b64_tr_b16 a[202:203], v215 offset:18432\n\t" \
    "ds_read_b64_tr_b16 a[216:217], v212 offset:128\n\t" \
    "ds_read_b64_tr_b16 a[218:219], v212 offset:18560\n\t" \
    "ds_read_b64_tr_b16 a[232:233], v213 offset:128\n\t" \
    "ds_read_b64_tr_b16 a[234:235], v213 offset:18560\n\t" \
    "s_waitcnt lgkmcnt(6)\n\t" \
    "ds_read_b64_tr_b16 a[248:249], v214 offset:128\n\t" \
    "ds_read_b64_tr_b16 a[250:251], v214 offset:18560\n\t" \
    "ds_read_b64_tr_b16 v[232:233], v215 offset:128\n\t" \
    "ds_read_b64_tr_b16 v[234:235], v215 offset:18560\n\t" \
    "ds_read_b64_tr_b16 v[248:249], v212 offset:256\n\t" \
    "ds_read_b64_tr_b16 v[250:251], v212 offset:18688\n\t" \
    "s_waitcnt lgkmcnt(6)\n\t" \
    "v_add_u32 v212, 0x9000, v212\n\t" \
    "v_add_u32 v213, 0x9000, v213\n\t" \
    "v_add_u32 v214, 0x9000, v214\n\t" \
    "v_add_u32 v215, 0x9000, v215\n\t" \
    "s_min_u32 s69, s84, 63\n\t" \
    "s_mul_i32 s69, s69, 36864\n\t" \
    "s_add_u32 s52, s50, s69\n\t" \
    "s_addc_u32 s53, s51, 0\n\t" \
    "s_add_u32 m0, s89, 36864\n\t" \
    "s_nop 0\n\t" \
    "global_load_lds_dwordx4 v203, s[52:53]\n\t" \
    "s_add_u32 m0, s89, 37888\n\t" \
    "s_nop 0\n\t" \
    "global_load_lds_dwordx4 v204, s[52:53]\n\t" \
    "s_add_u32 m0, s89, 38912\n\t" \
    "s_nop 0\n\t" \
    "global_load_lds_dwordx4 v205, s[52:53]\n\t" \
    "s_add_u32 m0, s89, 39936\n\t" \
    "s_nop 0\n\t" \
    "global_load_lds_dwordx4 v206, s[52:53]\n\t" \
    "s_add_u32 m0, s89, 40960\n\t" \
    "s_nop 0\n\t" \
    "global_load_lds_dwordx4 v207, s[52:53]\n\t" \
    "s_add_u32 m0, s89, 41984\n\t" \
    "s_nop 0\n\t" \
    "global_load_lds_dwordx4 v208, s[52:53]\n\t" \
    "s_add_u32 m0, s89, 43008\n\t" \
    "s_nop 0\n\t" \
    "global_load_lds_dwordx4 v209, s[52:53]\n\t" \
    "s_add_u32 m0, s89, 44032\n\t" \
    "s_nop 0\n\t" \
    "global_load_lds_dwordx4 v210, s[52:53]\n\t" \
    "s_add_u32 m0, s89, 45056\n\t" \
    "s_nop 0\n\t" \
    "global_load_lds_dwordx4 v211, s[52:53]\n\t" \
    "s_waitcnt vmcnt(18)\n\t" \
    "gv_C3_%=:\n\t" \
    "v_cmp_ne_u32 s[54:55], v166, v168\n\t" \
    "v_sub_u32 v166, v166, v165\n\t" \
    "v_cmp_ne_u32 vcc, 1, v166\n\t" \
    "s_nop 1\n\t" \
    "s_or_b64 vcc, vcc, s[54:55]\n\t" \
    "s_cbranch_scc0 gv_Cok4_%=\n\t" \
    "s_cmp_eq_u32 s68, 0\n\t" \
    "s_cbranch_scc1 gv_F6_%=\n\t" \
    "s_sub_u32 s68, s68, 1\n\t" \
    "s_sleep 6\n\t" \
    "s_branch gv_Cag5_%=\n\t" \
    "gv_F6_%=:\n\t" \
    "v_mov_b32 v168, 5767424\n\t" \
    "v_mov_b32 v169, 4\n\t" \
    "global_store_dword v168, v169, s[36:37] sc1\n\t" \
    "s_waitcnt vmcnt(0)\n\t" \
    "s_branch gv_Cok4_%=\n\t" \
    "gv_Cag5_%=:\n\t" \
    "global_load_dwordx2 v[166:167], v164, s[36:37] sc1\n\t" \
    "global_load_dword v168, v164, s[36:37] offset:2048 sc1\n\t" \
    "s_waitcnt vmcnt(0)\n\t" \
    "s_branch gv_C3_%=\n\t" \
    "gv_Cok4_%=:\n\t" \
    "v_readlane_b32 s69, v167, 2\n\t" \
    "v_readlane_b32 s70, v167, 3\n\t" \
    "s_nop 1\n\t" \
    "s_cmp_lt_u32 s70, s69\n\t" \
    "s_cselect_b32 s69, 0, 8192\n\t" \
    "s_add_u32 s100, s36, s69\n\t" \
    "s_addc_u32 s101, s37, 0\n\t" \
    "s_cmp_lg_u32 s66, 0\n\t" \
    "s_cbranch_scc1 gv_Ca7_%=\n\t" \
    "v_mov_b32 v164, 5768192\n\t" \
    "s_mov_b64 exec, 1\n\t" \
    "global_atomic_add v164, v166, s[36:37] sc1\n\t" \
    "s_mov_b64 exec, -1\n\t" \
    "gv_Ca7_%=:\n\t" \
    "v_mov_b32_dpp v164, v167 quad_perm:[1,0,3,2] row_mask:0xf bank_mask:0xf\n\t" \
    "v_cmp_lt_u32 s[44:45], v164, v167\n\t" \
    "s_nop 1\n\t" \
    "s_waitcnt lgkmcnt(0)\n\t" \
    "s_barrier\n\t" \
    "ds_read_b128 v[24:27], v1 offset:0\n\t" \
    "ds_read_b128 v[28:31], v1 offset:18432\n\t" \
    "ds_read_b128 v[32:35], v2 offset:0\n\t" \
    "ds_read_b128 v[36:39], v2 offset:18432\n\t" \
    "ds_read_b128 v[40:43], v1 offset:128\n\t" \
    "ds_read_b128 v[44:47], v1 offset:18560\n\t" \
    "s_mov_b64 vcc, 0\n\t" \
    ".p2align 6\n\t" \
    "s_nop 0\n\t" \
    "s_nop 0\n\t" \
    "gv_loop8_%=:\n\t" \
    "s_min_u32 s69, s85, 63\n\t" \
    "s_mul_i32 s69, s69, 36864\n\t" \
    "s_add_u32 s52, s50, s69\n\t" \
    "s_addc_u32 s53, s51, 0\n\t" \
    "s_add_u32 s90, s90, 36864\n\t" \
    "s_cmp_eq_u32 s90, 110592\n\t" \
    "s_cselect_b32 s90, 0, s90\n\t" \
    "s_add_u32 s91, s90, 36864\n\t" \
    "s_cmp_eq_u32 s91, 110592\n\t" \
    "s_cselect_b32 s91, 0, s91\n\t" \
    "s_add_u32 s91, s91, s89\n\t" \
    "v_add_u32 v22, s90, v0\n\t" \
    "v_xor_b32 v23, 64, v22\n\t" \
    "s_lshl_b32 s40, s65, 15\n\t" \
    "s_cselect_b32 s70, s65, 64\n\t" \
    "s_mul_i32 s69, s70, 171\n\t" \
    "s_lshr_b32 s69, s69, 9\n\t" \
    "s_mul_i32 s70, s70, 589824\n\t" \
    "s_mul_i32 s71, s69, 327680\n\t" \
    "s_add_u32 s70, s70, s71\n\t" \
    "s_lshl1_add_u32 s69, s69, 4\n\t" \
    "s_bitcmp1_b64 s[44:45], s69\n\t" \
    "s_cselect_b32 s69, 8192, 0\n\t" \
    "s_add_u32 s69, s69, s70\n\t" \
    "s_add_u32 s69, s69, 123731968\n\t" \
    "s_add_u32 s48, s36, s69\n\t" \
    "s_addc_u32 s49, s37, 0\n\t" \
    "s_waitcnt lgkmcnt(5)\n\t" \
    "v_mfma_f32_16x16x32_bf16 v[72:75], a[0:3], v[24:27], 0\n\t" \
    "ds_read_b128 v[48:51], v2 offset:128\n\t" \
    "s_waitcnt lgkmcnt(5)\n\t" \
    "v_mfma_f32_16x16x32_bf16 v[76:79], a[0:3], v[28:31], 0\n\t" \
    "ds_read_b128 v[52:55], v2 offset:18560\n\t" \
    "v_mfma_f32_16x16x32_bf16 v[80:83], a[72:75], v[24:27], 0\n\t" \
    "s_add_u32 m0, s91, 0\n\t" \
    "v_mfma_f32_16x16x32_bf16 v[84:87], a[72:75], v[28:31], 0\n\t" \
    "global_load_lds_dwordx4 v203, s[52:53]\n\t" \
    "s_waitcnt lgkmcnt(5)\n\t" \
    "v_mfma_f32_16x16x32_bf16 v[72:75], a[4:7], v[32:35], v[72:75]\n\t" \
    "ds_read_b128 v[56:59], v1 offset:256\n\t" \
    "s_waitcnt lgkmcnt(5)\n\t" \
    "v_mfma_f32_16x16x32_bf16 v[76:79], a[4:7], v[36:39], v[76:79]\n\t" \
    "ds_read_b128 v[60:63], v1 offset:18688\n\t" \
    "v_mfma_f32_16x16x32_bf16 v[80:83], a[76:79], v[32:35], v[80:83]\n\t" \
    "s_add_u32 m0, s91, 1024\n\t" \
    "v_mfma_f32_16x16x32_bf16 v[84:87], a[76:79], v[36:39], v[84:87]\n\t" \
    "global_load_lds_dwordx4 v204, s[52:53]\n\t" \
    "s_waitcnt lgkmcnt(5)\n\t" \
    "v_mfma_f32_16x16x32_bf16 v[72:75], a[8:11], v[40:43], v[72:75]\n\t" \
    "ds_read_b128 v[64:67], v2 offset:256\n\t" \
    "s_waitcnt lgkmcnt(5)\n\t" \
    "v_mfma_f32_16x16x32_bf16 v[76:79], a[8:11], v[44:47], v[76:79]\n\t" \
    "ds_read_b128 v[68:71], v2 offset:18688\n\t" \
    "v_mfma_f32_16x16x32_bf16 v[80:83], a[80:83], v[40:43], v[80:83]\n\t" \
    "s_add_u32 m0, s91, 2048\n\t" \
    "v_mfma_f32_16x16x32_bf16 v[84:87], a[80:83], v[44:47], v[84:87]\n\t" \
    "global_load_lds_dwordx4 v205, s[52:53]\n\t" \
    "s_waitcnt lgkmcnt(5)\n\t" \
    "v_mfma_f32_16x16x32_bf16 v[72:75], a[12:15], v[48:51], v[72:75]\n\t" \
    "ds_read_b128 v[24:27], v1 offset:384\n\t" \
    "s_waitcnt lgkmcnt(5)\n\t" \
    "v_mfma_f32_16x16x32_bf16 v[76:79], a[12:15], v[52:55], v[76:79]\n\t" \
    "ds_read_b128 v[28:31], v1 offset:18816\n\t" \
    "v_mfma_f32_16x16x32_bf16 v[80:83], a[84:87], v[48:51], v[80:83]\n\t" \
    "s_add_u32 m0, s91, 3072\n\t" \
    "v_mfma_f32_16x16x32_bf16 v[84:87], a[84:87], v[52:55], v[84:87]\n\t" \
    "global_load_lds_dwordx4 v206, s[52:53]\n\t" \
    "s_waitcnt lgkmcnt(5)\n\t" \
    "v_mfma_f32_16x16x32_bf16 v[72:75], a[16:19], v[56:59], v[72:75]\n\t" \
    "ds_read_b128 v[32:35], v2 offset:384\n\t" \
    "s_waitcnt lgkmcnt(5)\n\t" \
    "v_mfma_f32_16x16x32_bf16 v[76:79], a[16:19], v[60:63], v[76:79]\n\t" \
    "ds_read_b128 v[36:39], v2 offset:18816\n\t" \
    "v_mfma_f32_16x16x32_bf16 v[80:83], a[88:91], v[56:59], v[80:83]\n\t" \
    "s_add_u32 m0, s91, 4096\n\t" \
    "v_mfma_f32_16x16x32_bf16 v[84:87], a[88:91], v[60:63], v[84:87]\n\t" \
    "global_load_lds_dwordx4 v207, s[52:53]\n\t" \
    "s_waitcnt lgkmcnt(5)\n\t" \
    "v_mfma_f32_16x16x32_bf16 v[72:75], a[20:23], v[64:67], v[72:75]\n\t" \
    "ds_read_b128 v[40:43], v1 offset:512\n\t" \
    "s_waitcnt lgkmcnt(5)\n\t" \
    "v_mfma_f32_16x16x32_bf16 v[76:79], a[20:23], v[68:71], v[76:79]\n\t" \
    "ds_read_b128 v[44:47], v1 offset:18944\n\t" \
    "v_mfma_f32_16x16x32_bf16 v[80:83], a[92:95], v[64:67], v[80:83]\n\t" \
    "s_add_u32 m0, s91, 5120\n\t" \
    "v_mfma_f32_16x16x32_bf16 v[84:87], a[92:95], v[68:71], v[84:87]\n\t" \
    "global_load_lds_dwordx4 v208, s[52:53]\n\t" \
    "s_waitcnt lgkmcnt(5)\n\t" \
    "v_mfma_f32_16x16x32_bf16 v[72:75], a[24:27], v[24:27], v[72:75]\n\t" \
    "ds_read_b128 v[48:51], v2 offset:512\n\t" \
    "s_waitcnt lgkmcnt(5)\n\t" \
    "v_mfma_f32_16x16x32_bf16 v[76:79], a[24:27], v[28:31], v[76:79]\n\t" \
    "ds_read_b128 v[52:55], v2 offset:18944\n\t" \
    "v_mfma_f32_16x16x32_bf16 v[80:83], a[96:99], v[24:27], v[80:83]\n\t" \
    "s_add_u32 m0, s91, 6144\n\t" \
    "v_mfma_f32_16x16x32_bf16 v[84:87], a[96:99], v[28:31], v[84:87]\n\t" \
    "global_load_lds_dwordx4 v209, s[52:53]\n\t" \
    "s_waitcnt lgkmcnt(5)\n\t" \
    "v_mfma_f32_16x16x32_bf16 v[72:75], a[28:31], v[32:35], v[72:75]\n\t" \
    "ds_read_b128 v[56:59], v1 offset:640\n\t" \
    "s_waitcnt lgkmcnt(5)\n\t" \
    "v_mfma_f32_16x16x32_bf16 v[76:79], a[28:31], v[36:39], v[76:79]\n\t" \
    "ds_read_b128 v[60:63], v1 offset:19072\n\t" \
    "v_mfma_f32_16x16x32_bf16 v[80:83], a[100:103], v[32:35], v[80:83]\n\t" \
    "s_add_u32 m0, s91, 7168\n\t" \
    "v_mfma_f32_16x16x32_bf16 v[84:87], a[100:103], v[36:39], v[84:87]\n\t" \
    "global_load_lds_dwordx4 v210, s[52:53]\n\t" \
    "s_waitcnt lgkmcnt(5)\n\t" \
    "v_mfma_f32_16x16x32_bf16 v[72:75], a[32:35], v[40:43], v[72:75]\n\t" \
    "ds_read_b128 v[64:67], v2 offset:640\n\t" \
    "s_waitcnt lgkmcnt(5)\n\t" \
    "v_mfma_f32_16x16x32_bf16 v[76:79], a[32:35], v[44:47], v[76:79]\n\t" \
    "ds_read_b128 v[68:71], v2 offset:19072\n\t" \
    "v_mfma_f32_16x16x32_bf16 v[80:83], a[104:107], v[40:43], v[80:83]\n\t" \
    "s_add_u32 m0, s91, 8192\n\t" \
    "v_mfma_f32_16x16x32_bf16 v[84:87], a[104:107], v[44:47], v[84:87]\n\t" \
    "global_load_lds_dwordx4 v211, s[52:53]\n\t" \
    "s_waitcnt lgkmcnt(5)\n\t" \
    "v_mfma_f32_16x16x32_bf16 v[72:75], a[36:39], v[48:51], v[72:75]\n\t" \
    "ds_read_b128 v[24:27], v1 offset:768\n\t" \
    "s_waitcnt lgkmcnt(5)\n\t" \
    "v_mfma_f32_16x16x32_bf16 v[76:79], a[36:39], v[52:55], v[76:79]\n\t" \
    "ds_read_b128 v[28:31], v1 offset:19200\n\t" \
    "v_mfma_f32_16x16x32_bf16 v[80:83], a[108:111], v[48:51], v[80:83]\n\t" \
    "s_cbranch_vccnz gv_gol12_%=\n\t" \
    "gv_gob13_%=:\n\t" \
    "v_mfma_f32_16x16x32_bf16 v[84:87], a[108:111], v[52:55], v[84:87]\n\t" \
    "s_waitcnt lgkmcnt(5)\n\t" \
    "v_mfma_f32_16x16x32_bf16 v[72:75], a[40:43], v[56:59], v[72:75]\n\t" \
    "ds_read_b128 v[32:35], v2 offset:768\n\t" \
    "s_waitcnt lgkmcnt(5)\n\t" \
    "v_mfma_f32_16x16x32_bf16 v[76:79], a[40:43], v[60:63], v[76:79]\n\t" \
    "ds_read_b128 v[36:39], v2 offset:19200\n\t" \
    "v_mfma_f32_16x16x32_bf16 v[80:83], a[112:115], v[56:59], v[80:83]\n\t" \
    "v_mfma_f32_16x16x32_bf16 v[84:87], a[112:115], v[60:63], v[84:87]\n\t" \
    "global_store_dwordx4 v19, v[196:199], s[48:49] sc1\n\t" \
    "s_waitcnt lgkmcnt(5)\n\t" \
    "v_mfma_f32_16x16x32_bf16 v[72:75], a[44:47], v[64:67], v[72:75]\n\t" \
    "ds_read_b128 v[40:43], v1 offset:896\n\t" \
    "s_waitcnt lgkmcnt(5)\n\t" \
    "v_mfma_f32_16x16x32_bf16 v[76:79], a[44:47], v[68:71], v[76:79]\n\t" \
    "ds_read_b128 v[44:47], v1 offset:19328\n\t" \
    "v_mfma_f32_16x16x32_bf16 v[80:83], a[116:119], v[64:67], v[80:83]\n\t" \
    "v_mfma_f32_16x16x32_bf16 v[84:87], a[116:119], v[68:71], v[84:87]\n\t" \
    "s_waitcnt lgkmcnt(5)\n\t" \
    "v_mfma_f32_16x16x32_bf16 v[72:75], a[48:51], v[24:27], v[72:75]\n\t" \
    "ds_read_b128 v[48:51], v2 offset:896\n\t" \
    "s_waitcnt lgkmcnt(5)\n\t" \
    "v_mfma_f32_16x16x32_bf16 v[76:79], a[48:51], v[28:31], v[76:79]\n\t" \
    "ds_read_b128 v[52:55], v2 offset:19328\n\t" \
    "v_mfma_f32_16x16x32_bf16 v[80:83], a[120:123], v[24:27], v[80:83]\n\t" \
    "v_mfma_f32_16x16x32_bf16 v[84:87], a[120:123], v[28:31], v[84:87]\n\t" \
    "s_waitcnt lgkmcnt(5)\n\t" \
    "v_mfma_f32_16x16x32_bf16 v[72:75], a[52:55], v[32:35], v[72:75]\n\t" \
    "ds_read_b128 v[56:59], v1 offset:1024\n\t" \
    "s_waitcnt lgkmcnt(5)\n\t" \
    "v_mfma_f32_16x16x32_bf16 v[76:79], a[52:55], v[36:39], v[76:79]\n\t" \
    "ds_read_b128 v[60:63], v1 offset:19456\n\t" \
    "v_mfma_f32_16x16x32_bf16 v[80:83], a[124:127], v[32:35], v[80:83]\n\t" \
    "v_mfma_f32_16x16x32_bf16 v[84:87], a[124:127], v[36:39], v[84:87]\n\t" \
    "global_store_dwordx4 v20, v[196:199], s[48:49] sc1\n\t" \
    "s_waitcnt lgkmcnt(5)\n\t" \
    "v_mfma_f32_16x16x32_bf16 v[72:75], a[56:59], v[40:43], v[72:75]\n\t" \
    "ds_read_b128 v[64:67], v2 offset:1024\n\t" \
    "s_waitcnt lgkmcnt(5)\n\t" \
    "v_mfma_f32_16x16x32_bf16 v[76:79], a[56:59], v[44:47], v[76:79]\n\t" \
    "ds_read_b128 v[68:71], v2 offset:19456\n\t" \
    "v_mfma_f32_16x16x32_bf16 v[80:83], a[128:131], v[40:43], v[80:83]\n\t" \
    "v_mfma_f32_16x16x32_bf16 v[84:87], a[128:131], v[44:47], v[84:87]\n\t" \
    "s_waitcnt lgkmcnt(5)\n\t" \
    "v_mfma_f32_16x16x32_bf16 v[72:75], a[60:63], v[48:51], v[72:75]\n\t" \
    "s_waitcnt lgkmcnt(4)\n\t" \
    "v_mfma_f32_16x16x32_bf16 v[76:79], a[60:63], v[52:55], v[76:79]\n\t" \
    "v_mfma_f32_16x16x32_bf16 v[80:83], a[132:135], v[48:51], v[80:83]\n\t" \
    "v_mfma_f32_16x16x32_bf16 v[84:87], a[132:135], v[52:55], v[84:87]\n\t" \
    "s_waitcnt lgkmcnt(3)\n\t" \
    "v_mfma_f32_16x16x32_bf16 v[72:75], a[64:67], v[56:59], v[72:75]\n\t" \
    "s_waitcnt lgkmcnt(2)\n\t" \
    "v_mfma_f32_16x16x32_bf16 v[76:79], a[64:67], v[60:63], v[76:79]\n\t" \
    "v_mfma_f32_16x16x32_bf16 v[80:83], a[136:139], v[56:59], v[80:83]\n\t" \
    "v_mfma_f32_16x16x32_bf16 v[84:87], a[136:139], v[60:63], v[84:87]\n\t" \
    "global_store_dword v21, v196, s[48:49] sc1\n\t" \
    "s_waitcnt lgkmcnt(1)\n\t" \
    "v_mfma_f32_16x16x32_bf16 v[72:75], a[68:71], v[64:67], v[72:75]\n\t" \
    "s_waitcnt lgkmcnt(0)\n\t" \
    "v_mfma_f32_16x16x32_bf16 v[76:79], a[68:71], v[68:71], v[76:79]\n\t" \
    "v_mfma_f32_16x16x32_bf16 v[80:83], a[140:143], v[64:67], v[80:83]\n\t" \
    "v_mfma_f32_16x16x32_bf16 v[84:87], a[140:143], v[68:71], v[84:87]\n\t" \
    "v_mov_b32 v1, v22\n\t" \
    "v_mov_b32 v2, v23\n\t" \
    "s_nop 1\n\t" \
    "v_cvt_pk_bf16_f32 v104, v72, v73\n\t" \
    "v_cvt_pk_bf16_f32 v105, v74, v75\n\t" \
    "s_nop 0\n\t" \
    "v_cvt_pk_bf16_f32 v106, v80, v81\n\t" \
    "v_cvt_pk_bf16_f32 v107, v82, v83\n\t" \
    "ds_write_b128 v4, v[104:107] offset:0\n\t" \
    "v_cvt_pk_bf16_f32 v108, v76, v77\n\t" \
    "v_cvt_pk_bf16_f32 v109, v78, v79\n\t" \
    "v_cvt_pk_bf16_f32 v110, v84, v85\n\t" \
    "v_cvt_pk_bf16_f32 v111, v86, v87\n\t" \
    "ds_write_b128 v4, v[108:111] offset:1024\n\t" \
    "s_cmp_lt_i32 s86, 16\n\t" \
    "s_cbranch_scc1 gv_rest10_%=\n\t" \
    "gv_back11_%=:\n\t" \
    "s_waitcnt vmcnt(21) lgkmcnt(0)\n\t" \
    "s_barrier\n\t" \
    "ds_read_b128 v[164:167], v5 offset:0\n\t" \
    "ds_read_b128 v[168:171], v5 offset:1024\n\t" \
    "ds_read_b128 v[172:175], v5 offset:2048\n\t" \
    "ds_read_b128 v[176:179], v5 offset:3072\n\t" \
    "ds_read_b128 v[180:183], v5 offset:4096\n\t" \
    "ds_read_b128 v[184:187], v5 offset:5120\n\t" \
    "ds_read_b128 v[188:191], v5 offset:6144\n\t" \
    "ds_read_b128 v[192:195], v5 offset:7168\n\t" \
    "s_cmp_lt_i32 s86, 16\n\t" \
    "s_cselect_b32 s71, s86, s87\n\t" \
    "s_cmp_lg_u32 s66, 0\n\t" \
    "s_cbranch_scc1 gv_SF17_%=\n\t" \
    "s_lshl_b32 s69, s67, 7\n\t" \
    "s_add_u32 s69, s69, 3162112\n\t" \
    "s_add_u32 s70, s71, 0\n\t" \
    "v_mov_b32 v156, s69\n\t" \
    "v_mov_b32 v157, s70\n\t" \
    "s_mov_b64 exec, 1\n\t" \
    "global_store_dword v156, v157, s[100:101] sc1\n\t" \
    "s_mov_b64 exec, -1\n\t" \
    "gv_SF17_%=:\n\t" \
    "s_sub_u32 s69, s86, 2\n\t" \
    "s_lshr_b32 s69, s69, 3\n\t" \
    "s_xor_b32 s70, s66, 2\n\t" \
    "s_or_b32 s69, s69, s70\n\t" \
    "s_cbranch_scc1 gv_ef18_%=\n\t" \
    "s_lshr_b32 s69, s86, 1\n\t" \
    "s_lshl_b32 s69, s69, 6\n\t" \
    "s_add_u32 s69, s69, s67\n\t" \
    "s_lshl_b32 s69, s69, 6\n\t" \
    "s_add_u32 s69, s69, 5795840\n\t" \
    "v_mov_b32 v156, s69\n\t" \
    "v_mov_b32 v157, s86\n\t" \
    "global_store_dword v156, v157, s[36:37] sc1\n\t" \
    "gv_ef18_%=:\n\t" \
    "v_xor_b32 v4, 0x2000, v4\n\t" \
    "v_xor_b32 v5, 0x2000, v5\n\t" \
    "s_waitcnt lgkmcnt(7)\n\t" \
    "v_mfma_f32_16x16x32_bf16 v[72:75], v[164:167], a[144:147], 0\n\t" \
    "s_waitcnt lgkmcnt(6)\n\t" \
    "v_mfma_f32_16x16x32_bf16 v[76:79], v[168:171], a[144:147], 0\n\t" \
    "s_waitcnt lgkmcnt(5)\n\t" \
    "v_mfma_f32_16x16x32_bf16 v[72:75], v[172:175], a[148:151], v[72:75]\n\t" \
    "s_waitcnt lgkmcnt(4)\n\t" \
    "v_mfma_f32_16x16x32_bf16 v[76:79], v[176:179], a[148:151], v[76:79]\n\t" \
    "s_waitcnt lgkmcnt(3)\n\t" \
    "v_mfma_f32_16x16x32_bf16 v[72:75], v[180:183], a[152:155], v[72:75]\n\t" \
    "s_waitcnt lgkmcnt(2)\n\t" \
    "v_mfma_f32_16x16x32_bf16 v[76:79], v[184:187], a[152:155], v[76:79]\n\t" \
    "s_waitcnt lgkmcnt(1)\n\t" \
    "v_mfma_f32_16x16x32_bf16 v[72:75], v[188:191], a[156:159], v[72:75]\n\t" \
    "s_waitcnt lgkmcnt(0)\n\t" \
    "v_mfma_f32_16x16x32_bf16 v[76:79], v[192:195], a[156:159], v[76:79]\n\t" \
    "v_mfma_f32_16x16x32_bf16 v[80:83], v[164:167], a[160:163], 0\n\t" \
    "v_mfma_f32_16x16x32_bf16 v[84:87], v[168:171], a[160:163], 0\n\t" \
    "v_mfma_f32_16x16x32_bf16 v[80:83], v[172:175], a[164:167], v[80:83]\n\t" \
    "v_mfma_f32_16x16x32_bf16 v[84:87], v[176:179], a[164:167], v[84:87]\n\t" \
    "v_mfma_f32_16x16x32_bf16 v[80:83], v[180:183], a[168:171], v[80:83]\n\t" \
    "v_mfma_f32_16x16x32_bf16 v[84:87], v[184:187], a[168:171], v[84:87]\n\t" \
    "v_cvt_pk_bf16_f32 v104, v72, v73\n\t" \
    "v_mfma_f32_16x16x32_bf16 v[80:83], v[188:191], a[172:175], v[80:83]\n\t" \
    "v_cvt_pk_bf16_f32 v105, v74, v75\n\t" \
    "v_mfma_f32_16x16x32_bf16 v[84:87], v[192:195], a[172:175], v[84:87]\n\t" \
    "v_cvt_pk_bf16_f32 v106, v76, v77\n\t" \
    "v_mfma_f32_16x16x32_bf16 v[88:91], v[164:167], a[176:179], 0\n\t" \
    "v_cvt_pk_bf16_f32 v107, v78, v79\n\t" \
    "v_mfma_f32_16x16x32_bf16 v[92:95], v[168:171], a[176:179], 0\n\t" \
    "v_xor_b32 v18, s40, v8\n\t" \
    "v_mfma_f32_16x16x32_bf16 v[88:91], v[172:175], a[180:183], v[88:91]\n\t" \
    "global_store_dwordx4 v18, v[104:107], s[38:39] sc1\n\t" \
    "v_mfma_f32_16x16x32_bf16 v[92:95], v[176:179], a[180:183], v[92:95]\n\t" \
    "v_mfma_f32_16x16x32_bf16 v[88:91], v[180:183], a[184:187], v[88:91]\n\t" \
    "v_mfma_f32_16x16x32_bf16 v[92:95], v[184:187], a[184:187], v[92:95]\n\t" \
    "v_cvt_pk_bf16_f32 v108, v80, v81\n\t" \
    "v_mfma_f32_16x16x32_bf16 v[88:91], v[188:191], a[188:191], v[88:91]\n\t" \
    "v_cvt_pk_bf16_f32 v109, v82, v83\n\t" \
    "v_mfma_f32_16x16x32_bf16 v[92:95], v[192:195], a[188:191], v[92:95]\n\t" \
    "v_cvt_pk_bf16_f32 v110, v84, v85\n\t" \
    "v_mfma_f32_16x16x32_bf16 v[72:75], v[164:167], a[192:195], 0\n\t" \
    "v_cvt_pk_bf16_f32 v111, v86, v87\n\t" \
    "v_mfma_f32_16x16x32_bf16 v[76:79], v[168:171], a[192:195], 0\n\t" \
    "v_xor_b32 v18, s40, v9\n\t" \
    "v_mfma_f32_16x16x32_bf16 v[72:75], v[172:175], a[196:199], v[72:75]\n\t" \
    "global_store_dwordx4 v18, v[108:111], s[38:39] sc1\n\t" \
    "v_mfma_f32_16x16x32_bf16 v[76:79], v[176:179], a[196:199], v[76:79]\n\t" \
    "v_mfma_f32_16x16x32_bf16 v[72:75], v[180:183], a[200:203], v[72:75]\n\t" \
    "v_mfma_f32_16x16x32_bf16 v[76:79], v[184:187], a[200:203], v[76:79]\n\t" \
    "v_cvt_pk_bf16_f32 v104, v88, v89\n\t" \
    "v_mfma_f32_16x16x32_bf16 v[72:75], v[188:191], a[204:207], v[72:75]\n\t" \
    "v_cvt_pk_bf16_f32 v105, v90, v91\n\t" \
    "v_mfma_f32_16x16x32_bf16 v[76:79], v[192:195], a[204:207], v[76:79]\n\t" \
    "v_cvt_pk_bf16_f32 v106, v92, v93\n\t" \
    "v_mfma_f32_16x16x32_bf16 v[80:83], v[164:167], a[208:211], 0\n\t" \
    "v_cvt_pk_bf16_f32 v107, v94, v95\n\t" \
    "v_mfma_f32_16x16x32_bf16 v[84:87], v[168:171], a[208:211], 0\n\t" \
    "v_xor_b32 v18, s40, v10\n\t" \
    "v_mfma_f32_16x16x32_bf16 v[80:83], v[172:175], a[212:215], v[80:83]\n\t" \
    "global_store_dwordx4 v18, v[104:107], s[38:39] sc1\n\t" \
    "v_mfma_f32_16x16x32_bf16 v[84:87], v[176:179], a[212:215], v[84:87]\n\t" \
    "v_mfma_f32_16x16x32_bf16 v[80:83], v[180:183], a[216:219], v[80:83]\n\t" \
    "v_mfma_f32_16x16x32_bf16 v[84:87], v[184:187], a[216:219], v[84:87]\n\t" \
    "v_cvt_pk_bf16_f32 v108, v72, v73\n\t" \
    "v_mfma_f32_16x16x32_bf16 v[80:83], v[188:191], a[220:223], v[80:83]\n\t" \
    "v_cvt_pk_bf16_f32 v109, v74, v75\n\t" \
    "v_mfma_f32_16x16x32_bf16 v[84:87], v[192:195], a[220:223], v[84:87]\n\t" \
    "v_cvt_pk_bf16_f32 v110, v76, v77\n\t" \
    "v_mfma_f32_16x16x32_bf16 v[88:91], v[164:167], a[224:227], 0\n\t" \
    "v_cvt_pk_bf16_f32 v111, v78, v79\n\t" \
    "v_mfma_f32_16x16x32_bf16 v[92:95], v[168:171], a[224:227], 0\n\t" \
    "v_xor_b32 v18, s40, v11\n\t" \
    "v_mfma_f32_16x16x32_bf16 v[88:91], v[172:175], a[228:231], v[88:91]\n\t" \
    "global_store_dwordx4 v18, v[108:111], s[38:39] sc1\n\t" \
    "v_mfma_f32_16x16x32_bf16 v[92:95], v[176:179], a[228:231], v[92:95]\n\t" \
    "v_mfma_f32_16x16x32_bf16 v[88:91], v[180:183], a[232:235], v[88:91]\n\t" \
    "v_mfma_f32_16x16x32_bf16 v[92:95], v[184:187], a[232:235], v[92:95]\n\t" \
    "v_cvt_pk_bf16_f32 v104, v80, v81\n\t" \
    "v_mfma_f32_16x16x32_bf16 v[88:91], v[188:191], a[236:239], v[88:91]\n\t" \
    "v_cvt_pk_bf16_f32 v105, v82, v83\n\t" \
    "v_mfma_f32_16x16x32_bf16 v[92:95], v[192:195], a[236:239], v[92:95]\n\t" \
    "v_cvt_pk_bf16_f32 v106, v84, v85\n\t" \
    "v_mfma_f32_16x16x32_bf16 v[72:75], v[164:167], a[240:243], 0\n\t" \
    "v_cvt_pk_bf16_f32 v107, v86, v87\n\t" \
    "v_mfma_f32_16x16x32_bf16 v[76:79], v[168:171], a[240:243], 0\n\t" \
    "v_xor_b32 v18, s40, v12\n\t" \
    "v_mfma_f32_16x16x32_bf16 v[72:75], v[172:175], a[244:247], v[72:75]\n\t" \
    "global_store_dwordx4 v18, v[104:107], s[38:39] sc1\n\t" \
    "v_mfma_f32_16x16x32_bf16 v[76:79], v[176:179], a[244:247], v[76:79]\n\t" \
    "v_mfma_f32_16x16x32_bf16 v[72:75], v[180:183], a[248:251], v[72:75]\n\t" \
    "v_mfma_f32_16x16x32_bf16 v[76:79], v[184:187], a[248:251], v[76:79]\n\t" \
    "v_cvt_pk_bf16_f32 v108, v88, v89\n\t" \
    "v_mfma_f32_16x16x32_bf16 v[72:75], v[188:191], a[252:255], v[72:75]\n\t" \
    "v_cvt_pk_bf16_f32 v109, v90, v91\n\t" \
    "v_mfma_f32_16x16x32_bf16 v[76:79], v[192:195], a[252:255], v[76:79]\n\t" \
    "v_cvt_pk_bf16_f32 v110, v92, v93\n\t" \
    "v_mfma_f32_16x16x32_bf16 v[80:83], v[164:167], v[224:227], 0\n\t" \
    "v_cvt_pk_bf16_f32 v111, v94, v95\n\t" \
    "v_mfma_f32_16x16x32_bf16 v[84:87], v[168:171], v[224:227], 0\n\t" \
    "v_xor_b32 v18, s40, v13\n\t" \
    "v_mfma_f32_16x16x32_bf16 v[80:83], v[172:175], v[228:231], v[80:83]\n\t" \
    "global_store_dwordx4 v18, v[108:111], s[38:39] sc1\n\t" \
    "v_mfma_f32_16x16x32_bf16 v[84:87], v[176:179], v[228:231], v[84:87]\n\t" \
    "v_mfma_f32_16x16x32_bf16 v[80:83], v[180:183], v[232:235], v[80:83]\n\t" \
    "v_mfma_f32_16x16x32_bf16 v[84:87], v[184:187], v[232:235], v[84:87]\n\t" \
    "v_cvt_pk_bf16_f32 v104, v72, v73\n\t" \
    "v_mfma_f32_16x16x32_bf16 v[80:83], v[188:191], v[236:239], v[80:83]\n\t" \
    "v_cvt_pk_bf16_f32 v105, v74, v75\n\t" \
    "v_mfma_f32_16x16x32_bf16 v[84:87], v[192:195], v[236:239], v[84:87]\n\t" \
    "v_cvt_pk_bf16_f32 v106, v76, v77\n\t" \
    "v_mfma_f32_16x16x32_bf16 v[88:91], v[164:167], v[240:243], 0\n\t" \
    "v_cvt_pk_bf16_f32 v107, v78, v79\n\t" \
    "ds_read_b128 v[24:27], v22 offset:0\n\t" \
    "v_mfma_f32_16x16x32_bf16 v[92:95], v[168:171], v[240:243], 0\n\t" \
    "v_xor_b32 v18, s40, v14\n\t" \
    "ds_read_b128 v[28:31], v22 offset:18432\n\t" \
    "v_mfma_f32_16x16x32_bf16 v[88:91], v[172:175], v[244:247], v[88:91]\n\t" \
    "global_store_dwordx4 v18, v[104:107], s[38:39] sc1\n\t" \
    "ds_read_b128 v[32:35], v23 offset:0\n\t" \
    "v_mfma_f32_16x16x32_bf16 v[92:95], v[176:179], v[244:247], v[92:95]\n\t" \
    "ds_read_b128 v[36:39], v23 offset:18432\n\t" \
    "v_mfma_f32_16x16x32_bf16 v[88:91], v[180:183], v[248:251], v[88:91]\n\t" \
    "ds_read_b128 v[40:43], v22 offset:128\n\t" \
    "v_mfma_f32_16x16x32_bf16 v[92:95], v[184:187], v[248:251], v[92:95]\n\t" \
    "v_cvt_pk_bf16_f32 v108, v80, v81\n\t" \
    "ds_read_b128 v[44:47], v22 offset:18560\n\t" \
    "v_mfma_f32_16x16x32_bf16 v[88:91], v[188:191], v[252:255], v[88:91]\n\t" \
    "v_cvt_pk_bf16_f32 v109, v82, v83\n\t" \
    "v_mfma_f32_16x16x32_bf16 v[92:95], v[192:195], v[252:255], v[92:95]\n\t" \
    "v_cvt_pk_bf16_f32 v110, v84, v85\n\t" \
    "v_cvt_pk_bf16_f32 v111, v86, v87\n\t" \
    "v_xor_b32 v18, s40, v15\n\t" \
    "global_store_dwordx4 v18, v[108:111], s[38:39] sc1\n\t" \
    "s_nop 1\n\t" \
    "v_cvt_pk_bf16_f32 v104, v88, v89\n\t" \
    "v_cvt_pk_bf16_f32 v105, v90, v91\n\t" \
    "v_cvt_pk_bf16_f32 v106, v92, v93\n\t" \
    "v_cvt_pk_bf16_f32 v107, v94, v95\n\t" \
    "v_xor_b32 v18, s40, v16\n\t" \
    "global_store_dwordx4 v18, v[104:107], s[38:39] sc1\n\t" \
    "s_cmp_ge_u32 s65, 2\n\t" \
    "s_cbranch_scc1 gv_nofast9_%=\n\t" \
    "s_waitcnt vmcnt(0)\n\t" \
    "s_barrier\n\t" \
    "s_cmp_lg_u32 s66, 0\n\t" \
    "s_cbranch_scc1 gv_SF19_%=\n\t" \
    "s_lshl_b32 s69, s67, 7\n\t" \
    "s_add_u32 s69, s69, 3162112\n\t" \
    "s_add_u32 s70, s65, 0\n\t" \
    "v_mov_b32 v156, s69\n\t" \
    "v_mov_b32 v157, s70\n\t" \
    "s_mov_b64 exec, 1\n\t" \
    "global_store_dword v156, v157, s[100:101] sc1\n\t" \
    "s_mov_b64 exec, -1\n\t" \
    "gv_SF19_%=:\n\t" \
    "s_cmp_lg_u32 s66, 1\n\t" \
    "s_cbranch_scc1 gv_nofast9_%=\n\t" \
    "v_mov_b32 v120, 5769216\n\t" \
    "v_mov_b32 v121, 1\n\t" \
    "s_mov_b64 exec, 1\n\t" \
    "global_atomic_add v122, v120, v121, s[36:37] sc0 sc1\n\t" \
    "s_mov_b64 exec, -1\n\t" \
    "s_mov_b64 vcc, -1\n\t" \
    "gv_nofast9_%=:\n\t" \
    "s_mov_b32 s87, s86\n\t" \
    "s_mov_b32 s86, s65\n\t" \
    "s_mov_b32 s65, s84\n\t" \
    "s_mov_b32 s84, s85\n\t" \
    "s_cmp_lt_u32 s84, 24\n\t" \
    "s_addc_u32 s85, s84, 1\n\t" \
    "s_cmp_lt_u32 s65, s88\n\t" \
    "s_cbranch_scc1 gv_loop8_%=\n\t" \
    "s_waitcnt vmcnt(0) lgkmcnt(0)\n\t" \
    "s_barrier\n\t" \
    "s_mov_b32 s65, 99\n\t" \
    "s_cmp_lg_u32 s66, 0\n\t" \
    "s_cbranch_scc1 gv_SF21_%=\n\t" \
    "s_lshl_b32 s69, s67, 7\n\t" \
    "s_add_u32 s69, s69, 3162112\n\t" \
    "s_add_u32 s70, s65, 0\n\t" \
    "v_mov_b32 v156, s69\n\t" \
    "v_mov_b32 v157, s70\n\t" \
    "s_mov_b64 exec, 1\n\t" \
    "global_store_dword v156, v157, s[100:101] sc1\n\t" \
    "s_mov_b64 exec, -1\n\t" \
    "gv_SF21_%=:\n\t" \
    "s_branch gv_end22_%=\n\t" \
    "gv_gol12_%=:\n\t" \
    "s_mov_b64 vcc, 0\n\t" \
    "v_mbcnt_lo_u32_b32 v120, -1, 0\n\t" \
    "v_and_b32 v121, 3, v120\n\t" \
    "v_lshlrev_b32 v121, 16, v121\n\t" \
    "v_bfe_u32 v123, v120, 2, 2\n\t" \
    "v_lshl_add_u32 v121, v123, 11, v121\n\t" \
    "v_bfe_u32 v123, v120, 4, 1\n\t" \
    "v_lshl_add_u32 v121, v123, 14, v121\n\t" \
    "s_bitcmp1_b32 s44, 2\n\t" \
    "s_cselect_b32 s63, 8192, 0\n\t" \
    "s_or_b32 s63, s63, 0x200000\n\t" \
    "v_add_u32 v121, s63, v121\n\t" \
    "s_waitcnt vmcnt(9)\n\t" \
    "v_readfirstlane_b32 s69, v122\n\t" \
    "s_nop 1\n\t" \
    "s_cmp_lg_u32 s69, 126\n\t" \
    "s_cbranch_scc1 gv_go23_%=\n\t" \
    "s_memrealtime s[56:57]\n\t" \
    "s_waitcnt lgkmcnt(0)\n\t" \
    "s_lshl_b32 s69, s56, 4\n\t" \
    "s_add_u32 s69, s69, 2960\n\t" \
    "s_add_u32 s70, s56, 24\n\t" \
    "s_lshr_b32 s70, s70, 4\n\t" \
    "v_lshrrev_b32 v123, 2, v120\n\t" \
    "v_add_u32 v123, s70, v123\n\t" \
    "v_and_b32 v123, 63, v123\n\t" \
    "v_lshlrev_b32 v123, 6, v123\n\t" \
    "v_and_b32 v122, 3, v120\n\t" \
    "v_lshl_add_u32 v123, v122, 12, v123\n\t" \
    "v_add_u32 v123, 5783552, v123\n\t" \
    "v_mov_b32 v120, s69\n\t" \
    "s_mov_b32 exec_hi, 0\n\t" \
    "global_store_dword v123, v120, s[36:37] sc1\n\t" \
    "global_store_dword v121, v120, s[36:37] sc1\n\t" \
    "s_mov_b32 exec_hi, -1\n\t" \
    "gv_go23_%=:\n\t" \
    "s_branch gv_gob13_%=\n\t" \
    "gv_rest10_%=:\n\t" \
    "s_waitcnt vmcnt(12) lgkmcnt(0)\n\t" \
    "s_cmp_ge_i32 s86, 0\n\t" \
    "s_cbranch_scc1 gv_back11_%=\n\t" \
    "s_barrier\n\t" \
    "ds_read_b64_tr_b16 a[156:157], v212 offset:0\n\t" \
    "ds_read_b64_tr_b16 a[158:159], v212 offset:18432\n\t" \
    "ds_read_b64_tr_b16 a[172:173], v213 offset:0\n\t" \
    "ds_read_b64_tr_b16 a[174:175], v213 offset:18432\n\t" \
    "ds_read_b64_tr_b16 a[188:189], v214 offset:0\n\t" \
    "ds_read_b64_tr_b16 a[190:191], v214 offset:18432\n\t" \
    "s_waitcnt lgkmcnt(6)\n\t" \
    "ds_read_b64_tr_b16 a[204:205], v215 offset:0\n\t" \
    "ds_read_b64_tr_b16 a[206:207], v215 offset:18432\n\t" \
    "ds_read_b64_tr_b16 a[220:221], v212 offset:128\n\t" \
    "ds_read_b64_tr_b16 a[222:223], v212 offset:18560\n\t" \
    "ds_read_b64_tr_b16 a[236:237], v213 offset:128\n\t" \
    "ds_read_b64_tr_b16 a[238:239], v213 offset:18560\n\t" \
    "s_waitcnt lgkmcnt(6)\n\t" \
    "ds_read_b64_tr_b16 a[252:253], v214 offset:128\n\t" \
    "ds_read_b64_tr_b16 a[254:255], v214 offset:18560\n\t" \
    "ds_read_b64_tr_b16 v[236:237], v215 offset:128\n\t" \
    "ds_read_b64_tr_b16 v[238:239], v215 offset:18560\n\t" \
    "ds_read_b64_tr_b16 v[252:253], v212 offset:256\n\t" \
    "ds_read_b64_tr_b16 v[254:255], v212 offset:18688\n\t" \
    "s_waitcnt lgkmcnt(6)\n\t" \
    "v_add_u32 v212, 0x9000, v212\n\t" \
    "v_add_u32 v213, 0x9000, v213\n\t" \
    "v_add_u32 v214, 0x9000, v214\n\t" \
    "v_add_u32 v215, 0x9000, v215\n\t" \
    "s_branch gv_back11_%=\n\t" \
    "gv_end22_%=:\n\t" \
    "gv_last_%=:\n\t" \
    ""
#define ASM_CV \
    "v_mov_b32 v0, %0\n\t" \
    "v_mov_b32 v2, %2\n\t" \
    "v_subrev_u32 v1, 768, %1\n\t" \
    "v_mov_b32 v4, 0\n\t" \
    "v_mov_b32 v5, 0\n\t" \
    "v_mov_b32 v6, 0\n\t" \
    "v_mov_b32 v7, 0\n\t" \
    "v_mov_b32 v8, 0\n\t" \
    "v_mov_b32 v9, 0\n\t" \
    "v_mov_b32 v10, 0\n\t" \
    "v_mov_b32 v11, 0\n\t" \
    "v_mov_b32 v12, 0\n\t" \
    "v_mov_b32 v13, 0\n\t" \
    "v_mov_b32 v14, 0\n\t" \
    "v_mov_b32 v15, 0\n\t" \
    "v_mov_b32 v16, 0\n\t" \
    "v_mov_b32 v17, 0\n\t" \
    "v_mov_b32 v18, 0\n\t" \
    "v_mov_b32 v19, 0\n\t" \
    "v_mov_b32 v20, 0\n\t" \
    "v_mov_b32 v21, 0\n\t" \
    "v_mov_b32 v22, 0\n\t" \
    "v_mov_b32 v23, 0\n\t" \
    "v_mov_b32 v24, 0\n\t" \
    "v_mov_b32 v25, 0\n\t" \
    "v_mov_b32 v26, 0\n\t" \
    "v_mov_b32 v27, 0\n\t" \
    "v_mov_b32 v28, 0\n\t" \
    "v_mov_b32 v29, 0\n\t" \
    "v_mov_b32 v30, 0\n\t" \
    "v_mov_b32 v31, 0\n\t" \
    "v_mov_b32 v32, 0\n\t" \
    "v_mov_b32 v33, 0\n\t" \
    "v_mov_b32 v34, 0\n\t" \
    "v_mov_b32 v35, 0\n\t" \
    "s_waitcnt lgkmcnt(0)\n\t" \
    "ds_read_b64 v[36:37], v0 offset:0\n\t" \
    "ds_read_b64 v[38:39], v0 offset:8\n\t" \
    "ds_read_b64 v[40:41], v0 offset:32\n\t" \
    "ds_read_b64 v[42:43], v0 offset:40\n\t" \
    "ds_read_b128 v[44:47], v1 offset:768\n\t" \
    "ds_read_b128 v[48:51], v1 offset:2304\n\t" \
    "ds_read_b128 v[52:55], v1 offset:3840\n\t" \
    "ds_read_b128 v[56:59], v1 offset:5376\n\t" \
    "s_mov_b32 s69, 8\n\t" \
    "cv_c1_%=:\n\t" \
    "s_waitcnt lgkmcnt(3)\n\t" \
    "v_mfma_f32_16x16x32_bf16 v[4:7], v[36:39], v[44:47], v[4:7]\n\t" \
    "ds_read_b64 v[76:77], v0 offset:64\n\t" \
    "ds_read_b64 v[78:79], v0 offset:72\n\t" \
    "ds_read_b64 v[80:81], v0 offset:96\n\t" \
    "v_mfma_f32_16x16x32_bf16 v[20:23], v[40:43], v[44:47], v[20:23]\n\t" \
    "ds_read_b64 v[82:83], v0 offset:104\n\t" \
    "ds_read_b128 v[84:87], v1 offset:672\n\t" \
    "ds_read_b128 v[88:91], v1 offset:2208\n\t" \
    "s_waitcnt lgkmcnt(8)\n\t" \
    "v_mfma_f32_16x16x32_bf16 v[8:11], v[36:39], v[48:51], v[8:11]\n\t" \
    "ds_read_b128 v[92:95], v1 offset:3744\n\t" \
    "ds_read_b128 v[96:99], v1 offset:5280\n\t" \
    "v_mfma_f32_16x16x32_bf16 v[24:27], v[40:43], v[48:51], v[24:27]\n\t" \
    "s_waitcnt lgkmcnt(9)\n\t" \
    "v_mfma_f32_16x16x32_bf16 v[12:15], v[36:39], v[52:55], v[12:15]\n\t" \
    "v_mfma_f32_16x16x32_bf16 v[28:31], v[40:43], v[52:55], v[28:31]\n\t" \
    "s_waitcnt lgkmcnt(8)\n\t" \
    "v_mfma_f32_16x16x32_bf16 v[16:19], v[36:39], v[56:59], v[16:19]\n\t" \
    "v_mfma_f32_16x16x32_bf16 v[32:35], v[40:43], v[56:59], v[32:35]\n\t" \
    "s_waitcnt lgkmcnt(3)\n\t" \
    "v_mfma_f32_16x16x32_bf16 v[4:7], v[76:79], v[84:87], v[4:7]\n\t" \
    "ds_read_b64 v[36:37], v0 offset:128\n\t" \
    "ds_read_b64 v[38:39], v0 offset:136\n\t" \
    "ds_read_b64 v[40:41], v0 offset:160\n\t" \
    "v_mfma_f32_16x16x32_bf16 v[20:23], v[80:83], v[84:87], v[20:23]\n\t" \
    "ds_read_b64 v[42:43], v0 offset:168\n\t" \
    "ds_read_b128 v[44:47], v1 offset:576\n\t" \
    "ds_read_b128 v[48:51], v1 offset:2112\n\t" \
    "s_waitcnt lgkmcnt(8)\n\t" \
    "v_mfma_f32_16x16x32_bf16 v[8:11], v[76:79], v[88:91], v[8:11]\n\t" \
    "ds_read_b128 v[52:55], v1 offset:3648\n\t" \
    "ds_read_b128 v[56:59], v1 offset:5184\n\t" \
    "v_mfma_f32_16x16x32_bf16 v[24:27], v[80:83], v[88:91], v[24:27]\n\t" \
    "s_waitcnt lgkmcnt(9)\n\t" \
    "v_mfma_f32_16x16x32_bf16 v[12:15], v[76:79], v[92:95], v[12:15]\n\t" \
    "v_mfma_f32_16x16x32_bf16 v[28:31], v[80:83], v[92:95], v[28:31]\n\t" \
    "s_waitcnt lgkmcnt(8)\n\t" \
    "v_mfma_f32_16x16x32_bf16 v[16:19], v[76:79], v[96:99], v[16:19]\n\t" \
    "v_mfma_f32_16x16x32_bf16 v[32:35], v[80:83], v[96:99], v[32:35]\n\t" \
    "v_add_u32 v0, 128, v0\n\t" \
    "v_subrev_u32 v1, 192, v1\n\t" \
    "s_sub_u32 s69, s69, 1\n\t" \
    "s_cmp_lg_u32 s69, 0\n\t" \
    "s_cbranch_scc1 cv_c1_%=\n\t" \
    "s_waitcnt lgkmcnt(0)\n\t" \
    "ds_read_b64 v[36:37], v0 offset:0\n\t" \
    "ds_read_b64 v[38:39], v0 offset:8\n\t" \
    "ds_read_b64 v[40:41], v0 offset:32\n\t" \
    "ds_read_b64 v[42:43], v0 offset:40\n\t" \
    "ds_read_b128 v[48:51], v1 offset:2304\n\t" \
    "ds_read_b128 v[52:55], v1 offset:3840\n\t" \
    "ds_read_b128 v[56:59], v1 offset:5376\n\t" \
    "ds_read_b64 v[76:77], v0 offset:64\n\t" \
    "ds_read_b64 v[78:79], v0 offset:72\n\t" \
    "ds_read_b64 v[80:81], v0 offset:96\n\t" \
    "ds_read_b64 v[82:83], v0 offset:104\n\t" \
    "ds_read_b128 v[88:91], v1 offset:2208\n\t" \
    "ds_read_b128 v[92:95], v1 offset:3744\n\t" \
    "ds_read_b128 v[96:99], v1 offset:5280\n\t" \
    "s_mov_b32 s69, 4\n\t" \
    "cv_c2_%=:\n\t" \
    "s_waitcnt lgkmcnt(9)\n\t" \
    "v_mfma_f32_16x16x32_bf16 v[8:11], v[36:39], v[48:51], v[8:11]\n\t" \
    "ds_read_b64 v[116:117], v0 offset:128\n\t" \
    "ds_read_b64 v[118:119], v0 offset:136\n\t" \
    "ds_read_b64 v[120:121], v0 offset:160\n\t" \
    "v_mfma_f32_16x16x32_bf16 v[24:27], v[40:43], v[48:51], v[24:27]\n\t" \
    "ds_read_b64 v[122:123], v0 offset:168\n\t" \
    "ds_read_b128 v[128:131], v1 offset:2112\n\t" \
    "ds_read_b128 v[132:135], v1 offset:3648\n\t" \
    "s_waitcnt lgkmcnt(14)\n\t" \
    "v_mfma_f32_16x16x32_bf16 v[12:15], v[36:39], v[52:55], v[12:15]\n\t" \
    "ds_read_b128 v[136:139], v1 offset:5184\n\t" \
    "v_mfma_f32_16x16x32_bf16 v[28:31], v[40:43], v[52:55], v[28:31]\n\t" \
    "s_waitcnt lgkmcnt(14)\n\t" \
    "v_mfma_f32_16x16x32_bf16 v[16:19], v[36:39], v[56:59], v[16:19]\n\t" \
    "ds_read_b64 v[156:157], v0 offset:192\n\t" \
    "v_mfma_f32_16x16x32_bf16 v[32:35], v[40:43], v[56:59], v[32:35]\n\t" \
    "s_waitcnt lgkmcnt(10)\n\t" \
    "v_mfma_f32_16x16x32_bf16 v[8:11], v[76:79], v[88:91], v[8:11]\n\t" \
    "ds_read_b64 v[158:159], v0 offset:200\n\t" \
    "ds_read_b64 v[160:161], v0 offset:224\n\t" \
    "ds_read_b64 v[162:163], v0 offset:232\n\t" \
    "v_mfma_f32_16x16x32_bf16 v[24:27], v[80:83], v[88:91], v[24:27]\n\t" \
    "ds_read_b128 v[168:171], v1 offset:2016\n\t" \
    "ds_read_b128 v[172:175], v1 offset:3552\n\t" \
    "s_waitcnt lgkmcnt(14)\n\t" \
    "v_mfma_f32_16x16x32_bf16 v[12:15], v[76:79], v[92:95], v[12:15]\n\t" \
    "ds_read_b128 v[176:179], v1 offset:5088\n\t" \
    "v_mfma_f32_16x16x32_bf16 v[28:31], v[80:83], v[92:95], v[28:31]\n\t" \
    "s_waitcnt lgkmcnt(14)\n\t" \
    "v_mfma_f32_16x16x32_bf16 v[16:19], v[76:79], v[96:99], v[16:19]\n\t" \
    "v_mfma_f32_16x16x32_bf16 v[32:35], v[80:83], v[96:99], v[32:35]\n\t" \
    "s_waitcnt lgkmcnt(9)\n\t" \
    "v_mfma_f32_16x16x32_bf16 v[8:11], v[116:119], v[128:131], v[8:11]\n\t" \
    "ds_read_b64 v[36:37], v0 offset:256\n\t" \
    "ds_read_b64 v[38:39], v0 offset:264\n\t" \
    "ds_read_b64 v[40:41], v0 offset:288\n\t" \
    "v_mfma_f32_16x16x32_bf16 v[24:27], v[120:123], v[128:131], v[24:27]\n\t" \
    "ds_read_b64 v[42:43], v0 offset:296\n\t" \
    "ds_read_b128 v[48:51], v1 offset:1920\n\t" \
    "ds_read_b128 v[52:55], v1 offset:3456\n\t" \
    "s_waitcnt lgkmcnt(14)\n\t" \
    "v_mfma_f32_16x16x32_bf16 v[12:15], v[116:119], v[132:135], v[12:15]\n\t" \
    "ds_read_b128 v[56:59], v1 offset:4992\n\t" \
    "v_mfma_f32_16x16x32_bf16 v[28:31], v[120:123], v[132:135], v[28:31]\n\t" \
    "s_waitcnt lgkmcnt(14)\n\t" \
    "v_mfma_f32_16x16x32_bf16 v[16:19], v[116:119], v[136:139], v[16:19]\n\t" \
    "ds_read_b64 v[76:77], v0 offset:320\n\t" \
    "v_mfma_f32_16x16x32_bf16 v[32:35], v[120:123], v[136:139], v[32:35]\n\t" \
    "s_waitcnt lgkmcnt(10)\n\t" \
    "v_mfma_f32_16x16x32_bf16 v[8:11], v[156:159], v[168:171], v[8:11]\n\t" \
    "ds_read_b64 v[78:79], v0 offset:328\n\t" \
    "ds_read_b64 v[80:81], v0 offset:352\n\t" \
    "ds_read_b64 v[82:83], v0 offset:360\n\t" \
    "v_mfma_f32_16x16x32_bf16 v[24:27], v[160:163], v[168:171], v[24:27]\n\t" \
    "ds_read_b128 v[88:91], v1 offset:1824\n\t" \
    "ds_read_b128 v[92:95], v1 offset:3360\n\t" \
    "s_waitcnt lgkmcnt(14)\n\t" \
    "v_mfma_f32_16x16x32_bf16 v[12:15], v[156:159], v[172:175], v[12:15]\n\t" \
    "ds_read_b128 v[96:99], v1 offset:4896\n\t" \
    "v_mfma_f32_16x16x32_bf16 v[28:31], v[160:163], v[172:175], v[28:31]\n\t" \
    "s_waitcnt lgkmcnt(14)\n\t" \
    "v_mfma_f32_16x16x32_bf16 v[16:19], v[156:159], v[176:179], v[16:19]\n\t" \
    "v_mfma_f32_16x16x32_bf16 v[32:35], v[160:163], v[176:179], v[32:35]\n\t" \
    "v_add_u32 v0, 256, v0\n\t" \
    "v_subrev_u32 v1, 384, v1\n\t" \
    "s_sub_u32 s69, s69, 1\n\t" \
    "s_cmp_lg_u32 s69, 0\n\t" \
    "s_cbranch_scc1 cv_c2_%=\n\t" \
    "s_waitcnt lgkmcnt(0)\n\t" \
    "s_mov_b32 s69, 4\n\t" \
    "cv_c3_%=:\n\t" \
    "s_waitcnt lgkmcnt(7)\n\t" \
    "v_mfma_f32_16x16x32_bf16 v[12:15], v[36:39], v[52:55], v[12:15]\n\t" \
    "ds_read_b64 v[116:117], v0 offset:128\n\t" \
    "ds_read_b64 v[118:119], v0 offset:136\n\t" \
    "ds_read_b64 v[120:121], v0 offset:160\n\t" \
    "v_mfma_f32_16x16x32_bf16 v[28:31], v[40:43], v[52:55], v[28:31]\n\t" \
    "ds_read_b64 v[122:123], v0 offset:168\n\t" \
    "ds_read_b128 v[132:135], v1 offset:3648\n\t" \
    "ds_read_b128 v[136:139], v1 offset:5184\n\t" \
    "s_waitcnt lgkmcnt(12)\n\t" \
    "v_mfma_f32_16x16x32_bf16 v[16:19], v[36:39], v[56:59], v[16:19]\n\t" \
    "ds_read_b64 v[156:157], v0 offset:192\n\t" \
    "ds_read_b64 v[158:159], v0 offset:200\n\t" \
    "ds_read_b64 v[160:161], v0 offset:224\n\t" \
    "v_mfma_f32_16x16x32_bf16 v[32:35], v[40:43], v[56:59], v[32:35]\n\t" \
    "s_waitcnt lgkmcnt(10)\n\t" \
    "v_mfma_f32_16x16x32_bf16 v[12:15], v[76:79], v[92:95], v[12:15]\n\t" \
    "ds_read_b64 v[162:163], v0 offset:232\n\t" \
    "ds_read_b128 v[172:175], v1 offset:3552\n\t" \
    "ds_read_b128 v[176:179], v1 offset:5088\n\t" \
    "v_mfma_f32_16x16x32_bf16 v[28:31], v[80:83], v[92:95], v[28:31]\n\t" \
    "s_waitcnt lgkmcnt(12)\n\t" \
    "v_mfma_f32_16x16x32_bf16 v[16:19], v[76:79], v[96:99], v[16:19]\n\t" \
    "v_mfma_f32_16x16x32_bf16 v[32:35], v[80:83], v[96:99], v[32:35]\n\t" \
    "s_waitcnt lgkmcnt(7)\n\t" \
    "v_mfma_f32_16x16x32_bf16 v[12:15], v[116:119], v[132:135], v[12:15]\n\t" \
    "ds_read_b64 v[36:37], v0 offset:256\n\t" \
    "ds_read_b64 v[38:39], v0 offset:264\n\t" \
    "ds_read_b64 v[40:41], v0 offset:288\n\t" \
    "v_mfma_f32_16x16x32_bf16 v[28:31], v[120:123], v[132:135], v[28:31]\n\t" \
    "ds_read_b64 v[42:43], v0 offset:296\n\t" \
    "ds_read_b128 v[52:55], v1 offset:3456\n\t" \
    "ds_read_b128 v[56:59], v1 offset:4992\n\t" \
    "s_waitcnt lgkmcnt(12)\n\t" \
    "v_mfma_f32_16x16x32_bf16 v[16:19], v[116:119], v[136:139], v[16:19]\n\t" \
    "ds_read_b64 v[76:77], v0 offset:320\n\t" \
    "ds_read_b64 v[78:79], v0 offset:328\n\t" \
    "ds_read_b64 v[80:81], v0 offset:352\n\t" \
    "v_mfma_f32_16x16x32_bf16 v[32:35], v[120:123], v[136:139], v[32:35]\n\t" \
    "s_waitcnt lgkmcnt(10)\n\t" \
    "v_mfma_f32_16x16x32_bf16 v[12:15], v[156:159], v[172:175], v[12:15]\n\t" \
    "ds_read_b64 v[82:83], v0 offset:360\n\t" \
    "ds_read_b128 v[92:95], v1 offset:3360\n\t" \
    "ds_read_b128 v[96:99], v1 offset:4896\n\t" \
    "v_mfma_f32_16x16x32_bf16 v[28:31], v[160:163], v[172:175], v[28:31]\n\t" \
    "s_waitcnt lgkmcnt(12)\n\t" \
    "v_mfma_f32_16x16x32_bf16 v[16:19], v[156:159], v[176:179], v[16:19]\n\t" \
    "v_mfma_f32_16x16x32_bf16 v[32:35], v[160:163], v[176:179], v[32:35]\n\t" \
    "v_add_u32 v0, 256, v0\n\t" \
    "v_subrev_u32 v1, 384, v1\n\t" \
    "s_sub_u32 s69, s69, 1\n\t" \
    "s_cmp_lg_u32 s69, 0\n\t" \
    "s_cbranch_scc1 cv_c3_%=\n\t" \
    "s_waitcnt lgkmcnt(0)\n\t" \
    "s_mov_b32 s69, 4\n\t" \
    "cv_c4_%=:\n\t" \
    "s_waitcnt lgkmcnt(5)\n\t" \
    "v_mfma_f32_16x16x32_bf16 v[16:19], v[36:39], v[56:59], v[16:19]\n\t" \
    "ds_read_b64 v[116:117], v0 offset:128\n\t" \
    "ds_read_b64 v[118:119], v0 offset:136\n\t" \
    "ds_read_b64 v[120:121], v0 offset:160\n\t" \
    "v_mfma_f32_16x16x32_bf16 v[32:35], v[40:43], v[56:59], v[32:35]\n\t" \
    "ds_read_b64 v[122:123], v0 offset:168\n\t" \
    "ds_read_b128 v[136:139], v1 offset:5184\n\t" \
    "ds_read_b64 v[156:157], v0 offset:192\n\t" \
    "s_waitcnt lgkmcnt(6)\n\t" \
    "v_mfma_f32_16x16x32_bf16 v[16:19], v[76:79], v[96:99], v[16:19]\n\t" \
    "ds_read_b64 v[158:159], v0 offset:200\n\t" \
    "ds_read_b64 v[160:161], v0 offset:224\n\t" \
    "ds_read_b64 v[162:163], v0 offset:232\n\t" \
    "v_mfma_f32_16x16x32_bf16 v[32:35], v[80:83], v[96:99], v[32:35]\n\t" \
    "ds_read_b128 v[176:179], v1 offset:5088\n\t" \
    "s_waitcnt lgkmcnt(5)\n\t" \
    "v_mfma_f32_16x16x32_bf16 v[16:19], v[116:119], v[136:139], v[16:19]\n\t" \
    "ds_read_b64 v[36:37], v0 offset:256\n\t" \
    "ds_read_b64 v[38:39], v0 offset:264\n\t" \
    "ds_read_b64 v[40:41], v0 offset:288\n\t" \
    "v_mfma_f32_16x16x32_bf16 v[32:35], v[120:123], v[136:139], v[32:35]\n\t" \
    "ds_read_b64 v[42:43], v0 offset:296\n\t" \
    "ds_read_b128 v[56:59], v1 offset:4992\n\t" \
    "ds_read_b64 v[76:77], v0 offset:320\n\t" \
    "s_waitcnt lgkmcnt(6)\n\t" \
    "v_mfma_f32_16x16x32_bf16 v[16:19], v[156:159], v[176:179], v[16:19]\n\t" \
    "ds_read_b64 v[78:79], v0 offset:328\n\t" \
    "ds_read_b64 v[80:81], v0 offset:352\n\t" \
    "ds_read_b64 v[82:83], v0 offset:360\n\t" \
    "v_mfma_f32_16x16x32_bf16 v[32:35], v[160:163], v[176:179], v[32:35]\n\t" \
    "ds_read_b128 v[96:99], v1 offset:4896\n\t" \
    "v_add_u32 v0, 256, v0\n\t" \
    "v_subrev_u32 v1, 384, v1\n\t" \
    "s_sub_u32 s69, s69, 1\n\t" \
    "s_cmp_lg_u32 s69, 0\n\t" \
    "s_cbranch_scc1 cv_c4_%=\n\t" \
    "s_waitcnt lgkmcnt(0)\n\t" \
    "s_nop 7\n\t" \
    "ds_write_b128 v2, v[4:7] offset:0\n\t" \
    "ds_write_b128 v2, v[8:11] offset:1024\n\t" \
    "ds_write_b128 v2, v[12:15] offset:2048\n\t" \
    "ds_write_b128 v2, v[16:19] offset:3072\n\t" \
    "ds_write_b128 v2, v[20:23] offset:4096\n\t" \
    "ds_write_b128 v2, v[24:27] offset:5120\n\t" \
    "ds_write_b128 v2, v[28:31] offset:6144\n\t" \
    "ds_write_b128 v2, v[32:35] offset:7168\n\t" \
    "s_waitcnt lgkmcnt(0)\n\t" \
    "cv_last_%=:\n\t" \
    ""
#define ASM_EA \
    "s_bitcmp1_b32 %1, 2\n\t" \
    "s_cselect_b32 s40, 0x30, 0x28\n\t" \
    "s_cmp_lt_u32 %1, 64\n\t" \
    "s_cselect_b32 s40, s40, 0x38\n\t" \
    "s_load_dwordx2 s[42:43], %0, s40\n\t" \
    "s_load_dword s48, %0, 0x40\n\t" \
    "s_getpc_b64 s[44:45]\n\t" \
    "ea_pc_%=:\n\t" \
    "s_bfe_u32 s40, %1, 0x20003\n\t" \
    "s_lshl_b32 s40, s40, 15\n\t" \
    "v_lshl_add_u32 v1, %2, 7, s40\n\t" \
    "v_min_u32 v1, code_end_marker-ea_pc_%=, v1\n\t" \
    "s_mov_b32 m0, 155648\n\t" \
    "v_readfirstlane_b32 s41, %2\n\t" \
    "global_load_lds_dword v1, s[44:45]\n\t" \
    "s_bitcmp1_b32 %1, 2\n\t" \
    "s_cselect_b32 s40, 24, 16\n\t" \
    "s_lshr_b32 s47, %1, 3\n\t" \
    "s_cmp_ge_u32 s47, s40\n\t" \
    "s_cbranch_scc1 ea_done1_%=\n\t" \
    "s_mov_b32 s46, pad_qk-ea_pc_%=\n\t" \
    "s_mov_b32 s40, pad_vv-ea_pc_%=\n\t" \
    "s_bitcmp1_b32 %1, 2\n\t" \
    "s_cselect_b32 s46, pad_gx-ea_pc_%=, s46\n\t" \
    "s_cselect_b32 s40, pad_gv-ea_pc_%=, s40\n\t" \
    "s_cmp_lt_u32 %1, 64\n\t" \
    "s_cselect_b32 s40, s46, s40\n\t" \
    "s_add_u32 s44, s44, s40\n\t" \
    "s_addc_u32 s45, s45, 0\n\t" \
    "s_swappc_b64 s[46:47], s[44:45]\n\t" \
    "s_lshl_b32 m0, s41, 4\n\t" \
    "s_and_b32 s40, %1, 3\n\t" \
    "s_lshl_b32 s40, s40, 11\n\t" \
    "s_bfe_u32 s46, %1, 0x30003\n\t" \
    "s_lshl_b32 s46, s46, 8\n\t" \
    "s_add_u32 s40, s40, s46\n\t" \
    "s_and_b32 s41, s41, 0x80\n\t" \
    "s_xor_b32 s40, s40, s41\n\t" \
    "v_lshrrev_b32 v1, 3, %2\n\t" \
    "v_and_b32 v1, 6, v1\n\t" \
    "v_xor_b32 v1, v1, %2\n\t" \
    "v_and_b32 v1, 15, v1\n\t" \
    "v_lshlrev_b32 v1, 4, v1\n\t" \
    "v_xor_b32 v1, s40, v1\n\t" \
    "v_lshrrev_b32 v2, 4, %2\n\t" \
    "v_lshl_add_u32 v1, v2, 13, v1\n\t" \
    "s_mov_b32 s40, 16\n\t" \
    "s_waitcnt lgkmcnt(0)\n\t" \
    "ea_lp2_%=:\n\t" \
    "global_load_lds_dwordx4 v1, s[42:43]\n\t" \
    "v_add_u32 v1, 0x20000, v1\n\t" \
    "s_add_u32 m0, m0, 4096\n\t" \
    "s_sub_u32 s40, s40, 1\n\t" \
    "s_cmp_lg_u32 s40, 0\n\t" \
    "s_cbranch_scc1 ea_lp2_%=\n\t" \
    "ea_done1_%=:\n\t" \
    "s_waitcnt lgkmcnt(0)\n\t" \
    "ea_last_%=:\n\t" \
    ""
#define ASM_CLOB "v0", "v1", "v2", "v3", "v4", "v5", "v6", "v7", "v8", "v9", "v10", "v11", "v12", "v13", "v14", "v15", "v16", "v17", "v18", "v19", "v20", "v21", "v22", "v23", "v24", "v25", "v26", "v27", "v28", "v29", "v30", "v31", "v32", "v33", "v34", "v35", "v36", "v37", "v38", "v39", "v40", "v41", "v42", "v43", "v44", "v45", "v46", "v47", "v48", "v49", "v50", "v51", "v52", "v53", "v54", "v55", "v56", "v57", "v58", "v59", "v60", "v61", "v62", "v63", "v64", "v65", "v66", "v67", "v68", "v69", "v70", "v71", "v72", "v73", "v74", "v75", "v76", "v77", "v78", "v79", "v80", "v81", "v82", "v83", "v84", "v85", "v86", "v87", "v88", "v89", "v90", "v91", "v92", "v93", "v94", "v95", "v96", "v97", "v98", "v99", "v100", "v101", "v102", "v103", "v104", "v105", "v106", "v107", "v108", "v109", "v110", "v111", "v112", "v113", "v114", "v115", "v116", "v117", "v118", "v119", "v120", "v121", "v122", "v123", "v124", "v125", "v126", "v127", "v128", "v129", "v130", "v131", "v132", "v133", "v134", "v135", "v136", "v137", "v138", "v139", "v140", "v141", "v142", "v143", "v144", "v145", "v146", "v147", "v148", "v149", "v150", "v151", "v152", "v153", "v154", "v155", "v156", "v157", "v158", "v159", "v160", "v161", "v162", "v163", "v164", "v165", "v166", "v167", "v168", "v169", "v170", "v171", "v172", "v173", "v174", "v175", "v176", "v177", "v178", "v179", "v180", "v181", "v182", "v183", "v184", "v185", "v186", "v187", "v188", "v189", "v190", "v191", "v192", "v193", "v194", "v195", "v196", "v197", "v198", "v199", "v200", "v201", "v202", "v203", "v204", "v205", "v206", "v207", "v208", "v209", "v210", "v211", "v212", "v213", "v214", "v215", "v222", "v223", "v224", "v225", "v226", "v227", "v228", "v229", "v230", "v231", "v232", "v233", "v234", "v235", "v236", "v237", "v238", "v239", "v240", "v241", "v242", "v243", "v244", "v245", "v246", "v247", "v248", "v249", "v250", "v251", "v252", "v253", "v254", "v255", "a0", "a1", "a2", "a3", "a4", "a5", "a6", "a7", "a8", "a9", "a10", "a11", "a12", "a13", "a14", "a15", "a16", "a17", "a18", "a19", "a20", "a21", "a22", "a23", "a24", "a25", "a26", "a27", "a28", "a29", "a30", "a31", "a32", "a33", "a34", "a35", "a36", "a37", "a38", "a39", "a40", "a41", "a42", "a43", "a44", "a45", "a46", "a47", "a48", "a49", "a50", "a51", "a52", "a53", "a54", "a55", "a56", "a57", "a58", "a59", "a60", "a61", "a62", "a63", "a64", "a65", "a66", "a67", "a68", "a69", "a70", "a71", "a72", "a73", "a74", "a75", "a76", "a77", "a78", "a79", "a80", "a81", "a82", "a83", "a84", "a85", "a86", "a87", "a88", "a89", "a90", "a91", "a92", "a93", "a94", "a95", "a96", "a97", "a98", "a99", "a100", "a101", "a102", "a103", "a104", "a105", "a106", "a107", "a108", "a109", "a110", "a111", "a112", "a113", "a114", "a115", "a116", "a117", "a118", "a119", "a120", "a121", "a122", "a123", "a124", "a125", "a126", "a127", "a128", "a129", "a130", "a131", "a132", "a133", "a134", "a135", "a136", "a137", "a138", "a139", "a140", "a141", "a142", "a143", "a144", "a145", "a146", "a147", "a148", "a149", "a150", "a151", "a152", "a153", "a154", "a155", "a156", "a157", "a158", "a159", "a160", "a161", "a162", "a163", "a164", "a165", "a166", "a167", "a168", "a169", "a170", "a171", "a172", "a173", "a174", "a175", "a176", "a177", "a178", "a179", "a180", "a181", "a182", "a183", "a184", "a185", "a186", "a187", "a188", "a189", "a190", "a191", "a192", "a193", "a194", "a195", "a196", "a197", "a198", "a199", "a200", "a201", "a202", "a203", "a204", "a205", "a206", "a207", "a208", "a209", "a210", "a211", "a212", "a213", "a214", "a215", "a216", "a217", "a218", "a219", "a220", "a221", "a222", "a223", "a224", "a225", "a226", "a227", "a228", "a229", "a230", "a231", "a232", "a233", "a234", "a235", "a236", "a237", "a238", "a239", "a240", "a241", "a242", "a243", "a244", "a245", "a246", "a247", "a248", "a249", "a250", "a251", "a252", "a253", "a254", "a255", "s33", "s34", "s35", "s36", "s37", "s38", "s39", "s40", "s41", "s42", "s43", "s44", "s45", "s46", "s47", "s48", "s49", "s50", "s51", "s52", "s53", "s54", "s55", "s56", "s57", "s58", "s59", "s60", "s61", "s62", "s63", "s64", "s65", "s66", "s67", "s68", "s69", "s70", "s71", "s72", "s73", "s74", "s75", "s76", "s77", "s78", "s79", "s80", "s81", "s82", "s83", "s84", "s85", "s86", "s87", "s88", "s89", "s90", "s91", "s92", "s93", "s94", "s95", "s96", "s97", "s98", "s99", "s100", "s101", "vcc", "scc", "memory"
#define ASM_CLOBW "v32", "v33", "v34", "v35", "v36", "v37", "v38", "v39", "v40", "v41", "v42", "v43", "v44", "v45", "v46", "v47", "v48", "v49", "v50", "v51", "v52", "v53", "v54", "v55", "v56", "v57", "v58", "v59", "v60", "v61", "v62", "v63", "v64", "v65", "v66", "v67", "v68", "v69", "v70", "v71", "v72", "v73", "v74", "v75", "v76", "v77", "v78", "v79", "v80", "v81", "v82", "v83", "v84", "v85", "v86", "v87", "v88", "v89", "v90", "v91", "v92", "v93", "v94", "v95", "v96", "v97", "v98", "v99", "v100", "v101", "v102", "v103", "v104", "v105", "v106", "v107", "v108", "v109", "v110", "v111", "v112", "v113", "v114", "v115", "v116", "v117", "v118", "v119", "v120", "v121", "v122", "v123", "v124", "v125", "v126", "v127", "v128", "v129", "v130", "v131", "v132", "v133", "v134", "v135", "v136", "v137", "v138", "v139", "v140", "v141", "v142", "v143", "v144", "v145", "v146", "v147", "v148", "v149", "v150", "v151", "v152", "v153", "v154", "v155", "v156", "v157", "v158", "v159", "v160", "v161", "v162", "v163", "v164", "v165", "v166", "v167", "v168", "v169", "v170", "v171", "v172", "v173", "v174", "v175", "v176", "v177", "v178", "v179", "v180", "v181", "v182", "v183", "v184", "v185", "v186", "v187", "v188", "v189", "v190", "v191", "v192", "v193", "v194", "v195", "v196", "v197", "v198", "v199", "v222", "v223", "v224", "v225", "v226", "v227", "v228", "v229", "v230", "v231", "v232", "v233", "v234", "v235", "v236", "v237", "v238", "v239", "v240", "v241", "v242", "v243", "v244", "v245", "v246", "v247", "v248", "v249", "v250", "v251", "v252", "v253", "v254", "v255", "a0", "a1", "a2", "a3", "a4", "a5", "a6", "a7", "a8", "a9", "a10", "a11", "a12", "a13", "a14", "a15", "a16", "a17", "a18", "a19", "a20", "a21", "a22", "a23", "a24", "a25", "a26", "a27", "a28", "a29", "a30", "a31", "a32", "a33", "a34", "a35", "a36", "a37", "a38", "a39", "a40", "a41", "a42", "a43", "a44", "a45", "a46", "a47", "a48", "a49", "a50", "a51", "a52", "a53", "a54", "a55", "a56", "a57", "a58", "a59", "a60", "a61", "a62", "a63", "a64", "a65", "a66", "a67", "a68", "a69", "a70", "a71", "a72", "a73", "a74", "a75", "a76", "a77", "a78", "a79", "a80", "a81", "a82", "a83", "a84", "a85", "a86", "a87", "a88", "a89", "a90", "a91", "a92", "a93", "a94", "a95", "a96", "a97", "a98", "a99", "a100", "a101", "a102", "a103", "a104", "a105", "a106", "a107", "a108", "a109", "a110", "a111", "a112", "a113", "a114", "a115", "a116", "a117", "a118", "a119", "a120", "a121", "a122", "a123", "a124", "a125", "a126", "a127", "a128", "a129", "a130", "a131", "a132", "a133", "a134", "a135", "a136", "a137", "a138", "a139", "a140", "a141", "a142", "a143", "a144", "a145", "a146", "a147", "a148", "a149", "a150", "a151", "a152", "a153", "a154", "a155", "a156", "a157", "a158", "a159", "a160", "a161", "a162", "a163", "a164", "a165", "a166", "a167", "a168", "a169", "a170", "a171", "a172", "a173", "a174", "a175", "a176", "a177", "a178", "a179", "a180", "a181", "a182", "a183", "a184", "a185", "a186", "a187", "a188", "a189", "a190", "a191", "a192", "a193", "a194", "a195", "a196", "a197", "a198", "a199", "a200", "a201", "a202", "a203", "a204", "a205", "a206", "a207", "a208", "a209", "a210", "a211", "a212", "a213", "a214", "a215", "a216", "a217", "a218", "a219", "a220", "a221", "a222", "a223", "a224", "a225", "a226", "a227", "a228", "a229", "a230", "a231", "a232", "a233", "a234", "a235", "a236", "a237", "a238", "a239", "a240", "a241", "a242", "a243", "a244", "a245", "a246", "a247", "a248", "a249", "a250", "a251", "a252", "a253", "a254", "a255", "s33", "s34", "s35", "s36", "s37", "s38", "s39", "s40", "s41", "s42", "s43", "s44", "s45", "s46", "s47", "s48", "s49", "s50", "s51", "s52", "s53", "s54", "s55", "s56", "s57", "s58", "s59", "s60", "s61", "s62", "s63", "s64", "s65", "s66", "s67", "s68", "s69", "s70", "s71", "s72", "s73", "s74", "s75", "s76", "s77", "s78", "s79", "s80", "s81", "s82", "s83", "s84", "s85", "s86", "s87", "s88", "s89", "s90", "s91", "s92", "s93", "s94", "s95", "s96", "s97", "s98", "s99", "s100", "s101", "vcc", "scc", "memory"
#define CV_CLOB "v0", "v1", "v2", "v3", "v4", "v5", "v6", "v7", "v8", "v9", "v10", "v11", "v12", "v13", "v14", "v15", "v16", "v17", "v18", "v19", "v20", "v21", "v22", "v23", "v24", "v25", "v26", "v27", "v28", "v29", "v30", "v31", "v32", "v33", "v34", "v35", "v36", "v37", "v38", "v39", "v40", "v41", "v42", "v43", "v44", "v45", "v46", "v47", "v48", "v49", "v50", "v51", "v52", "v53", "v54", "v55", "v56", "v57", "v58", "v59", "v60", "v61", "v62", "v63", "v64", "v65", "v66", "v67", "v68", "v69", "v70", "v71", "v72", "v73", "v74", "v75", "v76", "v77", "v78", "v79", "v80", "v81", "v82", "v83", "v84", "v85", "v86", "v87", "v88", "v89", "v90", "v91", "v92", "v93", "v94", "v95", "v96", "v97", "v98", "v99", "v100", "v101", "v102", "v103", "v104", "v105", "v106", "v107", "v108", "v109", "v110", "v111", "v112", "v113", "v114", "v115", "v116", "v117", "v118", "v119", "v120", "v121", "v122", "v123", "v124", "v125", "v126", "v127", "v128", "v129", "v130", "v131", "v132", "v133", "v134", "v135", "v136", "v137", "v138", "v139", "v140", "v141", "v142", "v143", "v144", "v145", "v146", "v147", "v148", "v149", "v150", "v151", "v152", "v153", "v154", "v155", "v156", "v157", "v158", "v159", "v160", "v161", "v162", "v163", "v164", "v165", "v166", "v167", "v168", "v169", "v170", "v171", "v172", "v173", "v174", "v175", "v176", "v177", "v178", "v179", "v180", "v181", "v182", "v183", "v184", "v185", "v186", "v187", "v188", "v189", "v190", "v191", "v192", "v193", "v194", "v195", "s69", "scc", "memory"
static constexpr unsigned G_STEP = 0x10000;
static constexpr unsigned G_WS_CTL = 0x300000;
static constexpr unsigned G_WS_FIX = 0x580000;
static constexpr unsigned G_WS_DONE = 0x5f0000;
static constexpr unsigned G_WS_EP = 0x5f2000;
static constexpr unsigned G_WS_STAMPS = 0xc000000;
static constexpr unsigned G_C_TASK = 0x200;
static constexpr unsigned G_C_GX = 0x1000;
static constexpr unsigned G_C_GV = 0x4000;
static constexpr unsigned G_C_KM = 0x8000;
static constexpr unsigned G_C_UM = 0x9000;
static constexpr unsigned G_C_PC = 0xc000;
static constexpr unsigned G_C_YC = 0xd000;
static constexpr unsigned G_C_GQ = 0x10000;
static constexpr unsigned G_C_FX = 0x280000;
static constexpr unsigned G_C_ERR = 0x280100;
static constexpr unsigned G_C_ARR = 0x280400;
static constexpr unsigned G_C_GO = 0x280800;
static constexpr unsigned G_C_UT = 0x281000;
static constexpr unsigned G_C_RDY = 0x282000;
static constexpr unsigned G_C_GT = 0x284000;
static constexpr unsigned G_C_KG = 0x288000;
static constexpr unsigned G_C_EF = 0x287000;
static constexpr unsigned G_C_RW = 0x280000;
static constexpr unsigned G_WS_H = 0x0;
static constexpr unsigned G_HS8 = 0x80000;
static constexpr unsigned G_HS4 = 0x40000;
static constexpr unsigned G_UXS = 0x200000;
static constexpr unsigned G_UMS = 0x90000;
static constexpr unsigned G_PROBE = 0x1bd400;
static constexpr unsigned G_D_QX = 0x600000;
static constexpr unsigned G_D_KX = 0x1600000;
static constexpr unsigned G_D_KM = 0x2600000;
static constexpr unsigned G_D_UX = 0x2e00000;
static constexpr unsigned G_D_UM = 0x7600000;
static constexpr unsigned G_D_PX = 0xa200000;
static constexpr unsigned G_D_PM = 0xa600000;
static constexpr unsigned G_D_KT = 0xa700000;
static constexpr unsigned G_D_UT = 0xac00000;
static constexpr unsigned G_D_YB = 0xaf00000;
static constexpr unsigned G_D_END = 0xb200000;
static constexpr unsigned G_SKM = 0x7;
static constexpr unsigned G_S1 = 0x18;
static constexpr unsigned G_G2 = 0x0;
static constexpr unsigned G_S0 = 0x7;
static constexpr unsigned G_GVN = 0x2;
static constexpr unsigned G_STG = 0x9000;
static constexpr unsigned G_KROW = 0x30;
static constexpr unsigned G_NHL = 0x1;
#define FLG(x_) (kDefault[5])
#define FLH(x_) (kDefault[7])
static constexpr int kDefault[8] = {PRM_DEFAULT};
#define MFMA32(a_, b_, c_) __builtin_amdgcn_mfma_f32_16x16x32_bf16(__builtin_bit_cast(bf16x8, a_), __builtin_bit_cast(bf16x8, b_), c_, 0, 0, 0)
#define MFMA16(a_, b_, c_) __builtin_amdgcn_mfma_f32_16x16x16bf16_1k(__builtin_bit_cast(s16x4, a_), __builtin_bit_cast(s16x4, b_), c_, 0, 0, 0)
#define WLD8(d_, voff_, base_, off_) asm volatile("s_nop 4\n\tglobal_load_dwordx2 %0, %1, %2 offset:%3 sc1" : "=v"(d_) : "v"(voff_), "s"(base_), "i"(off_) : "memory")
#define WLD16(d_, voff_, base_, off_) asm volatile("s_nop 4\n\tglobal_load_dwordx4 %0, %1, %2 offset:%3 sc1" : "=v"(d_) : "v"(voff_), "s"(base_), "i"(off_) : "memory")
#define WST16(voff_, d_, base_, off_) asm volatile("s_nop 4\n\tglobal_store_dwordx4 %0, %1, %2 offset:%3 sc1\n\ts_nop 1" :: "v"(voff_), "v"(d_), "s"(base_), "i"(off_) : "memory")
#define OMOD ""
#define OST8(p_, d_) asm volatile("global_store_dwordx2 %0, %1, off " OMOD :: "v"(p_), "v"(d_) : "memory")
#define OST16(p_, d_) asm volatile("global_store_dwordx4 %0, %1, off " OMOD "\n\ts_nop 1" :: "v"(p_), "v"(d_) : "memory")
#define OWT 0
#define OWM "sc1"
#define OST16W(p_, d_) asm volatile("global_store_dwordx4 %0, %1, off " OWM "\n\ts_nop 1" :: "v"(p_), "v"(d_) : "memory")
#define WLD16X4(d0, d1, d2, d3, voff_, base_) asm volatile("s_nop 4\n\tglobal_load_dwordx4 %0, %4, %5 sc1\n\tglobal_load_dwordx4 %1, %4, %5 offset:1024 sc1\n\t" \
    "global_load_dwordx4 %2, %4, %5 offset:2048 sc1\n\tglobal_load_dwordx4 %3, %4, %5 offset:3072 sc1" \
    : "=&v"(d0), "=&v"(d1), "=&v"(d2), "=&v"(d3) : "v"(voff_), "s"(base_) : "memory")

__device__ __forceinline__ u32x4 pack8(const f32x4& a, const f32x4& b) {
    return u32x4{pk2bf(a[0], a[1]), pk2bf(a[2], a[3]), pk2bf(b[0], b[1]), pk2bf(b[2], b[3])};
}
template <class T> __device__ __forceinline__ T* uni(T* q) {
    const u64 v = (u64)q;
    unsigned lo = __builtin_amdgcn_readfirstlane((unsigned)v), hi = __builtin_amdgcn_readfirstlane((unsigned)(v >> 32));
    asm volatile("s_nop 4" : "+s"(lo), "+s"(hi));
    return (T*)(((u64)hi << 32) | lo);
}
#define WC(p_) ((char*)(p_).X)
template <bool FAST = false>
__device__ __forceinline__ void wait_word(const MainP& p, const char* f, int want, unsigned code) {
    unsigned tries = 0;
#pragma unroll 1
    while (__any((int)__hip_atomic_load((const gu32*)f, RLX_AGENT) < want)) {
        if (++tries > kWaitCap) { __hip_atomic_store((gu32*)(p.ws + G_WS_CTL + G_C_ERR), code, RLX_AGENT); break; }
        if ((tries & 1023u) == 0u && __hip_atomic_load((const gu32*)(p.ws + G_WS_CTL + G_C_ERR), RLX_AGENT) != 0xffffffffu) break;
        if (FAST) __builtin_amdgcn_s_sleep(1); else __builtin_amdgcn_s_sleep(10);
    }
}

__device__ __forceinline__ int skew(int s, int id) { return (s ^ (G_SKM * id)) & 63; }
__device__ __forceinline__ unsigned um_base(int s, unsigned nm) { return G_D_UM + (s / 3) * 0x200000 + (s % 3) * G_UMS + ((nm >> (2 + s / 3)) & 1) * 8192; }
typedef GAS unsigned long long gu64;
#define IST(k_, slot_)
__device__ __forceinline__ void init_done(char* ws, int slot, unsigned val) {
    const unsigned e = __hip_atomic_load((const gu32*)(ws + G_WS_EP), RLX_AGENT);
    __hip_atomic_store((gu64*)(ws + G_WS_DONE + slot * 64), ((u64)val << 32) | (e + 1u), RLX_AGENT);
}
__device__ __forceinline__ void init_wait(char* ws, int w, int lane) {
    if (w == 0) {
        const unsigned e = __hip_atomic_load((const gu32*)(ws + G_WS_EP), RLX_AGENT);
        for (unsigned tries = 0; tries < kWaitCap; ++tries) {
            const unsigned d0 = __hip_atomic_load((const gu32*)(ws + G_WS_DONE + lane * 64), RLX_AGENT), d1 = __hip_atomic_load((const gu32*)(ws + G_WS_DONE + 2048 + lane * 64), RLX_AGENT);
            if (!__any((d0 != e + 1u) | (d1 != e + 1u))) break;
            __builtin_amdgcn_s_sleep(8);
        }
    }
    __syncthreads();
    if (w == 0 && lane == 0) __hip_atomic_fetch_add((gu32*)(ws + G_WS_CTL + G_C_ARR), 1u, RLX_AGENT);
}
__device__ __forceinline__ void fill_block(char* ws, int b, int tid, const u32x4& v0, const u32x4& v1) {
    const unsigned o = b * 8192 + tid * 16, o2 = o + 4096;
    asm volatile("global_store_dwordx4 %0, %1, %2 sc1\n\ts_nop 1" :: "v"(o), "v"(v0), "s"(ws) : "memory");
    if (b >= 256 || (b & 6) != 6 || tid < 64)
        asm volatile("global_store_dwordx4 %0, %1, %2 sc1\n\ts_nop 1" :: "v"(o2), "v"(v1), "s"(ws) : "memory");
}
__device__ __forceinline__ void init_fill(char* ws, const u16* mem0, int f, int tid) {
    const u32x4 sent = {0xffffffffu, 0xffffffffu, 0xffffffffu, 0xffffffffu};
    u32x4 v0 = sent, v1 = sent;
    if (f < 32 && (f & 4)) {
#pragma unroll
        for (int k = 0; k < 2; ++k) {
            const int q = tid + 256 * k + ((f & 2) ? 256 : 0), m = q & 15;
            if (!(f & 2) || (q >= 512 && q < 576)) {
                const unsigned* src = (const unsigned*)(mem0 + (2 * (m >> 2) + (m & 1)) * DM + 16 * (q >> 4) + (m & 2));
                const u32x4 o = {min(src[0], 0xfffffffeu), min(src[2], 0xfffffffeu), min(src[4], 0xfffffffeu), min(src[6], 0xfffffffeu)};
                if (k) v1 = o; else v0 = o;
            }
        }
    }
#pragma unroll 1
    for (int q = f; q < (G_G2 ? 216 : 184); q += 48) {
        if (q >= 136 && q < 152 && (tid & 127)) { v0 = u32x4{0u, 0u, 0u, 0u}; v1 = v0; }
        fill_block(ws, q < 64 ? q : q < 136 ? q - 64 + G_D_UM / 8192 : q < 168 ? 256 + ((q - 136) >> 2) * 8 + (q & 3) : q < 178 ? q - 168 + G_WS_CTL / 8192 : q < 184 ? q - 178 + G_WS_FIX / 8192
                                      : 260 + ((q - 184) >> 2) * 8 + (q & 3), tid, v0, v1);
        v0 = sent; v1 = sent;
    }
    const unsigned e = __hip_atomic_load((const gu32*)(ws + G_WS_EP), RLX_AGENT);
    asm volatile("s_waitcnt vmcnt(0)" :: "v"(e) : "memory");
    __syncthreads();
    if (tid == 0) __hip_atomic_store((gu64*)(ws + G_WS_DONE + (48 + f) * 64), (u64)(e + 1u), RLX_AGENT);
    if (tid == 0) IST(0, 48 + f);
}
__device__ __forceinline__ void init_cal(char* ws, int id, int tid, int n) {
    if (tid < 64) {
        const int pg = id >> 1;
        char* a = ws + (pg < 2 ? pg * 0x200000u : G_D_UM + (pg - 2) * 0x200000u) + G_PROBE + (id & 1) * 8192;
        const u32x4 z = {0u, 0u, 0u, 0u};
        asm volatile("global_store_dwordx4 %0, %1, off sc1\n\tglobal_store_dwordx4 %0, %1, off offset:1024 sc1\n\ts_waitcnt vmcnt(0)" :: "v"(a + tid * 16), "v"(z) : "memory");
        if (tid == 0) {
            unsigned best = 0xffffu;
            const unsigned ts1 = (unsigned)__builtin_amdgcn_s_memrealtime() << 4;
#pragma unroll 1
            for (int it = 0; it < n; ++it) {
                const unsigned c0 = (unsigned)__builtin_amdgcn_s_memtime();
                const unsigned v = __hip_atomic_load((const gu32*)(a + it * 128), RLX_AGENT);
                asm volatile("s_waitcnt vmcnt(0)" :: "v"(v) : "memory");
                best = min(best, (unsigned)__builtin_amdgcn_s_memtime() - c0);
            }
            init_done(ws, id, best);
            IST(0, id);
        }
    }
    __syncthreads();
}
__device__ __forceinline__ unsigned near_mask(char* ws, int lane) {
    const gu32* q = (const gu32*)(ws + G_WS_DONE + (lane & 31) * 128 + 4);
    return (unsigned)__ballot(__hip_atomic_load(q + 16, RLX_AGENT) < __hip_atomic_load(q, RLX_AGENT));
}

#define TST(slot_, i_)
#define PIN8(a) asm volatile("" : "+v"(a[0]), "+v"(a[1]), "+v"(a[2]), "+v"(a[3]), "+v"(a[4]), "+v"(a[5]), "+v"(a[6]), "+v"(a[7]))
struct AOps { u32x4 q[4], k[8]; int fl; };
struct APtr { const char* q; const char* k; const char* m; unsigned voff, vkm, voffm, vfl; };
__device__ __forceinline__ void attn_batch(const MainP& p, AOps& o, const APtr& a) {
    const char* q2 = a.q + 4096; const char* k2 = a.k + 4096;
    WLD16(o.q[0], a.voff, a.q, 0); WLD16(o.q[1], a.voff, a.q, 2048); WLD16(o.q[2], a.voff, q2, 0); WLD16(o.q[3], a.voff, q2, 2048);
    WLD16X4(o.k[0], o.k[1], o.k[2], o.k[3], a.voff, a.k); WLD16X4(o.k[4], o.k[5], o.k[6], o.k[7], a.voff, k2);
    asm volatile("global_load_dword %0, %1, %2 sc1" : "=v"(o.fl) : "v"(a.vfl), "s"(WC(p)) : "memory");
}
__device__ __forceinline__ void attn_pin(AOps& o) {
    asm volatile("" : "+v"(o.fl), "+v"(o.q[0]), "+v"(o.q[1]), "+v"(o.q[2]), "+v"(o.q[3])); PIN8(o.k);
}
static constexpr int NFINE = 6;
static constexpr int NG1 = 64 - NFINE;
__device__ __forceinline__ void score_round4(float* r, const f32x4& x) {
    asm volatile("v_cvt_pk_bf16_f32 %0, 0, %4\n\tv_cvt_pk_bf16_f32 %1, 0, %5\n\tv_cvt_pk_bf16_f32 %2, 0, %6\n\tv_cvt_pk_bf16_f32 %3, 0, %7\n\t"
                 "v_mul_f32 %0, 0x3db504f3, %0\n\tv_mul_f32 %1, 0x3db504f3, %1\n\tv_mul_f32 %2, 0x3db504f3, %2\n\tv_mul_f32 %3, 0x3db504f3, %3\n\t"
                 "v_cvt_pk_bf16_f32 %0, 0, %0\n\tv_cvt_pk_bf16_f32 %1, 0, %1\n\tv_cvt_pk_bf16_f32 %2, 0, %2\n\tv_cvt_pk_bf16_f32 %3, 0, %3"
                 : "=&v"(r[0]), "=&v"(r[1]), "=&v"(r[2]), "=&v"(r[3]) : "v"(x[0]), "v"(x[1]), "v"(x[2]), "v"(x[3]));
}
__device__ __forceinline__ float vmax3(float a, float b, float c) { float r; asm("v_max3_f32 %0, %1, %2, %3" : "=v"(r) : "v"(a), "v"(b), "v"(c)); return r; }
template <int N>
__device__ __forceinline__ void attn_soft(float (&q)[12], u32x2& Pm, u32x4& Px) {
    float m = vmax3(q[0], q[1], q[2]);
#pragma unroll
    for (int i = 3; i < N; i += 2) m = vmax3(m, q[i], q[i + 1 < N ? i + 1 : i]);
    { auto a = __builtin_amdgcn_permlane16_swap(__float_as_uint(m), __float_as_uint(m), false, false); m = vmax3(m, __uint_as_float(a[0]), __uint_as_float(a[1])); }
    { auto a = __builtin_amdgcn_permlane32_swap(__float_as_uint(m), __float_as_uint(m), false, false); m = vmax3(m, __uint_as_float(a[0]), __uint_as_float(a[1])); }
    const float ml = -m * 1.4426950408889634f;
#pragma unroll
    for (int i = 0; i < N; ++i) q[i] = __builtin_amdgcn_exp2f(fmaf(q[i], 1.4426950408889634f, ml));
    float sum = 0.5f * ((q[0] + q[1]) + (q[2] + q[3])) + ((q[4] + q[5]) + (q[6] + q[7]));
    if (N > 8) sum += (q[8] + q[9]) + (q[10] + q[11]);
    sum = rowsum4(sum);
    const float inv = __builtin_amdgcn_rcpf(sum);
    Pm = u32x2{pk2bf(q[0] * inv, q[1] * inv), pk2bf(q[2] * inv, q[3] * inv)};
    Px = u32x4{pk2bf(q[4] * inv, q[5] * inv), pk2bf(q[6] * inv, q[7] * inv), N > 8 ? pk2bf(q[8] * inv, q[9] * inv) : 0u, N > 8 ? pk2bf(q[10] * inv, q[11] * inv) : 0u};
}
#define NASPLIT 12
#define KTOUCH 0
#define HSL 1
#define NAG 2
static constexpr int NA = NASPLIT;
__device__ __forceinline__ int attn_next(int s) { return s < NA ? s + 2 : s + NAG; }
__device__ __forceinline__ unsigned attn_flag(int s, int hq) { return G_WS_CTL + G_C_PC + (s < NA ? s & 1 : (s - NA) % NAG) * 1024 + hq * 128; }
struct BOps { u32x4 q[8], k[8]; };
#define WLD16X4A(d0, d1, d2, d3, voff_, base_) asm volatile("s_nop 4\n\tglobal_load_dwordx4 %0, %4, %5 sc1\n\tglobal_load_dwordx4 %1, %4, %5 offset:1024 sc1\n\t" \
    "global_load_dwordx4 %2, %4, %5 offset:2048 sc1\n\tglobal_load_dwordx4 %3, %4, %5 offset:3072 sc1" \
    : "=&a"(d0), "=&a"(d1), "=&a"(d2), "=&a"(d3) : "v"(voff_), "s"(base_) : "memory")
#define PIN8A(a) asm volatile("" : "+a"(a[0]), "+a"(a[1]), "+a"(a[2]), "+a"(a[3]), "+a"(a[4]), "+a"(a[5]), "+a"(a[6]), "+a"(a[7]))
#define MFMA_AV0(acc, a_, b_) asm volatile("v_mfma_f32_16x16x32_bf16 %0, %1, %2, 0" : "=&v"(acc) : "a"(a_), "v"(b_))
#define MFMA_AV(acc, a_, b_) asm volatile("v_mfma_f32_16x16x32_bf16 %0, %1, %2, %0" : "+v"(acc) : "a"(a_), "v"(b_))
__device__ __forceinline__ void attn_batch8(const MainP& p, BOps& o, const APtr& a) {
    const char* q2 = a.q + 4096; const char* k2 = a.k + 4096;
    WLD16X4(o.q[0], o.q[1], o.q[2], o.q[3], a.voff, a.q); WLD16X4(o.q[4], o.q[5], o.q[6], o.q[7], a.voff, q2);
    WLD16X4A(o.k[0], o.k[1], o.k[2], o.k[3], a.voff, a.k); WLD16X4A(o.k[4], o.k[5], o.k[6], o.k[7], a.voff, k2);
}
__device__ __forceinline__ void attn_step(const MainP& p, BOps& o, BOps& n1, BOps& n2, APtr& a, int& kg, int& kk, int s, int h, int w, int lane) {
    const int g = lane >> 4, c = lane & 15;
    const float scale = 0.08838834764831845f;
    TST(s * 64 + h * 2, 0);
    const int s2 = min(attn_next(attn_next(s)), NSEG - 1);
    if (kg < s2 || kk < s) {
        VMC0();
        if (s < 50) wait_word(p, WC(p) + a.vfl, (lane & 1) ? s : s2, 13u); else wait_word<true>(p, WC(p) + a.vfl, (lane & 1) ? s : s2, 13u);
    }
    TST(s * 64 + h * 2, 1);
    a.q = p.ws + G_D_QX + (size_t)h * G_HS8 + skew(s2, h) * 8192; a.k = a.q + (G_D_KX - G_D_QX); a.m = p.ws + G_D_KM + (size_t)h * G_HS4 + skew(s, h) * 4096;
    __builtin_amdgcn_sched_barrier(0);
    u32x4 km[4];
    int fl;
    WLD16X4A(km[0], km[1], km[2], km[3], a.vkm, a.m);
    asm volatile("global_load_dword %0, %1, %2 sc1" : "=v"(fl) : "v"(a.vfl), "s"(WC(p)) : "memory");
    attn_batch8(p, n2, a);
    f32x4 y[5];
#pragma unroll
    for (int wc = 0; wc < 4; ++wc)
#pragma unroll
        for (int i = 0; i < 3; ++i) { if (wc == 0) MFMA_AV0(y[i], o.k[i >> 1], o.q[(i + 1) >> 1]); else MFMA_AV(y[i], o.k[wc * 2 + (i >> 1)], o.q[wc * 2 + ((i + 1) >> 1)]); }
    asm volatile("s_nop 7" : "+v"(y[0]), "+v"(y[1]), "+v"(y[2]));
    float q0[12], q1[12];
    score_round4(q0 + 4, y[0]); score_round4(q1 + 4, y[1]); score_round4(q1 + 8, y[2]);
#pragma unroll
    for (int i = 0; i < 4; ++i) {
        const bool vis = 4 * g + i <= c;
        q0[4 + i] = vis ? q0[4 + i] : -INFINITY; q1[8 + i] = vis ? q1[8 + i] : -INFINITY;
    }
    __builtin_amdgcn_sched_barrier(0);
    asm volatile("s_waitcnt vmcnt(16)\n\ts_barrier" : "+a"(km[0]), "+a"(km[1]), "+a"(km[2]), "+a"(km[3]), "+v"(fl) :: "memory");
    PIN8(n1.q); PIN8A(n1.k);
    { const unsigned fo = attn_flag(s, h >> 2); const int fv = s - 1;
      asm volatile("s_cmp_lg_u32 %3, 0\n\ts_cbranch_scc1 1f\n\tglobal_store_dword %0, %1, %2 sc1\n1:" :: "v"(fo), "v"(fv), "s"(WC(p)), "s"(w) : "scc", "memory"); }
#pragma unroll
    for (int wc = 0; wc < 4; ++wc)
#pragma unroll
        for (int i = 0; i < 2; ++i) { if (wc == 0) MFMA_AV0(y[3 + i], km[0], o.q[i]); else MFMA_AV(y[3 + i], km[wc], o.q[wc * 2 + i]); }
    asm volatile("s_nop 7" : "+v"(y[3]), "+v"(y[4]));
    kg = __builtin_amdgcn_readlane(fl, 0); kk = __builtin_amdgcn_readlane(fl, 1);
    score_round4(q0, y[3]); score_round4(q1, y[4]);
    u32x2 Pm0, Pm1; u32x4 Px0, Px1;
    attn_soft<8>(q0, Pm0, Px0); attn_soft<12>(q1, Pm1, Px1);
    char* pxb = p.ws + G_D_PX + (size_t)(s * 32 + h) * 2048;
    char* pmb = p.ws + G_D_PM + (size_t)s * 16384 + h * 256; char* pm2 = pmb + 8192;
    WST16(a.voff, Px0, pxb, 0); WST16(a.voff, Px1, pxb, 1024);
    asm volatile("global_store_dwordx2 %0, %1, %3 sc1\n\tglobal_store_dwordx2 %0, %2, %4 sc1" :: "v"(a.voffm), "v"(Pm0), "v"(Pm1), "s"(pmb), "s"(pm2) : "memory");
    { const unsigned fo = attn_flag(s, h >> 2);
      asm volatile("s_cmp_lt_u32 %4, 46\n\ts_cbranch_scc1 1f\n\ts_waitcnt vmcnt(0)\n\ts_barrier\n\ts_cmp_lg_u32 %3, 0\n\ts_cbranch_scc1 1f\n\tglobal_store_dword %0, %1, %2 sc1\n1:"
                   :: "v"(fo), "v"(s), "s"(WC(p)), "s"(w), "s"(s) : "scc", "memory"); }
    TST(s * 64 + h * 2, 2);
}
__device__ __forceinline__ void attn_role(const MainP& p, int s, int h, int w, int lane) {
    APtr a;
    a.q = p.ws + G_D_QX + (size_t)h * G_HS8 + skew(s, h) * 8192; a.k = a.q + (G_D_KX - G_D_QX);
    a.voff = lane * 16; a.vkm = (lane & 55) * 16; a.voffm = (lane & 15) * 16 + ((lane >> 4) & 1) * 8;
    a.vfl = G_WS_CTL + ((lane & 1) ? G_C_KM : G_C_GQ) + h * 128;
    BOps o0, o1, o2;
    wait_word(p, WC(p) + G_WS_CTL + G_C_GQ + h * 128, attn_next(s), 10u);
    attn_batch8(p, o0, a);
    a.q = p.ws + G_D_QX + (size_t)h * G_HS8 + skew(attn_next(s), h) * 8192; a.k = a.q + (G_D_KX - G_D_QX);
    attn_batch8(p, o1, a);
    VMC0();
    PIN8(o0.q); PIN8A(o0.k); PIN8(o1.q); PIN8A(o1.k);
    int kg = -1, kk = -1;
    static_assert(NA % 2 == 0, "ranges");
#pragma unroll 1
    for (;;) {
        attn_step(p, o0, o1, o2, a, kg, kk, s, h, w, lane);
        if ((s = attn_next(s)) >= NG1) break;
        attn_step(p, o1, o2, o0, a, kg, kk, s, h, w, lane);
        if ((s = attn_next(s)) >= NG1) break;
        attn_step(p, o2, o0, o1, a, kg, kk, s, h, w, lane);
        if ((s = attn_next(s)) >= NG1) break;
    }
    VMC0();
}
__device__ __forceinline__ unsigned umax4(const u32x4& v) { return max(max(v[0], v[1]), max(v[2], v[3])); }
__device__ __forceinline__ bool give_up(const MainP& p, unsigned& tries, unsigned code) {
    if (++tries <= kWaitCap / 4) return false;
    __hip_atomic_store((gu32*)(p.ws + G_WS_CTL + G_C_ERR), code, RLX_AGENT);
    return true;
}
__device__ __forceinline__ void task_attn_tail(const MainP& p, int s, int h, int w, int lane) {
    const int g = lane >> 4, c = lane & 15, qt = w & 1;
    const float scale = 0.08838834764831845f;
    APtr a;
    a.q = p.ws + G_D_QX + (size_t)h * G_HS8 + skew(s, h) * 8192 + qt * 1024; a.k = p.ws + G_D_KX + (size_t)h * G_HS8 + skew(s, h) * 8192; a.m = p.ws + G_D_KM + (size_t)h * G_HS4 + skew(s, h) * 4096;
    a.voff = lane * 16; a.vkm = (lane & 55) * 16; a.voffm = c * 16 + (g & 1) * 8; a.vfl = G_WS_CTL;
    AOps o;
    wait_word(p, WC(p) + G_WS_CTL + G_C_GQ + h * 128, s, 14u);
    attn_batch(p, o, a);
    VMC0();
    attn_pin(o);
    float q[12];
    u32x4 km[4];
#pragma unroll
    for (int kt = 1; kt >= -1; --kt) {
        if (kt < 0) {
            TST(s * 64 + h * 2 + qt, 0);
            wait_word(p, WC(p) + G_WS_CTL + G_C_KM + h * 128, s - 2, 15u);
            if (FLG(p.prm[5]) & 2) wait_word<true>(p, WC(p) + G_WS_CTL + G_C_KM + h * 128, s - 1, 15u);
            for (int i = 0; i < ((FLH(p.prm[7]) >> 14) & 15); ++i) __builtin_amdgcn_s_sleep(1);
            for (unsigned tries = 0;;) {
                WLD16X4(km[0], km[1], km[2], km[3], a.vkm, a.m);
                asm volatile("s_waitcnt vmcnt(0)" : "+v"(km[0]), "+v"(km[1]), "+v"(km[2]), "+v"(km[3]) :: "memory");
                if (!__any(max(max(umax4(km[0]), umax4(km[1])), max(umax4(km[2]), umax4(km[3]))) == 0xffffffffu) || give_up(p, tries, 16u)) break;
            }
            TST(s * 64 + h * 2 + qt, 1);
        }
        f32x4 y = {0.f, 0.f, 0.f, 0.f};
#pragma unroll
        for (int wc = 0; wc < 4; ++wc) y = MFMA32(kt < 0 ? km[wc] : o.k[wc * 2 + (kt < 0 ? 0 : kt)], o.q[wc], y);
#pragma unroll
        for (int i = 0; i < 4; ++i) q[4 + 4 * kt + i] = (16 * kt + 4 * g + i <= 16 * qt + c) ? bfr(bfr(y[i]) * scale) : -INFINITY;
    }
    u32x2 Pm; u32x4 Px;
    attn_soft<12>(q, Pm, Px);
    char* pxb = p.ws + G_D_PX + (size_t)(s * 32 + h) * 2048 + qt * 1024;
    char* pmb = p.ws + G_D_PM + (size_t)s * 16384 + h * 256 + qt * 8192;
    WST16(a.voff, Px, pxb, 0);
    asm volatile("global_store_dwordx2 %0, %1, %2 sc1" :: "v"(a.voffm), "v"(Pm), "s"(pmb) : "memory");
    TST(s * 64 + h * 2 + qt, 2);
}

#define RLD(d_, voff_, base_, off_) asm volatile("global_load_dwordx4 %0, %1, %2 offset:%3 sc1" : "=v"(d_) : "v"(voff_), "s"(base_), "i"(off_) : "memory")
#define RLDA(d_, voff_, base_, off_) asm volatile("global_load_dwordx4 %0, %1, %2 offset:%3 sc1" : "=a"(d_) : "v"(voff_), "s"(base_), "i"(off_) : "memory")
#define MFMA_ACC_A(acc, a_, b_) asm volatile("v_mfma_f32_16x16x32_bf16 %0, %1, %2, %0" : "+v"(acc) : "a"(a_), "v"(b_))
#define VMW(n_) asm volatile("s_waitcnt vmcnt(%0)" :: "i"(n_) : "memory")
template <int NT, int DEPTH, bool TAIL, int NM = 2>
__device__ __forceinline__ void task_out(const MainP& p, char* smem, int s, int ct0, int w, int lane, int m0 = 0) {
    const int g = lane >> 4, c = lane & 15;
    const unsigned voff = lane * 16;
    f32x4 acc[NT][2];
#pragma unroll
    for (int t = 0; t < NT; ++t) { acc[t][0] = f32x4{0.f, 0.f, 0.f, 0.f}; acc[t][1] = acc[t][0]; asm volatile("" : "+v"(acc[t][0]), "+v"(acc[t][1])); }
    const char* ub[NT];
#pragma unroll
    for (int t = 0; t < NT; ++t) ub[t] = uni(p.ws + G_D_UX + (size_t)(ct0 + t) * G_UXS + skew(s, ct0 + t) * 32768 + w * 8192);
    const char* pb = uni(p.ws + G_D_PX + (size_t)(s * 32 + w * 8) * 2048 + m0 * 1024);
    const char* um = uni(p.ws + um_base(s, p.prm[0]) + ct0 * 16384);
    unsigned vu[NT];
#pragma unroll
    for (int t = 0; t < NT; ++t) { vu[t] = t * 16384 + w * 2048 + voff; asm volatile("" : "+v"(vu[t])); }
    const char* pm = uni(p.ws + G_D_PM + (size_t)s * 16384 + w * 2048 + m0 * 8192);
    constexpr int NR = DEPTH + 1 > 10 ? 10 : DEPTH + 1;
    u32x4 a[NR][NT], b[10][2];
    unsigned vb = voff, va = voff;
    TST(4096 + (s * 36 + ct0) * 4 + w, 0);
    const int f0 = (int)__hip_atomic_load((const gu32*)(WC(p) + G_WS_CTL + (lane < 8 ? G_C_UM + (w * 8 + lane) * 128 : lane < 16 ? G_C_GV + ((s < (int)G_S1 ? s & 1 : 0) * 32 + w * 8 + lane - 8) * 128
                                                                                                                                : attn_flag(s, w * 2 + (lane & 1)) - G_WS_CTL)), RLX_AGENT);
    const bool early = !TAIL && !__any(f0 < s);
    if (!early) wait_word(p, WC(p) + G_WS_CTL + (lane < 8 ? G_C_GV + ((s < (int)G_S1 ? s & 1 : 0) * 32 + w * 8 + lane) * 128 : G_C_UM + w * 8 * 128), lane < 8 ? s : s - 3, 21u);
#define ALOAD(q) do { if ((q) < 8) { _Pragma("unroll") for (int t = 0; t < NT; ++t) RLDA(a[(q) % NR][t], va, ub[t], 0);  \
                                     asm volatile("v_add_u32 %0, 0x400, %0" : "+v"(va)); }                                            \
                      else { _Pragma("unroll") for (int t = 0; t < NT; ++t) RLDA(a[(q) % NR][t], vu[t], um, ((q) & 1) * 1024); } } while (0)
    constexpr int D0 = DEPTH < 8 ? DEPTH : 8;
    asm volatile("s_nop 4");
#pragma unroll
    for (int q = 0; q < D0; ++q) ALOAD(q);
    asm volatile("s_cmp_lg_u32 %0, 0\n\ts_cbranch_scc1 1f\n\ts_waitcnt vmcnt(0)\n1:\n\ts_waitcnt vmcnt(30)" :: "s"(__builtin_amdgcn_readfirstlane((int)early)) : "scc", "memory");
    unsigned tries = 0;
    if (TAIL) {
        const char* sp = pm + (lane & 7) * 256 + ((lane >> 3) & 1) * 8192;
        const int dl = (FLG(p.prm[5]) >> 26) & 31;
        if (dl) {
            wait_word<true>(p, WC(p) + G_WS_CTL + G_C_KM + ((ct0 >> 1) + w) * 128, s - 1, 25u);
            for (int i = 0; i < dl + (s < 63 ? 5 : 0); ++i) __builtin_amdgcn_s_sleep(2);
        } else
        while (__any(__hip_atomic_load((const gu32*)sp, RLX_AGENT) == 0xffffffffu) && !give_up(p, tries, 23u)) __builtin_amdgcn_s_sleep(1);
    } else if (!early) wait_word(p, WC(p) + G_WS_CTL + (lane < 8 ? G_C_UM + (w * 8 + lane) * 128 : attn_flag(s, w * 2 + (lane & 1)) - G_WS_CTL), s, 22u);
    if (!early) TST(4096 + (s * 36 + ct0) * 4 + w, 1);
    for (;;) {
    asm volatile("s_nop 4");
#pragma unroll
    for (int q = 0; q < 8; ++q) { RLD(b[q][0], vb, pb, 0); if (NM == 2) RLD(b[q][1], vb, pb, 1024); asm volatile("v_add_u32 %0, 0x800, %0" : "+v"(vb)); }
#pragma unroll
    for (int q = 8; q < 10; ++q) { RLD(b[q][0], voff, pm, (q & 1) * 1024); if (NM == 2) RLD(b[q][1], voff, pm + 8192, (q & 1) * 1024); }
#pragma unroll
    for (int q = D0; q < NR; ++q) ALOAD(q);
#pragma unroll
    for (int q = 0; q < 10; ++q) {
        const int issuedA = (NR + q < 10 ? NR + q : 10) - D0;
        if (q < D0) VMW(NM * (9 - q) + issuedA * NT); else VMW((issuedA - (q - D0 + 1)) * NT);
#pragma unroll
        for (int t = 0; t < NT; ++t) { MFMA_ACC_A(acc[t][0], a[q % NR][t], b[q][0]); if (NM == 2) MFMA_ACC_A(acc[t][1], a[q % NR][t], b[q][1]); }
        if (NR + q < 10) ALOAD(NR + q);
    }
    asm volatile("s_nop 7" ::: "memory");
#pragma unroll
    for (int t = 0; t < NT; ++t) asm volatile("" : "+v"(acc[t][0]), "+v"(acc[t][1]));
    if (!TAIL) break;
    f32x4 chk = acc[0][0];
#pragma unroll
    for (int t = 0; t < NT; ++t) chk += acc[t][1] + acc[t][0];
    const float ck = (chk[0] + chk[1]) + (chk[2] + chk[3]);
    if (!__any(ck != ck)) break;
    unsigned mx = 0;
#pragma unroll
    for (int q = 0; q < 10; ++q) { asm volatile("" : "+v"(b[q][0])); mx = max(mx, umax4(b[q][0])); if (NM == 2) { asm volatile("" : "+v"(b[q][1])); mx = max(mx, umax4(b[q][1])); } }
#pragma unroll
    for (int t = 0; t < NT; ++t) { asm volatile("" : "+a"(a[8 % NR][t]), "+a"(a[9 % NR][t])); mx = max(mx, max(umax4(a[8 % NR][t]), umax4(a[9 % NR][t]))); }
    if (!__any(mx == 0xffffffffu) || give_up(p, tries, 24u)) break;
    vb = voff;
#pragma unroll
    for (int t = 0; t < NT; ++t) { acc[t][0] = f32x4{0.f, 0.f, 0.f, 0.f}; acc[t][1] = acc[t][0]; asm volatile("" : "+v"(acc[t][0]), "+v"(acc[t][1])); }
    }
    TST(4096 + (s * 36 + ct0) * 4 + w, 2);
    f32x4* red = (f32x4*)smem;
#pragma unroll
    for (int t = 0; t < NT; ++t) { red[((w * NT + t) * 2 + 0) * 64 + lane] = acc[t][0]; if (NM == 2) red[((w * NT + t) * 2 + 1) * 64 + lane] = acc[t][1]; }
    __syncthreads();
    const int row = lane >> 2, pc = lane & 3;
#pragma unroll
    for (int it = 0; it < NT / 2 * NM; ++it)
        if ((it & 3) == w) {
            const int m = it % NM, t = 2 * (it / NM) + (pc >> 1);
            const f32x4* r0 = red + (t * 2 + m) * 64 + (pc & 1) * 32 + row;
            const f32x4 v0 = (r0[0] + r0[NT * 128]) + (r0[NT * 256] + r0[NT * 384]), v1 = (r0[16] + r0[NT * 128 + 16]) + (r0[NT * 256 + 16] + r0[NT * 384 + 16]);
            u16* o = p.out + (size_t)(32 * s + 16 * (m + m0) + row) * OUTW + 16 * ct0 + 32 * (it / NM) + 8 * pc;
            if (TAIL || s >= OWT) OST16W(o, pack8(v0, v1)); else OST16(o, pack8(v0, v1));
        }
}

__device__ __forceinline__ int index_of_pos(int pv) { return min(min(pv, 128 + (pv >> 1)), min(256 + (pv >> 2), 384 + (pv >> 3))); }
static constexpr int KROW = G_KROW;
static constexpr int KARR = 80 * KROW;
static constexpr int UCS = 2208;
struct BCh {
    u16 uc[4][UCS];
    f32x4 cst[NST];
    unsigned Kd[648];
    u16 Kh[KARR];
    u16 Kl[KARR];
};
static_assert(4 * sizeof(BCh) <= LDS_TOTAL - 32, "LDS");
__device__ __forceinline__ unsigned prm(unsigned hi, unsigned lo, unsigned sel) { return __builtin_amdgcn_perm(hi, lo, sel); }
#define BST(i) asm volatile("" ::: "memory")
#define UT_LOAD(p_) __builtin_nontemporal_load(p_)
template <class F>
__device__ __forceinline__ void task_ut(const MainP& p, char* smem, int rb, int w, int lane, F more) {
    const int tid = w * 64 + lane, g = lane >> 4, c = lane & 15;
    { char* ws = p.ws; if (tid == 0) IST(4, rb); }
    const u32x4* src = (const u32x4*)(p.U + (size_t)rb * 64 * DM);
    u32x4 v[18];
#pragma unroll
    for (int i = 0; i < 18; ++i) v[i] = UT_LOAD(src + tid + 256 * i);
#pragma unroll
    for (int i = 0; i < 18; ++i) { const int ch = tid + 256 * i, row = ch / 72; *(u32x4*)(smem + row * 1184 + (ch - 72 * row) * 16) = v[i]; }
    __syncthreads();
    { char* ws = p.ws; if (tid == 0) IST(3, rb); }
    const unsigned ad = (unsigned)(uintptr_t)smem + (16 * g + (c >> 2)) * 1184 + (w * 144 + 4 * (c & 3)) * 2;
    char* tw = smem + 77824 + w * 20736;
#pragma unroll
    for (int i = 0; i < 9; ++i) {
        u32x2 x0, x1, x2, x3;
        asm volatile("ds_read_b64_tr_b16 %0, %4 offset:%5\n\tds_read_b64_tr_b16 %1, %4 offset:%6\n\tds_read_b64_tr_b16 %2, %4 offset:%7\n\tds_read_b64_tr_b16 %3, %4 offset:%8\n\ts_waitcnt lgkmcnt(0)"
                     : "=&v"(x0), "=&v"(x1), "=&v"(x2), "=&v"(x3) : "v"(ad), "i"(i * 32), "i"(4 * 1184 + i * 32), "i"(8 * 1184 + i * 32), "i"(12 * 1184 + i * 32) : "memory");
        u32x4* o = (u32x4*)(tw + (16 * i + c) * 144 + g * 32);
        o[0] = u32x4{x0[0], x0[1], x1[0], x1[1]}; o[1] = u32x4{x2[0], x2[1], x3[0], x3[1]};
    }
    asm volatile("" ::: "memory");
    const unsigned vo = (w * 144 + (lane >> 3)) * (SL * 2) + rb * 128 + (lane & 7) * 16;
    const char* dst = p.ws + G_D_UT;
#pragma unroll
    for (int i = 0; i < 18; ++i) {
        const u32x4 a = *(const u32x4*)(tw + (8 * i + (lane >> 3)) * 144 + (lane & 7) * 16);
        const unsigned vi = vo + i * 8 * (SL * 2);
        WST16(vi, a, dst, 0);
    }
    VMC0();
    init_wait(p.ws, w, lane);
    if (tid == 0) __hip_atomic_store((gu32*)(p.ws + G_WS_CTL + G_C_UT + rb * 128), 1u, RLX_AGENT);
    { char* ws = p.ws; if (tid == 0) IST(2, rb); }
    more();
    VMC0();
    __syncthreads();
    if (tid == 0) __hip_atomic_store((gu32*)(p.ws + G_WS_CTL + G_C_UT + rb * 128), 2u, RLX_AGENT);
}
struct KPar { u16 ldt, dsk, pa[2][4]; };
__device__ __forceinline__ KPar kgen_par(const MainP& p, int d, int lane) {
    KPar k;
    k.ldt = p.logdt[d]; k.dsk = p.Dskip[d];
#pragma unroll
    for (int q = 0; q < 2; ++q) { const int i = d * NST + lane + 64 * q; k.pa[q][0] = p.logA[i]; k.pa[q][1] = p.Aim[i]; k.pa[q][2] = p.Cre[i]; k.pa[q][3] = p.Cim[i]; }
    return k;
}
__device__ __forceinline__ void task_s4d(const MainP& p, BCh& L, int d, int lane, bool first, int& nb, KPar& kp, int& pend) {
    const int g = lane >> 4, c = lane & 15;
    BST(0);
    const float dsk = bf2f(kp.dsk);
    const auto& pa = kp.pa;
    const float dt = bfr(expf(bf2f(kp.ldt)));
#pragma unroll
    for (int q = 0; q < 2; ++q) {
        const float ar = -bfr(expf(bf2f(pa[q][0])));
        const float cr = bf2f(pa[q][2]) * dt, ci = bf2f(pa[q][3]) * dt;
        L.cst[lane + 64 * q] = f32x4{ar * dt * 1.4426950408889634f, bf2f(pa[q][1]) * dt * 0.15915494309189535f,
                                     __log2f(hypotf(cr, ci)), atan2f(ci, cr) * 0.15915494309189535f};
    }
    const char* ut = uni(p.ws + G_D_UT + (size_t)d * 4096);
    const unsigned voff = lane * 16;
    u32x4 uu[4];
    unsigned tk = nb >= 0 ? 1u : 0u;
    __builtin_amdgcn_sched_barrier(0);
    asm volatile("s_mov_b64 exec, 1\n\tglobal_atomic_add %0, %1, %0, %2 sc0\n\ts_mov_b64 exec, -1" : "+v"(tk) : "v"(G_WS_CTL + G_C_TASK + 128), "s"(WC(p)) : "memory");
    const char* ue = uni(first ? (const char*)p.logA : ut);
    WLD16X4A(uu[0], uu[1], uu[2], uu[3], voff, ue);
    BST(1);
    {
        f32x4 acc[3];
#pragma unroll
        for (int v = 0; v < 3; ++v) acc[v] = f32x4{0.f, 0.f, 0.f, 0.f};
        auto base_of = [](int v, int r) { return v == 0 ? (r & 7) + 128 * (r >> 3) : v == 1 ? (r < 8 ? 256 + 2 * (r & 3) + 128 * (r >> 2) : 512 + 4 * (r & 1) + 128 * ((r - 8) >> 1))
                                                                                           : 1024 + 128 * min(r, 8); };
        const float b0 = (float)base_of(0, c), b1 = (float)base_of(1, c), b2 = (float)base_of(2, c), off = (float)(8 * c);
#define KG_POLAR(h_, l_, lg_, f_, a_, b_) do { const float m_ = __builtin_amdgcn_exp2f(fmaf(cc[0], b_, lg_)), p_ = __builtin_amdgcn_fractf(fmaf(a_, b_, f_)); \
            const float re_ = m_ * __builtin_amdgcn_cosf(p_), im_ = m_ * __builtin_amdgcn_sinf(p_);                                                            \
            h_ = pk2bf(re_, im_); l_ = pk2bf(re_ - lo2f(h_), im_ - hi2f(h_)); } while (0)
#pragma unroll 1
        for (int t = 0; t < NST / 16; ++t) {
            u32x4 fh, fl, ah[3], al[3];
#pragma unroll
            for (int j = 0; j < 4; ++j) {
                const f32x4 cc = L.cst[16 * t + 4 * g + j];
                KG_POLAR(fh[j], fl[j], 0.f, 0.f, -cc[1], off);
                KG_POLAR(ah[0][j], al[0][j], cc[2], cc[3], cc[1], b0); KG_POLAR(ah[1][j], al[1][j], cc[2], cc[3], cc[1], b1); KG_POLAR(ah[2][j], al[2][j], cc[2], cc[3], cc[1], b2);
            }
#pragma unroll
            for (int v = 0; v < 3; ++v) { acc[v] = MFMA32(al[v], fh, acc[v]); acc[v] = MFMA32(ah[v], fl, acc[v]); acc[v] = MFMA32(ah[v], fh, acc[v]); }
        }
        BST(2);
        auto split = [](float k) { const unsigned hi = pk2bf(k, 0.f) & 0xffffu; return hi | (pk2bf(0.f, k - __uint_as_float(hi << 16)) & 0xffff0000u); };
#pragma unroll
        for (int v = 0; v < 3; ++v)
#pragma unroll
            for (int i = 0; i < 4; ++i) {
                const int pos = base_of(v, 4 * g + i) + 8 * c;
                if (pos <= 2048) L.Kd[index_of_pos(pos)] = split(2.0f * acc[v][i]);
            }
    }
    __builtin_amdgcn_sched_barrier(0);
    asm volatile("s_waitcnt vmcnt(0)" : "+v"(tk), "+a"(uu[0]), "+a"(uu[1]), "+a"(uu[2]), "+a"(uu[3]) :: "memory");
    if (first) {
        wait_word(p, p.ws + G_WS_CTL + G_C_UT + (lane & 31) * 128, 1, 30u);
        WLD16X4A(uu[0], uu[1], uu[2], uu[3], voff, ut);
        asm volatile("s_waitcnt vmcnt(0)" : "+a"(uu[0]), "+a"(uu[1]), "+a"(uu[2]), "+a"(uu[3]) :: "memory");
    }
    if (pend >= 0 && lane == 0) __hip_atomic_fetch_add((gu32*)(WC(p) + G_WS_CTL + G_C_YC + pend * 128), 1u, RLX_AGENT);
    nb = __builtin_amdgcn_readfirstlane(tk) + 1;
    const u32x4 z4 = {0u, 0u, 0u, 0u};
    *(u32x4*)&L.Kh[8 * lane] = z4; *(u32x4*)&L.Kl[8 * lane] = z4;
    *(u32x4*)&L.Kh[512 + 8 * (lane & 31)] = z4; *(u32x4*)&L.Kl[512 + 8 * (lane & 31)] = z4;
    *(u32x4*)&L.uc[0][64 + SL] = z4;
    *(u32x4*)&L.uc[(lane >> 3) & 3][8 * (lane & 7)] = z4;
    asm volatile("" ::: "memory");
#pragma unroll
    for (int q = 0; q < 4; ++q) {
        const int e = lane + 64 * q, at = (16 + (e >> 2)) * KROW + 24 - 8 * (e & 3);
        unsigned k[8];
        const unsigned* kd = L.Kd + index_of_pos(8 * e);
        if (q == 0) {
            const bool two = lane >= 32;
#pragma unroll
            for (int r = 0; r < 8; ++r) k[7 - r] = kd[two ? (0x43222100 >> (4 * r)) & 7 : r];
        } else if (q == 1) {
            const unsigned k0 = kd[0], k1 = kd[1], k2 = kd[2];
            k[7] = k[6] = k[5] = k0; k[4] = k[3] = k[2] = k1; k[1] = k[0] = k2;
        } else {
            const unsigned k0 = kd[0], k1 = kd[1];
            k[7] = k[6] = k[5] = k[4] = k0; k[3] = (e & 1) ? k1 : k0; k[2] = k[1] = k[0] = k1;
        }
        u32x4 kh, kl;
#pragma unroll
        for (int j = 0; j < 4; ++j) { kh[j] = prm(k[2 * j + 1], k[2 * j], 0x05040100u); kl[j] = prm(k[2 * j + 1], k[2 * j], 0x07060302u); }
        *(u32x4*)&L.Kh[at] = kh;
        if (G_NHL == 2) *(u32x4*)&L.Kl[at] = kl;
    }
    BST(3);
#pragma unroll
    for (int q = 0; q < 4; ++q) *(u32x4*)&L.uc[0][64 + 8 * (lane + 64 * q)] = uu[q];
    asm volatile("" ::: "memory");
#pragma unroll
    for (int q = 0; q < 4; ++q) {
        const int e = lane + 64 * q;
        const int ue = 56 + 8 * e;
        const u32x4 a = *(const u32x4*)&L.uc[0][ue];
        const u32x2 b = *(const u32x2*)&L.uc[0][ue + 8];
        const unsigned s0 = __builtin_amdgcn_alignbit(a[1], a[0], 16), s1 = __builtin_amdgcn_alignbit(a[2], a[1], 16), s2 = __builtin_amdgcn_alignbit(a[3], a[2], 16),
                       s3 = __builtin_amdgcn_alignbit(b[0], a[3], 16), s4 = __builtin_amdgcn_alignbit(b[1], b[0], 16);
        *(u32x4*)&L.uc[1][ue] = u32x4{s0, s1, s2, s3};
        *(u32x4*)&L.uc[2][ue] = u32x4{a[1], a[2], a[3], b[0]};
        *(u32x4*)&L.uc[3][ue] = u32x4{s1, s2, s3, s4};
    }
    if (lane == 0) {
        const u32x4 a = *(const u32x4*)&L.uc[0][56 + SL];
        const unsigned s0 = __builtin_amdgcn_alignbit(a[1], a[0], 16), s1 = __builtin_amdgcn_alignbit(a[2], a[1], 16), s2 = __builtin_amdgcn_alignbit(a[3], a[2], 16), s3 = a[3] >> 16;
        *(u32x4*)&L.uc[1][56 + SL] = u32x4{s0, s1, s2, s3};
        *(u32x4*)&L.uc[2][56 + SL] = u32x4{a[1], a[2], a[3], 0u};
        *(u32x4*)&L.uc[3][56 + SL] = u32x4{s1, s2, s3, 0u};
    }
    kp = kgen_par(p, min(nb, DM - 1), lane);
    BST(4);
    {
        const int o = 33 + c + 8 * g;
        const unsigned ua = (unsigned)(uintptr_t)&L.uc[o & 3][o & ~3], ka = (unsigned)(uintptr_t)&L.Kh[(16 + c) * KROW + 8 * g];
        const f32x4* res = (const f32x4*)L.Kh + lane;
        asm volatile(ASM_CV :: "v"(ua), "v"(ka), "v"((unsigned)(uintptr_t)res) : CV_CLOB);
        asm volatile("" :: "v"(kp.ldt), "v"(kp.dsk), "v"(kp.pa[0][0]), "v"(kp.pa[0][1]), "v"(kp.pa[0][2]), "v"(kp.pa[0][3]), "v"(kp.pa[1][0]), "v"(kp.pa[1][1]), "v"(kp.pa[1][2]), "v"(kp.pa[1][3]));
        BST(5);
#pragma unroll
        for (int par = 0; par < 2; ++par)
#pragma unroll
            for (int t = 0; t < 4; ++t) {
                const int n = 16 * (par + 2 * (16 * t + c)) + 4 * g;
                const u32x2 uu = *(const u32x2*)&L.uc[0][64 + n];
                const f32x4 y4 = res[(par * 4 + t) * 64];
                const float r0 = bfr(y4[0]) + bfr(lo2f(uu[0]) * dsk), r1 = bfr(y4[1]) + bfr(hi2f(uu[0]) * dsk);
                const float r2 = bfr(y4[2]) + bfr(lo2f(uu[1]) * dsk), r3 = bfr(y4[3]) + bfr(hi2f(uu[1]) * dsk);
                *(u32x2*)&L.uc[1][n] = u32x2{pk2bf(r0, r1), pk2bf(r2, r3)};
            }
    }
    BST(6);
    char* yb = uni(p.ws + G_D_YB + (size_t)d * 4096);
#pragma unroll
    for (int q = 0; q < 4; ++q) { const u32x4 o = *(const u32x4*)&L.uc[1][8 * (lane + 64 * q)]; WST16(voff, o, yb, q * 1024); }
    pend = d >> 6;
    BST(7);
}
__device__ __forceinline__ void s4d_wave(const MainP& p, char* smem, int w, int lane, int quota) {
    int nb = 0, pend = -1;
    if (lane == 0) nb = (int)(__hip_atomic_fetch_add((gu32*)(WC(p) + G_WS_CTL + G_C_TASK + 128), 1u, RLX_AGENT) + 1u);
    nb = __builtin_amdgcn_readfirstlane(nb);
    KPar kp = kgen_par(p, min(nb, DM - 1), lane);
#pragma unroll 1
    for (int n = 0; n < quota; ++n) {
        const int d = nb;
        if (d >= DM) break;
        nb = n + 1 < quota ? 0 : -1;
        task_s4d(p, reinterpret_cast<BCh*>(smem)[w], d, lane, n == 0, nb, kp, pend);
    }
    VMC0();
    if (pend >= 0 && lane == 0) __hip_atomic_fetch_add((gu32*)(WC(p) + G_WS_CTL + G_C_YC + pend * 128), 1u, RLX_AGENT);
}
__device__ __forceinline__ void watch_role(const MainP& p, int lane) {
    const char* c = WC(p) + G_WS_CTL;
#pragma unroll 1
    for (int s = G_S0; s < NSEG; ++s) {
        const int n = min(s + 3, NSEG - 1);
        wait_word(p, c + G_C_GV + lane * 128, n - (n < (int)G_S1 ? (n ^ (lane >> 5)) & 1 : 0), 60u);
        wait_word(p, (lane < 32 ? c + G_C_GX : p.ws + G_WS_CTL + G_C_UT - 4096) + lane * 128, lane < 32 ? min(s + 1 + KTOUCH, NSEG - 1) : s < 40 ? -1 : 2, 61u);
        if (lane == 0) __hip_atomic_store((gu32*)(p.ws + G_WS_CTL + G_C_RDY + s * 128), 1u, RLX_AGENT);
    }
}
__device__ __forceinline__ void task_merge(const MainP& p, char* smem, int gr, int i, int w, int lane) {
    const int g = lane >> 4, c = lane & 15;
    wait_word(p, WC(p) + G_WS_CTL + G_C_YC + gr * 128, 63, 31u);
    const char* sb = uni(p.ws + G_D_YB + (size_t)gr * 262144 + i * 1024 + w * 256);
    const unsigned vt = g * 4096 + c * 16;
    u32x4 z[16];
#pragma unroll
    for (int j = 0; j < 16; ++j) { const char* sj = sb + j * 16384; WLD16(z[j], vt, sj, 0); }
    asm volatile("s_waitcnt vmcnt(0)" : "+v"(z[0]), "+v"(z[1]), "+v"(z[2]), "+v"(z[3]), "+v"(z[4]), "+v"(z[5]), "+v"(z[6]), "+v"(z[7]),
                 "+v"(z[8]), "+v"(z[9]), "+v"(z[10]), "+v"(z[11]), "+v"(z[12]), "+v"(z[13]), "+v"(z[14]), "+v"(z[15]) :: "memory");
    char* tw = smem + w * 17408;
#pragma unroll
    for (int j = 0; j < 16; ++j) *(u32x4*)(tw + (4 * j + g) * 272 + c * 16) = z[j];
    const unsigned ad = (unsigned)(uintptr_t)tw + (8 * g + (c >> 2)) * 272 + (c & 3) * 8;
    u16* orow = p.out + (size_t)(512 * i + 128 * w + c) * OUTW + DM + 64 * gr + 8 * g;
#pragma unroll
    for (int k = 0; k < 8; ++k) {
        u32x2 x0, x1, x2, x3;
        asm volatile("ds_read_b64_tr_b16 %0, %4 offset:%5\n\tds_read_b64_tr_b16 %1, %4 offset:%6\n\tds_read_b64_tr_b16 %2, %4 offset:%7\n\tds_read_b64_tr_b16 %3, %4 offset:%8\n\ts_waitcnt lgkmcnt(0)"
                     : "=&v"(x0), "=&v"(x1), "=&v"(x2), "=&v"(x3) : "v"(ad), "i"(k * 32), "i"(4 * 272 + k * 32), "i"(32 * 272 + k * 32), "i"(36 * 272 + k * 32) : "memory");
        OST16(orow + (size_t)16 * k * OUTW, (u32x4{x0[0], x0[1], x1[0], x1[1]}));
        OST16(orow + (size_t)16 * k * OUTW + 32, (u32x4{x2[0], x2[1], x3[0], x3[1]}));
    }
}

static constexpr int Q1 = NG1 * 6 + 36, Q2 = Q1 + (NFINE - 2) * 25;
static constexpr int NTASK = Q2 + 34 + 52;
__host__ __device__ __forceinline__ void task_of(int u, int m0, int& sg, int& r) {
    if (u < 6 * m0) { sg = u / 6; r = u % 6; }
    else if (u < 6 * m0 + 108) { sg = m0 + (u - 6 * m0) / 9; r = (u - 6 * m0) % 9; }
    else if (u < Q1) { sg = (u - 36) / 6; r = (u - 36) % 6; }
    else if (u < Q2) { sg = NG1 + (u - Q1) / 25; r = (u - Q1) % 25; }
    else { sg = u < Q2 + 34 ? 62 : 63; r = u < Q2 + 34 ? u - Q2 : u - Q2 - 34; }
}
__device__ __forceinline__ void worker_body(const MainP& p, char* smem, const int w, const int lane) {
    const int tid = w * 64 + lane;
    int* slot = (int*)(smem + LDS_TOTAL - 16);
    int nst = 0, nxt = -1;
    gu32* qa = (gu32*)(WC(p) + G_WS_CTL + G_C_TASK);
    const int m0 = (FLH(p.prm[7]) >> 4) & 63;
    for (;;) {
        if (tid == 0) {
            *slot = nxt >= 0 ? nxt : (int)(__hip_atomic_fetch_add(qa, 1u, RLX_AGENT) + 1u);
            nxt = -1;
        }
        __syncthreads();
        const int u = __builtin_amdgcn_readfirstlane(*slot);
        if (u >= NTASK) break;
        if (tid == 0 && u < Q1 - 140) nxt = (int)(__hip_atomic_fetch_add(qa, 1u, RLX_AGENT) + 1u);
        const bool last2 = u >= Q2;
        int sg, r;
        task_of(u, m0, sg, r);
        if (u < Q1) {
            if (r < 6) task_out<6, 6, false>(p, smem, sg, 6 * r, w, lane);
            else { const int m = 3 * (sg - m0) + r - 6; task_merge(p, smem, m >> 2, m & 3, w, lane); }
        }
        else if (r < 16) task_attn_tail(p, sg, 2 * r + (w >> 1), w, lane);
        else if (sg == 63) task_out<2, 9, true, 1>(p, smem, sg, (r - 16) & ~1, w, lane, r & 1);
        else if (last2) task_out<2, 9, true>(p, smem, sg, 2 * (r - 16), w, lane);
        else task_out<4, 9, true>(p, smem, sg, 4 * (r - 16), w, lane);
        if (last2) break;
        __syncthreads();
    }
}

__host__ __device__ constexpr unsigned phys(unsigned x) { return (x >> 13) * 16384 + (x & 8191); }
__host__ __device__ constexpr unsigned aMB(int f, int b) { return phys(f < 8 ? 16384 + f * 1024 + b : 28672 + b); }
__host__ __device__ constexpr unsigned aPX(int pr, int b) { return phys(pr * 1024 + b); }
__host__ __device__ constexpr unsigned aPM(int u, int b) { return phys(24576 + u * 1024 + b); }
static constexpr int L_KI = 8192, L_QI = 16384, L_VI = 147456;

__device__ __forceinline__ int lane_id() { int l; asm volatile("v_mbcnt_lo_u32_b32 %0, -1, 0\n\tv_mbcnt_hi_u32_b32 %0, -1, %0" : "=v"(l)); return l; }
typedef const __attribute__((address_space(4))) MainP* KArg;

__global__ __launch_bounds__(256, 1) void main_kernel(MainP p_) {
    __shared__ __attribute__((aligned(16))) char smem[LDS_TOTAL];
    KArg ka = (KArg)__builtin_amdgcn_kernarg_segment_ptr();
    static_assert(offsetof(MainP, W) == 0x28, "argument layout");
    asm volatile(ASM_EA :: "s"(ka), "s"(blockIdx.x), "v"(threadIdx.x) : "v1", "v2", "s40", "s41", "s42", "s43", "s44", "s45", "s46", "s47", "s48", "s49", "scc", "memory");
    asm volatile("" : "+s"(ka));
    const int b = blockIdx.x, x = b & 7, ix = b >> 3;
    const int w = __builtin_amdgcn_readfirstlane(threadIdx.x >> 6);
    const bool isr = x < 4 && ix >= 16;
    constexpr int NRF = 8 + 8 * G_GVN;
    if (__builtin_expect(ix < (x < 4 ? 16 : NRF), 1)) {
        const int h = (x & 3) * 8 + (ix & 7), id = h + (ix >> 4) * 32;
        const unsigned stb = 0;
#define UNR _Pragma("unroll")
#define CW(k, val) do { if ((k) < 16) ca[(k) & 15] = (unsigned)(val); else if ((k) < 32) cb[(k) & 15] = (unsigned)(val); else cl[((k) - 40) & 15] = (unsigned)(val); } while (0)
#define ROLEW(A_) asm volatile(A_ : "+{v[0:15]}"(ca), "+{v[16:31]}"(cb), "+{v[200:215]}"(cl) : "s"(ka), "s"(w), "s"(id), "s"(stb) : ASM_CLOBW)
#define PAD(n_) asm volatile("s_branch skip_" n_ "\npad_" n_ ":\n\ts_setpc_b64 s[46:47]\nskip_" n_ ":")
#define CW_ALL const int lane = lane_id(), g = lane >> 4, c = lane & 15, q = c >> 2; u32x16 ca = {}, cb = {}, cl = {};                                      \
        CW(0, lane * 16); CW(40, (4 * w + g) * 8192 + h * 256 + (c ^ (2 * g + 8 * (w >> 1))) * 16);                                                          \
        UNR for (int k = 0; k < 2; ++k) CW(41 + k, (8 * g + q) * 256 + ((4 * w + 2 * k + ((c >> 1) & 1)) ^ (2 * q + 8 * (g & 1))) * 16 + (c & 1) * 8)
#define CW_SRC(b_, key_) UNR for (int k = 0; k < 9; ++k) { const int pp = 64 * k + lane, r = pp / 72, pc = pp - 72 * r; CW(43 + k, (b_) + (8 * w + r) * 1152 + (pc ^ (key_)) * 16); }
#define UNIT(u_, key_) (((u_) >> 2) * 128 + (((u_) & 3) ^ (key_)) * 32 + (c & 3) * 8)
#define CW_NEAR CW(1, x * 65536 + aMB(2 * w, lane * 16)); CW(2, x * 65536 + aMB(8, w * 256 + lane * 4)); CW(3, 2 * w * 1024 + lane * 16);                            \
        UNR for (int i = 0; i < 6; ++i) CW(4 + i, ((2 * w + 2 + i) & 7) * 1024 + lane * 16);                                                                 \
        CW(10, (c & 2) ? 0xEEEE : 0x4444);                                                                                                                   \
        CW(20, x * 65536 + 22528 + (lane & 31) * 64); CW(21, lane < 32 ? (lane & 3) * 65536 + phys((lane >> 2) * 2048) + h * 64 : 32768 + h * 128);          \
        CW(22, lane * 4); CW(23, x * 65536 + aMB(2 * w + 1, lane * 16))
#define CW_FAR CW(0, c * 1152 + (g ^ (c >> 1)) * 16); CW(1, ca[0]); CW(2, ca[0] ^ 64); CW(3, w * 9216 + lane * 16); CW_SRC(0, (r >> 1) + 4 * (w & 1))                                   \
        CW(4, L_VI + w * 2048 + lane * 16); CW(5, L_VI + lane * 16); CW(6, lane * 4)
        if (x >= 4 && ix >= 8) {
            PAD("gv"); CW_ALL; CW_FAR;
            UNR for (int t = 0; t < 9; ++t) CW(8 + t, (9 * w + t) * G_UXS + skew(0, 9 * w + t) * 32768 + h * 1024 + lane * 16);
            UNR for (int k = 0; k < 2; ++k) CW(19 + k, (9 * w + 4 * k + g) * 16384 + h * 256 + c * 16);
            CW(21, (9 * w + 8) * 16384 + h * 256 + lane * 4);
            UNR for (int k = 0; k < 4; ++k) CW(52 + k, (4 * g + q) * 1152 + ((9 * w + k) >> 2) * 128 + ((2 * ((9 * w + k) & 3) + ((c >> 1) & 1)) ^ (2 * g + (q >> 1))) * 16 + (c & 1) * 8);
            ROLEW(ASM_GV);
        } else if (x >= 4) {
            PAD("gx"); CW_ALL; CW_FAR;
            CW(8, w * 2048 + lane * 16);
            ROLEW(ASM_GX);
        } else if (ix < 8) {
            PAD("qk"); CW_ALL; CW_NEAR;
            CW(12, L_QI + (16 * w + (c & 13)) * 32 + 8 * g + 4 * ((c >> 1) & 1));
            CW(13, L_QI + (8 * g + q) * 32 + (c & 3) * 8);
            CW(14, G_WS_CTL + G_C_KM + h * 128); CW(15, lane * 16 + 4096);
            CW(16, L_KI + (32 * w + c) * 16 + 4 * g);
            CW(17, L_KI + (4 * g + q) * 16 + (c & 1) * 8);
            const int mm = 8 * (w & 1) + 4 * (g >> 1) + (g & 1);
            CW(18, (x ^ 3) * 65536 + aPX(h >> 1, (h & 1) * 512 + (w & 1) * 256 + g * 64 + (c >> 3) * 32 + ((c >> 1) & 1) * 16 + (2 * (q & 1) + (c & 1)) * 4));
            CW(19, ((c & 1) + 2 * (c >> 3)) * 65536 + aPM(h >> 3, ((h >> 1) & 3) * 256 + (mm + 2 * ((c >> 1) & 1)) * 16 + (h & 1) * 8 + (q & 1) * 4));
            CW(24, G_WS_CTL + G_C_GX + h * 128);
            ROLEW(ASM_QK);
        } else {
            PAD("vv"); CW_ALL; CW_NEAR; CW_SRC(h * 147456, r & 4);
            const int tq = 9 * w + 2 * (g & 1) + (g >> 1);
            CW(14, tq * 16384 + h * 256 + c * 16);
            CW(15, (tq + 4) * 16384 + h * 256 + c * 16);
            CW(16, (9 * w + 8) * 16384 + h * 256 + c * 16 + (g & 1) * 8);
            CW(19, (c & 8) ? 0xEEEE : 0x4444);
            CW(12, L_QI + w * 576 + c * 16 + 4 * g);
            CW(13, L_QI + (8 * g + q) * 16 + (c & 1) * 8 + ((c >> 1) & 1) * 576);
            CW(25, L_QI + (8 * g + q) * 16 + (c & 1) * 8 + (1 - ((c >> 1) & 1)) * 576);
            UNR for (int k = 0; k < 4; ++k) CW(52 + k, ((k >> 1) ^ (q >> 1)) * 36864 + (4 * g + (q & 1)) * 1152 + UNIT(9 * w + 2 * (k & 1) + (q >> 1), 2 * (g & 1)));
            CW(24, (q >> 1) * 36864 + (4 * g + (q & 1)) * 1152 + UNIT(9 * w + 8, 2 * (g & 1)));
            ROLEW(ASM_V);
        }
    } else {
    if (isr && (ix < 25 || ix >= 29)) {
        const int id = x * 12 + (ix < 25 ? ix - 13 : ix - 29);
        init_cal(ka->ws, id, threadIdx.x, (FLG(ka->prm[5]) >> 16) & 15);
        if (FLG(ka->prm[5]) & 4) {
            const unsigned o = (id * 384 + threadIdx.x) * 128, o2 = o + (threadIdx.x & 128 ? 16384 : 32768);
            const char* a = (const char*)ka->X + o; const char* b2 = (const char*)ka->U + o;
            const char* c2 = (const char*)(w < 2 ? ka->X : ka->U) + o2;
            const char* ps = (const char*)(w == 0 ? ka->logA : w == 1 ? ka->Aim : w == 2 ? ka->Cre : ka->Cim) + id * 3072 + min((int)(threadIdx.x & 63), 23) * 128;
            asm volatile("s_mov_b32 m0, %4\n\ts_nop 0\n\tglobal_load_lds_dword %0, off\n\tglobal_load_lds_dword %1, off\n\tglobal_load_lds_dword %2, off\n\tglobal_load_lds_dword %3, off"
                         :: "v"(a), "v"(c2), "v"(b2), "v"(ps), "s"(163072) : "memory");
        }
    }
    if (x >= 4 ? ix >= 24 : (ix >= 25 && ix < 29)) init_fill(ka->ws, ka->mem0, x >= 4 ? (ix - 24) * 4 + (x & 3) : 32 + (ix - 25) * 4 + x, threadIdx.x);
    if (x >= 4 && !(FLG(ka->prm[5]) & 1)) {
        const char* src = (const char*)ka->W[3] + (size_t)((x & 3) * 8 + (ix & 7)) * 147456 + threadIdx.x * 128;
#pragma unroll
        for (int i = 0; i < 5; ++i)
            if (i < 4 || w < 2) asm volatile("s_mov_b32 m0, %1\n\ts_nop 0\n\tglobal_load_lds_dword %0, off" :: "v"(src + i * 32768), "s"(155648) : "memory");
    }
    if (isr && ix >= 25) {
        for (int i = 0; i < (ix < 29 ? 3 : 0) + HSL; ++i) __builtin_amdgcn_s_sleep(40);
        init_wait(ka->ws, w, threadIdx.x & 63);
    }
    asm volatile("" : "+s"(ka));
    if (x < 4 && (ix < 25 || w == 0)) {
        const int lane = threadIdx.x & 63, g = lane >> 4, c = lane & 15;
        const unsigned lb = w * G_STG;
        unsigned* ct = (unsigned*)(smem + lb) + lane;
        asm volatile("" :: "v"(ct) : "memory");
#define CTW(k, val) ct[(k) * 64] = (unsigned)(val)
        const u64 ws = (u64)ka->ws, prm = (u64)ka;
        const u64 stb = 0;
        CTW(0, lane * 16);
        const int j = x * 9 + ix - 16;
        if (ix >= 25) {
            CTW(1, x * 65536 + 22528 + (lane & 31) * 64);
            CTW(2, G_WS_CTL + (lane < 32 || !KTOUCH ? G_C_EF + 3 * 2048 + (lane & 31) * 64 : G_C_GX + (8 * x + (lane & 7)) * 128)); CTW(3, lane < 32 || !KTOUCH ? 2048 : 0);
            for (int k = 0; k < 7; ++k) {
                const int q = min(ix - 25 + 7 * k, KTOUCH ? 43 : 35), id = q < 36 ? 9 * x + (q >> 2) : 8 * x + q - 36;
                CTW(4 + k, (q < 36 ? G_D_UX + id * G_UXS + skew(0, id) * 32768 + (q & 3) * 8192 : G_D_KX + id * G_HS8 + skew(0, id) * 8192) + lane * 128);
                CTW(11 + k, q < 36 ? 32768 : 8192);
            }
        } else {
            const int rot = (FLG(ka->prm[5]) & 16) ? 0 : (x ^ 3) * 4;
            if (w >= 2) {
                return;
            }
            CTW(1, lane * 16 + 4096);
            CTW(2, x * 65536 + aMB(j >> 2, (j & 3) * 256 + (4 * g + 2 * ((c >> 1) & 1) + (c & 1)) * 16 + (c >> 2) * 4));
            for (int k = 0; k < 6; ++k) { const int q = 9 * k + ix - 16; CTW(46 + k, x * 65536 + lane * 16 + (q < 29 ? phys(q * 1024) : 0x200000 + phys((q < 45 ? q - 29 : q < 53 ? q - 21 : 20) * 1024))); }
            CTW(4, (c & 2) ? 0xEEEE : 0x4444); CTW(5, (c & 1) ? 0x03020706u : 0x05040100u);
            CTW(6, G_WS_CTL + G_C_GV + ((w & (G_GVN - 1)) * 32 + ((lane & 31) ^ (2 * rot))) * 128); CTW(7, x * 65536 + (lane & 31) * 64); CTW(8, lane * 4); CTW(9, 131072);
            for (int k = 0; k < 16; ++k) { const int i = k ^ rot; CTW(10 + k, x * 65536 + aPX(i, (g >> 1) * 512 + (2 * (c >> 2) + (c & 1)) * 64 + (g & 1) * 32 + ((c >> 1) & 1) * 16)); CTW(26 + k, i * 2048 + lane * 16); }
            for (int k = 0; k < 4; ++k) CTW(42 + k, x * 65536 + aPM(k, lane * 16));
        }
        const int id = ix >= 25 ? ix - 25 : j;
#define ROLE(A_) asm volatile(A_ :: "s"(ws), "s"(ws), "s"(ws), "s"(w), "s"(id), "s"(ws), "s"(prm), "s"(lb), "s"(stb) : ASM_CLOB)
        if (ix >= 25) ROLE(ASM_H);
        else ROLE(ASM_R);
    }
    else if (x >= 4 && ix >= 24) {
        if (ix >= 25) for (int i = 0; i < ((FLG(ka->prm[5]) >> 5) & 7); ++i) __builtin_amdgcn_s_sleep(36);
        const char* xs = (const char*)ka->X + (size_t)((ix - 24) * 4 + (x & 3)) * 73728 + threadIdx.x * 128;
        const int f = (ix - 24) * 4 + (x & 3);
        const u16* ps = (w == 0 ? ka->logA : w == 1 ? ka->Aim : w == 2 ? ka->Cre : ka->Cim) + f * 2304 + min((int)(threadIdx.x & 63), 35) * 64;
        if (ix < 25 || !(FLG(ka->prm[5]) & 4))
        asm volatile("s_mov_b32 m0, %4\n\ts_nop 0\n\tglobal_load_lds_dword %0, off\n\tglobal_load_lds_dword %1, off\n\tglobal_load_lds_dword %2, off\n\tglobal_load_lds_dword %3, off"
                     :: "v"(xs), "v"(xs + 32768), "v"(xs + 65536 - w * 8192), "v"(ps), "s"(163072) : "memory");
        if (ix < 25) for (int i = 0; i < ((FLG(ka->prm[5]) >> 5) & 7); ++i) __builtin_amdgcn_s_sleep(36);
    }
    }
    asm volatile("" : "+s"(ka));
    MainP p;
    p.prm[5] = ka->prm[5]; p.prm[7] = ka->prm[7];
    p.U = ka->U; p.ws = ka->ws; p.logA = ka->logA; p.Aim = ka->Aim; p.Cre = ka->Cre; p.Cim = ka->Cim; p.logdt = ka->logdt; p.Dskip = ka->Dskip; p.out = ka->out;
    if (x >= 4 && ix >= 24) {
        const u32x4 sent = {0xffffffffu, 0xffffffffu, 0xffffffffu, 0xffffffffu};
        const int tid = w * 64 + lane_id(), h = (ix - 24) * 4 + (x & 3);
        task_ut(p, smem, h, w, lane_id(), [&] {
#pragma unroll 1
            for (int sg = NG1; sg < 64; ++sg) {
                const unsigned ok = G_D_KM + h * G_HS4 + skew(sg, h) * 4096 + tid * 16;
                const unsigned op = tid < 128 ? G_D_PX + (sg * 32 + h) * 2048 + tid * 16 : G_D_PM + sg * 16384 + ((tid >> 4) & 1) * 8192 + h * 256 + (tid & 15) * 16;
                asm volatile("global_store_dwordx4 %0, %2, %3 sc1\n\tglobal_store_dwordx4 %1, %2, %3 sc1\n\ts_nop 1" :: "v"(ok), "v"(op), "v"(sent), "s"(p.ws) : "memory");
            }
        });
    }
    p.prm[0] = near_mask(p.ws, lane_id());
    p.X = (const u16*)(p.ws + ((p.prm[0] & 2) ? 0 : 8192));
    {
        const int nq = x < 4 ? (w + ((FLH(p.prm[7]) >> 1) & 3) > 3 ? 0 : 1000) : ix < 8 ? (FLG(p.prm[5]) >> 8) & 3 : ix < 16 ? (FLG(p.prm[5]) >> 10) & 3 : (ix & 2) && ((31 - ix) >> 2) < NAG ? 0 : ix < 24 ? (ix < 16 + ((FLH(p.prm[7]) >> 10) & 15) ? (FLG(p.prm[5]) >> 12) & 3 : 0) : (FLG(p.prm[5]) >> 14) & 3;
        if (b == 239 && w == 0) watch_role(p, lane_id());
        else if (nq || b == 239) {
            for (int i = 0; i < (int)(((ix & 7) * 4 + w) * ((FLG(p.prm[5]) >> 23) & 7)); ++i) __builtin_amdgcn_s_sleep(3);
            s4d_wave(p, smem, w, lane_id(), nq == 3 || b == 239 ? 1000 : nq);
        }
        if (x < 4) return;
        __syncthreads();
    }
    if ((ix & 18) == 18 && ((31 - ix) >> 2) < NAG) { attn_role(p, ((31 - ix) >> 2) + (ix < 24 ? NA : 0), (x & 3) * 8 + (ix & 1) * 4 + w, w, lane_id());
        if (b == 255) {
            wait_word(p, p.ws + G_WS_CTL + G_C_ARR, 255, 50u);
            if (w == 0 && lane_id() == 0) __hip_atomic_fetch_add((gu32*)(p.ws + G_WS_EP), 1u, RLX_AGENT);
        }
        __syncthreads();
    }
    worker_body(p, smem, w, lane_id());
}

extern "C" __global__ void code_end_marker() {}

static constexpr int g_prm[8] = {PRM_DEFAULT};

extern "C" void kernel_launch(void* const* d_in, const int* in_sizes, int n_in, void* d_out, int out_size,
                              void* d_ws, size_t ws_size, hipStream_t stream) {
    char* ws = (char*)(((uintptr_t)d_ws + 0x1FFFFF) & ~(uintptr_t)0x1FFFFF);
    MainP p;
    for (int i = 0; i < 8; ++i) p.prm[i] = g_prm[i];
    p.mem0 = (const u16*)d_in[1];
    for (int i = 0; i < 4; ++i) p.W[i] = (const u16*)d_in[2 + i];
    p.X = (const u16*)d_in[0]; p.U = (const u16*)d_in[7]; p.ws = ws;
    p.logA = (const u16*)d_in[8]; p.Aim = (const u16*)d_in[9]; p.Cre = (const u16*)d_in[10]; p.Cim = (const u16*)d_in[11];
    p.logdt = (const u16*)d_in[12]; p.Dskip = (const u16*)d_in[13]; p.out = (u16*)d_out;
    main_kernel<<<256, 256, 0, stream>>>(p);
}
```

```cpp
#include <hip/hip_runtime.h>
#include <hip/hip_bf16.h>
#include <stdint.h>
#include <stddef.h>

typedef __attribute__((ext_vector_type(8))) __bf16 bf16x8;
typedef __attribute__((ext_vector_type(4))) short s16x4;
typedef __attribute__((ext_vector_type(4))) float f32x4;
typedef __attribute__((ext_vector_type(4))) unsigned u32x4;
typedef __attribute__((ext_vector_type(2))) unsigned u32x2;
typedef __attribute__((ext_vector_type(2))) float f32x2;
typedef __attribute__((ext_vector_type(16))) unsigned u32x16;
typedef unsigned short u16;
typedef unsigned long long u64;

#define GAS __attribute__((address_space(1)))
typedef GAS unsigned gu32;
#define RLX_AGENT __ATOMIC_RELAXED, __HIP_MEMORY_SCOPE_AGENT

static constexpr int DM = 576, NH = 32, DH = 128, MM = 8, SEG = 32, NSEG = 64, SL = 2048;
static constexpr int HD = NH * DH;
static constexpr int OUTW = 2 * DM;
static constexpr int NST = 128;

__device__ __forceinline__ float bf2f(u16 h) { return __uint_as_float(((unsigned)h) << 16); }
typedef __attribute__((ext_vector_type(2))) __bf16 bf16x2_t;
__device__ __forceinline__ unsigned pk2bf(float a, float b) {
    bf16x2_t v; v[0] = (__bf16)a; v[1] = (__bf16)b;
    return __builtin_bit_cast(unsigned, v);
}
__device__ __forceinline__ float bfr(float f) { return __uint_as_float(pk2bf(f, f) << 16); }
__device__ __forceinline__ float lo2f(unsigned u) { return __uint_as_float(u << 16); }
__device__ __forceinline__ float hi2f(unsigned u) { return __uint_as_float(u & 0xffff0000u); }
__device__ __forceinline__ float rowmax4(float x) {
    auto a = __builtin_amdgcn_permlane16_swap(__float_as_uint(x), __float_as_uint(x), false, false);
    x = fmaxf(__uint_as_float(a[0]), __uint_as_float(a[1]));
    auto b = __builtin_amdgcn_permlane32_swap(__float_as_uint(x), __float_as_uint(x), false, false);
    return fmaxf(__uint_as_float(b[0]), __uint_as_float(b[1]));
}
__device__ __forceinline__ float rowsum4(float x) {
    auto a = __builtin_amdgcn_permlane16_swap(__float_as_uint(x), __float_as_uint(x), false, false);
    x = __uint_as_float(a[0]) + __uint_as_float(a[1]);
    auto b = __builtin_amdgcn_permlane32_swap(__float_as_uint(x), __float_as_uint(x), false, false);
    return __uint_as_float(b[0]) + __uint_as_float(b[1]);
}

struct MainP {
    int prm[8];
    const u16* mem0; const u16* W[4]; const u16* X; const u16* U;
    char* ws;
    const u16* logA; const u16* Aim; const u16* Cre; const u16* Cim; const u16* logdt; const u16* Dskip;
    u16* out;
};
static constexpr int LDS_TOTAL = 163840;
static constexpr unsigned kWaitCap = 1u << 18;
#define VMC0() asm volatile("s_waitcnt vmcnt(0)" ::: "memory")
#define PRM_DEFAULT 1020, 600, 128, 640, 736, 1434873899, 3145976, 52930
#define ASM_QK \
    "s_load_dwordx2 s[42:43], %3, 40\n\t" \
    "s_load_dwordx2 s[44:45], %3, 48\n\t" \
    "s_load_dwordx2 s[36:37], %3, 0x58\n\t" \
    "s_load_dwordx2 s[50:51], %3, 0x48\n\t" \
    "s_load_dwordx8 s[84:91], %3, 0\n\t" \
    "s_mov_b32 s66, %4\n\t" \
    "s_mov_b32 s67, %5\n\t" \
    "s_mov_b32 s68, 262144\n\t" \
    "s_mov_b32 s72, 0\n\t" \
    "s_mov_b32 s83, -1\n\t" \
    "s_mov_b32 s65, 0\n\t" \
    "s_lshl_b32 s82, s66, 10\n\t" \
    "s_waitcnt lgkmcnt(0)\n\t" \
    "s_add_u32 s46, s42, 2097152\n\t" \
    "s_addc_u32 s47, s43, 0\n\t" \
    "s_add_u32 m0, s82, 65536\n\t" \
    "s_nop 0\n\t" \
    "global_load_lds_dwordx4 v200, s[46:47]\n\t" \
    "s_add_u32 s46, s42, 2228224\n\t" \
    "s_addc_u32 s47, s43, 0\n\t" \
    "s_add_u32 m0, s82, 69632\n\t" \
    "s_nop 0\n\t" \
    "global_load_lds_dwordx4 v200, s[46:47]\n\t" \
    "s_add_u32 s46, s42, 2359296\n\t" \
    "s_addc_u32 s47, s43, 0\n\t" \
    "s_add_u32 m0, s82, 73728\n\t" \
    "s_nop 0\n\t" \
    "global_load_lds_dwordx4 v200, s[46:47]\n\t" \
    "s_add_u32 s46, s42, 2490368\n\t" \
    "s_addc_u32 s47, s43, 0\n\t" \
    "s_add_u32 m0, s82, 77824\n\t" \
    "s_nop 0\n\t" \
    "global_load_lds_dwordx4 v200, s[46:47]\n\t" \
    "s_add_u32 s46, s42, 2621440\n\t" \
    "s_addc_u32 s47, s43, 0\n\t" \
    "s_add_u32 m0, s82, 81920\n\t" \
    "s_nop 0\n\t" \
    "global_load_lds_dwordx4 v200, s[46:47]\n\t" \
    "s_add_u32 s46, s42, 2752512\n\t" \
    "s_addc_u32 s47, s43, 0\n\t" \
    "s_add_u32 m0, s82, 86016\n\t" \
    "s_nop 0\n\t" \
    "global_load_lds_dwordx4 v200, s[46:47]\n\t" \
    "s_add_u32 s46, s42, 2883584\n\t" \
    "s_addc_u32 s47, s43, 0\n\t" \
    "s_add_u32 m0, s82, 90112\n\t" \
    "s_nop 0\n\t" \
    "global_load_lds_dwordx4 v200, s[46:47]\n\t" \
    "s_add_u32 s46, s42, 3014656\n\t" \
    "s_addc_u32 s47, s43, 0\n\t" \
    "s_add_u32 m0, s82, 94208\n\t" \
    "s_nop 0\n\t" \
    "global_load_lds_dwordx4 v200, s[46:47]\n\t" \
    "s_add_u32 s46, s42, 3145728\n\t" \
    "s_addc_u32 s47, s43, 0\n\t" \
    "s_add_u32 m0, s82, 98304\n\t" \
    "s_nop 0\n\t" \
    "global_load_lds_dwordx4 v200, s[46:47]\n\t" \
    "s_add_u32 s46, s42, 3276800\n\t" \
    "s_addc_u32 s47, s43, 0\n\t" \
    "s_add_u32 m0, s82, 102400\n\t" \
    "s_nop 0\n\t" \
    "global_load_lds_dwordx4 v200, s[46:47]\n\t" \
    "s_add_u32 s46, s42, 3407872\n\t" \
    "s_addc_u32 s47, s43, 0\n\t" \
    "s_add_u32 m0, s82, 106496\n\t" \
    "s_nop 0\n\t" \
    "global_load_lds_dwordx4 v200, s[46:47]\n\t" \
    "s_add_u32 s46, s42, 3538944\n\t" \
    "s_addc_u32 s47, s43, 0\n\t" \
    "s_add_u32 m0, s82, 110592\n\t" \
    "s_nop 0\n\t" \
    "global_load_lds_dwordx4 v200, s[46:47]\n\t" \
    "s_add_u32 s46, s42, 3670016\n\t" \
    "s_addc_u32 s47, s43, 0\n\t" \
    "s_add_u32 m0, s82, 114688\n\t" \
    "s_nop 0\n\t" \
    "global_load_lds_dwordx4 v200, s[46:47]\n\t" \
    "s_add_u32 s46, s42, 3801088\n\t" \
    "s_addc_u32 s47, s43, 0\n\t" \
    "s_add_u32 m0, s82, 118784\n\t" \
    "s_nop 0\n\t" \
    "global_load_lds_dwordx4 v200, s[46:47]\n\t" \
    "s_add_u32 s46, s42, 3932160\n\t" \
    "s_addc_u32 s47, s43, 0\n\t" \
    "s_add_u32 m0, s82, 122880\n\t" \
    "s_nop 0\n\t" \
    "global_load_lds_dwordx4 v200, s[46:47]\n\t" \
    "s_add_u32 s46, s42, 4063232\n\t" \
    "s_addc_u32 s47, s43, 0\n\t" \
    "s_add_u32 m0, s82, 126976\n\t" \
    "s_nop 0\n\t" \
    "global_load_lds_dwordx4 v200, s[46:47]\n\t" \
    "s_add_u32 s46, s42, 4194304\n\t" \
    "s_addc_u32 s47, s43, 0\n\t" \
    "s_add_u32 m0, s82, 131072\n\t" \
    "s_nop 0\n\t" \
    "global_load_lds_dwordx4 v200, s[46:47]\n\t" \
    "s_add_u32 s46, s42, 4325376\n\t" \
    "s_addc_u32 s47, s43, 0\n\t" \
    "s_add_u32 m0, s82, 135168\n\t" \
    "s_nop 0\n\t" \
    "global_load_lds_dwordx4 v200, s[46:47]\n\t" \
    "s_add_u32 s46, s42, 4456448\n\t" \
    "s_addc_u32 s47, s43, 0\n\t" \
    "s_add_u32 m0, s82, 139264\n\t" \
    "s_nop 0\n\t" \
    "global_load_lds_dwordx4 v200, s[46:47]\n\t" \
    "s_add_u32 s46, s42, 4587520\n\t" \
    "s_addc_u32 s47, s43, 0\n\t" \
    "s_add_u32 m0, s82, 143360\n\t" \
    "s_nop 0\n\t" \
    "global_load_lds_dwordx4 v200, s[46:47]\n\t" \
    "s_mov_b32 s77, 3019898880\n\t" \
    "s_mov_b32 s78, 3054198966\n\t" \
    "s_waitcnt lgkmcnt(0)\n\t" \
    "s_mov_b32 s70, 116\n\t" \
    "qk_E1_%=:\n\t" \
    "s_memtime s[56:57]\n\t" \
    "s_memrealtime s[2:3]\n\t" \
    "s_nop 0\n\t" \
    "s_memrealtime s[4:5]\n\t" \
    "s_nop 0\n\t" \
    "s_memrealtime s[6:7]\n\t" \
    "s_nop 0\n\t" \
    "s_memrealtime s[8:9]\n\t" \
    "s_nop 0\n\t" \
    "s_memrealtime s[10:11]\n\t" \
    "s_nop 0\n\t" \
    "s_memrealtime s[12:13]\n\t" \
    "s_nop 0\n\t" \
    "s_memrealtime s[14:15]\n\t" \
    "s_nop 0\n\t" \
    "s_memrealtime s[16:17]\n\t" \
    "s_nop 0\n\t" \
    "s_memrealtime s[18:19]\n\t" \
    "s_nop 0\n\t" \
    "s_memrealtime s[20:21]\n\t" \
    "s_nop 0\n\t" \
    "s_memrealtime s[22:23]\n\t" \
    "s_nop 0\n\t" \
    "s_memrealtime s[24:25]\n\t" \
    "s_nop 0\n\t" \
    "s_memrealtime s[26:27]\n\t" \
    "s_nop 0\n\t" \
    "s_memrealtime s[28:29]\n\t" \
    "s_memtime s[54:55]\n\t" \
    "s_add_u32 s70, s70, 8\n\t" \
    "s_waitcnt lgkmcnt(0)\n\t" \
    "s_sub_u32 s69, s54, s56\n\t" \
    "s_cmp_gt_u32 s69, s70\n\t" \
    "s_cbranch_scc1 qk_E1_%=\n\t" \
    "s_add_u32 s2, s2, s4\n\t" \
    "s_add_u32 s6, s6, s8\n\t" \
    "s_add_u32 s10, s10, s12\n\t" \
    "s_add_u32 s14, s14, s16\n\t" \
    "s_add_u32 s18, s18, s20\n\t" \
    "s_add_u32 s22, s22, s24\n\t" \
    "s_add_u32 s2, s2, s6\n\t" \
    "s_add_u32 s10, s10, s14\n\t" \
    "s_add_u32 s18, s18, s22\n\t" \
    "s_add_u32 s2, s2, s26\n\t" \
    "s_add_u32 s10, s10, s18\n\t" \
    "s_add_u32 s2, s2, s10\n\t" \
    "s_mul_i32 s70, s28, 13\n\t" \
    "s_sub_u32 s70, s70, s2\n\t" \
    "s_min_u32 s70, s70, 52\n\t" \
    "s_mul_i32 s69, s69, s70\n\t" \
    "s_mul_hi_u32 s69, s69, 76695844\n\t" \
    "s_mov_b32 s75, s73\n\t" \
    "s_mov_b32 s76, s74\n\t" \
    "s_add_u32 s73, s56, s69\n\t" \
    "s_lshl_b32 s74, s28, 4\n\t" \
    "s_waitcnt vmcnt(32) lgkmcnt(0)\n\t" \
    "s_barrier\n\t" \
    "ds_read_b64_tr_b16 a[0:1], v201 offset:0\n\t" \
    "ds_read_b64_tr_b16 a[2:3], v201 offset:1024\n\t" \
    "ds_read_b64_tr_b16 a[4:5], v201 offset:8192\n\t" \
    "ds_read_b64_tr_b16 a[6:7], v201 offset:9216\n\t" \
    "ds_read_b64_tr_b16 a[72:73], v202 offset:0\n\t" \
    "ds_read_b64_tr_b16 a[74:75], v202 offset:1024\n\t" \
    "ds_read_b64_tr_b16 a[76:77], v202 offset:8192\n\t" \
    "ds_read_b64_tr_b16 a[78:79], v202 offset:9216\n\t" \
    "s_waitcnt vmcnt(28) lgkmcnt(0)\n\t" \
    "s_barrier\n\t" \
    "ds_read_b64_tr_b16 a[8:9], v201 offset:16384\n\t" \
    "ds_read_b64_tr_b16 a[10:11], v201 offset:17408\n\t" \
    "ds_read_b64_tr_b16 a[12:13], v201 offset:24576\n\t" \
    "ds_read_b64_tr_b16 a[14:15], v201 offset:25600\n\t" \
    "ds_read_b64_tr_b16 a[80:81], v202 offset:16384\n\t" \
    "ds_read_b64_tr_b16 a[82:83], v202 offset:17408\n\t" \
    "ds_read_b64_tr_b16 a[84:85], v202 offset:24576\n\t" \
    "ds_read_b64_tr_b16 a[86:87], v202 offset:25600\n\t" \
    "s_add_u32 s46, s44, 0\n\t" \
    "s_addc_u32 s47, s45, 0\n\t" \
    "s_add_u32 m0, s82, 0\n\t" \
    "s_nop 0\n\t" \
    "global_load_lds_dwordx4 v200, s[46:47]\n\t" \
    "s_add_u32 s46, s44, 131072\n\t" \
    "s_addc_u32 s47, s45, 0\n\t" \
    "s_add_u32 m0, s82, 4096\n\t" \
    "s_nop 0\n\t" \
    "global_load_lds_dwordx4 v200, s[46:47]\n\t" \
    "s_add_u32 s46, s44, 262144\n\t" \
    "s_addc_u32 s47, s45, 0\n\t" \
    "s_add_u32 m0, s82, 8192\n\t" \
    "s_nop 0\n\t" \
    "global_load_lds_dwordx4 v200, s[46:47]\n\t" \
    "s_add_u32 s46, s44, 393216\n\t" \
    "s_addc_u32 s47, s45, 0\n\t" \
    "s_add_u32 m0, s82, 12288\n\t" \
    "s_nop 0\n\t" \
    "global_load_lds_dwordx4 v200, s[46:47]\n\t" \
    "s_waitcnt vmcnt(28) lgkmcnt(0)\n\t" \
    "s_barrier\n\t" \
    "ds_read_b64_tr_b16 a[16:17], v201 offset:32768\n\t" \
    "ds_read_b64_tr_b16 a[18:19], v201 offset:33792\n\t" \
    "ds_read_b64_tr_b16 a[20:21], v201 offset:40960\n\t" \
    "ds_read_b64_tr_b16 a[22:23], v201 offset:41984\n\t" \
    "ds_read_b64_tr_b16 a[88:89], v202 offset:32768\n\t" \
    "ds_read_b64_tr_b16 a[90:91], v202 offset:33792\n\t" \
    "ds_read_b64_tr_b16 a[92:93], v202 offset:40960\n\t" \
    "ds_read_b64_tr_b16 a[94:95], v202 offset:41984\n\t" \
    "s_add_u32 s46, s44, 524288\n\t" \
    "s_addc_u32 s47, s45, 0\n\t" \
    "s_add_u32 m0, s82, 16384\n\t" \
    "s_nop 0\n\t" \
    "global_load_lds_dwordx4 v200, s[46:47]\n\t" \
    "s_add_u32 s46, s44, 655360\n\t" \
    "s_addc_u32 s47, s45, 0\n\t" \
    "s_add_u32 m0, s82, 20480\n\t" \
    "s_nop 0\n\t" \
    "global_load_lds_dwordx4 v200, s[46:47]\n\t" \
    "s_add_u32 s46, s44, 786432\n\t" \
    "s_addc_u32 s47, s45, 0\n\t" \
    "s_add_u32 m0, s82, 24576\n\t" \
    "s_nop 0\n\t" \
    "global_load_lds_dwordx4 v200, s[46:47]\n\t" \
    "s_add_u32 s46, s44, 917504\n\t" \
    "s_addc_u32 s47, s45, 0\n\t" \
    "s_add_u32 m0, s82, 28672\n\t" \
    "s_nop 0\n\t" \
    "global_load_lds_dwordx4 v200, s[46:47]\n\t" \
    "s_waitcnt vmcnt(28) lgkmcnt(0)\n\t" \
    "s_barrier\n\t" \
    "ds_read_b64_tr_b16 a[24:25], v201 offset:49152\n\t" \
    "ds_read_b64_tr_b16 a[26:27], v201 offset:50176\n\t" \
    "ds_read_b64_tr_b16 a[28:29], v201 offset:57344\n\t" \
    "ds_read_b64_tr_b16 a[30:31], v201 offset:58368\n\t" \
    "ds_read_b64_tr_b16 a[96:97], v202 offset:49152\n\t" \
    "ds_read_b64_tr_b16 a[98:99], v202 offset:50176\n\t" \
    "ds_read_b64_tr_b16 a[100:101], v202 offset:57344\n\t" \
    "ds_read_b64_tr_b16 a[102:103], v202 offset:58368\n\t" \
    "s_add_u32 s46, s44, 1048576\n\t" \
    "s_addc_u32 s47, s45, 0\n\t" \
    "s_add_u32 m0, s82, 32768\n\t" \
    "s_nop 0\n\t" \
    "global_load_lds_dwordx4 v200, s[46:47]\n\t" \
    "s_add_u32 s46, s44, 1179648\n\t" \
    "s_addc_u32 s47, s45, 0\n\t" \
    "s_add_u32 m0, s82, 36864\n\t" \
    "s_nop 0\n\t" \
    "global_load_lds_dwordx4 v200, s[46:47]\n\t" \
    "s_add_u32 s46, s44, 1310720\n\t" \
    "s_addc_u32 s47, s45, 0\n\t" \
    "s_add_u32 m0, s82, 40960\n\t" \
    "s_nop 0\n\t" \
    "global_load_lds_dwordx4 v200, s[46:47]\n\t" \
    "s_add_u32 s46, s44, 1441792\n\t" \
    "s_addc_u32 s47, s45, 0\n\t" \
    "s_add_u32 m0, s82, 45056\n\t" \
    "s_nop 0\n\t" \
    "global_load_lds_dwordx4 v200, s[46:47]\n\t" \
    "s_waitcnt vmcnt(28) lgkmcnt(0)\n\t" \
    "s_barrier\n\t" \
    "v_add_u32 v201, 0x10000, v201\n\t" \
    "v_add_u32 v202, 0x10000, v202\n\t" \
    "ds_read_b64_tr_b16 a[32:33], v201 offset:0\n\t" \
    "ds_read_b64_tr_b16 a[34:35], v201 offset:1024\n\t" \
    "ds_read_b64_tr_b16 a[36:37], v201 offset:8192\n\t" \
    "ds_read_b64_tr_b16 a[38:39], v201 offset:9216\n\t" \
    "ds_read_b64_tr_b16 a[104:105], v202 offset:0\n\t" \
    "ds_read_b64_tr_b16 a[106:107], v202 offset:1024\n\t" \
    "ds_read_b64_tr_b16 a[108:109], v202 offset:8192\n\t" \
    "ds_read_b64_tr_b16 a[110:111], v202 offset:9216\n\t" \
    "s_add_u32 s46, s44, 1572864\n\t" \
    "s_addc_u32 s47, s45, 0\n\t" \
    "s_add_u32 m0, s82, 49152\n\t" \
    "s_nop 0\n\t" \
    "global_load_lds_dwordx4 v200, s[46:47]\n\t" \
    "s_add_u32 s46, s44, 1703936\n\t" \
    "s_addc_u32 s47, s45, 0\n\t" \
    "s_add_u32 m0, s82, 53248\n\t" \
    "s_nop 0\n\t" \
    "global_load_lds_dwordx4 v200, s[46:47]\n\t" \
    "s_add_u32 s46, s44, 1835008\n\t" \
    "s_addc_u32 s47, s45, 0\n\t" \
    "s_add_u32 m0, s82, 57344\n\t" \
    "s_nop 0\n\t" \
    "global_load_lds_dwordx4 v200, s[46:47]\n\t" \
    "s_add_u32 s46, s44, 1966080\n\t" \
    "s_addc_u32 s47, s45, 0\n\t" \
    "s_add_u32 m0, s82, 61440\n\t" \
    "s_nop 0\n\t" \
    "global_load_lds_dwordx4 v200, s[46:47]\n\t" \
    "s_waitcnt vmcnt(28) lgkmcnt(0)\n\t" \
    "s_barrier\n\t" \
    "ds_read_b64_tr_b16 a[40:41], v201 offset:16384\n\t" \
    "ds_read_b64_tr_b16 a[42:43], v201 offset:17408\n\t" \
    "ds_read_b64_tr_b16 a[44:45], v201 offset:24576\n\t" \
    "ds_read_b64_tr_b16 a[46:47], v201 offset:25600\n\t" \
    "ds_read_b64_tr_b16 a[112:113], v202 offset:16384\n\t" \
    "ds_read_b64_tr_b16 a[114:115], v202 offset:17408\n\t" \
    "ds_read_b64_tr_b16 a[116:117], v202 offset:24576\n\t" \
    "ds_read_b64_tr_b16 a[118:119], v202 offset:25600\n\t" \
    "s_add_u32 s46, s44, 2097152\n\t" \
    "s_addc_u32 s47, s45, 0\n\t" \
    "s_add_u32 m0, s82, 65536\n\t" \
    "s_nop 0\n\t" \
    "global_load_lds_dwordx4 v200, s[46:47]\n\t" \
    "s_add_u32 s46, s44, 2228224\n\t" \
    "s_addc_u32 s47, s45, 0\n\t" \
    "s_add_u32 m0, s82, 69632\n\t" \
    "s_nop 0\n\t" \
    "global_load_lds_dwordx4 v200, s[46:47]\n\t" \
    "s_add_u32 s46, s44, 2359296\n\t" \
    "s_addc_u32 s47, s45, 0\n\t" \
    "s_add_u32 m0, s82, 73728\n\t" \
    "s_nop 0\n\t" \
    "global_load_lds_dwordx4 v200, s[46:47]\n\t" \
    "s_add_u32 s46, s44, 2490368\n\t" \
    "s_addc_u32 s47, s45, 0\n\t" \
    "s_add_u32 m0, s82, 77824\n\t" \
    "s_nop 0\n\t" \
    "global_load_lds_dwordx4 v200, s[46:47]\n\t" \
    "s_waitcnt vmcnt(28) lgkmcnt(0)\n\t" \
    "s_barrier\n\t" \
    "ds_read_b64_tr_b16 a[48:49], v201 offset:32768\n\t" \
    "ds_read_b64_tr_b16 a[50:51], v201 offset:33792\n\t" \
    "ds_read_b64_tr_b16 a[52:53], v201 offset:40960\n\t" \
    "ds_read_b64_tr_b16 a[54:55], v201 offset:41984\n\t" \
    "ds_read_b64_tr_b16 a[120:121], v202 offset:32768\n\t" \
    "ds_read_b64_tr_b16 a[122:123], v202 offset:33792\n\t" \
    "ds_read_b64_tr_b16 a[124:125], v202 offset:40960\n\t" \
    "ds_read_b64_tr_b16 a[126:127], v202 offset:41984\n\t" \
    "s_add_u32 s46, s44, 2621440\n\t" \
    "s_addc_u32 s47, s45, 0\n\t" \
    "s_add_u32 m0, s82, 81920\n\t" \
    "s_nop 0\n\t" \
    "global_load_lds_dwordx4 v200, s[46:47]\n\t" \
    "s_add_u32 s46, s44, 2752512\n\t" \
    "s_addc_u32 s47, s45, 0\n\t" \
    "s_add_u32 m0, s82, 86016\n\t" \
    "s_nop 0\n\t" \
    "global_load_lds_dwordx4 v200, s[46:47]\n\t" \
    "s_add_u32 s46, s44, 2883584\n\t" \
    "s_addc_u32 s47, s45, 0\n\t" \
    "s_add_u32 m0, s82, 90112\n\t" \
    "s_nop 0\n\t" \
    "global_load_lds_dwordx4 v200, s[46:47]\n\t" \
    "s_add_u32 s46, s44, 3014656\n\t" \
    "s_addc_u32 s47, s45, 0\n\t" \
    "s_add_u32 m0, s82, 94208\n\t" \
    "s_nop 0\n\t" \
    "global_load_lds_dwordx4 v200, s[46:47]\n\t" \
    "s_waitcnt vmcnt(28) lgkmcnt(0)\n\t" \
    "s_barrier\n\t" \
    "ds_read_b64_tr_b16 a[56:57], v201 offset:49152\n\t" \
    "ds_read_b64_tr_b16 a[58:59], v201 offset:50176\n\t" \
    "ds_read_b64_tr_b16 a[60:61], v201 offset:57344\n\t" \
    "ds_read_b64_tr_b16 a[62:63], v201 offset:58368\n\t" \
    "ds_read_b64_tr_b16 a[128:129], v202 offset:49152\n\t" \
    "ds_read_b64_tr_b16 a[130:131], v202 offset:50176\n\t" \
    "ds_read_b64_tr_b16 a[132:133], v202 offset:57344\n\t" \
    "ds_read_b64_tr_b16 a[134:135], v202 offset:58368\n\t" \
    "s_add_u32 s46, s44, 3145728\n\t" \
    "s_addc_u32 s47, s45, 0\n\t" \
    "s_add_u32 m0, s82, 98304\n\t" \
    "s_nop 0\n\t" \
    "global_load_lds_dwordx4 v200, s[46:47]\n\t" \
    "s_add_u32 s46, s44, 3276800\n\t" \
    "s_addc_u32 s47, s45, 0\n\t" \
    "s_add_u32 m0, s82, 102400\n\t" \
    "s_nop 0\n\t" \
    "global_load_lds_dwordx4 v200, s[46:47]\n\t" \
    "s_add_u32 s46, s44, 3407872\n\t" \
    "s_addc_u32 s47, s45, 0\n\t" \
    "s_add_u32 m0, s82, 106496\n\t" \
    "s_nop 0\n\t" \
    "global_load_lds_dwordx4 v200, s[46:47]\n\t" \
    "s_add_u32 s46, s44, 3538944\n\t" \
    "s_addc_u32 s47, s45, 0\n\t" \
    "s_add_u32 m0, s82, 110592\n\t" \
    "s_nop 0\n\t" \
    "global_load_lds_dwordx4 v200, s[46:47]\n\t" \
    "v_mbcnt_lo_u32_b32 v160, -1, 0\n\t" \
    "v_mbcnt_hi_u32_b32 v160, -1, v160\n\t" \
    "v_lshlrev_b32 v160, 6, v160\n\t" \
    "v_add_u32 v160, 6225920, v160\n\t" \
    "v_mov_b32 v161, 6234112\n\t" \
    "global_load_dword v161, v161, s[36:37] sc1\n\t" \
    "global_load_dwordx2 v[162:163], v160, s[36:37] sc1\n\t" \
    "global_load_dword v164, v160, s[36:37] offset:2048 sc1\n\t" \
    "s_waitcnt vmcnt(31) lgkmcnt(0)\n\t" \
    "s_barrier\n\t" \
    "v_add_u32 v201, 0x10000, v201\n\t" \
    "v_add_u32 v202, 0x10000, v202\n\t" \
    "ds_read_b64_tr_b16 a[64:65], v201 offset:0\n\t" \
    "ds_read_b64_tr_b16 a[66:67], v201 offset:1024\n\t" \
    "ds_read_b64_tr_b16 a[68:69], v201 offset:8192\n\t" \
    "ds_read_b64_tr_b16 a[70:71], v201 offset:9216\n\t" \
    "ds_read_b64_tr_b16 a[136:137], v202 offset:0\n\t" \
    "ds_read_b64_tr_b16 a[138:139], v202 offset:1024\n\t" \
    "ds_read_b64_tr_b16 a[140:141], v202 offset:8192\n\t" \
    "ds_read_b64_tr_b16 a[142:143], v202 offset:9216\n\t" \
    "v_add_u32 v201, 0xfffe0000, v201\n\t" \
    "v_add_u32 v202, 0xfffe0000, v202\n\t" \
    "s_add_u32 s46, s44, 3670016\n\t" \
    "s_addc_u32 s47, s45, 0\n\t" \
    "s_add_u32 m0, s82, 114688\n\t" \
    "s_nop 0\n\t" \
    "global_load_lds_dwordx4 v200, s[46:47]\n\t" \
    "s_add_u32 s46, s44, 3801088\n\t" \
    "s_addc_u32 s47, s45, 0\n\t" \
    "s_add_u32 m0, s82, 118784\n\t" \
    "s_nop 0\n\t" \
    "global_load_lds_dwordx4 v200, s[46:47]\n\t" \
    "s_add_u32 s46, s44, 3932160\n\t" \
    "s_addc_u32 s47, s45, 0\n\t" \
    "s_add_u32 m0, s82, 122880\n\t" \
    "s_nop 0\n\t" \
    "global_load_lds_dwordx4 v200, s[46:47]\n\t" \
    "s_add_u32 s46, s44, 4063232\n\t" \
    "s_addc_u32 s47, s45, 0\n\t" \
    "s_add_u32 m0, s82, 126976\n\t" \
    "s_nop 0\n\t" \
    "global_load_lds_dwordx4 v200, s[46:47]\n\t" \
    "s_waitcnt lgkmcnt(0)\n\t" \
    "s_barrier\n\t" \
    "s_add_u32 s46, s44, 4194304\n\t" \
    "s_addc_u32 s47, s45, 0\n\t" \
    "s_add_u32 m0, s82, 131072\n\t" \
    "s_nop 0\n\t" \
    "global_load_lds_dwordx4 v200, s[46:47]\n\t" \
    "s_add_u32 s46, s44, 4325376\n\t" \
    "s_addc_u32 s47, s45, 0\n\t" \
    "s_add_u32 m0, s82, 135168\n\t" \
    "s_nop 0\n\t" \
    "global_load_lds_dwordx4 v200, s[46:47]\n\t" \
    "s_add_u32 s46, s44, 4456448\n\t" \
    "s_addc_u32 s47, s45, 0\n\t" \
    "s_add_u32 m0, s82, 139264\n\t" \
    "s_nop 0\n\t" \
    "global_load_lds_dwordx4 v200, s[46:47]\n\t" \
    "s_add_u32 s46, s44, 4587520\n\t" \
    "s_addc_u32 s47, s45, 0\n\t" \
    "s_add_u32 m0, s82, 143360\n\t" \
    "s_nop 0\n\t" \
    "global_load_lds_dwordx4 v200, s[46:47]\n\t" \
    "s_waitcnt vmcnt(35) lgkmcnt(0)\n\t" \
    "s_barrier\n\t" \
    "ds_read_b64_tr_b16 a[144:145], v201 offset:0\n\t" \
    "ds_read_b64_tr_b16 a[146:147], v201 offset:1024\n\t" \
    "ds_read_b64_tr_b16 a[148:149], v201 offset:8192\n\t" \
    "ds_read_b64_tr_b16 a[150:151], v201 offset:9216\n\t" \
    "ds_read_b64_tr_b16 a[216:217], v202 offset:0\n\t" \
    "ds_read_b64_tr_b16 a[218:219], v202 offset:1024\n\t" \
    "ds_read_b64_tr_b16 a[220:221], v202 offset:8192\n\t" \
    "ds_read_b64_tr_b16 a[222:223], v202 offset:9216\n\t" \
    "s_waitcnt vmcnt(31) lgkmcnt(0)\n\t" \
    "s_barrier\n\t" \
    "ds_read_b64_tr_b16 a[152:153], v201 offset:16384\n\t" \
    "ds_read_b64_tr_b16 a[154:155], v201 offset:17408\n\t" \
    "ds_read_b64_tr_b16 a[156:157], v201 offset:24576\n\t" \
    "ds_read_b64_tr_b16 a[158:159], v201 offset:25600\n\t" \
    "ds_read_b64_tr_b16 a[224:225], v202 offset:16384\n\t" \
    "ds_read_b64_tr_b16 a[226:227], v202 offset:17408\n\t" \
    "ds_read_b64_tr_b16 a[228:229], v202 offset:24576\n\t" \
    "ds_read_b64_tr_b16 a[230:231], v202 offset:25600\n\t" \
    "s_waitcnt vmcnt(27) lgkmcnt(0)\n\t" \
    "s_barrier\n\t" \
    "ds_read_b64_tr_b16 a[160:161], v201 offset:32768\n\t" \
    "ds_read_b64_tr_b16 a[162:163], v201 offset:33792\n\t" \
    "ds_read_b64_tr_b16 a[164:165], v201 offset:40960\n\t" \
    "ds_read_b64_tr_b16 a[166:167], v201 offset:41984\n\t" \
    "ds_read_b64_tr_b16 a[232:233], v202 offset:32768\n\t" \
    "ds_read_b64_tr_b16 a[234:235], v202 offset:33792\n\t" \
    "ds_read_b64_tr_b16 a[236:237], v202 offset:40960\n\t" \
    "ds_read_b64_tr_b16 a[238:239], v202 offset:41984\n\t" \
    "s_waitcnt vmcnt(23) lgkmcnt(0)\n\t" \
    "s_barrier\n\t" \
    "ds_read_b64_tr_b16 a[168:169], v201 offset:49152\n\t" \
    "ds_read_b64_tr_b16 a[170:171], v201 offset:50176\n\t" \
    "ds_read_b64_tr_b16 a[172:173], v201 offset:57344\n\t" \
    "ds_read_b64_tr_b16 a[174:175], v201 offset:58368\n\t" \
    "ds_read_b64_tr_b16 a[240:241], v202 offset:49152\n\t" \
    "ds_read_b64_tr_b16 a[242:243], v202 offset:50176\n\t" \
    "ds_read_b64_tr_b16 a[244:245], v202 offset:57344\n\t" \
    "ds_read_b64_tr_b16 a[246:247], v202 offset:58368\n\t" \
    "s_waitcnt vmcnt(19) lgkmcnt(0)\n\t" \
    "s_barrier\n\t" \
    "v_add_u32 v201, 0x10000, v201\n\t" \
    "v_add_u32 v202, 0x10000, v202\n\t" \
    "ds_read_b64_tr_b16 a[176:177], v201 offset:0\n\t" \
    "ds_read_b64_tr_b16 a[178:179], v201 offset:1024\n\t" \
    "ds_read_b64_tr_b16 a[180:181], v201 offset:8192\n\t" \
    "ds_read_b64_tr_b16 a[182:183], v201 offset:9216\n\t" \
    "ds_read_b64_tr_b16 a[248:249], v202 offset:0\n\t" \
    "ds_read_b64_tr_b16 a[250:251], v202 offset:1024\n\t" \
    "ds_read_b64_tr_b16 a[252:253], v202 offset:8192\n\t" \
    "ds_read_b64_tr_b16 a[254:255], v202 offset:9216\n\t" \
    "s_waitcnt vmcnt(15) lgkmcnt(0)\n\t" \
    "s_barrier\n\t" \
    "ds_read_b64_tr_b16 a[184:185], v201 offset:16384\n\t" \
    "ds_read_b64_tr_b16 a[186:187], v201 offset:17408\n\t" \
    "ds_read_b64_tr_b16 a[188:189], v201 offset:24576\n\t" \
    "ds_read_b64_tr_b16 a[190:191], v201 offset:25600\n\t" \
    "ds_read_b64_tr_b16 v[224:225], v202 offset:16384\n\t" \
    "ds_read_b64_tr_b16 v[226:227], v202 offset:17408\n\t" \
    "ds_read_b64_tr_b16 v[228:229], v202 offset:24576\n\t" \
    "ds_read_b64_tr_b16 v[230:231], v202 offset:25600\n\t" \
    "s_waitcnt vmcnt(11) lgkmcnt(0)\n\t" \
    "s_barrier\n\t" \
    "ds_read_b64_tr_b16 a[192:193], v201 offset:32768\n\t" \
    "ds_read_b64_tr_b16 a[194:195], v201 offset:33792\n\t" \
    "ds_read_b64_tr_b16 a[196:197], v201 offset:40960\n\t" \
    "ds_read_b64_tr_b16 a[198:199], v201 offset:41984\n\t" \
    "ds_read_b64_tr_b16 v[232:233], v202 offset:32768\n\t" \
    "ds_read_b64_tr_b16 v[234:235], v202 offset:33792\n\t" \
    "ds_read_b64_tr_b16 v[236:237], v202 offset:40960\n\t" \
    "ds_read_b64_tr_b16 v[238:239], v202 offset:41984\n\t" \
    "s_waitcnt vmcnt(8)\n\t" \
    "qk_C2_%=:\n\t" \
    "v_cmp_ne_u32 s[54:55], v162, v164\n\t" \
    "v_sub_u32 v162, v162, v161\n\t" \
    "v_cmp_ne_u32 vcc, 1, v162\n\t" \
    "s_nop 1\n\t" \
    "s_or_b64 vcc, vcc, s[54:55]\n\t" \
    "s_cbranch_scc0 qk_Cok3_%=\n\t" \
    "s_cmp_eq_u32 s68, 0\n\t" \
    "s_cbranch_scc1 qk_F5_%=\n\t" \
    "s_sub_u32 s68, s68, 1\n\t" \
    "s_sleep 6\n\t" \
    "s_branch qk_Cag4_%=\n\t" \
    "qk_F5_%=:\n\t" \
    "v_mov_b32 v164, 5767424\n\t" \
    "v_mov_b32 v165, 4\n\t" \
    "global_store_dword v164, v165, s[36:37] sc1\n\t" \
    "s_waitcnt vmcnt(0)\n\t" \
    "s_branch qk_Cok3_%=\n\t" \
    "qk_Cag4_%=:\n\t" \
    "global_load_dwordx2 v[162:163], v160, s[36:37] sc1\n\t" \
    "global_load_dword v164, v160, s[36:37] offset:2048 sc1\n\t" \
    "s_waitcnt vmcnt(0)\n\t" \
    "s_branch qk_C2_%=\n\t" \
    "qk_Cok3_%=:\n\t" \
    "v_readlane_b32 s69, v163, 2\n\t" \
    "v_readlane_b32 s70, v163, 3\n\t" \
    "s_nop 1\n\t" \
    "s_cmp_lt_u32 s70, s69\n\t" \
    "s_cselect_b32 s69, 0, 8192\n\t" \
    "s_add_u32 s100, s36, s69\n\t" \
    "s_addc_u32 s101, s37, 0\n\t" \
    "s_cmp_lg_u32 s66, 0\n\t" \
    "s_cbranch_scc1 qk_Ca6_%=\n\t" \
    "v_mov_b32 v160, 5768192\n\t" \
    "s_mov_b64 exec, 1\n\t" \
    "global_atomic_add v160, v162, s[36:37] sc1\n\t" \
    "s_mov_b64 exec, -1\n\t" \
    "qk_Ca6_%=:\n\t" \
    "v_mov_b32_dpp v160, v163 quad_perm:[1,0,3,2] row_mask:0xf bank_mask:0xf\n\t" \
    "v_cmp_lt_u32 s[44:45], v160, v163\n\t" \
    "v_min_u32 v160, v160, v163\n\t" \
    "v_and_b32 v161, 1, v160\n\t" \
    "v_cmp_ne_u32 vcc, 0, v161\n\t" \
    "s_nop 1\n\t" \
    "s_and_b32 s44, s44, 0x55555555\n\t" \
    "s_and_b32 s69, vcc_lo, 0xaaaaaaaa\n\t" \
    "s_or_b32 s44, s44, s69\n\t" \
    "s_and_b32 s45, s45, 0x55555555\n\t" \
    "s_and_b32 s69, vcc_hi, 0xaaaaaaaa\n\t" \
    "s_or_b32 s45, s45, s69\n\t" \
    "s_bitcmp1_b32 s44, 0\n\t" \
    "s_cselect_b32 s62, 8192, 0\n\t" \
    "s_bitcmp1_b32 s44, 2\n\t" \
    "s_cselect_b32 s63, 8192, 0\n\t" \
    "s_or_b32 s63, s63, 0x200000\n\t" \
    "v_cmp_lt_u32 vcc, 1320, v160\n\t" \
    "s_nop 1\n\t" \
    "s_bcnt1_i32_b64 s69, vcc\n\t" \
    "s_cmp_gt_u32 s69, 16\n\t" \
    "s_cselect_b32 s69, 1, 0\n\t" \
    "s_mul_i32 s70, s69, 160\n\t" \
    "s_add_u32 s86, s86, s70\n\t" \
    "s_mul_i32 s70, s69, 536\n\t" \
    "s_add_u32 s87, s87, s70\n\t" \
    "s_mul_i32 s70, s69, 256\n\t" \
    "s_add_u32 s88, s88, s70\n\t" \
    "global_load_dword v25, v24, s[100:101] sc1\n\t" \
    "s_waitcnt vmcnt(5) lgkmcnt(0)\n\t" \
    "s_barrier\n\t" \
    "ds_read_b64_tr_b16 a[200:201], v201 offset:49152\n\t" \
    "ds_read_b64_tr_b16 a[202:203], v201 offset:50176\n\t" \
    "ds_read_b64_tr_b16 a[204:205], v201 offset:57344\n\t" \
    "ds_read_b64_tr_b16 a[206:207], v201 offset:58368\n\t" \
    "ds_read_b64_tr_b16 v[240:241], v202 offset:49152\n\t" \
    "ds_read_b64_tr_b16 v[242:243], v202 offset:50176\n\t" \
    "ds_read_b64_tr_b16 v[244:245], v202 offset:57344\n\t" \
    "ds_read_b64_tr_b16 v[246:247], v202 offset:58368\n\t" \
    "s_waitcnt vmcnt(1) lgkmcnt(0)\n\t" \
    "s_barrier\n\t" \
    "v_add_u32 v201, 0x10000, v201\n\t" \
    "v_add_u32 v202, 0x10000, v202\n\t" \
    "ds_read_b64_tr_b16 a[208:209], v201 offset:0\n\t" \
    "ds_read_b64_tr_b16 a[210:211], v201 offset:1024\n\t" \
    "ds_read_b64_tr_b16 a[212:213], v201 offset:8192\n\t" \
    "ds_read_b64_tr_b16 a[214:215], v201 offset:9216\n\t" \
    "ds_read_b64_tr_b16 v[248:249], v202 offset:0\n\t" \
    "ds_read_b64_tr_b16 v[250:251], v202 offset:1024\n\t" \
    "ds_read_b64_tr_b16 v[252:253], v202 offset:8192\n\t" \
    "ds_read_b64_tr_b16 v[254:255], v202 offset:9216\n\t" \
    "v_add_u32 v201, 0xfffe0000, v201\n\t" \
    "v_add_u32 v202, 0xfffe0000, v202\n\t" \
    "s_waitcnt lgkmcnt(0)\n\t" \
    "s_barrier\n\t" \
    "s_cmp_lg_u32 s66, 3\n\t" \
    "s_cbranch_scc1 qk_na7_%=\n\t" \
    "v_mov_b32 v172, 5769216\n\t" \
    "v_mov_b32 v173, 1\n\t" \
    "s_mov_b64 exec, 1\n\t" \
    "global_atomic_add v174, v172, v173, s[36:37] sc0 sc1\n\t" \
    "s_mov_b64 exec, -1\n\t" \
    "v_mbcnt_lo_u32_b32 v172, -1, 0\n\t" \
    "v_and_b32 v173, 1, v172\n\t" \
    "v_lshlrev_b32 v173, 14, v173\n\t" \
    "v_bfe_u32 v175, v172, 1, 1\n\t" \
    "v_lshl_add_u32 v173, v175, 17, v173\n\t" \
    "v_bfe_u32 v175, v172, 2, 2\n\t" \
    "v_lshl_add_u32 v173, v175, 11, v173\n\t" \
    "v_bfe_u32 v175, v172, 4, 1\n\t" \
    "v_lshl_add_u32 v173, v175, 15, v173\n\t" \
    "v_add_u32 v173, s63, v173\n\t" \
    "s_waitcnt vmcnt(0)\n\t" \
    "v_readfirstlane_b32 s69, v174\n\t" \
    "s_nop 1\n\t" \
    "s_cmp_lg_u32 s69, 126\n\t" \
    "s_cbranch_scc1 qk_go8_%=\n\t" \
    "s_memrealtime s[56:57]\n\t" \
    "s_waitcnt lgkmcnt(0)\n\t" \
    "s_lshl_b32 s69, s56, 4\n\t" \
    "s_add_u32 s69, s69, 4160\n\t" \
    "s_add_u32 s70, s56, 24\n\t" \
    "s_lshr_b32 s70, s70, 4\n\t" \
    "v_lshrrev_b32 v175, 2, v172\n\t" \
    "v_add_u32 v175, s70, v175\n\t" \
    "v_and_b32 v175, 63, v175\n\t" \
    "v_lshlrev_b32 v175, 6, v175\n\t" \
    "v_and_b32 v174, 3, v172\n\t" \
    "v_lshl_add_u32 v175, v174, 12, v175\n\t" \
    "v_add_u32 v175, 5783552, v175\n\t" \
    "v_mov_b32 v172, s69\n\t" \
    "s_mov_b32 exec_hi, 0\n\t" \
    "global_store_dword v175, v172, s[36:37] sc1\n\t" \
    "global_store_dword v173, v172, s[36:37] sc1\n\t" \
    "s_mov_b32 exec_hi, -1\n\t" \
    "qk_go8_%=:\n\t" \
    "qk_na7_%=:\n\t" \
    "s_mul_i32 s70, s67, 7\n\t" \
    "s_add_u32 s69, s65, 0\n\t" \
    "s_xor_b32 s69, s69, s70\n\t" \
    "s_and_b32 s69, s69, 63\n\t" \
    "s_lshl_b32 s69, s69, 13\n\t" \
    "s_mul_i32 s70, s67, 524288\n\t" \
    "s_add_u32 s69, s69, s70\n\t" \
    "s_add_u32 s69, s69, 23068672\n\t" \
    "s_add_u32 s38, s36, s69\n\t" \
    "s_addc_u32 s39, s37, 0\n\t" \
    "v_mov_b32 v26, 0x3db504f3\n\t" \
    "v_mov_b32 v28, 0x3fb8aa3b\n\t" \
    "v_mov_b32 v27, 0x3db504f3\n\t" \
    "v_mov_b32 v29, 0x3fb8aa3b\n\t" \
    "s_bitcmp0_b32 s44, 1\n\t" \
    "s_cselect_b32 s69, 16384, 0\n\t" \
    "v_xor_b32 v1, s69, v1\n\t" \
    "v_xor_b32 v2, s69, v2\n\t" \
    "v_xor_b32 v23, s69, v23\n\t" \
    "v_xor_b32 v18, s69, v18\n\t" \
    "s_bitcmp1_b32 s44, 3\n\t" \
    "s_cselect_b32 s69, 16384, 0\n\t" \
    "v_xor_b32 v20, s69, v20\n\t" \
    "v_xor_b32 v180, 16384, v18\n\t" \
    "v_xor_b32 v181, 131072, v18\n\t" \
    "v_xor_b32 v182, 147456, v18\n\t" \
    "s_mov_b32 s48, 0xaaaaaaaa\n\t" \
    "s_mov_b32 s49, 0xaaaaaaaa\n\t" \
    "s_mov_b32 s58, 0x00ff00ff\n\t" \
    "s_mov_b32 s59, 0x00ff00ff\n\t" \
    "v_mov_b32 v31, -1\n\t" \
    "s_mov_b32 s91, -1\n\t" \
    "ds_write_b32 v12, v31 offset:0\n\t" \
    "ds_write_b32 v12, v31 offset:64\n\t" \
    "s_waitcnt vmcnt(0)\n\t" \
    "s_cmp_gt_u32 s66, 1\n\t" \
    "s_cbranch_scc1 qk_nk9_%=\n\t" \
    "v_readfirstlane_b32 s83, v25\n\t" \
    "s_nop 1\n\t" \
    "qk_WF10_%=:\n\t" \
    "s_cmp_ge_i32 s83, s65\n\t" \
    "s_cbranch_scc1 qk_WFok11_%=\n\t" \
    "global_load_dword v160, v24, s[100:101] sc1\n\t" \
    "s_waitcnt vmcnt(0)\n\t" \
    "v_readfirstlane_b32 s83, v160\n\t" \
    "s_nop 1\n\t" \
    "s_cmp_ge_i32 s83, s65\n\t" \
    "s_cbranch_scc1 qk_WFok11_%=\n\t" \
    "s_cmp_eq_u32 s68, 0\n\t" \
    "s_cbranch_scc1 qk_F12_%=\n\t" \
    "s_sub_u32 s68, s68, 1\n\t" \
    "s_sleep 2\n\t" \
    "s_branch qk_WF10_%=\n\t" \
    "qk_F12_%=:\n\t" \
    "v_mov_b32 v160, 5767424\n\t" \
    "v_mov_b32 v161, 8\n\t" \
    "global_store_dword v160, v161, s[36:37] sc1\n\t" \
    "s_waitcnt vmcnt(0)\n\t" \
    "s_branch qk_WFok11_%=\n\t" \
    "qk_WFok11_%=:\n\t" \
    "global_load_dwordx4 v[100:103], v0, s[38:39] offset:0 sc1\n\t" \
    "global_load_dwordx4 v[104:107], v0, s[38:39] offset:2048 sc1\n\t" \
    "global_load_dwordx4 v[108:111], v15, s[38:39] offset:0 sc1\n\t" \
    "global_load_dwordx4 v[112:115], v15, s[38:39] offset:2048 sc1\n\t" \
    "global_load_dwordx4 v[116:119], v0, s[38:39] offset:1024 sc1\n\t" \
    "global_load_dwordx4 v[120:123], v0, s[38:39] offset:3072 sc1\n\t" \
    "global_load_dwordx4 v[124:127], v15, s[38:39] offset:1024 sc1\n\t" \
    "global_load_dwordx4 v[128:131], v15, s[38:39] offset:3072 sc1\n\t" \
    "qk_nk9_%=:\n\t" \
    "v_mov_b32 v33, v21\n\t" \
    "v_readlane_b32 s69, v21, 63\n\t" \
    "s_nop 1\n\t" \
    "v_mov_b32 v21, s69\n\t" \
    "s_mov_b32 s79, s74\n\t" \
    "s_mov_b32 s80, s74\n\t" \
    "s_cmp_eq_u32 s66, 1\n\t" \
    "s_cbranch_scc1 qk_loop14_%=\n\t" \
    "s_cmp_eq_u32 s66, 2\n\t" \
    "s_cbranch_scc1 qk_loop15_%=\n\t" \
    "s_cmp_eq_u32 s66, 3\n\t" \
    "s_cbranch_scc1 qk_loop16_%=\n\t" \
    "qk_loop13_%=:\n\t" \
    "v_mov_b32 v72, 0\n\t" \
    "v_mov_b32 v73, 0\n\t" \
    "v_mov_b32 v74, 0\n\t" \
    "v_mov_b32 v75, 0\n\t" \
    "v_mov_b32 v76, 0\n\t" \
    "v_mov_b32 v77, 0\n\t" \
    "v_mov_b32 v78, 0\n\t" \
    "v_mov_b32 v79, 0\n\t" \
    "v_mov_b32 v80, 0\n\t" \
    "v_mov_b32 v81, 0\n\t" \
    "v_mov_b32 v82, 0\n\t" \
    "v_mov_b32 v83, 0\n\t" \
    "v_mov_b32 v84, 0\n\t" \
    "v_mov_b32 v85, 0\n\t" \
    "v_mov_b32 v86, 0\n\t" \
    "v_mov_b32 v87, 0\n\t" \
    "v_mov_b32 v88, 0\n\t" \
    "v_mov_b32 v89, 0\n\t" \
    "v_mov_b32 v90, 0\n\t" \
    "v_mov_b32 v91, 0\n\t" \
    "v_mov_b32 v92, 0\n\t" \
    "v_mov_b32 v93, 0\n\t" \
    "v_mov_b32 v94, 0\n\t" \
    "v_mov_b32 v95, 0\n\t" \
    "v_mov_b32 v96, 0\n\t" \
    "v_mov_b32 v97, 0\n\t" \
    "v_mov_b32 v98, 0\n\t" \
    "v_mov_b32 v99, 0\n\t" \
    "s_add_u32 s69, s65, 0\n\t" \
    "s_and_b32 s69, s69, 7\n\t" \
    "s_lshl_b32 s69, s69, 18\n\t" \
    "s_add_u32 s69, s69, s62\n\t" \
    "s_add_u32 s46, s36, s69\n\t" \
    "s_addc_u32 s47, s37, 0\n\t" \
    "s_bitcmp0_b32 s65, 0\n\t" \
    "s_cselect_b32 s69, 16384, 0\n\t" \
    "v_xor_b32 v1, s69, v1\n\t" \
    "v_xor_b32 v2, s69, v2\n\t" \
    "v_xor_b32 v23, s69, v23\n\t" \
    "v_xor_b32 v18, s69, v18\n\t" \
    "v_xor_b32 v180, s69, v180\n\t" \
    "v_xor_b32 v181, s69, v181\n\t" \
    "v_xor_b32 v182, s69, v182\n\t" \
    "s_sub_u32 s70, s79, s74\n\t" \
    "s_max_i32 s70, s70, 0\n\t" \
    "s_min_i32 s70, s70, 16000\n\t" \
    "s_lshl_b32 s70, s70, 1\n\t" \
    "s_mul_hi_u32 s70, s70, s77\n\t" \
    "s_add_u32 s70, s70, s73\n\t" \
    ".p2align 6\n\t" \
    "s_nop 0\n\t" \
    "s_nop 0\n\t" \
    "s_nop 0\n\t" \
    "s_nop 0\n\t" \
    "s_nop 0\n\t" \
    "s_nop 0\n\t" \
    "qk_go18_%=:\n\t" \
    "s_getpc_b64 s[30:31]\n\t" \
    "qk_Wp20_%=:\n\t" \
    "s_add_u32 s30, s30, qk_We21_%=-qk_Wp20_%=\n\t" \
    "s_addc_u32 s31, s31, 0\n\t" \
    "s_sub_u32 s70, s70, 96\n\t" \
    "qk_W19_%=:\n\t" \
    "s_memtime s[56:57]\n\t" \
    "s_waitcnt lgkmcnt(0)\n\t" \
    "s_sub_u32 s69, s70, s56\n\t" \
    "s_cmp_gt_i32 s69, 80\n\t" \
    "s_cbranch_scc1 qk_W19_%=\n\t" \
    "s_max_i32 s69, s69, 0\n\t" \
    "s_and_b32 s69, s69, -4\n\t" \
    "s_sub_u32 s30, s30, s69\n\t" \
    "s_subb_u32 s31, s31, 0\n\t" \
    "s_setpc_b64 s[30:31]\n\t" \
    "s_nop 0\n\t" \
    "s_nop 0\n\t" \
    "s_nop 0\n\t" \
    "s_nop 0\n\t" \
    "s_nop 0\n\t" \
    "s_nop 0\n\t" \
    "s_nop 0\n\t" \
    "s_nop 0\n\t" \
    "s_nop 0\n\t" \
    "s_nop 0\n\t" \
    "s_nop 0\n\t" \
    "s_nop 0\n\t" \
    "s_nop 0\n\t" \
    "s_nop 0\n\t" \
    "s_nop 0\n\t" \
    "s_nop 0\n\t" \
    "s_nop 0\n\t" \
    "s_nop 0\n\t" \
    "s_nop 0\n\t" \
    "s_nop 0\n\t" \
    "qk_We21_%=:\n\t" \
    "qk_G22_%=:\n\t" \
    "global_load_dwordx4 v[36:39], v1, s[46:47] sc1\n\t" \
    "global_load_dword v161, v2, s[46:47] sc1\n\t" \
    "global_load_dwordx4 v[40:43], v23, s[46:47] sc1\n\t" \
    "s_waitcnt vmcnt(2)\n\t" \
    "qk_Garr27_%=:\n\t" \
    "ds_write_b128 v3, v[36:39]\n\t" \
    "v_smfmac_f32_16x16x64_bf16 v[72:75], v[36:39], a[0:7], v10\n\t" \
    "s_waitcnt vmcnt(1)\n\t" \
    "ds_write_b32 v22, v161 offset:19456\n\t" \
    "v_smfmac_f32_16x16x64_bf16 v[76:79], v[36:39], a[72:79], v10\n\t" \
    "s_waitcnt vmcnt(0)\n\t" \
    "ds_write_b128 v3, v[40:43] offset:1024\n\t" \
    "v_max3_u32 v160, v36, v40, v161\n\t" \
    "v_cmp_eq_u32 vcc, -1, v160\n\t" \
    "v_smfmac_f32_16x16x64_bf16 v[80:83], v[36:39], a[144:151], v10\n\t" \
    "v_smfmac_f32_16x16x64_bf16 v[84:87], v[36:39], a[216:223], v10\n\t" \
    "v_smfmac_f32_16x16x64_bf16 v[72:75], v[40:43], a[8:15], v10\n\t" \
    "s_cbranch_vccnz qk_Gbad23_%=\n\t" \
    "qk_Gc24_%=:\n\t" \
    "s_waitcnt lgkmcnt(0)\n\t" \
    "s_barrier\n\t" \
    "ds_read_b128 v[48:51], v4\n\t" \
    "ds_read_b128 v[52:55], v5\n\t" \
    "v_smfmac_f32_16x16x64_bf16 v[76:79], v[40:43], a[80:87], v10\n\t" \
    "ds_read_b128 v[56:59], v6\n\t" \
    "ds_read_b128 v[60:63], v7\n\t" \
    "v_smfmac_f32_16x16x64_bf16 v[80:83], v[40:43], a[152:159], v10\n\t" \
    "ds_read_b128 v[64:67], v8\n\t" \
    "v_smfmac_f32_16x16x64_bf16 v[84:87], v[40:43], a[224:231], v10\n\t" \
    "ds_read_b128 v[68:71], v9\n\t" \
    "s_waitcnt lgkmcnt(5)\n\t" \
    "v_smfmac_f32_16x16x64_bf16 v[72:75], v[48:51], a[16:23], v10\n\t" \
    "ds_read_b128 v[44:47], v0 offset:19456\n\t" \
    "s_waitcnt lgkmcnt(5)\n\t" \
    "v_smfmac_f32_16x16x64_bf16 v[72:75], v[52:55], a[24:31], v10\n\t" \
    "s_waitcnt lgkmcnt(4)\n\t" \
    "v_smfmac_f32_16x16x64_bf16 v[72:75], v[56:59], a[32:39], v10\n\t" \
    "s_waitcnt lgkmcnt(3)\n\t" \
    "v_smfmac_f32_16x16x64_bf16 v[72:75], v[60:63], a[40:47], v10\n\t" \
    "s_waitcnt lgkmcnt(2)\n\t" \
    "v_smfmac_f32_16x16x64_bf16 v[72:75], v[64:67], a[48:55], v10\n\t" \
    "s_waitcnt lgkmcnt(1)\n\t" \
    "v_smfmac_f32_16x16x64_bf16 v[72:75], v[68:71], a[56:63], v10\n\t" \
    "s_waitcnt lgkmcnt(0)\n\t" \
    "v_smfmac_f32_16x16x64_bf16 v[72:75], v[44:47], a[64:71], v10\n\t" \
    "s_nop 2\n\t" \
    "v_smfmac_f32_16x16x64_bf16 v[76:79], v[48:51], a[88:95], v10\n\t" \
    "s_nop 2\n\t" \
    "v_smfmac_f32_16x16x64_bf16 v[76:79], v[52:55], a[96:103], v10\n\t" \
    "v_add_f32 v72, v72, v74\n\t" \
    "v_smfmac_f32_16x16x64_bf16 v[76:79], v[56:59], a[104:111], v10\n\t" \
    "v_add_f32 v73, v73, v75\n\t" \
    "v_smfmac_f32_16x16x64_bf16 v[76:79], v[60:63], a[112:119], v10\n\t" \
    "v_cvt_pk_bf16_f32 v156, v72, v73\n\t" \
    "v_smfmac_f32_16x16x64_bf16 v[76:79], v[64:67], a[120:127], v10\n\t" \
    "ds_write_b32 v12, v156 offset:0\n\t" \
    "v_smfmac_f32_16x16x64_bf16 v[76:79], v[68:71], a[128:135], v10\n\t" \
    "v_smfmac_f32_16x16x64_bf16 v[76:79], v[44:47], a[136:143], v10\n\t" \
    "s_nop 2\n\t" \
    "v_smfmac_f32_16x16x64_bf16 v[80:83], v[48:51], a[160:167], v10\n\t" \
    "s_nop 2\n\t" \
    "v_smfmac_f32_16x16x64_bf16 v[80:83], v[52:55], a[168:175], v10\n\t" \
    "v_add_f32 v76, v76, v78\n\t" \
    "v_smfmac_f32_16x16x64_bf16 v[80:83], v[56:59], a[176:183], v10\n\t" \
    "v_add_f32 v77, v77, v79\n\t" \
    "v_smfmac_f32_16x16x64_bf16 v[80:83], v[60:63], a[184:191], v10\n\t" \
    "v_cvt_pk_bf16_f32 v157, v76, v77\n\t" \
    "v_smfmac_f32_16x16x64_bf16 v[80:83], v[64:67], a[192:199], v10\n\t" \
    "ds_write_b32 v12, v157 offset:64\n\t" \
    "v_smfmac_f32_16x16x64_bf16 v[80:83], v[68:71], a[200:207], v10\n\t" \
    "v_smfmac_f32_16x16x64_bf16 v[80:83], v[44:47], a[208:215], v10\n\t" \
    "s_nop 2\n\t" \
    "v_smfmac_f32_16x16x64_bf16 v[84:87], v[48:51], a[232:239], v10\n\t" \
    "s_nop 2\n\t" \
    "v_smfmac_f32_16x16x64_bf16 v[84:87], v[52:55], a[240:247], v10\n\t" \
    "v_add_f32 v80, v80, v82\n\t" \
    "v_smfmac_f32_16x16x64_bf16 v[84:87], v[56:59], a[248:255], v10\n\t" \
    "ds_read_b64_tr_b16 v[132:133], v13 offset:0\n\t" \
    "v_add_f32 v81, v81, v83\n\t" \
    "v_smfmac_f32_16x16x64_bf16 v[84:87], v[60:63], v[224:231], v10\n\t" \
    "ds_read_b64_tr_b16 v[134:135], v13 offset:128\n\t" \
    "v_cvt_pk_bf16_f32 v158, v80, v81\n\t" \
    "v_smfmac_f32_16x16x64_bf16 v[84:87], v[64:67], v[232:239], v10\n\t" \
    "ds_write_b32 v16, v158 offset:0\n\t" \
    "v_smfmac_f32_16x16x64_bf16 v[84:87], v[68:71], v[240:247], v10\n\t" \
    "ds_read_b64_tr_b16 v[136:137], v13 offset:1024\n\t" \
    "ds_read_b64_tr_b16 v[138:139], v13 offset:1152\n\t" \
    "v_smfmac_f32_16x16x64_bf16 v[84:87], v[44:47], v[248:255], v10\n\t" \
    "s_waitcnt lgkmcnt(3)\n\t" \
    "v_smfmac_f32_16x16x64_bf16 v[88:91], v[132:135], v[100:107], v10\n\t" \
    "s_nop 4\n\t" \
    "v_cmp_u_f32 vcc, v72, v73\n\t" \
    "v_pk_add_f32 v[84:85], v[84:85], v[86:87]\n\t" \
    "v_cvt_pk_bf16_f32 v159, v84, v85\n\t" \
    "ds_write_b32 v16, v159 offset:256\n\t" \
    "v_smfmac_f32_16x16x64_bf16 v[92:95], v[132:135], v[116:123], v10\n\t" \
    "s_waitcnt lgkmcnt(1)\n\t" \
    "v_smfmac_f32_16x16x64_bf16 v[88:91], v[136:139], v[108:115], v10\n\t" \
    "s_cbranch_vccnz qk_Gslow25_%=\n\t" \
    "qk_Gback26_%=:\n\t" \
    "v_smfmac_f32_16x16x64_bf16 v[92:95], v[136:139], v[124:131], v10\n\t" \
    "s_waitcnt lgkmcnt(0)\n\t" \
    "s_barrier\n\t" \
    "ds_read_b64_tr_b16 v[140:141], v17 offset:0\n\t" \
    "ds_read_b64_tr_b16 v[142:143], v17 offset:256\n\t" \
    "ds_read_b64_tr_b16 v[144:145], v17 offset:512\n\t" \
    "ds_read_b64_tr_b16 v[146:147], v17 offset:768\n\t" \
    "ds_read_b64_tr_b16 v[148:149], v17 offset:1024\n\t" \
    "ds_read_b64_tr_b16 v[150:151], v17 offset:1280\n\t" \
    "ds_read_b64_tr_b16 v[152:153], v17 offset:1536\n\t" \
    "ds_read_b64_tr_b16 v[154:155], v17 offset:1792\n\t" \
    "qk_qb29_%=:\n\t" \
    "v_add_f32 v168, v88, v90\n\t" \
    "v_add_f32 v169, v92, v94\n\t" \
    "v_cmp_u_f32 vcc, v168, v169\n\t" \
    "v_cvt_pk_bf16_f32 v168, 0, v168\n\t" \
    "v_cvt_pk_bf16_f32 v169, 0, v169\n\t" \
    "s_waitcnt lgkmcnt(4)\n\t" \
    "v_smfmac_f32_16x16x64_bf16 v[96:99], v[132:135], v[140:147], v10\n\t" \
    "s_waitcnt lgkmcnt(0)\n\t" \
    "v_smfmac_f32_16x16x64_bf16 v[96:99], v[136:139], v[148:155], v10\n\t" \
    "s_cbranch_vccnz qk_qs28_%=\n\t" \
    "qk_qb230_%=:\n\t" \
    "v_mul_f32 v168, v168, v26\n\t" \
    "v_mul_f32 v169, v169, v26\n\t" \
    "v_cvt_pk_bf16_f32 v168, 0, v168\n\t" \
    "v_cvt_pk_bf16_f32 v169, 0, v169\n\t" \
    "v_max_f32 v178, v168, v169\n\t" \
    "v_mul_f32 v168, v168, v28\n\t" \
    "v_mul_f32 v169, v169, v28\n\t" \
    "v_max_f32_dpp v178, v178, v178 row_mirror row_mask:0xf bank_mask:0xf\n\t" \
    "v_add_f32 v172, v96, v98\n\t" \
    "v_cvt_pk_bf16_f32 v172, 0, v172\n\t" \
    "v_mul_f32 v172, v172, v26\n\t" \
    "v_cvt_pk_bf16_f32 v172, 0, v172\n\t" \
    "v_max_f32 v173, v178, v172\n\t" \
    "v_mul_f32 v172, v172, v28\n\t" \
    "s_nop 0\n\t" \
    "v_max_f32_dpp v173, v173, v173 quad_perm:[1,0,3,2] row_mask:0xf bank_mask:0xf\n\t" \
    "s_nop 1\n\t" \
    "v_max_f32_dpp v173, v173, v173 quad_perm:[2,3,0,1] row_mask:0xf bank_mask:0xf\n\t" \
    "s_nop 1\n\t" \
    "v_max_f32_dpp v173, v173, v173 row_half_mirror row_mask:0xf bank_mask:0xf\n\t" \
    "v_fma_f32 v172, -v173, v28, v172\n\t" \
    "v_fma_f32 v168, -v173, v28, v168\n\t" \
    "v_fma_f32 v169, -v173, v28, v169\n\t" \
    "v_exp_f32 v172, v172\n\t" \
    "v_exp_f32 v168, v168\n\t" \
    "v_exp_f32 v169, v169\n\t" \
    "v_fma_f32 v173, v172, 0.5, v168\n\t" \
    "v_add_f32 v173, v173, v169\n\t" \
    "v_mov_b32_dpp v174, v172 quad_perm:[0,0,2,2] row_mask:0xf bank_mask:0xf\n\t" \
    "v_mov_b32_dpp v175, v172 quad_perm:[1,1,3,3] row_mask:0xf bank_mask:0xf\n\t" \
    "v_add_f32_dpp v173, v173, v173 quad_perm:[1,0,3,2] row_mask:0xf bank_mask:0xf\n\t" \
    "v_mov_b32_dpp v176, v168 quad_perm:[1,0,3,2] row_mask:0xf bank_mask:0xf\n\t" \
    "v_mov_b32_dpp v177, v169 quad_perm:[1,0,3,2] row_mask:0xf bank_mask:0xf\n\t" \
    "v_add_f32_dpp v173, v173, v173 quad_perm:[2,3,0,1] row_mask:0xf bank_mask:0xf\n\t" \
    "v_cndmask_b32 v168, v168, v177, s[48:49]\n\t" \
    "v_cndmask_b32 v169, v176, v169, s[48:49]\n\t" \
    "v_add_f32_dpp v173, v173, v173 row_half_mirror row_mask:0xf bank_mask:0xf\n\t" \
    "s_nop 1\n\t" \
    "v_add_f32_dpp v173, v173, v173 row_mirror row_mask:0xf bank_mask:0xf\n\t" \
    "v_rcp_f32 v173, v173\n\t" \
    "s_nop 0\n\t" \
    "v_pk_mul_f32 v[168:169], v[168:169], v[172:173] op_sel:[0,1] op_sel_hi:[1,1]\n\t" \
    "v_cvt_pk_bf16_f32 v176, v168, v169\n\t" \
    "global_store_dword v18, v176, s[46:47] sc1\n\t" \
    "global_store_dword v180, v176, s[46:47] sc1\n\t" \
    "global_store_dword v181, v176, s[46:47] sc1\n\t" \
    "global_store_dword v182, v176, s[46:47] sc1\n\t" \
    "v_pk_mul_f32 v[174:175], v[174:175], v[172:173] op_sel:[0,1] op_sel_hi:[1,1]\n\t" \
    "v_cvt_pk_bf16_f32 v177, v174, v175\n\t" \
    "global_store_dword v19, v177, s[46:47] sc1\n\t" \
    "s_cmp_eq_u32 s65, 63\n\t" \
    "s_cbranch_scc1 qk_last17_%=\n\t" \
    "v_readfirstlane_b32 s69, v25\n\t" \
    "s_add_u32 s65, s65, 1\n\t" \
    "s_max_i32 s83, s83, s69\n\t" \
    "s_cmp_gt_u32 s65, 7\n\t" \
    "s_cselect_b32 s83, 99, s83\n\t" \
    "v_mov_b32 v72, 0\n\t" \
    "v_mov_b32 v73, 0\n\t" \
    "v_mov_b32 v74, 0\n\t" \
    "v_mov_b32 v75, 0\n\t" \
    "v_mov_b32 v76, 0\n\t" \
    "v_mov_b32 v77, 0\n\t" \
    "v_mov_b32 v78, 0\n\t" \
    "v_mov_b32 v79, 0\n\t" \
    "v_mov_b32 v80, 0\n\t" \
    "v_mov_b32 v81, 0\n\t" \
    "v_mov_b32 v82, 0\n\t" \
    "v_mov_b32 v83, 0\n\t" \
    "v_mov_b32 v84, 0\n\t" \
    "v_mov_b32 v85, 0\n\t" \
    "v_mov_b32 v86, 0\n\t" \
    "v_mov_b32 v87, 0\n\t" \
    "v_mov_b32 v88, 0\n\t" \
    "v_mov_b32 v89, 0\n\t" \
    "v_mov_b32 v90, 0\n\t" \
    "v_mov_b32 v91, 0\n\t" \
    "v_mov_b32 v92, 0\n\t" \
    "v_mov_b32 v93, 0\n\t" \
    "v_mov_b32 v94, 0\n\t" \
    "v_mov_b32 v95, 0\n\t" \
    "v_mov_b32 v96, 0\n\t" \
    "v_mov_b32 v97, 0\n\t" \
    "v_mov_b32 v98, 0\n\t" \
    "v_mov_b32 v99, 0\n\t" \
    "s_add_u32 s69, s65, 0\n\t" \
    "s_and_b32 s69, s69, 7\n\t" \
    "s_lshl_b32 s69, s69, 18\n\t" \
    "s_add_u32 s69, s69, s62\n\t" \
    "s_add_u32 s46, s36, s69\n\t" \
    "s_addc_u32 s47, s37, 0\n\t" \
    "s_bitcmp0_b32 s65, 0\n\t" \
    "s_cselect_b32 s69, 16384, 0\n\t" \
    "v_xor_b32 v1, s69, v1\n\t" \
    "v_xor_b32 v2, s69, v2\n\t" \
    "v_xor_b32 v23, s69, v23\n\t" \
    "v_xor_b32 v18, s69, v18\n\t" \
    "v_xor_b32 v180, s69, v180\n\t" \
    "v_xor_b32 v181, s69, v181\n\t" \
    "v_xor_b32 v182, s69, v182\n\t" \
    "s_mul_i32 s70, s67, 7\n\t" \
    "s_add_u32 s69, s65, 0\n\t" \
    "s_xor_b32 s69, s69, s70\n\t" \
    "s_and_b32 s69, s69, 63\n\t" \
    "s_lshl_b32 s69, s69, 13\n\t" \
    "s_mul_i32 s70, s67, 524288\n\t" \
    "s_add_u32 s69, s69, s70\n\t" \
    "s_add_u32 s69, s69, 23068672\n\t" \
    "s_add_u32 s38, s36, s69\n\t" \
    "s_addc_u32 s39, s37, 0\n\t" \
    "qk_WF31_%=:\n\t" \
    "s_cmp_ge_i32 s83, s65\n\t" \
    "s_cbranch_scc1 qk_WFok32_%=\n\t" \
    "global_load_dword v160, v24, s[100:101] sc1\n\t" \
    "s_waitcnt vmcnt(0)\n\t" \
    "v_readfirstlane_b32 s83, v160\n\t" \
    "s_nop 1\n\t" \
    "s_cmp_ge_i32 s83, s65\n\t" \
    "s_cbranch_scc1 qk_WFok32_%=\n\t" \
    "s_cmp_eq_u32 s68, 0\n\t" \
    "s_cbranch_scc1 qk_F33_%=\n\t" \
    "s_sub_u32 s68, s68, 1\n\t" \
    "s_sleep 2\n\t" \
    "s_branch qk_WF31_%=\n\t" \
    "qk_F33_%=:\n\t" \
    "v_mov_b32 v160, 5767424\n\t" \
    "v_mov_b32 v161, 9\n\t" \
    "global_store_dword v160, v161, s[36:37] sc1\n\t" \
    "s_waitcnt vmcnt(0)\n\t" \
    "s_branch qk_WFok32_%=\n\t" \
    "qk_WFok32_%=:\n\t" \
    "global_load_dwordx4 v[100:103], v0, s[38:39] offset:0 sc1\n\t" \
    "global_load_dwordx4 v[104:107], v0, s[38:39] offset:2048 sc1\n\t" \
    "global_load_dwordx4 v[108:111], v15, s[38:39] offset:0 sc1\n\t" \
    "global_load_dwordx4 v[112:115], v15, s[38:39] offset:2048 sc1\n\t" \
    "global_load_dwordx4 v[116:119], v0, s[38:39] offset:1024 sc1\n\t" \
    "global_load_dwordx4 v[120:123], v0, s[38:39] offset:3072 sc1\n\t" \
    "global_load_dwordx4 v[124:127], v15, s[38:39] offset:1024 sc1\n\t" \
    "global_load_dwordx4 v[128:131], v15, s[38:39] offset:3072 sc1\n\t" \
    "s_cmp_gt_i32 s83, s65\n\t" \
    "s_cbranch_scc1 qk_nf34_%=\n\t" \
    "global_load_dword v25, v24, s[100:101] sc1\n\t" \
    "qk_nf34_%=:\n\t" \
    "s_barrier\n\t" \
    "ds_read_b32 v168, v11 offset:20480\n\t" \
    "s_waitcnt lgkmcnt(0)\n\t" \
    "v_readfirstlane_b32 s70, v168\n\t" \
    "ds_write_b32 v12, v31 offset:0\n\t" \
    "ds_write_b32 v12, v31 offset:64\n\t" \
    "s_branch qk_go18_%=\n\t" \
    "qk_Gbad23_%=:\n\t" \
    "s_waitcnt vmcnt(0) lgkmcnt(0)\n\t" \
    "s_nop 7\n\t" \
    "v_mov_b32 v72, 0\n\t" \
    "v_mov_b32 v73, 0\n\t" \
    "v_mov_b32 v74, 0\n\t" \
    "v_mov_b32 v75, 0\n\t" \
    "v_mov_b32 v76, 0\n\t" \
    "v_mov_b32 v77, 0\n\t" \
    "v_mov_b32 v78, 0\n\t" \
    "v_mov_b32 v79, 0\n\t" \
    "v_mov_b32 v80, 0\n\t" \
    "v_mov_b32 v81, 0\n\t" \
    "v_mov_b32 v82, 0\n\t" \
    "v_mov_b32 v83, 0\n\t" \
    "v_mov_b32 v84, 0\n\t" \
    "v_mov_b32 v85, 0\n\t" \
    "v_mov_b32 v86, 0\n\t" \
    "v_mov_b32 v87, 0\n\t" \
    "s_cmp_eq_u32 s68, 0\n\t" \
    "s_cbranch_scc1 qk_F35_%=\n\t" \
    "s_sub_u32 s68, s68, 1\n\t" \
    "s_branch qk_G22_%=\n\t" \
    "qk_F35_%=:\n\t" \
    "v_mov_b32 v160, 5767424\n\t" \
    "v_mov_b32 v161, 1\n\t" \
    "global_store_dword v160, v161, s[36:37] sc1\n\t" \
    "s_waitcnt vmcnt(0)\n\t" \
    "s_branch qk_Gc24_%=\n\t" \
    "qk_Gslow25_%=:\n\t" \
    "v_max_u32 v160, v36, v37\n\t" \
    "v_max3_u32 v160, v160, v38, v39\n\t" \
    "v_max3_u32 v160, v160, v40, v41\n\t" \
    "v_max3_u32 v160, v160, v42, v43\n\t" \
    "v_max3_u32 v160, v160, v44, v45\n\t" \
    "v_max3_u32 v160, v160, v46, v47\n\t" \
    "v_max3_u32 v160, v160, v48, v49\n\t" \
    "v_max3_u32 v160, v160, v50, v51\n\t" \
    "v_max3_u32 v160, v160, v52, v53\n\t" \
    "v_max3_u32 v160, v160, v54, v55\n\t" \
    "v_max3_u32 v160, v160, v56, v57\n\t" \
    "v_max3_u32 v160, v160, v58, v59\n\t" \
    "v_max3_u32 v160, v160, v60, v61\n\t" \
    "v_max3_u32 v160, v160, v62, v63\n\t" \
    "v_max3_u32 v160, v160, v64, v65\n\t" \
    "v_max3_u32 v160, v160, v66, v67\n\t" \
    "v_max3_u32 v160, v160, v68, v69\n\t" \
    "v_max3_u32 v160, v160, v70, v71\n\t" \
    "v_cmp_eq_u32 vcc, -1, v160\n\t" \
    "s_cbranch_vccz qk_Gback26_%=\n\t" \
    "s_waitcnt lgkmcnt(0)\n\t" \
    "v_sub_u32 v160, v1, v3\n\t" \
    "s_nop 0\n\t" \
    "v_readfirstlane_b32 s69, v160\n\t" \
    "v_add_u32 v163, 36864, v0\n\t" \
    "s_nop 7\n\t" \
    "s_add_u32 s30, s46, s69\n\t" \
    "s_addc_u32 s31, s47, 0\n\t" \
    "qk_Gag36_%=:\n\t" \
    "global_load_dwordx4 v[36:39], v1, s[46:47] sc1\n\t" \
    "global_load_dwordx4 v[40:43], v23, s[46:47] sc1\n\t" \
    "global_load_dwordx4 v[44:47], v163, s[30:31] sc1\n\t" \
    "global_load_dwordx4 v[48:51], v4, s[30:31] sc1\n\t" \
    "global_load_dwordx4 v[52:55], v5, s[30:31] sc1\n\t" \
    "global_load_dwordx4 v[56:59], v6, s[30:31] sc1\n\t" \
    "global_load_dwordx4 v[60:63], v7, s[30:31] sc1\n\t" \
    "global_load_dwordx4 v[64:67], v8, s[30:31] sc1\n\t" \
    "global_load_dwordx4 v[68:71], v9, s[30:31] sc1\n\t" \
    "s_waitcnt vmcnt(0)\n\t" \
    "v_max_u32 v160, v36, v37\n\t" \
    "v_max3_u32 v160, v160, v38, v39\n\t" \
    "v_max3_u32 v160, v160, v40, v41\n\t" \
    "v_max3_u32 v160, v160, v42, v43\n\t" \
    "v_max3_u32 v160, v160, v44, v45\n\t" \
    "v_max3_u32 v160, v160, v46, v47\n\t" \
    "v_max3_u32 v160, v160, v48, v49\n\t" \
    "v_max3_u32 v160, v160, v50, v51\n\t" \
    "v_max3_u32 v160, v160, v52, v53\n\t" \
    "v_max3_u32 v160, v160, v54, v55\n\t" \
    "v_max3_u32 v160, v160, v56, v57\n\t" \
    "v_max3_u32 v160, v160, v58, v59\n\t" \
    "v_max3_u32 v160, v160, v60, v61\n\t" \
    "v_max3_u32 v160, v160, v62, v63\n\t" \
    "v_max3_u32 v160, v160, v64, v65\n\t" \
    "v_max3_u32 v160, v160, v66, v67\n\t" \
    "v_max3_u32 v160, v160, v68, v69\n\t" \
    "v_max3_u32 v160, v160, v70, v71\n\t" \
    "v_cmp_eq_u32 vcc, -1, v160\n\t" \
    "s_cbranch_vccz qk_Gredo37_%=\n\t" \
    "s_cmp_eq_u32 s68, 0\n\t" \
    "s_cbranch_scc1 qk_F38_%=\n\t" \
    "s_sub_u32 s68, s68, 1\n\t" \
    "s_sleep 1\n\t" \
    "s_branch qk_Gag36_%=\n\t" \
    "qk_F38_%=:\n\t" \
    "v_mov_b32 v160, 5767424\n\t" \
    "v_mov_b32 v161, 2\n\t" \
    "global_store_dword v160, v161, s[36:37] sc1\n\t" \
    "s_waitcnt vmcnt(0)\n\t" \
    "s_branch qk_Gredo37_%=\n\t" \
    "qk_Gredo37_%=:\n\t" \
    "v_mov_b32 v72, 0\n\t" \
    "v_mov_b32 v73, 0\n\t" \
    "v_mov_b32 v74, 0\n\t" \
    "v_mov_b32 v75, 0\n\t" \
    "v_mov_b32 v76, 0\n\t" \
    "v_mov_b32 v77, 0\n\t" \
    "v_mov_b32 v78, 0\n\t" \
    "v_mov_b32 v79, 0\n\t" \
    "v_mov_b32 v80, 0\n\t" \
    "v_mov_b32 v81, 0\n\t" \
    "v_mov_b32 v82, 0\n\t" \
    "v_mov_b32 v83, 0\n\t" \
    "v_mov_b32 v84, 0\n\t" \
    "v_mov_b32 v85, 0\n\t" \
    "v_mov_b32 v86, 0\n\t" \
    "v_mov_b32 v87, 0\n\t" \
    "v_smfmac_f32_16x16x64_bf16 v[72:75], v[36:39], a[0:7], v10\n\t" \
    "v_smfmac_f32_16x16x64_bf16 v[72:75], v[40:43], a[8:15], v10\n\t" \
    "v_smfmac_f32_16x16x64_bf16 v[72:75], v[48:51], a[16:23], v10\n\t" \
    "v_smfmac_f32_16x16x64_bf16 v[72:75], v[52:55], a[24:31], v10\n\t" \
    "v_smfmac_f32_16x16x64_bf16 v[72:75], v[56:59], a[32:39], v10\n\t" \
    "v_smfmac_f32_16x16x64_bf16 v[72:75], v[60:63], a[40:47], v10\n\t" \
    "v_smfmac_f32_16x16x64_bf16 v[72:75], v[64:67], a[48:55], v10\n\t" \
    "v_smfmac_f32_16x16x64_bf16 v[72:75], v[68:71], a[56:63], v10\n\t" \
    "v_smfmac_f32_16x16x64_bf16 v[72:75], v[44:47], a[64:71], v10\n\t" \
    "v_smfmac_f32_16x16x64_bf16 v[76:79], v[36:39], a[72:79], v10\n\t" \
    "v_smfmac_f32_16x16x64_bf16 v[76:79], v[40:43], a[80:87], v10\n\t" \
    "v_smfmac_f32_16x16x64_bf16 v[76:79], v[48:51], a[88:95], v10\n\t" \
    "v_smfmac_f32_16x16x64_bf16 v[76:79], v[52:55], a[96:103], v10\n\t" \
    "v_smfmac_f32_16x16x64_bf16 v[76:79], v[56:59], a[104:111], v10\n\t" \
    "v_smfmac_f32_16x16x64_bf16 v[76:79], v[60:63], a[112:119], v10\n\t" \
    "v_smfmac_f32_16x16x64_bf16 v[76:79], v[64:67], a[120:127], v10\n\t" \
    "v_smfmac_f32_16x16x64_bf16 v[76:79], v[68:71], a[128:135], v10\n\t" \
    "v_smfmac_f32_16x16x64_bf16 v[76:79], v[44:47], a[136:143], v10\n\t" \
    "v_smfmac_f32_16x16x64_bf16 v[80:83], v[36:39], a[144:151], v10\n\t" \
    "v_smfmac_f32_16x16x64_bf16 v[80:83], v[40:43], a[152:159], v10\n\t" \
    "v_smfmac_f32_16x16x64_bf16 v[80:83], v[48:51], a[160:167], v10\n\t" \
    "v_smfmac_f32_16x16x64_bf16 v[80:83], v[52:55], a[168:175], v10\n\t" \
    "v_smfmac_f32_16x16x64_bf16 v[80:83], v[56:59], a[176:183], v10\n\t" \
    "v_smfmac_f32_16x16x64_bf16 v[80:83], v[60:63], a[184:191], v10\n\t" \
    "v_smfmac_f32_16x16x64_bf16 v[80:83], v[64:67], a[192:199], v10\n\t" \
    "v_smfmac_f32_16x16x64_bf16 v[80:83], v[68:71], a[200:207], v10\n\t" \
    "v_smfmac_f32_16x16x64_bf16 v[80:83], v[44:47], a[208:215], v10\n\t" \
    "v_smfmac_f32_16x16x64_bf16 v[84:87], v[36:39], a[216:223], v10\n\t" \
    "v_smfmac_f32_16x16x64_bf16 v[84:87], v[40:43], a[224:231], v10\n\t" \
    "v_smfmac_f32_16x16x64_bf16 v[84:87], v[48:51], a[232:239], v10\n\t" \
    "v_smfmac_f32_16x16x64_bf16 v[84:87], v[52:55], a[240:247], v10\n\t" \
    "v_smfmac_f32_16x16x64_bf16 v[84:87], v[56:59], a[248:255], v10\n\t" \
    "v_smfmac_f32_16x16x64_bf16 v[84:87], v[60:63], v[224:231], v10\n\t" \
    "v_smfmac_f32_16x16x64_bf16 v[84:87], v[64:67], v[232:239], v10\n\t" \
    "v_smfmac_f32_16x16x64_bf16 v[84:87], v[68:71], v[240:247], v10\n\t" \
    "v_smfmac_f32_16x16x64_bf16 v[84:87], v[44:47], v[248:255], v10\n\t" \
    "v_add_f32 v72, v72, v74\n\t" \
    "v_add_f32 v73, v73, v75\n\t" \
    "v_cvt_pk_bf16_f32 v156, v72, v73\n\t" \
    "ds_write_b32 v12, v156 offset:0\n\t" \
    "v_add_f32 v76, v76, v78\n\t" \
    "v_add_f32 v77, v77, v79\n\t" \
    "v_cvt_pk_bf16_f32 v157, v76, v77\n\t" \
    "ds_write_b32 v12, v157 offset:64\n\t" \
    "v_add_f32 v80, v80, v82\n\t" \
    "v_add_f32 v81, v81, v83\n\t" \
    "v_cvt_pk_bf16_f32 v158, v80, v81\n\t" \
    "ds_write_b32 v16, v158 offset:0\n\t" \
    "v_pk_add_f32 v[84:85], v[84:85], v[86:87]\n\t" \
    "v_cvt_pk_bf16_f32 v159, v84, v85\n\t" \
    "ds_write_b32 v16, v159 offset:256\n\t" \
    "s_branch qk_Gback26_%=\n\t" \
    "qk_qs28_%=:\n\t" \
    "s_cmp_eq_u32 s91, s65\n\t" \
    "s_cbranch_scc1 qk_qb230_%=\n\t" \
    "s_mov_b32 s91, s65\n\t" \
    "s_nop 7\n\t" \
    "ds_read_b64_tr_b16 v[132:133], v13 offset:0\n\t" \
    "ds_read_b64_tr_b16 v[134:135], v13 offset:128\n\t" \
    "ds_read_b64_tr_b16 v[136:137], v13 offset:1024\n\t" \
    "ds_read_b64_tr_b16 v[138:139], v13 offset:1152\n\t" \
    "v_mov_b32 v88, 0\n\t" \
    "v_mov_b32 v89, 0\n\t" \
    "v_mov_b32 v90, 0\n\t" \
    "v_mov_b32 v91, 0\n\t" \
    "v_mov_b32 v92, 0\n\t" \
    "v_mov_b32 v93, 0\n\t" \
    "v_mov_b32 v94, 0\n\t" \
    "v_mov_b32 v95, 0\n\t" \
    "v_mov_b32 v96, 0\n\t" \
    "v_mov_b32 v97, 0\n\t" \
    "v_mov_b32 v98, 0\n\t" \
    "v_mov_b32 v99, 0\n\t" \
    "s_waitcnt lgkmcnt(0)\n\t" \
    "v_smfmac_f32_16x16x64_bf16 v[88:91], v[132:135], v[100:107], v10\n\t" \
    "v_smfmac_f32_16x16x64_bf16 v[92:95], v[132:135], v[116:123], v10\n\t" \
    "v_smfmac_f32_16x16x64_bf16 v[88:91], v[136:139], v[108:115], v10\n\t" \
    "v_smfmac_f32_16x16x64_bf16 v[92:95], v[136:139], v[124:131], v10\n\t" \
    "s_nop 7\n\t" \
    "s_branch qk_qb29_%=\n\t" \
    "qk_last17_%=:\n\t" \
    "s_endpgm\n\t" \
    "qk_loop14_%=:\n\t" \
    "v_mov_b32 v72, 0\n\t" \
    "v_mov_b32 v73, 0\n\t" \
    "v_mov_b32 v74, 0\n\t" \
    "v_mov_b32 v75, 0\n\t" \
    "v_mov_b32 v76, 0\n\t" \
    "v_mov_b32 v77, 0\n\t" \
    "v_mov_b32 v78, 0\n\t" \
    "v_mov_b32 v79, 0\n\t" \
    "v_mov_b32 v80, 0\n\t" \
    "v_mov_b32 v81, 0\n\t" \
    "v_mov_b32 v82, 0\n\t" \
    "v_mov_b32 v83, 0\n\t" \
    "v_mov_b32 v84, 0\n\t" \
    "v_mov_b32 v85, 0\n\t" \
    "v_mov_b32 v86, 0\n\t" \
    "v_mov_b32 v87, 0\n\t" \
    "v_mov_b32 v88, 0\n\t" \
    "v_mov_b32 v89, 0\n\t" \
    "v_mov_b32 v90, 0\n\t" \
    "v_mov_b32 v91, 0\n\t" \
    "v_mov_b32 v92, 0\n\t" \
    "v_mov_b32 v93, 0\n\t" \
    "v_mov_b32 v94, 0\n\t" \
    "v_mov_b32 v95, 0\n\t" \
    "v_mov_b32 v96, 0\n\t" \
    "v_mov_b32 v97, 0\n\t" \
    "v_mov_b32 v98, 0\n\t" \
    "v_mov_b32 v99, 0\n\t" \
    "s_add_u32 s69, s65, 0\n\t" \
    "s_and_b32 s69, s69, 7\n\t" \
    "s_lshl_b32 s69, s69, 18\n\t" \
    "s_add_u32 s69, s69, s62\n\t" \
    "s_add_u32 s46, s36, s69\n\t" \
    "s_addc_u32 s47, s37, 0\n\t" \
    "s_bitcmp0_b32 s65, 0\n\t" \
    "s_cselect_b32 s69, 16384, 0\n\t" \
    "v_xor_b32 v1, s69, v1\n\t" \
    "v_xor_b32 v2, s69, v2\n\t" \
    "v_xor_b32 v23, s69, v23\n\t" \
    "v_xor_b32 v18, s69, v18\n\t" \
    "v_xor_b32 v180, s69, v180\n\t" \
    "v_xor_b32 v181, s69, v181\n\t" \
    "v_xor_b32 v182, s69, v182\n\t" \
    "s_sub_u32 s70, s79, s74\n\t" \
    "s_max_i32 s70, s70, 0\n\t" \
    "s_min_i32 s70, s70, 16000\n\t" \
    "s_lshl_b32 s70, s70, 1\n\t" \
    "s_mul_hi_u32 s70, s70, s77\n\t" \
    "s_add_u32 s70, s70, s73\n\t" \
    ".p2align 6\n\t" \
    "s_nop 0\n\t" \
    "s_nop 0\n\t" \
    "s_nop 0\n\t" \
    "s_nop 0\n\t" \
    "s_nop 0\n\t" \
    "s_nop 0\n\t" \
    "qk_go40_%=:\n\t" \
    "s_getpc_b64 s[30:31]\n\t" \
    "qk_Wp42_%=:\n\t" \
    "s_add_u32 s30, s30, qk_We43_%=-qk_Wp42_%=\n\t" \
    "s_addc_u32 s31, s31, 0\n\t" \
    "s_sub_u32 s70, s70, 96\n\t" \
    "qk_W41_%=:\n\t" \
    "s_memtime s[56:57]\n\t" \
    "s_waitcnt lgkmcnt(0)\n\t" \
    "s_sub_u32 s69, s70, s56\n\t" \
    "s_cmp_gt_i32 s69, 80\n\t" \
    "s_cbranch_scc1 qk_W41_%=\n\t" \
    "s_max_i32 s69, s69, 0\n\t" \
    "s_and_b32 s69, s69, -4\n\t" \
    "s_sub_u32 s30, s30, s69\n\t" \
    "s_subb_u32 s31, s31, 0\n\t" \
    "s_setpc_b64 s[30:31]\n\t" \
    "s_nop 0\n\t" \
    "s_nop 0\n\t" \
    "s_nop 0\n\t" \
    "s_nop 0\n\t" \
    "s_nop 0\n\t" \
    "s_nop 0\n\t" \
    "s_nop 0\n\t" \
    "s_nop 0\n\t" \
    "s_nop 0\n\t" \
    "s_nop 0\n\t" \
    "s_nop 0\n\t" \
    "s_nop 0\n\t" \
    "s_nop 0\n\t" \
    "s_nop 0\n\t" \
    "s_nop 0\n\t" \
    "s_nop 0\n\t" \
    "s_nop 0\n\t" \
    "s_nop 0\n\t" \
    "s_nop 0\n\t" \
    "s_nop 0\n\t" \
    "qk_We43_%=:\n\t" \
    "qk_G44_%=:\n\t" \
    "global_load_dwordx4 v[36:39], v1, s[46:47] sc1\n\t" \
    "global_load_dword v161, v2, s[46:47] sc1\n\t" \
    "global_load_dwordx4 v[40:43], v23, s[46:47] sc1\n\t" \
    "s_waitcnt vmcnt(2)\n\t" \
    "qk_Garr49_%=:\n\t" \
    "ds_write_b128 v3, v[36:39]\n\t" \
    "v_smfmac_f32_16x16x64_bf16 v[72:75], v[36:39], a[16:23], v10\n\t" \
    "s_waitcnt vmcnt(1)\n\t" \
    "ds_write_b32 v22, v161 offset:19712\n\t" \
    "v_smfmac_f32_16x16x64_bf16 v[76:79], v[36:39], a[88:95], v10\n\t" \
    "s_waitcnt vmcnt(0)\n\t" \
    "ds_write_b128 v3, v[40:43] offset:1024\n\t" \
    "v_max3_u32 v160, v36, v40, v161\n\t" \
    "v_cmp_eq_u32 vcc, -1, v160\n\t" \
    "v_smfmac_f32_16x16x64_bf16 v[80:83], v[36:39], a[160:167], v10\n\t" \
    "v_smfmac_f32_16x16x64_bf16 v[84:87], v[36:39], a[232:239], v10\n\t" \
    "v_smfmac_f32_16x16x64_bf16 v[72:75], v[40:43], a[24:31], v10\n\t" \
    "s_cbranch_vccnz qk_Gbad45_%=\n\t" \
    "qk_Gc46_%=:\n\t" \
    "s_waitcnt lgkmcnt(0)\n\t" \
    "s_barrier\n\t" \
    "ds_read_b128 v[48:51], v4\n\t" \
    "ds_read_b128 v[52:55], v5\n\t" \
    "v_smfmac_f32_16x16x64_bf16 v[76:79], v[40:43], a[96:103], v10\n\t" \
    "ds_read_b128 v[56:59], v6\n\t" \
    "ds_read_b128 v[60:63], v7\n\t" \
    "v_smfmac_f32_16x16x64_bf16 v[80:83], v[40:43], a[168:175], v10\n\t" \
    "ds_read_b128 v[64:67], v8\n\t" \
    "v_smfmac_f32_16x16x64_bf16 v[84:87], v[40:43], a[240:247], v10\n\t" \
    "ds_read_b128 v[68:71], v9\n\t" \
    "s_waitcnt lgkmcnt(5)\n\t" \
    "v_smfmac_f32_16x16x64_bf16 v[72:75], v[48:51], a[32:39], v10\n\t" \
    "ds_read_b128 v[44:47], v0 offset:19456\n\t" \
    "s_waitcnt lgkmcnt(5)\n\t" \
    "v_smfmac_f32_16x16x64_bf16 v[72:75], v[52:55], a[40:47], v10\n\t" \
    "s_waitcnt lgkmcnt(4)\n\t" \
    "v_smfmac_f32_16x16x64_bf16 v[72:75], v[56:59], a[48:55], v10\n\t" \
    "s_waitcnt lgkmcnt(3)\n\t" \
    "v_smfmac_f32_16x16x64_bf16 v[72:75], v[60:63], a[56:63], v10\n\t" \
    "s_waitcnt lgkmcnt(2)\n\t" \
    "v_smfmac_f32_16x16x64_bf16 v[72:75], v[64:67], a[0:7], v10\n\t" \
    "s_waitcnt lgkmcnt(1)\n\t" \
    "v_smfmac_f32_16x16x64_bf16 v[72:75], v[68:71], a[8:15], v10\n\t" \
    "s_waitcnt lgkmcnt(0)\n\t" \
    "v_smfmac_f32_16x16x64_bf16 v[72:75], v[44:47], a[64:71], v10\n\t" \
    "s_nop 2\n\t" \
    "v_smfmac_f32_16x16x64_bf16 v[76:79], v[48:51], a[104:111], v10\n\t" \
    "s_nop 2\n\t" \
    "v_smfmac_f32_16x16x64_bf16 v[76:79], v[52:55], a[112:119], v10\n\t" \
    "v_add_f32 v72, v72, v74\n\t" \
    "v_smfmac_f32_16x16x64_bf16 v[76:79], v[56:59], a[120:127], v10\n\t" \
    "v_add_f32 v73, v73, v75\n\t" \
    "v_smfmac_f32_16x16x64_bf16 v[76:79], v[60:63], a[128:135], v10\n\t" \
    "v_cvt_pk_bf16_f32 v156, v72, v73\n\t" \
    "v_smfmac_f32_16x16x64_bf16 v[76:79], v[64:67], a[72:79], v10\n\t" \
    "ds_write_b32 v12, v156 offset:0\n\t" \
    "v_smfmac_f32_16x16x64_bf16 v[76:79], v[68:71], a[80:87], v10\n\t" \
    "v_smfmac_f32_16x16x64_bf16 v[76:79], v[44:47], a[136:143], v10\n\t" \
    "s_nop 2\n\t" \
    "v_smfmac_f32_16x16x64_bf16 v[80:83], v[48:51], a[176:183], v10\n\t" \
    "s_nop 2\n\t" \
    "v_smfmac_f32_16x16x64_bf16 v[80:83], v[52:55], a[184:191], v10\n\t" \
    "v_add_f32 v76, v76, v78\n\t" \
    "v_smfmac_f32_16x16x64_bf16 v[80:83], v[56:59], a[192:199], v10\n\t" \
    "v_add_f32 v77, v77, v79\n\t" \
    "v_smfmac_f32_16x16x64_bf16 v[80:83], v[60:63], a[200:207], v10\n\t" \
    "v_cvt_pk_bf16_f32 v157, v76, v77\n\t" \
    "v_smfmac_f32_16x16x64_bf16 v[80:83], v[64:67], a[144:151], v10\n\t" \
    "ds_write_b32 v12, v157 offset:64\n\t" \
    "v_smfmac_f32_16x16x64_bf16 v[80:83], v[68:71], a[152:159], v10\n\t" \
    "v_smfmac_f32_16x16x64_bf16 v[80:83], v[44:47], a[208:215], v10\n\t" \
    "s_nop 2\n\t" \
    "v_smfmac_f32_16x16x64_bf16 v[84:87], v[48:51], a[248:255], v10\n\t" \
    "s_nop 2\n\t" \
    "v_smfmac_f32_16x16x64_bf16 v[84:87], v[52:55], v[224:231], v10\n\t" \
    "v_add_f32 v80, v80, v82\n\t" \
    "v_smfmac_f32_16x16x64_bf16 v[84:87], v[56:59], v[232:239], v10\n\t" \
    "ds_read_b64_tr_b16 v[132:133], v13 offset:0\n\t" \
    "v_add_f32 v81, v81, v83\n\t" \
    "v_smfmac_f32_16x16x64_bf16 v[84:87], v[60:63], v[240:247], v10\n\t" \
    "ds_read_b64_tr_b16 v[134:135], v13 offset:128\n\t" \
    "v_cvt_pk_bf16_f32 v158, v80, v81\n\t" \
    "v_smfmac_f32_16x16x64_bf16 v[84:87], v[64:67], a[216:223], v10\n\t" \
    "ds_write_b32 v16, v158 offset:0\n\t" \
    "v_smfmac_f32_16x16x64_bf16 v[84:87], v[68:71], a[224:231], v10\n\t" \
    "ds_read_b64_tr_b16 v[136:137], v13 offset:1024\n\t" \
    "ds_read_b64_tr_b16 v[138:139], v13 offset:1152\n\t" \
    "v_smfmac_f32_16x16x64_bf16 v[84:87], v[44:47], v[248:255], v10\n\t" \
    "s_waitcnt lgkmcnt(3)\n\t" \
    "v_smfmac_f32_16x16x64_bf16 v[88:91], v[132:135], v[100:107], v10\n\t" \
    "s_nop 4\n\t" \
    "v_cmp_u_f32 vcc, v72, v73\n\t" \
    "v_pk_add_f32 v[84:85], v[84:85], v[86:87]\n\t" \
    "v_cvt_pk_bf16_f32 v159, v84, v85\n\t" \
    "ds_write_b32 v16, v159 offset:256\n\t" \
    "v_smfmac_f32_16x16x64_bf16 v[92:95], v[132:135], v[116:123], v10\n\t" \
    "s_waitcnt lgkmcnt(1)\n\t" \
    "v_smfmac_f32_16x16x64_bf16 v[88:91], v[136:139], v[108:115], v10\n\t" \
    "s_cbranch_vccnz qk_Gslow47_%=\n\t" \
    "qk_Gback48_%=:\n\t" \
    "v_smfmac_f32_16x16x64_bf16 v[92:95], v[136:139], v[124:131], v10\n\t" \
    "s_waitcnt lgkmcnt(0)\n\t" \
    "s_barrier\n\t" \
    "ds_read_b64_tr_b16 v[140:141], v17 offset:0\n\t" \
    "ds_read_b64_tr_b16 v[142:143], v17 offset:256\n\t" \
    "ds_read_b64_tr_b16 v[144:145], v17 offset:512\n\t" \
    "ds_read_b64_tr_b16 v[146:147], v17 offset:768\n\t" \
    "ds_read_b64_tr_b16 v[148:149], v17 offset:1024\n\t" \
    "ds_read_b64_tr_b16 v[150:151], v17 offset:1280\n\t" \
    "ds_read_b64_tr_b16 v[152:153], v17 offset:1536\n\t" \
    "ds_read_b64_tr_b16 v[154:155], v17 offset:1792\n\t" \
    "qk_qb51_%=:\n\t" \
    "v_add_f32 v168, v89, v91\n\t" \
    "v_add_f32 v169, v93, v95\n\t" \
    "v_cmp_u_f32 vcc, v168, v169\n\t" \
    "v_cvt_pk_bf16_f32 v168, 0, v168\n\t" \
    "v_cvt_pk_bf16_f32 v169, 0, v169\n\t" \
    "s_waitcnt lgkmcnt(4)\n\t" \
    "v_smfmac_f32_16x16x64_bf16 v[96:99], v[132:135], v[140:147], v10\n\t" \
    "s_waitcnt lgkmcnt(0)\n\t" \
    "v_smfmac_f32_16x16x64_bf16 v[96:99], v[136:139], v[148:155], v10\n\t" \
    "s_cbranch_vccnz qk_qs50_%=\n\t" \
    "qk_qb252_%=:\n\t" \
    "v_mul_f32 v168, v168, v26\n\t" \
    "v_mul_f32 v169, v169, v26\n\t" \
    "v_cvt_pk_bf16_f32 v168, 0, v168\n\t" \
    "v_cvt_pk_bf16_f32 v169, 0, v169\n\t" \
    "v_max_f32 v178, v168, v169\n\t" \
    "v_mul_f32 v168, v168, v28\n\t" \
    "v_mul_f32 v169, v169, v28\n\t" \
    "v_max_f32_dpp v178, v178, v178 row_mirror row_mask:0xf bank_mask:0xf\n\t" \
    "v_add_f32 v172, v97, v99\n\t" \
    "v_cvt_pk_bf16_f32 v172, 0, v172\n\t" \
    "v_mul_f32 v172, v172, v26\n\t" \
    "v_cvt_pk_bf16_f32 v172, 0, v172\n\t" \
    "v_max_f32 v173, v178, v172\n\t" \
    "v_mul_f32 v172, v172, v28\n\t" \
    "s_nop 0\n\t" \
    "v_max_f32_dpp v173, v173, v173 quad_perm:[1,0,3,2] row_mask:0xf bank_mask:0xf\n\t" \
    "s_nop 1\n\t" \
    "v_max_f32_dpp v173, v173, v173 quad_perm:[2,3,0,1] row_mask:0xf bank_mask:0xf\n\t" \
    "s_nop 1\n\t" \
    "v_max_f32_dpp v173, v173, v173 row_half_mirror row_mask:0xf bank_mask:0xf\n\t" \
    "v_fma_f32 v172, -v173, v28, v172\n\t" \
    "v_fma_f32 v168, -v173, v28, v168\n\t" \
    "v_fma_f32 v169, -v173, v28, v169\n\t" \
    "v_exp_f32 v172, v172\n\t" \
    "v_exp_f32 v168, v168\n\t" \
    "v_exp_f32 v169, v169\n\t" \
    "v_fma_f32 v173, v172, 0.5, v168\n\t" \
    "v_add_f32 v173, v173, v169\n\t" \
    "v_mov_b32_dpp v174, v172 quad_perm:[0,0,2,2] row_mask:0xf bank_mask:0xf\n\t" \
    "v_mov_b32_dpp v175, v172 quad_perm:[1,1,3,3] row_mask:0xf bank_mask:0xf\n\t" \
    "v_add_f32_dpp v173, v173, v173 quad_perm:[1,0,3,2] row_mask:0xf bank_mask:0xf\n\t" \
    "v_mov_b32_dpp v176, v168 quad_perm:[1,0,3,2] row_mask:0xf bank_mask:0xf\n\t" \
    "v_mov_b32_dpp v177, v169 quad_perm:[1,0,3,2] row_mask:0xf bank_mask:0xf\n\t" \
    "v_add_f32_dpp v173, v173, v173 quad_perm:[2,3,0,1] row_mask:0xf bank_mask:0xf\n\t" \
    "v_cndmask_b32 v168, v168, v177, s[48:49]\n\t" \
    "v_cndmask_b32 v169, v176, v169, s[48:49]\n\t" \
    "v_add_f32_dpp v173, v173, v173 row_half_mirror row_mask:0xf bank_mask:0xf\n\t" \
    "s_nop 1\n\t" \
    "v_add_f32_dpp v173, v173, v173 row_mirror row_mask:0xf bank_mask:0xf\n\t" \
    "v_rcp_f32 v173, v173\n\t" \
    "s_nop 0\n\t" \
    "v_pk_mul_f32 v[168:169], v[168:169], v[172:173] op_sel:[0,1] op_sel_hi:[1,1]\n\t" \
    "v_cvt_pk_bf16_f32 v176, v168, v169\n\t" \
    "global_store_dword v18, v176, s[46:47] sc1\n\t" \
    "global_store_dword v180, v176, s[46:47] sc1\n\t" \
    "global_store_dword v181, v176, s[46:47] sc1\n\t" \
    "global_store_dword v182, v176, s[46:47] sc1\n\t" \
    "v_pk_mul_f32 v[174:175], v[174:175], v[172:173] op_sel:[0,1] op_sel_hi:[1,1]\n\t" \
    "v_cvt_pk_bf16_f32 v177, v174, v175\n\t" \
    "global_store_dword v19, v177, s[46:47] sc1\n\t" \
    "s_cmp_eq_u32 s65, 63\n\t" \
    "s_cbranch_scc1 qk_last39_%=\n\t" \
    "v_readfirstlane_b32 s69, v25\n\t" \
    "s_add_u32 s65, s65, 1\n\t" \
    "s_max_i32 s83, s83, s69\n\t" \
    "s_cmp_gt_u32 s65, 7\n\t" \
    "s_cselect_b32 s83, 99, s83\n\t" \
    "v_mov_b32 v72, 0\n\t" \
    "v_mov_b32 v73, 0\n\t" \
    "v_mov_b32 v74, 0\n\t" \
    "v_mov_b32 v75, 0\n\t" \
    "v_mov_b32 v76, 0\n\t" \
    "v_mov_b32 v77, 0\n\t" \
    "v_mov_b32 v78, 0\n\t" \
    "v_mov_b32 v79, 0\n\t" \
    "v_mov_b32 v80, 0\n\t" \
    "v_mov_b32 v81, 0\n\t" \
    "v_mov_b32 v82, 0\n\t" \
    "v_mov_b32 v83, 0\n\t" \
    "v_mov_b32 v84, 0\n\t" \
    "v_mov_b32 v85, 0\n\t" \
    "v_mov_b32 v86, 0\n\t" \
    "v_mov_b32 v87, 0\n\t" \
    "v_mov_b32 v88, 0\n\t" \
    "v_mov_b32 v89, 0\n\t" \
    "v_mov_b32 v90, 0\n\t" \
    "v_mov_b32 v91, 0\n\t" \
    "v_mov_b32 v92, 0\n\t" \
    "v_mov_b32 v93, 0\n\t" \
    "v_mov_b32 v94, 0\n\t" \
    "v_mov_b32 v95, 0\n\t" \
    "v_mov_b32 v96, 0\n\t" \
    "v_mov_b32 v97, 0\n\t" \
    "v_mov_b32 v98, 0\n\t" \
    "v_mov_b32 v99, 0\n\t" \
    "s_add_u32 s69, s65, 0\n\t" \
    "s_and_b32 s69, s69, 7\n\t" \
    "s_lshl_b32 s69, s69, 18\n\t" \
    "s_add_u32 s69, s69, s62\n\t" \
    "s_add_u32 s46, s36, s69\n\t" \
    "s_addc_u32 s47, s37, 0\n\t" \
    "s_bitcmp0_b32 s65, 0\n\t" \
    "s_cselect_b32 s69, 16384, 0\n\t" \
    "v_xor_b32 v1, s69, v1\n\t" \
    "v_xor_b32 v2, s69, v2\n\t" \
    "v_xor_b32 v23, s69, v23\n\t" \
    "v_xor_b32 v18, s69, v18\n\t" \
    "v_xor_b32 v180, s69, v180\n\t" \
    "v_xor_b32 v181, s69, v181\n\t" \
    "v_xor_b32 v182, s69, v182\n\t" \
    "s_mul_i32 s70, s67, 7\n\t" \
    "s_add_u32 s69, s65, 0\n\t" \
    "s_xor_b32 s69, s69, s70\n\t" \
    "s_and_b32 s69, s69, 63\n\t" \
    "s_lshl_b32 s69, s69, 13\n\t" \
    "s_mul_i32 s70, s67, 524288\n\t" \
    "s_add_u32 s69, s69, s70\n\t" \
    "s_add_u32 s69, s69, 23068672\n\t" \
    "s_add_u32 s38, s36, s69\n\t" \
    "s_addc_u32 s39, s37, 0\n\t" \
    "qk_WF53_%=:\n\t" \
    "s_cmp_ge_i32 s83, s65\n\t" \
    "s_cbranch_scc1 qk_WFok54_%=\n\t" \
    "global_load_dword v160, v24, s[100:101] sc1\n\t" \
    "s_waitcnt vmcnt(0)\n\t" \
    "v_readfirstlane_b32 s83, v160\n\t" \
    "s_nop 1\n\t" \
    "s_cmp_ge_i32 s83, s65\n\t" \
    "s_cbranch_scc1 qk_WFok54_%=\n\t" \
    "s_cmp_eq_u32 s68, 0\n\t" \
    "s_cbranch_scc1 qk_F55_%=\n\t" \
    "s_sub_u32 s68, s68, 1\n\t" \
    "s_sleep 2\n\t" \
    "s_branch qk_WF53_%=\n\t" \
    "qk_F55_%=:\n\t" \
    "v_mov_b32 v160, 5767424\n\t" \
    "v_mov_b32 v161, 9\n\t" \
    "global_store_dword v160, v161, s[36:37] sc1\n\t" \
    "s_waitcnt vmcnt(0)\n\t" \
    "s_branch qk_WFok54_%=\n\t" \
    "qk_WFok54_%=:\n\t" \
    "global_load_dwordx4 v[100:103], v0, s[38:39] offset:0 sc1\n\t" \
    "global_load_dwordx4 v[104:107], v0, s[38:39] offset:2048 sc1\n\t" \
    "global_load_dwordx4 v[108:111], v15, s[38:39] offset:0 sc1\n\t" \
    "global_load_dwordx4 v[112:115], v15, s[38:39] offset:2048 sc1\n\t" \
    "global_load_dwordx4 v[116:119], v0, s[38:39] offset:1024 sc1\n\t" \
    "global_load_dwordx4 v[120:123], v0, s[38:39] offset:3072 sc1\n\t" \
    "global_load_dwordx4 v[124:127], v15, s[38:39] offset:1024 sc1\n\t" \
    "global_load_dwordx4 v[128:131], v15, s[38:39] offset:3072 sc1\n\t" \
    "s_cmp_gt_i32 s83, s65\n\t" \
    "s_cbranch_scc1 qk_nf56_%=\n\t" \
    "global_load_dword v25, v24, s[100:101] sc1\n\t" \
    "qk_nf56_%=:\n\t" \
    "s_barrier\n\t" \
    "ds_read_b32 v168, v11 offset:20480\n\t" \
    "s_waitcnt lgkmcnt(0)\n\t" \
    "v_readfirstlane_b32 s70, v168\n\t" \
    "ds_write_b32 v12, v31 offset:0\n\t" \
    "ds_write_b32 v12, v31 offset:64\n\t" \
    "s_branch qk_go40_%=\n\t" \
    "qk_Gbad45_%=:\n\t" \
    "s_waitcnt vmcnt(0) lgkmcnt(0)\n\t" \
    "s_nop 7\n\t" \
    "v_mov_b32 v72, 0\n\t" \
    "v_mov_b32 v73, 0\n\t" \
    "v_mov_b32 v74, 0\n\t" \
    "v_mov_b32 v75, 0\n\t" \
    "v_mov_b32 v76, 0\n\t" \
    "v_mov_b32 v77, 0\n\t" \
    "v_mov_b32 v78, 0\n\t" \
    "v_mov_b32 v79, 0\n\t" \
    "v_mov_b32 v80, 0\n\t" \
    "v_mov_b32 v81, 0\n\t" \
    "v_mov_b32 v82, 0\n\t" \
    "v_mov_b32 v83, 0\n\t" \
    "v_mov_b32 v84, 0\n\t" \
    "v_mov_b32 v85, 0\n\t" \
    "v_mov_b32 v86, 0\n\t" \
    "v_mov_b32 v87, 0\n\t" \
    "s_cmp_eq_u32 s68, 0\n\t" \
    "s_cbranch_scc1 qk_F57_%=\n\t" \
    "s_sub_u32 s68, s68, 1\n\t" \
    "s_branch qk_G44_%=\n\t" \
    "qk_F57_%=:\n\t" \
    "v_mov_b32 v160, 5767424\n\t" \
    "v_mov_b32 v161, 1\n\t" \
    "global_store_dword v160, v161, s[36:37] sc1\n\t" \
    "s_waitcnt vmcnt(0)\n\t" \
    "s_branch qk_Gc46_%=\n\t" \
    "qk_Gslow47_%=:\n\t" \
    "v_max_u32 v160, v36, v37\n\t" \
    "v_max3_u32 v160, v160, v38, v39\n\t" \
    "v_max3_u32 v160, v160, v40, v41\n\t" \
    "v_max3_u32 v160, v160, v42, v43\n\t" \
    "v_max3_u32 v160, v160, v44, v45\n\t" \
    "v_max3_u32 v160, v160, v46, v47\n\t" \
    "v_max3_u32 v160, v160, v48, v49\n\t" \
    "v_max3_u32 v160, v160, v50, v51\n\t" \
    "v_max3_u32 v160, v160, v52, v53\n\t" \
    "v_max3_u32 v160, v160, v54, v55\n\t" \
    "v_max3_u32 v160, v160, v56, v57\n\t" \
    "v_max3_u32 v160, v160, v58, v59\n\t" \
    "v_max3_u32 v160, v160, v60, v61\n\t" \
    "v_max3_u32 v160, v160, v62, v63\n\t" \
    "v_max3_u32 v160, v160, v64, v65\n\t" \
    "v_max3_u32 v160, v160, v66, v67\n\t" \
    "v_max3_u32 v160, v160, v68, v69\n\t" \
    "v_max3_u32 v160, v160, v70, v71\n\t" \
    "v_cmp_eq_u32 vcc, -1, v160\n\t" \
    "s_cbranch_vccz qk_Gback48_%=\n\t" \
    "s_waitcnt lgkmcnt(0)\n\t" \
    "v_sub_u32 v160, v1, v3\n\t" \
    "s_nop 0\n\t" \
    "v_readfirstlane_b32 s69, v160\n\t" \
    "v_add_u32 v163, 36864, v0\n\t" \
    "s_nop 7\n\t" \
    "s_add_u32 s30, s46, s69\n\t" \
    "s_addc_u32 s31, s47, 0\n\t" \
    "qk_Gag58_%=:\n\t" \
    "global_load_dwordx4 v[36:39], v1, s[46:47] sc1\n\t" \
    "global_load_dwordx4 v[40:43], v23, s[46:47] sc1\n\t" \
    "global_load_dwordx4 v[44:47], v163, s[30:31] sc1\n\t" \
    "global_load_dwordx4 v[48:51], v4, s[30:31] sc1\n\t" \
    "global_load_dwordx4 v[52:55], v5, s[30:31] sc1\n\t" \
    "global_load_dwordx4 v[56:59], v6, s[30:31] sc1\n\t" \
    "global_load_dwordx4 v[60:63], v7, s[30:31] sc1\n\t" \
    "global_load_dwordx4 v[64:67], v8, s[30:31] sc1\n\t" \
    "global_load_dwordx4 v[68:71], v9, s[30:31] sc1\n\t" \
    "s_waitcnt vmcnt(0)\n\t" \
    "v_max_u32 v160, v36, v37\n\t" \
    "v_max3_u32 v160, v160, v38, v39\n\t" \
    "v_max3_u32 v160, v160, v40, v41\n\t" \
    "v_max3_u32 v160, v160, v42, v43\n\t" \
    "v_max3_u32 v160, v160, v44, v45\n\t" \
    "v_max3_u32 v160, v160, v46, v47\n\t" \
    "v_max3_u32 v160, v160, v48, v49\n\t" \
    "v_max3_u32 v160, v160, v50, v51\n\t" \
    "v_max3_u32 v160, v160, v52, v53\n\t" \
    "v_max3_u32 v160, v160, v54, v55\n\t" \
    "v_max3_u32 v160, v160, v56, v57\n\t" \
    "v_max3_u32 v160, v160, v58, v59\n\t" \
    "v_max3_u32 v160, v160, v60, v61\n\t" \
    "v_max3_u32 v160, v160, v62, v63\n\t" \
    "v_max3_u32 v160, v160, v64, v65\n\t" \
    "v_max3_u32 v160, v160, v66, v67\n\t" \
    "v_max3_u32 v160, v160, v68, v69\n\t" \
    "v_max3_u32 v160, v160, v70, v71\n\t" \
    "v_cmp_eq_u32 vcc, -1, v160\n\t" \
    "s_cbranch_vccz qk_Gredo59_%=\n\t" \
    "s_cmp_eq_u32 s68, 0\n\t" \
    "s_cbranch_scc1 qk_F60_%=\n\t" \
    "s_sub_u32 s68, s68, 1\n\t" \
    "s_sleep 1\n\t" \
    "s_branch qk_Gag58_%=\n\t" \
    "qk_F60_%=:\n\t" \
    "v_mov_b32 v160, 5767424\n\t" \
    "v_mov_b32 v161, 2\n\t" \
    "global_store_dword v160, v161, s[36:37] sc1\n\t" \
    "s_waitcnt vmcnt(0)\n\t" \
    "s_branch qk_Gredo59_%=\n\t" \
    "qk_Gredo59_%=:\n\t" \
    "v_mov_b32 v72, 0\n\t" \
    "v_mov_b32 v73, 0\n\t" \
    "v_mov_b32 v74, 0\n\t" \
    "v_mov_b32 v75, 0\n\t" \
    "v_mov_b32 v76, 0\n\t" \
    "v_mov_b32 v77, 0\n\t" \
    "v_mov_b32 v78, 0\n\t" \
    "v_mov_b32 v79, 0\n\t" \
    "v_mov_b32 v80, 0\n\t" \
    "v_mov_b32 v81, 0\n\t" \
    "v_mov_b32 v82, 0\n\t" \
    "v_mov_b32 v83, 0\n\t" \
    "v_mov_b32 v84, 0\n\t" \
    "v_mov_b32 v85, 0\n\t" \
    "v_mov_b32 v86, 0\n\t" \
    "v_mov_b32 v87, 0\n\t" \
    "v_smfmac_f32_16x16x64_bf16 v[72:75], v[36:39], a[16:23], v10\n\t" \
    "v_smfmac_f32_16x16x64_bf16 v[72:75], v[40:43], a[24:31], v10\n\t" \
    "v_smfmac_f32_16x16x64_bf16 v[72:75], v[48:51], a[32:39], v10\n\t" \
    "v_smfmac_f32_16x16x64_bf16 v[72:75], v[52:55], a[40:47], v10\n\t" \
    "v_smfmac_f32_16x16x64_bf16 v[72:75], v[56:59], a[48:55], v10\n\t" \
    "v_smfmac_f32_16x16x64_bf16 v[72:75], v[60:63], a[56:63], v10\n\t" \
    "v_smfmac_f32_16x16x64_bf16 v[72:75], v[64:67], a[0:7], v10\n\t" \
    "v_smfmac_f32_16x16x64_bf16 v[72:75], v[68:71], a[8:15], v10\n\t" \
    "v_smfmac_f32_16x16x64_bf16 v[72:75], v[44:47], a[64:71], v10\n\t" \
    "v_smfmac_f32_16x16x64_bf16 v[76:79], v[36:39], a[88:95], v10\n\t" \
    "v_smfmac_f32_16x16x64_bf16 v[76:79], v[40:43], a[96:103], v10\n\t" \
    "v_smfmac_f32_16x16x64_bf16 v[76:79], v[48:51], a[104:111], v10\n\t" \
    "v_smfmac_f32_16x16x64_bf16 v[76:79], v[52:55], a[112:119], v10\n\t" \
    "v_smfmac_f32_16x16x64_bf16 v[76:79], v[56:59], a[120:127], v10\n\t" \
    "v_smfmac_f32_16x16x64_bf16 v[76:79], v[60:63], a[128:135], v10\n\t" \
    "v_smfmac_f32_16x16x64_bf16 v[76:79], v[64:67], a[72:79], v10\n\t" \
    "v_smfmac_f32_16x16x64_bf16 v[76:79], v[68:71], a[80:87], v10\n\t" \
    "v_smfmac_f32_16x16x64_bf16 v[76:79], v[44:47], a[136:143], v10\n\t" \
    "v_smfmac_f32_16x16x64_bf16 v[80:83], v[36:39], a[160:167], v10\n\t" \
    "v_smfmac_f32_16x16x64_bf16 v[80:83], v[40:43], a[168:175], v10\n\t" \
    "v_smfmac_f32_16x16x64_bf16 v[80:83], v[48:51], a[176:183], v10\n\t" \
    "v_smfmac_f32_16x16x64_bf16 v[80:83], v[52:55], a[184:191], v10\n\t" \
    "v_smfmac_f32_16x16x64_bf16 v[80:83], v[56:59], a[192:199], v10\n\t" \
    "v_smfmac_f32_16x16x64_bf16 v[80:83], v[60:63], a[200:207], v10\n\t" \
    "v_smfmac_f32_16x16x64_bf16 v[80:83], v[64:67], a[144:151], v10\n\t" \
    "v_smfmac_f32_16x16x64_bf16 v[80:83], v[68:71], a[152:159], v10\n\t" \
    "v_smfmac_f32_16x16x64_bf16 v[80:83], v[44:47], a[208:215], v10\n\t" \
    "v_smfmac_f32_16x16x64_bf16 v[84:87], v[36:39], a[232:239], v10\n\t" \
    "v_smfmac_f32_16x16x64_bf16 v[84:87], v[40:43], a[240:247], v10\n\t" \
    "v_smfmac_f32_16x16x64_bf16 v[84:87], v[48:51], a[248:255], v10\n\t" \
    "v_smfmac_f32_16x16x64_bf16 v[84:87], v[52:55], v[224:231], v10\n\t" \
    "v_smfmac_f32_16x16x64_bf16 v[84:87], v[56:59], v[232:239], v10\n\t" \
    "v_smfmac_f32_16x16x64_bf16 v[84:87], v[60:63], v[240:247], v10\n\t" \
    "v_smfmac_f32_16x16x64_bf16 v[84:87], v[64:67], a[216:223], v10\n\t" \
    "v_smfmac_f32_16x16x64_bf16 v[84:87], v[68:71], a[224:231], v10\n\t" \
    "v_smfmac_f32_16x16x64_bf16 v[84:87], v[44:47], v[248:255], v10\n\t" \
    "v_add_f32 v72, v72, v74\n\t" \
    "v_add_f32 v73, v73, v75\n\t" \
    "v_cvt_pk_bf16_f32 v156, v72, v73\n\t" \
    "ds_write_b32 v12, v156 offset:0\n\t" \
    "v_add_f32 v76, v76, v78\n\t" \
    "v_add_f32 v77, v77, v79\n\t" \
    "v_cvt_pk_bf16_f32 v157, v76, v77\n\t" \
    "ds_write_b32 v12, v157 offset:64\n\t" \
    "v_add_f32 v80, v80, v82\n\t" \
    "v_add_f32 v81, v81, v83\n\t" \
    "v_cvt_pk_bf16_f32 v158, v80, v81\n\t" \
    "ds_write_b32 v16, v158 offset:0\n\t" \
    "v_pk_add_f32 v[84:85], v[84:85], v[86:87]\n\t" \
    "v_cvt_pk_bf16_f32 v159, v84, v85\n\t" \
    "ds_write_b32 v16, v159 offset:256\n\t" \
    "s_branch qk_Gback48_%=\n\t" \
    "qk_qs50_%=:\n\t" \
    "s_cmp_eq_u32 s91, s65\n\t" \
    "s_cbranch_scc1 qk_qb252_%=\n\t" \
    "s_mov_b32 s91, s65\n\t" \
    "s_nop 7\n\t" \
    "ds_read_b64_tr_b16 v[132:133], v13 offset:0\n\t" \
    "ds_read_b64_tr_b16 v[134:135], v13 offset:128\n\t" \
    "ds_read_b64_tr_b16 v[136:137], v13 offset:1024\n\t" \
    "ds_read_b64_tr_b16 v[138:139], v13 offset:1152\n\t" \
    "v_mov_b32 v88, 0\n\t" \
    "v_mov_b32 v89, 0\n\t" \
    "v_mov_b32 v90, 0\n\t" \
    "v_mov_b32 v91, 0\n\t" \
    "v_mov_b32 v92, 0\n\t" \
    "v_mov_b32 v93, 0\n\t" \
    "v_mov_b32 v94, 0\n\t" \
    "v_mov_b32 v95, 0\n\t" \
    "v_mov_b32 v96, 0\n\t" \
    "v_mov_b32 v97, 0\n\t" \
    "v_mov_b32 v98, 0\n\t" \
    "v_mov_b32 v99, 0\n\t" \
    "s_waitcnt lgkmcnt(0)\n\t" \
    "v_smfmac_f32_16x16x64_bf16 v[88:91], v[132:135], v[100:107], v10\n\t" \
    "v_smfmac_f32_16x16x64_bf16 v[92:95], v[132:135], v[116:123], v10\n\t" \
    "v_smfmac_f32_16x16x64_bf16 v[88:91], v[136:139], v[108:115], v10\n\t" \
    "v_smfmac_f32_16x16x64_bf16 v[92:95], v[136:139], v[124:131], v10\n\t" \
    "s_nop 7\n\t" \
    "s_branch qk_qb51_%=\n\t" \
    "qk_last39_%=:\n\t" \
    "s_endpgm\n\t" \
    "qk_loop15_%=:\n\t" \
    "v_mov_b32 v72, 0\n\t" \
    "v_mov_b32 v73, 0\n\t" \
    "v_mov_b32 v74, 0\n\t" \
    "v_mov_b32 v75, 0\n\t" \
    "v_mov_b32 v76, 0\n\t" \
    "v_mov_b32 v77, 0\n\t" \
    "v_mov_b32 v78, 0\n\t" \
    "v_mov_b32 v79, 0\n\t" \
    "v_mov_b32 v80, 0\n\t" \
    "v_mov_b32 v81, 0\n\t" \
    "v_mov_b32 v82, 0\n\t" \
    "v_mov_b32 v83, 0\n\t" \
    "v_mov_b32 v84, 0\n\t" \
    "v_mov_b32 v85, 0\n\t" \
    "v_mov_b32 v86, 0\n\t" \
    "v_mov_b32 v87, 0\n\t" \
    "s_add_u32 s69, s65, 0\n\t" \
    "s_and_b32 s69, s69, 7\n\t" \
    "s_lshl_b32 s69, s69, 18\n\t" \
    "s_add_u32 s69, s69, s62\n\t" \
    "s_add_u32 s46, s36, s69\n\t" \
    "s_addc_u32 s47, s37, 0\n\t" \
    "s_bitcmp0_b32 s65, 0\n\t" \
    "s_cselect_b32 s69, 16384, 0\n\t" \
    "v_xor_b32 v1, s69, v1\n\t" \
    "v_xor_b32 v2, s69, v2\n\t" \
    "v_xor_b32 v23, s69, v23\n\t" \
    "s_mul_i32 s70, s67, 7\n\t" \
    "s_add_u32 s69, s65, 0\n\t" \
    "s_xor_b32 s69, s69, s70\n\t" \
    "s_and_b32 s69, s69, 63\n\t" \
    "s_lshl_b32 s69, s69, 12\n\t" \
    "s_mul_i32 s70, s67, 262144\n\t" \
    "s_add_u32 s69, s69, s70\n\t" \
    "s_add_u32 s69, s69, 39845888\n\t" \
    "s_add_u32 s40, s36, s69\n\t" \
    "s_addc_u32 s41, s37, 0\n\t" \
    "s_sub_u32 s91, s65, 1\n\t" \
    "v_mov_b32 v25, s91\n\t" \
    "s_sub_u32 s70, s79, s74\n\t" \
    "s_max_i32 s70, s70, 0\n\t" \
    "s_min_i32 s70, s70, 16000\n\t" \
    "s_lshl_b32 s70, s70, 1\n\t" \
    "s_mul_hi_u32 s70, s70, s77\n\t" \
    "s_add_u32 s70, s70, s73\n\t" \
    ".p2align 6\n\t" \
    "s_nop 0\n\t" \
    "s_nop 0\n\t" \
    "s_nop 0\n\t" \
    "s_nop 0\n\t" \
    "s_nop 0\n\t" \
    "s_nop 0\n\t" \
    "qk_go62_%=:\n\t" \
    "s_getpc_b64 s[30:31]\n\t" \
    "qk_Wp66_%=:\n\t" \
    "s_add_u32 s30, s30, qk_We67_%=-qk_Wp66_%=\n\t" \
    "s_addc_u32 s31, s31, 0\n\t" \
    "s_sub_u32 s70, s70, 96\n\t" \
    "qk_W65_%=:\n\t" \
    "s_memtime s[56:57]\n\t" \
    "s_waitcnt lgkmcnt(0)\n\t" \
    "s_sub_u32 s69, s70, s56\n\t" \
    "s_cmp_gt_i32 s69, 80\n\t" \
    "s_cbranch_scc1 qk_W65_%=\n\t" \
    "s_max_i32 s69, s69, 0\n\t" \
    "s_and_b32 s69, s69, -4\n\t" \
    "s_sub_u32 s30, s30, s69\n\t" \
    "s_subb_u32 s31, s31, 0\n\t" \
    "s_setpc_b64 s[30:31]\n\t" \
    "s_nop 0\n\t" \
    "s_nop 0\n\t" \
    "s_nop 0\n\t" \
    "s_nop 0\n\t" \
    "s_nop 0\n\t" \
    "s_nop 0\n\t" \
    "s_nop 0\n\t" \
    "s_nop 0\n\t" \
    "s_nop 0\n\t" \
    "s_nop 0\n\t" \
    "s_nop 0\n\t" \
    "s_nop 0\n\t" \
    "s_nop 0\n\t" \
    "s_nop 0\n\t" \
    "s_nop 0\n\t" \
    "s_nop 0\n\t" \
    "s_nop 0\n\t" \
    "s_nop 0\n\t" \
    "s_nop 0\n\t" \
    "s_nop 0\n\t" \
    "qk_We67_%=:\n\t" \
    "qk_G68_%=:\n\t" \
    "global_load_dwordx4 v[36:39], v1, s[46:47] sc1\n\t" \
    "global_load_dword v161, v2, s[46:47] sc1\n\t" \
    "global_load_dwordx4 v[40:43], v23, s[46:47] sc1\n\t" \
    "s_waitcnt vmcnt(2)\n\t" \
    "qk_Garr73_%=:\n\t" \
    "ds_write_b128 v3, v[36:39]\n\t" \
    "v_smfmac_f32_16x16x64_bf16 v[72:75], v[36:39], a[32:39], v10\n\t" \
    "s_waitcnt vmcnt(1)\n\t" \
    "ds_write_b32 v22, v161 offset:19968\n\t" \
    "v_smfmac_f32_16x16x64_bf16 v[76:79], v[36:39], a[104:111], v10\n\t" \
    "s_waitcnt vmcnt(0)\n\t" \
    "ds_write_b128 v3, v[40:43] offset:1024\n\t" \
    "v_max3_u32 v160, v36, v40, v161\n\t" \
    "v_cmp_eq_u32 vcc, -1, v160\n\t" \
    "v_smfmac_f32_16x16x64_bf16 v[80:83], v[36:39], a[176:183], v10\n\t" \
    "v_smfmac_f32_16x16x64_bf16 v[84:87], v[36:39], a[248:255], v10\n\t" \
    "v_smfmac_f32_16x16x64_bf16 v[72:75], v[40:43], a[40:47], v10\n\t" \
    "s_cbranch_vccnz qk_Gbad69_%=\n\t" \
    "qk_Gc70_%=:\n\t" \
    "s_cmp_ge_u32 s91, 57\n\t" \
    "s_cbranch_scc1 qk_kf63_%=\n\t" \
    "qk_kb64_%=:\n\t" \
    "s_waitcnt lgkmcnt(0)\n\t" \
    "s_barrier\n\t" \
    "ds_read_b128 v[48:51], v4\n\t" \
    "ds_read_b128 v[52:55], v5\n\t" \
    "v_smfmac_f32_16x16x64_bf16 v[76:79], v[40:43], a[112:119], v10\n\t" \
    "ds_read_b128 v[56:59], v6\n\t" \
    "ds_read_b128 v[60:63], v7\n\t" \
    "v_smfmac_f32_16x16x64_bf16 v[80:83], v[40:43], a[184:191], v10\n\t" \
    "ds_read_b128 v[64:67], v8\n\t" \
    "v_smfmac_f32_16x16x64_bf16 v[84:87], v[40:43], v[224:231], v10\n\t" \
    "ds_read_b128 v[68:71], v9\n\t" \
    "s_waitcnt lgkmcnt(5)\n\t" \
    "v_smfmac_f32_16x16x64_bf16 v[72:75], v[48:51], a[48:55], v10\n\t" \
    "ds_read_b128 v[44:47], v0 offset:19456\n\t" \
    "s_waitcnt lgkmcnt(5)\n\t" \
    "v_smfmac_f32_16x16x64_bf16 v[72:75], v[52:55], a[56:63], v10\n\t" \
    "s_waitcnt lgkmcnt(4)\n\t" \
    "v_smfmac_f32_16x16x64_bf16 v[72:75], v[56:59], a[0:7], v10\n\t" \
    "s_waitcnt lgkmcnt(3)\n\t" \
    "v_smfmac_f32_16x16x64_bf16 v[72:75], v[60:63], a[8:15], v10\n\t" \
    "s_waitcnt lgkmcnt(2)\n\t" \
    "v_smfmac_f32_16x16x64_bf16 v[72:75], v[64:67], a[16:23], v10\n\t" \
    "s_waitcnt lgkmcnt(1)\n\t" \
    "v_smfmac_f32_16x16x64_bf16 v[72:75], v[68:71], a[24:31], v10\n\t" \
    "s_waitcnt lgkmcnt(0)\n\t" \
    "v_smfmac_f32_16x16x64_bf16 v[72:75], v[44:47], a[64:71], v10\n\t" \
    "s_nop 2\n\t" \
    "v_smfmac_f32_16x16x64_bf16 v[76:79], v[48:51], a[120:127], v10\n\t" \
    "s_nop 2\n\t" \
    "v_smfmac_f32_16x16x64_bf16 v[76:79], v[52:55], a[128:135], v10\n\t" \
    "v_add_f32 v72, v72, v74\n\t" \
    "v_smfmac_f32_16x16x64_bf16 v[76:79], v[56:59], a[72:79], v10\n\t" \
    "v_add_f32 v73, v73, v75\n\t" \
    "v_smfmac_f32_16x16x64_bf16 v[76:79], v[60:63], a[80:87], v10\n\t" \
    "v_cvt_pk_bf16_f32 v156, v72, v73\n\t" \
    "v_smfmac_f32_16x16x64_bf16 v[76:79], v[64:67], a[88:95], v10\n\t" \
    "ds_write_b32 v12, v156 offset:0\n\t" \
    "v_smfmac_f32_16x16x64_bf16 v[76:79], v[68:71], a[96:103], v10\n\t" \
    "v_smfmac_f32_16x16x64_bf16 v[76:79], v[44:47], a[136:143], v10\n\t" \
    "s_nop 2\n\t" \
    "v_smfmac_f32_16x16x64_bf16 v[80:83], v[48:51], a[192:199], v10\n\t" \
    "s_nop 2\n\t" \
    "v_smfmac_f32_16x16x64_bf16 v[80:83], v[52:55], a[200:207], v10\n\t" \
    "v_add_f32 v76, v76, v78\n\t" \
    "v_smfmac_f32_16x16x64_bf16 v[80:83], v[56:59], a[144:151], v10\n\t" \
    "v_add_f32 v77, v77, v79\n\t" \
    "v_smfmac_f32_16x16x64_bf16 v[80:83], v[60:63], a[152:159], v10\n\t" \
    "v_cvt_pk_bf16_f32 v157, v76, v77\n\t" \
    "v_smfmac_f32_16x16x64_bf16 v[80:83], v[64:67], a[160:167], v10\n\t" \
    "ds_write_b32 v12, v157 offset:64\n\t" \
    "v_smfmac_f32_16x16x64_bf16 v[80:83], v[68:71], a[168:175], v10\n\t" \
    "v_smfmac_f32_16x16x64_bf16 v[80:83], v[44:47], a[208:215], v10\n\t" \
    "s_nop 2\n\t" \
    "v_smfmac_f32_16x16x64_bf16 v[84:87], v[48:51], v[232:239], v10\n\t" \
    "s_nop 2\n\t" \
    "v_smfmac_f32_16x16x64_bf16 v[84:87], v[52:55], v[240:247], v10\n\t" \
    "v_add_f32 v80, v80, v82\n\t" \
    "v_smfmac_f32_16x16x64_bf16 v[84:87], v[56:59], a[216:223], v10\n\t" \
    "v_add_f32 v81, v81, v83\n\t" \
    "v_smfmac_f32_16x16x64_bf16 v[84:87], v[60:63], a[224:231], v10\n\t" \
    "v_cvt_pk_bf16_f32 v158, v80, v81\n\t" \
    "v_smfmac_f32_16x16x64_bf16 v[84:87], v[64:67], a[232:239], v10\n\t" \
    "ds_write_b32 v16, v158 offset:0\n\t" \
    "v_smfmac_f32_16x16x64_bf16 v[84:87], v[68:71], a[240:247], v10\n\t" \
    "v_smfmac_f32_16x16x64_bf16 v[84:87], v[44:47], v[248:255], v10\n\t" \
    "s_nop 6\n\t" \
    "v_cmp_u_f32 vcc, v72, v73\n\t" \
    "v_pk_add_f32 v[84:85], v[84:85], v[86:87]\n\t" \
    "v_cvt_pk_bf16_f32 v159, v84, v85\n\t" \
    "ds_write_b32 v16, v159 offset:256\n\t" \
    "s_cbranch_vccnz qk_Gslow71_%=\n\t" \
    "qk_Gback72_%=:\n\t" \
    "s_waitcnt lgkmcnt(0)\n\t" \
    "s_barrier\n\t" \
    "s_sleep 1\n\t" \
    "ds_read_b64_tr_b16 v[140:141], v17 offset:0\n\t" \
    "ds_read_b64_tr_b16 v[142:143], v17 offset:256\n\t" \
    "ds_read_b64_tr_b16 v[144:145], v17 offset:512\n\t" \
    "ds_read_b64_tr_b16 v[146:147], v17 offset:768\n\t" \
    "ds_read_b64_tr_b16 v[148:149], v17 offset:1024\n\t" \
    "ds_read_b64_tr_b16 v[150:151], v17 offset:1280\n\t" \
    "ds_read_b64_tr_b16 v[152:153], v17 offset:1536\n\t" \
    "ds_read_b64_tr_b16 v[154:155], v17 offset:1792\n\t" \
    "s_waitcnt lgkmcnt(0)\n\t" \
    "s_cmp_eq_u32 s65, 63\n\t" \
    "s_cbranch_scc1 qk_last61_%=\n\t" \
    "s_cmp_ge_u32 s65, 58\n\t" \
    "s_cbranch_scc1 qk_nsl74_%=\n\t" \
    "s_sleep 6\n\t" \
    "global_store_dword v14, v25, s[100:101] sc1\n\t" \
    "qk_nsl74_%=:\n\t" \
    "s_mov_b64 exec, s[58:59]\n\t" \
    "global_store_dwordx4 v0, v[140:143], s[40:41] offset:0 sc1\n\t" \
    "global_store_dwordx4 v0, v[144:147], s[40:41] offset:1024 sc1\n\t" \
    "global_store_dwordx4 v0, v[148:151], s[40:41] offset:2048 sc1\n\t" \
    "global_store_dwordx4 v0, v[152:155], s[40:41] offset:3072 sc1\n\t" \
    "s_mov_b64 exec, -1\n\t" \
    "s_add_u32 s65, s65, 1\n\t" \
    "v_mov_b32 v72, 0\n\t" \
    "v_mov_b32 v73, 0\n\t" \
    "v_mov_b32 v74, 0\n\t" \
    "v_mov_b32 v75, 0\n\t" \
    "v_mov_b32 v76, 0\n\t" \
    "v_mov_b32 v77, 0\n\t" \
    "v_mov_b32 v78, 0\n\t" \
    "v_mov_b32 v79, 0\n\t" \
    "v_mov_b32 v80, 0\n\t" \
    "v_mov_b32 v81, 0\n\t" \
    "v_mov_b32 v82, 0\n\t" \
    "v_mov_b32 v83, 0\n\t" \
    "v_mov_b32 v84, 0\n\t" \
    "v_mov_b32 v85, 0\n\t" \
    "v_mov_b32 v86, 0\n\t" \
    "v_mov_b32 v87, 0\n\t" \
    "s_add_u32 s69, s65, 0\n\t" \
    "s_and_b32 s69, s69, 7\n\t" \
    "s_lshl_b32 s69, s69, 18\n\t" \
    "s_add_u32 s69, s69, s62\n\t" \
    "s_add_u32 s46, s36, s69\n\t" \
    "s_addc_u32 s47, s37, 0\n\t" \
    "s_bitcmp0_b32 s65, 0\n\t" \
    "s_cselect_b32 s69, 16384, 0\n\t" \
    "v_xor_b32 v1, s69, v1\n\t" \
    "v_xor_b32 v2, s69, v2\n\t" \
    "v_xor_b32 v23, s69, v23\n\t" \
    "s_mul_i32 s70, s67, 7\n\t" \
    "s_add_u32 s69, s65, 0\n\t" \
    "s_xor_b32 s69, s69, s70\n\t" \
    "s_and_b32 s69, s69, 63\n\t" \
    "s_lshl_b32 s69, s69, 12\n\t" \
    "s_mul_i32 s70, s67, 262144\n\t" \
    "s_add_u32 s69, s69, s70\n\t" \
    "s_add_u32 s69, s69, 39845888\n\t" \
    "s_add_u32 s40, s36, s69\n\t" \
    "s_addc_u32 s41, s37, 0\n\t" \
    "s_sub_u32 s91, s65, 1\n\t" \
    "v_mov_b32 v25, s91\n\t" \
    "s_barrier\n\t" \
    "ds_read_b32 v168, v11 offset:20480\n\t" \
    "s_waitcnt lgkmcnt(0)\n\t" \
    "v_readfirstlane_b32 s70, v168\n\t" \
    "ds_write_b32 v12, v31 offset:0\n\t" \
    "ds_write_b32 v12, v31 offset:64\n\t" \
    "s_branch qk_go62_%=\n\t" \
    "qk_Gbad69_%=:\n\t" \
    "s_waitcnt vmcnt(0) lgkmcnt(0)\n\t" \
    "s_nop 7\n\t" \
    "v_mov_b32 v72, 0\n\t" \
    "v_mov_b32 v73, 0\n\t" \
    "v_mov_b32 v74, 0\n\t" \
    "v_mov_b32 v75, 0\n\t" \
    "v_mov_b32 v76, 0\n\t" \
    "v_mov_b32 v77, 0\n\t" \
    "v_mov_b32 v78, 0\n\t" \
    "v_mov_b32 v79, 0\n\t" \
    "v_mov_b32 v80, 0\n\t" \
    "v_mov_b32 v81, 0\n\t" \
    "v_mov_b32 v82, 0\n\t" \
    "v_mov_b32 v83, 0\n\t" \
    "v_mov_b32 v84, 0\n\t" \
    "v_mov_b32 v85, 0\n\t" \
    "v_mov_b32 v86, 0\n\t" \
    "v_mov_b32 v87, 0\n\t" \
    "s_cmp_eq_u32 s68, 0\n\t" \
    "s_cbranch_scc1 qk_F75_%=\n\t" \
    "s_sub_u32 s68, s68, 1\n\t" \
    "s_branch qk_G68_%=\n\t" \
    "qk_F75_%=:\n\t" \
    "v_mov_b32 v160, 5767424\n\t" \
    "v_mov_b32 v161, 1\n\t" \
    "global_store_dword v160, v161, s[36:37] sc1\n\t" \
    "s_waitcnt vmcnt(0)\n\t" \
    "s_branch qk_Gc70_%=\n\t" \
    "qk_Gslow71_%=:\n\t" \
    "v_max_u32 v160, v36, v37\n\t" \
    "v_max3_u32 v160, v160, v38, v39\n\t" \
    "v_max3_u32 v160, v160, v40, v41\n\t" \
    "v_max3_u32 v160, v160, v42, v43\n\t" \
    "v_max3_u32 v160, v160, v44, v45\n\t" \
    "v_max3_u32 v160, v160, v46, v47\n\t" \
    "v_max3_u32 v160, v160, v48, v49\n\t" \
    "v_max3_u32 v160, v160, v50, v51\n\t" \
    "v_max3_u32 v160, v160, v52, v53\n\t" \
    "v_max3_u32 v160, v160, v54, v55\n\t" \
    "v_max3_u32 v160, v160, v56, v57\n\t" \
    "v_max3_u32 v160, v160, v58, v59\n\t" \
    "v_max3_u32 v160, v160, v60, v61\n\t" \
    "v_max3_u32 v160, v160, v62, v63\n\t" \
    "v_max3_u32 v160, v160, v64, v65\n\t" \
    "v_max3_u32 v160, v160, v66, v67\n\t" \
    "v_max3_u32 v160, v160, v68, v69\n\t" \
    "v_max3_u32 v160, v160, v70, v71\n\t" \
    "v_cmp_eq_u32 vcc, -1, v160\n\t" \
    "s_cbranch_vccz qk_Gback72_%=\n\t" \
    "s_waitcnt lgkmcnt(0)\n\t" \
    "v_sub_u32 v160, v1, v3\n\t" \
    "s_nop 0\n\t" \
    "v_readfirstlane_b32 s69, v160\n\t" \
    "v_add_u32 v163, 36864, v0\n\t" \
    "s_nop 7\n\t" \
    "s_add_u32 s30, s46, s69\n\t" \
    "s_addc_u32 s31, s47, 0\n\t" \
    "qk_Gag76_%=:\n\t" \
    "global_load_dwordx4 v[36:39], v1, s[46:47] sc1\n\t" \
    "global_load_dwordx4 v[40:43], v23, s[46:47] sc1\n\t" \
    "global_load_dwordx4 v[44:47], v163, s[30:31] sc1\n\t" \
    "global_load_dwordx4 v[48:51], v4, s[30:31] sc1\n\t" \
    "global_load_dwordx4 v[52:55], v5, s[30:31] sc1\n\t" \
    "global_load_dwordx4 v[56:59], v6, s[30:31] sc1\n\t" \
    "global_load_dwordx4 v[60:63], v7, s[30:31] sc1\n\t" \
    "global_load_dwordx4 v[64:67], v8, s[30:31] sc1\n\t" \
    "global_load_dwordx4 v[68:71], v9, s[30:31] sc1\n\t" \
    "s_waitcnt vmcnt(0)\n\t" \
    "v_max_u32 v160, v36, v37\n\t" \
    "v_max3_u32 v160, v160, v38, v39\n\t" \
    "v_max3_u32 v160, v160, v40, v41\n\t" \
    "v_max3_u32 v160, v160, v42, v43\n\t" \
    "v_max3_u32 v160, v160, v44, v45\n\t" \
    "v_max3_u32 v160, v160, v46, v47\n\t" \
    "v_max3_u32 v160, v160, v48, v49\n\t" \
    "v_max3_u32 v160, v160, v50, v51\n\t" \
    "v_max3_u32 v160, v160, v52, v53\n\t" \
    "v_max3_u32 v160, v160, v54, v55\n\t" \
    "v_max3_u32 v160, v160, v56, v57\n\t" \
    "v_max3_u32 v160, v160, v58, v59\n\t" \
    "v_max3_u32 v160, v160, v60, v61\n\t" \
    "v_max3_u32 v160, v160, v62, v63\n\t" \
    "v_max3_u32 v160, v160, v64, v65\n\t" \
    "v_max3_u32 v160, v160, v66, v67\n\t" \
    "v_max3_u32 v160, v160, v68, v69\n\t" \
    "v_max3_u32 v160, v160, v70, v71\n\t" \
    "v_cmp_eq_u32 vcc, -1, v160\n\t" \
    "s_cbranch_vccz qk_Gredo77_%=\n\t" \
    "s_cmp_eq_u32 s68, 0\n\t" \
    "s_cbranch_scc1 qk_F78_%=\n\t" \
    "s_sub_u32 s68, s68, 1\n\t" \
    "s_sleep 1\n\t" \
    "s_branch qk_Gag76_%=\n\t" \
    "qk_F78_%=:\n\t" \
    "v_mov_b32 v160, 5767424\n\t" \
    "v_mov_b32 v161, 2\n\t" \
    "global_store_dword v160, v161, s[36:37] sc1\n\t" \
    "s_waitcnt vmcnt(0)\n\t" \
    "s_branch qk_Gredo77_%=\n\t" \
    "qk_Gredo77_%=:\n\t" \
    "v_mov_b32 v72, 0\n\t" \
    "v_mov_b32 v73, 0\n\t" \
    "v_mov_b32 v74, 0\n\t" \
    "v_mov_b32 v75, 0\n\t" \
    "v_mov_b32 v76, 0\n\t" \
    "v_mov_b32 v77, 0\n\t" \
    "v_mov_b32 v78, 0\n\t" \
    "v_mov_b32 v79, 0\n\t" \
    "v_mov_b32 v80, 0\n\t" \
    "v_mov_b32 v81, 0\n\t" \
    "v_mov_b32 v82, 0\n\t" \
    "v_mov_b32 v83, 0\n\t" \
    "v_mov_b32 v84, 0\n\t" \
    "v_mov_b32 v85, 0\n\t" \
    "v_mov_b32 v86, 0\n\t" \
    "v_mov_b32 v87, 0\n\t" \
    "v_smfmac_f32_16x16x64_bf16 v[72:75], v[36:39], a[32:39], v10\n\t" \
    "v_smfmac_f32_16x16x64_bf16 v[72:75], v[40:43], a[40:47], v10\n\t" \
    "v_smfmac_f32_16x16x64_bf16 v[72:75], v[48:51], a[48:55], v10\n\t" \
    "v_smfmac_f32_16x16x64_bf16 v[72:75], v[52:55], a[56:63], v10\n\t" \
    "v_smfmac_f32_16x16x64_bf16 v[72:75], v[56:59], a[0:7], v10\n\t" \
    "v_smfmac_f32_16x16x64_bf16 v[72:75], v[60:63], a[8:15], v10\n\t" \
    "v_smfmac_f32_16x16x64_bf16 v[72:75], v[64:67], a[16:23], v10\n\t" \
    "v_smfmac_f32_16x16x64_bf16 v[72:75], v[68:71], a[24:31], v10\n\t" \
    "v_smfmac_f32_16x16x64_bf16 v[72:75], v[44:47], a[64:71], v10\n\t" \
    "v_smfmac_f32_16x16x64_bf16 v[76:79], v[36:39], a[104:111], v10\n\t" \
    "v_smfmac_f32_16x16x64_bf16 v[76:79], v[40:43], a[112:119], v10\n\t" \
    "v_smfmac_f32_16x16x64_bf16 v[76:79], v[48:51], a[120:127], v10\n\t" \
    "v_smfmac_f32_16x16x64_bf16 v[76:79], v[52:55], a[128:135], v10\n\t" \
    "v_smfmac_f32_16x16x64_bf16 v[76:79], v[56:59], a[72:79], v10\n\t" \
    "v_smfmac_f32_16x16x64_bf16 v[76:79], v[60:63], a[80:87], v10\n\t" \
    "v_smfmac_f32_16x16x64_bf16 v[76:79], v[64:67], a[88:95], v10\n\t" \
    "v_smfmac_f32_16x16x64_bf16 v[76:79], v[68:71], a[96:103], v10\n\t" \
    "v_smfmac_f32_16x16x64_bf16 v[76:79], v[44:47], a[136:143], v10\n\t" \
    "v_smfmac_f32_16x16x64_bf16 v[80:83], v[36:39], a[176:183], v10\n\t" \
    "v_smfmac_f32_16x16x64_bf16 v[80:83], v[40:43], a[184:191], v10\n\t" \
    "v_smfmac_f32_16x16x64_bf16 v[80:83], v[48:51], a[192:199], v10\n\t" \
    "v_smfmac_f32_16x16x64_bf16 v[80:83], v[52:55], a[200:207], v10\n\t" \
    "v_smfmac_f32_16x16x64_bf16 v[80:83], v[56:59], a[144:151], v10\n\t" \
    "v_smfmac_f32_16x16x64_bf16 v[80:83], v[60:63], a[152:159], v10\n\t" \
    "v_smfmac_f32_16x16x64_bf16 v[80:83], v[64:67], a[160:167], v10\n\t" \
    "v_smfmac_f32_16x16x64_bf16 v[80:83], v[68:71], a[168:175], v10\n\t" \
    "v_smfmac_f32_16x16x64_bf16 v[80:83], v[44:47], a[208:215], v10\n\t" \
    "v_smfmac_f32_16x16x64_bf16 v[84:87], v[36:39], a[248:255], v10\n\t" \
    "v_smfmac_f32_16x16x64_bf16 v[84:87], v[40:43], v[224:231], v10\n\t" \
    "v_smfmac_f32_16x16x64_bf16 v[84:87], v[48:51], v[232:239], v10\n\t" \
    "v_smfmac_f32_16x16x64_bf16 v[84:87], v[52:55], v[240:247], v10\n\t" \
    "v_smfmac_f32_16x16x64_bf16 v[84:87], v[56:59], a[216:223], v10\n\t" \
    "v_smfmac_f32_16x16x64_bf16 v[84:87], v[60:63], a[224:231], v10\n\t" \
    "v_smfmac_f32_16x16x64_bf16 v[84:87], v[64:67], a[232:239], v10\n\t" \
    "v_smfmac_f32_16x16x64_bf16 v[84:87], v[68:71], a[240:247], v10\n\t" \
    "v_smfmac_f32_16x16x64_bf16 v[84:87], v[44:47], v[248:255], v10\n\t" \
    "v_add_f32 v72, v72, v74\n\t" \
    "v_add_f32 v73, v73, v75\n\t" \
    "v_cvt_pk_bf16_f32 v156, v72, v73\n\t" \
    "ds_write_b32 v12, v156 offset:0\n\t" \
    "v_add_f32 v76, v76, v78\n\t" \
    "v_add_f32 v77, v77, v79\n\t" \
    "v_cvt_pk_bf16_f32 v157, v76, v77\n\t" \
    "ds_write_b32 v12, v157 offset:64\n\t" \
    "v_add_f32 v80, v80, v82\n\t" \
    "v_add_f32 v81, v81, v83\n\t" \
    "v_cvt_pk_bf16_f32 v158, v80, v81\n\t" \
    "ds_write_b32 v16, v158 offset:0\n\t" \
    "v_pk_add_f32 v[84:85], v[84:85], v[86:87]\n\t" \
    "v_cvt_pk_bf16_f32 v159, v84, v85\n\t" \
    "ds_write_b32 v16, v159 offset:256\n\t" \
    "s_branch qk_Gback72_%=\n\t" \
    "qk_kf63_%=:\n\t" \
    "global_store_dword v14, v25, s[100:101] sc1\n\t" \
    "s_branch qk_kb64_%=\n\t" \
    "qk_last61_%=:\n\t" \
    "s_mov_b64 exec, s[58:59]\n\t" \
    "global_store_dwordx4 v0, v[140:143], s[40:41] offset:0 sc1\n\t" \
    "global_store_dwordx4 v0, v[144:147], s[40:41] offset:1024 sc1\n\t" \
    "global_store_dwordx4 v0, v[148:151], s[40:41] offset:2048 sc1\n\t" \
    "global_store_dwordx4 v0, v[152:155], s[40:41] offset:3072 sc1\n\t" \
    "s_mov_b64 exec, -1\n\t" \
    "s_endpgm\n\t" \
    "qk_loop16_%=:\n\t" \
    "s_mov_b32 s38, 0x1b8d00\n\t" \
    "s_mov_b32 s39, 0x0\n\t" \
    "v_mov_b32 v72, 0\n\t" \
    "v_mov_b32 v73, 0\n\t" \
    "v_mov_b32 v74, 0\n\t" \
    "v_mov_b32 v75, 0\n\t" \
    "v_mov_b32 v76, 0\n\t" \
    "v_mov_b32 v77, 0\n\t" \
    "v_mov_b32 v78, 0\n\t" \
    "v_mov_b32 v79, 0\n\t" \
    "v_mov_b32 v80, 0\n\t" \
    "v_mov_b32 v81, 0\n\t" \
    "v_mov_b32 v82, 0\n\t" \
    "v_mov_b32 v83, 0\n\t" \
    "v_mov_b32 v84, 0\n\t" \
    "v_mov_b32 v85, 0\n\t" \
    "v_mov_b32 v86, 0\n\t" \
    "v_mov_b32 v87, 0\n\t" \
    "s_add_u32 s69, s65, 0\n\t" \
    "s_and_b32 s69, s69, 7\n\t" \
    "s_lshl_b32 s69, s69, 18\n\t" \
    "s_add_u32 s69, s69, s62\n\t" \
    "s_add_u32 s46, s36, s69\n\t" \
    "s_addc_u32 s47, s37, 0\n\t" \
    "s_bitcmp0_b32 s65, 0\n\t" \
    "s_cselect_b32 s69, 16384, 0\n\t" \
    "v_xor_b32 v1, s69, v1\n\t" \
    "v_xor_b32 v2, s69, v2\n\t" \
    "v_xor_b32 v23, s69, v23\n\t" \
    "s_lshr_b64 s[38:39], s[38:39], 4\n\t" \
    "s_and_b32 s97, s38, 15\n\t" \
    "s_lshl_b32 s97, s97, 6\n\t" \
    "s_and_b32 s69, s65, 7\n\t" \
    "s_lshl_b32 s69, s69, 18\n\t" \
    "s_add_u32 s69, s69, s63\n\t" \
    "s_add_u32 s52, s36, s69\n\t" \
    "s_addc_u32 s53, s37, 0\n\t" \
    "s_sub_u32 s70, s79, s74\n\t" \
    "s_max_i32 s70, s70, 0\n\t" \
    "s_min_i32 s70, s70, 16000\n\t" \
    "s_lshl_b32 s70, s70, 1\n\t" \
    "s_mul_hi_u32 s70, s70, s77\n\t" \
    "s_add_u32 s70, s70, s73\n\t" \
    ".p2align 6\n\t" \
    "s_nop 0\n\t" \
    "s_nop 0\n\t" \
    "s_nop 0\n\t" \
    "s_nop 0\n\t" \
    "s_nop 0\n\t" \
    "s_nop 0\n\t" \
    "qk_go80_%=:\n\t" \
    "s_getpc_b64 s[30:31]\n\t" \
    "qk_Wp82_%=:\n\t" \
    "s_add_u32 s30, s30, qk_We83_%=-qk_Wp82_%=\n\t" \
    "s_addc_u32 s31, s31, 0\n\t" \
    "s_sub_u32 s70, s70, 96\n\t" \
    "qk_W81_%=:\n\t" \
    "s_memtime s[56:57]\n\t" \
    "s_waitcnt lgkmcnt(0)\n\t" \
    "s_sub_u32 s69, s70, s56\n\t" \
    "s_cmp_gt_i32 s69, 80\n\t" \
    "s_cbranch_scc1 qk_W81_%=\n\t" \
    "s_max_i32 s69, s69, 0\n\t" \
    "s_and_b32 s69, s69, -4\n\t" \
    "s_sub_u32 s30, s30, s69\n\t" \
    "s_subb_u32 s31, s31, 0\n\t" \
    "s_setpc_b64 s[30:31]\n\t" \
    "s_nop 0\n\t" \
    "s_nop 0\n\t" \
    "s_nop 0\n\t" \
    "s_nop 0\n\t" \
    "s_nop 0\n\t" \
    "s_nop 0\n\t" \
    "s_nop 0\n\t" \
    "s_nop 0\n\t" \
    "s_nop 0\n\t" \
    "s_nop 0\n\t" \
    "s_nop 0\n\t" \
    "s_nop 0\n\t" \
    "s_nop 0\n\t" \
    "s_nop 0\n\t" \
    "s_nop 0\n\t" \
    "s_nop 0\n\t" \
    "s_nop 0\n\t" \
    "s_nop 0\n\t" \
    "s_nop 0\n\t" \
    "s_nop 0\n\t" \
    "qk_We83_%=:\n\t" \
    "qk_G84_%=:\n\t" \
    "global_load_dwordx4 v[36:39], v1, s[46:47] sc1\n\t" \
    "global_load_dword v161, v2, s[46:47] sc1\n\t" \
    "global_load_dwordx4 v[40:43], v23, s[46:47] sc1\n\t" \
    "s_mul_hi_u32 s69, s84, s78\n\t" \
    "s_add_u32 s33, s74, s69\n\t" \
    "s_add_u32 s33, s33, s97\n\t" \
    "s_lshr_b32 s69, s90, 16\n\t" \
    "s_add_u32 s34, s33, s69\n\t" \
    "s_and_b32 s69, s90, 0xffff\n\t" \
    "s_sub_u32 s35, s80, s74\n\t" \
    "s_sub_u32 s35, s35, s69\n\t" \
    "s_add_u32 s99, s35, 240\n\t" \
    "s_add_u32 s98, s33, 448\n\t" \
    "s_sub_u32 s96, s79, s74\n\t" \
    "s_cmp_le_u32 s65, 0\n\t" \
    "s_cselect_b32 s69, 112, 0\n\t" \
    "s_add_u32 s69, s69, 448\n\t" \
    "s_add_u32 s96, s96, s69\n\t" \
    "s_max_i32 s35, s35, s96\n\t" \
    "s_add_u32 s99, s35, 716\n\t" \
    "s_mul_i32 s69, s35, 10\n\t" \
    "s_ashr_i32 s69, s69, 4\n\t" \
    "s_sub_u32 s69, s33, s69\n\t" \
    "v_lshrrev_b32_e64 v163, 1, s69\n\t" \
    "s_waitcnt vmcnt(2)\n\t" \
    "qk_Garr89_%=:\n\t" \
    "ds_write_b128 v3, v[36:39]\n\t" \
    "v_smfmac_f32_16x16x64_bf16 v[72:75], v[36:39], a[48:55], v10\n\t" \
    "s_waitcnt vmcnt(1)\n\t" \
    "ds_write_b32 v22, v161 offset:20224\n\t" \
    "v_smfmac_f32_16x16x64_bf16 v[76:79], v[36:39], a[120:127], v10\n\t" \
    "s_waitcnt vmcnt(0)\n\t" \
    "ds_write_b128 v3, v[40:43] offset:1024\n\t" \
    "v_max3_u32 v160, v36, v40, v161\n\t" \
    "v_cmp_eq_u32 vcc, -1, v160\n\t" \
    "v_smfmac_f32_16x16x64_bf16 v[80:83], v[36:39], a[192:199], v10\n\t" \
    "v_smfmac_f32_16x16x64_bf16 v[84:87], v[36:39], v[232:239], v10\n\t" \
    "v_smfmac_f32_16x16x64_bf16 v[72:75], v[40:43], a[56:63], v10\n\t" \
    "s_cbranch_vccnz qk_Gbad85_%=\n\t" \
    "qk_Gc86_%=:\n\t" \
    "s_waitcnt lgkmcnt(0)\n\t" \
    "s_barrier\n\t" \
    "s_memtime s[56:57]\n\t" \
    "ds_read_b128 v[48:51], v4\n\t" \
    "ds_read_b128 v[52:55], v5\n\t" \
    "v_smfmac_f32_16x16x64_bf16 v[76:79], v[40:43], a[128:135], v10\n\t" \
    "ds_read_b128 v[56:59], v6\n\t" \
    "ds_read_b128 v[60:63], v7\n\t" \
    "v_smfmac_f32_16x16x64_bf16 v[80:83], v[40:43], a[200:207], v10\n\t" \
    "ds_read_b128 v[64:67], v8\n\t" \
    "ds_read_b128 v[68:71], v9\n\t" \
    "v_smfmac_f32_16x16x64_bf16 v[84:87], v[40:43], v[240:247], v10\n\t" \
    "ds_read_b128 v[44:47], v0 offset:19456\n\t" \
    "s_waitcnt lgkmcnt(6)\n\t" \
    "v_smfmac_f32_16x16x64_bf16 v[72:75], v[48:51], a[0:7], v10\n\t" \
    "s_sub_u32 s69, s56, s73\n\t" \
    "s_mul_hi_u32 s69, s69, s78\n\t" \
    "s_waitcnt lgkmcnt(5)\n\t" \
    "v_smfmac_f32_16x16x64_bf16 v[72:75], v[52:55], a[8:15], v10\n\t" \
    "s_max_i32 s69, s69, s96\n\t" \
    "s_max_i32 s70, s69, s35\n\t" \
    "s_waitcnt lgkmcnt(4)\n\t" \
    "v_smfmac_f32_16x16x64_bf16 v[72:75], v[56:59], a[16:23], v10\n\t" \
    "s_min_i32 s70, s70, s99\n\t" \
    "s_mul_i32 s70, s70, 10\n\t" \
    "s_waitcnt lgkmcnt(3)\n\t" \
    "v_smfmac_f32_16x16x64_bf16 v[72:75], v[60:63], a[24:31], v10\n\t" \
    "s_lshl4_add_u32 s70, s69, s70\n\t" \
    "s_ashr_i32 s70, s70, 5\n\t" \
    "v_add_lshl_u32 v162, s70, v163, 1\n\t" \
    "s_waitcnt lgkmcnt(2)\n\t" \
    "v_smfmac_f32_16x16x64_bf16 v[72:75], v[64:67], a[32:39], v10\n\t" \
    "global_store_dword v21, v162, s[52:53] sc1\n\t" \
    "s_waitcnt lgkmcnt(1)\n\t" \
    "v_smfmac_f32_16x16x64_bf16 v[72:75], v[68:71], a[40:47], v10\n\t" \
    "s_waitcnt lgkmcnt(0)\n\t" \
    "v_smfmac_f32_16x16x64_bf16 v[72:75], v[44:47], a[64:71], v10\n\t" \
    "s_nop 2\n\t" \
    "v_smfmac_f32_16x16x64_bf16 v[76:79], v[48:51], a[72:79], v10\n\t" \
    "s_nop 2\n\t" \
    "v_smfmac_f32_16x16x64_bf16 v[76:79], v[52:55], a[80:87], v10\n\t" \
    "v_add_f32 v72, v72, v74\n\t" \
    "v_smfmac_f32_16x16x64_bf16 v[76:79], v[56:59], a[88:95], v10\n\t" \
    "v_add_f32 v73, v73, v75\n\t" \
    "v_smfmac_f32_16x16x64_bf16 v[76:79], v[60:63], a[96:103], v10\n\t" \
    "v_cvt_pk_bf16_f32 v156, v72, v73\n\t" \
    "v_smfmac_f32_16x16x64_bf16 v[76:79], v[64:67], a[104:111], v10\n\t" \
    "ds_write_b32 v12, v156 offset:0\n\t" \
    "v_smfmac_f32_16x16x64_bf16 v[76:79], v[68:71], a[112:119], v10\n\t" \
    "v_smfmac_f32_16x16x64_bf16 v[76:79], v[44:47], a[136:143], v10\n\t" \
    "s_nop 2\n\t" \
    "v_smfmac_f32_16x16x64_bf16 v[80:83], v[48:51], a[144:151], v10\n\t" \
    "s_nop 2\n\t" \
    "v_smfmac_f32_16x16x64_bf16 v[80:83], v[52:55], a[152:159], v10\n\t" \
    "v_add_f32 v76, v76, v78\n\t" \
    "v_smfmac_f32_16x16x64_bf16 v[80:83], v[56:59], a[160:167], v10\n\t" \
    "v_add_f32 v77, v77, v79\n\t" \
    "v_smfmac_f32_16x16x64_bf16 v[80:83], v[60:63], a[168:175], v10\n\t" \
    "v_cvt_pk_bf16_f32 v157, v76, v77\n\t" \
    "v_smfmac_f32_16x16x64_bf16 v[80:83], v[64:67], a[176:183], v10\n\t" \
    "ds_write_b32 v12, v157 offset:64\n\t" \
    "v_smfmac_f32_16x16x64_bf16 v[80:83], v[68:71], a[184:191], v10\n\t" \
    "v_smfmac_f32_16x16x64_bf16 v[80:83], v[44:47], a[208:215], v10\n\t" \
    "s_nop 2\n\t" \
    "v_smfmac_f32_16x16x64_bf16 v[84:87], v[48:51], a[216:223], v10\n\t" \
    "s_nop 2\n\t" \
    "v_smfmac_f32_16x16x64_bf16 v[84:87], v[52:55], a[224:231], v10\n\t" \
    "v_add_f32 v80, v80, v82\n\t" \
    "v_smfmac_f32_16x16x64_bf16 v[84:87], v[56:59], a[232:239], v10\n\t" \
    "v_add_f32 v81, v81, v83\n\t" \
    "v_smfmac_f32_16x16x64_bf16 v[84:87], v[60:63], a[240:247], v10\n\t" \
    "v_cvt_pk_bf16_f32 v158, v80, v81\n\t" \
    "v_smfmac_f32_16x16x64_bf16 v[84:87], v[64:67], a[248:255], v10\n\t" \
    "ds_write_b32 v16, v158 offset:0\n\t" \
    "v_smfmac_f32_16x16x64_bf16 v[84:87], v[68:71], v[224:231], v10\n\t" \
    "v_smfmac_f32_16x16x64_bf16 v[84:87], v[44:47], v[248:255], v10\n\t" \
    "s_nop 6\n\t" \
    "v_cmp_u_f32 vcc, v72, v73\n\t" \
    "v_pk_add_f32 v[84:85], v[84:85], v[86:87]\n\t" \
    "v_cvt_pk_bf16_f32 v159, v84, v85\n\t" \
    "ds_write_b32 v16, v159 offset:256\n\t" \
    "s_cbranch_vccnz qk_Gslow87_%=\n\t" \
    "qk_Gback88_%=:\n\t" \
    "s_waitcnt lgkmcnt(0)\n\t" \
    "s_barrier\n\t" \
    "s_cmp_eq_u32 s65, 63\n\t" \
    "s_cbranch_scc1 qk_last79_%=\n\t" \
    "s_cmp_lg_u32 s65, 0\n\t" \
    "s_cbranch_scc1 qk_ng90_%=\n\t" \
    "v_mov_b32 v21, v33\n\t" \
    "qk_ng90_%=:\n\t" \
    "s_sub_u32 s70, s80, s74\n\t" \
    "s_max_i32 s70, s70, 0\n\t" \
    "s_min_i32 s70, s70, 16000\n\t" \
    "s_lshl_b32 s70, s70, 1\n\t" \
    "s_mul_hi_u32 s70, s70, s77\n\t" \
    "s_add_u32 s70, s70, s73\n\t" \
    "qk_W91_%=:\n\t" \
    "s_memtime s[56:57]\n\t" \
    "s_waitcnt lgkmcnt(0)\n\t" \
    "s_sub_u32 s69, s70, s56\n\t" \
    "s_cmp_gt_i32 s69, 24\n\t" \
    "s_cbranch_scc1 qk_W91_%=\n\t" \
    "global_load_dword v160, v20, s[52:53] sc1\n\t" \
    "s_waitcnt lgkmcnt(0)\n\t" \
    "s_mov_b32 s70, 116\n\t" \
    "qk_E95_%=:\n\t" \
    "s_memtime s[56:57]\n\t" \
    "s_memrealtime s[2:3]\n\t" \
    "s_nop 0\n\t" \
    "s_memrealtime s[4:5]\n\t" \
    "s_nop 0\n\t" \
    "s_memrealtime s[6:7]\n\t" \
    "s_nop 0\n\t" \
    "s_memrealtime s[8:9]\n\t" \
    "s_nop 0\n\t" \
    "s_memrealtime s[10:11]\n\t" \
    "s_nop 0\n\t" \
    "s_memrealtime s[12:13]\n\t" \
    "s_nop 0\n\t" \
    "s_memrealtime s[14:15]\n\t" \
    "s_nop 0\n\t" \
    "s_memrealtime s[16:17]\n\t" \
    "s_nop 0\n\t" \
    "s_memrealtime s[18:19]\n\t" \
    "s_nop 0\n\t" \
    "s_memrealtime s[20:21]\n\t" \
    "s_nop 0\n\t" \
    "s_memrealtime s[22:23]\n\t" \
    "s_nop 0\n\t" \
    "s_memrealtime s[24:25]\n\t" \
    "s_nop 0\n\t" \
    "s_memrealtime s[26:27]\n\t" \
    "s_nop 0\n\t" \
    "s_memrealtime s[28:29]\n\t" \
    "s_memtime s[54:55]\n\t" \
    "s_add_u32 s70, s70, 8\n\t" \
    "s_waitcnt lgkmcnt(0)\n\t" \
    "s_sub_u32 s69, s54, s56\n\t" \
    "s_cmp_gt_u32 s69, s70\n\t" \
    "s_cbranch_scc1 qk_E95_%=\n\t" \
    "s_add_u32 s2, s2, s4\n\t" \
    "s_add_u32 s6, s6, s8\n\t" \
    "s_add_u32 s10, s10, s12\n\t" \
    "s_add_u32 s14, s14, s16\n\t" \
    "s_add_u32 s18, s18, s20\n\t" \
    "s_add_u32 s22, s22, s24\n\t" \
    "s_add_u32 s2, s2, s6\n\t" \
    "s_add_u32 s10, s10, s14\n\t" \
    "s_add_u32 s18, s18, s22\n\t" \
    "s_add_u32 s2, s2, s26\n\t" \
    "s_add_u32 s10, s10, s18\n\t" \
    "s_add_u32 s2, s2, s10\n\t" \
    "s_mul_i32 s70, s28, 13\n\t" \
    "s_sub_u32 s70, s70, s2\n\t" \
    "s_min_u32 s70, s70, 52\n\t" \
    "s_mul_i32 s69, s69, s70\n\t" \
    "s_mul_hi_u32 s69, s69, 76695844\n\t" \
    "s_mov_b32 s75, s73\n\t" \
    "s_mov_b32 s76, s74\n\t" \
    "s_add_u32 s73, s56, s69\n\t" \
    "s_lshl_b32 s74, s28, 4\n\t" \
    "s_sub_u32 s69, s73, s75\n\t" \
    "s_sub_u32 s70, s74, s76\n\t" \
    "s_sub_u32 s71, s70, 1600\n\t" \
    "s_cmp_lt_u32 s71, 400000\n\t" \
    "s_cbranch_scc0 qk_K96_%=\n\t" \
    "v_cvt_f32_u32 v168, s69\n\t" \
    "v_cvt_f32_u32 v169, s70\n\t" \
    "v_rcp_f32 v170, v168\n\t" \
    "v_rcp_f32 v171, v169\n\t" \
    "v_mul_f32 v170, v170, v169\n\t" \
    "v_mul_f32 v171, v171, v168\n\t" \
    "v_mul_f32 v170, 0x4f800000, v170\n\t" \
    "v_mul_f32 v171, 0x4f000000, v171\n\t" \
    "v_cvt_u32_f32 v170, v170\n\t" \
    "v_cvt_u32_f32 v171, v171\n\t" \
    "v_readfirstlane_b32 s69, v170\n\t" \
    "v_readfirstlane_b32 s70, v171\n\t" \
    "s_nop 1\n\t" \
    "s_min_u32 s71, s65, 1\n\t" \
    "s_sub_u32 s69, s69, s78\n\t" \
    "s_ashr_i32 s69, s69, s71\n\t" \
    "s_add_u32 s78, s78, s69\n\t" \
    "s_sub_u32 s70, s70, s77\n\t" \
    "s_ashr_i32 s70, s70, s71\n\t" \
    "s_add_u32 s77, s77, s70\n\t" \
    "qk_K96_%=:\n\t" \
    "s_add_u32 s65, s65, 1\n\t" \
    "v_mov_b32 v72, 0\n\t" \
    "v_mov_b32 v73, 0\n\t" \
    "v_mov_b32 v74, 0\n\t" \
    "v_mov_b32 v75, 0\n\t" \
    "v_mov_b32 v76, 0\n\t" \
    "v_mov_b32 v77, 0\n\t" \
    "v_mov_b32 v78, 0\n\t" \
    "v_mov_b32 v79, 0\n\t" \
    "v_mov_b32 v80, 0\n\t" \
    "v_mov_b32 v81, 0\n\t" \
    "v_mov_b32 v82, 0\n\t" \
    "v_mov_b32 v83, 0\n\t" \
    "v_mov_b32 v84, 0\n\t" \
    "v_mov_b32 v85, 0\n\t" \
    "v_mov_b32 v86, 0\n\t" \
    "v_mov_b32 v87, 0\n\t" \
    "s_add_u32 s69, s65, 0\n\t" \
    "s_and_b32 s69, s69, 7\n\t" \
    "s_lshl_b32 s69, s69, 18\n\t" \
    "s_add_u32 s69, s69, s62\n\t" \
    "s_add_u32 s46, s36, s69\n\t" \
    "s_addc_u32 s47, s37, 0\n\t" \
    "s_bitcmp0_b32 s65, 0\n\t" \
    "s_cselect_b32 s69, 16384, 0\n\t" \
    "v_xor_b32 v1, s69, v1\n\t" \
    "v_xor_b32 v2, s69, v2\n\t" \
    "v_xor_b32 v23, s69, v23\n\t" \
    "s_lshr_b64 s[38:39], s[38:39], 4\n\t" \
    "s_and_b32 s97, s38, 15\n\t" \
    "s_lshl_b32 s97, s97, 6\n\t" \
    "s_waitcnt vmcnt(0)\n\t" \
    "v_cmp_eq_u32 vcc, -1, v160\n\t" \
    "s_cbranch_vccz qk_Hok93_%=\n\t" \
    "qk_H92_%=:\n\t" \
    "global_load_dword v160, v20, s[52:53] sc1\n\t" \
    "s_waitcnt vmcnt(0)\n\t" \
    "v_cmp_eq_u32 vcc, -1, v160\n\t" \
    "s_cbranch_vccz qk_Hc94_%=\n\t" \
    "s_cmp_eq_u32 s68, 0\n\t" \
    "s_cbranch_scc1 qk_F97_%=\n\t" \
    "s_sub_u32 s68, s68, 1\n\t" \
    "s_sleep 2\n\t" \
    "s_branch qk_H92_%=\n\t" \
    "qk_F97_%=:\n\t" \
    "v_mov_b32 v161, 5767424\n\t" \
    "v_mov_b32 v162, 5\n\t" \
    "global_store_dword v161, v162, s[36:37] sc1\n\t" \
    "s_waitcnt vmcnt(0)\n\t" \
    "s_branch qk_Hc94_%=\n\t" \
    "qk_Hc94_%=:\n\t" \
    "s_memtime s[56:57]\n\t" \
    "s_waitcnt lgkmcnt(0)\n\t" \
    "s_sub_u32 s69, s56, s73\n\t" \
    "s_cmp_lt_u32 s69, 3000\n\t" \
    "s_cbranch_scc1 qk_St98_%=\n\t" \
    "s_waitcnt lgkmcnt(0)\n\t" \
    "s_mov_b32 s70, 116\n\t" \
    "qk_E99_%=:\n\t" \
    "s_memtime s[56:57]\n\t" \
    "s_memrealtime s[2:3]\n\t" \
    "s_nop 0\n\t" \
    "s_memrealtime s[4:5]\n\t" \
    "s_nop 0\n\t" \
    "s_memrealtime s[6:7]\n\t" \
    "s_nop 0\n\t" \
    "s_memrealtime s[8:9]\n\t" \
    "s_nop 0\n\t" \
    "s_memrealtime s[10:11]\n\t" \
    "s_nop 0\n\t" \
    "s_memrealtime s[12:13]\n\t" \
    "s_nop 0\n\t" \
    "s_memrealtime s[14:15]\n\t" \
    "s_nop 0\n\t" \
    "s_memrealtime s[16:17]\n\t" \
    "s_nop 0\n\t" \
    "s_memrealtime s[18:19]\n\t" \
    "s_nop 0\n\t" \
    "s_memrealtime s[20:21]\n\t" \
    "s_nop 0\n\t" \
    "s_memrealtime s[22:23]\n\t" \
    "s_nop 0\n\t" \
    "s_memrealtime s[24:25]\n\t" \
    "s_nop 0\n\t" \
    "s_memrealtime s[26:27]\n\t" \
    "s_nop 0\n\t" \
    "s_memrealtime s[28:29]\n\t" \
    "s_memtime s[54:55]\n\t" \
    "s_add_u32 s70, s70, 8\n\t" \
    "s_waitcnt lgkmcnt(0)\n\t" \
    "s_sub_u32 s69, s54, s56\n\t" \
    "s_cmp_gt_u32 s69, s70\n\t" \
    "s_cbranch_scc1 qk_E99_%=\n\t" \
    "s_add_u32 s2, s2, s4\n\t" \
    "s_add_u32 s6, s6, s8\n\t" \
    "s_add_u32 s10, s10, s12\n\t" \
    "s_add_u32 s14, s14, s16\n\t" \
    "s_add_u32 s18, s18, s20\n\t" \
    "s_add_u32 s22, s22, s24\n\t" \
    "s_add_u32 s2, s2, s6\n\t" \
    "s_add_u32 s10, s10, s14\n\t" \
    "s_add_u32 s18, s18, s22\n\t" \
    "s_add_u32 s2, s2, s26\n\t" \
    "s_add_u32 s10, s10, s18\n\t" \
    "s_add_u32 s2, s2, s10\n\t" \
    "s_mul_i32 s70, s28, 13\n\t" \
    "s_sub_u32 s70, s70, s2\n\t" \
    "s_min_u32 s70, s70, 52\n\t" \
    "s_mul_i32 s69, s69, s70\n\t" \
    "s_mul_hi_u32 s69, s69, 76695844\n\t" \
    "s_mov_b32 s75, s73\n\t" \
    "s_mov_b32 s76, s74\n\t" \
    "s_add_u32 s73, s56, s69\n\t" \
    "s_lshl_b32 s74, s28, 4\n\t" \
    "qk_St98_%=:\n\t" \
    "qk_Hok93_%=:\n\t" \
    "v_max_u32_dpp v160, v160, v160 row_ror:1 row_mask:0xf bank_mask:0xf\n\t" \
    "s_nop 1\n\t" \
    "v_max_u32_dpp v160, v160, v160 row_ror:2 row_mask:0xf bank_mask:0xf\n\t" \
    "s_nop 1\n\t" \
    "v_max_u32_dpp v160, v160, v160 row_ror:4 row_mask:0xf bank_mask:0xf\n\t" \
    "s_nop 1\n\t" \
    "v_max_u32_dpp v160, v160, v160 row_ror:8 row_mask:0xf bank_mask:0xf\n\t" \
    "s_nop 0\n\t" \
    "v_readlane_b32 s69, v160, 0\n\t" \
    "v_readlane_b32 s71, v160, 16\n\t" \
    "s_nop 1\n\t" \
    "s_max_u32 s81, s69, s71\n\t" \
    "s_mul_hi_u32 s69, s85, s78\n\t" \
    "s_add_u32 s79, s81, s69\n\t" \
    "s_add_u32 s79, s79, s87\n\t" \
    "s_cmp_lt_u32 s65, 6\n\t" \
    "s_cselect_b32 s69, 48, 0\n\t" \
    "s_add_u32 s79, s79, s69\n\t" \
    "s_add_u32 s80, s79, s88\n\t" \
    "s_sub_u32 s70, s79, s74\n\t" \
    "s_max_i32 s70, s70, 0\n\t" \
    "s_min_i32 s70, s70, 16000\n\t" \
    "s_lshl_b32 s70, s70, 1\n\t" \
    "s_mul_hi_u32 s70, s70, s77\n\t" \
    "s_add_u32 s70, s70, s73\n\t" \
    "v_mov_b32 v168, s70\n\t" \
    "ds_write_b32 v11, v168 offset:20480\n\t" \
    "s_and_b32 s69, s65, 7\n\t" \
    "s_lshl_b32 s69, s69, 18\n\t" \
    "s_add_u32 s69, s69, s63\n\t" \
    "s_add_u32 s52, s36, s69\n\t" \
    "s_addc_u32 s53, s37, 0\n\t" \
    "s_waitcnt lgkmcnt(0)\n\t" \
    "s_barrier\n\t" \
    "ds_write_b32 v12, v31 offset:0\n\t" \
    "ds_write_b32 v12, v31 offset:64\n\t" \
    "s_branch qk_go80_%=\n\t" \
    "qk_Gbad85_%=:\n\t" \
    "s_waitcnt vmcnt(0) lgkmcnt(0)\n\t" \
    "s_nop 7\n\t" \
    "v_mov_b32 v72, 0\n\t" \
    "v_mov_b32 v73, 0\n\t" \
    "v_mov_b32 v74, 0\n\t" \
    "v_mov_b32 v75, 0\n\t" \
    "v_mov_b32 v76, 0\n\t" \
    "v_mov_b32 v77, 0\n\t" \
    "v_mov_b32 v78, 0\n\t" \
    "v_mov_b32 v79, 0\n\t" \
    "v_mov_b32 v80, 0\n\t" \
    "v_mov_b32 v81, 0\n\t" \
    "v_mov_b32 v82, 0\n\t" \
    "v_mov_b32 v83, 0\n\t" \
    "v_mov_b32 v84, 0\n\t" \
    "v_mov_b32 v85, 0\n\t" \
    "v_mov_b32 v86, 0\n\t" \
    "v_mov_b32 v87, 0\n\t" \
    "s_cmp_eq_u32 s68, 0\n\t" \
    "s_cbranch_scc1 qk_F100_%=\n\t" \
    "s_sub_u32 s68, s68, 1\n\t" \
    "s_branch qk_G84_%=\n\t" \
    "qk_F100_%=:\n\t" \
    "v_mov_b32 v160, 5767424\n\t" \
    "v_mov_b32 v161, 1\n\t" \
    "global_store_dword v160, v161, s[36:37] sc1\n\t" \
    "s_waitcnt vmcnt(0)\n\t" \
    "s_branch qk_Gc86_%=\n\t" \
    "qk_Gslow87_%=:\n\t" \
    "v_max_u32 v160, v36, v37\n\t" \
    "v_max3_u32 v160, v160, v38, v39\n\t" \
    "v_max3_u32 v160, v160, v40, v41\n\t" \
    "v_max3_u32 v160, v160, v42, v43\n\t" \
    "v_max3_u32 v160, v160, v44, v45\n\t" \
    "v_max3_u32 v160, v160, v46, v47\n\t" \
    "v_max3_u32 v160, v160, v48, v49\n\t" \
    "v_max3_u32 v160, v160, v50, v51\n\t" \
    "v_max3_u32 v160, v160, v52, v53\n\t" \
    "v_max3_u32 v160, v160, v54, v55\n\t" \
    "v_max3_u32 v160, v160, v56, v57\n\t" \
    "v_max3_u32 v160, v160, v58, v59\n\t" \
    "v_max3_u32 v160, v160, v60, v61\n\t" \
    "v_max3_u32 v160, v160, v62, v63\n\t" \
    "v_max3_u32 v160, v160, v64, v65\n\t" \
    "v_max3_u32 v160, v160, v66, v67\n\t" \
    "v_max3_u32 v160, v160, v68, v69\n\t" \
    "v_max3_u32 v160, v160, v70, v71\n\t" \
    "v_cmp_eq_u32 vcc, -1, v160\n\t" \
    "s_cbranch_vccz qk_Gback88_%=\n\t" \
    "s_waitcnt lgkmcnt(0)\n\t" \
    "v_sub_u32 v160, v1, v3\n\t" \
    "s_nop 0\n\t" \
    "v_readfirstlane_b32 s69, v160\n\t" \
    "v_add_u32 v163, 36864, v0\n\t" \
    "s_nop 7\n\t" \
    "s_add_u32 s30, s46, s69\n\t" \
    "s_addc_u32 s31, s47, 0\n\t" \
    "qk_Gag101_%=:\n\t" \
    "global_load_dwordx4 v[36:39], v1, s[46:47] sc1\n\t" \
    "global_load_dwordx4 v[40:43], v23, s[46:47] sc1\n\t" \
    "global_load_dwordx4 v[44:47], v163, s[30:31] sc1\n\t" \
    "global_load_dwordx4 v[48:51], v4, s[30:31] sc1\n\t" \
    "global_load_dwordx4 v[52:55], v5, s[30:31] sc1\n\t" \
    "global_load_dwordx4 v[56:59], v6, s[30:31] sc1\n\t" \
    "global_load_dwordx4 v[60:63], v7, s[30:31] sc1\n\t" \
    "global_load_dwordx4 v[64:67], v8, s[30:31] sc1\n\t" \
    "global_load_dwordx4 v[68:71], v9, s[30:31] sc1\n\t" \
    "s_waitcnt vmcnt(0)\n\t" \
    "v_max_u32 v160, v36, v37\n\t" \
    "v_max3_u32 v160, v160, v38, v39\n\t" \
    "v_max3_u32 v160, v160, v40, v41\n\t" \
    "v_max3_u32 v160, v160, v42, v43\n\t" \
    "v_max3_u32 v160, v160, v44, v45\n\t" \
    "v_max3_u32 v160, v160, v46, v47\n\t" \
    "v_max3_u32 v160, v160, v48, v49\n\t" \
    "v_max3_u32 v160, v160, v50, v51\n\t" \
    "v_max3_u32 v160, v160, v52, v53\n\t" \
    "v_max3_u32 v160, v160, v54, v55\n\t" \
    "v_max3_u32 v160, v160, v56, v57\n\t" \
    "v_max3_u32 v160, v160, v58, v59\n\t" \
    "v_max3_u32 v160, v160, v60, v61\n\t" \
    "v_max3_u32 v160, v160, v62, v63\n\t" \
    "v_max3_u32 v160, v160, v64, v65\n\t" \
    "v_max3_u32 v160, v160, v66, v67\n\t" \
    "v_max3_u32 v160, v160, v68, v69\n\t" \
    "v_max3_u32 v160, v160, v70, v71\n\t" \
    "v_cmp_eq_u32 vcc, -1, v160\n\t" \
    "s_cbranch_vccz qk_Gredo102_%=\n\t" \
    "s_cmp_eq_u32 s68, 0\n\t" \
    "s_cbranch_scc1 qk_F103_%=\n\t" \
    "s_sub_u32 s68, s68, 1\n\t" \
    "s_sleep 1\n\t" \
    "s_branch qk_Gag101_%=\n\t" \
    "qk_F103_%=:\n\t" \
    "v_mov_b32 v160, 5767424\n\t" \
    "v_mov_b32 v161, 2\n\t" \
    "global_store_dword v160, v161, s[36:37] sc1\n\t" \
    "s_waitcnt vmcnt(0)\n\t" \
    "s_branch qk_Gredo102_%=\n\t" \
    "qk_Gredo102_%=:\n\t" \
    "v_mov_b32 v72, 0\n\t" \
    "v_mov_b32 v73, 0\n\t" \
    "v_mov_b32 v74, 0\n\t" \
    "v_mov_b32 v75, 0\n\t" \
    "v_mov_b32 v76, 0\n\t" \
    "v_mov_b32 v77, 0\n\t" \
    "v_mov_b32 v78, 0\n\t" \
    "v_mov_b32 v79, 0\n\t" \
    "v_mov_b32 v80, 0\n\t" \
    "v_mov_b32 v81, 0\n\t" \
    "v_mov_b32 v82, 0\n\t" \
    "v_mov_b32 v83, 0\n\t" \
    "v_mov_b32 v84, 0\n\t" \
    "v_mov_b32 v85, 0\n\t" \
    "v_mov_b32 v86, 0\n\t" \
    "v_mov_b32 v87, 0\n\t" \
    "v_smfmac_f32_16x16x64_bf16 v[72:75], v[36:39], a[48:55], v10\n\t" \
    "v_smfmac_f32_16x16x64_bf16 v[72:75], v[40:43], a[56:63], v10\n\t" \
    "v_smfmac_f32_16x16x64_bf16 v[72:75], v[48:51], a[0:7], v10\n\t" \
    "v_smfmac_f32_16x16x64_bf16 v[72:75], v[52:55], a[8:15], v10\n\t" \
    "v_smfmac_f32_16x16x64_bf16 v[72:75], v[56:59], a[16:23], v10\n\t" \
    "v_smfmac_f32_16x16x64_bf16 v[72:75], v[60:63], a[24:31], v10\n\t" \
    "v_smfmac_f32_16x16x64_bf16 v[72:75], v[64:67], a[32:39], v10\n\t" \
    "v_smfmac_f32_16x16x64_bf16 v[72:75], v[68:71], a[40:47], v10\n\t" \
    "v_smfmac_f32_16x16x64_bf16 v[72:75], v[44:47], a[64:71], v10\n\t" \
    "v_smfmac_f32_16x16x64_bf16 v[76:79], v[36:39], a[120:127], v10\n\t" \
    "v_smfmac_f32_16x16x64_bf16 v[76:79], v[40:43], a[128:135], v10\n\t" \
    "v_smfmac_f32_16x16x64_bf16 v[76:79], v[48:51], a[72:79], v10\n\t" \
    "v_smfmac_f32_16x16x64_bf16 v[76:79], v[52:55], a[80:87], v10\n\t" \
    "v_smfmac_f32_16x16x64_bf16 v[76:79], v[56:59], a[88:95], v10\n\t" \
    "v_smfmac_f32_16x16x64_bf16 v[76:79], v[60:63], a[96:103], v10\n\t" \
    "v_smfmac_f32_16x16x64_bf16 v[76:79], v[64:67], a[104:111], v10\n\t" \
    "v_smfmac_f32_16x16x64_bf16 v[76:79], v[68:71], a[112:119], v10\n\t" \
    "v_smfmac_f32_16x16x64_bf16 v[76:79], v[44:47], a[136:143], v10\n\t" \
    "v_smfmac_f32_16x16x64_bf16 v[80:83], v[36:39], a[192:199], v10\n\t" \
    "v_smfmac_f32_16x16x64_bf16 v[80:83], v[40:43], a[200:207], v10\n\t" \
    "v_smfmac_f32_16x16x64_bf16 v[80:83], v[48:51], a[144:151], v10\n\t" \
    "v_smfmac_f32_16x16x64_bf16 v[80:83], v[52:55], a[152:159], v10\n\t" \
    "v_smfmac_f32_16x16x64_bf16 v[80:83], v[56:59], a[160:167], v10\n\t" \
    "v_smfmac_f32_16x16x64_bf16 v[80:83], v[60:63], a[168:175], v10\n\t" \
    "v_smfmac_f32_16x16x64_bf16 v[80:83], v[64:67], a[176:183], v10\n\t" \
    "v_smfmac_f32_16x16x64_bf16 v[80:83], v[68:71], a[184:191], v10\n\t" \
    "v_smfmac_f32_16x16x64_bf16 v[80:83], v[44:47], a[208:215], v10\n\t" \
    "v_smfmac_f32_16x16x64_bf16 v[84:87], v[36:39], v[232:239], v10\n\t" \
    "v_smfmac_f32_16x16x64_bf16 v[84:87], v[40:43], v[240:247], v10\n\t" \
    "v_smfmac_f32_16x16x64_bf16 v[84:87], v[48:51], a[216:223], v10\n\t" \
    "v_smfmac_f32_16x16x64_bf16 v[84:87], v[52:55], a[224:231], v10\n\t" \
    "v_smfmac_f32_16x16x64_bf16 v[84:87], v[56:59], a[232:239], v10\n\t" \
    "v_smfmac_f32_16x16x64_bf16 v[84:87], v[60:63], a[240:247], v10\n\t" \
    "v_smfmac_f32_16x16x64_bf16 v[84:87], v[64:67], a[248:255], v10\n\t" \
    "v_smfmac_f32_16x16x64_bf16 v[84:87], v[68:71], v[224:231], v10\n\t" \
    "v_smfmac_f32_16x16x64_bf16 v[84:87], v[44:47], v[248:255], v10\n\t" \
    "v_add_f32 v72, v72, v74\n\t" \
    "v_add_f32 v73, v73, v75\n\t" \
    "v_cvt_pk_bf16_f32 v156, v72, v73\n\t" \
    "ds_write_b32 v12, v156 offset:0\n\t" \
    "v_add_f32 v76, v76, v78\n\t" \
    "v_add_f32 v77, v77, v79\n\t" \
    "v_cvt_pk_bf16_f32 v157, v76, v77\n\t" \
    "ds_write_b32 v12, v157 offset:64\n\t" \
    "v_add_f32 v80, v80, v82\n\t" \
    "v_add_f32 v81, v81, v83\n\t" \
    "v_cvt_pk_bf16_f32 v158, v80, v81\n\t" \
    "ds_write_b32 v16, v158 offset:0\n\t" \
    "v_pk_add_f32 v[84:85], v[84:85], v[86:87]\n\t" \
    "v_cvt_pk_bf16_f32 v159, v84, v85\n\t" \
    "ds_write_b32 v16, v159 offset:256\n\t" \
    "s_branch qk_Gback88_%=\n\t" \
    "qk_last79_%=:\n\t" \
    "s_endpgm\n\t" \
    "qk_last_%=:\n\t" \
    ""
#define ASM_V \
    "s_load_dwordx2 s[42:43], %3, 56\n\t" \
    "s_load_dwordx2 s[44:45], %3, 64\n\t" \
    "s_load_dwordx2 s[36:37], %3, 0x58\n\t" \
    "s_load_dwordx2 s[50:51], %3, 0x48\n\t" \
    "s_load_dwordx8 s[84:91], %3, 0\n\t" \
    "s_mov_b32 s66, %4\n\t" \
    "s_mov_b32 s67, %5\n\t" \
    "s_mov_b32 s68, 262144\n\t" \
    "s_mov_b32 s72, 0\n\t" \
    "s_mov_b32 s83, -1\n\t" \
    "s_mov_b32 s65, 0\n\t" \
    "s_lshl_b32 s82, s66, 10\n\t" \
    "s_waitcnt lgkmcnt(0)\n\t" \
    "s_add_u32 s46, s42, 2097152\n\t" \
    "s_addc_u32 s47, s43, 0\n\t" \
    "s_add_u32 m0, s82, 65536\n\t" \
    "s_nop 0\n\t" \
    "global_load_lds_dwordx4 v200, s[46:47]\n\t" \
    "s_add_u32 s46, s42, 2228224\n\t" \
    "s_addc_u32 s47, s43, 0\n\t" \
    "s_add_u32 m0, s82, 69632\n\t" \
    "s_nop 0\n\t" \
    "global_load_lds_dwordx4 v200, s[46:47]\n\t" \
    "s_add_u32 s46, s42, 2359296\n\t" \
    "s_addc_u32 s47, s43, 0\n\t" \
    "s_add_u32 m0, s82, 73728\n\t" \
    "s_nop 0\n\t" \
    "global_load_lds_dwordx4 v200, s[46:47]\n\t" \
    "s_add_u32 s46, s42, 2490368\n\t" \
    "s_addc_u32 s47, s43, 0\n\t" \
    "s_add_u32 m0, s82, 77824\n\t" \
    "s_nop 0\n\t" \
    "global_load_lds_dwordx4 v200, s[46:47]\n\t" \
    "s_add_u32 s46, s42, 2621440\n\t" \
    "s_addc_u32 s47, s43, 0\n\t" \
    "s_add_u32 m0, s82, 81920\n\t" \
    "s_nop 0\n\t" \
    "global_load_lds_dwordx4 v200, s[46:47]\n\t" \
    "s_add_u32 s46, s42, 2752512\n\t" \
    "s_addc_u32 s47, s43, 0\n\t" \
    "s_add_u32 m0, s82, 86016\n\t" \
    "s_nop 0\n\t" \
    "global_load_lds_dwordx4 v200, s[46:47]\n\t" \
    "s_add_u32 s46, s42, 2883584\n\t" \
    "s_addc_u32 s47, s43, 0\n\t" \
    "s_add_u32 m0, s82, 90112\n\t" \
    "s_nop 0\n\t" \
    "global_load_lds_dwordx4 v200, s[46:47]\n\t" \
    "s_add_u32 s46, s42, 3014656\n\t" \
    "s_addc_u32 s47, s43, 0\n\t" \
    "s_add_u32 m0, s82, 94208\n\t" \
    "s_nop 0\n\t" \
    "global_load_lds_dwordx4 v200, s[46:47]\n\t" \
    "s_add_u32 s46, s42, 3145728\n\t" \
    "s_addc_u32 s47, s43, 0\n\t" \
    "s_add_u32 m0, s82, 98304\n\t" \
    "s_nop 0\n\t" \
    "global_load_lds_dwordx4 v200, s[46:47]\n\t" \
    "s_add_u32 s46, s42, 3276800\n\t" \
    "s_addc_u32 s47, s43, 0\n\t" \
    "s_add_u32 m0, s82, 102400\n\t" \
    "s_nop 0\n\t" \
    "global_load_lds_dwordx4 v200, s[46:47]\n\t" \
    "s_add_u32 s46, s42, 3407872\n\t" \
    "s_addc_u32 s47, s43, 0\n\t" \
    "s_add_u32 m0, s82, 106496\n\t" \
    "s_nop 0\n\t" \
    "global_load_lds_dwordx4 v200, s[46:47]\n\t" \
    "s_add_u32 s46, s42, 3538944\n\t" \
    "s_addc_u32 s47, s43, 0\n\t" \
    "s_add_u32 m0, s82, 110592\n\t" \
    "s_nop 0\n\t" \
    "global_load_lds_dwordx4 v200, s[46:47]\n\t" \
    "s_add_u32 s46, s42, 3670016\n\t" \
    "s_addc_u32 s47, s43, 0\n\t" \
    "s_add_u32 m0, s82, 114688\n\t" \
    "s_nop 0\n\t" \
    "global_load_lds_dwordx4 v200, s[46:47]\n\t" \
    "s_add_u32 s46, s42, 3801088\n\t" \
    "s_addc_u32 s47, s43, 0\n\t" \
    "s_add_u32 m0, s82, 118784\n\t" \
    "s_nop 0\n\t" \
    "global_load_lds_dwordx4 v200, s[46:47]\n\t" \
    "s_add_u32 s46, s42, 3932160\n\t" \
    "s_addc_u32 s47, s43, 0\n\t" \
    "s_add_u32 m0, s82, 122880\n\t" \
    "s_nop 0\n\t" \
    "global_load_lds_dwordx4 v200, s[46:47]\n\t" \
    "s_add_u32 s46, s42, 4063232\n\t" \
    "s_addc_u32 s47, s43, 0\n\t" \
    "s_add_u32 m0, s82, 126976\n\t" \
    "s_nop 0\n\t" \
    "global_load_lds_dwordx4 v200, s[46:47]\n\t" \
    "s_add_u32 s46, s42, 4194304\n\t" \
    "s_addc_u32 s47, s43, 0\n\t" \
    "s_add_u32 m0, s82, 131072\n\t" \
    "s_nop 0\n\t" \
    "global_load_lds_dwordx4 v200, s[46:47]\n\t" \
    "s_add_u32 s46, s42, 4325376\n\t" \
    "s_addc_u32 s47, s43, 0\n\t" \
    "s_add_u32 m0, s82, 135168\n\t" \
    "s_nop 0\n\t" \
    "global_load_lds_dwordx4 v200, s[46:47]\n\t" \
    "s_add_u32 s46, s42, 4456448\n\t" \
    "s_addc_u32 s47, s43, 0\n\t" \
    "s_add_u32 m0, s82, 139264\n\t" \
    "s_nop 0\n\t" \
    "global_load_lds_dwordx4 v200, s[46:47]\n\t" \
    "s_add_u32 s46, s42, 4587520\n\t" \
    "s_addc_u32 s47, s43, 0\n\t" \
    "s_add_u32 m0, s82, 143360\n\t" \
    "s_nop 0\n\t" \
    "global_load_lds_dwordx4 v200, s[46:47]\n\t" \
    "s_mov_b32 s77, 3019898880\n\t" \
    "s_mov_b32 s78, 3054198966\n\t" \
    "s_waitcnt lgkmcnt(0)\n\t" \
    "s_mov_b32 s70, 116\n\t" \
    "vv_E1_%=:\n\t" \
    "s_memtime s[56:57]\n\t" \
    "s_memrealtime s[2:3]\n\t" \
    "s_nop 0\n\t" \
    "s_memrealtime s[4:5]\n\t" \
    "s_nop 0\n\t" \
    "s_memrealtime s[6:7]\n\t" \
    "s_nop 0\n\t" \
    "s_memrealtime s[8:9]\n\t" \
    "s_nop 0\n\t" \
    "s_memrealtime s[10:11]\n\t" \
    "s_nop 0\n\t" \
    "s_memrealtime s[12:13]\n\t" \
    "s_nop 0\n\t" \
    "s_memrealtime s[14:15]\n\t" \
    "s_nop 0\n\t" \
    "s_memrealtime s[16:17]\n\t" \
    "s_nop 0\n\t" \
    "s_memrealtime s[18:19]\n\t" \
    "s_nop 0\n\t" \
    "s_memrealtime s[20:21]\n\t" \
    "s_nop 0\n\t" \
    "s_memrealtime s[22:23]\n\t" \
    "s_nop 0\n\t" \
    "s_memrealtime s[24:25]\n\t" \
    "s_nop 0\n\t" \
    "s_memrealtime s[26:27]\n\t" \
    "s_nop 0\n\t" \
    "s_memrealtime s[28:29]\n\t" \
    "s_memtime s[54:55]\n\t" \
    "s_add_u32 s70, s70, 8\n\t" \
    "s_waitcnt lgkmcnt(0)\n\t" \
    "s_sub_u32 s69, s54, s56\n\t" \
    "s_cmp_gt_u32 s69, s70\n\t" \
    "s_cbranch_scc1 vv_E1_%=\n\t" \
    "s_add_u32 s2, s2, s4\n\t" \
    "s_add_u32 s6, s6, s8\n\t" \
    "s_add_u32 s10, s10, s12\n\t" \
    "s_add_u32 s14, s14, s16\n\t" \
    "s_add_u32 s18, s18, s20\n\t" \
    "s_add_u32 s22, s22, s24\n\t" \
    "s_add_u32 s2, s2, s6\n\t" \
    "s_add_u32 s10, s10, s14\n\t" \
    "s_add_u32 s18, s18, s22\n\t" \
    "s_add_u32 s2, s2, s26\n\t" \
    "s_add_u32 s10, s10, s18\n\t" \
    "s_add_u32 s2, s2, s10\n\t" \
    "s_mul_i32 s70, s28, 13\n\t" \
    "s_sub_u32 s70, s70, s2\n\t" \
    "s_min_u32 s70, s70, 52\n\t" \
    "s_mul_i32 s69, s69, s70\n\t" \
    "s_mul_hi_u32 s69, s69, 76695844\n\t" \
    "s_mov_b32 s75, s73\n\t" \
    "s_mov_b32 s76, s74\n\t" \
    "s_add_u32 s73, s56, s69\n\t" \
    "s_lshl_b32 s74, s28, 4\n\t" \
    "s_waitcnt vmcnt(32) lgkmcnt(0)\n\t" \
    "s_barrier\n\t" \
    "ds_read_b64_tr_b16 a[0:1], v201 offset:0\n\t" \
    "ds_read_b64_tr_b16 a[2:3], v201 offset:1024\n\t" \
    "ds_read_b64_tr_b16 a[4:5], v201 offset:8192\n\t" \
    "ds_read_b64_tr_b16 a[6:7], v201 offset:9216\n\t" \
    "ds_read_b64_tr_b16 a[72:73], v202 offset:0\n\t" \
    "ds_read_b64_tr_b16 a[74:75], v202 offset:1024\n\t" \
    "ds_read_b64_tr_b16 a[76:77], v202 offset:8192\n\t" \
    "ds_read_b64_tr_b16 a[78:79], v202 offset:9216\n\t" \
    "s_waitcnt vmcnt(28) lgkmcnt(0)\n\t" \
    "s_barrier\n\t" \
    "ds_read_b64_tr_b16 a[8:9], v201 offset:16384\n\t" \
    "ds_read_b64_tr_b16 a[10:11], v201 offset:17408\n\t" \
    "ds_read_b64_tr_b16 a[12:13], v201 offset:24576\n\t" \
    "ds_read_b64_tr_b16 a[14:15], v201 offset:25600\n\t" \
    "ds_read_b64_tr_b16 a[80:81], v202 offset:16384\n\t" \
    "ds_read_b64_tr_b16 a[82:83], v202 offset:17408\n\t" \
    "ds_read_b64_tr_b16 a[84:85], v202 offset:24576\n\t" \
    "ds_read_b64_tr_b16 a[86:87], v202 offset:25600\n\t" \
    "s_waitcnt vmcnt(24) lgkmcnt(0)\n\t" \
    "s_barrier\n\t" \
    "ds_read_b64_tr_b16 a[16:17], v201 offset:32768\n\t" \
    "ds_read_b64_tr_b16 a[18:19], v201 offset:33792\n\t" \
    "ds_read_b64_tr_b16 a[20:21], v201 offset:40960\n\t" \
    "ds_read_b64_tr_b16 a[22:23], v201 offset:41984\n\t" \
    "ds_read_b64_tr_b16 a[88:89], v202 offset:32768\n\t" \
    "ds_read_b64_tr_b16 a[90:91], v202 offset:33792\n\t" \
    "ds_read_b64_tr_b16 a[92:93], v202 offset:40960\n\t" \
    "ds_read_b64_tr_b16 a[94:95], v202 offset:41984\n\t" \
    "s_waitcnt vmcnt(20) lgkmcnt(0)\n\t" \
    "s_barrier\n\t" \
    "ds_read_b64_tr_b16 a[24:25], v201 offset:49152\n\t" \
    "ds_read_b64_tr_b16 a[26:27], v201 offset:50176\n\t" \
    "ds_read_b64_tr_b16 a[28:29], v201 offset:57344\n\t" \
    "ds_read_b64_tr_b16 a[30:31], v201 offset:58368\n\t" \
    "ds_read_b64_tr_b16 a[96:97], v202 offset:49152\n\t" \
    "ds_read_b64_tr_b16 a[98:99], v202 offset:50176\n\t" \
    "ds_read_b64_tr_b16 a[100:101], v202 offset:57344\n\t" \
    "ds_read_b64_tr_b16 a[102:103], v202 offset:58368\n\t" \
    "s_add_u32 s46, s44, 0\n\t" \
    "s_addc_u32 s47, s45, 0\n\t" \
    "s_mul_i32 s69, s82, 9\n\t" \
    "s_add_u32 m0, s69, 0\n\t" \
    "s_nop 0\n\t" \
    "global_load_lds_dwordx4 v203, s[46:47]\n\t" \
    "s_add_u32 m0, s69, 1024\n\t" \
    "s_nop 0\n\t" \
    "global_load_lds_dwordx4 v204, s[46:47]\n\t" \
    "s_add_u32 m0, s69, 2048\n\t" \
    "s_nop 0\n\t" \
    "global_load_lds_dwordx4 v205, s[46:47]\n\t" \
    "s_add_u32 m0, s69, 3072\n\t" \
    "s_nop 0\n\t" \
    "global_load_lds_dwordx4 v206, s[46:47]\n\t" \
    "s_add_u32 m0, s69, 4096\n\t" \
    "s_nop 0\n\t" \
    "global_load_lds_dwordx4 v207, s[46:47]\n\t" \
    "s_add_u32 m0, s69, 5120\n\t" \
    "s_nop 0\n\t" \
    "global_load_lds_dwordx4 v208, s[46:47]\n\t" \
    "s_add_u32 m0, s69, 6144\n\t" \
    "s_nop 0\n\t" \
    "global_load_lds_dwordx4 v209, s[46:47]\n\t" \
    "s_add_u32 m0, s69, 7168\n\t" \
    "s_nop 0\n\t" \
    "global_load_lds_dwordx4 v210, s[46:47]\n\t" \
    "s_add_u32 m0, s69, 8192\n\t" \
    "s_nop 0\n\t" \
    "global_load_lds_dwordx4 v211, s[46:47]\n\t" \
    "s_waitcnt vmcnt(25) lgkmcnt(0)\n\t" \
    "s_barrier\n\t" \
    "v_add_u32 v201, 0x10000, v201\n\t" \
    "v_add_u32 v202, 0x10000, v202\n\t" \
    "ds_read_b64_tr_b16 a[32:33], v201 offset:0\n\t" \
    "ds_read_b64_tr_b16 a[34:35], v201 offset:1024\n\t" \
    "ds_read_b64_tr_b16 a[36:37], v201 offset:8192\n\t" \
    "ds_read_b64_tr_b16 a[38:39], v201 offset:9216\n\t" \
    "ds_read_b64_tr_b16 a[104:105], v202 offset:0\n\t" \
    "ds_read_b64_tr_b16 a[106:107], v202 offset:1024\n\t" \
    "ds_read_b64_tr_b16 a[108:109], v202 offset:8192\n\t" \
    "ds_read_b64_tr_b16 a[110:111], v202 offset:9216\n\t" \
    "s_waitcnt vmcnt(21) lgkmcnt(0)\n\t" \
    "s_barrier\n\t" \
    "ds_read_b64_tr_b16 a[40:41], v201 offset:16384\n\t" \
    "ds_read_b64_tr_b16 a[42:43], v201 offset:17408\n\t" \
    "ds_read_b64_tr_b16 a[44:45], v201 offset:24576\n\t" \
    "ds_read_b64_tr_b16 a[46:47], v201 offset:25600\n\t" \
    "ds_read_b64_tr_b16 a[112:113], v202 offset:16384\n\t" \
    "ds_read_b64_tr_b16 a[114:115], v202 offset:17408\n\t" \
    "ds_read_b64_tr_b16 a[116:117], v202 offset:24576\n\t" \
    "ds_read_b64_tr_b16 a[118:119], v202 offset:25600\n\t" \
    "s_add_u32 s46, s44, 36864\n\t" \
    "s_addc_u32 s47, s45, 0\n\t" \
    "s_mul_i32 s69, s82, 9\n\t" \
    "s_add_u32 m0, s69, 36864\n\t" \
    "s_nop 0\n\t" \
    "global_load_lds_dwordx4 v203, s[46:47]\n\t" \
    "s_add_u32 m0, s69, 37888\n\t" \
    "s_nop 0\n\t" \
    "global_load_lds_dwordx4 v204, s[46:47]\n\t" \
    "s_add_u32 m0, s69, 38912\n\t" \
    "s_nop 0\n\t" \
    "global_load_lds_dwordx4 v205, s[46:47]\n\t" \
    "s_add_u32 m0, s69, 39936\n\t" \
    "s_nop 0\n\t" \
    "global_load_lds_dwordx4 v206, s[46:47]\n\t" \
    "s_add_u32 m0, s69, 40960\n\t" \
    "s_nop 0\n\t" \
    "global_load_lds_dwordx4 v207, s[46:47]\n\t" \
    "s_add_u32 m0, s69, 41984\n\t" \
    "s_nop 0\n\t" \
    "global_load_lds_dwordx4 v208, s[46:47]\n\t" \
    "s_add_u32 m0, s69, 43008\n\t" \
    "s_nop 0\n\t" \
    "global_load_lds_dwordx4 v209, s[46:47]\n\t" \
    "s_add_u32 m0, s69, 44032\n\t" \
    "s_nop 0\n\t" \
    "global_load_lds_dwordx4 v210, s[46:47]\n\t" \
    "s_add_u32 m0, s69, 45056\n\t" \
    "s_nop 0\n\t" \
    "global_load_lds_dwordx4 v211, s[46:47]\n\t" \
    "s_waitcnt vmcnt(26) lgkmcnt(0)\n\t" \
    "s_barrier\n\t" \
    "ds_read_b64_tr_b16 a[48:49], v201 offset:32768\n\t" \
    "ds_read_b64_tr_b16 a[50:51], v201 offset:33792\n\t" \
    "ds_read_b64_tr_b16 a[52:53], v201 offset:40960\n\t" \
    "ds_read_b64_tr_b16 a[54:55], v201 offset:41984\n\t" \
    "ds_read_b64_tr_b16 a[120:121], v202 offset:32768\n\t" \
    "ds_read_b64_tr_b16 a[122:123], v202 offset:33792\n\t" \
    "ds_read_b64_tr_b16 a[124:125], v202 offset:40960\n\t" \
    "ds_read_b64_tr_b16 a[126:127], v202 offset:41984\n\t" \
    "s_waitcnt vmcnt(22) lgkmcnt(0)\n\t" \
    "s_barrier\n\t" \
    "ds_read_b64_tr_b16 a[56:57], v201 offset:49152\n\t" \
    "ds_read_b64_tr_b16 a[58:59], v201 offset:50176\n\t" \
    "ds_read_b64_tr_b16 a[60:61], v201 offset:57344\n\t" \
    "ds_read_b64_tr_b16 a[62:63], v201 offset:58368\n\t" \
    "ds_read_b64_tr_b16 a[128:129], v202 offset:49152\n\t" \
    "ds_read_b64_tr_b16 a[130:131], v202 offset:50176\n\t" \
    "ds_read_b64_tr_b16 a[132:133], v202 offset:57344\n\t" \
    "ds_read_b64_tr_b16 a[134:135], v202 offset:58368\n\t" \
    "s_add_u32 s46, s44, 73728\n\t" \
    "s_addc_u32 s47, s45, 0\n\t" \
    "s_mul_i32 s69, s82, 9\n\t" \
    "s_add_u32 m0, s69, 73728\n\t" \
    "s_nop 0\n\t" \
    "global_load_lds_dwordx4 v203, s[46:47]\n\t" \
    "s_add_u32 m0, s69, 74752\n\t" \
    "s_nop 0\n\t" \
    "global_load_lds_dwordx4 v204, s[46:47]\n\t" \
    "s_add_u32 m0, s69, 75776\n\t" \
    "s_nop 0\n\t" \
    "global_load_lds_dwordx4 v205, s[46:47]\n\t" \
    "s_add_u32 m0, s69, 76800\n\t" \
    "s_nop 0\n\t" \
    "global_load_lds_dwordx4 v206, s[46:47]\n\t" \
    "s_add_u32 m0, s69, 77824\n\t" \
    "s_nop 0\n\t" \
    "global_load_lds_dwordx4 v207, s[46:47]\n\t" \
    "s_add_u32 m0, s69, 78848\n\t" \
    "s_nop 0\n\t" \
    "global_load_lds_dwordx4 v208, s[46:47]\n\t" \
    "s_add_u32 m0, s69, 79872\n\t" \
    "s_nop 0\n\t" \
    "global_load_lds_dwordx4 v209, s[46:47]\n\t" \
    "s_add_u32 m0, s69, 80896\n\t" \
    "s_nop 0\n\t" \
    "global_load_lds_dwordx4 v210, s[46:47]\n\t" \
    "s_add_u32 m0, s69, 81920\n\t" \
    "s_nop 0\n\t" \
    "global_load_lds_dwordx4 v211, s[46:47]\n\t" \
    "v_mbcnt_lo_u32_b32 v140, -1, 0\n\t" \
    "v_mbcnt_hi_u32_b32 v140, -1, v140\n\t" \
    "v_lshlrev_b32 v140, 6, v140\n\t" \
    "v_add_u32 v140, 6225920, v140\n\t" \
    "v_mov_b32 v141, 6234112\n\t" \
    "global_load_dword v141, v141, s[36:37] sc1\n\t" \
    "global_load_dwordx2 v[142:143], v140, s[36:37] sc1\n\t" \
    "global_load_dword v144, v140, s[36:37] offset:2048 sc1\n\t" \
    "s_waitcnt vmcnt(30) lgkmcnt(0)\n\t" \
    "s_barrier\n\t" \
    "v_add_u32 v201, 0x10000, v201\n\t" \
    "v_add_u32 v202, 0x10000, v202\n\t" \
    "ds_read_b64_tr_b16 a[64:65], v201 offset:0\n\t" \
    "ds_read_b64_tr_b16 a[66:67], v201 offset:1024\n\t" \
    "ds_read_b64_tr_b16 a[68:69], v201 offset:8192\n\t" \
    "ds_read_b64_tr_b16 a[70:71], v201 offset:9216\n\t" \
    "ds_read_b64_tr_b16 a[136:137], v202 offset:0\n\t" \
    "ds_read_b64_tr_b16 a[138:139], v202 offset:1024\n\t" \
    "ds_read_b64_tr_b16 a[140:141], v202 offset:8192\n\t" \
    "ds_read_b64_tr_b16 a[142:143], v202 offset:9216\n\t" \
    "v_add_u32 v201, 0xfffe0000, v201\n\t" \
    "v_add_u32 v202, 0xfffe0000, v202\n\t" \
    "s_waitcnt lgkmcnt(0)\n\t" \
    "s_barrier\n\t" \
    "s_add_u32 s46, s44, 110592\n\t" \
    "s_addc_u32 s47, s45, 0\n\t" \
    "s_mul_i32 s69, s82, 9\n\t" \
    "s_add_u32 m0, s69, 110592\n\t" \
    "s_nop 0\n\t" \
    "global_load_lds_dwordx4 v203, s[46:47]\n\t" \
    "s_add_u32 m0, s69, 111616\n\t" \
    "s_nop 0\n\t" \
    "global_load_lds_dwordx4 v204, s[46:47]\n\t" \
    "s_add_u32 m0, s69, 112640\n\t" \
    "s_nop 0\n\t" \
    "global_load_lds_dwordx4 v205, s[46:47]\n\t" \
    "s_add_u32 m0, s69, 113664\n\t" \
    "s_nop 0\n\t" \
    "global_load_lds_dwordx4 v206, s[46:47]\n\t" \
    "s_add_u32 m0, s69, 114688\n\t" \
    "s_nop 0\n\t" \
    "global_load_lds_dwordx4 v207, s[46:47]\n\t" \
    "s_add_u32 m0, s69, 115712\n\t" \
    "s_nop 0\n\t" \
    "global_load_lds_dwordx4 v208, s[46:47]\n\t" \
    "s_add_u32 m0, s69, 116736\n\t" \
    "s_nop 0\n\t" \
    "global_load_lds_dwordx4 v209, s[46:47]\n\t" \
    "s_add_u32 m0, s69, 117760\n\t" \
    "s_nop 0\n\t" \
    "global_load_lds_dwordx4 v210, s[46:47]\n\t" \
    "s_add_u32 m0, s69, 118784\n\t" \
    "s_nop 0\n\t" \
    "global_load_lds_dwordx4 v211, s[46:47]\n\t" \
    "s_waitcnt vmcnt(21) lgkmcnt(0)\n\t" \
    "s_barrier\n\t" \
    "ds_read_b64_tr_b16 a[144:145], v212 offset:0\n\t" \
    "ds_read_b64_tr_b16 a[146:147], v212 offset:2304\n\t" \
    "ds_read_b64_tr_b16 a[148:149], v212 offset:18432\n\t" \
    "ds_read_b64_tr_b16 a[150:151], v212 offset:20736\n\t" \
    "s_waitcnt lgkmcnt(6)\n\t" \
    "ds_read_b64_tr_b16 a[176:177], v213 offset:0\n\t" \
    "ds_read_b64_tr_b16 a[178:179], v213 offset:2304\n\t" \
    "ds_read_b64_tr_b16 a[180:181], v213 offset:18432\n\t" \
    "ds_read_b64_tr_b16 a[182:183], v213 offset:20736\n\t" \
    "s_waitcnt lgkmcnt(6)\n\t" \
    "ds_read_b64_tr_b16 a[208:209], v212 offset:128\n\t" \
    "ds_read_b64_tr_b16 a[210:211], v212 offset:2432\n\t" \
    "ds_read_b64_tr_b16 a[212:213], v212 offset:18560\n\t" \
    "ds_read_b64_tr_b16 a[214:215], v212 offset:20864\n\t" \
    "s_waitcnt lgkmcnt(6)\n\t" \
    "ds_read_b64_tr_b16 a[240:241], v213 offset:128\n\t" \
    "ds_read_b64_tr_b16 a[242:243], v213 offset:2432\n\t" \
    "ds_read_b64_tr_b16 a[244:245], v213 offset:18560\n\t" \
    "ds_read_b64_tr_b16 a[246:247], v213 offset:20864\n\t" \
    "s_waitcnt lgkmcnt(6)\n\t" \
    "ds_read_b64_tr_b16 a[152:153], v214 offset:0\n\t" \
    "ds_read_b64_tr_b16 a[154:155], v214 offset:2304\n\t" \
    "ds_read_b64_tr_b16 a[156:157], v214 offset:18432\n\t" \
    "ds_read_b64_tr_b16 a[158:159], v214 offset:20736\n\t" \
    "s_waitcnt lgkmcnt(6)\n\t" \
    "ds_read_b64_tr_b16 a[184:185], v215 offset:0\n\t" \
    "ds_read_b64_tr_b16 a[186:187], v215 offset:2304\n\t" \
    "ds_read_b64_tr_b16 a[188:189], v215 offset:18432\n\t" \
    "ds_read_b64_tr_b16 a[190:191], v215 offset:20736\n\t" \
    "s_waitcnt lgkmcnt(6)\n\t" \
    "ds_read_b64_tr_b16 a[216:217], v214 offset:128\n\t" \
    "ds_read_b64_tr_b16 a[218:219], v214 offset:2432\n\t" \
    "ds_read_b64_tr_b16 a[220:221], v214 offset:18560\n\t" \
    "ds_read_b64_tr_b16 a[222:223], v214 offset:20864\n\t" \
    "s_waitcnt lgkmcnt(6)\n\t" \
    "ds_read_b64_tr_b16 a[248:249], v215 offset:128\n\t" \
    "ds_read_b64_tr_b16 a[250:251], v215 offset:2432\n\t" \
    "ds_read_b64_tr_b16 a[252:253], v215 offset:18560\n\t" \
    "ds_read_b64_tr_b16 a[254:255], v215 offset:20864\n\t" \
    "s_waitcnt lgkmcnt(6)\n\t" \
    "ds_read_b64_tr_b16 v[240:241], v24 offset:0\n\t" \
    "ds_read_b64_tr_b16 v[242:243], v24 offset:2304\n\t" \
    "ds_read_b64_tr_b16 v[244:245], v24 offset:18432\n\t" \
    "ds_read_b64_tr_b16 v[246:247], v24 offset:20736\n\t" \
    "v_add_u32 v212, 0x12000, v212\n\t" \
    "v_add_u32 v213, 0x12000, v213\n\t" \
    "v_add_u32 v214, 0x12000, v214\n\t" \
    "v_add_u32 v215, 0x12000, v215\n\t" \
    "v_add_u32 v24, 0x12000, v24\n\t" \
    "s_waitcnt vmcnt(9)\n\t" \
    "vv_C2_%=:\n\t" \
    "v_cmp_ne_u32 s[54:55], v142, v144\n\t" \
    "v_sub_u32 v142, v142, v141\n\t" \
    "v_cmp_ne_u32 vcc, 1, v142\n\t" \
    "s_nop 1\n\t" \
    "s_or_b64 vcc, vcc, s[54:55]\n\t" \
    "s_cbranch_scc0 vv_Cok3_%=\n\t" \
    "s_cmp_eq_u32 s68, 0\n\t" \
    "s_cbranch_scc1 vv_F5_%=\n\t" \
    "s_sub_u32 s68, s68, 1\n\t" \
    "s_sleep 6\n\t" \
    "s_branch vv_Cag4_%=\n\t" \
    "vv_F5_%=:\n\t" \
    "v_mov_b32 v144, 5767424\n\t" \
    "v_mov_b32 v145, 4\n\t" \
    "global_store_dword v144, v145, s[36:37] sc1\n\t" \
    "s_waitcnt vmcnt(0)\n\t" \
    "s_branch vv_Cok3_%=\n\t" \
    "vv_Cag4_%=:\n\t" \
    "global_load_dwordx2 v[142:143], v140, s[36:37] sc1\n\t" \
    "global_load_dword v144, v140, s[36:37] offset:2048 sc1\n\t" \
    "s_waitcnt vmcnt(0)\n\t" \
    "s_branch vv_C2_%=\n\t" \
    "vv_Cok3_%=:\n\t" \
    "v_readlane_b32 s69, v143, 2\n\t" \
    "v_readlane_b32 s70, v143, 3\n\t" \
    "s_nop 1\n\t" \
    "s_cmp_lt_u32 s70, s69\n\t" \
    "s_cselect_b32 s69, 0, 8192\n\t" \
    "s_add_u32 s100, s36, s69\n\t" \
    "s_addc_u32 s101, s37, 0\n\t" \
    "s_cmp_lg_u32 s66, 0\n\t" \
    "s_cbranch_scc1 vv_Ca6_%=\n\t" \
    "v_mov_b32 v140, 5768192\n\t" \
    "s_mov_b64 exec, 1\n\t" \
    "global_atomic_add v140, v142, s[36:37] sc1\n\t" \
    "s_mov_b64 exec, -1\n\t" \
    "vv_Ca6_%=:\n\t" \
    "v_mov_b32_dpp v140, v143 quad_perm:[1,0,3,2] row_mask:0xf bank_mask:0xf\n\t" \
    "v_cmp_lt_u32 s[44:45], v140, v143\n\t" \
    "v_min_u32 v140, v140, v143\n\t" \
    "v_and_b32 v141, 1, v140\n\t" \
    "v_cmp_ne_u32 vcc, 0, v141\n\t" \
    "s_nop 1\n\t" \
    "s_and_b32 s44, s44, 0x55555555\n\t" \
    "s_and_b32 s69, vcc_lo, 0xaaaaaaaa\n\t" \
    "s_or_b32 s44, s44, s69\n\t" \
    "s_and_b32 s45, s45, 0x55555555\n\t" \
    "s_and_b32 s69, vcc_hi, 0xaaaaaaaa\n\t" \
    "s_or_b32 s45, s45, s69\n\t" \
    "s_bitcmp1_b32 s44, 0\n\t" \
    "s_cselect_b32 s62, 8192, 0\n\t" \
    "s_bitcmp1_b32 s44, 2\n\t" \
    "s_cselect_b32 s63, 8192, 0\n\t" \
    "s_or_b32 s63, s63, 0x200000\n\t" \
    "v_cmp_lt_u32 vcc, 1320, v140\n\t" \
    "s_nop 1\n\t" \
    "s_bcnt1_i32_b64 s69, vcc\n\t" \
    "s_cmp_gt_u32 s69, 16\n\t" \
    "s_cselect_b32 s69, 1, 0\n\t" \
    "s_mul_i32 s70, s69, 160\n\t" \
    "s_add_u32 s86, s86, s70\n\t" \
    "s_mul_i32 s70, s69, 536\n\t" \
    "s_add_u32 s87, s87, s70\n\t" \
    "s_mul_i32 s70, s69, 256\n\t" \
    "s_add_u32 s88, s88, s70\n\t" \
    "s_waitcnt vmcnt(0) lgkmcnt(0)\n\t" \
    "s_barrier\n\t" \
    "ds_read_b64_tr_b16 a[160:161], v212 offset:0\n\t" \
    "ds_read_b64_tr_b16 a[162:163], v212 offset:2304\n\t" \
    "ds_read_b64_tr_b16 a[164:165], v212 offset:18432\n\t" \
    "ds_read_b64_tr_b16 a[166:167], v212 offset:20736\n\t" \
    "s_waitcnt lgkmcnt(6)\n\t" \
    "ds_read_b64_tr_b16 a[192:193], v213 offset:0\n\t" \
    "ds_read_b64_tr_b16 a[194:195], v213 offset:2304\n\t" \
    "ds_read_b64_tr_b16 a[196:197], v213 offset:18432\n\t" \
    "ds_read_b64_tr_b16 a[198:199], v213 offset:20736\n\t" \
    "s_waitcnt lgkmcnt(6)\n\t" \
    "ds_read_b64_tr_b16 a[224:225], v212 offset:128\n\t" \
    "ds_read_b64_tr_b16 a[226:227], v212 offset:2432\n\t" \
    "ds_read_b64_tr_b16 a[228:229], v212 offset:18560\n\t" \
    "ds_read_b64_tr_b16 a[230:231], v212 offset:20864\n\t" \
    "s_waitcnt lgkmcnt(6)\n\t" \
    "ds_read_b64_tr_b16 v[224:225], v213 offset:128\n\t" \
    "ds_read_b64_tr_b16 v[226:227], v213 offset:2432\n\t" \
    "ds_read_b64_tr_b16 v[228:229], v213 offset:18560\n\t" \
    "ds_read_b64_tr_b16 v[230:231], v213 offset:20864\n\t" \
    "s_waitcnt lgkmcnt(6)\n\t" \
    "ds_read_b64_tr_b16 a[168:169], v214 offset:0\n\t" \
    "ds_read_b64_tr_b16 a[170:171], v214 offset:2304\n\t" \
    "ds_read_b64_tr_b16 a[172:173], v214 offset:18432\n\t" \
    "ds_read_b64_tr_b16 a[174:175], v214 offset:20736\n\t" \
    "s_waitcnt lgkmcnt(6)\n\t" \
    "ds_read_b64_tr_b16 a[200:201], v215 offset:0\n\t" \
    "ds_read_b64_tr_b16 a[202:203], v215 offset:2304\n\t" \
    "ds_read_b64_tr_b16 a[204:205], v215 offset:18432\n\t" \
    "ds_read_b64_tr_b16 a[206:207], v215 offset:20736\n\t" \
    "s_waitcnt lgkmcnt(6)\n\t" \
    "ds_read_b64_tr_b16 a[232:233], v214 offset:128\n\t" \
    "ds_read_b64_tr_b16 a[234:235], v214 offset:2432\n\t" \
    "ds_read_b64_tr_b16 a[236:237], v214 offset:18560\n\t" \
    "ds_read_b64_tr_b16 a[238:239], v214 offset:20864\n\t" \
    "s_waitcnt lgkmcnt(6)\n\t" \
    "ds_read_b64_tr_b16 v[232:233], v215 offset:128\n\t" \
    "ds_read_b64_tr_b16 v[234:235], v215 offset:2432\n\t" \
    "ds_read_b64_tr_b16 v[236:237], v215 offset:18560\n\t" \
    "ds_read_b64_tr_b16 v[238:239], v215 offset:20864\n\t" \
    "s_waitcnt lgkmcnt(6)\n\t" \
    "ds_read_b64_tr_b16 v[248:249], v24 offset:0\n\t" \
    "ds_read_b64_tr_b16 v[250:251], v24 offset:2304\n\t" \
    "ds_read_b64_tr_b16 v[252:253], v24 offset:18432\n\t" \
    "ds_read_b64_tr_b16 v[254:255], v24 offset:20736\n\t" \
    "v_add_u32 v212, 0x12000, v212\n\t" \
    "v_add_u32 v213, 0x12000, v213\n\t" \
    "v_add_u32 v214, 0x12000, v214\n\t" \
    "v_add_u32 v215, 0x12000, v215\n\t" \
    "v_add_u32 v24, 0x12000, v24\n\t" \
    "s_waitcnt lgkmcnt(0)\n\t" \
    "s_barrier\n\t" \
    "s_cmp_lg_u32 s66, 3\n\t" \
    "s_cbranch_scc1 vv_na7_%=\n\t" \
    "v_mov_b32 v172, 5769216\n\t" \
    "v_mov_b32 v173, 1\n\t" \
    "s_mov_b64 exec, 1\n\t" \
    "global_atomic_add v174, v172, v173, s[36:37] sc0 sc1\n\t" \
    "s_mov_b64 exec, -1\n\t" \
    "v_mbcnt_lo_u32_b32 v172, -1, 0\n\t" \
    "v_and_b32 v173, 1, v172\n\t" \
    "v_lshlrev_b32 v173, 14, v173\n\t" \
    "v_bfe_u32 v175, v172, 1, 1\n\t" \
    "v_lshl_add_u32 v173, v175, 17, v173\n\t" \
    "v_bfe_u32 v175, v172, 2, 2\n\t" \
    "v_lshl_add_u32 v173, v175, 11, v173\n\t" \
    "v_bfe_u32 v175, v172, 4, 1\n\t" \
    "v_lshl_add_u32 v173, v175, 15, v173\n\t" \
    "v_add_u32 v173, s63, v173\n\t" \
    "s_waitcnt vmcnt(0)\n\t" \
    "v_readfirstlane_b32 s69, v174\n\t" \
    "s_nop 1\n\t" \
    "s_cmp_lg_u32 s69, 126\n\t" \
    "s_cbranch_scc1 vv_go8_%=\n\t" \
    "s_memrealtime s[56:57]\n\t" \
    "s_waitcnt lgkmcnt(0)\n\t" \
    "s_lshl_b32 s69, s56, 4\n\t" \
    "s_add_u32 s69, s69, 4160\n\t" \
    "s_add_u32 s70, s56, 24\n\t" \
    "s_lshr_b32 s70, s70, 4\n\t" \
    "v_lshrrev_b32 v175, 2, v172\n\t" \
    "v_add_u32 v175, s70, v175\n\t" \
    "v_and_b32 v175, 63, v175\n\t" \
    "v_lshlrev_b32 v175, 6, v175\n\t" \
    "v_and_b32 v174, 3, v172\n\t" \
    "v_lshl_add_u32 v175, v174, 12, v175\n\t" \
    "v_add_u32 v175, 5783552, v175\n\t" \
    "v_mov_b32 v172, s69\n\t" \
    "s_mov_b32 exec_hi, 0\n\t" \
    "global_store_dword v175, v172, s[36:37] sc1\n\t" \
    "global_store_dword v173, v172, s[36:37] sc1\n\t" \
    "s_mov_b32 exec_hi, -1\n\t" \
    "vv_go8_%=:\n\t" \
    "vv_na7_%=:\n\t" \
    "s_mov_b32 s79, s74\n\t" \
    "s_sub_u32 s80, s74, 32000\n\t" \
    "s_bitcmp0_b32 s44, 1\n\t" \
    "s_cselect_b32 s69, 16384, 0\n\t" \
    "v_xor_b32 v1, s69, v1\n\t" \
    "v_xor_b32 v2, s69, v2\n\t" \
    "v_xor_b32 v23, s69, v23\n\t" \
    "s_bitcmp1_b32 s44, 3\n\t" \
    "s_cselect_b32 s69, 16384, 0\n\t" \
    "v_xor_b32 v20, s69, v20\n\t" \
    "v_mov_b32 v72, 0\n\t" \
    "v_mov_b32 v73, 0\n\t" \
    "v_mov_b32 v74, 0\n\t" \
    "v_mov_b32 v75, 0\n\t" \
    "v_mov_b32 v76, 0\n\t" \
    "v_mov_b32 v77, 0\n\t" \
    "v_mov_b32 v78, 0\n\t" \
    "v_mov_b32 v79, 0\n\t" \
    "v_mov_b32 v108, 0\n\t" \
    "v_mov_b32 v109, 0\n\t" \
    "v_mov_b32 v110, 0\n\t" \
    "v_mov_b32 v111, 0\n\t" \
    "v_mov_b32 v112, 0\n\t" \
    "v_mov_b32 v113, 0\n\t" \
    "v_mov_b32 v114, 0\n\t" \
    "v_mov_b32 v115, 0\n\t" \
    "v_mov_b32 v116, 0\n\t" \
    "v_mov_b32 v117, 0\n\t" \
    "v_mov_b32 v118, 0\n\t" \
    "v_mov_b32 v119, 0\n\t" \
    "v_mov_b32 v120, 0\n\t" \
    "v_mov_b32 v121, 0\n\t" \
    "v_mov_b32 v122, 0\n\t" \
    "v_mov_b32 v123, 0\n\t" \
    "v_mov_b32 v124, 0\n\t" \
    "v_mov_b32 v125, 0\n\t" \
    "v_mov_b32 v126, 0\n\t" \
    "v_mov_b32 v127, 0\n\t" \
    "s_add_u32 s69, s65, 0\n\t" \
    "s_and_b32 s69, s69, 7\n\t" \
    "s_lshl_b32 s69, s69, 18\n\t" \
    "s_add_u32 s69, s69, s62\n\t" \
    "s_add_u32 s46, s36, s69\n\t" \
    "s_addc_u32 s47, s37, 0\n\t" \
    "s_bitcmp0_b32 s65, 0\n\t" \
    "s_cselect_b32 s69, 16384, 0\n\t" \
    "v_xor_b32 v1, s69, v1\n\t" \
    "v_xor_b32 v2, s69, v2\n\t" \
    "v_xor_b32 v23, s69, v23\n\t" \
    "s_add_u32 s70, s65, 0\n\t" \
    "s_mul_i32 s69, s70, 171\n\t" \
    "s_lshr_b32 s69, s69, 9\n\t" \
    "s_mul_i32 s71, s69, 3\n\t" \
    "s_sub_u32 s70, s70, s71\n\t" \
    "s_mul_i32 s70, s70, 589824\n\t" \
    "s_lshl1_add_u32 s71, s69, 4\n\t" \
    "s_lshr_b64 s[38:39], s[44:45], s71\n\t" \
    "s_lshl_b32 s71, s66, 1\n\t" \
    "s_xor_b32 s71, s71, s38\n\t" \
    "s_and_b32 s71, s71, 3\n\t" \
    "s_lshl_b32 s71, s71, 13\n\t" \
    "s_add_u32 s70, s70, s71\n\t" \
    "s_lshl_b32 s69, s69, 21\n\t" \
    "s_add_u32 s69, s69, 123731968\n\t" \
    "v_add_u32 v151, s70, v14\n\t" \
    "v_and_b32 v150, 0x80000, v151\n\t" \
    "v_lshrrev_b32 v150, 5, v150\n\t" \
    "v_xor_b32 v151, v151, v150\n\t" \
    "v_add_u32 v152, s70, v15\n\t" \
    "v_and_b32 v150, 0x80000, v152\n\t" \
    "v_lshrrev_b32 v150, 5, v150\n\t" \
    "v_xor_b32 v152, v152, v150\n\t" \
    "v_add_u32 v153, s70, v16\n\t" \
    "v_and_b32 v150, 0x80000, v153\n\t" \
    "v_lshrrev_b32 v150, 5, v150\n\t" \
    "v_xor_b32 v153, v153, v150\n\t" \
    "s_add_u32 s38, s36, s69\n\t" \
    "s_addc_u32 s39, s37, 0\n\t" \
    "s_and_b32 s69, s69, 0xffe00000\n\t" \
    "s_add_u32 s69, s69, 1807360\n\t" \
    "v_mov_b32 v17, s69\n\t" \
    "global_load_dword v17, v17, s[36:37]\n\t" \
    "s_and_b32 s69, s65, 7\n\t" \
    "s_lshl_b32 s69, s69, 18\n\t" \
    "s_add_u32 s69, s69, s63\n\t" \
    "s_add_u32 s52, s36, s69\n\t" \
    "s_addc_u32 s53, s37, 0\n\t" \
    "v_mov_b32 v100, 0\n\t" \
    "v_mov_b32 v101, 0\n\t" \
    "v_mov_b32 v102, 0\n\t" \
    "v_mov_b32 v103, 0\n\t" \
    "s_sub_u32 s70, s79, s74\n\t" \
    "s_max_i32 s70, s70, 0\n\t" \
    "s_min_i32 s70, s70, 16000\n\t" \
    "s_lshl_b32 s70, s70, 1\n\t" \
    "s_mul_hi_u32 s70, s70, s77\n\t" \
    "s_add_u32 s70, s70, s73\n\t" \
    "s_cmp_eq_u32 s66, 1\n\t" \
    "s_cbranch_scc1 vv_loop12_%=\n\t" \
    "s_cmp_eq_u32 s66, 2\n\t" \
    "s_cbranch_scc1 vv_loop13_%=\n\t" \
    "s_cmp_eq_u32 s66, 3\n\t" \
    "s_cbranch_scc1 vv_loop14_%=\n\t" \
    ".p2align 6\n\t" \
    "s_nop 0\n\t" \
    "s_nop 0\n\t" \
    "s_nop 0\n\t" \
    "s_nop 0\n\t" \
    "s_nop 0\n\t" \
    "s_nop 0\n\t" \
    "vv_loop11_%=:\n\t" \
    "s_getpc_b64 s[30:31]\n\t" \
    "vv_Wp17_%=:\n\t" \
    "s_add_u32 s30, s30, vv_We18_%=-vv_Wp17_%=\n\t" \
    "s_addc_u32 s31, s31, 0\n\t" \
    "s_sub_u32 s70, s70, -54\n\t" \
    "vv_W16_%=:\n\t" \
    "s_memtime s[56:57]\n\t" \
    "s_waitcnt lgkmcnt(0)\n\t" \
    "s_sub_u32 s69, s70, s56\n\t" \
    "s_cmp_gt_i32 s69, 80\n\t" \
    "s_cbranch_scc1 vv_W16_%=\n\t" \
    "s_max_i32 s69, s69, 0\n\t" \
    "s_and_b32 s69, s69, -4\n\t" \
    "s_sub_u32 s30, s30, s69\n\t" \
    "s_subb_u32 s31, s31, 0\n\t" \
    "s_setpc_b64 s[30:31]\n\t" \
    "s_nop 0\n\t" \
    "s_nop 0\n\t" \
    "s_nop 0\n\t" \
    "s_nop 0\n\t" \
    "s_nop 0\n\t" \
    "s_nop 0\n\t" \
    "s_nop 0\n\t" \
    "s_nop 0\n\t" \
    "s_nop 0\n\t" \
    "s_nop 0\n\t" \
    "s_nop 0\n\t" \
    "s_nop 0\n\t" \
    "s_nop 0\n\t" \
    "s_nop 0\n\t" \
    "s_nop 0\n\t" \
    "s_nop 0\n\t" \
    "s_nop 0\n\t" \
    "s_nop 0\n\t" \
    "s_nop 0\n\t" \
    "s_nop 0\n\t" \
    "vv_We18_%=:\n\t" \
    "vv_G19_%=:\n\t" \
    "global_load_dwordx4 v[36:39], v1, s[46:47] sc1\n\t" \
    "global_load_dword v141, v2, s[46:47] sc1\n\t" \
    "global_load_dwordx4 v[40:43], v23, s[46:47] sc1\n\t" \
    "s_waitcnt vmcnt(2)\n\t" \
    "vv_Garr24_%=:\n\t" \
    "ds_write_b128 v3, v[36:39]\n\t" \
    "v_smfmac_f32_16x16x64_bf16 v[72:75], v[36:39], a[0:7], v10\n\t" \
    "s_waitcnt vmcnt(1)\n\t" \
    "ds_write_b32 v22, v141 offset:19456\n\t" \
    "s_waitcnt vmcnt(0)\n\t" \
    "ds_write_b128 v3, v[40:43] offset:1024\n\t" \
    "v_max3_u32 v140, v36, v40, v141\n\t" \
    "v_cmp_eq_u32 vcc, -1, v140\n\t" \
    "s_cbranch_vccnz vv_Gbad20_%=\n\t" \
    "vv_Gc21_%=:\n\t" \
    "s_waitcnt lgkmcnt(0)\n\t" \
    "s_barrier\n\t" \
    "ds_read_b128 v[48:51], v4\n\t" \
    "ds_read_b128 v[52:55], v5\n\t" \
    "v_smfmac_f32_16x16x64_bf16 v[76:79], v[36:39], a[72:79], v10\n\t" \
    "ds_read_b128 v[56:59], v6\n\t" \
    "ds_read_b128 v[60:63], v7\n\t" \
    "v_smfmac_f32_16x16x64_bf16 v[72:75], v[40:43], a[8:15], v10\n\t" \
    "ds_read_b128 v[64:67], v8\n\t" \
    "v_smfmac_f32_16x16x64_bf16 v[76:79], v[40:43], a[80:87], v10\n\t" \
    "ds_read_b128 v[68:71], v9\n\t" \
    "s_waitcnt lgkmcnt(5)\n\t" \
    "v_smfmac_f32_16x16x64_bf16 v[72:75], v[48:51], a[16:23], v10\n\t" \
    "ds_read_b128 v[44:47], v0 offset:19456\n\t" \
    "s_waitcnt lgkmcnt(5)\n\t" \
    "v_smfmac_f32_16x16x64_bf16 v[72:75], v[52:55], a[24:31], v10\n\t" \
    "s_waitcnt lgkmcnt(4)\n\t" \
    "v_smfmac_f32_16x16x64_bf16 v[72:75], v[56:59], a[32:39], v10\n\t" \
    "s_waitcnt lgkmcnt(3)\n\t" \
    "v_smfmac_f32_16x16x64_bf16 v[72:75], v[60:63], a[40:47], v10\n\t" \
    "s_waitcnt lgkmcnt(2)\n\t" \
    "v_smfmac_f32_16x16x64_bf16 v[72:75], v[64:67], a[48:55], v10\n\t" \
    "s_waitcnt lgkmcnt(1)\n\t" \
    "v_smfmac_f32_16x16x64_bf16 v[72:75], v[68:71], a[56:63], v10\n\t" \
    "s_waitcnt lgkmcnt(0)\n\t" \
    "v_smfmac_f32_16x16x64_bf16 v[72:75], v[44:47], a[64:71], v10\n\t" \
    "s_nop 2\n\t" \
    "v_smfmac_f32_16x16x64_bf16 v[76:79], v[48:51], a[88:95], v10\n\t" \
    "s_nop 2\n\t" \
    "v_smfmac_f32_16x16x64_bf16 v[76:79], v[52:55], a[96:103], v10\n\t" \
    "v_add_f32 v72, v72, v74\n\t" \
    "v_smfmac_f32_16x16x64_bf16 v[76:79], v[56:59], a[104:111], v10\n\t" \
    "v_add_f32 v73, v73, v75\n\t" \
    "v_smfmac_f32_16x16x64_bf16 v[76:79], v[60:63], a[112:119], v10\n\t" \
    "v_cvt_pk_bf16_f32 v80, v72, v73\n\t" \
    "v_smfmac_f32_16x16x64_bf16 v[76:79], v[64:67], a[120:127], v10\n\t" \
    "ds_write_b32 v12, v80 offset:0\n\t" \
    "v_smfmac_f32_16x16x64_bf16 v[76:79], v[68:71], a[128:135], v10\n\t" \
    "v_smfmac_f32_16x16x64_bf16 v[76:79], v[44:47], a[136:143], v10\n\t" \
    "s_nop 6\n\t" \
    "v_cmp_u_f32 vcc, v72, v73\n\t" \
    "v_pk_add_f32 v[76:77], v[76:77], v[78:79]\n\t" \
    "v_cvt_pk_bf16_f32 v81, v76, v77\n\t" \
    "ds_write_b32 v12, v81 offset:256\n\t" \
    "s_cbranch_vccnz vv_Gslow22_%=\n\t" \
    "vv_Gback23_%=:\n\t" \
    "s_waitcnt lgkmcnt(0)\n\t" \
    "s_barrier\n\t" \
    "ds_read_b64_tr_b16 v[84:85], v13 offset:0\n\t" \
    "v_smfmac_f32_16x16x64_bf16 v[100:103], v[36:39], a[0:7], v10\n\t" \
    "ds_read_b64_tr_b16 v[86:87], v13 offset:64\n\t" \
    "ds_read_b64_tr_b16 v[92:93], v13 offset:1152\n\t" \
    "v_smfmac_f32_16x16x64_bf16 v[100:103], v[36:39], a[0:7], v10\n\t" \
    "ds_read_b64_tr_b16 v[94:95], v13 offset:1216\n\t" \
    "ds_read_b64_tr_b16 v[88:89], v25 offset:0\n\t" \
    "v_smfmac_f32_16x16x64_bf16 v[100:103], v[36:39], a[0:7], v10\n\t" \
    "ds_read_b64_tr_b16 v[90:91], v25 offset:64\n\t" \
    "s_waitcnt lgkmcnt(4)\n\t" \
    "v_smfmac_f32_16x16x64_bf16 v[124:127], v[84:87], v[240:247], v19\n\t" \
    "ds_read_b64_tr_b16 v[96:97], v25 offset:1152\n\t" \
    "ds_read_b64_tr_b16 v[98:99], v25 offset:1216\n\t" \
    "s_waitcnt lgkmcnt(4)\n\t" \
    "v_smfmac_f32_16x16x64_bf16 v[124:127], v[92:95], v[248:255], v19\n\t" \
    "s_nop 2\n\t" \
    "v_smfmac_f32_16x16x64_bf16 v[108:111], v[84:87], a[144:151], v19\n\t" \
    "s_nop 2\n\t" \
    "v_smfmac_f32_16x16x64_bf16 v[108:111], v[92:95], a[160:167], v19\n\t" \
    "s_nop 1\n\t" \
    "v_permlane32_swap_b32 v124, v126\n\t" \
    "v_smfmac_f32_16x16x64_bf16 v[112:115], v[84:87], a[176:183], v19\n\t" \
    "s_nop 1\n\t" \
    "v_permlane32_swap_b32 v125, v127\n\t" \
    "v_smfmac_f32_16x16x64_bf16 v[112:115], v[92:95], a[192:199], v19\n\t" \
    "s_nop 0\n\t" \
    "v_add_f32 v124, v124, v126\n\t" \
    "s_waitcnt lgkmcnt(2)\n\t" \
    "v_smfmac_f32_16x16x64_bf16 v[108:111], v[88:91], a[152:159], v19\n\t" \
    "s_nop 1\n\t" \
    "v_add_f32 v125, v125, v127\n\t" \
    "v_smfmac_f32_16x16x64_bf16 v[112:115], v[88:91], a[184:191], v19\n\t" \
    "s_nop 0\n\t" \
    "v_cvt_pk_bf16_f32 v136, v124, v125\n\t" \
    "s_waitcnt lgkmcnt(0)\n\t" \
    "v_smfmac_f32_16x16x64_bf16 v[108:111], v[96:99], a[168:175], v19\n\t" \
    "s_nop 1\n\t" \
    "v_cvt_pk_bf16_f32 v137, v124, v125\n\t" \
    "v_smfmac_f32_16x16x64_bf16 v[112:115], v[96:99], a[200:207], v19\n\t" \
    "s_nop 1\n\t" \
    "v_permlane32_swap_b32 v136, v137\n\t" \
    "v_smfmac_f32_16x16x64_bf16 v[116:119], v[84:87], a[208:215], v19\n\t" \
    "s_mov_b32 exec_hi, 0\n\t" \
    "global_store_dwordx2 v153, v[136:137], s[38:39] sc1\n\t" \
    "s_mov_b32 exec_hi, -1\n\t" \
    "v_cvt_pk_bf16_f32 v128, v108, v109\n\t" \
    "v_smfmac_f32_16x16x64_bf16 v[116:119], v[88:91], a[216:223], v19\n\t" \
    "s_nop 1\n\t" \
    "v_cvt_pk_bf16_f32 v129, v110, v111\n\t" \
    "v_smfmac_f32_16x16x64_bf16 v[116:119], v[92:95], a[224:231], v19\n\t" \
    "s_nop 1\n\t" \
    "v_cvt_pk_bf16_f32 v130, v112, v113\n\t" \
    "v_smfmac_f32_16x16x64_bf16 v[116:119], v[96:99], a[232:239], v19\n\t" \
    "s_nop 1\n\t" \
    "v_cvt_pk_bf16_f32 v131, v114, v115\n\t" \
    "v_smfmac_f32_16x16x64_bf16 v[120:123], v[84:87], a[240:247], v19\n\t" \
    "s_nop 1\n\t" \
    "v_permlane16_swap_b32 v128, v130\n\t" \
    "v_smfmac_f32_16x16x64_bf16 v[120:123], v[88:91], a[248:255], v19\n\t" \
    "s_nop 1\n\t" \
    "v_permlane16_swap_b32 v129, v131\n\t" \
    "v_smfmac_f32_16x16x64_bf16 v[120:123], v[92:95], v[224:231], v19\n\t" \
    "s_nop 0\n\t" \
    "global_store_dwordx4 v151, v[128:131], s[38:39] sc1\n\t" \
    "v_cvt_pk_bf16_f32 v132, v116, v117\n\t" \
    "v_smfmac_f32_16x16x64_bf16 v[120:123], v[96:99], v[232:239], v19\n\t" \
    "s_nop 1\n\t" \
    "v_cvt_pk_bf16_f32 v133, v118, v119\n\t" \
    "s_nop 4\n\t" \
    "v_cvt_pk_bf16_f32 v134, v120, v121\n\t" \
    "v_cvt_pk_bf16_f32 v135, v122, v123\n\t" \
    "s_nop 0\n\t" \
    "v_permlane16_swap_b32 v132, v134\n\t" \
    "v_permlane16_swap_b32 v133, v135\n\t" \
    "global_store_dwordx4 v152, v[132:135], s[38:39] sc1\n\t" \
    "s_cmp_eq_u32 s65, 63\n\t" \
    "s_cbranch_scc1 vv_end15_%=\n\t" \
    "s_cmp_lg_u32 s66, 0\n\t" \
    "s_cbranch_scc1 vv_SF25_%=\n\t" \
    "s_lshl_b32 s69, s67, 7\n\t" \
    "s_add_u32 s69, s69, 3182592\n\t" \
    "s_add_u32 s70, s65, -1\n\t" \
    "v_mov_b32 v142, s69\n\t" \
    "v_mov_b32 v143, s70\n\t" \
    "s_mov_b64 exec, 1\n\t" \
    "global_store_dword v142, v143, s[100:101] sc1\n\t" \
    "s_mov_b64 exec, -1\n\t" \
    "vv_SF25_%=:\n\t" \
    "s_add_u32 s65, s65, 1\n\t" \
    "v_mov_b32 v72, 0\n\t" \
    "v_mov_b32 v73, 0\n\t" \
    "v_mov_b32 v74, 0\n\t" \
    "v_mov_b32 v75, 0\n\t" \
    "v_mov_b32 v76, 0\n\t" \
    "v_mov_b32 v77, 0\n\t" \
    "v_mov_b32 v78, 0\n\t" \
    "v_mov_b32 v79, 0\n\t" \
    "v_mov_b32 v108, 0\n\t" \
    "v_mov_b32 v109, 0\n\t" \
    "v_mov_b32 v110, 0\n\t" \
    "v_mov_b32 v111, 0\n\t" \
    "v_mov_b32 v112, 0\n\t" \
    "v_mov_b32 v113, 0\n\t" \
    "v_mov_b32 v114, 0\n\t" \
    "v_mov_b32 v115, 0\n\t" \
    "v_mov_b32 v116, 0\n\t" \
    "v_mov_b32 v117, 0\n\t" \
    "v_mov_b32 v118, 0\n\t" \
    "v_mov_b32 v119, 0\n\t" \
    "v_mov_b32 v120, 0\n\t" \
    "v_mov_b32 v121, 0\n\t" \
    "v_mov_b32 v122, 0\n\t" \
    "v_mov_b32 v123, 0\n\t" \
    "v_mov_b32 v124, 0\n\t" \
    "v_mov_b32 v125, 0\n\t" \
    "v_mov_b32 v126, 0\n\t" \
    "v_mov_b32 v127, 0\n\t" \
    "s_add_u32 s69, s65, 0\n\t" \
    "s_and_b32 s69, s69, 7\n\t" \
    "s_lshl_b32 s69, s69, 18\n\t" \
    "s_add_u32 s69, s69, s62\n\t" \
    "s_add_u32 s46, s36, s69\n\t" \
    "s_addc_u32 s47, s37, 0\n\t" \
    "s_bitcmp0_b32 s65, 0\n\t" \
    "s_cselect_b32 s69, 16384, 0\n\t" \
    "v_xor_b32 v1, s69, v1\n\t" \
    "v_xor_b32 v2, s69, v2\n\t" \
    "v_xor_b32 v23, s69, v23\n\t" \
    "s_add_u32 s70, s65, 0\n\t" \
    "s_mul_i32 s69, s70, 171\n\t" \
    "s_lshr_b32 s69, s69, 9\n\t" \
    "s_mul_i32 s71, s69, 3\n\t" \
    "s_sub_u32 s70, s70, s71\n\t" \
    "s_mul_i32 s70, s70, 589824\n\t" \
    "s_lshl1_add_u32 s71, s69, 4\n\t" \
    "s_lshr_b64 s[38:39], s[44:45], s71\n\t" \
    "s_lshl_b32 s71, s66, 1\n\t" \
    "s_xor_b32 s71, s71, s38\n\t" \
    "s_and_b32 s71, s71, 3\n\t" \
    "s_lshl_b32 s71, s71, 13\n\t" \
    "s_add_u32 s70, s70, s71\n\t" \
    "s_lshl_b32 s69, s69, 21\n\t" \
    "s_add_u32 s69, s69, 123731968\n\t" \
    "v_add_u32 v151, s70, v14\n\t" \
    "v_and_b32 v150, 0x80000, v151\n\t" \
    "v_lshrrev_b32 v150, 5, v150\n\t" \
    "v_xor_b32 v151, v151, v150\n\t" \
    "v_add_u32 v152, s70, v15\n\t" \
    "v_and_b32 v150, 0x80000, v152\n\t" \
    "v_lshrrev_b32 v150, 5, v150\n\t" \
    "v_xor_b32 v152, v152, v150\n\t" \
    "v_add_u32 v153, s70, v16\n\t" \
    "v_and_b32 v150, 0x80000, v153\n\t" \
    "v_lshrrev_b32 v150, 5, v150\n\t" \
    "v_xor_b32 v153, v153, v150\n\t" \
    "s_add_u32 s38, s36, s69\n\t" \
    "s_addc_u32 s39, s37, 0\n\t" \
    "s_and_b32 s69, s69, 0xffe00000\n\t" \
    "s_add_u32 s69, s69, 1807360\n\t" \
    "v_mov_b32 v17, s69\n\t" \
    "global_load_dword v17, v17, s[36:37]\n\t" \
    "s_and_b32 s69, s65, 7\n\t" \
    "s_lshl_b32 s69, s69, 18\n\t" \
    "s_add_u32 s69, s69, s63\n\t" \
    "s_add_u32 s52, s36, s69\n\t" \
    "s_addc_u32 s53, s37, 0\n\t" \
    "s_barrier\n\t" \
    "ds_read_b32 v160, v11 offset:20480\n\t" \
    "s_waitcnt lgkmcnt(0)\n\t" \
    "v_readfirstlane_b32 s70, v160\n\t" \
    "s_branch vv_loop11_%=\n\t" \
    "vv_Gbad20_%=:\n\t" \
    "s_waitcnt vmcnt(0) lgkmcnt(0)\n\t" \
    "s_nop 7\n\t" \
    "v_mov_b32 v72, 0\n\t" \
    "v_mov_b32 v73, 0\n\t" \
    "v_mov_b32 v74, 0\n\t" \
    "v_mov_b32 v75, 0\n\t" \
    "v_mov_b32 v76, 0\n\t" \
    "v_mov_b32 v77, 0\n\t" \
    "v_mov_b32 v78, 0\n\t" \
    "v_mov_b32 v79, 0\n\t" \
    "s_cmp_eq_u32 s68, 0\n\t" \
    "s_cbranch_scc1 vv_F26_%=\n\t" \
    "s_sub_u32 s68, s68, 1\n\t" \
    "s_branch vv_G19_%=\n\t" \
    "vv_F26_%=:\n\t" \
    "v_mov_b32 v140, 5767424\n\t" \
    "v_mov_b32 v141, 1\n\t" \
    "global_store_dword v140, v141, s[36:37] sc1\n\t" \
    "s_waitcnt vmcnt(0)\n\t" \
    "s_branch vv_Gc21_%=\n\t" \
    "vv_Gslow22_%=:\n\t" \
    "v_max_u32 v140, v36, v37\n\t" \
    "v_max3_u32 v140, v140, v38, v39\n\t" \
    "v_max3_u32 v140, v140, v40, v41\n\t" \
    "v_max3_u32 v140, v140, v42, v43\n\t" \
    "v_max3_u32 v140, v140, v44, v45\n\t" \
    "v_max3_u32 v140, v140, v46, v47\n\t" \
    "v_max3_u32 v140, v140, v48, v49\n\t" \
    "v_max3_u32 v140, v140, v50, v51\n\t" \
    "v_max3_u32 v140, v140, v52, v53\n\t" \
    "v_max3_u32 v140, v140, v54, v55\n\t" \
    "v_max3_u32 v140, v140, v56, v57\n\t" \
    "v_max3_u32 v140, v140, v58, v59\n\t" \
    "v_max3_u32 v140, v140, v60, v61\n\t" \
    "v_max3_u32 v140, v140, v62, v63\n\t" \
    "v_max3_u32 v140, v140, v64, v65\n\t" \
    "v_max3_u32 v140, v140, v66, v67\n\t" \
    "v_max3_u32 v140, v140, v68, v69\n\t" \
    "v_max3_u32 v140, v140, v70, v71\n\t" \
    "v_cmp_eq_u32 vcc, -1, v140\n\t" \
    "s_cbranch_vccz vv_Gback23_%=\n\t" \
    "s_waitcnt lgkmcnt(0)\n\t" \
    "v_sub_u32 v140, v1, v3\n\t" \
    "s_nop 0\n\t" \
    "v_readfirstlane_b32 s69, v140\n\t" \
    "v_add_u32 v143, 36864, v0\n\t" \
    "s_nop 7\n\t" \
    "s_add_u32 s30, s46, s69\n\t" \
    "s_addc_u32 s31, s47, 0\n\t" \
    "vv_Gag27_%=:\n\t" \
    "global_load_dwordx4 v[36:39], v1, s[46:47] sc1\n\t" \
    "global_load_dwordx4 v[40:43], v23, s[46:47] sc1\n\t" \
    "global_load_dwordx4 v[44:47], v143, s[30:31] sc1\n\t" \
    "global_load_dwordx4 v[48:51], v4, s[30:31] sc1\n\t" \
    "global_load_dwordx4 v[52:55], v5, s[30:31] sc1\n\t" \
    "global_load_dwordx4 v[56:59], v6, s[30:31] sc1\n\t" \
    "global_load_dwordx4 v[60:63], v7, s[30:31] sc1\n\t" \
    "global_load_dwordx4 v[64:67], v8, s[30:31] sc1\n\t" \
    "global_load_dwordx4 v[68:71], v9, s[30:31] sc1\n\t" \
    "s_waitcnt vmcnt(0)\n\t" \
    "v_max_u32 v140, v36, v37\n\t" \
    "v_max3_u32 v140, v140, v38, v39\n\t" \
    "v_max3_u32 v140, v140, v40, v41\n\t" \
    "v_max3_u32 v140, v140, v42, v43\n\t" \
    "v_max3_u32 v140, v140, v44, v45\n\t" \
    "v_max3_u32 v140, v140, v46, v47\n\t" \
    "v_max3_u32 v140, v140, v48, v49\n\t" \
    "v_max3_u32 v140, v140, v50, v51\n\t" \
    "v_max3_u32 v140, v140, v52, v53\n\t" \
    "v_max3_u32 v140, v140, v54, v55\n\t" \
    "v_max3_u32 v140, v140, v56, v57\n\t" \
    "v_max3_u32 v140, v140, v58, v59\n\t" \
    "v_max3_u32 v140, v140, v60, v61\n\t" \
    "v_max3_u32 v140, v140, v62, v63\n\t" \
    "v_max3_u32 v140, v140, v64, v65\n\t" \
    "v_max3_u32 v140, v140, v66, v67\n\t" \
    "v_max3_u32 v140, v140, v68, v69\n\t" \
    "v_max3_u32 v140, v140, v70, v71\n\t" \
    "v_cmp_eq_u32 vcc, -1, v140\n\t" \
    "s_cbranch_vccz vv_Gredo28_%=\n\t" \
    "s_cmp_eq_u32 s68, 0\n\t" \
    "s_cbranch_scc1 vv_F29_%=\n\t" \
    "s_sub_u32 s68, s68, 1\n\t" \
    "s_sleep 1\n\t" \
    "s_branch vv_Gag27_%=\n\t" \
    "vv_F29_%=:\n\t" \
    "v_mov_b32 v140, 5767424\n\t" \
    "v_mov_b32 v141, 2\n\t" \
    "global_store_dword v140, v141, s[36:37] sc1\n\t" \
    "s_waitcnt vmcnt(0)\n\t" \
    "s_branch vv_Gredo28_%=\n\t" \
    "vv_Gredo28_%=:\n\t" \
    "v_mov_b32 v72, 0\n\t" \
    "v_mov_b32 v73, 0\n\t" \
    "v_mov_b32 v74, 0\n\t" \
    "v_mov_b32 v75, 0\n\t" \
    "v_mov_b32 v76, 0\n\t" \
    "v_mov_b32 v77, 0\n\t" \
    "v_mov_b32 v78, 0\n\t" \
    "v_mov_b32 v79, 0\n\t" \
    "v_smfmac_f32_16x16x64_bf16 v[72:75], v[36:39], a[0:7], v10\n\t" \
    "v_smfmac_f32_16x16x64_bf16 v[72:75], v[40:43], a[8:15], v10\n\t" \
    "v_smfmac_f32_16x16x64_bf16 v[72:75], v[48:51], a[16:23], v10\n\t" \
    "v_smfmac_f32_16x16x64_bf16 v[72:75], v[52:55], a[24:31], v10\n\t" \
    "v_smfmac_f32_16x16x64_bf16 v[72:75], v[56:59], a[32:39], v10\n\t" \
    "v_smfmac_f32_16x16x64_bf16 v[72:75], v[60:63], a[40:47], v10\n\t" \
    "v_smfmac_f32_16x16x64_bf16 v[72:75], v[64:67], a[48:55], v10\n\t" \
    "v_smfmac_f32_16x16x64_bf16 v[72:75], v[68:71], a[56:63], v10\n\t" \
    "v_smfmac_f32_16x16x64_bf16 v[72:75], v[44:47], a[64:71], v10\n\t" \
    "v_smfmac_f32_16x16x64_bf16 v[76:79], v[36:39], a[72:79], v10\n\t" \
    "v_smfmac_f32_16x16x64_bf16 v[76:79], v[40:43], a[80:87], v10\n\t" \
    "v_smfmac_f32_16x16x64_bf16 v[76:79], v[48:51], a[88:95], v10\n\t" \
    "v_smfmac_f32_16x16x64_bf16 v[76:79], v[52:55], a[96:103], v10\n\t" \
    "v_smfmac_f32_16x16x64_bf16 v[76:79], v[56:59], a[104:111], v10\n\t" \
    "v_smfmac_f32_16x16x64_bf16 v[76:79], v[60:63], a[112:119], v10\n\t" \
    "v_smfmac_f32_16x16x64_bf16 v[76:79], v[64:67], a[120:127], v10\n\t" \
    "v_smfmac_f32_16x16x64_bf16 v[76:79], v[68:71], a[128:135], v10\n\t" \
    "v_smfmac_f32_16x16x64_bf16 v[76:79], v[44:47], a[136:143], v10\n\t" \
    "v_add_f32 v72, v72, v74\n\t" \
    "v_add_f32 v73, v73, v75\n\t" \
    "v_cvt_pk_bf16_f32 v80, v72, v73\n\t" \
    "ds_write_b32 v12, v80 offset:0\n\t" \
    "s_nop 3\n\t" \
    "v_pk_add_f32 v[76:77], v[76:77], v[78:79]\n\t" \
    "v_cvt_pk_bf16_f32 v81, v76, v77\n\t" \
    "ds_write_b32 v12, v81 offset:256\n\t" \
    "s_branch vv_Gback23_%=\n\t" \
    "vv_end15_%=:\n\t" \
    "s_endpgm\n\t" \
    ".p2align 6\n\t" \
    "s_nop 0\n\t" \
    "s_nop 0\n\t" \
    "s_nop 0\n\t" \
    "s_nop 0\n\t" \
    "s_nop 0\n\t" \
    "s_nop 0\n\t" \
    "vv_loop12_%=:\n\t" \
    "s_getpc_b64 s[30:31]\n\t" \
    "vv_Wp32_%=:\n\t" \
    "s_add_u32 s30, s30, vv_We33_%=-vv_Wp32_%=\n\t" \
    "s_addc_u32 s31, s31, 0\n\t" \
    "s_sub_u32 s70, s70, -54\n\t" \
    "vv_W31_%=:\n\t" \
    "s_memtime s[56:57]\n\t" \
    "s_waitcnt lgkmcnt(0)\n\t" \
    "s_sub_u32 s69, s70, s56\n\t" \
    "s_cmp_gt_i32 s69, 80\n\t" \
    "s_cbranch_scc1 vv_W31_%=\n\t" \
    "s_max_i32 s69, s69, 0\n\t" \
    "s_and_b32 s69, s69, -4\n\t" \
    "s_sub_u32 s30, s30, s69\n\t" \
    "s_subb_u32 s31, s31, 0\n\t" \
    "s_setpc_b64 s[30:31]\n\t" \
    "s_nop 0\n\t" \
    "s_nop 0\n\t" \
    "s_nop 0\n\t" \
    "s_nop 0\n\t" \
    "s_nop 0\n\t" \
    "s_nop 0\n\t" \
    "s_nop 0\n\t" \
    "s_nop 0\n\t" \
    "s_nop 0\n\t" \
    "s_nop 0\n\t" \
    "s_nop 0\n\t" \
    "s_nop 0\n\t" \
    "s_nop 0\n\t" \
    "s_nop 0\n\t" \
    "s_nop 0\n\t" \
    "s_nop 0\n\t" \
    "s_nop 0\n\t" \
    "s_nop 0\n\t" \
    "s_nop 0\n\t" \
    "s_nop 0\n\t" \
    "vv_We33_%=:\n\t" \
    "vv_G34_%=:\n\t" \
    "global_load_dwordx4 v[36:39], v1, s[46:47] sc1\n\t" \
    "global_load_dword v141, v2, s[46:47] sc1\n\t" \
    "global_load_dwordx4 v[40:43], v23, s[46:47] sc1\n\t" \
    "s_waitcnt vmcnt(2)\n\t" \
    "vv_Garr39_%=:\n\t" \
    "ds_write_b128 v3, v[36:39]\n\t" \
    "v_smfmac_f32_16x16x64_bf16 v[72:75], v[36:39], a[16:23], v10\n\t" \
    "s_waitcnt vmcnt(1)\n\t" \
    "ds_write_b32 v22, v141 offset:19712\n\t" \
    "s_waitcnt vmcnt(0)\n\t" \
    "ds_write_b128 v3, v[40:43] offset:1024\n\t" \
    "v_max3_u32 v140, v36, v40, v141\n\t" \
    "v_cmp_eq_u32 vcc, -1, v140\n\t" \
    "s_cbranch_vccnz vv_Gbad35_%=\n\t" \
    "vv_Gc36_%=:\n\t" \
    "s_waitcnt lgkmcnt(0)\n\t" \
    "s_barrier\n\t" \
    "ds_read_b128 v[48:51], v4\n\t" \
    "ds_read_b128 v[52:55], v5\n\t" \
    "v_smfmac_f32_16x16x64_bf16 v[76:79], v[36:39], a[88:95], v10\n\t" \
    "ds_read_b128 v[56:59], v6\n\t" \
    "ds_read_b128 v[60:63], v7\n\t" \
    "v_smfmac_f32_16x16x64_bf16 v[72:75], v[40:43], a[24:31], v10\n\t" \
    "ds_read_b128 v[64:67], v8\n\t" \
    "v_smfmac_f32_16x16x64_bf16 v[76:79], v[40:43], a[96:103], v10\n\t" \
    "ds_read_b128 v[68:71], v9\n\t" \
    "s_waitcnt lgkmcnt(5)\n\t" \
    "v_smfmac_f32_16x16x64_bf16 v[72:75], v[48:51], a[32:39], v10\n\t" \
    "ds_read_b128 v[44:47], v0 offset:19456\n\t" \
    "s_waitcnt lgkmcnt(5)\n\t" \
    "v_smfmac_f32_16x16x64_bf16 v[72:75], v[52:55], a[40:47], v10\n\t" \
    "s_waitcnt lgkmcnt(4)\n\t" \
    "v_smfmac_f32_16x16x64_bf16 v[72:75], v[56:59], a[48:55], v10\n\t" \
    "s_waitcnt lgkmcnt(3)\n\t" \
    "v_smfmac_f32_16x16x64_bf16 v[72:75], v[60:63], a[56:63], v10\n\t" \
    "s_waitcnt lgkmcnt(2)\n\t" \
    "v_smfmac_f32_16x16x64_bf16 v[72:75], v[64:67], a[0:7], v10\n\t" \
    "s_waitcnt lgkmcnt(1)\n\t" \
    "v_smfmac_f32_16x16x64_bf16 v[72:75], v[68:71], a[8:15], v10\n\t" \
    "s_waitcnt lgkmcnt(0)\n\t" \
    "v_smfmac_f32_16x16x64_bf16 v[72:75], v[44:47], a[64:71], v10\n\t" \
    "s_nop 2\n\t" \
    "v_smfmac_f32_16x16x64_bf16 v[76:79], v[48:51], a[104:111], v10\n\t" \
    "s_nop 2\n\t" \
    "v_smfmac_f32_16x16x64_bf16 v[76:79], v[52:55], a[112:119], v10\n\t" \
    "v_add_f32 v72, v72, v74\n\t" \
    "v_smfmac_f32_16x16x64_bf16 v[76:79], v[56:59], a[120:127], v10\n\t" \
    "v_add_f32 v73, v73, v75\n\t" \
    "v_smfmac_f32_16x16x64_bf16 v[76:79], v[60:63], a[128:135], v10\n\t" \
    "v_cvt_pk_bf16_f32 v80, v72, v73\n\t" \
    "v_smfmac_f32_16x16x64_bf16 v[76:79], v[64:67], a[72:79], v10\n\t" \
    "ds_write_b32 v12, v80 offset:0\n\t" \
    "v_smfmac_f32_16x16x64_bf16 v[76:79], v[68:71], a[80:87], v10\n\t" \
    "v_smfmac_f32_16x16x64_bf16 v[76:79], v[44:47], a[136:143], v10\n\t" \
    "s_nop 6\n\t" \
    "v_cmp_u_f32 vcc, v72, v73\n\t" \
    "v_pk_add_f32 v[76:77], v[76:77], v[78:79]\n\t" \
    "v_cvt_pk_bf16_f32 v81, v76, v77\n\t" \
    "ds_write_b32 v12, v81 offset:256\n\t" \
    "s_cbranch_vccnz vv_Gslow37_%=\n\t" \
    "vv_Gback38_%=:\n\t" \
    "s_waitcnt lgkmcnt(0)\n\t" \
    "s_barrier\n\t" \
    "ds_read_b64_tr_b16 v[84:85], v13 offset:0\n\t" \
    "v_smfmac_f32_16x16x64_bf16 v[100:103], v[36:39], a[0:7], v10\n\t" \
    "ds_read_b64_tr_b16 v[86:87], v13 offset:64\n\t" \
    "ds_read_b64_tr_b16 v[92:93], v13 offset:1152\n\t" \
    "v_smfmac_f32_16x16x64_bf16 v[100:103], v[36:39], a[0:7], v10\n\t" \
    "ds_read_b64_tr_b16 v[94:95], v13 offset:1216\n\t" \
    "ds_read_b64_tr_b16 v[88:89], v25 offset:0\n\t" \
    "v_smfmac_f32_16x16x64_bf16 v[100:103], v[36:39], a[0:7], v10\n\t" \
    "ds_read_b64_tr_b16 v[90:91], v25 offset:64\n\t" \
    "s_waitcnt lgkmcnt(4)\n\t" \
    "v_smfmac_f32_16x16x64_bf16 v[124:127], v[84:87], v[240:247], v19\n\t" \
    "ds_read_b64_tr_b16 v[96:97], v25 offset:1152\n\t" \
    "ds_read_b64_tr_b16 v[98:99], v25 offset:1216\n\t" \
    "s_waitcnt lgkmcnt(4)\n\t" \
    "v_smfmac_f32_16x16x64_bf16 v[124:127], v[92:95], v[248:255], v19\n\t" \
    "s_nop 2\n\t" \
    "v_smfmac_f32_16x16x64_bf16 v[108:111], v[84:87], a[144:151], v19\n\t" \
    "s_nop 2\n\t" \
    "v_smfmac_f32_16x16x64_bf16 v[108:111], v[92:95], a[160:167], v19\n\t" \
    "s_nop 1\n\t" \
    "v_permlane32_swap_b32 v124, v126\n\t" \
    "v_smfmac_f32_16x16x64_bf16 v[112:115], v[84:87], a[176:183], v19\n\t" \
    "s_nop 1\n\t" \
    "v_permlane32_swap_b32 v125, v127\n\t" \
    "v_smfmac_f32_16x16x64_bf16 v[112:115], v[92:95], a[192:199], v19\n\t" \
    "s_nop 0\n\t" \
    "v_add_f32 v124, v124, v126\n\t" \
    "s_waitcnt lgkmcnt(2)\n\t" \
    "v_smfmac_f32_16x16x64_bf16 v[108:111], v[88:91], a[152:159], v19\n\t" \
    "s_nop 1\n\t" \
    "v_add_f32 v125, v125, v127\n\t" \
    "v_smfmac_f32_16x16x64_bf16 v[112:115], v[88:91], a[184:191], v19\n\t" \
    "s_nop 0\n\t" \
    "v_cvt_pk_bf16_f32 v136, v124, v125\n\t" \
    "s_waitcnt lgkmcnt(0)\n\t" \
    "v_smfmac_f32_16x16x64_bf16 v[108:111], v[96:99], a[168:175], v19\n\t" \
    "s_nop 1\n\t" \
    "v_cvt_pk_bf16_f32 v137, v124, v125\n\t" \
    "v_smfmac_f32_16x16x64_bf16 v[112:115], v[96:99], a[200:207], v19\n\t" \
    "s_nop 1\n\t" \
    "v_permlane32_swap_b32 v136, v137\n\t" \
    "v_smfmac_f32_16x16x64_bf16 v[116:119], v[84:87], a[208:215], v19\n\t" \
    "s_mov_b32 exec_hi, 0\n\t" \
    "global_store_dwordx2 v153, v[136:137], s[38:39] sc1\n\t" \
    "s_mov_b32 exec_hi, -1\n\t" \
    "v_cvt_pk_bf16_f32 v128, v108, v109\n\t" \
    "v_smfmac_f32_16x16x64_bf16 v[116:119], v[88:91], a[216:223], v19\n\t" \
    "s_nop 1\n\t" \
    "v_cvt_pk_bf16_f32 v129, v110, v111\n\t" \
    "v_smfmac_f32_16x16x64_bf16 v[116:119], v[92:95], a[224:231], v19\n\t" \
    "s_nop 1\n\t" \
    "v_cvt_pk_bf16_f32 v130, v112, v113\n\t" \
    "v_smfmac_f32_16x16x64_bf16 v[116:119], v[96:99], a[232:239], v19\n\t" \
    "s_nop 1\n\t" \
    "v_cvt_pk_bf16_f32 v131, v114, v115\n\t" \
    "v_smfmac_f32_16x16x64_bf16 v[120:123], v[84:87], a[240:247], v19\n\t" \
    "s_nop 1\n\t" \
    "v_permlane16_swap_b32 v128, v130\n\t" \
    "v_smfmac_f32_16x16x64_bf16 v[120:123], v[88:91], a[248:255], v19\n\t" \
    "s_nop 1\n\t" \
    "v_permlane16_swap_b32 v129, v131\n\t" \
    "v_smfmac_f32_16x16x64_bf16 v[120:123], v[92:95], v[224:231], v19\n\t" \
    "s_nop 0\n\t" \
    "global_store_dwordx4 v151, v[128:131], s[38:39] sc1\n\t" \
    "v_cvt_pk_bf16_f32 v132, v116, v117\n\t" \
    "v_smfmac_f32_16x16x64_bf16 v[120:123], v[96:99], v[232:239], v19\n\t" \
    "s_nop 1\n\t" \
    "v_cvt_pk_bf16_f32 v133, v118, v119\n\t" \
    "s_nop 4\n\t" \
    "v_cvt_pk_bf16_f32 v134, v120, v121\n\t" \
    "v_cvt_pk_bf16_f32 v135, v122, v123\n\t" \
    "s_nop 0\n\t" \
    "v_permlane16_swap_b32 v132, v134\n\t" \
    "v_permlane16_swap_b32 v133, v135\n\t" \
    "global_store_dwordx4 v152, v[132:135], s[38:39] sc1\n\t" \
    "s_cmp_eq_u32 s65, 63\n\t" \
    "s_cbranch_scc1 vv_end30_%=\n\t" \
    "s_add_u32 s65, s65, 1\n\t" \
    "v_mov_b32 v72, 0\n\t" \
    "v_mov_b32 v73, 0\n\t" \
    "v_mov_b32 v74, 0\n\t" \
    "v_mov_b32 v75, 0\n\t" \
    "v_mov_b32 v76, 0\n\t" \
    "v_mov_b32 v77, 0\n\t" \
    "v_mov_b32 v78, 0\n\t" \
    "v_mov_b32 v79, 0\n\t" \
    "v_mov_b32 v108, 0\n\t" \
    "v_mov_b32 v109, 0\n\t" \
    "v_mov_b32 v110, 0\n\t" \
    "v_mov_b32 v111, 0\n\t" \
    "v_mov_b32 v112, 0\n\t" \
    "v_mov_b32 v113, 0\n\t" \
    "v_mov_b32 v114, 0\n\t" \
    "v_mov_b32 v115, 0\n\t" \
    "v_mov_b32 v116, 0\n\t" \
    "v_mov_b32 v117, 0\n\t" \
    "v_mov_b32 v118, 0\n\t" \
    "v_mov_b32 v119, 0\n\t" \
    "v_mov_b32 v120, 0\n\t" \
    "v_mov_b32 v121, 0\n\t" \
    "v_mov_b32 v122, 0\n\t" \
    "v_mov_b32 v123, 0\n\t" \
    "v_mov_b32 v124, 0\n\t" \
    "v_mov_b32 v125, 0\n\t" \
    "v_mov_b32 v126, 0\n\t" \
    "v_mov_b32 v127, 0\n\t" \
    "s_add_u32 s69, s65, 0\n\t" \
    "s_and_b32 s69, s69, 7\n\t" \
    "s_lshl_b32 s69, s69, 18\n\t" \
    "s_add_u32 s69, s69, s62\n\t" \
    "s_add_u32 s46, s36, s69\n\t" \
    "s_addc_u32 s47, s37, 0\n\t" \
    "s_bitcmp0_b32 s65, 0\n\t" \
    "s_cselect_b32 s69, 16384, 0\n\t" \
    "v_xor_b32 v1, s69, v1\n\t" \
    "v_xor_b32 v2, s69, v2\n\t" \
    "v_xor_b32 v23, s69, v23\n\t" \
    "s_add_u32 s70, s65, 0\n\t" \
    "s_mul_i32 s69, s70, 171\n\t" \
    "s_lshr_b32 s69, s69, 9\n\t" \
    "s_mul_i32 s71, s69, 3\n\t" \
    "s_sub_u32 s70, s70, s71\n\t" \
    "s_mul_i32 s70, s70, 589824\n\t" \
    "s_lshl1_add_u32 s71, s69, 4\n\t" \
    "s_lshr_b64 s[38:39], s[44:45], s71\n\t" \
    "s_lshl_b32 s71, s66, 1\n\t" \
    "s_xor_b32 s71, s71, s38\n\t" \
    "s_and_b32 s71, s71, 3\n\t" \
    "s_lshl_b32 s71, s71, 13\n\t" \
    "s_add_u32 s70, s70, s71\n\t" \
    "s_lshl_b32 s69, s69, 21\n\t" \
    "s_add_u32 s69, s69, 123731968\n\t" \
    "v_add_u32 v151, s70, v14\n\t" \
    "v_and_b32 v150, 0x80000, v151\n\t" \
    "v_lshrrev_b32 v150, 5, v150\n\t" \
    "v_xor_b32 v151, v151, v150\n\t" \
    "v_add_u32 v152, s70, v15\n\t" \
    "v_and_b32 v150, 0x80000, v152\n\t" \
    "v_lshrrev_b32 v150, 5, v150\n\t" \
    "v_xor_b32 v152, v152, v150\n\t" \
    "v_add_u32 v153, s70, v16\n\t" \
    "v_and_b32 v150, 0x80000, v153\n\t" \
    "v_lshrrev_b32 v150, 5, v150\n\t" \
    "v_xor_b32 v153, v153, v150\n\t" \
    "s_add_u32 s38, s36, s69\n\t" \
    "s_addc_u32 s39, s37, 0\n\t" \
    "s_and_b32 s69, s69, 0xffe00000\n\t" \
    "s_add_u32 s69, s69, 1807360\n\t" \
    "v_mov_b32 v17, s69\n\t" \
    "global_load_dword v17, v17, s[36:37]\n\t" \
    "s_and_b32 s69, s65, 7\n\t" \
    "s_lshl_b32 s69, s69, 18\n\t" \
    "s_add_u32 s69, s69, s63\n\t" \
    "s_add_u32 s52, s36, s69\n\t" \
    "s_addc_u32 s53, s37, 0\n\t" \
    "s_barrier\n\t" \
    "ds_read_b32 v160, v11 offset:20480\n\t" \
    "s_waitcnt lgkmcnt(0)\n\t" \
    "v_readfirstlane_b32 s70, v160\n\t" \
    "s_branch vv_loop12_%=\n\t" \
    "vv_Gbad35_%=:\n\t" \
    "s_waitcnt vmcnt(0) lgkmcnt(0)\n\t" \
    "s_nop 7\n\t" \
    "v_mov_b32 v72, 0\n\t" \
    "v_mov_b32 v73, 0\n\t" \
    "v_mov_b32 v74, 0\n\t" \
    "v_mov_b32 v75, 0\n\t" \
    "v_mov_b32 v76, 0\n\t" \
    "v_mov_b32 v77, 0\n\t" \
    "v_mov_b32 v78, 0\n\t" \
    "v_mov_b32 v79, 0\n\t" \
    "s_cmp_eq_u32 s68, 0\n\t" \
    "s_cbranch_scc1 vv_F40_%=\n\t" \
    "s_sub_u32 s68, s68, 1\n\t" \
    "s_branch vv_G34_%=\n\t" \
    "vv_F40_%=:\n\t" \
    "v_mov_b32 v140, 5767424\n\t" \
    "v_mov_b32 v141, 1\n\t" \
    "global_store_dword v140, v141, s[36:37] sc1\n\t" \
    "s_waitcnt vmcnt(0)\n\t" \
    "s_branch vv_Gc36_%=\n\t" \
    "vv_Gslow37_%=:\n\t" \
    "v_max_u32 v140, v36, v37\n\t" \
    "v_max3_u32 v140, v140, v38, v39\n\t" \
    "v_max3_u32 v140, v140, v40, v41\n\t" \
    "v_max3_u32 v140, v140, v42, v43\n\t" \
    "v_max3_u32 v140, v140, v44, v45\n\t" \
    "v_max3_u32 v140, v140, v46, v47\n\t" \
    "v_max3_u32 v140, v140, v48, v49\n\t" \
    "v_max3_u32 v140, v140, v50, v51\n\t" \
    "v_max3_u32 v140, v140, v52, v53\n\t" \
    "v_max3_u32 v140, v140, v54, v55\n\t" \
    "v_max3_u32 v140, v140, v56, v57\n\t" \
    "v_max3_u32 v140, v140, v58, v59\n\t" \
    "v_max3_u32 v140, v140, v60, v61\n\t" \
    "v_max3_u32 v140, v140, v62, v63\n\t" \
    "v_max3_u32 v140, v140, v64, v65\n\t" \
    "v_max3_u32 v140, v140, v66, v67\n\t" \
    "v_max3_u32 v140, v140, v68, v69\n\t" \
    "v_max3_u32 v140, v140, v70, v71\n\t" \
    "v_cmp_eq_u32 vcc, -1, v140\n\t" \
    "s_cbranch_vccz vv_Gback38_%=\n\t" \
    "s_waitcnt lgkmcnt(0)\n\t" \
    "v_sub_u32 v140, v1, v3\n\t" \
    "s_nop 0\n\t" \
    "v_readfirstlane_b32 s69, v140\n\t" \
    "v_add_u32 v143, 36864, v0\n\t" \
    "s_nop 7\n\t" \
    "s_add_u32 s30, s46, s69\n\t" \
    "s_addc_u32 s31, s47, 0\n\t" \
    "vv_Gag41_%=:\n\t" \
    "global_load_dwordx4 v[36:39], v1, s[46:47] sc1\n\t" \
    "global_load_dwordx4 v[40:43], v23, s[46:47] sc1\n\t" \
    "global_load_dwordx4 v[44:47], v143, s[30:31] sc1\n\t" \
    "global_load_dwordx4 v[48:51], v4, s[30:31] sc1\n\t" \
    "global_load_dwordx4 v[52:55], v5, s[30:31] sc1\n\t" \
    "global_load_dwordx4 v[56:59], v6, s[30:31] sc1\n\t" \
    "global_load_dwordx4 v[60:63], v7, s[30:31] sc1\n\t" \
    "global_load_dwordx4 v[64:67], v8, s[30:31] sc1\n\t" \
    "global_load_dwordx4 v[68:71], v9, s[30:31] sc1\n\t" \
    "s_waitcnt vmcnt(0)\n\t" \
    "v_max_u32 v140, v36, v37\n\t" \
    "v_max3_u32 v140, v140, v38, v39\n\t" \
    "v_max3_u32 v140, v140, v40, v41\n\t" \
    "v_max3_u32 v140, v140, v42, v43\n\t" \
    "v_max3_u32 v140, v140, v44, v45\n\t" \
    "v_max3_u32 v140, v140, v46, v47\n\t" \
    "v_max3_u32 v140, v140, v48, v49\n\t" \
    "v_max3_u32 v140, v140, v50, v51\n\t" \
    "v_max3_u32 v140, v140, v52, v53\n\t" \
    "v_max3_u32 v140, v140, v54, v55\n\t" \
    "v_max3_u32 v140, v140, v56, v57\n\t" \
    "v_max3_u32 v140, v140, v58, v59\n\t" \
    "v_max3_u32 v140, v140, v60, v61\n\t" \
    "v_max3_u32 v140, v140, v62, v63\n\t" \
    "v_max3_u32 v140, v140, v64, v65\n\t" \
    "v_max3_u32 v140, v140, v66, v67\n\t" \
    "v_max3_u32 v140, v140, v68, v69\n\t" \
    "v_max3_u32 v140, v140, v70, v71\n\t" \
    "v_cmp_eq_u32 vcc, -1, v140\n\t" \
    "s_cbranch_vccz vv_Gredo42_%=\n\t" \
    "s_cmp_eq_u32 s68, 0\n\t" \
    "s_cbranch_scc1 vv_F43_%=\n\t" \
    "s_sub_u32 s68, s68, 1\n\t" \
    "s_sleep 1\n\t" \
    "s_branch vv_Gag41_%=\n\t" \
    "vv_F43_%=:\n\t" \
    "v_mov_b32 v140, 5767424\n\t" \
    "v_mov_b32 v141, 2\n\t" \
    "global_store_dword v140, v141, s[36:37] sc1\n\t" \
    "s_waitcnt vmcnt(0)\n\t" \
    "s_branch vv_Gredo42_%=\n\t" \
    "vv_Gredo42_%=:\n\t" \
    "v_mov_b32 v72, 0\n\t" \
    "v_mov_b32 v73, 0\n\t" \
    "v_mov_b32 v74, 0\n\t" \
    "v_mov_b32 v75, 0\n\t" \
    "v_mov_b32 v76, 0\n\t" \
    "v_mov_b32 v77, 0\n\t" \
    "v_mov_b32 v78, 0\n\t" \
    "v_mov_b32 v79, 0\n\t" \
    "v_smfmac_f32_16x16x64_bf16 v[72:75], v[36:39], a[16:23], v10\n\t" \
    "v_smfmac_f32_16x16x64_bf16 v[72:75], v[40:43], a[24:31], v10\n\t" \
    "v_smfmac_f32_16x16x64_bf16 v[72:75], v[48:51], a[32:39], v10\n\t" \
    "v_smfmac_f32_16x16x64_bf16 v[72:75], v[52:55], a[40:47], v10\n\t" \
    "v_smfmac_f32_16x16x64_bf16 v[72:75], v[56:59], a[48:55], v10\n\t" \
    "v_smfmac_f32_16x16x64_bf16 v[72:75], v[60:63], a[56:63], v10\n\t" \
    "v_smfmac_f32_16x16x64_bf16 v[72:75], v[64:67], a[0:7], v10\n\t" \
    "v_smfmac_f32_16x16x64_bf16 v[72:75], v[68:71], a[8:15], v10\n\t" \
    "v_smfmac_f32_16x16x64_bf16 v[72:75], v[44:47], a[64:71], v10\n\t" \
    "v_smfmac_f32_16x16x64_bf16 v[76:79], v[36:39], a[88:95], v10\n\t" \
    "v_smfmac_f32_16x16x64_bf16 v[76:79], v[40:43], a[96:103], v10\n\t" \
    "v_smfmac_f32_16x16x64_bf16 v[76:79], v[48:51], a[104:111], v10\n\t" \
    "v_smfmac_f32_16x16x64_bf16 v[76:79], v[52:55], a[112:119], v10\n\t" \
    "v_smfmac_f32_16x16x64_bf16 v[76:79], v[56:59], a[120:127], v10\n\t" \
    "v_smfmac_f32_16x16x64_bf16 v[76:79], v[60:63], a[128:135], v10\n\t" \
    "v_smfmac_f32_16x16x64_bf16 v[76:79], v[64:67], a[72:79], v10\n\t" \
    "v_smfmac_f32_16x16x64_bf16 v[76:79], v[68:71], a[80:87], v10\n\t" \
    "v_smfmac_f32_16x16x64_bf16 v[76:79], v[44:47], a[136:143], v10\n\t" \
    "v_add_f32 v72, v72, v74\n\t" \
    "v_add_f32 v73, v73, v75\n\t" \
    "v_cvt_pk_bf16_f32 v80, v72, v73\n\t" \
    "ds_write_b32 v12, v80 offset:0\n\t" \
    "s_nop 3\n\t" \
    "v_pk_add_f32 v[76:77], v[76:77], v[78:79]\n\t" \
    "v_cvt_pk_bf16_f32 v81, v76, v77\n\t" \
    "ds_write_b32 v12, v81 offset:256\n\t" \
    "s_branch vv_Gback38_%=\n\t" \
    "vv_end30_%=:\n\t" \
    "s_endpgm\n\t" \
    ".p2align 6\n\t" \
    "s_nop 0\n\t" \
    "s_nop 0\n\t" \
    "s_nop 0\n\t" \
    "s_nop 0\n\t" \
    "s_nop 0\n\t" \
    "s_nop 0\n\t" \
    "vv_loop13_%=:\n\t" \
    "s_getpc_b64 s[30:31]\n\t" \
    "vv_Wp46_%=:\n\t" \
    "s_add_u32 s30, s30, vv_We47_%=-vv_Wp46_%=\n\t" \
    "s_addc_u32 s31, s31, 0\n\t" \
    "s_sub_u32 s70, s70, -54\n\t" \
    "vv_W45_%=:\n\t" \
    "s_memtime s[56:57]\n\t" \
    "s_waitcnt lgkmcnt(0)\n\t" \
    "s_sub_u32 s69, s70, s56\n\t" \
    "s_cmp_gt_i32 s69, 80\n\t" \
    "s_cbranch_scc1 vv_W45_%=\n\t" \
    "s_max_i32 s69, s69, 0\n\t" \
    "s_and_b32 s69, s69, -4\n\t" \
    "s_sub_u32 s30, s30, s69\n\t" \
    "s_subb_u32 s31, s31, 0\n\t" \
    "s_setpc_b64 s[30:31]\n\t" \
    "s_nop 0\n\t" \
    "s_nop 0\n\t" \
    "s_nop 0\n\t" \
    "s_nop 0\n\t" \
    "s_nop 0\n\t" \
    "s_nop 0\n\t" \
    "s_nop 0\n\t" \
    "s_nop 0\n\t" \
    "s_nop 0\n\t" \
    "s_nop 0\n\t" \
    "s_nop 0\n\t" \
    "s_nop 0\n\t" \
    "s_nop 0\n\t" \
    "s_nop 0\n\t" \
    "s_nop 0\n\t" \
    "s_nop 0\n\t" \
    "s_nop 0\n\t" \
    "s_nop 0\n\t" \
    "s_nop 0\n\t" \
    "s_nop 0\n\t" \
    "vv_We47_%=:\n\t" \
    "vv_G48_%=:\n\t" \
    "global_load_dwordx4 v[36:39], v1, s[46:47] sc1\n\t" \
    "global_load_dword v141, v2, s[46:47] sc1\n\t" \
    "global_load_dwordx4 v[40:43], v23, s[46:47] sc1\n\t" \
    "s_waitcnt vmcnt(2)\n\t" \
    "vv_Garr53_%=:\n\t" \
    "ds_write_b128 v3, v[36:39]\n\t" \
    "v_smfmac_f32_16x16x64_bf16 v[72:75], v[36:39], a[32:39], v10\n\t" \
    "s_waitcnt vmcnt(1)\n\t" \
    "ds_write_b32 v22, v141 offset:19968\n\t" \
    "s_waitcnt vmcnt(0)\n\t" \
    "ds_write_b128 v3, v[40:43] offset:1024\n\t" \
    "v_max3_u32 v140, v36, v40, v141\n\t" \
    "v_cmp_eq_u32 vcc, -1, v140\n\t" \
    "s_cbranch_vccnz vv_Gbad49_%=\n\t" \
    "vv_Gc50_%=:\n\t" \
    "s_waitcnt lgkmcnt(0)\n\t" \
    "s_barrier\n\t" \
    "ds_read_b128 v[48:51], v4\n\t" \
    "ds_read_b128 v[52:55], v5\n\t" \
    "v_smfmac_f32_16x16x64_bf16 v[76:79], v[36:39], a[104:111], v10\n\t" \
    "ds_read_b128 v[56:59], v6\n\t" \
    "ds_read_b128 v[60:63], v7\n\t" \
    "v_smfmac_f32_16x16x64_bf16 v[72:75], v[40:43], a[40:47], v10\n\t" \
    "ds_read_b128 v[64:67], v8\n\t" \
    "v_smfmac_f32_16x16x64_bf16 v[76:79], v[40:43], a[112:119], v10\n\t" \
    "ds_read_b128 v[68:71], v9\n\t" \
    "s_waitcnt lgkmcnt(5)\n\t" \
    "v_smfmac_f32_16x16x64_bf16 v[72:75], v[48:51], a[48:55], v10\n\t" \
    "ds_read_b128 v[44:47], v0 offset:19456\n\t" \
    "s_waitcnt lgkmcnt(5)\n\t" \
    "v_smfmac_f32_16x16x64_bf16 v[72:75], v[52:55], a[56:63], v10\n\t" \
    "s_waitcnt lgkmcnt(4)\n\t" \
    "v_smfmac_f32_16x16x64_bf16 v[72:75], v[56:59], a[0:7], v10\n\t" \
    "s_waitcnt lgkmcnt(3)\n\t" \
    "v_smfmac_f32_16x16x64_bf16 v[72:75], v[60:63], a[8:15], v10\n\t" \
    "s_waitcnt lgkmcnt(2)\n\t" \
    "v_smfmac_f32_16x16x64_bf16 v[72:75], v[64:67], a[16:23], v10\n\t" \
    "s_waitcnt lgkmcnt(1)\n\t" \
    "v_smfmac_f32_16x16x64_bf16 v[72:75], v[68:71], a[24:31], v10\n\t" \
    "s_waitcnt lgkmcnt(0)\n\t" \
    "v_smfmac_f32_16x16x64_bf16 v[72:75], v[44:47], a[64:71], v10\n\t" \
    "s_nop 2\n\t" \
    "v_smfmac_f32_16x16x64_bf16 v[76:79], v[48:51], a[120:127], v10\n\t" \
    "s_nop 2\n\t" \
    "v_smfmac_f32_16x16x64_bf16 v[76:79], v[52:55], a[128:135], v10\n\t" \
    "v_add_f32 v72, v72, v74\n\t" \
    "v_smfmac_f32_16x16x64_bf16 v[76:79], v[56:59], a[72:79], v10\n\t" \
    "v_add_f32 v73, v73, v75\n\t" \
    "v_smfmac_f32_16x16x64_bf16 v[76:79], v[60:63], a[80:87], v10\n\t" \
    "v_cvt_pk_bf16_f32 v80, v72, v73\n\t" \
    "v_smfmac_f32_16x16x64_bf16 v[76:79], v[64:67], a[88:95], v10\n\t" \
    "ds_write_b32 v12, v80 offset:0\n\t" \
    "v_smfmac_f32_16x16x64_bf16 v[76:79], v[68:71], a[96:103], v10\n\t" \
    "v_smfmac_f32_16x16x64_bf16 v[76:79], v[44:47], a[136:143], v10\n\t" \
    "s_nop 6\n\t" \
    "v_cmp_u_f32 vcc, v72, v73\n\t" \
    "v_pk_add_f32 v[76:77], v[76:77], v[78:79]\n\t" \
    "v_cvt_pk_bf16_f32 v81, v76, v77\n\t" \
    "ds_write_b32 v12, v81 offset:256\n\t" \
    "s_cbranch_vccnz vv_Gslow51_%=\n\t" \
    "vv_Gback52_%=:\n\t" \
    "s_waitcnt lgkmcnt(0)\n\t" \
    "s_barrier\n\t" \
    "ds_read_b64_tr_b16 v[84:85], v13 offset:0\n\t" \
    "v_smfmac_f32_16x16x64_bf16 v[100:103], v[36:39], a[0:7], v10\n\t" \
    "ds_read_b64_tr_b16 v[86:87], v13 offset:64\n\t" \
    "ds_read_b64_tr_b16 v[92:93], v13 offset:1152\n\t" \
    "v_smfmac_f32_16x16x64_bf16 v[100:103], v[36:39], a[0:7], v10\n\t" \
    "ds_read_b64_tr_b16 v[94:95], v13 offset:1216\n\t" \
    "ds_read_b64_tr_b16 v[88:89], v25 offset:0\n\t" \
    "v_smfmac_f32_16x16x64_bf16 v[100:103], v[36:39], a[0:7], v10\n\t" \
    "ds_read_b64_tr_b16 v[90:91], v25 offset:64\n\t" \
    "s_waitcnt lgkmcnt(4)\n\t" \
    "v_smfmac_f32_16x16x64_bf16 v[124:127], v[84:87], v[240:247], v19\n\t" \
    "ds_read_b64_tr_b16 v[96:97], v25 offset:1152\n\t" \
    "ds_read_b64_tr_b16 v[98:99], v25 offset:1216\n\t" \
    "s_waitcnt lgkmcnt(4)\n\t" \
    "v_smfmac_f32_16x16x64_bf16 v[124:127], v[92:95], v[248:255], v19\n\t" \
    "s_nop 2\n\t" \
    "v_smfmac_f32_16x16x64_bf16 v[108:111], v[84:87], a[144:151], v19\n\t" \
    "s_nop 2\n\t" \
    "v_smfmac_f32_16x16x64_bf16 v[108:111], v[92:95], a[160:167], v19\n\t" \
    "s_nop 1\n\t" \
    "v_permlane32_swap_b32 v124, v126\n\t" \
    "v_smfmac_f32_16x16x64_bf16 v[112:115], v[84:87], a[176:183], v19\n\t" \
    "s_nop 1\n\t" \
    "v_permlane32_swap_b32 v125, v127\n\t" \
    "v_smfmac_f32_16x16x64_bf16 v[112:115], v[92:95], a[192:199], v19\n\t" \
    "s_nop 0\n\t" \
    "v_add_f32 v124, v124, v126\n\t" \
    "s_waitcnt lgkmcnt(2)\n\t" \
    "v_smfmac_f32_16x16x64_bf16 v[108:111], v[88:91], a[152:159], v19\n\t" \
    "s_nop 1\n\t" \
    "v_add_f32 v125, v125, v127\n\t" \
    "v_smfmac_f32_16x16x64_bf16 v[112:115], v[88:91], a[184:191], v19\n\t" \
    "s_nop 0\n\t" \
    "v_cvt_pk_bf16_f32 v136, v124, v125\n\t" \
    "s_waitcnt lgkmcnt(0)\n\t" \
    "v_smfmac_f32_16x16x64_bf16 v[108:111], v[96:99], a[168:175], v19\n\t" \
    "s_nop 1\n\t" \
    "v_cvt_pk_bf16_f32 v137, v124, v125\n\t" \
    "v_smfmac_f32_16x16x64_bf16 v[112:115], v[96:99], a[200:207], v19\n\t" \
    "s_nop 1\n\t" \
    "v_permlane32_swap_b32 v136, v137\n\t" \
    "v_smfmac_f32_16x16x64_bf16 v[116:119], v[84:87], a[208:215], v19\n\t" \
    "s_mov_b32 exec_hi, 0\n\t" \
    "global_store_dwordx2 v153, v[136:137], s[38:39] sc1\n\t" \
    "s_mov_b32 exec_hi, -1\n\t" \
    "v_cvt_pk_bf16_f32 v128, v108, v109\n\t" \
    "v_smfmac_f32_16x16x64_bf16 v[116:119], v[88:91], a[216:223], v19\n\t" \
    "s_nop 1\n\t" \
    "v_cvt_pk_bf16_f32 v129, v110, v111\n\t" \
    "v_smfmac_f32_16x16x64_bf16 v[116:119], v[92:95], a[224:231], v19\n\t" \
    "s_nop 1\n\t" \
    "v_cvt_pk_bf16_f32 v130, v112, v113\n\t" \
    "v_smfmac_f32_16x16x64_bf16 v[116:119], v[96:99], a[232:239], v19\n\t" \
    "s_nop 1\n\t" \
    "v_cvt_pk_bf16_f32 v131, v114, v115\n\t" \
    "v_smfmac_f32_16x16x64_bf16 v[120:123], v[84:87], a[240:247], v19\n\t" \
    "s_nop 1\n\t" \
    "v_permlane16_swap_b32 v128, v130\n\t" \
    "v_smfmac_f32_16x16x64_bf16 v[120:123], v[88:91], a[248:255], v19\n\t" \
    "s_nop 1\n\t" \
    "v_permlane16_swap_b32 v129, v131\n\t" \
    "v_smfmac_f32_16x16x64_bf16 v[120:123], v[92:95], v[224:231], v19\n\t" \
    "s_nop 0\n\t" \
    "global_store_dwordx4 v151, v[128:131], s[38:39] sc1\n\t" \
    "v_cvt_pk_bf16_f32 v132, v116, v117\n\t" \
    "v_smfmac_f32_16x16x64_bf16 v[120:123], v[96:99], v[232:239], v19\n\t" \
    "s_nop 1\n\t" \
    "v_cvt_pk_bf16_f32 v133, v118, v119\n\t" \
    "s_nop 4\n\t" \
    "v_cvt_pk_bf16_f32 v134, v120, v121\n\t" \
    "v_cvt_pk_bf16_f32 v135, v122, v123\n\t" \
    "s_nop 0\n\t" \
    "v_permlane16_swap_b32 v132, v134\n\t" \
    "v_permlane16_swap_b32 v133, v135\n\t" \
    "global_store_dwordx4 v152, v[132:135], s[38:39] sc1\n\t" \
    "s_cmp_eq_u32 s65, 63\n\t" \
    "s_cbranch_scc1 vv_end44_%=\n\t" \
    "s_add_u32 s65, s65, 1\n\t" \
    "v_mov_b32 v72, 0\n\t" \
    "v_mov_b32 v73, 0\n\t" \
    "v_mov_b32 v74, 0\n\t" \
    "v_mov_b32 v75, 0\n\t" \
    "v_mov_b32 v76, 0\n\t" \
    "v_mov_b32 v77, 0\n\t" \
    "v_mov_b32 v78, 0\n\t" \
    "v_mov_b32 v79, 0\n\t" \
    "v_mov_b32 v108, 0\n\t" \
    "v_mov_b32 v109, 0\n\t" \
    "v_mov_b32 v110, 0\n\t" \
    "v_mov_b32 v111, 0\n\t" \
    "v_mov_b32 v112, 0\n\t" \
    "v_mov_b32 v113, 0\n\t" \
    "v_mov_b32 v114, 0\n\t" \
    "v_mov_b32 v115, 0\n\t" \
    "v_mov_b32 v116, 0\n\t" \
    "v_mov_b32 v117, 0\n\t" \
    "v_mov_b32 v118, 0\n\t" \
    "v_mov_b32 v119, 0\n\t" \
    "v_mov_b32 v120, 0\n\t" \
    "v_mov_b32 v121, 0\n\t" \
    "v_mov_b32 v122, 0\n\t" \
    "v_mov_b32 v123, 0\n\t" \
    "v_mov_b32 v124, 0\n\t" \
    "v_mov_b32 v125, 0\n\t" \
    "v_mov_b32 v126, 0\n\t" \
    "v_mov_b32 v127, 0\n\t" \
    "s_add_u32 s69, s65, 0\n\t" \
    "s_and_b32 s69, s69, 7\n\t" \
    "s_lshl_b32 s69, s69, 18\n\t" \
    "s_add_u32 s69, s69, s62\n\t" \
    "s_add_u32 s46, s36, s69\n\t" \
    "s_addc_u32 s47, s37, 0\n\t" \
    "s_bitcmp0_b32 s65, 0\n\t" \
    "s_cselect_b32 s69, 16384, 0\n\t" \
    "v_xor_b32 v1, s69, v1\n\t" \
    "v_xor_b32 v2, s69, v2\n\t" \
    "v_xor_b32 v23, s69, v23\n\t" \
    "s_add_u32 s70, s65, 0\n\t" \
    "s_mul_i32 s69, s70, 171\n\t" \
    "s_lshr_b32 s69, s69, 9\n\t" \
    "s_mul_i32 s71, s69, 3\n\t" \
    "s_sub_u32 s70, s70, s71\n\t" \
    "s_mul_i32 s70, s70, 589824\n\t" \
    "s_lshl1_add_u32 s71, s69, 4\n\t" \
    "s_lshr_b64 s[38:39], s[44:45], s71\n\t" \
    "s_lshl_b32 s71, s66, 1\n\t" \
    "s_xor_b32 s71, s71, s38\n\t" \
    "s_and_b32 s71, s71, 3\n\t" \
    "s_lshl_b32 s71, s71, 13\n\t" \
    "s_add_u32 s70, s70, s71\n\t" \
    "s_lshl_b32 s69, s69, 21\n\t" \
    "s_add_u32 s69, s69, 123731968\n\t" \
    "v_add_u32 v151, s70, v14\n\t" \
    "v_and_b32 v150, 0x80000, v151\n\t" \
    "v_lshrrev_b32 v150, 5, v150\n\t" \
    "v_xor_b32 v151, v151, v150\n\t" \
    "v_add_u32 v152, s70, v15\n\t" \
    "v_and_b32 v150, 0x80000, v152\n\t" \
    "v_lshrrev_b32 v150, 5, v150\n\t" \
    "v_xor_b32 v152, v152, v150\n\t" \
    "v_add_u32 v153, s70, v16\n\t" \
    "v_and_b32 v150, 0x80000, v153\n\t" \
    "v_lshrrev_b32 v150, 5, v150\n\t" \
    "v_xor_b32 v153, v153, v150\n\t" \
    "s_add_u32 s38, s36, s69\n\t" \
    "s_addc_u32 s39, s37, 0\n\t" \
    "s_and_b32 s69, s69, 0xffe00000\n\t" \
    "s_add_u32 s69, s69, 1807360\n\t" \
    "v_mov_b32 v17, s69\n\t" \
    "global_load_dword v17, v17, s[36:37]\n\t" \
    "s_and_b32 s69, s65, 7\n\t" \
    "s_lshl_b32 s69, s69, 18\n\t" \
    "s_add_u32 s69, s69, s63\n\t" \
    "s_add_u32 s52, s36, s69\n\t" \
    "s_addc_u32 s53, s37, 0\n\t" \
    "s_barrier\n\t" \
    "ds_read_b32 v160, v11 offset:20480\n\t" \
    "s_waitcnt lgkmcnt(0)\n\t" \
    "v_readfirstlane_b32 s70, v160\n\t" \
    "s_branch vv_loop13_%=\n\t" \
    "vv_Gbad49_%=:\n\t" \
    "s_waitcnt vmcnt(0) lgkmcnt(0)\n\t" \
    "s_nop 7\n\t" \
    "v_mov_b32 v72, 0\n\t" \
    "v_mov_b32 v73, 0\n\t" \
    "v_mov_b32 v74, 0\n\t" \
    "v_mov_b32 v75, 0\n\t" \
    "v_mov_b32 v76, 0\n\t" \
    "v_mov_b32 v77, 0\n\t" \
    "v_mov_b32 v78, 0\n\t" \
    "v_mov_b32 v79, 0\n\t" \
    "s_cmp_eq_u32 s68, 0\n\t" \
    "s_cbranch_scc1 vv_F54_%=\n\t" \
    "s_sub_u32 s68, s68, 1\n\t" \
    "s_branch vv_G48_%=\n\t" \
    "vv_F54_%=:\n\t" \
    "v_mov_b32 v140, 5767424\n\t" \
    "v_mov_b32 v141, 1\n\t" \
    "global_store_dword v140, v141, s[36:37] sc1\n\t" \
    "s_waitcnt vmcnt(0)\n\t" \
    "s_branch vv_Gc50_%=\n\t" \
    "vv_Gslow51_%=:\n\t" \
    "v_max_u32 v140, v36, v37\n\t" \
    "v_max3_u32 v140, v140, v38, v39\n\t" \
    "v_max3_u32 v140, v140, v40, v41\n\t" \
    "v_max3_u32 v140, v140, v42, v43\n\t" \
    "v_max3_u32 v140, v140, v44, v45\n\t" \
    "v_max3_u32 v140, v140, v46, v47\n\t" \
    "v_max3_u32 v140, v140, v48, v49\n\t" \
    "v_max3_u32 v140, v140, v50, v51\n\t" \
    "v_max3_u32 v140, v140, v52, v53\n\t" \
    "v_max3_u32 v140, v140, v54, v55\n\t" \
    "v_max3_u32 v140, v140, v56, v57\n\t" \
    "v_max3_u32 v140, v140, v58, v59\n\t" \
    "v_max3_u32 v140, v140, v60, v61\n\t" \
    "v_max3_u32 v140, v140, v62, v63\n\t" \
    "v_max3_u32 v140, v140, v64, v65\n\t" \
    "v_max3_u32 v140, v140, v66, v67\n\t" \
    "v_max3_u32 v140, v140, v68, v69\n\t" \
    "v_max3_u32 v140, v140, v70, v71\n\t" \
    "v_cmp_eq_u32 vcc, -1, v140\n\t" \
    "s_cbranch_vccz vv_Gback52_%=\n\t" \
    "s_waitcnt lgkmcnt(0)\n\t" \
    "v_sub_u32 v140, v1, v3\n\t" \
    "s_nop 0\n\t" \
    "v_readfirstlane_b32 s69, v140\n\t" \
    "v_add_u32 v143, 36864, v0\n\t" \
    "s_nop 7\n\t" \
    "s_add_u32 s30, s46, s69\n\t" \
    "s_addc_u32 s31, s47, 0\n\t" \
    "vv_Gag55_%=:\n\t" \
    "global_load_dwordx4 v[36:39], v1, s[46:47] sc1\n\t" \
    "global_load_dwordx4 v[40:43], v23, s[46:47] sc1\n\t" \
    "global_load_dwordx4 v[44:47], v143, s[30:31] sc1\n\t" \
    "global_load_dwordx4 v[48:51], v4, s[30:31] sc1\n\t" \
    "global_load_dwordx4 v[52:55], v5, s[30:31] sc1\n\t" \
    "global_load_dwordx4 v[56:59], v6, s[30:31] sc1\n\t" \
    "global_load_dwordx4 v[60:63], v7, s[30:31] sc1\n\t" \
    "global_load_dwordx4 v[64:67], v8, s[30:31] sc1\n\t" \
    "global_load_dwordx4 v[68:71], v9, s[30:31] sc1\n\t" \
    "s_waitcnt vmcnt(0)\n\t" \
    "v_max_u32 v140, v36, v37\n\t" \
    "v_max3_u32 v140, v140, v38, v39\n\t" \
    "v_max3_u32 v140, v140, v40, v41\n\t" \
    "v_max3_u32 v140, v140, v42, v43\n\t" \
    "v_max3_u32 v140, v140, v44, v45\n\t" \
    "v_max3_u32 v140, v140, v46, v47\n\t" \
    "v_max3_u32 v140, v140, v48, v49\n\t" \
    "v_max3_u32 v140, v140, v50, v51\n\t" \
    "v_max3_u32 v140, v140, v52, v53\n\t" \
    "v_max3_u32 v140, v140, v54, v55\n\t" \
    "v_max3_u32 v140, v140, v56, v57\n\t" \
    "v_max3_u32 v140, v140, v58, v59\n\t" \
    "v_max3_u32 v140, v140, v60, v61\n\t" \
    "v_max3_u32 v140, v140, v62, v63\n\t" \
    "v_max3_u32 v140, v140, v64, v65\n\t" \
    "v_max3_u32 v140, v140, v66, v67\n\t" \
    "v_max3_u32 v140, v140, v68, v69\n\t" \
    "v_max3_u32 v140, v140, v70, v71\n\t" \
    "v_cmp_eq_u32 vcc, -1, v140\n\t" \
    "s_cbranch_vccz vv_Gredo56_%=\n\t" \
    "s_cmp_eq_u32 s68, 0\n\t" \
    "s_cbranch_scc1 vv_F57_%=\n\t" \
    "s_sub_u32 s68, s68, 1\n\t" \
    "s_sleep 1\n\t" \
    "s_branch vv_Gag55_%=\n\t" \
    "vv_F57_%=:\n\t" \
    "v_mov_b32 v140, 5767424\n\t" \
    "v_mov_b32 v141, 2\n\t" \
    "global_store_dword v140, v141, s[36:37] sc1\n\t" \
    "s_waitcnt vmcnt(0)\n\t" \
    "s_branch vv_Gredo56_%=\n\t" \
    "vv_Gredo56_%=:\n\t" \
    "v_mov_b32 v72, 0\n\t" \
    "v_mov_b32 v73, 0\n\t" \
    "v_mov_b32 v74, 0\n\t" \
    "v_mov_b32 v75, 0\n\t" \
    "v_mov_b32 v76, 0\n\t" \
    "v_mov_b32 v77, 0\n\t" \
    "v_mov_b32 v78, 0\n\t" \
    "v_mov_b32 v79, 0\n\t" \
    "v_smfmac_f32_16x16x64_bf16 v[72:75], v[36:39], a[32:39], v10\n\t" \
    "v_smfmac_f32_16x16x64_bf16 v[72:75], v[40:43], a[40:47], v10\n\t" \
    "v_smfmac_f32_16x16x64_bf16 v[72:75], v[48:51], a[48:55], v10\n\t" \
    "v_smfmac_f32_16x16x64_bf16 v[72:75], v[52:55], a[56:63], v10\n\t" \
    "v_smfmac_f32_16x16x64_bf16 v[72:75], v[56:59], a[0:7], v10\n\t" \
    "v_smfmac_f32_16x16x64_bf16 v[72:75], v[60:63], a[8:15], v10\n\t" \
    "v_smfmac_f32_16x16x64_bf16 v[72:75], v[64:67], a[16:23], v10\n\t" \
    "v_smfmac_f32_16x16x64_bf16 v[72:75], v[68:71], a[24:31], v10\n\t" \
    "v_smfmac_f32_16x16x64_bf16 v[72:75], v[44:47], a[64:71], v10\n\t" \
    "v_smfmac_f32_16x16x64_bf16 v[76:79], v[36:39], a[104:111], v10\n\t" \
    "v_smfmac_f32_16x16x64_bf16 v[76:79], v[40:43], a[112:119], v10\n\t" \
    "v_smfmac_f32_16x16x64_bf16 v[76:79], v[48:51], a[120:127], v10\n\t" \
    "v_smfmac_f32_16x16x64_bf16 v[76:79], v[52:55], a[128:135], v10\n\t" \
    "v_smfmac_f32_16x16x64_bf16 v[76:79], v[56:59], a[72:79], v10\n\t" \
    "v_smfmac_f32_16x16x64_bf16 v[76:79], v[60:63], a[80:87], v10\n\t" \
    "v_smfmac_f32_16x16x64_bf16 v[76:79], v[64:67], a[88:95], v10\n\t" \
    "v_smfmac_f32_16x16x64_bf16 v[76:79], v[68:71], a[96:103], v10\n\t" \
    "v_smfmac_f32_16x16x64_bf16 v[76:79], v[44:47], a[136:143], v10\n\t" \
    "v_add_f32 v72, v72, v74\n\t" \
    "v_add_f32 v73, v73, v75\n\t" \
    "v_cvt_pk_bf16_f32 v80, v72, v73\n\t" \
    "ds_write_b32 v12, v80 offset:0\n\t" \
    "s_nop 3\n\t" \
    "v_pk_add_f32 v[76:77], v[76:77], v[78:79]\n\t" \
    "v_cvt_pk_bf16_f32 v81, v76, v77\n\t" \
    "ds_write_b32 v12, v81 offset:256\n\t" \
    "s_branch vv_Gback52_%=\n\t" \
    "vv_end44_%=:\n\t" \
    "s_endpgm\n\t" \
    ".p2align 6\n\t" \
    "s_nop 0\n\t" \
    "s_nop 0\n\t" \
    "s_nop 0\n\t" \
    "s_nop 0\n\t" \
    "s_nop 0\n\t" \
    "s_nop 0\n\t" \
    "vv_loop14_%=:\n\t" \
    "s_getpc_b64 s[30:31]\n\t" \
    "vv_Wp60_%=:\n\t" \
    "s_add_u32 s30, s30, vv_We61_%=-vv_Wp60_%=\n\t" \
    "s_addc_u32 s31, s31, 0\n\t" \
    "s_sub_u32 s70, s70, -54\n\t" \
    "vv_W59_%=:\n\t" \
    "s_memtime s[56:57]\n\t" \
    "s_waitcnt lgkmcnt(0)\n\t" \
    "s_sub_u32 s69, s70, s56\n\t" \
    "s_cmp_gt_i32 s69, 80\n\t" \
    "s_cbranch_scc1 vv_W59_%=\n\t" \
    "s_max_i32 s69, s69, 0\n\t" \
    "s_and_b32 s69, s69, -4\n\t" \
    "s_sub_u32 s30, s30, s69\n\t" \
    "s_subb_u32 s31, s31, 0\n\t" \
    "s_setpc_b64 s[30:31]\n\t" \
    "s_nop 0\n\t" \
    "s_nop 0\n\t" \
    "s_nop 0\n\t" \
    "s_nop 0\n\t" \
    "s_nop 0\n\t" \
    "s_nop 0\n\t" \
    "s_nop 0\n\t" \
    "s_nop 0\n\t" \
    "s_nop 0\n\t" \
    "s_nop 0\n\t" \
    "s_nop 0\n\t" \
    "s_nop 0\n\t" \
    "s_nop 0\n\t" \
    "s_nop 0\n\t" \
    "s_nop 0\n\t" \
    "s_nop 0\n\t" \
    "s_nop 0\n\t" \
    "s_nop 0\n\t" \
    "s_nop 0\n\t" \
    "s_nop 0\n\t" \
    "vv_We61_%=:\n\t" \
    "vv_G62_%=:\n\t" \
    "global_load_dwordx4 v[36:39], v1, s[46:47] sc1\n\t" \
    "global_load_dword v141, v2, s[46:47] sc1\n\t" \
    "global_load_dwordx4 v[40:43], v23, s[46:47] sc1\n\t" \
    "s_waitcnt vmcnt(2)\n\t" \
    "vv_Garr67_%=:\n\t" \
    "ds_write_b128 v3, v[36:39]\n\t" \
    "v_smfmac_f32_16x16x64_bf16 v[72:75], v[36:39], a[48:55], v10\n\t" \
    "s_waitcnt vmcnt(1)\n\t" \
    "ds_write_b32 v22, v141 offset:20224\n\t" \
    "s_waitcnt vmcnt(0)\n\t" \
    "ds_write_b128 v3, v[40:43] offset:1024\n\t" \
    "v_max3_u32 v140, v36, v40, v141\n\t" \
    "v_cmp_eq_u32 vcc, -1, v140\n\t" \
    "s_cbranch_vccnz vv_Gbad63_%=\n\t" \
    "vv_Gc64_%=:\n\t" \
    "s_waitcnt lgkmcnt(0)\n\t" \
    "s_barrier\n\t" \
    "ds_read_b128 v[48:51], v4\n\t" \
    "ds_read_b128 v[52:55], v5\n\t" \
    "v_smfmac_f32_16x16x64_bf16 v[76:79], v[36:39], a[120:127], v10\n\t" \
    "ds_read_b128 v[56:59], v6\n\t" \
    "ds_read_b128 v[60:63], v7\n\t" \
    "v_smfmac_f32_16x16x64_bf16 v[72:75], v[40:43], a[56:63], v10\n\t" \
    "ds_read_b128 v[64:67], v8\n\t" \
    "v_smfmac_f32_16x16x64_bf16 v[76:79], v[40:43], a[128:135], v10\n\t" \
    "ds_read_b128 v[68:71], v9\n\t" \
    "s_waitcnt lgkmcnt(5)\n\t" \
    "v_smfmac_f32_16x16x64_bf16 v[72:75], v[48:51], a[0:7], v10\n\t" \
    "ds_read_b128 v[44:47], v0 offset:19456\n\t" \
    "s_waitcnt lgkmcnt(5)\n\t" \
    "v_smfmac_f32_16x16x64_bf16 v[72:75], v[52:55], a[8:15], v10\n\t" \
    "s_waitcnt lgkmcnt(4)\n\t" \
    "v_smfmac_f32_16x16x64_bf16 v[72:75], v[56:59], a[16:23], v10\n\t" \
    "s_waitcnt lgkmcnt(3)\n\t" \
    "v_smfmac_f32_16x16x64_bf16 v[72:75], v[60:63], a[24:31], v10\n\t" \
    "s_waitcnt lgkmcnt(2)\n\t" \
    "v_smfmac_f32_16x16x64_bf16 v[72:75], v[64:67], a[32:39], v10\n\t" \
    "s_waitcnt lgkmcnt(1)\n\t" \
    "v_smfmac_f32_16x16x64_bf16 v[72:75], v[68:71], a[40:47], v10\n\t" \
    "s_waitcnt lgkmcnt(0)\n\t" \
    "v_smfmac_f32_16x16x64_bf16 v[72:75], v[44:47], a[64:71], v10\n\t" \
    "s_nop 2\n\t" \
    "v_smfmac_f32_16x16x64_bf16 v[76:79], v[48:51], a[72:79], v10\n\t" \
    "s_nop 2\n\t" \
    "v_smfmac_f32_16x16x64_bf16 v[76:79], v[52:55], a[80:87], v10\n\t" \
    "v_add_f32 v72, v72, v74\n\t" \
    "v_smfmac_f32_16x16x64_bf16 v[76:79], v[56:59], a[88:95], v10\n\t" \
    "v_add_f32 v73, v73, v75\n\t" \
    "v_smfmac_f32_16x16x64_bf16 v[76:79], v[60:63], a[96:103], v10\n\t" \
    "v_cvt_pk_bf16_f32 v80, v72, v73\n\t" \
    "v_smfmac_f32_16x16x64_bf16 v[76:79], v[64:67], a[104:111], v10\n\t" \
    "ds_write_b32 v12, v80 offset:0\n\t" \
    "v_smfmac_f32_16x16x64_bf16 v[76:79], v[68:71], a[112:119], v10\n\t" \
    "v_smfmac_f32_16x16x64_bf16 v[76:79], v[44:47], a[136:143], v10\n\t" \
    "s_nop 6\n\t" \
    "v_cmp_u_f32 vcc, v72, v73\n\t" \
    "v_pk_add_f32 v[76:77], v[76:77], v[78:79]\n\t" \
    "v_cvt_pk_bf16_f32 v81, v76, v77\n\t" \
    "ds_write_b32 v12, v81 offset:256\n\t" \
    "s_cbranch_vccnz vv_Gslow65_%=\n\t" \
    "vv_Gback66_%=:\n\t" \
    "s_waitcnt lgkmcnt(0)\n\t" \
    "s_barrier\n\t" \
    "ds_read_b64_tr_b16 v[84:85], v13 offset:0\n\t" \
    "v_smfmac_f32_16x16x64_bf16 v[100:103], v[36:39], a[0:7], v10\n\t" \
    "ds_read_b64_tr_b16 v[86:87], v13 offset:64\n\t" \
    "ds_read_b64_tr_b16 v[92:93], v13 offset:1152\n\t" \
    "v_smfmac_f32_16x16x64_bf16 v[100:103], v[36:39], a[0:7], v10\n\t" \
    "ds_read_b64_tr_b16 v[94:95], v13 offset:1216\n\t" \
    "ds_read_b64_tr_b16 v[88:89], v25 offset:0\n\t" \
    "v_smfmac_f32_16x16x64_bf16 v[100:103], v[36:39], a[0:7], v10\n\t" \
    "ds_read_b64_tr_b16 v[90:91], v25 offset:64\n\t" \
    "s_waitcnt lgkmcnt(4)\n\t" \
    "v_smfmac_f32_16x16x64_bf16 v[124:127], v[84:87], v[240:247], v19\n\t" \
    "ds_read_b64_tr_b16 v[96:97], v25 offset:1152\n\t" \
    "ds_read_b64_tr_b16 v[98:99], v25 offset:1216\n\t" \
    "s_waitcnt lgkmcnt(4)\n\t" \
    "v_smfmac_f32_16x16x64_bf16 v[124:127], v[92:95], v[248:255], v19\n\t" \
    "s_nop 2\n\t" \
    "v_smfmac_f32_16x16x64_bf16 v[108:111], v[84:87], a[144:151], v19\n\t" \
    "s_nop 2\n\t" \
    "v_smfmac_f32_16x16x64_bf16 v[108:111], v[92:95], a[160:167], v19\n\t" \
    "s_nop 1\n\t" \
    "v_permlane32_swap_b32 v124, v126\n\t" \
    "v_smfmac_f32_16x16x64_bf16 v[112:115], v[84:87], a[176:183], v19\n\t" \
    "s_nop 1\n\t" \
    "v_permlane32_swap_b32 v125, v127\n\t" \
    "v_smfmac_f32_16x16x64_bf16 v[112:115], v[92:95], a[192:199], v19\n\t" \
    "s_nop 0\n\t" \
    "v_add_f32 v124, v124, v126\n\t" \
    "s_waitcnt lgkmcnt(2)\n\t" \
    "v_smfmac_f32_16x16x64_bf16 v[108:111], v[88:91], a[152:159], v19\n\t" \
    "s_nop 1\n\t" \
    "v_add_f32 v125, v125, v127\n\t" \
    "v_smfmac_f32_16x16x64_bf16 v[112:115], v[88:91], a[184:191], v19\n\t" \
    "s_nop 0\n\t" \
    "v_cvt_pk_bf16_f32 v136, v124, v125\n\t" \
    "s_waitcnt lgkmcnt(0)\n\t" \
    "v_smfmac_f32_16x16x64_bf16 v[108:111], v[96:99], a[168:175], v19\n\t" \
    "s_nop 1\n\t" \
    "v_cvt_pk_bf16_f32 v137, v124, v125\n\t" \
    "v_smfmac_f32_16x16x64_bf16 v[112:115], v[96:99], a[200:207], v19\n\t" \
    "s_nop 1\n\t" \
    "v_permlane32_swap_b32 v136, v137\n\t" \
    "v_smfmac_f32_16x16x64_bf16 v[116:119], v[84:87], a[208:215], v19\n\t" \
    "s_mov_b32 exec_hi, 0\n\t" \
    "global_store_dwordx2 v153, v[136:137], s[38:39] sc1\n\t" \
    "s_mov_b32 exec_hi, -1\n\t" \
    "v_cvt_pk_bf16_f32 v128, v108, v109\n\t" \
    "v_smfmac_f32_16x16x64_bf16 v[116:119], v[88:91], a[216:223], v19\n\t" \
    "s_nop 1\n\t" \
    "v_cvt_pk_bf16_f32 v129, v110, v111\n\t" \
    "v_smfmac_f32_16x16x64_bf16 v[116:119], v[92:95], a[224:231], v19\n\t" \
    "s_nop 1\n\t" \
    "v_cvt_pk_bf16_f32 v130, v112, v113\n\t" \
    "v_smfmac_f32_16x16x64_bf16 v[116:119], v[96:99], a[232:239], v19\n\t" \
    "s_nop 1\n\t" \
    "v_cvt_pk_bf16_f32 v131, v114, v115\n\t" \
    "v_smfmac_f32_16x16x64_bf16 v[120:123], v[84:87], a[240:247], v19\n\t" \
    "s_nop 1\n\t" \
    "v_permlane16_swap_b32 v128, v130\n\t" \
    "v_smfmac_f32_16x16x64_bf16 v[120:123], v[88:91], a[248:255], v19\n\t" \
    "s_nop 1\n\t" \
    "v_permlane16_swap_b32 v129, v131\n\t" \
    "v_smfmac_f32_16x16x64_bf16 v[120:123], v[92:95], v[224:231], v19\n\t" \
    "s_nop 0\n\t" \
    "global_store_dwordx4 v151, v[128:131], s[38:39] sc1\n\t" \
    "v_cvt_pk_bf16_f32 v132, v116, v117\n\t" \
    "v_smfmac_f32_16x16x64_bf16 v[120:123], v[96:99], v[232:239], v19\n\t" \
    "s_nop 1\n\t" \
    "v_cvt_pk_bf16_f32 v133, v118, v119\n\t" \
    "s_nop 4\n\t" \
    "v_cvt_pk_bf16_f32 v134, v120, v121\n\t" \
    "v_cvt_pk_bf16_f32 v135, v122, v123\n\t" \
    "s_nop 0\n\t" \
    "v_permlane16_swap_b32 v132, v134\n\t" \
    "v_permlane16_swap_b32 v133, v135\n\t" \
    "global_store_dwordx4 v152, v[132:135], s[38:39] sc1\n\t" \
    "global_load_dword v140, v20, s[52:53] sc1\n\t" \
    "s_cmp_eq_u32 s65, 63\n\t" \
    "s_cbranch_scc1 vv_end58_%=\n\t" \
    "s_cmp_lg_u32 s65, 0\n\t" \
    "s_cbranch_scc1 vv_ng68_%=\n\t" \
    "vv_ng68_%=:\n\t" \
    "s_waitcnt lgkmcnt(0)\n\t" \
    "s_mov_b32 s70, 116\n\t" \
    "vv_E72_%=:\n\t" \
    "s_memtime s[56:57]\n\t" \
    "s_memrealtime s[2:3]\n\t" \
    "s_nop 0\n\t" \
    "s_memrealtime s[4:5]\n\t" \
    "s_nop 0\n\t" \
    "s_memrealtime s[6:7]\n\t" \
    "s_nop 0\n\t" \
    "s_memrealtime s[8:9]\n\t" \
    "s_nop 0\n\t" \
    "s_memrealtime s[10:11]\n\t" \
    "s_nop 0\n\t" \
    "s_memrealtime s[12:13]\n\t" \
    "s_nop 0\n\t" \
    "s_memrealtime s[14:15]\n\t" \
    "s_nop 0\n\t" \
    "s_memrealtime s[16:17]\n\t" \
    "s_nop 0\n\t" \
    "s_memrealtime s[18:19]\n\t" \
    "s_nop 0\n\t" \
    "s_memrealtime s[20:21]\n\t" \
    "s_nop 0\n\t" \
    "s_memrealtime s[22:23]\n\t" \
    "s_nop 0\n\t" \
    "s_memrealtime s[24:25]\n\t" \
    "s_nop 0\n\t" \
    "s_memrealtime s[26:27]\n\t" \
    "s_nop 0\n\t" \
    "s_memrealtime s[28:29]\n\t" \
    "s_memtime s[54:55]\n\t" \
    "s_add_u32 s70, s70, 8\n\t" \
    "s_waitcnt lgkmcnt(0)\n\t" \
    "s_sub_u32 s69, s54, s56\n\t" \
    "s_cmp_gt_u32 s69, s70\n\t" \
    "s_cbranch_scc1 vv_E72_%=\n\t" \
    "s_add_u32 s2, s2, s4\n\t" \
    "s_add_u32 s6, s6, s8\n\t" \
    "s_add_u32 s10, s10, s12\n\t" \
    "s_add_u32 s14, s14, s16\n\t" \
    "s_add_u32 s18, s18, s20\n\t" \
    "s_add_u32 s22, s22, s24\n\t" \
    "s_add_u32 s2, s2, s6\n\t" \
    "s_add_u32 s10, s10, s14\n\t" \
    "s_add_u32 s18, s18, s22\n\t" \
    "s_add_u32 s2, s2, s26\n\t" \
    "s_add_u32 s10, s10, s18\n\t" \
    "s_add_u32 s2, s2, s10\n\t" \
    "s_mul_i32 s70, s28, 13\n\t" \
    "s_sub_u32 s70, s70, s2\n\t" \
    "s_min_u32 s70, s70, 52\n\t" \
    "s_mul_i32 s69, s69, s70\n\t" \
    "s_mul_hi_u32 s69, s69, 76695844\n\t" \
    "s_mov_b32 s75, s73\n\t" \
    "s_mov_b32 s76, s74\n\t" \
    "s_add_u32 s73, s56, s69\n\t" \
    "s_lshl_b32 s74, s28, 4\n\t" \
    "s_sub_u32 s69, s73, s75\n\t" \
    "s_sub_u32 s70, s74, s76\n\t" \
    "s_sub_u32 s71, s70, 1600\n\t" \
    "s_cmp_lt_u32 s71, 400000\n\t" \
    "s_cbranch_scc0 vv_K73_%=\n\t" \
    "v_cvt_f32_u32 v160, s69\n\t" \
    "v_cvt_f32_u32 v161, s70\n\t" \
    "v_rcp_f32 v162, v160\n\t" \
    "v_rcp_f32 v163, v161\n\t" \
    "v_mul_f32 v162, v162, v161\n\t" \
    "v_mul_f32 v163, v163, v160\n\t" \
    "v_mul_f32 v162, 0x4f800000, v162\n\t" \
    "v_mul_f32 v163, 0x4f000000, v163\n\t" \
    "v_cvt_u32_f32 v162, v162\n\t" \
    "v_cvt_u32_f32 v163, v163\n\t" \
    "v_readfirstlane_b32 s69, v162\n\t" \
    "v_readfirstlane_b32 s70, v163\n\t" \
    "s_nop 1\n\t" \
    "s_min_u32 s71, s65, 1\n\t" \
    "s_sub_u32 s69, s69, s78\n\t" \
    "s_ashr_i32 s69, s69, s71\n\t" \
    "s_add_u32 s78, s78, s69\n\t" \
    "s_sub_u32 s70, s70, s77\n\t" \
    "s_ashr_i32 s70, s70, s71\n\t" \
    "s_add_u32 s77, s77, s70\n\t" \
    "vv_K73_%=:\n\t" \
    "s_add_u32 s65, s65, 1\n\t" \
    "v_mov_b32 v72, 0\n\t" \
    "v_mov_b32 v73, 0\n\t" \
    "v_mov_b32 v74, 0\n\t" \
    "v_mov_b32 v75, 0\n\t" \
    "v_mov_b32 v76, 0\n\t" \
    "v_mov_b32 v77, 0\n\t" \
    "v_mov_b32 v78, 0\n\t" \
    "v_mov_b32 v79, 0\n\t" \
    "v_mov_b32 v108, 0\n\t" \
    "v_mov_b32 v109, 0\n\t" \
    "v_mov_b32 v110, 0\n\t" \
    "v_mov_b32 v111, 0\n\t" \
    "v_mov_b32 v112, 0\n\t" \
    "v_mov_b32 v113, 0\n\t" \
    "v_mov_b32 v114, 0\n\t" \
    "v_mov_b32 v115, 0\n\t" \
    "v_mov_b32 v116, 0\n\t" \
    "v_mov_b32 v117, 0\n\t" \
    "v_mov_b32 v118, 0\n\t" \
    "v_mov_b32 v119, 0\n\t" \
    "v_mov_b32 v120, 0\n\t" \
    "v_mov_b32 v121, 0\n\t" \
    "v_mov_b32 v122, 0\n\t" \
    "v_mov_b32 v123, 0\n\t" \
    "v_mov_b32 v124, 0\n\t" \
    "v_mov_b32 v125, 0\n\t" \
    "v_mov_b32 v126, 0\n\t" \
    "v_mov_b32 v127, 0\n\t" \
    "s_add_u32 s69, s65, 0\n\t" \
    "s_and_b32 s69, s69, 7\n\t" \
    "s_lshl_b32 s69, s69, 18\n\t" \
    "s_add_u32 s69, s69, s62\n\t" \
    "s_add_u32 s46, s36, s69\n\t" \
    "s_addc_u32 s47, s37, 0\n\t" \
    "s_bitcmp0_b32 s65, 0\n\t" \
    "s_cselect_b32 s69, 16384, 0\n\t" \
    "v_xor_b32 v1, s69, v1\n\t" \
    "v_xor_b32 v2, s69, v2\n\t" \
    "v_xor_b32 v23, s69, v23\n\t" \
    "s_add_u32 s70, s65, 0\n\t" \
    "s_mul_i32 s69, s70, 171\n\t" \
    "s_lshr_b32 s69, s69, 9\n\t" \
    "s_mul_i32 s71, s69, 3\n\t" \
    "s_sub_u32 s70, s70, s71\n\t" \
    "s_mul_i32 s70, s70, 589824\n\t" \
    "s_lshl1_add_u32 s71, s69, 4\n\t" \
    "s_lshr_b64 s[38:39], s[44:45], s71\n\t" \
    "s_lshl_b32 s71, s66, 1\n\t" \
    "s_xor_b32 s71, s71, s38\n\t" \
    "s_and_b32 s71, s71, 3\n\t" \
    "s_lshl_b32 s71, s71, 13\n\t" \
    "s_add_u32 s70, s70, s71\n\t" \
    "s_lshl_b32 s69, s69, 21\n\t" \
    "s_add_u32 s69, s69, 123731968\n\t" \
    "v_add_u32 v151, s70, v14\n\t" \
    "v_and_b32 v150, 0x80000, v151\n\t" \
    "v_lshrrev_b32 v150, 5, v150\n\t" \
    "v_xor_b32 v151, v151, v150\n\t" \
    "v_add_u32 v152, s70, v15\n\t" \
    "v_and_b32 v150, 0x80000, v152\n\t" \
    "v_lshrrev_b32 v150, 5, v150\n\t" \
    "v_xor_b32 v152, v152, v150\n\t" \
    "v_add_u32 v153, s70, v16\n\t" \
    "v_and_b32 v150, 0x80000, v153\n\t" \
    "v_lshrrev_b32 v150, 5, v150\n\t" \
    "v_xor_b32 v153, v153, v150\n\t" \
    "s_add_u32 s38, s36, s69\n\t" \
    "s_addc_u32 s39, s37, 0\n\t" \
    "s_and_b32 s69, s69, 0xffe00000\n\t" \
    "s_add_u32 s69, s69, 1807360\n\t" \
    "v_mov_b32 v17, s69\n\t" \
    "global_load_dword v17, v17, s[36:37]\n\t" \
    "s_waitcnt vmcnt(0)\n\t" \
    "v_cmp_eq_u32 vcc, -1, v140\n\t" \
    "s_cbranch_vccz vv_Hok70_%=\n\t" \
    "vv_H69_%=:\n\t" \
    "global_load_dword v140, v20, s[52:53] sc1\n\t" \
    "s_waitcnt vmcnt(0)\n\t" \
    "v_cmp_eq_u32 vcc, -1, v140\n\t" \
    "s_cbranch_vccz vv_Hc71_%=\n\t" \
    "s_cmp_eq_u32 s68, 0\n\t" \
    "s_cbranch_scc1 vv_F74_%=\n\t" \
    "s_sub_u32 s68, s68, 1\n\t" \
    "s_sleep 2\n\t" \
    "s_branch vv_H69_%=\n\t" \
    "vv_F74_%=:\n\t" \
    "v_mov_b32 v141, 5767424\n\t" \
    "v_mov_b32 v142, 6\n\t" \
    "global_store_dword v141, v142, s[36:37] sc1\n\t" \
    "s_waitcnt vmcnt(0)\n\t" \
    "s_branch vv_Hc71_%=\n\t" \
    "vv_Hc71_%=:\n\t" \
    "s_memtime s[56:57]\n\t" \
    "s_waitcnt lgkmcnt(0)\n\t" \
    "s_sub_u32 s69, s56, s73\n\t" \
    "s_cmp_lt_u32 s69, 3000\n\t" \
    "s_cbranch_scc1 vv_St75_%=\n\t" \
    "s_waitcnt lgkmcnt(0)\n\t" \
    "s_mov_b32 s70, 116\n\t" \
    "vv_E76_%=:\n\t" \
    "s_memtime s[56:57]\n\t" \
    "s_memrealtime s[2:3]\n\t" \
    "s_nop 0\n\t" \
    "s_memrealtime s[4:5]\n\t" \
    "s_nop 0\n\t" \
    "s_memrealtime s[6:7]\n\t" \
    "s_nop 0\n\t" \
    "s_memrealtime s[8:9]\n\t" \
    "s_nop 0\n\t" \
    "s_memrealtime s[10:11]\n\t" \
    "s_nop 0\n\t" \
    "s_memrealtime s[12:13]\n\t" \
    "s_nop 0\n\t" \
    "s_memrealtime s[14:15]\n\t" \
    "s_nop 0\n\t" \
    "s_memrealtime s[16:17]\n\t" \
    "s_nop 0\n\t" \
    "s_memrealtime s[18:19]\n\t" \
    "s_nop 0\n\t" \
    "s_memrealtime s[20:21]\n\t" \
    "s_nop 0\n\t" \
    "s_memrealtime s[22:23]\n\t" \
    "s_nop 0\n\t" \
    "s_memrealtime s[24:25]\n\t" \
    "s_nop 0\n\t" \
    "s_memrealtime s[26:27]\n\t" \
    "s_nop 0\n\t" \
    "s_memrealtime s[28:29]\n\t" \
    "s_memtime s[54:55]\n\t" \
    "s_add_u32 s70, s70, 8\n\t" \
    "s_waitcnt lgkmcnt(0)\n\t" \
    "s_sub_u32 s69, s54, s56\n\t" \
    "s_cmp_gt_u32 s69, s70\n\t" \
    "s_cbranch_scc1 vv_E76_%=\n\t" \
    "s_add_u32 s2, s2, s4\n\t" \
    "s_add_u32 s6, s6, s8\n\t" \
    "s_add_u32 s10, s10, s12\n\t" \
    "s_add_u32 s14, s14, s16\n\t" \
    "s_add_u32 s18, s18, s20\n\t" \
    "s_add_u32 s22, s22, s24\n\t" \
    "s_add_u32 s2, s2, s6\n\t" \
    "s_add_u32 s10, s10, s14\n\t" \
    "s_add_u32 s18, s18, s22\n\t" \
    "s_add_u32 s2, s2, s26\n\t" \
    "s_add_u32 s10, s10, s18\n\t" \
    "s_add_u32 s2, s2, s10\n\t" \
    "s_mul_i32 s70, s28, 13\n\t" \
    "s_sub_u32 s70, s70, s2\n\t" \
    "s_min_u32 s70, s70, 52\n\t" \
    "s_mul_i32 s69, s69, s70\n\t" \
    "s_mul_hi_u32 s69, s69, 76695844\n\t" \
    "s_mov_b32 s75, s73\n\t" \
    "s_mov_b32 s76, s74\n\t" \
    "s_add_u32 s73, s56, s69\n\t" \
    "s_lshl_b32 s74, s28, 4\n\t" \
    "vv_St75_%=:\n\t" \
    "vv_Hok70_%=:\n\t" \
    "v_max_u32_dpp v140, v140, v140 row_ror:1 row_mask:0xf bank_mask:0xf\n\t" \
    "s_nop 1\n\t" \
    "v_max_u32_dpp v140, v140, v140 row_ror:2 row_mask:0xf bank_mask:0xf\n\t" \
    "s_nop 1\n\t" \
    "v_max_u32_dpp v140, v140, v140 row_ror:4 row_mask:0xf bank_mask:0xf\n\t" \
    "s_nop 1\n\t" \
    "v_max_u32_dpp v140, v140, v140 row_ror:8 row_mask:0xf bank_mask:0xf\n\t" \
    "s_nop 0\n\t" \
    "v_readlane_b32 s69, v140, 0\n\t" \
    "v_readlane_b32 s71, v140, 16\n\t" \
    "s_nop 1\n\t" \
    "s_max_u32 s81, s69, s71\n\t" \
    "s_mul_hi_u32 s69, s85, s78\n\t" \
    "s_add_u32 s79, s81, s69\n\t" \
    "s_add_u32 s79, s79, s87\n\t" \
    "s_cmp_lt_u32 s65, 6\n\t" \
    "s_cselect_b32 s69, 48, 0\n\t" \
    "s_add_u32 s79, s79, s69\n\t" \
    "s_add_u32 s80, s79, s88\n\t" \
    "s_sub_u32 s70, s79, s74\n\t" \
    "s_max_i32 s70, s70, 0\n\t" \
    "s_min_i32 s70, s70, 16000\n\t" \
    "s_lshl_b32 s70, s70, 1\n\t" \
    "s_mul_hi_u32 s70, s70, s77\n\t" \
    "s_add_u32 s70, s70, s73\n\t" \
    "v_mov_b32 v160, s70\n\t" \
    "ds_write_b32 v11, v160 offset:20480\n\t" \
    "s_and_b32 s69, s65, 7\n\t" \
    "s_lshl_b32 s69, s69, 18\n\t" \
    "s_add_u32 s69, s69, s63\n\t" \
    "s_add_u32 s52, s36, s69\n\t" \
    "s_addc_u32 s53, s37, 0\n\t" \
    "s_waitcnt lgkmcnt(0)\n\t" \
    "s_barrier\n\t" \
    "s_branch vv_loop14_%=\n\t" \
    "vv_Gbad63_%=:\n\t" \
    "s_waitcnt vmcnt(0) lgkmcnt(0)\n\t" \
    "s_nop 7\n\t" \
    "v_mov_b32 v72, 0\n\t" \
    "v_mov_b32 v73, 0\n\t" \
    "v_mov_b32 v74, 0\n\t" \
    "v_mov_b32 v75, 0\n\t" \
    "v_mov_b32 v76, 0\n\t" \
    "v_mov_b32 v77, 0\n\t" \
    "v_mov_b32 v78, 0\n\t" \
    "v_mov_b32 v79, 0\n\t" \
    "s_cmp_eq_u32 s68, 0\n\t" \
    "s_cbranch_scc1 vv_F77_%=\n\t" \
    "s_sub_u32 s68, s68, 1\n\t" \
    "s_branch vv_G62_%=\n\t" \
    "vv_F77_%=:\n\t" \
    "v_mov_b32 v140, 5767424\n\t" \
    "v_mov_b32 v141, 1\n\t" \
    "global_store_dword v140, v141, s[36:37] sc1\n\t" \
    "s_waitcnt vmcnt(0)\n\t" \
    "s_branch vv_Gc64_%=\n\t" \
    "vv_Gslow65_%=:\n\t" \
    "v_max_u32 v140, v36, v37\n\t" \
    "v_max3_u32 v140, v140, v38, v39\n\t" \
    "v_max3_u32 v140, v140, v40, v41\n\t" \
    "v_max3_u32 v140, v140, v42, v43\n\t" \
    "v_max3_u32 v140, v140, v44, v45\n\t" \
    "v_max3_u32 v140, v140, v46, v47\n\t" \
    "v_max3_u32 v140, v140, v48, v49\n\t" \
    "v_max3_u32 v140, v140, v50, v51\n\t" \
    "v_max3_u32 v140, v140, v52, v53\n\t" \
    "v_max3_u32 v140, v140, v54, v55\n\t" \
    "v_max3_u32 v140, v140, v56, v57\n\t" \
    "v_max3_u32 v140, v140, v58, v59\n\t" \
    "v_max3_u32 v140, v140, v60, v61\n\t" \
    "v_max3_u32 v140, v140, v62, v63\n\t" \
    "v_max3_u32 v140, v140, v64, v65\n\t" \
    "v_max3_u32 v140, v140, v66, v67\n\t" \
    "v_max3_u32 v140, v140, v68, v69\n\t" \
    "v_max3_u32 v140, v140, v70, v71\n\t" \
    "v_cmp_eq_u32 vcc, -1, v140\n\t" \
    "s_cbranch_vccz vv_Gback66_%=\n\t" \
    "s_waitcnt lgkmcnt(0)\n\t" \
    "v_sub_u32 v140, v1, v3\n\t" \
    "s_nop 0\n\t" \
    "v_readfirstlane_b32 s69, v140\n\t" \
    "v_add_u32 v143, 36864, v0\n\t" \
    "s_nop 7\n\t" \
    "s_add_u32 s30, s46, s69\n\t" \
    "s_addc_u32 s31, s47, 0\n\t" \
    "vv_Gag78_%=:\n\t" \
    "global_load_dwordx4 v[36:39], v1, s[46:47] sc1\n\t" \
    "global_load_dwordx4 v[40:43], v23, s[46:47] sc1\n\t" \
    "global_load_dwordx4 v[44:47], v143, s[30:31] sc1\n\t" \
    "global_load_dwordx4 v[48:51], v4, s[30:31] sc1\n\t" \
    "global_load_dwordx4 v[52:55], v5, s[30:31] sc1\n\t" \
    "global_load_dwordx4 v[56:59], v6, s[30:31] sc1\n\t" \
    "global_load_dwordx4 v[60:63], v7, s[30:31] sc1\n\t" \
    "global_load_dwordx4 v[64:67], v8, s[30:31] sc1\n\t" \
    "global_load_dwordx4 v[68:71], v9, s[30:31] sc1\n\t" \
    "s_waitcnt vmcnt(0)\n\t" \
    "v_max_u32 v140, v36, v37\n\t" \
    "v_max3_u32 v140, v140, v38, v39\n\t" \
    "v_max3_u32 v140, v140, v40, v41\n\t" \
    "v_max3_u32 v140, v140, v42, v43\n\t" \
    "v_max3_u32 v140, v140, v44, v45\n\t" \
    "v_max3_u32 v140, v140, v46, v47\n\t" \
    "v_max3_u32 v140, v140, v48, v49\n\t" \
    "v_max3_u32 v140, v140, v50, v51\n\t" \
    "v_max3_u32 v140, v140, v52, v53\n\t" \
    "v_max3_u32 v140, v140, v54, v55\n\t" \
    "v_max3_u32 v140, v140, v56, v57\n\t" \
    "v_max3_u32 v140, v140, v58, v59\n\t" \
    "v_max3_u32 v140, v140, v60, v61\n\t" \
    "v_max3_u32 v140, v140, v62, v63\n\t" \
    "v_max3_u32 v140, v140, v64, v65\n\t" \
    "v_max3_u32 v140, v140, v66, v67\n\t" \
    "v_max3_u32 v140, v140, v68, v69\n\t" \
    "v_max3_u32 v140, v140, v70, v71\n\t" \
    "v_cmp_eq_u32 vcc, -1, v140\n\t" \
    "s_cbranch_vccz vv_Gredo79_%=\n\t" \
    "s_cmp_eq_u32 s68, 0\n\t" \
    "s_cbranch_scc1 vv_F80_%=\n\t" \
    "s_sub_u32 s68, s68, 1\n\t" \
    "s_sleep 1\n\t" \
    "s_branch vv_Gag78_%=\n\t" \
    "vv_F80_%=:\n\t" \
    "v_mov_b32 v140, 5767424\n\t" \
    "v_mov_b32 v141, 2\n\t" \
    "global_store_dword v140, v141, s[36:37] sc1\n\t" \
    "s_waitcnt vmcnt(0)\n\t" \
    "s_branch vv_Gredo79_%=\n\t" \
    "vv_Gredo79_%=:\n\t" \
    "v_mov_b32 v72, 0\n\t" \
    "v_mov_b32 v73, 0\n\t" \
    "v_mov_b32 v74, 0\n\t" \
    "v_mov_b32 v75, 0\n\t" \
    "v_mov_b32 v76, 0\n\t" \
    "v_mov_b32 v77, 0\n\t" \
    "v_mov_b32 v78, 0\n\t" \
    "v_mov_b32 v79, 0\n\t" \
    "v_smfmac_f32_16x16x64_bf16 v[72:75], v[36:39], a[48:55], v10\n\t" \
    "v_smfmac_f32_16x16x64_bf16 v[72:75], v[40:43], a[56:63], v10\n\t" \
    "v_smfmac_f32_16x16x64_bf16 v[72:75], v[48:51], a[0:7], v10\n\t" \
    "v_smfmac_f32_16x16x64_bf16 v[72:75], v[52:55], a[8:15], v10\n\t" \
    "v_smfmac_f32_16x16x64_bf16 v[72:75], v[56:59], a[16:23], v10\n\t" \
    "v_smfmac_f32_16x16x64_bf16 v[72:75], v[60:63], a[24:31], v10\n\t" \
    "v_smfmac_f32_16x16x64_bf16 v[72:75], v[64:67], a[32:39], v10\n\t" \
    "v_smfmac_f32_16x16x64_bf16 v[72:75], v[68:71], a[40:47], v10\n\t" \
    "v_smfmac_f32_16x16x64_bf16 v[72:75], v[44:47], a[64:71], v10\n\t" \
    "v_smfmac_f32_16x16x64_bf16 v[76:79], v[36:39], a[120:127], v10\n\t" \
    "v_smfmac_f32_16x16x64_bf16 v[76:79], v[40:43], a[128:135], v10\n\t" \
    "v_smfmac_f32_16x16x64_bf16 v[76:79], v[48:51], a[72:79], v10\n\t" \
    "v_smfmac_f32_16x16x64_bf16 v[76:79], v[52:55], a[80:87], v10\n\t" \
    "v_smfmac_f32_16x16x64_bf16 v[76:79], v[56:59], a[88:95], v10\n\t" \
    "v_smfmac_f32_16x16x64_bf16 v[76:79], v[60:63], a[96:103], v10\n\t" \
    "v_smfmac_f32_16x16x64_bf16 v[76:79], v[64:67], a[104:111], v10\n\t" \
    "v_smfmac_f32_16x16x64_bf16 v[76:79], v[68:71], a[112:119], v10\n\t" \
    "v_smfmac_f32_16x16x64_bf16 v[76:79], v[44:47], a[136:143], v10\n\t" \
    "v_add_f32 v72, v72, v74\n\t" \
    "v_add_f32 v73, v73, v75\n\t" \
    "v_cvt_pk_bf16_f32 v80, v72, v73\n\t" \
    "ds_write_b32 v12, v80 offset:0\n\t" \
    "s_nop 3\n\t" \
    "v_pk_add_f32 v[76:77], v[76:77], v[78:79]\n\t" \
    "v_cvt_pk_bf16_f32 v81, v76, v77\n\t" \
    "ds_write_b32 v12, v81 offset:256\n\t" \
    "s_branch vv_Gback66_%=\n\t" \
    "vv_end58_%=:\n\t" \
    "s_endpgm\n\t" \
    "vv_last_%=:\n\t" \
    ""
#define ASM_R \
    "s_mov_b64 s[36:37], %0\n\t" \
    "s_mov_b64 s[42:43], %1\n\t" \
    "s_mov_b64 s[44:45], %2\n\t" \
    "s_mov_b32 s66, %3\n\t" \
    "s_mov_b32 s67, %4\n\t" \
    "s_mov_b64 s[50:51], %5\n\t" \
    "s_mov_b32 s82, %7\n\t" \
    "s_load_dwordx8 s[84:91], %6, 0\n\t" \
    "v_mbcnt_lo_u32_b32 v223, -1, 0\n\t" \
    "v_mbcnt_hi_u32_b32 v223, -1, v223\n\t" \
    "s_getpc_b64 s[46:47]\n\t" \
    "rr_pc_%=:\n\t" \
    "s_lshl_b32 s69, s66, 13\n\t" \
    "v_lshl_add_u32 v222, v223, 7, s69\n\t" \
    "v_min_u32 v222, rr_last_%=-rr_pc_%=-4, v222\n\t" \
    "global_load_dword v222, v222, s[46:47]\n\t" \
    "v_lshlrev_b32 v223, 2, v223\n\t" \
    "v_add_u32 v223, s82, v223\n\t" \
    "ds_read_b32 v0, v223 offset:0\n\t" \
    "ds_read_b32 v1, v223 offset:256\n\t" \
    "ds_read_b32 v2, v223 offset:512\n\t" \
    "ds_read_b32 v3, v223 offset:768\n\t" \
    "ds_read_b32 v4, v223 offset:1024\n\t" \
    "ds_read_b32 v5, v223 offset:1280\n\t" \
    "ds_read_b32 v6, v223 offset:1536\n\t" \
    "ds_read_b32 v7, v223 offset:1792\n\t" \
    "ds_read_b32 v8, v223 offset:2048\n\t" \
    "ds_read_b32 v9, v223 offset:2304\n\t" \
    "ds_read_b32 v10, v223 offset:2560\n\t" \
    "ds_read_b32 v11, v223 offset:2816\n\t" \
    "s_waitcnt lgkmcnt(0)\n\t" \
    "ds_read_b32 v12, v223 offset:3072\n\t" \
    "ds_read_b32 v13, v223 offset:3328\n\t" \
    "ds_read_b32 v14, v223 offset:3584\n\t" \
    "ds_read_b32 v15, v223 offset:3840\n\t" \
    "ds_read_b32 v16, v223 offset:4096\n\t" \
    "ds_read_b32 v17, v223 offset:4352\n\t" \
    "ds_read_b32 v18, v223 offset:4608\n\t" \
    "ds_read_b32 v19, v223 offset:4864\n\t" \
    "ds_read_b32 v20, v223 offset:5120\n\t" \
    "ds_read_b32 v21, v223 offset:5376\n\t" \
    "ds_read_b32 v22, v223 offset:5632\n\t" \
    "ds_read_b32 v23, v223 offset:5888\n\t" \
    "s_waitcnt lgkmcnt(0)\n\t" \
    "ds_read_b32 v24, v223 offset:6144\n\t" \
    "ds_read_b32 v25, v223 offset:6400\n\t" \
    "ds_read_b32 v26, v223 offset:6656\n\t" \
    "ds_read_b32 v27, v223 offset:6912\n\t" \
    "ds_read_b32 v28, v223 offset:7168\n\t" \
    "ds_read_b32 v29, v223 offset:7424\n\t" \
    "ds_read_b32 v30, v223 offset:7680\n\t" \
    "ds_read_b32 v31, v223 offset:7936\n\t" \
    "ds_read_b32 v32, v223 offset:8192\n\t" \
    "ds_read_b32 v33, v223 offset:8448\n\t" \
    "ds_read_b32 v34, v223 offset:8704\n\t" \
    "ds_read_b32 v35, v223 offset:8960\n\t" \
    "s_waitcnt lgkmcnt(0)\n\t" \
    "ds_read_b32 v36, v223 offset:9216\n\t" \
    "ds_read_b32 v37, v223 offset:9472\n\t" \
    "ds_read_b32 v38, v223 offset:9728\n\t" \
    "ds_read_b32 v39, v223 offset:9984\n\t" \
    "ds_read_b32 v40, v223 offset:10240\n\t" \
    "ds_read_b32 v41, v223 offset:10496\n\t" \
    "ds_read_b32 v42, v223 offset:10752\n\t" \
    "ds_read_b32 v43, v223 offset:11008\n\t" \
    "ds_read_b32 v44, v223 offset:11264\n\t" \
    "ds_read_b32 v45, v223 offset:11520\n\t" \
    "ds_read_b32 v183, v223 offset:11776\n\t" \
    "ds_read_b32 v184, v223 offset:12032\n\t" \
    "s_waitcnt lgkmcnt(0)\n\t" \
    "ds_read_b32 v185, v223 offset:12288\n\t" \
    "ds_read_b32 v186, v223 offset:12544\n\t" \
    "ds_read_b32 v187, v223 offset:12800\n\t" \
    "ds_read_b32 v188, v223 offset:13056\n\t" \
    "s_waitcnt lgkmcnt(0)\n\t" \
    "s_mov_b32 s68, 262144\n\t" \
    "s_mov_b32 s72, 0\n\t" \
    "s_mov_b32 s83, -1\n\t" \
    "s_mov_b32 s65, 0\n\t" \
    "v_xor_b32 v180, 16384, v2\n\t" \
    "v_xor_b32 v181, 131072, v2\n\t" \
    "v_xor_b32 v182, 147456, v2\n\t" \
    "v_add_u32 v189, 4096, v7\n\t" \
    "v_add_u32 v190, 32768, v7\n\t" \
    "v_add_u32 v191, 36864, v7\n\t" \
    "s_mul_i32 s69, s67, 57\n\t" \
    "s_lshr_b32 s69, s69, 9\n\t" \
    "s_mul_i32 s69, s69, 9\n\t" \
    "s_sub_u32 s69, s67, s69\n\t" \
    "s_cmp_lt_u32 s69, 4\n\t" \
    "s_cselect_b32 s70, 21, 16\n\t" \
    "s_cmp_eq_u32 s69, 8\n\t" \
    "s_cselect_b32 s70, 26, s70\n\t" \
    "s_lshr_b32 s97, s91, s70\n\t" \
    "s_and_b32 s97, s97, 31\n\t" \
    "s_min_u32 s97, s97, 0\n\t" \
    "s_lshl_b32 s97, s97, 4\n\t" \
    "s_mov_b32 s77, 3019898880\n\t" \
    "s_mov_b32 s78, 3054198966\n\t" \
    "s_waitcnt lgkmcnt(0)\n\t" \
    "s_mov_b32 s70, 116\n\t" \
    "rr_E1_%=:\n\t" \
    "s_memtime s[56:57]\n\t" \
    "s_memrealtime s[2:3]\n\t" \
    "s_nop 0\n\t" \
    "s_memrealtime s[4:5]\n\t" \
    "s_nop 0\n\t" \
    "s_memrealtime s[6:7]\n\t" \
    "s_nop 0\n\t" \
    "s_memrealtime s[8:9]\n\t" \
    "s_nop 0\n\t" \
    "s_memrealtime s[10:11]\n\t" \
    "s_nop 0\n\t" \
    "s_memrealtime s[12:13]\n\t" \
    "s_nop 0\n\t" \
    "s_memrealtime s[14:15]\n\t" \
    "s_nop 0\n\t" \
    "s_memrealtime s[16:17]\n\t" \
    "s_nop 0\n\t" \
    "s_memrealtime s[18:19]\n\t" \
    "s_nop 0\n\t" \
    "s_memrealtime s[20:21]\n\t" \
    "s_nop 0\n\t" \
    "s_memrealtime s[22:23]\n\t" \
    "s_nop 0\n\t" \
    "s_memrealtime s[24:25]\n\t" \
    "s_nop 0\n\t" \
    "s_memrealtime s[26:27]\n\t" \
    "s_nop 0\n\t" \
    "s_memrealtime s[28:29]\n\t" \
    "s_memtime s[54:55]\n\t" \
    "s_add_u32 s70, s70, 8\n\t" \
    "s_waitcnt lgkmcnt(0)\n\t" \
    "s_sub_u32 s69, s54, s56\n\t" \
    "s_cmp_gt_u32 s69, s70\n\t" \
    "s_cbranch_scc1 rr_E1_%=\n\t" \
    "s_add_u32 s2, s2, s4\n\t" \
    "s_add_u32 s6, s6, s8\n\t" \
    "s_add_u32 s10, s10, s12\n\t" \
    "s_add_u32 s14, s14, s16\n\t" \
    "s_add_u32 s18, s18, s20\n\t" \
    "s_add_u32 s22, s22, s24\n\t" \
    "s_add_u32 s2, s2, s6\n\t" \
    "s_add_u32 s10, s10, s14\n\t" \
    "s_add_u32 s18, s18, s22\n\t" \
    "s_add_u32 s2, s2, s26\n\t" \
    "s_add_u32 s10, s10, s18\n\t" \
    "s_add_u32 s2, s2, s10\n\t" \
    "s_mul_i32 s70, s28, 13\n\t" \
    "s_sub_u32 s70, s70, s2\n\t" \
    "s_min_u32 s70, s70, 52\n\t" \
    "s_mul_i32 s69, s69, s70\n\t" \
    "s_mul_hi_u32 s69, s69, 76695844\n\t" \
    "s_mov_b32 s75, s73\n\t" \
    "s_mov_b32 s76, s74\n\t" \
    "s_add_u32 s73, s56, s69\n\t" \
    "s_lshl_b32 s74, s28, 4\n\t" \
    "v_mov_b32 v172, 1807360\n\t" \
    "v_mov_b32 v173, 3904512\n\t" \
    "global_load_dword v172, v172, s[36:37]\n\t" \
    "global_load_dword v173, v173, s[36:37]\n\t" \
    "s_mov_b32 s62, 0\n\t" \
    "s_mov_b32 s63, 0x200000\n\t" \
    "s_mov_b64 s[44:45], 0\n\t" \
    "v_mov_b32 v170, 0\n\t" \
    "s_mov_b32 s65, s66\n\t" \
    "s_mul_i32 s59, s67, 57\n\t" \
    "s_lshr_b32 s59, s59, 9\n\t" \
    "s_add_u32 s69, s59, 1\n\t" \
    "s_lshr_b32 s69, s69, 1\n\t" \
    "s_mul_i32 s69, s69, 9\n\t" \
    "s_sub_u32 s58, s67, s69\n\t" \
    "s_lshl_b32 s58, s58, 15\n\t" \
    "s_sub_u32 s80, s74, 32000\n\t" \
    "s_mov_b32 s64, 1\n\t" \
    "s_mov_b32 s65, 5\n\t" \
    "s_mov_b32 s83, 99\n\t" \
    "s_add_u32 s70, s65, 59\n\t" \
    "s_mul_i32 s69, s70, 171\n\t" \
    "s_lshr_b32 s69, s69, 9\n\t" \
    "s_mul_i32 s71, s69, 3\n\t" \
    "s_sub_u32 s70, s70, s71\n\t" \
    "s_mul_i32 s70, s70, 589824\n\t" \
    "s_lshl1_add_u32 s71, s69, 4\n\t" \
    "s_lshr_b64 s[40:41], s[44:45], s71\n\t" \
    "s_lshl_b32 s71, s59, 1\n\t" \
    "s_xor_b32 s71, s71, s40\n\t" \
    "s_and_b32 s71, s71, 3\n\t" \
    "s_lshl_b32 s71, s71, 13\n\t" \
    "s_add_u32 s70, s70, s71\n\t" \
    "s_lshl_b32 s69, s69, 21\n\t" \
    "s_add_u32 s69, s69, 123731968\n\t" \
    "s_add_u32 s70, s70, s58\n\t" \
    "s_lshr_b32 s71, s70, 5\n\t" \
    "s_and_b32 s71, s71, 0x4000\n\t" \
    "s_xor_b32 s70, s70, s71\n\t" \
    "s_add_u32 s69, s69, s70\n\t" \
    "s_add_u32 s40, s36, s69\n\t" \
    "s_addc_u32 s41, s37, 0\n\t" \
    "v_mov_b32 v192, 0\n\t" \
    "s_mov_b64 exec, 1\n\t" \
    "s_branch rr_act3_%=\n\t" \
    "rr_g214_%=:\n\t" \
    "s_mov_b64 exec, -1\n\t" \
    "s_waitcnt vmcnt(0)\n\t" \
    "v_mbcnt_lo_u32_b32 v164, -1, 0\n\t" \
    "v_mbcnt_hi_u32_b32 v164, -1, v164\n\t" \
    "v_lshlrev_b32 v164, 6, v164\n\t" \
    "v_add_u32 v164, 6225920, v164\n\t" \
    "v_mov_b32 v165, 6234112\n\t" \
    "global_load_dword v165, v165, s[36:37] sc1\n\t" \
    "global_load_dwordx2 v[166:167], v164, s[36:37] sc1\n\t" \
    "global_load_dword v168, v164, s[36:37] offset:2048 sc1\n\t" \
    "s_waitcnt vmcnt(0)\n\t" \
    "rr_C15_%=:\n\t" \
    "v_cmp_ne_u32 s[54:55], v166, v168\n\t" \
    "v_sub_u32 v166, v166, v165\n\t" \
    "v_cmp_ne_u32 vcc, 1, v166\n\t" \
    "s_nop 1\n\t" \
    "s_or_b64 vcc, vcc, s[54:55]\n\t" \
    "s_cbranch_scc0 rr_Cok16_%=\n\t" \
    "s_cmp_eq_u32 s68, 0\n\t" \
    "s_cbranch_scc1 rr_F18_%=\n\t" \
    "s_sub_u32 s68, s68, 1\n\t" \
    "s_sleep 6\n\t" \
    "s_branch rr_Cag17_%=\n\t" \
    "rr_F18_%=:\n\t" \
    "v_mov_b32 v168, 5767424\n\t" \
    "v_mov_b32 v169, 4\n\t" \
    "global_store_dword v168, v169, s[36:37] sc1\n\t" \
    "s_waitcnt vmcnt(0)\n\t" \
    "s_branch rr_Cok16_%=\n\t" \
    "rr_Cag17_%=:\n\t" \
    "global_load_dwordx2 v[166:167], v164, s[36:37] sc1\n\t" \
    "global_load_dword v168, v164, s[36:37] offset:2048 sc1\n\t" \
    "s_waitcnt vmcnt(0)\n\t" \
    "s_branch rr_C15_%=\n\t" \
    "rr_Cok16_%=:\n\t" \
    "v_readlane_b32 s69, v167, 2\n\t" \
    "v_readlane_b32 s70, v167, 3\n\t" \
    "s_nop 1\n\t" \
    "s_cmp_lt_u32 s70, s69\n\t" \
    "s_cselect_b32 s69, 0, 8192\n\t" \
    "s_add_u32 s100, s36, s69\n\t" \
    "s_addc_u32 s101, s37, 0\n\t" \
    "s_cmp_lg_u32 s66, 0\n\t" \
    "s_cbranch_scc1 rr_Ca19_%=\n\t" \
    "v_mov_b32 v164, 5768192\n\t" \
    "s_mov_b64 exec, 1\n\t" \
    "global_atomic_add v164, v166, s[36:37] sc1\n\t" \
    "s_mov_b64 exec, -1\n\t" \
    "rr_Ca19_%=:\n\t" \
    "v_mov_b32_dpp v164, v167 quad_perm:[1,0,3,2] row_mask:0xf bank_mask:0xf\n\t" \
    "v_cmp_lt_u32 s[44:45], v164, v167\n\t" \
    "v_min_u32 v164, v164, v167\n\t" \
    "v_and_b32 v165, 1, v164\n\t" \
    "v_cmp_ne_u32 vcc, 0, v165\n\t" \
    "s_nop 1\n\t" \
    "s_and_b32 s44, s44, 0x55555555\n\t" \
    "s_and_b32 s69, vcc_lo, 0xaaaaaaaa\n\t" \
    "s_or_b32 s44, s44, s69\n\t" \
    "s_and_b32 s45, s45, 0x55555555\n\t" \
    "s_and_b32 s69, vcc_hi, 0xaaaaaaaa\n\t" \
    "s_or_b32 s45, s45, s69\n\t" \
    "s_bitcmp1_b32 s44, 0\n\t" \
    "s_cselect_b32 s62, 8192, 0\n\t" \
    "s_bitcmp1_b32 s44, 2\n\t" \
    "s_cselect_b32 s63, 8192, 0\n\t" \
    "s_or_b32 s63, s63, 0x200000\n\t" \
    "v_cmp_lt_u32 vcc, 1320, v164\n\t" \
    "s_nop 1\n\t" \
    "s_bcnt1_i32_b64 s69, vcc\n\t" \
    "s_cmp_gt_u32 s69, 16\n\t" \
    "s_cselect_b32 s69, 1, 0\n\t" \
    "s_mul_i32 s70, s69, 160\n\t" \
    "s_add_u32 s86, s86, s70\n\t" \
    "s_mul_i32 s70, s69, 536\n\t" \
    "s_add_u32 s87, s87, s70\n\t" \
    "s_mul_i32 s70, s69, 256\n\t" \
    "s_add_u32 s88, s88, s70\n\t" \
    "v_mov_b32 v172, -1\n\t" \
    "v_mov_b32 v173, -1\n\t" \
    "v_mov_b32 v174, -1\n\t" \
    "v_mov_b32 v175, -1\n\t" \
    "v_mov_b32 v176, -1\n\t" \
    "s_sub_u32 s69, s63, s62\n\t" \
    "s_sub_u32 s69, s69, 0x200000\n\t" \
    "v_bfe_u32 v164, v186, 21, 1\n\t" \
    "v_mad_i32_i24 v186, v164, s69, v186\n\t" \
    "v_bfe_u32 v164, v187, 21, 1\n\t" \
    "v_mad_i32_i24 v187, v164, s69, v187\n\t" \
    "v_bfe_u32 v164, v188, 21, 1\n\t" \
    "v_mad_i32_i24 v188, v164, s69, v188\n\t" \
    "s_bitcmp1_b32 s44, 1\n\t" \
    "s_cselect_b32 s69, 16384, 0\n\t" \
    "v_xor_b32 v10, s69, v10\n\t" \
    "v_xor_b32 v11, s69, v11\n\t" \
    "v_xor_b32 v12, s69, v12\n\t" \
    "v_xor_b32 v13, s69, v13\n\t" \
    "v_xor_b32 v14, s69, v14\n\t" \
    "v_xor_b32 v15, s69, v15\n\t" \
    "v_xor_b32 v16, s69, v16\n\t" \
    "v_xor_b32 v17, s69, v17\n\t" \
    "v_xor_b32 v18, s69, v18\n\t" \
    "v_xor_b32 v19, s69, v19\n\t" \
    "v_xor_b32 v20, s69, v20\n\t" \
    "v_xor_b32 v21, s69, v21\n\t" \
    "v_xor_b32 v22, s69, v22\n\t" \
    "v_xor_b32 v23, s69, v23\n\t" \
    "v_xor_b32 v24, s69, v24\n\t" \
    "v_xor_b32 v25, s69, v25\n\t" \
    "v_xor_b32 v42, s69, v42\n\t" \
    "v_xor_b32 v43, s69, v43\n\t" \
    "v_xor_b32 v44, s69, v44\n\t" \
    "v_xor_b32 v45, s69, v45\n\t" \
    "s_bitcmp1_b32 s44, 3\n\t" \
    "s_cselect_b32 s69, 16384, 0\n\t" \
    "v_xor_b32 v7, s69, v7\n\t" \
    "v_xor_b32 v189, s69, v189\n\t" \
    "v_xor_b32 v190, s69, v190\n\t" \
    "v_xor_b32 v191, s69, v191\n\t" \
    "s_mov_b32 s64, 0\n\t" \
    "s_mov_b32 s65, s66\n\t" \
    "s_mov_b32 s83, 1\n\t" \
    "s_mov_b32 s77, 3154116608\n\t" \
    "rr_pf9_%=:\n\t" \
    "s_cmp_gt_u32 s65, 8\n\t" \
    "s_cselect_b32 s83, 99, s83\n\t" \
    "v_cmp_gt_i32 vcc, s65, v176\n\t" \
    "s_nop 1\n\t" \
    "s_max_i32 s69, s83, s65\n\t" \
    "s_cmp_eq_u64 vcc, 0\n\t" \
    "s_cselect_b32 s83, s69, s83\n\t" \
    "s_lshl_b32 s69, s65, 11\n\t" \
    "s_add_u32 s69, s69, 5799936\n\t" \
    "v_and_b32 v169, 0x7c0, v7\n\t" \
    "v_add_u32 v169, s69, v169\n\t" \
    "s_cmp_lg_u32 s65, 2\n\t" \
    "s_cbranch_scc1 rr_n2s20_%=\n\t" \
    "s_sleep 10\n\t" \
    "rr_n2s20_%=:\n\t" \
    "rr_WF21_%=:\n\t" \
    "s_cmp_ge_i32 s83, s65\n\t" \
    "s_cbranch_scc1 rr_WFok22_%=\n\t" \
    "global_load_dword v164, v169, s[36:37] sc1\n\t" \
    "s_waitcnt vmcnt(0)\n\t" \
    "v_min_i32_dpp v164, v164, v164 row_ror:1 row_mask:0xf bank_mask:0xf\n\t" \
    "s_nop 1\n\t" \
    "v_min_i32_dpp v164, v164, v164 row_ror:2 row_mask:0xf bank_mask:0xf\n\t" \
    "s_nop 1\n\t" \
    "v_min_i32_dpp v164, v164, v164 row_ror:4 row_mask:0xf bank_mask:0xf\n\t" \
    "s_nop 1\n\t" \
    "v_min_i32_dpp v164, v164, v164 row_ror:8 row_mask:0xf bank_mask:0xf\n\t" \
    "s_nop 0\n\t" \
    "v_readlane_b32 s69, v164, 0\n\t" \
    "v_readlane_b32 s70, v164, 16\n\t" \
    "s_nop 1\n\t" \
    "s_min_i32 s83, s69, s70\n\t" \
    "s_cmp_ge_i32 s83, s65\n\t" \
    "s_cbranch_scc1 rr_WFok22_%=\n\t" \
    "s_cmp_eq_u32 s68, 0\n\t" \
    "s_cbranch_scc1 rr_F23_%=\n\t" \
    "s_sub_u32 s68, s68, 1\n\t" \
    "s_sleep 2\n\t" \
    "s_branch rr_WF21_%=\n\t" \
    "rr_F23_%=:\n\t" \
    "v_mov_b32 v164, 5767424\n\t" \
    "v_mov_b32 v165, 10\n\t" \
    "global_store_dword v164, v165, s[36:37] sc1\n\t" \
    "s_waitcnt vmcnt(0)\n\t" \
    "s_branch rr_WFok22_%=\n\t" \
    "rr_WFok22_%=:\n\t" \
    "s_mul_i32 s70, s67, 7\n\t" \
    "s_add_u32 s69, s65, 0\n\t" \
    "s_xor_b32 s69, s69, s70\n\t" \
    "s_and_b32 s69, s69, 63\n\t" \
    "s_lshl_b32 s69, s69, 15\n\t" \
    "s_mul_i32 s70, s67, 2097152\n\t" \
    "s_add_u32 s69, s69, s70\n\t" \
    "s_add_u32 s69, s69, 48234496\n\t" \
    "s_add_u32 s38, s36, s69\n\t" \
    "s_addc_u32 s39, s37, 0\n\t" \
    "s_add_u32 s70, s65, 0\n\t" \
    "s_mul_i32 s69, s70, 171\n\t" \
    "s_lshr_b32 s69, s69, 9\n\t" \
    "s_mul_i32 s71, s69, 3\n\t" \
    "s_sub_u32 s70, s70, s71\n\t" \
    "s_mul_i32 s70, s70, 589824\n\t" \
    "s_lshl1_add_u32 s71, s69, 4\n\t" \
    "s_lshr_b64 s[40:41], s[44:45], s71\n\t" \
    "s_lshl_b32 s71, s59, 1\n\t" \
    "s_xor_b32 s71, s71, s40\n\t" \
    "s_and_b32 s71, s71, 3\n\t" \
    "s_lshl_b32 s71, s71, 13\n\t" \
    "s_add_u32 s70, s70, s71\n\t" \
    "s_lshl_b32 s69, s69, 21\n\t" \
    "s_add_u32 s69, s69, 123731968\n\t" \
    "s_add_u32 s70, s70, s58\n\t" \
    "s_lshr_b32 s71, s70, 5\n\t" \
    "s_and_b32 s71, s71, 0x4000\n\t" \
    "s_xor_b32 s70, s70, s71\n\t" \
    "s_add_u32 s69, s69, s70\n\t" \
    "s_add_u32 s40, s36, s69\n\t" \
    "s_addc_u32 s41, s37, 0\n\t" \
    "s_and_b32 s69, s69, 0xffe00000\n\t" \
    "s_add_u32 s69, s69, 1807360\n\t" \
    "v_mov_b32 v168, s69\n\t" \
    "global_load_dword v168, v168, s[36:37]\n\t" \
    "s_cmp_gt_u32 s65, 61\n\t" \
    "s_cbranch_scc1 rr_idle2_%=\n\t" \
    "s_add_u32 s69, s65, 2\n\t" \
    "s_and_b32 s69, s69, 7\n\t" \
    "s_lshl_b32 s69, s69, 18\n\t" \
    "s_add_u32 s69, s69, s62\n\t" \
    "s_add_u32 s46, s36, s69\n\t" \
    "s_addc_u32 s47, s37, 0\n\t" \
    "global_store_dwordx4 v183, v[172:175], s[46:47] sc1\n\t" \
    "global_store_dwordx4 v184, v[172:175], s[46:47] sc1\n\t" \
    "global_store_dwordx4 v185, v[172:175], s[46:47] sc1\n\t" \
    "global_store_dwordx4 v186, v[172:175], s[46:47] sc1\n\t" \
    "global_store_dwordx4 v187, v[172:175], s[46:47] sc1\n\t" \
    "rr_idle2_%=:\n\t" \
    "v_mov_b32 v200, 0\n\t" \
    "s_cmp_lg_u32 s67, 35\n\t" \
    "s_cbranch_scc1 rr_cke24_%=\n\t" \
    "s_cmp_lt_u32 s65, 7\n\t" \
    "s_cbranch_scc1 rr_cke24_%=\n\t" \
    "s_lshl_b32 s69, s65, 7\n\t" \
    "s_add_u32 s69, s69, 5775360\n\t" \
    "v_mov_b32 v201, s69\n\t" \
    "global_load_dword v200, v201, s[36:37] sc1\n\t" \
    "rr_cke24_%=:\n\t" \
    "s_cmp_gt_u32 s65, 62\n\t" \
    "s_cbranch_scc1 rr_n225_%=\n\t" \
    "s_cmp_lt_u32 s65, 2\n\t" \
    "s_cbranch_scc1 rr_nw226_%=\n\t" \
    "s_mul_hi_u32 s69, s85, s78\n\t" \
    "s_add_u32 s79, s81, s69\n\t" \
    "s_add_u32 s79, s79, s87\n\t" \
    "s_cmp_lt_u32 s65, 7\n\t" \
    "s_cselect_b32 s69, 48, 0\n\t" \
    "s_add_u32 s79, s79, s69\n\t" \
    "s_add_u32 s80, s79, s88\n\t" \
    "s_cmp_gt_u32 s65, 4\n\t" \
    "s_cselect_b32 s71, 240, 64\n\t" \
    "s_add_u32 s71, s71, s79\n\t" \
    "s_sub_u32 s70, s71, s74\n\t" \
    "s_max_i32 s70, s70, 0\n\t" \
    "s_min_i32 s70, s70, 16000\n\t" \
    "s_lshl_b32 s70, s70, 1\n\t" \
    "s_mul_hi_u32 s70, s70, s77\n\t" \
    "s_add_u32 s70, s70, s73\n\t" \
    "rr_W27_%=:\n\t" \
    "s_memtime s[56:57]\n\t" \
    "s_waitcnt lgkmcnt(0)\n\t" \
    "s_sub_u32 s69, s70, s56\n\t" \
    "s_cmp_gt_i32 s69, 24\n\t" \
    "s_cbranch_scc1 rr_W27_%=\n\t" \
    "s_branch rr_g113_%=\n\t" \
    "rr_nw226_%=:\n\t" \
    "s_lshl_b32 s71, s59, 12\n\t" \
    "s_add_u32 s71, s71, 5783552\n\t" \
    "s_mov_b32 s96, 96\n\t" \
    "rr_g012_%=:\n\t" \
    "s_memrealtime s[54:55]\n\t" \
    "s_waitcnt lgkmcnt(0)\n\t" \
    "s_bfe_u32 s69, s54, 0x60004\n\t" \
    "s_lshl_b32 s69, s69, 6\n\t" \
    "s_add_u32 s69, s69, s71\n\t" \
    "v_mov_b32 v165, s69\n\t" \
    "global_load_dword v165, v165, s[36:37] sc1\n\t" \
    "s_sub_u32 s96, s96, 1\n\t" \
    "s_cbranch_scc1 rr_gs28_%=\n\t" \
    "s_waitcnt vmcnt(0)\n\t" \
    "v_readfirstlane_b32 s70, v165\n\t" \
    "s_sleep 1\n\t" \
    "s_cmp_eq_u32 s70, -1\n\t" \
    "s_cbranch_scc1 rr_g012_%=\n\t" \
    "s_mul_i32 s69, s66, 352\n\t" \
    "s_add_u32 s70, s70, s69\n\t" \
    "s_sub_u32 s70, s70, 2688\n\t" \
    "rr_gw29_%=:\n\t" \
    "s_memrealtime s[54:55]\n\t" \
    "s_waitcnt lgkmcnt(0)\n\t" \
    "s_lshl_b32 s69, s54, 4\n\t" \
    "s_sub_u32 s69, s70, s69\n\t" \
    "s_cmp_lt_u32 s69, 8000\n\t" \
    "s_cbranch_scc1 rr_gw29_%=\n\t" \
    "rr_gs28_%=:\n\t" \
    "s_waitcnt vmcnt(0)\n\t" \
    "s_mov_b32 s83, -1\n\t" \
    "rr_WF30_%=:\n\t" \
    "s_cmp_ge_i32 s83, s65\n\t" \
    "s_cbranch_scc1 rr_WFok31_%=\n\t" \
    "global_load_dword v164, v169, s[36:37] sc1\n\t" \
    "s_waitcnt vmcnt(0)\n\t" \
    "v_min_i32_dpp v164, v164, v164 row_ror:1 row_mask:0xf bank_mask:0xf\n\t" \
    "s_nop 1\n\t" \
    "v_min_i32_dpp v164, v164, v164 row_ror:2 row_mask:0xf bank_mask:0xf\n\t" \
    "s_nop 1\n\t" \
    "v_min_i32_dpp v164, v164, v164 row_ror:4 row_mask:0xf bank_mask:0xf\n\t" \
    "s_nop 1\n\t" \
    "v_min_i32_dpp v164, v164, v164 row_ror:8 row_mask:0xf bank_mask:0xf\n\t" \
    "s_nop 0\n\t" \
    "v_readlane_b32 s69, v164, 0\n\t" \
    "v_readlane_b32 s70, v164, 16\n\t" \
    "s_nop 1\n\t" \
    "s_min_i32 s83, s69, s70\n\t" \
    "s_cmp_ge_i32 s83, s65\n\t" \
    "s_cbranch_scc1 rr_WFok31_%=\n\t" \
    "s_cmp_eq_u32 s68, 0\n\t" \
    "s_cbranch_scc1 rr_F32_%=\n\t" \
    "s_sub_u32 s68, s68, 1\n\t" \
    "s_sleep 2\n\t" \
    "s_branch rr_WF30_%=\n\t" \
    "rr_F32_%=:\n\t" \
    "v_mov_b32 v164, 5767424\n\t" \
    "v_mov_b32 v165, 16\n\t" \
    "global_store_dword v164, v165, s[36:37] sc1\n\t" \
    "s_waitcnt vmcnt(0)\n\t" \
    "s_branch rr_WFok31_%=\n\t" \
    "rr_WFok31_%=:\n\t" \
    "rr_g113_%=:\n\t" \
    "global_load_dwordx4 a[0:3], v26, s[38:39] offset:0 sc1\n\t" \
    "global_load_dwordx4 a[4:7], v26, s[38:39] offset:1024 sc1\n\t" \
    "global_load_dwordx4 a[8:11], v27, s[38:39] offset:0 sc1\n\t" \
    "global_load_dwordx4 a[12:15], v27, s[38:39] offset:1024 sc1\n\t" \
    "global_load_dwordx4 a[16:19], v28, s[38:39] offset:0 sc1\n\t" \
    "global_load_dwordx4 a[20:23], v28, s[38:39] offset:1024 sc1\n\t" \
    "global_load_dwordx4 a[24:27], v29, s[38:39] offset:0 sc1\n\t" \
    "global_load_dwordx4 a[28:31], v29, s[38:39] offset:1024 sc1\n\t" \
    "global_load_dwordx4 a[32:35], v30, s[38:39] offset:0 sc1\n\t" \
    "global_load_dwordx4 a[36:39], v30, s[38:39] offset:1024 sc1\n\t" \
    "global_load_dwordx4 a[40:43], v31, s[38:39] offset:0 sc1\n\t" \
    "global_load_dwordx4 a[44:47], v31, s[38:39] offset:1024 sc1\n\t" \
    "global_load_dwordx4 a[48:51], v32, s[38:39] offset:0 sc1\n\t" \
    "global_load_dwordx4 a[52:55], v32, s[38:39] offset:1024 sc1\n\t" \
    "global_load_dwordx4 a[56:59], v33, s[38:39] offset:0 sc1\n\t" \
    "global_load_dwordx4 a[60:63], v33, s[38:39] offset:1024 sc1\n\t" \
    "global_load_dwordx4 a[64:67], v34, s[38:39] offset:0 sc1\n\t" \
    "global_load_dwordx4 a[68:71], v34, s[38:39] offset:1024 sc1\n\t" \
    "global_load_dwordx4 a[72:75], v35, s[38:39] offset:0 sc1\n\t" \
    "global_load_dwordx4 a[76:79], v35, s[38:39] offset:1024 sc1\n\t" \
    "global_load_dwordx4 a[80:83], v36, s[38:39] offset:0 sc1\n\t" \
    "global_load_dwordx4 a[84:87], v36, s[38:39] offset:1024 sc1\n\t" \
    "global_load_dwordx4 a[88:91], v37, s[38:39] offset:0 sc1\n\t" \
    "global_load_dwordx4 a[92:95], v37, s[38:39] offset:1024 sc1\n\t" \
    "global_load_dwordx4 a[96:99], v38, s[38:39] offset:0 sc1\n\t" \
    "global_load_dwordx4 a[100:103], v38, s[38:39] offset:1024 sc1\n\t" \
    "global_load_dwordx4 a[104:107], v39, s[38:39] offset:0 sc1\n\t" \
    "global_load_dwordx4 a[108:111], v39, s[38:39] offset:1024 sc1\n\t" \
    "global_load_dwordx4 a[112:115], v40, s[38:39] offset:0 sc1\n\t" \
    "global_load_dwordx4 a[116:119], v40, s[38:39] offset:1024 sc1\n\t" \
    "global_load_dwordx4 a[120:123], v41, s[38:39] offset:0 sc1\n\t" \
    "global_load_dwordx4 a[124:127], v41, s[38:39] offset:1024 sc1\n\t" \
    "rr_n225_%=:\n\t" \
    "s_cmp_eq_u32 s65, 0\n\t" \
    "s_cbranch_scc1 rr_act3_%=\n\t" \
    "s_waitcnt vmcnt(0)\n\t" \
    "rr_ckl34_%=:\n\t" \
    "v_cmp_eq_u32 vcc, -1, v200\n\t" \
    "s_cbranch_vccz rr_ck33_%=\n\t" \
    "global_load_dword v200, v201, s[36:37] sc1\n\t" \
    "s_waitcnt vmcnt(0)\n\t" \
    "s_cmp_eq_u32 s68, 0\n\t" \
    "s_cbranch_scc1 rr_F35_%=\n\t" \
    "s_sub_u32 s68, s68, 1\n\t" \
    "s_sleep 1\n\t" \
    "s_branch rr_ckl34_%=\n\t" \
    "rr_F35_%=:\n\t" \
    "v_mov_b32 v166, 5767424\n\t" \
    "v_mov_b32 v167, 15\n\t" \
    "global_store_dword v166, v167, s[36:37] sc1\n\t" \
    "s_waitcnt vmcnt(0)\n\t" \
    "s_branch rr_ck33_%=\n\t" \
    "rr_ck33_%=:\n\t" \
    "s_barrier\n\t" \
    "s_cmp_gt_u32 s65, 62\n\t" \
    "s_cbranch_scc1 rr_end4_%=\n\t" \
    "ds_read_b32 v164, v9\n\t" \
    "s_waitcnt lgkmcnt(0)\n\t" \
    "v_readfirstlane_b32 s81, v164\n\t" \
    "s_nop 1\n\t" \
    "s_cmp_lg_u32 s65, 1\n\t" \
    "s_cbranch_scc1 rr_n136_%=\n\t" \
    "s_waitcnt lgkmcnt(0)\n\t" \
    "s_mov_b32 s70, 116\n\t" \
    "rr_E37_%=:\n\t" \
    "s_memtime s[56:57]\n\t" \
    "s_memrealtime s[2:3]\n\t" \
    "s_nop 0\n\t" \
    "s_memrealtime s[4:5]\n\t" \
    "s_nop 0\n\t" \
    "s_memrealtime s[6:7]\n\t" \
    "s_nop 0\n\t" \
    "s_memrealtime s[8:9]\n\t" \
    "s_nop 0\n\t" \
    "s_memrealtime s[10:11]\n\t" \
    "s_nop 0\n\t" \
    "s_memrealtime s[12:13]\n\t" \
    "s_nop 0\n\t" \
    "s_memrealtime s[14:15]\n\t" \
    "s_nop 0\n\t" \
    "s_memrealtime s[16:17]\n\t" \
    "s_nop 0\n\t" \
    "s_memrealtime s[18:19]\n\t" \
    "s_nop 0\n\t" \
    "s_memrealtime s[20:21]\n\t" \
    "s_nop 0\n\t" \
    "s_memrealtime s[22:23]\n\t" \
    "s_nop 0\n\t" \
    "s_memrealtime s[24:25]\n\t" \
    "s_nop 0\n\t" \
    "s_memrealtime s[26:27]\n\t" \
    "s_nop 0\n\t" \
    "s_memrealtime s[28:29]\n\t" \
    "s_memtime s[54:55]\n\t" \
    "s_add_u32 s70, s70, 8\n\t" \
    "s_waitcnt lgkmcnt(0)\n\t" \
    "s_sub_u32 s69, s54, s56\n\t" \
    "s_cmp_gt_u32 s69, s70\n\t" \
    "s_cbranch_scc1 rr_E37_%=\n\t" \
    "s_add_u32 s2, s2, s4\n\t" \
    "s_add_u32 s6, s6, s8\n\t" \
    "s_add_u32 s10, s10, s12\n\t" \
    "s_add_u32 s14, s14, s16\n\t" \
    "s_add_u32 s18, s18, s20\n\t" \
    "s_add_u32 s22, s22, s24\n\t" \
    "s_add_u32 s2, s2, s6\n\t" \
    "s_add_u32 s10, s10, s14\n\t" \
    "s_add_u32 s18, s18, s22\n\t" \
    "s_add_u32 s2, s2, s26\n\t" \
    "s_add_u32 s10, s10, s18\n\t" \
    "s_add_u32 s2, s2, s10\n\t" \
    "s_mul_i32 s70, s28, 13\n\t" \
    "s_sub_u32 s70, s70, s2\n\t" \
    "s_min_u32 s70, s70, 52\n\t" \
    "s_mul_i32 s69, s69, s70\n\t" \
    "s_mul_hi_u32 s69, s69, 76695844\n\t" \
    "s_mov_b32 s75, s73\n\t" \
    "s_mov_b32 s76, s74\n\t" \
    "s_add_u32 s73, s56, s69\n\t" \
    "s_lshl_b32 s74, s28, 4\n\t" \
    "rr_n136_%=:\n\t" \
    "s_mul_hi_u32 s69, s85, s78\n\t" \
    "s_add_u32 s79, s81, s69\n\t" \
    "s_add_u32 s79, s79, s87\n\t" \
    "s_cmp_lt_u32 s65, 6\n\t" \
    "s_cselect_b32 s69, 48, 0\n\t" \
    "s_add_u32 s79, s79, s69\n\t" \
    "s_add_u32 s80, s79, s88\n\t" \
    "s_sub_u32 s70, s79, s74\n\t" \
    "s_max_i32 s70, s70, 0\n\t" \
    "s_min_i32 s70, s70, 16000\n\t" \
    "s_lshl_b32 s70, s70, 1\n\t" \
    "s_mul_hi_u32 s70, s70, s77\n\t" \
    "s_add_u32 s70, s70, s73\n\t" \
    "rr_W38_%=:\n\t" \
    "s_memtime s[56:57]\n\t" \
    "s_waitcnt lgkmcnt(0)\n\t" \
    "s_sub_u32 s69, s70, s56\n\t" \
    "s_cmp_gt_i32 s69, 24\n\t" \
    "s_cbranch_scc1 rr_W38_%=\n\t" \
    "rr_act3_%=:\n\t" \
    "s_add_u32 s69, s65, 0\n\t" \
    "s_and_b32 s69, s69, 7\n\t" \
    "s_lshl_b32 s69, s69, 18\n\t" \
    "s_add_u32 s69, s69, s62\n\t" \
    "s_add_u32 s46, s36, s69\n\t" \
    "s_addc_u32 s47, s37, 0\n\t" \
    "s_add_u32 s69, s65, 1\n\t" \
    "s_and_b32 s69, s69, 7\n\t" \
    "s_lshl_b32 s69, s69, 18\n\t" \
    "s_add_u32 s69, s69, s62\n\t" \
    "s_add_u32 s48, s36, s69\n\t" \
    "s_addc_u32 s49, s37, 0\n\t" \
    "s_and_b32 s69, s65, 7\n\t" \
    "s_lshl_b32 s69, s69, 18\n\t" \
    "s_add_u32 s69, s69, s63\n\t" \
    "s_add_u32 s52, s36, s69\n\t" \
    "s_addc_u32 s53, s37, 0\n\t" \
    "s_sub_u32 s69, s65, 2\n\t" \
    "s_cmp_ge_u32 s69, 5\n\t" \
    "s_cbranch_scc1 rr_nf39_%=\n\t" \
    "s_cmp_lg_u32 s64, 0\n\t" \
    "s_cbranch_scc1 rr_nf39_%=\n\t" \
    "v_add_u32 v169, 4096, v169\n\t" \
    "global_load_dword v176, v169, s[36:37] sc1\n\t" \
    "rr_nf39_%=:\n\t" \
    "s_waitcnt lgkmcnt(0)\n\t" \
    "s_mov_b32 s70, 116\n\t" \
    "rr_E40_%=:\n\t" \
    "s_memtime s[56:57]\n\t" \
    "s_memrealtime s[2:3]\n\t" \
    "s_nop 0\n\t" \
    "s_memrealtime s[4:5]\n\t" \
    "s_nop 0\n\t" \
    "s_memrealtime s[6:7]\n\t" \
    "s_nop 0\n\t" \
    "s_memrealtime s[8:9]\n\t" \
    "s_nop 0\n\t" \
    "s_memrealtime s[10:11]\n\t" \
    "s_nop 0\n\t" \
    "s_memrealtime s[12:13]\n\t" \
    "s_nop 0\n\t" \
    "s_memrealtime s[14:15]\n\t" \
    "s_nop 0\n\t" \
    "s_memrealtime s[16:17]\n\t" \
    "s_nop 0\n\t" \
    "s_memrealtime s[18:19]\n\t" \
    "s_nop 0\n\t" \
    "s_memrealtime s[20:21]\n\t" \
    "s_nop 0\n\t" \
    "s_memrealtime s[22:23]\n\t" \
    "s_nop 0\n\t" \
    "s_memrealtime s[24:25]\n\t" \
    "s_nop 0\n\t" \
    "s_memrealtime s[26:27]\n\t" \
    "s_nop 0\n\t" \
    "s_memrealtime s[28:29]\n\t" \
    "s_memtime s[54:55]\n\t" \
    "s_add_u32 s70, s70, 8\n\t" \
    "s_waitcnt lgkmcnt(0)\n\t" \
    "s_sub_u32 s69, s54, s56\n\t" \
    "s_cmp_gt_u32 s69, s70\n\t" \
    "s_cbranch_scc1 rr_E40_%=\n\t" \
    "s_add_u32 s2, s2, s4\n\t" \
    "s_add_u32 s6, s6, s8\n\t" \
    "s_add_u32 s10, s10, s12\n\t" \
    "s_add_u32 s14, s14, s16\n\t" \
    "s_add_u32 s18, s18, s20\n\t" \
    "s_add_u32 s22, s22, s24\n\t" \
    "s_add_u32 s2, s2, s6\n\t" \
    "s_add_u32 s10, s10, s14\n\t" \
    "s_add_u32 s18, s18, s22\n\t" \
    "s_add_u32 s2, s2, s26\n\t" \
    "s_add_u32 s10, s10, s18\n\t" \
    "s_add_u32 s2, s2, s10\n\t" \
    "s_mul_i32 s70, s28, 13\n\t" \
    "s_sub_u32 s70, s70, s2\n\t" \
    "s_min_u32 s70, s70, 52\n\t" \
    "s_mul_i32 s69, s69, s70\n\t" \
    "s_mul_hi_u32 s69, s69, 76695844\n\t" \
    "s_mov_b32 s75, s73\n\t" \
    "s_mov_b32 s76, s74\n\t" \
    "s_add_u32 s73, s56, s69\n\t" \
    "s_lshl_b32 s74, s28, 4\n\t" \
    "s_sub_u32 s69, s73, s75\n\t" \
    "s_sub_u32 s70, s74, s76\n\t" \
    "s_sub_u32 s71, s70, 1600\n\t" \
    "s_cmp_lt_u32 s71, 400000\n\t" \
    "s_cbranch_scc0 rr_K41_%=\n\t" \
    "v_cvt_f32_u32 v164, s69\n\t" \
    "v_cvt_f32_u32 v165, s70\n\t" \
    "v_rcp_f32 v166, v164\n\t" \
    "v_rcp_f32 v167, v165\n\t" \
    "v_mul_f32 v166, v166, v165\n\t" \
    "v_mul_f32 v167, v167, v164\n\t" \
    "v_mul_f32 v166, 0x4f800000, v166\n\t" \
    "v_mul_f32 v167, 0x4f000000, v167\n\t" \
    "v_cvt_u32_f32 v166, v166\n\t" \
    "v_cvt_u32_f32 v167, v167\n\t" \
    "v_readfirstlane_b32 s69, v166\n\t" \
    "v_readfirstlane_b32 s70, v167\n\t" \
    "s_nop 1\n\t" \
    "s_lshr_b32 s71, s65, 1\n\t" \
    "s_min_u32 s71, s71, 1\n\t" \
    "s_sub_u32 s69, s69, s78\n\t" \
    "s_ashr_i32 s69, s69, s71\n\t" \
    "s_add_u32 s78, s78, s69\n\t" \
    "s_sub_u32 s70, s70, s77\n\t" \
    "s_ashr_i32 s70, s70, s71\n\t" \
    "s_add_u32 s77, s77, s70\n\t" \
    "rr_K41_%=:\n\t" \
    "s_mov_b32 s91, s64\n\t" \
    "s_cmp_lg_u32 s64, 0\n\t" \
    "s_cselect_b32 s77, 0, s77\n\t" \
    "v_mov_b32 v160, 0\n\t" \
    "v_mov_b32 v161, 0\n\t" \
    "v_mov_b32 v162, 0\n\t" \
    "v_mov_b32 v163, 0\n\t" \
    ".p2align 6\n\t" \
    "s_nop 0\n\t" \
    "s_nop 0\n\t" \
    "s_sub_u32 s70, s80, s74\n\t" \
    "s_max_i32 s70, s70, 0\n\t" \
    "s_min_i32 s70, s70, 16000\n\t" \
    "s_lshl_b32 s70, s70, 1\n\t" \
    "s_mul_hi_u32 s70, s70, s77\n\t" \
    "s_add_u32 s70, s70, s73\n\t" \
    "s_getpc_b64 s[30:31]\n\t" \
    "rr_Wp44_%=:\n\t" \
    "s_add_u32 s30, s30, rr_We45_%=-rr_Wp44_%=\n\t" \
    "s_addc_u32 s31, s31, 0\n\t" \
    "s_sub_u32 s70, s70, 96\n\t" \
    "rr_W43_%=:\n\t" \
    "s_memtime s[56:57]\n\t" \
    "s_waitcnt lgkmcnt(0)\n\t" \
    "s_sub_u32 s69, s70, s56\n\t" \
    "s_cmp_gt_i32 s69, 80\n\t" \
    "s_cbranch_scc1 rr_W43_%=\n\t" \
    "s_max_i32 s69, s69, 0\n\t" \
    "s_and_b32 s69, s69, -4\n\t" \
    "s_sub_u32 s30, s30, s69\n\t" \
    "s_subb_u32 s31, s31, 0\n\t" \
    "s_setpc_b64 s[30:31]\n\t" \
    "s_nop 0\n\t" \
    "s_nop 0\n\t" \
    "s_nop 0\n\t" \
    "s_nop 0\n\t" \
    "s_nop 0\n\t" \
    "s_nop 0\n\t" \
    "s_nop 0\n\t" \
    "s_nop 0\n\t" \
    "s_nop 0\n\t" \
    "s_nop 0\n\t" \
    "s_nop 0\n\t" \
    "s_nop 0\n\t" \
    "s_nop 0\n\t" \
    "s_nop 0\n\t" \
    "s_nop 0\n\t" \
    "s_nop 0\n\t" \
    "s_nop 0\n\t" \
    "s_nop 0\n\t" \
    "s_nop 0\n\t" \
    "s_nop 0\n\t" \
    "rr_We45_%=:\n\t" \
    "global_load_dword v192, v7, s[52:53] offset:0 sc1\n\t" \
    "s_sub_u32 s71, s86, s74\n\t" \
    "s_lshl_b32 s71, s71, 2\n\t" \
    "s_lshr_b32 s33, s77, 1\n\t" \
    "s_sub_u32 s34, s73, 176\n\t" \
    "s_mov_b32 s96, 0\n\t" \
    "s_getpc_b64 s[30:31]\n\t" \
    "rr_rpc49_%=:\n\t" \
    "s_add_u32 s50, s30, rr_pgo52_%=-rr_rpc49_%=\n\t" \
    "s_addc_u32 s51, s31, 0\n\t" \
    "s_add_u32 s30, s30, rr_again5_%=-rr_rpc49_%=\n\t" \
    "s_addc_u32 s31, s31, 0\n\t" \
    "s_nop 7\n\t" \
    "s_nop 7\n\t" \
    "s_nop 7\n\t" \
    "s_nop 7\n\t" \
    "s_nop 3\n\t" \
    "global_load_dword v193, v7, s[52:53] offset:2048 sc1\n\t" \
    "s_nop 7\n\t" \
    "s_nop 7\n\t" \
    "s_nop 7\n\t" \
    "s_nop 7\n\t" \
    "s_nop 7\n\t" \
    "s_nop 7\n\t" \
    "global_load_dword v194, v189, s[52:53] offset:0 sc1\n\t" \
    "s_waitcnt vmcnt(2)\n\t" \
    "rr_hok46_%=:\n\t" \
    "s_memtime s[54:55]\n\t" \
    "v_max_u32_dpp v192, v192, v192 row_ror:1 row_mask:0xf bank_mask:0xf\n\t" \
    "s_nop 1\n\t" \
    "v_max_u32_dpp v192, v192, v192 row_ror:2 row_mask:0xf bank_mask:0xf\n\t" \
    "s_nop 1\n\t" \
    "v_max_u32_dpp v192, v192, v192 row_ror:4 row_mask:0xf bank_mask:0xf\n\t" \
    "s_nop 1\n\t" \
    "v_max_u32_dpp v192, v192, v192 row_ror:8 row_mask:0xf bank_mask:0xf\n\t" \
    "s_nop 0\n\t" \
    "v_readlane_b32 s69, v192, 0\n\t" \
    "v_readlane_b32 s70, v192, 16\n\t" \
    "s_waitcnt lgkmcnt(0)\n\t" \
    "rr_hmax55_%=:\n\t" \
    "s_sub_u32 s56, s34, s54\n\t" \
    "s_max_u32 s81, s69, s70\n\t" \
    "s_cmp_eq_u32 s81, -1\n\t" \
    "s_cbranch_scc1 rr_hlate47_%=\n\t" \
    "s_lshl2_add_u32 s70, s81, s71\n\t" \
    "s_mul_hi_i32 s70, s70, s33\n\t" \
    "s_add_u32 s69, s70, s56\n\t" \
    "s_max_i32 s69, s69, 0\n\t" \
    "s_cmp_gt_i32 s69, 80\n\t" \
    "s_cbranch_scc1 rr_rpoll50_%=\n\t" \
    "rr_rback51_%=:\n\t" \
    "s_and_b32 s69, s69, -4\n\t" \
    "s_sub_u32 s30, s30, s69\n\t" \
    "s_subb_u32 s31, s31, 0\n\t" \
    "s_setpc_b64 s[30:31]\n\t" \
    "rr_rpoll50_%=:\n\t" \
    "s_min_i32 s70, s70, 24000\n\t" \
    "s_add_u32 s70, s70, s73\n\t" \
    "s_sub_u32 s70, s70, 124\n\t" \
    "s_sub_u32 s70, s70, s96\n\t" \
    "rr_wl48_%=:\n\t" \
    "s_memtime s[56:57]\n\t" \
    "s_waitcnt lgkmcnt(0)\n\t" \
    "s_sub_u32 s69, s70, s56\n\t" \
    "s_cmp_gt_i32 s69, 80\n\t" \
    "s_cbranch_scc1 rr_wl48_%=\n\t" \
    "s_max_i32 s69, s69, 0\n\t" \
    "s_branch rr_rback51_%=\n\t" \
    "s_nop 0\n\t" \
    "s_nop 0\n\t" \
    "s_nop 0\n\t" \
    "s_nop 0\n\t" \
    "s_nop 0\n\t" \
    "s_nop 0\n\t" \
    "s_nop 0\n\t" \
    "s_nop 0\n\t" \
    "s_nop 0\n\t" \
    "s_nop 0\n\t" \
    "s_nop 0\n\t" \
    "s_nop 0\n\t" \
    "s_nop 0\n\t" \
    "s_nop 0\n\t" \
    "s_nop 0\n\t" \
    "s_nop 0\n\t" \
    "s_nop 0\n\t" \
    "s_nop 0\n\t" \
    "s_nop 0\n\t" \
    "s_nop 0\n\t" \
    "rr_again5_%=:\n\t" \
    "rr_pgo52_%=:\n\t" \
    "global_load_dwordx4 v[48:51], v10, s[46:47] sc1\n\t" \
    "global_load_dwordx4 v[52:55], v11, s[46:47] sc1\n\t" \
    "global_load_dwordx4 v[56:59], v12, s[46:47] sc1\n\t" \
    "global_load_dwordx4 v[60:63], v13, s[46:47] sc1\n\t" \
    "global_load_dwordx4 v[64:67], v14, s[46:47] sc1\n\t" \
    "global_load_dwordx4 v[68:71], v15, s[46:47] sc1\n\t" \
    "global_load_dwordx4 v[72:75], v16, s[46:47] sc1\n\t" \
    "global_load_dwordx4 v[76:79], v17, s[46:47] sc1\n\t" \
    "global_load_dwordx4 v[80:83], v18, s[46:47] sc1\n\t" \
    "global_load_dwordx4 v[84:87], v19, s[46:47] sc1\n\t" \
    "global_load_dwordx4 v[88:91], v20, s[46:47] sc1\n\t" \
    "global_load_dwordx4 v[92:95], v21, s[46:47] sc1\n\t" \
    "global_load_dwordx4 v[96:99], v22, s[46:47] sc1\n\t" \
    "global_load_dwordx4 v[100:103], v23, s[46:47] sc1\n\t" \
    "global_load_dwordx4 v[104:107], v24, s[46:47] sc1\n\t" \
    "global_load_dwordx4 v[108:111], v25, s[46:47] sc1\n\t" \
    "global_load_dwordx4 v[112:115], v42, s[46:47] sc1\n\t" \
    "global_load_dwordx4 v[116:119], v43, s[46:47] sc1\n\t" \
    "global_load_dwordx4 v[120:123], v44, s[46:47] sc1\n\t" \
    "global_load_dwordx4 v[124:127], v45, s[46:47] sc1\n\t" \
    "global_load_dwordx4 v[128:131], v3, s[40:41] offset:0 sc1\n\t" \
    "global_load_dwordx4 v[132:135], v3, s[40:41] offset:1024 sc1\n\t" \
    "global_load_dwordx4 v[136:139], v3, s[40:41] offset:2048 sc1\n\t" \
    "global_load_dwordx4 v[140:143], v3, s[40:41] offset:3072 sc1\n\t" \
    "global_load_dwordx4 v[144:147], v1, s[40:41] offset:0 sc1\n\t" \
    "global_load_dwordx4 v[148:151], v1, s[40:41] offset:1024 sc1\n\t" \
    "global_load_dwordx4 v[152:155], v1, s[40:41] offset:2048 sc1\n\t" \
    "global_load_dwordx4 v[156:159], v1, s[40:41] offset:3072 sc1\n\t" \
    "s_cmp_lg_u32 s91, 0\n\t" \
    "s_cbranch_scc1 rr_nob8_%=\n\t" \
    "rr_hbk56_%=:\n\t" \
    "v_mov_b32 v164, s81\n\t" \
    "ds_write_b32 v9, v164\n\t" \
    "s_waitcnt lgkmcnt(0)\n\t" \
    "s_barrier\n\t" \
    "rr_nob8_%=:\n\t" \
    "s_waitcnt vmcnt(27)\n\t" \
    "v_smfmac_f32_16x16x64_bf16 v[160:163], v[48:51], a[0:7], v4\n\t" \
    "s_waitcnt vmcnt(26)\n\t" \
    "v_smfmac_f32_16x16x64_bf16 v[160:163], v[52:55], a[8:15], v4\n\t" \
    "s_waitcnt vmcnt(25)\n\t" \
    "v_smfmac_f32_16x16x64_bf16 v[160:163], v[56:59], a[16:23], v4\n\t" \
    "s_waitcnt vmcnt(24)\n\t" \
    "v_smfmac_f32_16x16x64_bf16 v[160:163], v[60:63], a[24:31], v4\n\t" \
    "s_waitcnt vmcnt(23)\n\t" \
    "v_smfmac_f32_16x16x64_bf16 v[160:163], v[64:67], a[32:39], v4\n\t" \
    "s_waitcnt vmcnt(22)\n\t" \
    "v_smfmac_f32_16x16x64_bf16 v[160:163], v[68:71], a[40:47], v4\n\t" \
    "s_waitcnt vmcnt(21)\n\t" \
    "v_smfmac_f32_16x16x64_bf16 v[160:163], v[72:75], a[48:55], v4\n\t" \
    "s_waitcnt vmcnt(20)\n\t" \
    "v_smfmac_f32_16x16x64_bf16 v[160:163], v[76:79], a[56:63], v4\n\t" \
    "s_waitcnt vmcnt(19)\n\t" \
    "v_smfmac_f32_16x16x64_bf16 v[160:163], v[80:83], a[64:71], v4\n\t" \
    "s_waitcnt vmcnt(18)\n\t" \
    "v_smfmac_f32_16x16x64_bf16 v[160:163], v[84:87], a[72:79], v4\n\t" \
    "s_waitcnt vmcnt(17)\n\t" \
    "v_smfmac_f32_16x16x64_bf16 v[160:163], v[88:91], a[80:87], v4\n\t" \
    "s_waitcnt vmcnt(16)\n\t" \
    "v_smfmac_f32_16x16x64_bf16 v[160:163], v[92:95], a[88:95], v4\n\t" \
    "s_waitcnt vmcnt(15)\n\t" \
    "v_smfmac_f32_16x16x64_bf16 v[160:163], v[96:99], a[96:103], v4\n\t" \
    "s_waitcnt vmcnt(14)\n\t" \
    "v_smfmac_f32_16x16x64_bf16 v[160:163], v[100:103], a[104:111], v4\n\t" \
    "s_waitcnt vmcnt(13)\n\t" \
    "v_smfmac_f32_16x16x64_bf16 v[160:163], v[104:107], a[112:119], v4\n\t" \
    "s_waitcnt vmcnt(12)\n\t" \
    "v_smfmac_f32_16x16x64_bf16 v[160:163], v[108:111], a[120:127], v4\n\t" \
    "s_waitcnt vmcnt(6)\n\t" \
    "v_smfmac_f32_16x16x64_bf16 v[160:163], v[112:115], v[128:135], v4\n\t" \
    "s_waitcnt vmcnt(4)\n\t" \
    "v_smfmac_f32_16x16x64_bf16 v[160:163], v[116:119], v[136:143], v4\n\t" \
    "s_waitcnt vmcnt(2)\n\t" \
    "v_smfmac_f32_16x16x64_bf16 v[160:163], v[120:123], v[144:151], v4\n\t" \
    "s_waitcnt vmcnt(0)\n\t" \
    "v_smfmac_f32_16x16x64_bf16 v[160:163], v[124:127], v[152:159], v4\n\t" \
    "s_nop 7\n\t" \
    "v_pk_add_f32 v[160:161], v[160:161], v[162:163]\n\t" \
    "v_add_f32 v166, v160, v161\n\t" \
    "v_cvt_pk_bf16_f32 v164, v160, v161\n\t" \
    "s_nop 0\n\t" \
    "v_add_f32_dpp v166, v166, v166 quad_perm:[1,0,3,2] row_mask:0xf bank_mask:0xf\n\t" \
    "v_mov_b32_dpp v165, v164 quad_perm:[1,0,3,2] row_mask:0xf bank_mask:0xf\n\t" \
    "v_cmp_u_f32 vcc, v166, v166\n\t" \
    "v_perm_b32 v167, v165, v164, v5\n\t" \
    "s_nop 0\n\t" \
    "v_cndmask_b32 v164, v167, v172, vcc\n\t" \
    "rr_cont7_%=:\n\t" \
    "global_store_dword v180, v164, s[48:49] sc1\n\t" \
    "global_store_dword v181, v164, s[48:49] sc1\n\t" \
    "global_store_dword v182, v164, s[48:49] sc1\n\t" \
    "global_store_dword v2, v164, s[48:49] sc1\n\t" \
    "s_cmp_lg_u32 s64, 0\n\t" \
    "s_cbranch_scc1 rr_g214_%=\n\t" \
    "s_cbranch_vccnz rr_bad6_%=\n\t" \
    "s_cmp_eq_u32 s65, 62\n\t" \
    "s_cbranch_scc1 rr_end4_%=\n\t" \
    "s_add_u32 s65, s65, 2\n\t" \
    "v_xor_b32 v10, 0x4000, v10\n\t" \
    "v_xor_b32 v11, 0x4000, v11\n\t" \
    "v_xor_b32 v12, 0x4000, v12\n\t" \
    "v_xor_b32 v13, 0x4000, v13\n\t" \
    "v_xor_b32 v14, 0x4000, v14\n\t" \
    "v_xor_b32 v15, 0x4000, v15\n\t" \
    "v_xor_b32 v16, 0x4000, v16\n\t" \
    "v_xor_b32 v17, 0x4000, v17\n\t" \
    "v_xor_b32 v18, 0x4000, v18\n\t" \
    "v_xor_b32 v19, 0x4000, v19\n\t" \
    "v_xor_b32 v20, 0x4000, v20\n\t" \
    "v_xor_b32 v21, 0x4000, v21\n\t" \
    "v_xor_b32 v22, 0x4000, v22\n\t" \
    "v_xor_b32 v23, 0x4000, v23\n\t" \
    "v_xor_b32 v24, 0x4000, v24\n\t" \
    "v_xor_b32 v25, 0x4000, v25\n\t" \
    "v_xor_b32 v42, 0x4000, v42\n\t" \
    "v_xor_b32 v43, 0x4000, v43\n\t" \
    "v_xor_b32 v44, 0x4000, v44\n\t" \
    "v_xor_b32 v45, 0x4000, v45\n\t" \
    "v_xor_b32 v7, 0x4000, v7\n\t" \
    "v_xor_b32 v189, 0x4000, v189\n\t" \
    "v_xor_b32 v190, 0x4000, v190\n\t" \
    "v_xor_b32 v191, 0x4000, v191\n\t" \
    "s_branch rr_pf9_%=\n\t" \
    "rr_bad6_%=:\n\t" \
    "s_cmp_lg_u32 s64, 0\n\t" \
    "s_cbranch_scc1 rr_cont7_%=\n\t" \
    "s_waitcnt lgkmcnt(0)\n\t" \
    "v_max_u32 v165, v48, v49\n\t" \
    "v_max3_u32 v165, v165, v50, v51\n\t" \
    "v_max3_u32 v165, v165, v52, v53\n\t" \
    "v_max3_u32 v165, v165, v54, v55\n\t" \
    "v_max3_u32 v165, v165, v56, v57\n\t" \
    "v_max3_u32 v165, v165, v58, v59\n\t" \
    "v_max3_u32 v165, v165, v60, v61\n\t" \
    "v_max3_u32 v165, v165, v62, v63\n\t" \
    "v_max3_u32 v165, v165, v64, v65\n\t" \
    "v_max3_u32 v165, v165, v66, v67\n\t" \
    "v_max3_u32 v165, v165, v68, v69\n\t" \
    "v_max3_u32 v165, v165, v70, v71\n\t" \
    "v_max3_u32 v165, v165, v72, v73\n\t" \
    "v_max3_u32 v165, v165, v74, v75\n\t" \
    "v_max3_u32 v165, v165, v76, v77\n\t" \
    "v_max3_u32 v165, v165, v78, v79\n\t" \
    "v_max3_u32 v165, v165, v80, v81\n\t" \
    "v_max3_u32 v165, v165, v82, v83\n\t" \
    "v_max3_u32 v165, v165, v84, v85\n\t" \
    "v_max3_u32 v165, v165, v86, v87\n\t" \
    "v_max3_u32 v165, v165, v88, v89\n\t" \
    "v_max3_u32 v165, v165, v90, v91\n\t" \
    "v_max3_u32 v165, v165, v92, v93\n\t" \
    "v_max3_u32 v165, v165, v94, v95\n\t" \
    "v_max3_u32 v165, v165, v96, v97\n\t" \
    "v_max3_u32 v165, v165, v98, v99\n\t" \
    "v_max3_u32 v165, v165, v100, v101\n\t" \
    "v_max3_u32 v165, v165, v102, v103\n\t" \
    "v_max3_u32 v165, v165, v104, v105\n\t" \
    "v_max3_u32 v165, v165, v106, v107\n\t" \
    "v_max3_u32 v165, v165, v108, v109\n\t" \
    "v_max3_u32 v165, v165, v110, v111\n\t" \
    "v_max3_u32 v165, v165, v112, v113\n\t" \
    "v_max3_u32 v165, v165, v114, v115\n\t" \
    "v_max3_u32 v165, v165, v116, v117\n\t" \
    "v_max3_u32 v165, v165, v118, v119\n\t" \
    "v_max3_u32 v165, v165, v120, v121\n\t" \
    "v_max3_u32 v165, v165, v122, v123\n\t" \
    "v_max3_u32 v165, v165, v124, v125\n\t" \
    "v_max3_u32 v165, v165, v126, v127\n\t" \
    "v_max_u32 v166, v128, v129\n\t" \
    "v_max3_u32 v166, v166, v130, v131\n\t" \
    "v_max3_u32 v166, v166, v132, v133\n\t" \
    "v_max3_u32 v166, v166, v134, v135\n\t" \
    "v_max3_u32 v166, v166, v136, v137\n\t" \
    "v_max3_u32 v166, v166, v138, v139\n\t" \
    "v_max3_u32 v166, v166, v140, v141\n\t" \
    "v_max3_u32 v166, v166, v142, v143\n\t" \
    "v_max3_u32 v166, v166, v144, v145\n\t" \
    "v_max3_u32 v166, v166, v146, v147\n\t" \
    "v_max3_u32 v166, v166, v148, v149\n\t" \
    "v_max3_u32 v166, v166, v150, v151\n\t" \
    "v_max3_u32 v166, v166, v152, v153\n\t" \
    "v_max3_u32 v166, v166, v154, v155\n\t" \
    "v_max3_u32 v166, v166, v156, v157\n\t" \
    "v_max3_u32 v166, v166, v158, v159\n\t" \
    "v_max_u32 v165, v165, v166\n\t" \
    "v_mov_b32 v164, v167\n\t" \
    "v_cmp_eq_u32 vcc, -1, v165\n\t" \
    "s_cbranch_vccz rr_cont7_%=\n\t" \
    "s_mov_b64 vcc, 0\n\t" \
    "s_mov_b32 s91, 1\n\t" \
    "s_mov_b32 s96, 0\n\t" \
    "v_mov_b32 v160, 0\n\t" \
    "v_mov_b32 v161, 0\n\t" \
    "v_mov_b32 v162, 0\n\t" \
    "v_mov_b32 v163, 0\n\t" \
    "s_cmp_eq_u32 s68, 0\n\t" \
    "s_cbranch_scc1 rr_F59_%=\n\t" \
    "s_sub_u32 s68, s68, 1\n\t" \
    "s_branch rr_again5_%=\n\t" \
    "rr_F59_%=:\n\t" \
    "v_mov_b32 v166, 5767424\n\t" \
    "v_mov_b32 v167, 3\n\t" \
    "global_store_dword v166, v167, s[36:37] sc1\n\t" \
    "s_waitcnt vmcnt(0)\n\t" \
    "s_branch rr_cont7_%=\n\t" \
    "rr_hlate47_%=:\n\t" \
    "s_cmp_lg_u32 s64, 0\n\t" \
    "s_cbranch_scc1 rr_hpd58_%=\n\t" \
    "global_load_dword v195, v189, s[52:53] offset:2048 sc1\n\t" \
    "s_waitcnt vmcnt(2)\n\t" \
    "v_mov_b32 v192, v193\n\t" \
    "v_cmp_eq_u32 vcc, -1, v193\n\t" \
    "s_cbranch_vccz rr_hok46_%=\n\t" \
    "global_load_dword v196, v190, s[52:53] offset:0 sc1\n\t" \
    "s_waitcnt vmcnt(2)\n\t" \
    "v_mov_b32 v192, v194\n\t" \
    "v_cmp_eq_u32 vcc, -1, v194\n\t" \
    "s_cbranch_vccz rr_hok46_%=\n\t" \
    "global_load_dword v197, v190, s[52:53] offset:2048 sc1\n\t" \
    "s_waitcnt vmcnt(2)\n\t" \
    "v_mov_b32 v192, v195\n\t" \
    "v_cmp_eq_u32 vcc, -1, v195\n\t" \
    "s_cbranch_vccz rr_hok46_%=\n\t" \
    "s_waitcnt vmcnt(1)\n\t" \
    "v_mov_b32 v192, v196\n\t" \
    "v_cmp_eq_u32 vcc, -1, v196\n\t" \
    "s_cbranch_vccz rr_hok46_%=\n\t" \
    "s_waitcnt vmcnt(0)\n\t" \
    "v_mov_b32 v192, v197\n\t" \
    "v_cmp_eq_u32 vcc, -1, v197\n\t" \
    "s_cbranch_vccz rr_hok46_%=\n\t" \
    "rr_hp57_%=:\n\t" \
    "global_load_dword v192, v191, s[52:53] offset:0 sc1\n\t" \
    "s_waitcnt vmcnt(0)\n\t" \
    "v_cmp_eq_u32 vcc, -1, v192\n\t" \
    "s_cbranch_vccz rr_hpd58_%=\n\t" \
    "s_cmp_eq_u32 s68, 0\n\t" \
    "s_cbranch_scc1 rr_F60_%=\n\t" \
    "s_sub_u32 s68, s68, 1\n\t" \
    "s_sleep 1\n\t" \
    "s_branch rr_hp57_%=\n\t" \
    "rr_F60_%=:\n\t" \
    "v_mov_b32 v166, 5767424\n\t" \
    "v_mov_b32 v167, 7\n\t" \
    "global_store_dword v166, v167, s[36:37] sc1\n\t" \
    "s_waitcnt vmcnt(0)\n\t" \
    "s_branch rr_hpd58_%=\n\t" \
    "rr_hpd58_%=:\n\t" \
    "v_and_b32 v192, -2, v192\n\t" \
    "s_waitcnt lgkmcnt(0)\n\t" \
    "s_mov_b32 s70, 116\n\t" \
    "rr_E61_%=:\n\t" \
    "s_memtime s[56:57]\n\t" \
    "s_memrealtime s[2:3]\n\t" \
    "s_nop 0\n\t" \
    "s_memrealtime s[4:5]\n\t" \
    "s_nop 0\n\t" \
    "s_memrealtime s[6:7]\n\t" \
    "s_nop 0\n\t" \
    "s_memrealtime s[8:9]\n\t" \
    "s_nop 0\n\t" \
    "s_memrealtime s[10:11]\n\t" \
    "s_nop 0\n\t" \
    "s_memrealtime s[12:13]\n\t" \
    "s_nop 0\n\t" \
    "s_memrealtime s[14:15]\n\t" \
    "s_nop 0\n\t" \
    "s_memrealtime s[16:17]\n\t" \
    "s_nop 0\n\t" \
    "s_memrealtime s[18:19]\n\t" \
    "s_nop 0\n\t" \
    "s_memrealtime s[20:21]\n\t" \
    "s_nop 0\n\t" \
    "s_memrealtime s[22:23]\n\t" \
    "s_nop 0\n\t" \
    "s_memrealtime s[24:25]\n\t" \
    "s_nop 0\n\t" \
    "s_memrealtime s[26:27]\n\t" \
    "s_nop 0\n\t" \
    "s_memrealtime s[28:29]\n\t" \
    "s_memtime s[54:55]\n\t" \
    "s_add_u32 s70, s70, 8\n\t" \
    "s_waitcnt lgkmcnt(0)\n\t" \
    "s_sub_u32 s69, s54, s56\n\t" \
    "s_cmp_gt_u32 s69, s70\n\t" \
    "s_cbranch_scc1 rr_E61_%=\n\t" \
    "s_add_u32 s2, s2, s4\n\t" \
    "s_add_u32 s6, s6, s8\n\t" \
    "s_add_u32 s10, s10, s12\n\t" \
    "s_add_u32 s14, s14, s16\n\t" \
    "s_add_u32 s18, s18, s20\n\t" \
    "s_add_u32 s22, s22, s24\n\t" \
    "s_add_u32 s2, s2, s6\n\t" \
    "s_add_u32 s10, s10, s14\n\t" \
    "s_add_u32 s18, s18, s22\n\t" \
    "s_add_u32 s2, s2, s26\n\t" \
    "s_add_u32 s10, s10, s18\n\t" \
    "s_add_u32 s2, s2, s10\n\t" \
    "s_mul_i32 s70, s28, 13\n\t" \
    "s_sub_u32 s70, s70, s2\n\t" \
    "s_min_u32 s70, s70, 52\n\t" \
    "s_mul_i32 s69, s69, s70\n\t" \
    "s_mul_hi_u32 s69, s69, 76695844\n\t" \
    "s_mov_b32 s75, s73\n\t" \
    "s_mov_b32 s76, s74\n\t" \
    "s_add_u32 s73, s56, s69\n\t" \
    "s_lshl_b32 s74, s28, 4\n\t" \
    "s_sub_u32 s71, s86, s74\n\t" \
    "s_lshl_b32 s71, s71, 2\n\t" \
    "s_lshr_b32 s33, s77, 1\n\t" \
    "s_sub_u32 s34, s73, 176\n\t" \
    "s_branch rr_hok46_%=\n\t" \
    "rr_end4_%=:\n\t" \
    "s_endpgm\n\t" \
    "rr_last_%=:\n\t" \
    ""
#define ASM_H \
    "s_mov_b64 s[36:37], %0\n\t" \
    "s_mov_b64 s[42:43], %1\n\t" \
    "s_mov_b64 s[44:45], %2\n\t" \
    "s_mov_b32 s66, %3\n\t" \
    "s_mov_b32 s67, %4\n\t" \
    "s_mov_b64 s[50:51], %5\n\t" \
    "s_mov_b32 s82, %7\n\t" \
    "s_load_dwordx8 s[84:91], %6, 0\n\t" \
    "v_mbcnt_lo_u32_b32 v223, -1, 0\n\t" \
    "v_mbcnt_hi_u32_b32 v223, -1, v223\n\t" \
    "s_getpc_b64 s[46:47]\n\t" \
    "hh_pc_%=:\n\t" \
    "s_lshl_b32 s69, s66, 13\n\t" \
    "v_lshl_add_u32 v222, v223, 7, s69\n\t" \
    "v_min_u32 v222, hh_last_%=-hh_pc_%=-4, v222\n\t" \
    "global_load_dword v222, v222, s[46:47]\n\t" \
    "v_lshlrev_b32 v223, 2, v223\n\t" \
    "v_add_u32 v223, s82, v223\n\t" \
    "ds_read_b32 v0, v223 offset:0\n\t" \
    "ds_read_b32 v1, v223 offset:256\n\t" \
    "ds_read_b32 v2, v223 offset:512\n\t" \
    "ds_read_b32 v3, v223 offset:768\n\t" \
    "ds_read_b32 v4, v223 offset:1024\n\t" \
    "ds_read_b32 v5, v223 offset:1280\n\t" \
    "ds_read_b32 v6, v223 offset:1536\n\t" \
    "ds_read_b32 v7, v223 offset:1792\n\t" \
    "ds_read_b32 v8, v223 offset:2048\n\t" \
    "ds_read_b32 v9, v223 offset:2304\n\t" \
    "ds_read_b32 v10, v223 offset:2560\n\t" \
    "ds_read_b32 v11, v223 offset:2816\n\t" \
    "s_waitcnt lgkmcnt(0)\n\t" \
    "ds_read_b32 v12, v223 offset:3072\n\t" \
    "ds_read_b32 v13, v223 offset:3328\n\t" \
    "ds_read_b32 v14, v223 offset:3584\n\t" \
    "ds_read_b32 v15, v223 offset:3840\n\t" \
    "ds_read_b32 v16, v223 offset:4096\n\t" \
    "ds_read_b32 v17, v223 offset:4352\n\t" \
    "s_waitcnt lgkmcnt(0)\n\t" \
    "s_mov_b32 s68, 262144\n\t" \
    "s_mov_b32 s72, 0\n\t" \
    "s_mov_b32 s83, -1\n\t" \
    "s_mov_b32 s65, 0\n\t" \
    "s_mov_b32 s77, 3019898880\n\t" \
    "s_mov_b32 s78, 3054198966\n\t" \
    "s_waitcnt lgkmcnt(0)\n\t" \
    "s_mov_b32 s70, 116\n\t" \
    "hh_E1_%=:\n\t" \
    "s_memtime s[56:57]\n\t" \
    "s_memrealtime s[2:3]\n\t" \
    "s_nop 0\n\t" \
    "s_memrealtime s[4:5]\n\t" \
    "s_nop 0\n\t" \
    "s_memrealtime s[6:7]\n\t" \
    "s_nop 0\n\t" \
    "s_memrealtime s[8:9]\n\t" \
    "s_nop 0\n\t" \
    "s_memrealtime s[10:11]\n\t" \
    "s_nop 0\n\t" \
    "s_memrealtime s[12:13]\n\t" \
    "s_nop 0\n\t" \
    "s_memrealtime s[14:15]\n\t" \
    "s_nop 0\n\t" \
    "s_memrealtime s[16:17]\n\t" \
    "s_nop 0\n\t" \
    "s_memrealtime s[18:19]\n\t" \
    "s_nop 0\n\t" \
    "s_memrealtime s[20:21]\n\t" \
    "s_nop 0\n\t" \
    "s_memrealtime s[22:23]\n\t" \
    "s_nop 0\n\t" \
    "s_memrealtime s[24:25]\n\t" \
    "s_nop 0\n\t" \
    "s_memrealtime s[26:27]\n\t" \
    "s_nop 0\n\t" \
    "s_memrealtime s[28:29]\n\t" \
    "s_memtime s[54:55]\n\t" \
    "s_add_u32 s70, s70, 8\n\t" \
    "s_waitcnt lgkmcnt(0)\n\t" \
    "s_sub_u32 s69, s54, s56\n\t" \
    "s_cmp_gt_u32 s69, s70\n\t" \
    "s_cbranch_scc1 hh_E1_%=\n\t" \
    "s_add_u32 s2, s2, s4\n\t" \
    "s_add_u32 s6, s6, s8\n\t" \
    "s_add_u32 s10, s10, s12\n\t" \
    "s_add_u32 s14, s14, s16\n\t" \
    "s_add_u32 s18, s18, s20\n\t" \
    "s_add_u32 s22, s22, s24\n\t" \
    "s_add_u32 s2, s2, s6\n\t" \
    "s_add_u32 s10, s10, s14\n\t" \
    "s_add_u32 s18, s18, s22\n\t" \
    "s_add_u32 s2, s2, s26\n\t" \
    "s_add_u32 s10, s10, s18\n\t" \
    "s_add_u32 s2, s2, s10\n\t" \
    "s_mul_i32 s70, s28, 13\n\t" \
    "s_sub_u32 s70, s70, s2\n\t" \
    "s_min_u32 s70, s70, 52\n\t" \
    "s_mul_i32 s69, s69, s70\n\t" \
    "s_mul_hi_u32 s69, s69, 76695844\n\t" \
    "s_mov_b32 s75, s73\n\t" \
    "s_mov_b32 s76, s74\n\t" \
    "s_add_u32 s73, s56, s69\n\t" \
    "s_lshl_b32 s74, s28, 4\n\t" \
    "v_mbcnt_lo_u32_b32 v30, -1, 0\n\t" \
    "v_mbcnt_hi_u32_b32 v30, -1, v30\n\t" \
    "v_lshlrev_b32 v30, 6, v30\n\t" \
    "v_add_u32 v30, 6225920, v30\n\t" \
    "v_mov_b32 v31, 6234112\n\t" \
    "global_load_dword v31, v31, s[36:37] sc1\n\t" \
    "global_load_dwordx2 v[32:33], v30, s[36:37] sc1\n\t" \
    "global_load_dword v34, v30, s[36:37] offset:2048 sc1\n\t" \
    "s_waitcnt vmcnt(0)\n\t" \
    "hh_C2_%=:\n\t" \
    "v_cmp_ne_u32 s[54:55], v32, v34\n\t" \
    "v_sub_u32 v32, v32, v31\n\t" \
    "v_cmp_ne_u32 vcc, 1, v32\n\t" \
    "s_nop 1\n\t" \
    "s_or_b64 vcc, vcc, s[54:55]\n\t" \
    "s_branch hh_Cok3_%=\n\t" \
    "s_cmp_eq_u32 s68, 0\n\t" \
    "s_cbranch_scc1 hh_F5_%=\n\t" \
    "s_sub_u32 s68, s68, 1\n\t" \
    "s_sleep 6\n\t" \
    "s_branch hh_Cag4_%=\n\t" \
    "hh_F5_%=:\n\t" \
    "v_mov_b32 v34, 5767424\n\t" \
    "v_mov_b32 v35, 4\n\t" \
    "global_store_dword v34, v35, s[36:37] sc1\n\t" \
    "s_waitcnt vmcnt(0)\n\t" \
    "s_branch hh_Cok3_%=\n\t" \
    "hh_Cag4_%=:\n\t" \
    "global_load_dwordx2 v[32:33], v30, s[36:37] sc1\n\t" \
    "global_load_dword v34, v30, s[36:37] offset:2048 sc1\n\t" \
    "s_waitcnt vmcnt(0)\n\t" \
    "s_branch hh_C2_%=\n\t" \
    "hh_Cok3_%=:\n\t" \
    "v_readlane_b32 s69, v33, 2\n\t" \
    "v_readlane_b32 s70, v33, 3\n\t" \
    "s_nop 1\n\t" \
    "s_cmp_lt_u32 s70, s69\n\t" \
    "s_cselect_b32 s69, 0, 8192\n\t" \
    "s_add_u32 s100, s36, s69\n\t" \
    "s_addc_u32 s101, s37, 0\n\t" \
    "v_mov_b32_dpp v30, v33 quad_perm:[1,0,3,2] row_mask:0xf bank_mask:0xf\n\t" \
    "v_cmp_lt_u32 s[44:45], v30, v33\n\t" \
    "v_min_u32 v30, v30, v33\n\t" \
    "v_and_b32 v31, 1, v30\n\t" \
    "v_cmp_ne_u32 vcc, 0, v31\n\t" \
    "s_nop 1\n\t" \
    "s_and_b32 s44, s44, 0x55555555\n\t" \
    "s_and_b32 s69, vcc_lo, 0xaaaaaaaa\n\t" \
    "s_or_b32 s44, s44, s69\n\t" \
    "s_and_b32 s45, s45, 0x55555555\n\t" \
    "s_and_b32 s69, vcc_hi, 0xaaaaaaaa\n\t" \
    "s_or_b32 s45, s45, s69\n\t" \
    "s_bitcmp1_b32 s44, 0\n\t" \
    "s_cselect_b32 s62, 8192, 0\n\t" \
    "s_bitcmp1_b32 s44, 2\n\t" \
    "s_cselect_b32 s63, 8192, 0\n\t" \
    "s_or_b32 s63, s63, 0x200000\n\t" \
    "v_cmp_lt_u32 vcc, 1320, v30\n\t" \
    "s_nop 1\n\t" \
    "s_bcnt1_i32_b64 s69, vcc\n\t" \
    "s_cmp_gt_u32 s69, 16\n\t" \
    "s_cselect_b32 s69, 1, 0\n\t" \
    "s_mul_i32 s70, s69, 160\n\t" \
    "s_add_u32 s86, s86, s70\n\t" \
    "s_mul_i32 s70, s69, 536\n\t" \
    "s_add_u32 s87, s87, s70\n\t" \
    "s_mul_i32 s70, s69, 256\n\t" \
    "s_add_u32 s88, s88, s70\n\t" \
    "s_bitcmp1_b32 s44, 3\n\t" \
    "s_cselect_b32 s69, 16384, 0\n\t" \
    "v_xor_b32 v1, s69, v1\n\t" \
    "s_waitcnt lgkmcnt(0)\n\t" \
    "s_mov_b32 s70, 116\n\t" \
    "hh_E6_%=:\n\t" \
    "s_memtime s[56:57]\n\t" \
    "s_memrealtime s[2:3]\n\t" \
    "s_nop 0\n\t" \
    "s_memrealtime s[4:5]\n\t" \
    "s_nop 0\n\t" \
    "s_memrealtime s[6:7]\n\t" \
    "s_nop 0\n\t" \
    "s_memrealtime s[8:9]\n\t" \
    "s_nop 0\n\t" \
    "s_memrealtime s[10:11]\n\t" \
    "s_nop 0\n\t" \
    "s_memrealtime s[12:13]\n\t" \
    "s_nop 0\n\t" \
    "s_memrealtime s[14:15]\n\t" \
    "s_nop 0\n\t" \
    "s_memrealtime s[16:17]\n\t" \
    "s_nop 0\n\t" \
    "s_memrealtime s[18:19]\n\t" \
    "s_nop 0\n\t" \
    "s_memrealtime s[20:21]\n\t" \
    "s_nop 0\n\t" \
    "s_memrealtime s[22:23]\n\t" \
    "s_nop 0\n\t" \
    "s_memrealtime s[24:25]\n\t" \
    "s_nop 0\n\t" \
    "s_memrealtime s[26:27]\n\t" \
    "s_nop 0\n\t" \
    "s_memrealtime s[28:29]\n\t" \
    "s_memtime s[54:55]\n\t" \
    "s_add_u32 s70, s70, 8\n\t" \
    "s_waitcnt lgkmcnt(0)\n\t" \
    "s_sub_u32 s69, s54, s56\n\t" \
    "s_cmp_gt_u32 s69, s70\n\t" \
    "s_cbranch_scc1 hh_E6_%=\n\t" \
    "s_add_u32 s2, s2, s4\n\t" \
    "s_add_u32 s6, s6, s8\n\t" \
    "s_add_u32 s10, s10, s12\n\t" \
    "s_add_u32 s14, s14, s16\n\t" \
    "s_add_u32 s18, s18, s20\n\t" \
    "s_add_u32 s22, s22, s24\n\t" \
    "s_add_u32 s2, s2, s6\n\t" \
    "s_add_u32 s10, s10, s14\n\t" \
    "s_add_u32 s18, s18, s22\n\t" \
    "s_add_u32 s2, s2, s26\n\t" \
    "s_add_u32 s10, s10, s18\n\t" \
    "s_add_u32 s2, s2, s10\n\t" \
    "s_mul_i32 s70, s28, 13\n\t" \
    "s_sub_u32 s70, s70, s2\n\t" \
    "s_min_u32 s70, s70, 52\n\t" \
    "s_mul_i32 s69, s69, s70\n\t" \
    "s_mul_hi_u32 s69, s69, 76695844\n\t" \
    "s_mov_b32 s75, s73\n\t" \
    "s_mov_b32 s76, s74\n\t" \
    "s_add_u32 s73, s56, s69\n\t" \
    "s_lshl_b32 s74, s28, 4\n\t" \
    "s_sub_u32 s69, s73, s75\n\t" \
    "s_sub_u32 s70, s74, s76\n\t" \
    "s_sub_u32 s71, s70, 1600\n\t" \
    "s_cmp_lt_u32 s71, 400000\n\t" \
    "s_cbranch_scc0 hh_K7_%=\n\t" \
    "v_cvt_f32_u32 v30, s69\n\t" \
    "v_cvt_f32_u32 v31, s70\n\t" \
    "v_rcp_f32 v32, v30\n\t" \
    "v_rcp_f32 v33, v31\n\t" \
    "v_mul_f32 v32, v32, v31\n\t" \
    "v_mul_f32 v33, v33, v30\n\t" \
    "v_mul_f32 v32, 0x4f800000, v32\n\t" \
    "v_mul_f32 v33, 0x4f000000, v33\n\t" \
    "v_cvt_u32_f32 v32, v32\n\t" \
    "v_cvt_u32_f32 v33, v33\n\t" \
    "v_readfirstlane_b32 s69, v32\n\t" \
    "v_readfirstlane_b32 s70, v33\n\t" \
    "s_nop 1\n\t" \
    "s_mov_b32 s78, s69\n\t" \
    "s_mov_b32 s77, s70\n\t" \
    "hh_K7_%=:\n\t" \
    "s_sub_u32 s80, s74, 32000\n\t" \
    "v_mov_b32 v38, -1\n\t" \
    "v_mov_b32 v39, -1\n\t" \
    "s_sub_u32 s69, s100, s36\n\t" \
    "v_cmp_eq_u32 vcc, 0, v3\n\t" \
    "v_mov_b32 v30, s69\n\t" \
    "s_nop 0\n\t" \
    "v_cndmask_b32 v30, 0, v30, vcc\n\t" \
    "v_add_u32 v2, v2, v30\n\t" \
    "s_mov_b32 m0, 155648\n\t" \
    "s_mul_i32 s96, s67, 16\n\t" \
    "s_add_u32 s96, s96, 64\n\t" \
    "hh_loop8_%=:\n\t" \
    "s_and_b32 s69, s65, 7\n\t" \
    "s_lshl_b32 s69, s69, 18\n\t" \
    "s_add_u32 s69, s69, s63\n\t" \
    "s_add_u32 s52, s36, s69\n\t" \
    "s_addc_u32 s53, s37, 0\n\t" \
    "s_add_u32 s71, s80, 608\n\t" \
    "s_sub_u32 s70, s71, s74\n\t" \
    "s_max_i32 s70, s70, 0\n\t" \
    "s_min_i32 s70, s70, 16000\n\t" \
    "s_lshl_b32 s70, s70, 1\n\t" \
    "s_mul_hi_u32 s70, s70, s77\n\t" \
    "s_add_u32 s70, s70, s73\n\t" \
    "hh_W11_%=:\n\t" \
    "s_memtime s[56:57]\n\t" \
    "s_waitcnt lgkmcnt(0)\n\t" \
    "s_sub_u32 s69, s70, s56\n\t" \
    "s_cmp_gt_i32 s69, 24\n\t" \
    "s_cbranch_scc1 hh_W11_%=\n\t" \
    "global_load_dword v30, v1, s[52:53] sc1\n\t" \
    "s_cmp_lt_u32 s65, 7\n\t" \
    "s_cselect_b32 s69, -2, 63\n\t" \
    "v_mov_b32 v38, s69\n\t" \
    "s_sub_u32 s69, s65, 2\n\t" \
    "s_cmp_ge_u32 s69, 5\n\t" \
    "s_cbranch_scc1 hh_ne12_%=\n\t" \
    "v_mad_u32_u24 v39, s65, v3, v2\n\t" \
    "global_load_dword v38, v39, s[36:37] sc1\n\t" \
    "hh_ne12_%=:\n\t" \
    "s_waitcnt lgkmcnt(0)\n\t" \
    "s_mov_b32 s70, 116\n\t" \
    "hh_E16_%=:\n\t" \
    "s_memtime s[56:57]\n\t" \
    "s_memrealtime s[2:3]\n\t" \
    "s_nop 0\n\t" \
    "s_memrealtime s[4:5]\n\t" \
    "s_nop 0\n\t" \
    "s_memrealtime s[6:7]\n\t" \
    "s_nop 0\n\t" \
    "s_memrealtime s[8:9]\n\t" \
    "s_nop 0\n\t" \
    "s_memrealtime s[10:11]\n\t" \
    "s_nop 0\n\t" \
    "s_memrealtime s[12:13]\n\t" \
    "s_nop 0\n\t" \
    "s_memrealtime s[14:15]\n\t" \
    "s_nop 0\n\t" \
    "s_memrealtime s[16:17]\n\t" \
    "s_nop 0\n\t" \
    "s_memrealtime s[18:19]\n\t" \
    "s_nop 0\n\t" \
    "s_memrealtime s[20:21]\n\t" \
    "s_nop 0\n\t" \
    "s_memrealtime s[22:23]\n\t" \
    "s_nop 0\n\t" \
    "s_memrealtime s[24:25]\n\t" \
    "s_nop 0\n\t" \
    "s_memrealtime s[26:27]\n\t" \
    "s_nop 0\n\t" \
    "s_memrealtime s[28:29]\n\t" \
    "s_memtime s[54:55]\n\t" \
    "s_add_u32 s70, s70, 8\n\t" \
    "s_waitcnt lgkmcnt(0)\n\t" \
    "s_sub_u32 s69, s54, s56\n\t" \
    "s_cmp_gt_u32 s69, s70\n\t" \
    "s_cbranch_scc1 hh_E16_%=\n\t" \
    "s_add_u32 s2, s2, s4\n\t" \
    "s_add_u32 s6, s6, s8\n\t" \
    "s_add_u32 s10, s10, s12\n\t" \
    "s_add_u32 s14, s14, s16\n\t" \
    "s_add_u32 s18, s18, s20\n\t" \
    "s_add_u32 s22, s22, s24\n\t" \
    "s_add_u32 s2, s2, s6\n\t" \
    "s_add_u32 s10, s10, s14\n\t" \
    "s_add_u32 s18, s18, s22\n\t" \
    "s_add_u32 s2, s2, s26\n\t" \
    "s_add_u32 s10, s10, s18\n\t" \
    "s_add_u32 s2, s2, s10\n\t" \
    "s_mul_i32 s70, s28, 13\n\t" \
    "s_sub_u32 s70, s70, s2\n\t" \
    "s_min_u32 s70, s70, 52\n\t" \
    "s_mul_i32 s69, s69, s70\n\t" \
    "s_mul_hi_u32 s69, s69, 76695844\n\t" \
    "s_mov_b32 s75, s73\n\t" \
    "s_mov_b32 s76, s74\n\t" \
    "s_add_u32 s73, s56, s69\n\t" \
    "s_lshl_b32 s74, s28, 4\n\t" \
    "s_sub_u32 s69, s73, s75\n\t" \
    "s_sub_u32 s70, s74, s76\n\t" \
    "s_sub_u32 s71, s70, 1600\n\t" \
    "s_cmp_lt_u32 s71, 400000\n\t" \
    "s_cbranch_scc0 hh_K17_%=\n\t" \
    "v_cvt_f32_u32 v34, s69\n\t" \
    "v_cvt_f32_u32 v35, s70\n\t" \
    "v_rcp_f32 v36, v34\n\t" \
    "v_rcp_f32 v37, v35\n\t" \
    "v_mul_f32 v36, v36, v35\n\t" \
    "v_mul_f32 v37, v37, v34\n\t" \
    "v_mul_f32 v36, 0x4f800000, v36\n\t" \
    "v_mul_f32 v37, 0x4f000000, v37\n\t" \
    "v_cvt_u32_f32 v36, v36\n\t" \
    "v_cvt_u32_f32 v37, v37\n\t" \
    "v_readfirstlane_b32 s69, v36\n\t" \
    "v_readfirstlane_b32 s70, v37\n\t" \
    "s_nop 1\n\t" \
    "s_sub_u32 s69, s69, s78\n\t" \
    "s_ashr_i32 s69, s69, 1\n\t" \
    "s_add_u32 s78, s78, s69\n\t" \
    "s_sub_u32 s70, s70, s77\n\t" \
    "s_ashr_i32 s70, s70, 1\n\t" \
    "s_add_u32 s77, s77, s70\n\t" \
    "hh_K17_%=:\n\t" \
    "s_waitcnt vmcnt(0)\n\t" \
    "v_cmp_eq_u32 vcc, -1, v30\n\t" \
    "s_cbranch_vccz hh_Hok14_%=\n\t" \
    "hh_H13_%=:\n\t" \
    "global_load_dword v30, v1, s[52:53] sc1\n\t" \
    "s_waitcnt vmcnt(0)\n\t" \
    "v_cmp_eq_u32 vcc, -1, v30\n\t" \
    "s_cbranch_vccz hh_Hc15_%=\n\t" \
    "s_cmp_eq_u32 s68, 0\n\t" \
    "s_cbranch_scc1 hh_F18_%=\n\t" \
    "s_sub_u32 s68, s68, 1\n\t" \
    "s_sleep 2\n\t" \
    "s_branch hh_H13_%=\n\t" \
    "hh_F18_%=:\n\t" \
    "v_mov_b32 v31, 5767424\n\t" \
    "v_mov_b32 v32, 14\n\t" \
    "global_store_dword v31, v32, s[36:37] sc1\n\t" \
    "s_waitcnt vmcnt(0)\n\t" \
    "s_branch hh_Hc15_%=\n\t" \
    "hh_Hc15_%=:\n\t" \
    "s_waitcnt lgkmcnt(0)\n\t" \
    "s_mov_b32 s70, 116\n\t" \
    "hh_E19_%=:\n\t" \
    "s_memtime s[56:57]\n\t" \
    "s_memrealtime s[2:3]\n\t" \
    "s_nop 0\n\t" \
    "s_memrealtime s[4:5]\n\t" \
    "s_nop 0\n\t" \
    "s_memrealtime s[6:7]\n\t" \
    "s_nop 0\n\t" \
    "s_memrealtime s[8:9]\n\t" \
    "s_nop 0\n\t" \
    "s_memrealtime s[10:11]\n\t" \
    "s_nop 0\n\t" \
    "s_memrealtime s[12:13]\n\t" \
    "s_nop 0\n\t" \
    "s_memrealtime s[14:15]\n\t" \
    "s_nop 0\n\t" \
    "s_memrealtime s[16:17]\n\t" \
    "s_nop 0\n\t" \
    "s_memrealtime s[18:19]\n\t" \
    "s_nop 0\n\t" \
    "s_memrealtime s[20:21]\n\t" \
    "s_nop 0\n\t" \
    "s_memrealtime s[22:23]\n\t" \
    "s_nop 0\n\t" \
    "s_memrealtime s[24:25]\n\t" \
    "s_nop 0\n\t" \
    "s_memrealtime s[26:27]\n\t" \
    "s_nop 0\n\t" \
    "s_memrealtime s[28:29]\n\t" \
    "s_memtime s[54:55]\n\t" \
    "s_add_u32 s70, s70, 8\n\t" \
    "s_waitcnt lgkmcnt(0)\n\t" \
    "s_sub_u32 s69, s54, s56\n\t" \
    "s_cmp_gt_u32 s69, s70\n\t" \
    "s_cbranch_scc1 hh_E19_%=\n\t" \
    "s_add_u32 s2, s2, s4\n\t" \
    "s_add_u32 s6, s6, s8\n\t" \
    "s_add_u32 s10, s10, s12\n\t" \
    "s_add_u32 s14, s14, s16\n\t" \
    "s_add_u32 s18, s18, s20\n\t" \
    "s_add_u32 s22, s22, s24\n\t" \
    "s_add_u32 s2, s2, s6\n\t" \
    "s_add_u32 s10, s10, s14\n\t" \
    "s_add_u32 s18, s18, s22\n\t" \
    "s_add_u32 s2, s2, s26\n\t" \
    "s_add_u32 s10, s10, s18\n\t" \
    "s_add_u32 s2, s2, s10\n\t" \
    "s_mul_i32 s70, s28, 13\n\t" \
    "s_sub_u32 s70, s70, s2\n\t" \
    "s_min_u32 s70, s70, 52\n\t" \
    "s_mul_i32 s69, s69, s70\n\t" \
    "s_mul_hi_u32 s69, s69, 76695844\n\t" \
    "s_mov_b32 s75, s73\n\t" \
    "s_mov_b32 s76, s74\n\t" \
    "s_add_u32 s73, s56, s69\n\t" \
    "s_lshl_b32 s74, s28, 4\n\t" \
    "s_sub_u32 s69, s73, s75\n\t" \
    "s_sub_u32 s70, s74, s76\n\t" \
    "s_sub_u32 s71, s70, 1600\n\t" \
    "s_cmp_lt_u32 s71, 400000\n\t" \
    "s_cbranch_scc0 hh_K20_%=\n\t" \
    "v_cvt_f32_u32 v34, s69\n\t" \
    "v_cvt_f32_u32 v35, s70\n\t" \
    "v_rcp_f32 v36, v34\n\t" \
    "v_rcp_f32 v37, v35\n\t" \
    "v_mul_f32 v36, v36, v35\n\t" \
    "v_mul_f32 v37, v37, v34\n\t" \
    "v_mul_f32 v36, 0x4f800000, v36\n\t" \
    "v_mul_f32 v37, 0x4f000000, v37\n\t" \
    "v_cvt_u32_f32 v36, v36\n\t" \
    "v_cvt_u32_f32 v37, v37\n\t" \
    "v_readfirstlane_b32 s69, v36\n\t" \
    "v_readfirstlane_b32 s70, v37\n\t" \
    "s_nop 1\n\t" \
    "s_sub_u32 s69, s69, s78\n\t" \
    "s_ashr_i32 s69, s69, 1\n\t" \
    "s_add_u32 s78, s78, s69\n\t" \
    "s_sub_u32 s70, s70, s77\n\t" \
    "s_ashr_i32 s70, s70, 1\n\t" \
    "s_add_u32 s77, s77, s70\n\t" \
    "hh_K20_%=:\n\t" \
    "hh_Hok14_%=:\n\t" \
    "v_max_u32_dpp v30, v30, v30 row_ror:1 row_mask:0xf bank_mask:0xf\n\t" \
    "s_nop 1\n\t" \
    "v_max_u32_dpp v30, v30, v30 row_ror:2 row_mask:0xf bank_mask:0xf\n\t" \
    "s_nop 1\n\t" \
    "v_max_u32_dpp v30, v30, v30 row_ror:4 row_mask:0xf bank_mask:0xf\n\t" \
    "s_nop 1\n\t" \
    "v_max_u32_dpp v30, v30, v30 row_ror:8 row_mask:0xf bank_mask:0xf\n\t" \
    "s_nop 0\n\t" \
    "v_readlane_b32 s69, v30, 0\n\t" \
    "v_readlane_b32 s71, v30, 16\n\t" \
    "s_nop 1\n\t" \
    "s_max_u32 s81, s69, s71\n\t" \
    "s_mul_hi_u32 s69, s85, s78\n\t" \
    "s_add_u32 s79, s81, s69\n\t" \
    "s_add_u32 s79, s79, s87\n\t" \
    "s_cmp_lt_u32 s65, 5\n\t" \
    "s_cselect_b32 s69, 48, 0\n\t" \
    "s_add_u32 s79, s79, s69\n\t" \
    "s_add_u32 s80, s79, s88\n\t" \
    "s_add_u32 s65, s65, 1\n\t" \
    "s_cmp_gt_u32 s65, 62\n\t" \
    "s_cbranch_scc1 hh_end10_%=\n\t" \
    "v_lshrrev_b32 v39, 11, v3\n\t" \
    "v_sub_u32 v38, v38, v39\n\t" \
    "s_min_u32 s69, s65, 32\n\t" \
    "v_cmp_ge_i32 vcc, s69, v38\n\t" \
    "s_cbranch_vccnz hh_loop8_%=\n\t" \
    "v_lshrrev_b32 v20, 15, v11\n\t" \
    "v_add3_u32 v20, v20, s65, 1\n\t" \
    "v_min_u32 v20, 63, v20\n\t" \
    "v_mul_u32_u24 v20, v20, v11\n\t" \
    "v_xor_b32 v20, v20, v4\n\t" \
    "v_lshrrev_b32 v21, 15, v12\n\t" \
    "v_add3_u32 v21, v21, s65, 1\n\t" \
    "v_min_u32 v21, 63, v21\n\t" \
    "v_mul_u32_u24 v21, v21, v12\n\t" \
    "v_xor_b32 v21, v21, v5\n\t" \
    "v_lshrrev_b32 v22, 15, v13\n\t" \
    "v_add3_u32 v22, v22, s65, 1\n\t" \
    "v_min_u32 v22, 63, v22\n\t" \
    "v_mul_u32_u24 v22, v22, v13\n\t" \
    "v_xor_b32 v22, v22, v6\n\t" \
    "v_lshrrev_b32 v23, 15, v14\n\t" \
    "v_add3_u32 v23, v23, s65, 1\n\t" \
    "v_min_u32 v23, 63, v23\n\t" \
    "v_mul_u32_u24 v23, v23, v14\n\t" \
    "v_xor_b32 v23, v23, v7\n\t" \
    "v_lshrrev_b32 v24, 15, v15\n\t" \
    "v_add3_u32 v24, v24, s65, 1\n\t" \
    "v_min_u32 v24, 63, v24\n\t" \
    "v_mul_u32_u24 v24, v24, v15\n\t" \
    "v_xor_b32 v24, v24, v8\n\t" \
    "v_lshrrev_b32 v25, 15, v16\n\t" \
    "v_add3_u32 v25, v25, s65, 1\n\t" \
    "v_min_u32 v25, 63, v25\n\t" \
    "v_mul_u32_u24 v25, v25, v16\n\t" \
    "v_xor_b32 v25, v25, v9\n\t" \
    "v_lshrrev_b32 v26, 15, v17\n\t" \
    "v_add3_u32 v26, v26, s65, 1\n\t" \
    "v_min_u32 v26, 63, v26\n\t" \
    "v_mul_u32_u24 v26, v26, v17\n\t" \
    "v_xor_b32 v26, v26, v10\n\t" \
    "s_add_u32 s71, s79, s96\n\t" \
    "s_sub_u32 s70, s71, s74\n\t" \
    "s_max_i32 s70, s70, 0\n\t" \
    "s_min_i32 s70, s70, 16000\n\t" \
    "s_lshl_b32 s70, s70, 1\n\t" \
    "s_mul_hi_u32 s70, s70, s77\n\t" \
    "s_add_u32 s70, s70, s73\n\t" \
    "s_getpc_b64 s[30:31]\n\t" \
    "hh_Wp23_%=:\n\t" \
    "s_add_u32 s30, s30, hh_We24_%=-hh_Wp23_%=\n\t" \
    "s_addc_u32 s31, s31, 0\n\t" \
    "s_sub_u32 s70, s70, 96\n\t" \
    "hh_W22_%=:\n\t" \
    "s_memtime s[56:57]\n\t" \
    "s_waitcnt lgkmcnt(0)\n\t" \
    "s_sub_u32 s69, s70, s56\n\t" \
    "s_cmp_gt_i32 s69, 80\n\t" \
    "s_cbranch_scc1 hh_W22_%=\n\t" \
    "s_max_i32 s69, s69, 0\n\t" \
    "s_and_b32 s69, s69, -4\n\t" \
    "s_sub_u32 s30, s30, s69\n\t" \
    "s_subb_u32 s31, s31, 0\n\t" \
    "s_setpc_b64 s[30:31]\n\t" \
    "s_nop 0\n\t" \
    "s_nop 0\n\t" \
    "s_nop 0\n\t" \
    "s_nop 0\n\t" \
    "s_nop 0\n\t" \
    "s_nop 0\n\t" \
    "s_nop 0\n\t" \
    "s_nop 0\n\t" \
    "s_nop 0\n\t" \
    "s_nop 0\n\t" \
    "s_nop 0\n\t" \
    "s_nop 0\n\t" \
    "s_nop 0\n\t" \
    "s_nop 0\n\t" \
    "s_nop 0\n\t" \
    "s_nop 0\n\t" \
    "s_nop 0\n\t" \
    "s_nop 0\n\t" \
    "s_nop 0\n\t" \
    "s_nop 0\n\t" \
    "hh_We24_%=:\n\t" \
    "global_load_lds_dword v26, s[36:37] sc1\n\t" \
    "s_sleep 2\n\t" \
    "s_nop 5\n\t" \
    "global_load_lds_dword v25, s[36:37] sc1\n\t" \
    "s_sleep 2\n\t" \
    "s_nop 5\n\t" \
    "global_load_lds_dword v24, s[36:37] sc1\n\t" \
    "s_sleep 2\n\t" \
    "s_nop 5\n\t" \
    "global_load_lds_dword v23, s[36:37] sc1\n\t" \
    "s_sleep 2\n\t" \
    "s_nop 5\n\t" \
    "global_load_lds_dword v22, s[36:37] sc1\n\t" \
    "s_sleep 2\n\t" \
    "s_nop 5\n\t" \
    "global_load_lds_dword v21, s[36:37] sc1\n\t" \
    "s_sleep 2\n\t" \
    "s_nop 5\n\t" \
    "global_load_lds_dword v20, s[36:37] sc1\n\t" \
    "s_branch hh_loop8_%=\n\t" \
    "hh_end10_%=:\n\t" \
    "s_waitcnt vmcnt(0)\n\t" \
    "s_endpgm\n\t" \
    "hh_last_%=:\n\t" \
    ""
#define ASM_GX \
    "s_load_dwordx2 s[42:43], %3, 48\n\t" \
    "s_load_dwordx2 s[44:45], %3, 40\n\t" \
    "s_load_dwordx2 s[36:37], %3, 0x58\n\t" \
    "s_load_dwordx2 s[50:51], %3, 0x48\n\t" \
    "s_mov_b32 s66, %4\n\t" \
    "s_mov_b32 s67, %5\n\t" \
    "s_mov_b32 s68, 262144\n\t" \
    "s_mov_b32 s72, 0\n\t" \
    "s_mov_b32 s83, -1\n\t" \
    "s_mov_b32 s65, 0\n\t" \
    "s_lshl_b32 s82, s66, 10\n\t" \
    "s_waitcnt lgkmcnt(0)\n\t" \
    "s_mul_i32 s89, s66, 9216\n\t" \
    "s_mov_b32 s90, 0\n\t" \
    "s_add_u32 s46, s42, 2097152\n\t" \
    "s_addc_u32 s47, s43, 0\n\t" \
    "s_add_u32 m0, s82, 65536\n\t" \
    "s_nop 0\n\t" \
    "global_load_lds_dwordx4 v200, s[46:47]\n\t" \
    "s_add_u32 s46, s42, 2228224\n\t" \
    "s_addc_u32 s47, s43, 0\n\t" \
    "s_add_u32 m0, s82, 69632\n\t" \
    "s_nop 0\n\t" \
    "global_load_lds_dwordx4 v200, s[46:47]\n\t" \
    "s_add_u32 s46, s42, 2359296\n\t" \
    "s_addc_u32 s47, s43, 0\n\t" \
    "s_add_u32 m0, s82, 73728\n\t" \
    "s_nop 0\n\t" \
    "global_load_lds_dwordx4 v200, s[46:47]\n\t" \
    "s_add_u32 s46, s42, 2490368\n\t" \
    "s_addc_u32 s47, s43, 0\n\t" \
    "s_add_u32 m0, s82, 77824\n\t" \
    "s_nop 0\n\t" \
    "global_load_lds_dwordx4 v200, s[46:47]\n\t" \
    "s_add_u32 s46, s42, 2621440\n\t" \
    "s_addc_u32 s47, s43, 0\n\t" \
    "s_add_u32 m0, s82, 81920\n\t" \
    "s_nop 0\n\t" \
    "global_load_lds_dwordx4 v200, s[46:47]\n\t" \
    "s_add_u32 s46, s42, 2752512\n\t" \
    "s_addc_u32 s47, s43, 0\n\t" \
    "s_add_u32 m0, s82, 86016\n\t" \
    "s_nop 0\n\t" \
    "global_load_lds_dwordx4 v200, s[46:47]\n\t" \
    "s_add_u32 s46, s42, 2883584\n\t" \
    "s_addc_u32 s47, s43, 0\n\t" \
    "s_add_u32 m0, s82, 90112\n\t" \
    "s_nop 0\n\t" \
    "global_load_lds_dwordx4 v200, s[46:47]\n\t" \
    "s_add_u32 s46, s42, 3014656\n\t" \
    "s_addc_u32 s47, s43, 0\n\t" \
    "s_add_u32 m0, s82, 94208\n\t" \
    "s_nop 0\n\t" \
    "global_load_lds_dwordx4 v200, s[46:47]\n\t" \
    "s_add_u32 s46, s42, 3145728\n\t" \
    "s_addc_u32 s47, s43, 0\n\t" \
    "s_add_u32 m0, s82, 98304\n\t" \
    "s_nop 0\n\t" \
    "global_load_lds_dwordx4 v200, s[46:47]\n\t" \
    "s_add_u32 s46, s42, 3276800\n\t" \
    "s_addc_u32 s47, s43, 0\n\t" \
    "s_add_u32 m0, s82, 102400\n\t" \
    "s_nop 0\n\t" \
    "global_load_lds_dwordx4 v200, s[46:47]\n\t" \
    "s_add_u32 s46, s42, 3407872\n\t" \
    "s_addc_u32 s47, s43, 0\n\t" \
    "s_add_u32 m0, s82, 106496\n\t" \
    "s_nop 0\n\t" \
    "global_load_lds_dwordx4 v200, s[46:47]\n\t" \
    "s_add_u32 s46, s42, 3538944\n\t" \
    "s_addc_u32 s47, s43, 0\n\t" \
    "s_add_u32 m0, s82, 110592\n\t" \
    "s_nop 0\n\t" \
    "global_load_lds_dwordx4 v200, s[46:47]\n\t" \
    "s_add_u32 s46, s42, 3670016\n\t" \
    "s_addc_u32 s47, s43, 0\n\t" \
    "s_add_u32 m0, s82, 114688\n\t" \
    "s_nop 0\n\t" \
    "global_load_lds_dwordx4 v200, s[46:47]\n\t" \
    "s_add_u32 s46, s42, 3801088\n\t" \
    "s_addc_u32 s47, s43, 0\n\t" \
    "s_add_u32 m0, s82, 118784\n\t" \
    "s_nop 0\n\t" \
    "global_load_lds_dwordx4 v200, s[46:47]\n\t" \
    "s_add_u32 s46, s42, 3932160\n\t" \
    "s_addc_u32 s47, s43, 0\n\t" \
    "s_add_u32 m0, s82, 122880\n\t" \
    "s_nop 0\n\t" \
    "global_load_lds_dwordx4 v200, s[46:47]\n\t" \
    "s_add_u32 s46, s42, 4063232\n\t" \
    "s_addc_u32 s47, s43, 0\n\t" \
    "s_add_u32 m0, s82, 126976\n\t" \
    "s_nop 0\n\t" \
    "global_load_lds_dwordx4 v200, s[46:47]\n\t" \
    "s_add_u32 s46, s42, 4194304\n\t" \
    "s_addc_u32 s47, s43, 0\n\t" \
    "s_add_u32 m0, s82, 131072\n\t" \
    "s_nop 0\n\t" \
    "global_load_lds_dwordx4 v200, s[46:47]\n\t" \
    "s_add_u32 s46, s42, 4325376\n\t" \
    "s_addc_u32 s47, s43, 0\n\t" \
    "s_add_u32 m0, s82, 135168\n\t" \
    "s_nop 0\n\t" \
    "global_load_lds_dwordx4 v200, s[46:47]\n\t" \
    "s_add_u32 s46, s42, 4456448\n\t" \
    "s_addc_u32 s47, s43, 0\n\t" \
    "s_add_u32 m0, s82, 139264\n\t" \
    "s_nop 0\n\t" \
    "global_load_lds_dwordx4 v200, s[46:47]\n\t" \
    "s_add_u32 s46, s42, 4587520\n\t" \
    "s_addc_u32 s47, s43, 0\n\t" \
    "s_add_u32 m0, s82, 143360\n\t" \
    "s_nop 0\n\t" \
    "global_load_lds_dwordx4 v200, s[46:47]\n\t" \
    "global_load_dwordx4 v[120:123], v203, s[50:51]\n\t" \
    "global_load_dwordx4 v[124:127], v204, s[50:51]\n\t" \
    "global_load_dwordx4 v[128:131], v205, s[50:51]\n\t" \
    "global_load_dwordx4 v[132:135], v206, s[50:51]\n\t" \
    "global_load_dwordx4 v[136:139], v207, s[50:51]\n\t" \
    "global_load_dwordx4 v[140:143], v208, s[50:51]\n\t" \
    "global_load_dwordx4 v[144:147], v209, s[50:51]\n\t" \
    "global_load_dwordx4 v[148:151], v210, s[50:51]\n\t" \
    "global_load_dwordx4 v[152:155], v211, s[50:51]\n\t" \
    "s_add_u32 s52, s50, 36864\n\t" \
    "s_addc_u32 s53, s51, 0\n\t" \
    "global_load_dwordx4 v[72:75], v203, s[52:53]\n\t" \
    "global_load_dwordx4 v[76:79], v204, s[52:53]\n\t" \
    "global_load_dwordx4 v[80:83], v205, s[52:53]\n\t" \
    "global_load_dwordx4 v[84:87], v206, s[52:53]\n\t" \
    "global_load_dwordx4 v[88:91], v207, s[52:53]\n\t" \
    "global_load_dwordx4 v[92:95], v208, s[52:53]\n\t" \
    "global_load_dwordx4 v[96:99], v209, s[52:53]\n\t" \
    "global_load_dwordx4 v[100:103], v210, s[52:53]\n\t" \
    "global_load_dwordx4 v[104:107], v211, s[52:53]\n\t" \
    "s_waitcnt vmcnt(50) lgkmcnt(0)\n\t" \
    "s_barrier\n\t" \
    "ds_read_b64_tr_b16 a[144:145], v201 offset:0\n\t" \
    "ds_read_b64_tr_b16 a[146:147], v201 offset:1024\n\t" \
    "ds_read_b64_tr_b16 a[148:149], v201 offset:8192\n\t" \
    "ds_read_b64_tr_b16 a[150:151], v201 offset:9216\n\t" \
    "ds_read_b64_tr_b16 a[216:217], v202 offset:0\n\t" \
    "ds_read_b64_tr_b16 a[218:219], v202 offset:1024\n\t" \
    "ds_read_b64_tr_b16 a[220:221], v202 offset:8192\n\t" \
    "ds_read_b64_tr_b16 a[222:223], v202 offset:9216\n\t" \
    "s_waitcnt vmcnt(46) lgkmcnt(0)\n\t" \
    "s_barrier\n\t" \
    "ds_read_b64_tr_b16 a[152:153], v201 offset:16384\n\t" \
    "ds_read_b64_tr_b16 a[154:155], v201 offset:17408\n\t" \
    "ds_read_b64_tr_b16 a[156:157], v201 offset:24576\n\t" \
    "ds_read_b64_tr_b16 a[158:159], v201 offset:25600\n\t" \
    "ds_read_b64_tr_b16 a[224:225], v202 offset:16384\n\t" \
    "ds_read_b64_tr_b16 a[226:227], v202 offset:17408\n\t" \
    "ds_read_b64_tr_b16 a[228:229], v202 offset:24576\n\t" \
    "ds_read_b64_tr_b16 a[230:231], v202 offset:25600\n\t" \
    "s_waitcnt vmcnt(42) lgkmcnt(0)\n\t" \
    "s_barrier\n\t" \
    "ds_read_b64_tr_b16 a[160:161], v201 offset:32768\n\t" \
    "ds_read_b64_tr_b16 a[162:163], v201 offset:33792\n\t" \
    "ds_read_b64_tr_b16 a[164:165], v201 offset:40960\n\t" \
    "ds_read_b64_tr_b16 a[166:167], v201 offset:41984\n\t" \
    "ds_read_b64_tr_b16 a[232:233], v202 offset:32768\n\t" \
    "ds_read_b64_tr_b16 a[234:235], v202 offset:33792\n\t" \
    "ds_read_b64_tr_b16 a[236:237], v202 offset:40960\n\t" \
    "ds_read_b64_tr_b16 a[238:239], v202 offset:41984\n\t" \
    "s_waitcnt vmcnt(38) lgkmcnt(0)\n\t" \
    "s_barrier\n\t" \
    "ds_read_b64_tr_b16 a[168:169], v201 offset:49152\n\t" \
    "ds_read_b64_tr_b16 a[170:171], v201 offset:50176\n\t" \
    "ds_read_b64_tr_b16 a[172:173], v201 offset:57344\n\t" \
    "ds_read_b64_tr_b16 a[174:175], v201 offset:58368\n\t" \
    "ds_read_b64_tr_b16 a[240:241], v202 offset:49152\n\t" \
    "ds_read_b64_tr_b16 a[242:243], v202 offset:50176\n\t" \
    "ds_read_b64_tr_b16 a[244:245], v202 offset:57344\n\t" \
    "ds_read_b64_tr_b16 a[246:247], v202 offset:58368\n\t" \
    "v_mbcnt_lo_u32_b32 v164, -1, 0\n\t" \
    "v_mbcnt_hi_u32_b32 v164, -1, v164\n\t" \
    "v_lshlrev_b32 v164, 6, v164\n\t" \
    "v_add_u32 v164, 6225920, v164\n\t" \
    "v_mov_b32 v165, 6234112\n\t" \
    "global_load_dword v165, v165, s[36:37] sc1\n\t" \
    "global_load_dwordx2 v[166:167], v164, s[36:37] sc1\n\t" \
    "global_load_dword v168, v164, s[36:37] offset:2048 sc1\n\t" \
    "s_waitcnt vmcnt(37) lgkmcnt(0)\n\t" \
    "s_barrier\n\t" \
    "v_add_u32 v201, 0x10000, v201\n\t" \
    "v_add_u32 v202, 0x10000, v202\n\t" \
    "ds_read_b64_tr_b16 a[176:177], v201 offset:0\n\t" \
    "ds_read_b64_tr_b16 a[178:179], v201 offset:1024\n\t" \
    "ds_read_b64_tr_b16 a[180:181], v201 offset:8192\n\t" \
    "ds_read_b64_tr_b16 a[182:183], v201 offset:9216\n\t" \
    "ds_read_b64_tr_b16 a[248:249], v202 offset:0\n\t" \
    "ds_read_b64_tr_b16 a[250:251], v202 offset:1024\n\t" \
    "ds_read_b64_tr_b16 a[252:253], v202 offset:8192\n\t" \
    "ds_read_b64_tr_b16 a[254:255], v202 offset:9216\n\t" \
    "s_waitcnt vmcnt(33) lgkmcnt(0)\n\t" \
    "s_barrier\n\t" \
    "ds_read_b64_tr_b16 a[184:185], v201 offset:16384\n\t" \
    "ds_read_b64_tr_b16 a[186:187], v201 offset:17408\n\t" \
    "ds_read_b64_tr_b16 a[188:189], v201 offset:24576\n\t" \
    "ds_read_b64_tr_b16 a[190:191], v201 offset:25600\n\t" \
    "ds_read_b64_tr_b16 v[224:225], v202 offset:16384\n\t" \
    "ds_read_b64_tr_b16 v[226:227], v202 offset:17408\n\t" \
    "ds_read_b64_tr_b16 v[228:229], v202 offset:24576\n\t" \
    "ds_read_b64_tr_b16 v[230:231], v202 offset:25600\n\t" \
    "s_waitcnt vmcnt(29) lgkmcnt(0)\n\t" \
    "s_barrier\n\t" \
    "ds_read_b64_tr_b16 a[192:193], v201 offset:32768\n\t" \
    "ds_read_b64_tr_b16 a[194:195], v201 offset:33792\n\t" \
    "ds_read_b64_tr_b16 a[196:197], v201 offset:40960\n\t" \
    "ds_read_b64_tr_b16 a[198:199], v201 offset:41984\n\t" \
    "ds_read_b64_tr_b16 v[232:233], v202 offset:32768\n\t" \
    "ds_read_b64_tr_b16 v[234:235], v202 offset:33792\n\t" \
    "ds_read_b64_tr_b16 v[236:237], v202 offset:40960\n\t" \
    "ds_read_b64_tr_b16 v[238:239], v202 offset:41984\n\t" \
    "s_waitcnt vmcnt(25) lgkmcnt(0)\n\t" \
    "s_barrier\n\t" \
    "ds_read_b64_tr_b16 a[200:201], v201 offset:49152\n\t" \
    "ds_read_b64_tr_b16 a[202:203], v201 offset:50176\n\t" \
    "ds_read_b64_tr_b16 a[204:205], v201 offset:57344\n\t" \
    "ds_read_b64_tr_b16 a[206:207], v201 offset:58368\n\t" \
    "ds_read_b64_tr_b16 v[240:241], v202 offset:49152\n\t" \
    "ds_read_b64_tr_b16 v[242:243], v202 offset:50176\n\t" \
    "ds_read_b64_tr_b16 v[244:245], v202 offset:57344\n\t" \
    "ds_read_b64_tr_b16 v[246:247], v202 offset:58368\n\t" \
    "s_waitcnt vmcnt(21) lgkmcnt(0)\n\t" \
    "s_barrier\n\t" \
    "v_add_u32 v201, 0x10000, v201\n\t" \
    "v_add_u32 v202, 0x10000, v202\n\t" \
    "ds_read_b64_tr_b16 a[208:209], v201 offset:0\n\t" \
    "ds_read_b64_tr_b16 a[210:211], v201 offset:1024\n\t" \
    "ds_read_b64_tr_b16 a[212:213], v201 offset:8192\n\t" \
    "ds_read_b64_tr_b16 a[214:215], v201 offset:9216\n\t" \
    "ds_read_b64_tr_b16 v[248:249], v202 offset:0\n\t" \
    "ds_read_b64_tr_b16 v[250:251], v202 offset:1024\n\t" \
    "ds_read_b64_tr_b16 v[252:253], v202 offset:8192\n\t" \
    "ds_read_b64_tr_b16 v[254:255], v202 offset:9216\n\t" \
    "v_add_u32 v201, 0xfffe0000, v201\n\t" \
    "v_add_u32 v202, 0xfffe0000, v202\n\t" \
    "s_waitcnt lgkmcnt(0)\n\t" \
    "s_barrier\n\t" \
    "s_waitcnt vmcnt(0)\n\t" \
    "ds_write_b128 v3, v[120:123] offset:0\n\t" \
    "ds_write_b128 v3, v[124:127] offset:1024\n\t" \
    "ds_write_b128 v3, v[128:131] offset:2048\n\t" \
    "ds_write_b128 v3, v[132:135] offset:3072\n\t" \
    "ds_write_b128 v3, v[136:139] offset:4096\n\t" \
    "ds_write_b128 v3, v[140:143] offset:5120\n\t" \
    "ds_write_b128 v3, v[144:147] offset:6144\n\t" \
    "ds_write_b128 v3, v[148:151] offset:7168\n\t" \
    "ds_write_b128 v3, v[152:155] offset:8192\n\t" \
    "ds_write_b128 v3, v[72:75] offset:36864\n\t" \
    "ds_write_b128 v3, v[76:79] offset:37888\n\t" \
    "ds_write_b128 v3, v[80:83] offset:38912\n\t" \
    "ds_write_b128 v3, v[84:87] offset:39936\n\t" \
    "ds_write_b128 v3, v[88:91] offset:40960\n\t" \
    "ds_write_b128 v3, v[92:95] offset:41984\n\t" \
    "ds_write_b128 v3, v[96:99] offset:43008\n\t" \
    "ds_write_b128 v3, v[100:103] offset:44032\n\t" \
    "ds_write_b128 v3, v[104:107] offset:45056\n\t" \
    "gx_C1_%=:\n\t" \
    "v_cmp_ne_u32 s[54:55], v166, v168\n\t" \
    "v_sub_u32 v166, v166, v165\n\t" \
    "v_cmp_ne_u32 vcc, 1, v166\n\t" \
    "s_nop 1\n\t" \
    "s_or_b64 vcc, vcc, s[54:55]\n\t" \
    "s_cbranch_scc0 gx_Cok2_%=\n\t" \
    "s_cmp_eq_u32 s68, 0\n\t" \
    "s_cbranch_scc1 gx_F4_%=\n\t" \
    "s_sub_u32 s68, s68, 1\n\t" \
    "s_sleep 6\n\t" \
    "s_branch gx_Cag3_%=\n\t" \
    "gx_F4_%=:\n\t" \
    "v_mov_b32 v168, 5767424\n\t" \
    "v_mov_b32 v169, 4\n\t" \
    "global_store_dword v168, v169, s[36:37] sc1\n\t" \
    "s_waitcnt vmcnt(0)\n\t" \
    "s_branch gx_Cok2_%=\n\t" \
    "gx_Cag3_%=:\n\t" \
    "global_load_dwordx2 v[166:167], v164, s[36:37] sc1\n\t" \
    "global_load_dword v168, v164, s[36:37] offset:2048 sc1\n\t" \
    "s_waitcnt vmcnt(0)\n\t" \
    "s_branch gx_C1_%=\n\t" \
    "gx_Cok2_%=:\n\t" \
    "v_readlane_b32 s69, v167, 2\n\t" \
    "v_readlane_b32 s70, v167, 3\n\t" \
    "s_nop 1\n\t" \
    "s_cmp_lt_u32 s70, s69\n\t" \
    "s_cselect_b32 s69, 0, 8192\n\t" \
    "s_add_u32 s100, s36, s69\n\t" \
    "s_addc_u32 s101, s37, 0\n\t" \
    "s_cmp_lg_u32 s66, 0\n\t" \
    "s_cbranch_scc1 gx_Ca5_%=\n\t" \
    "v_mov_b32 v164, 5768192\n\t" \
    "s_mov_b64 exec, 1\n\t" \
    "global_atomic_add v164, v166, s[36:37] sc1\n\t" \
    "s_mov_b64 exec, -1\n\t" \
    "gx_Ca5_%=:\n\t" \
    "s_waitcnt lgkmcnt(0)\n\t" \
    "s_barrier\n\t" \
    "ds_read_b128 v[24:27], v1 offset:0\n\t" \
    "ds_read_b128 v[28:31], v1 offset:18432\n\t" \
    "ds_read_b128 v[32:35], v2 offset:0\n\t" \
    "ds_read_b128 v[36:39], v2 offset:18432\n\t" \
    "ds_read_b128 v[40:43], v1 offset:128\n\t" \
    "ds_read_b128 v[44:47], v1 offset:18560\n\t" \
    "s_add_u32 s91, s89, 0x12000\n\t" \
    "s_add_u32 s85, s91, s89\n\t" \
    "s_add_u32 s52, s50, 73728\n\t" \
    "s_addc_u32 s53, s51, 0\n\t" \
    "s_mul_i32 s69, s67, 8192\n\t" \
    "s_add_u32 s69, s69, 175112192\n\t" \
    "s_add_u32 s42, s36, s69\n\t" \
    "s_addc_u32 s43, s37, 0\n\t" \
    "s_mov_b64 s[40:41], s[42:43]\n\t" \
    "gx_l16_%=:\n\t" \
    "s_waitcnt lgkmcnt(5)\n\t" \
    "v_mfma_f32_16x16x32_bf16 v[88:91], a[144:147], v[24:27], 0\n\t" \
    "ds_read_b128 v[48:51], v2 offset:128\n\t" \
    "s_waitcnt lgkmcnt(5)\n\t" \
    "v_mfma_f32_16x16x32_bf16 v[92:95], a[144:147], v[28:31], 0\n\t" \
    "ds_read_b128 v[52:55], v2 offset:18560\n\t" \
    "v_mfma_f32_16x16x32_bf16 v[96:99], a[216:219], v[24:27], 0\n\t" \
    "s_add_u32 m0, s91, 0\n\t" \
    "v_mfma_f32_16x16x32_bf16 v[100:103], a[216:219], v[28:31], 0\n\t" \
    "global_load_lds_dwordx4 v203, s[52:53]\n\t" \
    "s_waitcnt lgkmcnt(5)\n\t" \
    "v_mfma_f32_16x16x32_bf16 v[88:91], a[148:151], v[32:35], v[88:91]\n\t" \
    "ds_read_b128 v[56:59], v1 offset:256\n\t" \
    "s_waitcnt lgkmcnt(5)\n\t" \
    "v_mfma_f32_16x16x32_bf16 v[92:95], a[148:151], v[36:39], v[92:95]\n\t" \
    "ds_read_b128 v[60:63], v1 offset:18688\n\t" \
    "v_mfma_f32_16x16x32_bf16 v[96:99], a[220:223], v[32:35], v[96:99]\n\t" \
    "s_add_u32 m0, s91, 1024\n\t" \
    "v_mfma_f32_16x16x32_bf16 v[100:103], a[220:223], v[36:39], v[100:103]\n\t" \
    "global_load_lds_dwordx4 v204, s[52:53]\n\t" \
    "s_waitcnt lgkmcnt(5)\n\t" \
    "v_mfma_f32_16x16x32_bf16 v[88:91], a[152:155], v[40:43], v[88:91]\n\t" \
    "ds_read_b128 v[64:67], v2 offset:256\n\t" \
    "s_waitcnt lgkmcnt(5)\n\t" \
    "v_mfma_f32_16x16x32_bf16 v[92:95], a[152:155], v[44:47], v[92:95]\n\t" \
    "ds_read_b128 v[68:71], v2 offset:18688\n\t" \
    "v_cvt_pk_bf16_f32 v104, v72, v73\n\t" \
    "v_mfma_f32_16x16x32_bf16 v[96:99], a[224:227], v[40:43], v[96:99]\n\t" \
    "s_add_u32 m0, s91, 2048\n\t" \
    "v_cvt_pk_bf16_f32 v105, v74, v75\n\t" \
    "v_mfma_f32_16x16x32_bf16 v[100:103], a[224:227], v[44:47], v[100:103]\n\t" \
    "global_load_lds_dwordx4 v205, s[52:53]\n\t" \
    "s_waitcnt lgkmcnt(5)\n\t" \
    "v_mfma_f32_16x16x32_bf16 v[88:91], a[156:159], v[48:51], v[88:91]\n\t" \
    "ds_read_b128 v[24:27], v1 offset:384\n\t" \
    "s_waitcnt lgkmcnt(5)\n\t" \
    "v_mfma_f32_16x16x32_bf16 v[92:95], a[156:159], v[52:55], v[92:95]\n\t" \
    "ds_read_b128 v[28:31], v1 offset:18816\n\t" \
    "v_cvt_pk_bf16_f32 v106, v80, v81\n\t" \
    "v_mfma_f32_16x16x32_bf16 v[96:99], a[228:231], v[48:51], v[96:99]\n\t" \
    "s_add_u32 m0, s91, 3072\n\t" \
    "v_cvt_pk_bf16_f32 v107, v82, v83\n\t" \
    "v_mfma_f32_16x16x32_bf16 v[100:103], a[228:231], v[52:55], v[100:103]\n\t" \
    "global_load_lds_dwordx4 v206, s[52:53]\n\t" \
    "s_waitcnt lgkmcnt(5)\n\t" \
    "v_mfma_f32_16x16x32_bf16 v[88:91], a[160:163], v[56:59], v[88:91]\n\t" \
    "ds_read_b128 v[32:35], v2 offset:384\n\t" \
    "s_waitcnt lgkmcnt(5)\n\t" \
    "v_mfma_f32_16x16x32_bf16 v[92:95], a[160:163], v[60:63], v[92:95]\n\t" \
    "ds_read_b128 v[36:39], v2 offset:18816\n\t" \
    "global_store_dwordx4 v8, v[104:107], s[40:41] offset:0 sc1\n\t" \
    "v_mfma_f32_16x16x32_bf16 v[96:99], a[232:235], v[56:59], v[96:99]\n\t" \
    "s_add_u32 m0, s91, 4096\n\t" \
    "v_cvt_pk_bf16_f32 v108, v76, v77\n\t" \
    "v_mfma_f32_16x16x32_bf16 v[100:103], a[232:235], v[60:63], v[100:103]\n\t" \
    "global_load_lds_dwordx4 v207, s[52:53]\n\t" \
    "s_waitcnt lgkmcnt(5)\n\t" \
    "v_mfma_f32_16x16x32_bf16 v[88:91], a[164:167], v[64:67], v[88:91]\n\t" \
    "ds_read_b128 v[40:43], v1 offset:512\n\t" \
    "s_waitcnt lgkmcnt(5)\n\t" \
    "v_mfma_f32_16x16x32_bf16 v[92:95], a[164:167], v[68:71], v[92:95]\n\t" \
    "ds_read_b128 v[44:47], v1 offset:18944\n\t" \
    "v_cvt_pk_bf16_f32 v109, v78, v79\n\t" \
    "v_mfma_f32_16x16x32_bf16 v[96:99], a[236:239], v[64:67], v[96:99]\n\t" \
    "s_add_u32 m0, s91, 5120\n\t" \
    "v_cvt_pk_bf16_f32 v110, v84, v85\n\t" \
    "v_mfma_f32_16x16x32_bf16 v[100:103], a[236:239], v[68:71], v[100:103]\n\t" \
    "global_load_lds_dwordx4 v208, s[52:53]\n\t" \
    "s_waitcnt lgkmcnt(5)\n\t" \
    "v_mfma_f32_16x16x32_bf16 v[88:91], a[168:171], v[24:27], v[88:91]\n\t" \
    "ds_read_b128 v[48:51], v2 offset:512\n\t" \
    "s_waitcnt lgkmcnt(5)\n\t" \
    "v_mfma_f32_16x16x32_bf16 v[92:95], a[168:171], v[28:31], v[92:95]\n\t" \
    "ds_read_b128 v[52:55], v2 offset:18944\n\t" \
    "v_cvt_pk_bf16_f32 v111, v86, v87\n\t" \
    "v_mfma_f32_16x16x32_bf16 v[96:99], a[240:243], v[24:27], v[96:99]\n\t" \
    "s_add_u32 m0, s91, 6144\n\t" \
    "global_store_dwordx4 v8, v[108:111], s[40:41] offset:1024 sc1\n\t" \
    "v_mfma_f32_16x16x32_bf16 v[100:103], a[240:243], v[28:31], v[100:103]\n\t" \
    "global_load_lds_dwordx4 v209, s[52:53]\n\t" \
    "s_waitcnt lgkmcnt(5)\n\t" \
    "v_mfma_f32_16x16x32_bf16 v[88:91], a[172:175], v[32:35], v[88:91]\n\t" \
    "ds_read_b128 v[56:59], v1 offset:640\n\t" \
    "s_waitcnt lgkmcnt(5)\n\t" \
    "v_mfma_f32_16x16x32_bf16 v[92:95], a[172:175], v[36:39], v[92:95]\n\t" \
    "ds_read_b128 v[60:63], v1 offset:19072\n\t" \
    "v_mfma_f32_16x16x32_bf16 v[96:99], a[244:247], v[32:35], v[96:99]\n\t" \
    "s_add_u32 m0, s91, 7168\n\t" \
    "v_mfma_f32_16x16x32_bf16 v[100:103], a[244:247], v[36:39], v[100:103]\n\t" \
    "global_load_lds_dwordx4 v210, s[52:53]\n\t" \
    "s_waitcnt lgkmcnt(5)\n\t" \
    "v_mfma_f32_16x16x32_bf16 v[88:91], a[176:179], v[40:43], v[88:91]\n\t" \
    "ds_read_b128 v[64:67], v2 offset:640\n\t" \
    "s_waitcnt lgkmcnt(5)\n\t" \
    "v_mfma_f32_16x16x32_bf16 v[92:95], a[176:179], v[44:47], v[92:95]\n\t" \
    "ds_read_b128 v[68:71], v2 offset:19072\n\t" \
    "v_mfma_f32_16x16x32_bf16 v[96:99], a[248:251], v[40:43], v[96:99]\n\t" \
    "s_add_u32 m0, s91, 8192\n\t" \
    "v_mfma_f32_16x16x32_bf16 v[100:103], a[248:251], v[44:47], v[100:103]\n\t" \
    "global_load_lds_dwordx4 v211, s[52:53]\n\t" \
    "s_waitcnt lgkmcnt(5)\n\t" \
    "v_mfma_f32_16x16x32_bf16 v[88:91], a[180:183], v[48:51], v[88:91]\n\t" \
    "ds_read_b128 v[24:27], v1 offset:768\n\t" \
    "s_waitcnt lgkmcnt(5)\n\t" \
    "v_mfma_f32_16x16x32_bf16 v[92:95], a[180:183], v[52:55], v[92:95]\n\t" \
    "ds_read_b128 v[28:31], v1 offset:19200\n\t" \
    "v_mfma_f32_16x16x32_bf16 v[96:99], a[252:255], v[48:51], v[96:99]\n\t" \
    "v_mfma_f32_16x16x32_bf16 v[100:103], a[252:255], v[52:55], v[100:103]\n\t" \
    "s_waitcnt lgkmcnt(5)\n\t" \
    "v_mfma_f32_16x16x32_bf16 v[88:91], a[184:187], v[56:59], v[88:91]\n\t" \
    "ds_read_b128 v[32:35], v2 offset:768\n\t" \
    "s_waitcnt lgkmcnt(5)\n\t" \
    "v_mfma_f32_16x16x32_bf16 v[92:95], a[184:187], v[60:63], v[92:95]\n\t" \
    "ds_read_b128 v[36:39], v2 offset:19200\n\t" \
    "v_mfma_f32_16x16x32_bf16 v[96:99], v[224:227], v[56:59], v[96:99]\n\t" \
    "s_waitcnt vmcnt(11)\n\t" \
    "s_barrier\n\t" \
    "v_mfma_f32_16x16x32_bf16 v[100:103], v[224:227], v[60:63], v[100:103]\n\t" \
    "s_cmp_lg_u32 s66, 0\n\t" \
    "s_cbranch_scc1 gx_SF8_%=\n\t" \
    "s_lshl_b32 s69, s67, 7\n\t" \
    "s_add_u32 s69, s69, 3149824\n\t" \
    "s_add_u32 s70, s65, -2\n\t" \
    "v_mov_b32 v156, s69\n\t" \
    "v_mov_b32 v157, s70\n\t" \
    "s_mov_b64 exec, 1\n\t" \
    "global_store_dword v156, v157, s[100:101] sc1\n\t" \
    "s_mov_b64 exec, -1\n\t" \
    "gx_SF8_%=:\n\t" \
    "s_waitcnt lgkmcnt(5)\n\t" \
    "v_mfma_f32_16x16x32_bf16 v[88:91], a[188:191], v[64:67], v[88:91]\n\t" \
    "ds_read_b128 v[40:43], v1 offset:896\n\t" \
    "s_waitcnt lgkmcnt(5)\n\t" \
    "v_mfma_f32_16x16x32_bf16 v[92:95], a[188:191], v[68:71], v[92:95]\n\t" \
    "ds_read_b128 v[44:47], v1 offset:19328\n\t" \
    "v_mfma_f32_16x16x32_bf16 v[96:99], v[228:231], v[64:67], v[96:99]\n\t" \
    "v_add_u32 v22, 0x9000, v1\n\t" \
    "v_add_u32 v23, 0x9000, v2\n\t" \
    "v_mfma_f32_16x16x32_bf16 v[100:103], v[228:231], v[68:71], v[100:103]\n\t" \
    "s_add_u32 s48, s52, 36864\n\t" \
    "s_addc_u32 s49, s53, 0\n\t" \
    "s_waitcnt lgkmcnt(5)\n\t" \
    "v_mfma_f32_16x16x32_bf16 v[88:91], a[192:195], v[24:27], v[88:91]\n\t" \
    "ds_read_b128 v[48:51], v2 offset:896\n\t" \
    "s_waitcnt lgkmcnt(5)\n\t" \
    "v_mfma_f32_16x16x32_bf16 v[92:95], a[192:195], v[28:31], v[92:95]\n\t" \
    "ds_read_b128 v[52:55], v2 offset:19328\n\t" \
    "v_mfma_f32_16x16x32_bf16 v[96:99], v[232:235], v[24:27], v[96:99]\n\t" \
    "s_mul_i32 s70, s67, 7\n\t" \
    "s_add_u32 s69, s65, 0\n\t" \
    "v_mfma_f32_16x16x32_bf16 v[100:103], v[232:235], v[28:31], v[100:103]\n\t" \
    "s_xor_b32 s69, s69, s70\n\t" \
    "s_and_b32 s69, s69, 63\n\t" \
    "s_waitcnt lgkmcnt(5)\n\t" \
    "v_mfma_f32_16x16x32_bf16 v[88:91], a[196:199], v[32:35], v[88:91]\n\t" \
    "ds_read_b128 v[56:59], v1 offset:1024\n\t" \
    "s_waitcnt lgkmcnt(5)\n\t" \
    "v_mfma_f32_16x16x32_bf16 v[92:95], a[196:199], v[36:39], v[92:95]\n\t" \
    "ds_read_b128 v[60:63], v1 offset:19456\n\t" \
    "v_mfma_f32_16x16x32_bf16 v[96:99], v[236:239], v[32:35], v[96:99]\n\t" \
    "s_lshl_b32 s69, s69, 13\n\t" \
    "s_mul_i32 s70, s67, 524288\n\t" \
    "v_mfma_f32_16x16x32_bf16 v[100:103], v[236:239], v[36:39], v[100:103]\n\t" \
    "s_add_u32 s69, s69, s70\n\t" \
    "s_add_u32 s69, s69, 23068672\n\t" \
    "s_waitcnt lgkmcnt(5)\n\t" \
    "v_mfma_f32_16x16x32_bf16 v[88:91], a[200:203], v[40:43], v[88:91]\n\t" \
    "ds_read_b128 v[64:67], v2 offset:1024\n\t" \
    "s_waitcnt lgkmcnt(5)\n\t" \
    "v_mfma_f32_16x16x32_bf16 v[92:95], a[200:203], v[44:47], v[92:95]\n\t" \
    "ds_read_b128 v[68:71], v2 offset:19456\n\t" \
    "v_mfma_f32_16x16x32_bf16 v[96:99], v[240:243], v[40:43], v[96:99]\n\t" \
    "s_add_u32 s38, s36, s69\n\t" \
    "s_addc_u32 s39, s37, 0\n\t" \
    "v_mfma_f32_16x16x32_bf16 v[100:103], v[240:243], v[44:47], v[100:103]\n\t" \
    "s_waitcnt lgkmcnt(5)\n\t" \
    "v_mfma_f32_16x16x32_bf16 v[88:91], a[204:207], v[48:51], v[88:91]\n\t" \
    "ds_read_b128 v[24:27], v22 offset:0\n\t" \
    "s_waitcnt lgkmcnt(5)\n\t" \
    "v_mfma_f32_16x16x32_bf16 v[92:95], a[204:207], v[52:55], v[92:95]\n\t" \
    "ds_read_b128 v[28:31], v22 offset:18432\n\t" \
    "v_mfma_f32_16x16x32_bf16 v[96:99], v[244:247], v[48:51], v[96:99]\n\t" \
    "v_mfma_f32_16x16x32_bf16 v[100:103], v[244:247], v[52:55], v[100:103]\n\t" \
    "s_waitcnt lgkmcnt(5)\n\t" \
    "v_mfma_f32_16x16x32_bf16 v[88:91], a[208:211], v[56:59], v[88:91]\n\t" \
    "ds_read_b128 v[32:35], v23 offset:0\n\t" \
    "s_waitcnt lgkmcnt(5)\n\t" \
    "v_mfma_f32_16x16x32_bf16 v[92:95], a[208:211], v[60:63], v[92:95]\n\t" \
    "ds_read_b128 v[36:39], v23 offset:18432\n\t" \
    "v_mfma_f32_16x16x32_bf16 v[96:99], v[248:251], v[56:59], v[96:99]\n\t" \
    "v_mfma_f32_16x16x32_bf16 v[100:103], v[248:251], v[60:63], v[100:103]\n\t" \
    "s_waitcnt lgkmcnt(5)\n\t" \
    "v_mfma_f32_16x16x32_bf16 v[88:91], a[212:215], v[64:67], v[88:91]\n\t" \
    "ds_read_b128 v[40:43], v22 offset:128\n\t" \
    "s_waitcnt lgkmcnt(5)\n\t" \
    "v_mfma_f32_16x16x32_bf16 v[92:95], a[212:215], v[68:71], v[92:95]\n\t" \
    "ds_read_b128 v[44:47], v22 offset:18560\n\t" \
    "v_mfma_f32_16x16x32_bf16 v[96:99], v[252:255], v[64:67], v[96:99]\n\t" \
    "v_mfma_f32_16x16x32_bf16 v[100:103], v[252:255], v[68:71], v[100:103]\n\t" \
    "s_cmp_lg_u32 s65, 0\n\t" \
    "s_cbranch_scc1 gx_nf09_%=\n\t" \
    "v_cvt_pk_bf16_f32 v104, v88, v89\n\t" \
    "v_cvt_pk_bf16_f32 v105, v90, v91\n\t" \
    "s_nop 2\n\t" \
    "v_cvt_pk_bf16_f32 v106, v96, v97\n\t" \
    "v_cvt_pk_bf16_f32 v107, v98, v99\n\t" \
    "global_store_dwordx4 v8, v[104:107], s[38:39] offset:0 sc1\n\t" \
    "v_cvt_pk_bf16_f32 v108, v92, v93\n\t" \
    "v_cvt_pk_bf16_f32 v109, v94, v95\n\t" \
    "v_cvt_pk_bf16_f32 v110, v100, v101\n\t" \
    "v_cvt_pk_bf16_f32 v111, v102, v103\n\t" \
    "global_store_dwordx4 v8, v[108:111], s[38:39] offset:1024 sc1\n\t" \
    "s_waitcnt vmcnt(0)\n\t" \
    "s_barrier\n\t" \
    "s_cmp_lg_u32 s66, 0\n\t" \
    "s_cbranch_scc1 gx_SF10_%=\n\t" \
    "s_lshl_b32 s69, s67, 7\n\t" \
    "s_add_u32 s69, s69, 3149824\n\t" \
    "s_add_u32 s70, s65, 0\n\t" \
    "v_mov_b32 v156, s69\n\t" \
    "v_mov_b32 v157, s70\n\t" \
    "s_mov_b64 exec, 1\n\t" \
    "global_store_dword v156, v157, s[100:101] sc1\n\t" \
    "s_mov_b64 exec, -1\n\t" \
    "gx_SF10_%=:\n\t" \
    "gx_nf09_%=:\n\t" \
    "s_waitcnt lgkmcnt(5)\n\t" \
    "v_mfma_f32_16x16x32_bf16 v[72:75], a[144:147], v[24:27], 0\n\t" \
    "ds_read_b128 v[48:51], v23 offset:128\n\t" \
    "s_waitcnt lgkmcnt(5)\n\t" \
    "v_mfma_f32_16x16x32_bf16 v[76:79], a[144:147], v[28:31], 0\n\t" \
    "ds_read_b128 v[52:55], v23 offset:18560\n\t" \
    "v_mfma_f32_16x16x32_bf16 v[80:83], a[216:219], v[24:27], 0\n\t" \
    "s_add_u32 m0, s91, 36864\n\t" \
    "v_mfma_f32_16x16x32_bf16 v[84:87], a[216:219], v[28:31], 0\n\t" \
    "global_load_lds_dwordx4 v203, s[48:49]\n\t" \
    "s_waitcnt lgkmcnt(5)\n\t" \
    "v_mfma_f32_16x16x32_bf16 v[72:75], a[148:151], v[32:35], v[72:75]\n\t" \
    "ds_read_b128 v[56:59], v22 offset:256\n\t" \
    "s_waitcnt lgkmcnt(5)\n\t" \
    "v_mfma_f32_16x16x32_bf16 v[76:79], a[148:151], v[36:39], v[76:79]\n\t" \
    "ds_read_b128 v[60:63], v22 offset:18688\n\t" \
    "v_mfma_f32_16x16x32_bf16 v[80:83], a[220:223], v[32:35], v[80:83]\n\t" \
    "s_add_u32 m0, s91, 37888\n\t" \
    "v_mfma_f32_16x16x32_bf16 v[84:87], a[220:223], v[36:39], v[84:87]\n\t" \
    "global_load_lds_dwordx4 v204, s[48:49]\n\t" \
    "s_waitcnt lgkmcnt(5)\n\t" \
    "v_mfma_f32_16x16x32_bf16 v[72:75], a[152:155], v[40:43], v[72:75]\n\t" \
    "ds_read_b128 v[64:67], v23 offset:256\n\t" \
    "s_waitcnt lgkmcnt(5)\n\t" \
    "v_mfma_f32_16x16x32_bf16 v[76:79], a[152:155], v[44:47], v[76:79]\n\t" \
    "ds_read_b128 v[68:71], v23 offset:18688\n\t" \
    "v_cvt_pk_bf16_f32 v104, v88, v89\n\t" \
    "v_mfma_f32_16x16x32_bf16 v[80:83], a[224:227], v[40:43], v[80:83]\n\t" \
    "s_add_u32 m0, s91, 38912\n\t" \
    "v_cvt_pk_bf16_f32 v105, v90, v91\n\t" \
    "v_mfma_f32_16x16x32_bf16 v[84:87], a[224:227], v[44:47], v[84:87]\n\t" \
    "global_load_lds_dwordx4 v205, s[48:49]\n\t" \
    "s_waitcnt lgkmcnt(5)\n\t" \
    "v_mfma_f32_16x16x32_bf16 v[72:75], a[156:159], v[48:51], v[72:75]\n\t" \
    "ds_read_b128 v[24:27], v22 offset:384\n\t" \
    "s_waitcnt lgkmcnt(5)\n\t" \
    "v_mfma_f32_16x16x32_bf16 v[76:79], a[156:159], v[52:55], v[76:79]\n\t" \
    "ds_read_b128 v[28:31], v22 offset:18816\n\t" \
    "v_cvt_pk_bf16_f32 v106, v96, v97\n\t" \
    "v_mfma_f32_16x16x32_bf16 v[80:83], a[228:231], v[48:51], v[80:83]\n\t" \
    "s_add_u32 m0, s91, 39936\n\t" \
    "v_cvt_pk_bf16_f32 v107, v98, v99\n\t" \
    "v_mfma_f32_16x16x32_bf16 v[84:87], a[228:231], v[52:55], v[84:87]\n\t" \
    "global_load_lds_dwordx4 v206, s[48:49]\n\t" \
    "s_waitcnt lgkmcnt(5)\n\t" \
    "v_mfma_f32_16x16x32_bf16 v[72:75], a[160:163], v[56:59], v[72:75]\n\t" \
    "ds_read_b128 v[32:35], v23 offset:384\n\t" \
    "s_waitcnt lgkmcnt(5)\n\t" \
    "v_mfma_f32_16x16x32_bf16 v[76:79], a[160:163], v[60:63], v[76:79]\n\t" \
    "ds_read_b128 v[36:39], v23 offset:18816\n\t" \
    "global_store_dwordx4 v8, v[104:107], s[38:39] offset:0 sc1\n\t" \
    "v_mfma_f32_16x16x32_bf16 v[80:83], a[232:235], v[56:59], v[80:83]\n\t" \
    "s_add_u32 m0, s91, 40960\n\t" \
    "v_cvt_pk_bf16_f32 v108, v92, v93\n\t" \
    "v_mfma_f32_16x16x32_bf16 v[84:87], a[232:235], v[60:63], v[84:87]\n\t" \
    "global_load_lds_dwordx4 v207, s[48:49]\n\t" \
    "s_waitcnt lgkmcnt(5)\n\t" \
    "v_mfma_f32_16x16x32_bf16 v[72:75], a[164:167], v[64:67], v[72:75]\n\t" \
    "ds_read_b128 v[40:43], v22 offset:512\n\t" \
    "s_waitcnt lgkmcnt(5)\n\t" \
    "v_mfma_f32_16x16x32_bf16 v[76:79], a[164:167], v[68:71], v[76:79]\n\t" \
    "ds_read_b128 v[44:47], v22 offset:18944\n\t" \
    "v_cvt_pk_bf16_f32 v109, v94, v95\n\t" \
    "v_mfma_f32_16x16x32_bf16 v[80:83], a[236:239], v[64:67], v[80:83]\n\t" \
    "s_add_u32 m0, s91, 41984\n\t" \
    "v_cvt_pk_bf16_f32 v110, v100, v101\n\t" \
    "v_mfma_f32_16x16x32_bf16 v[84:87], a[236:239], v[68:71], v[84:87]\n\t" \
    "global_load_lds_dwordx4 v208, s[48:49]\n\t" \
    "s_waitcnt lgkmcnt(5)\n\t" \
    "v_mfma_f32_16x16x32_bf16 v[72:75], a[168:171], v[24:27], v[72:75]\n\t" \
    "ds_read_b128 v[48:51], v23 offset:512\n\t" \
    "s_waitcnt lgkmcnt(5)\n\t" \
    "v_mfma_f32_16x16x32_bf16 v[76:79], a[168:171], v[28:31], v[76:79]\n\t" \
    "ds_read_b128 v[52:55], v23 offset:18944\n\t" \
    "v_cvt_pk_bf16_f32 v111, v102, v103\n\t" \
    "v_mfma_f32_16x16x32_bf16 v[80:83], a[240:243], v[24:27], v[80:83]\n\t" \
    "s_add_u32 m0, s91, 43008\n\t" \
    "global_store_dwordx4 v8, v[108:111], s[38:39] offset:1024 sc1\n\t" \
    "v_mfma_f32_16x16x32_bf16 v[84:87], a[240:243], v[28:31], v[84:87]\n\t" \
    "global_load_lds_dwordx4 v209, s[48:49]\n\t" \
    "s_waitcnt lgkmcnt(5)\n\t" \
    "v_mfma_f32_16x16x32_bf16 v[72:75], a[172:175], v[32:35], v[72:75]\n\t" \
    "ds_read_b128 v[56:59], v22 offset:640\n\t" \
    "s_waitcnt lgkmcnt(5)\n\t" \
    "v_mfma_f32_16x16x32_bf16 v[76:79], a[172:175], v[36:39], v[76:79]\n\t" \
    "ds_read_b128 v[60:63], v22 offset:19072\n\t" \
    "v_mfma_f32_16x16x32_bf16 v[80:83], a[244:247], v[32:35], v[80:83]\n\t" \
    "s_add_u32 m0, s91, 44032\n\t" \
    "v_mfma_f32_16x16x32_bf16 v[84:87], a[244:247], v[36:39], v[84:87]\n\t" \
    "global_load_lds_dwordx4 v210, s[48:49]\n\t" \
    "s_waitcnt lgkmcnt(5)\n\t" \
    "v_mfma_f32_16x16x32_bf16 v[72:75], a[176:179], v[40:43], v[72:75]\n\t" \
    "ds_read_b128 v[64:67], v23 offset:640\n\t" \
    "s_waitcnt lgkmcnt(5)\n\t" \
    "v_mfma_f32_16x16x32_bf16 v[76:79], a[176:179], v[44:47], v[76:79]\n\t" \
    "ds_read_b128 v[68:71], v23 offset:19072\n\t" \
    "v_mfma_f32_16x16x32_bf16 v[80:83], a[248:251], v[40:43], v[80:83]\n\t" \
    "s_add_u32 m0, s91, 45056\n\t" \
    "v_mfma_f32_16x16x32_bf16 v[84:87], a[248:251], v[44:47], v[84:87]\n\t" \
    "global_load_lds_dwordx4 v211, s[48:49]\n\t" \
    "s_waitcnt lgkmcnt(5)\n\t" \
    "v_mfma_f32_16x16x32_bf16 v[72:75], a[180:183], v[48:51], v[72:75]\n\t" \
    "ds_read_b128 v[24:27], v22 offset:768\n\t" \
    "s_waitcnt lgkmcnt(5)\n\t" \
    "v_mfma_f32_16x16x32_bf16 v[76:79], a[180:183], v[52:55], v[76:79]\n\t" \
    "ds_read_b128 v[28:31], v22 offset:19200\n\t" \
    "v_mfma_f32_16x16x32_bf16 v[80:83], a[252:255], v[48:51], v[80:83]\n\t" \
    "v_mfma_f32_16x16x32_bf16 v[84:87], a[252:255], v[52:55], v[84:87]\n\t" \
    "s_waitcnt lgkmcnt(5)\n\t" \
    "v_mfma_f32_16x16x32_bf16 v[72:75], a[184:187], v[56:59], v[72:75]\n\t" \
    "ds_read_b128 v[32:35], v23 offset:768\n\t" \
    "s_waitcnt lgkmcnt(5)\n\t" \
    "v_mfma_f32_16x16x32_bf16 v[76:79], a[184:187], v[60:63], v[76:79]\n\t" \
    "ds_read_b128 v[36:39], v23 offset:19200\n\t" \
    "v_mfma_f32_16x16x32_bf16 v[80:83], v[224:227], v[56:59], v[80:83]\n\t" \
    "s_waitcnt vmcnt(11)\n\t" \
    "s_barrier\n\t" \
    "v_mfma_f32_16x16x32_bf16 v[84:87], v[224:227], v[60:63], v[84:87]\n\t" \
    "s_max_u32 s71, s65, 1\n\t" \
    "s_cmp_lg_u32 s66, 0\n\t" \
    "s_cbranch_scc1 gx_SF11_%=\n\t" \
    "s_lshl_b32 s69, s67, 7\n\t" \
    "s_add_u32 s69, s69, 3149824\n\t" \
    "s_add_u32 s70, s71, -1\n\t" \
    "v_mov_b32 v156, s69\n\t" \
    "v_mov_b32 v157, s70\n\t" \
    "s_mov_b64 exec, 1\n\t" \
    "global_store_dword v156, v157, s[100:101] sc1\n\t" \
    "s_mov_b64 exec, -1\n\t" \
    "gx_SF11_%=:\n\t" \
    "s_waitcnt lgkmcnt(5)\n\t" \
    "v_mfma_f32_16x16x32_bf16 v[72:75], a[188:191], v[64:67], v[72:75]\n\t" \
    "ds_read_b128 v[40:43], v22 offset:896\n\t" \
    "s_waitcnt lgkmcnt(5)\n\t" \
    "v_mfma_f32_16x16x32_bf16 v[76:79], a[188:191], v[68:71], v[76:79]\n\t" \
    "ds_read_b128 v[44:47], v22 offset:19328\n\t" \
    "v_mfma_f32_16x16x32_bf16 v[80:83], v[228:231], v[64:67], v[80:83]\n\t" \
    "s_xor_b32 s90, s90, 0x12000\n\t" \
    "v_add_u32 v1, s90, v0\n\t" \
    "v_mfma_f32_16x16x32_bf16 v[84:87], v[228:231], v[68:71], v[84:87]\n\t" \
    "v_xor_b32 v2, 64, v1\n\t" \
    "s_add_u32 s52, s52, 73728\n\t" \
    "s_waitcnt lgkmcnt(5)\n\t" \
    "v_mfma_f32_16x16x32_bf16 v[72:75], a[192:195], v[24:27], v[72:75]\n\t" \
    "ds_read_b128 v[48:51], v23 offset:896\n\t" \
    "s_waitcnt lgkmcnt(5)\n\t" \
    "v_mfma_f32_16x16x32_bf16 v[76:79], a[192:195], v[28:31], v[76:79]\n\t" \
    "ds_read_b128 v[52:55], v23 offset:19328\n\t" \
    "v_mfma_f32_16x16x32_bf16 v[80:83], v[232:235], v[24:27], v[80:83]\n\t" \
    "s_addc_u32 s53, s53, 0\n\t" \
    "s_mul_i32 s70, s67, 7\n\t" \
    "v_mfma_f32_16x16x32_bf16 v[84:87], v[232:235], v[28:31], v[84:87]\n\t" \
    "s_add_u32 s69, s65, 1\n\t" \
    "s_xor_b32 s69, s69, s70\n\t" \
    "s_waitcnt lgkmcnt(5)\n\t" \
    "v_mfma_f32_16x16x32_bf16 v[72:75], a[196:199], v[32:35], v[72:75]\n\t" \
    "ds_read_b128 v[56:59], v22 offset:1024\n\t" \
    "s_waitcnt lgkmcnt(5)\n\t" \
    "v_mfma_f32_16x16x32_bf16 v[76:79], a[196:199], v[36:39], v[76:79]\n\t" \
    "ds_read_b128 v[60:63], v22 offset:19456\n\t" \
    "v_mfma_f32_16x16x32_bf16 v[80:83], v[236:239], v[32:35], v[80:83]\n\t" \
    "s_and_b32 s69, s69, 63\n\t" \
    "s_lshl_b32 s69, s69, 13\n\t" \
    "v_mfma_f32_16x16x32_bf16 v[84:87], v[236:239], v[36:39], v[84:87]\n\t" \
    "s_mul_i32 s70, s67, 524288\n\t" \
    "s_add_u32 s69, s69, s70\n\t" \
    "s_waitcnt lgkmcnt(5)\n\t" \
    "v_mfma_f32_16x16x32_bf16 v[72:75], a[200:203], v[40:43], v[72:75]\n\t" \
    "ds_read_b128 v[64:67], v23 offset:1024\n\t" \
    "s_waitcnt lgkmcnt(5)\n\t" \
    "v_mfma_f32_16x16x32_bf16 v[76:79], a[200:203], v[44:47], v[76:79]\n\t" \
    "ds_read_b128 v[68:71], v23 offset:19456\n\t" \
    "v_mfma_f32_16x16x32_bf16 v[80:83], v[240:243], v[40:43], v[80:83]\n\t" \
    "s_add_u32 s69, s69, 23068672\n\t" \
    "s_add_u32 s40, s36, s69\n\t" \
    "v_mfma_f32_16x16x32_bf16 v[84:87], v[240:243], v[44:47], v[84:87]\n\t" \
    "s_addc_u32 s41, s37, 0\n\t" \
    "s_sub_u32 s91, s85, s91\n\t" \
    "s_waitcnt lgkmcnt(5)\n\t" \
    "v_mfma_f32_16x16x32_bf16 v[72:75], a[204:207], v[48:51], v[72:75]\n\t" \
    "ds_read_b128 v[24:27], v1 offset:0\n\t" \
    "s_waitcnt lgkmcnt(5)\n\t" \
    "v_mfma_f32_16x16x32_bf16 v[76:79], a[204:207], v[52:55], v[76:79]\n\t" \
    "ds_read_b128 v[28:31], v1 offset:18432\n\t" \
    "v_mfma_f32_16x16x32_bf16 v[80:83], v[244:247], v[48:51], v[80:83]\n\t" \
    "v_mfma_f32_16x16x32_bf16 v[84:87], v[244:247], v[52:55], v[84:87]\n\t" \
    "s_waitcnt lgkmcnt(5)\n\t" \
    "v_mfma_f32_16x16x32_bf16 v[72:75], a[208:211], v[56:59], v[72:75]\n\t" \
    "ds_read_b128 v[32:35], v2 offset:0\n\t" \
    "s_waitcnt lgkmcnt(5)\n\t" \
    "v_mfma_f32_16x16x32_bf16 v[76:79], a[208:211], v[60:63], v[76:79]\n\t" \
    "ds_read_b128 v[36:39], v2 offset:18432\n\t" \
    "v_mfma_f32_16x16x32_bf16 v[80:83], v[248:251], v[56:59], v[80:83]\n\t" \
    "v_mfma_f32_16x16x32_bf16 v[84:87], v[248:251], v[60:63], v[84:87]\n\t" \
    "s_waitcnt lgkmcnt(5)\n\t" \
    "v_mfma_f32_16x16x32_bf16 v[72:75], a[212:215], v[64:67], v[72:75]\n\t" \
    "ds_read_b128 v[40:43], v1 offset:128\n\t" \
    "s_waitcnt lgkmcnt(5)\n\t" \
    "v_mfma_f32_16x16x32_bf16 v[76:79], a[212:215], v[68:71], v[76:79]\n\t" \
    "ds_read_b128 v[44:47], v1 offset:18560\n\t" \
    "v_mfma_f32_16x16x32_bf16 v[80:83], v[252:255], v[64:67], v[80:83]\n\t" \
    "v_mfma_f32_16x16x32_bf16 v[84:87], v[252:255], v[68:71], v[84:87]\n\t" \
    "s_lshl_b32 s69, s65, 6\n\t" \
    "s_lshl_b32 s70, s66, 5\n\t" \
    "s_add_u32 s69, s69, s70\n\t" \
    "v_lshrrev_b32 v156, 3, v6\n\t" \
    "v_add_u32 v156, s69, v156\n\t" \
    "v_min_u32 v156, 575, v156\n\t" \
    "v_and_b32 v157, 4, v6\n\t" \
    "v_lshlrev_b32 v157, 5, v157\n\t" \
    "v_lshl_add_u32 v156, v156, 13, v157\n\t" \
    "s_lshl_b32 s69, s67, 8\n\t" \
    "v_add_u32 v156, s69, v156\n\t" \
    "s_mov_b32 m0, 155648\n\t" \
    "s_nop 0\n\t" \
    "global_load_lds_dword v156, s[44:45]\n\t" \
    "s_add_u32 s65, s65, 2\n\t" \
    "s_cmp_lt_u32 s65, 8\n\t" \
    "s_cbranch_scc1 gx_l16_%=\n\t" \
    "v_cvt_pk_bf16_f32 v104, v72, v73\n\t" \
    "v_cvt_pk_bf16_f32 v105, v74, v75\n\t" \
    "v_cvt_pk_bf16_f32 v106, v80, v81\n\t" \
    "v_cvt_pk_bf16_f32 v107, v82, v83\n\t" \
    "global_store_dwordx4 v8, v[104:107], s[40:41] offset:0 sc1\n\t" \
    "v_cvt_pk_bf16_f32 v108, v76, v77\n\t" \
    "v_cvt_pk_bf16_f32 v109, v78, v79\n\t" \
    "v_cvt_pk_bf16_f32 v110, v84, v85\n\t" \
    "v_cvt_pk_bf16_f32 v111, v86, v87\n\t" \
    "global_store_dwordx4 v8, v[108:111], s[40:41] offset:1024 sc1\n\t" \
    "s_waitcnt vmcnt(0) lgkmcnt(0)\n\t" \
    "s_barrier\n\t" \
    "s_cmp_lg_u32 s66, 0\n\t" \
    "s_cbranch_scc1 gx_SF12_%=\n\t" \
    "s_lshl_b32 s69, s67, 7\n\t" \
    "s_add_u32 s69, s69, 3149824\n\t" \
    "s_add_u32 s70, s65, -1\n\t" \
    "v_mov_b32 v156, s69\n\t" \
    "v_mov_b32 v157, s70\n\t" \
    "s_mov_b64 exec, 1\n\t" \
    "global_store_dword v156, v157, s[100:101] sc1\n\t" \
    "s_mov_b64 exec, -1\n\t" \
    "gx_SF12_%=:\n\t" \
    "s_add_u32 s46, s44, 0\n\t" \
    "s_addc_u32 s47, s45, 0\n\t" \
    "s_add_u32 m0, s82, 0\n\t" \
    "s_nop 0\n\t" \
    "global_load_lds_dwordx4 v200, s[46:47]\n\t" \
    "s_add_u32 s46, s44, 131072\n\t" \
    "s_addc_u32 s47, s45, 0\n\t" \
    "s_add_u32 m0, s82, 4096\n\t" \
    "s_nop 0\n\t" \
    "global_load_lds_dwordx4 v200, s[46:47]\n\t" \
    "s_add_u32 s46, s44, 262144\n\t" \
    "s_addc_u32 s47, s45, 0\n\t" \
    "s_add_u32 m0, s82, 8192\n\t" \
    "s_nop 0\n\t" \
    "global_load_lds_dwordx4 v200, s[46:47]\n\t" \
    "s_add_u32 s46, s44, 393216\n\t" \
    "s_addc_u32 s47, s45, 0\n\t" \
    "s_add_u32 m0, s82, 12288\n\t" \
    "s_nop 0\n\t" \
    "global_load_lds_dwordx4 v200, s[46:47]\n\t" \
    "s_add_u32 s46, s44, 524288\n\t" \
    "s_addc_u32 s47, s45, 0\n\t" \
    "s_add_u32 m0, s82, 16384\n\t" \
    "s_nop 0\n\t" \
    "global_load_lds_dwordx4 v200, s[46:47]\n\t" \
    "s_add_u32 s46, s44, 655360\n\t" \
    "s_addc_u32 s47, s45, 0\n\t" \
    "s_add_u32 m0, s82, 20480\n\t" \
    "s_nop 0\n\t" \
    "global_load_lds_dwordx4 v200, s[46:47]\n\t" \
    "s_add_u32 s46, s44, 786432\n\t" \
    "s_addc_u32 s47, s45, 0\n\t" \
    "s_add_u32 m0, s82, 24576\n\t" \
    "s_nop 0\n\t" \
    "global_load_lds_dwordx4 v200, s[46:47]\n\t" \
    "s_add_u32 s46, s44, 917504\n\t" \
    "s_addc_u32 s47, s45, 0\n\t" \
    "s_add_u32 m0, s82, 28672\n\t" \
    "s_nop 0\n\t" \
    "global_load_lds_dwordx4 v200, s[46:47]\n\t" \
    "s_add_u32 s46, s44, 1048576\n\t" \
    "s_addc_u32 s47, s45, 0\n\t" \
    "s_add_u32 m0, s82, 32768\n\t" \
    "s_nop 0\n\t" \
    "global_load_lds_dwordx4 v200, s[46:47]\n\t" \
    "s_add_u32 s46, s44, 1179648\n\t" \
    "s_addc_u32 s47, s45, 0\n\t" \
    "s_add_u32 m0, s82, 36864\n\t" \
    "s_nop 0\n\t" \
    "global_load_lds_dwordx4 v200, s[46:47]\n\t" \
    "s_add_u32 s46, s44, 1310720\n\t" \
    "s_addc_u32 s47, s45, 0\n\t" \
    "s_add_u32 m0, s82, 40960\n\t" \
    "s_nop 0\n\t" \
    "global_load_lds_dwordx4 v200, s[46:47]\n\t" \
    "s_add_u32 s46, s44, 1441792\n\t" \
    "s_addc_u32 s47, s45, 0\n\t" \
    "s_add_u32 m0, s82, 45056\n\t" \
    "s_nop 0\n\t" \
    "global_load_lds_dwordx4 v200, s[46:47]\n\t" \
    "s_add_u32 s46, s44, 1572864\n\t" \
    "s_addc_u32 s47, s45, 0\n\t" \
    "s_add_u32 m0, s82, 49152\n\t" \
    "s_nop 0\n\t" \
    "global_load_lds_dwordx4 v200, s[46:47]\n\t" \
    "s_add_u32 s46, s44, 1703936\n\t" \
    "s_addc_u32 s47, s45, 0\n\t" \
    "s_add_u32 m0, s82, 53248\n\t" \
    "s_nop 0\n\t" \
    "global_load_lds_dwordx4 v200, s[46:47]\n\t" \
    "s_add_u32 s46, s44, 1835008\n\t" \
    "s_addc_u32 s47, s45, 0\n\t" \
    "s_add_u32 m0, s82, 57344\n\t" \
    "s_nop 0\n\t" \
    "global_load_lds_dwordx4 v200, s[46:47]\n\t" \
    "s_add_u32 s46, s44, 1966080\n\t" \
    "s_addc_u32 s47, s45, 0\n\t" \
    "s_add_u32 m0, s82, 61440\n\t" \
    "s_nop 0\n\t" \
    "global_load_lds_dwordx4 v200, s[46:47]\n\t" \
    "s_add_u32 s46, s44, 2097152\n\t" \
    "s_addc_u32 s47, s45, 0\n\t" \
    "s_add_u32 m0, s82, 65536\n\t" \
    "s_nop 0\n\t" \
    "global_load_lds_dwordx4 v200, s[46:47]\n\t" \
    "s_add_u32 s46, s44, 2228224\n\t" \
    "s_addc_u32 s47, s45, 0\n\t" \
    "s_add_u32 m0, s82, 69632\n\t" \
    "s_nop 0\n\t" \
    "global_load_lds_dwordx4 v200, s[46:47]\n\t" \
    "s_add_u32 s46, s44, 2359296\n\t" \
    "s_addc_u32 s47, s45, 0\n\t" \
    "s_add_u32 m0, s82, 73728\n\t" \
    "s_nop 0\n\t" \
    "global_load_lds_dwordx4 v200, s[46:47]\n\t" \
    "s_add_u32 s46, s44, 2490368\n\t" \
    "s_addc_u32 s47, s45, 0\n\t" \
    "s_add_u32 m0, s82, 77824\n\t" \
    "s_nop 0\n\t" \
    "global_load_lds_dwordx4 v200, s[46:47]\n\t" \
    "s_add_u32 s46, s44, 2621440\n\t" \
    "s_addc_u32 s47, s45, 0\n\t" \
    "s_add_u32 m0, s82, 81920\n\t" \
    "s_nop 0\n\t" \
    "global_load_lds_dwordx4 v200, s[46:47]\n\t" \
    "s_add_u32 s46, s44, 2752512\n\t" \
    "s_addc_u32 s47, s45, 0\n\t" \
    "s_add_u32 m0, s82, 86016\n\t" \
    "s_nop 0\n\t" \
    "global_load_lds_dwordx4 v200, s[46:47]\n\t" \
    "s_add_u32 s46, s44, 2883584\n\t" \
    "s_addc_u32 s47, s45, 0\n\t" \
    "s_add_u32 m0, s82, 90112\n\t" \
    "s_nop 0\n\t" \
    "global_load_lds_dwordx4 v200, s[46:47]\n\t" \
    "s_add_u32 s46, s44, 3014656\n\t" \
    "s_addc_u32 s47, s45, 0\n\t" \
    "s_add_u32 m0, s82, 94208\n\t" \
    "s_nop 0\n\t" \
    "global_load_lds_dwordx4 v200, s[46:47]\n\t" \
    "s_add_u32 s46, s44, 3145728\n\t" \
    "s_addc_u32 s47, s45, 0\n\t" \
    "s_add_u32 m0, s82, 98304\n\t" \
    "s_nop 0\n\t" \
    "global_load_lds_dwordx4 v200, s[46:47]\n\t" \
    "s_add_u32 s46, s44, 3276800\n\t" \
    "s_addc_u32 s47, s45, 0\n\t" \
    "s_add_u32 m0, s82, 102400\n\t" \
    "s_nop 0\n\t" \
    "global_load_lds_dwordx4 v200, s[46:47]\n\t" \
    "s_add_u32 s46, s44, 3407872\n\t" \
    "s_addc_u32 s47, s45, 0\n\t" \
    "s_add_u32 m0, s82, 106496\n\t" \
    "s_nop 0\n\t" \
    "global_load_lds_dwordx4 v200, s[46:47]\n\t" \
    "s_add_u32 s46, s44, 3538944\n\t" \
    "s_addc_u32 s47, s45, 0\n\t" \
    "s_add_u32 m0, s82, 110592\n\t" \
    "s_nop 0\n\t" \
    "global_load_lds_dwordx4 v200, s[46:47]\n\t" \
    "s_add_u32 s46, s44, 3670016\n\t" \
    "s_addc_u32 s47, s45, 0\n\t" \
    "s_add_u32 m0, s82, 114688\n\t" \
    "s_nop 0\n\t" \
    "global_load_lds_dwordx4 v200, s[46:47]\n\t" \
    "s_add_u32 s46, s44, 3801088\n\t" \
    "s_addc_u32 s47, s45, 0\n\t" \
    "s_add_u32 m0, s82, 118784\n\t" \
    "s_nop 0\n\t" \
    "global_load_lds_dwordx4 v200, s[46:47]\n\t" \
    "s_add_u32 s46, s44, 3932160\n\t" \
    "s_addc_u32 s47, s45, 0\n\t" \
    "s_add_u32 m0, s82, 122880\n\t" \
    "s_nop 0\n\t" \
    "global_load_lds_dwordx4 v200, s[46:47]\n\t" \
    "s_add_u32 s46, s44, 4063232\n\t" \
    "s_addc_u32 s47, s45, 0\n\t" \
    "s_add_u32 m0, s82, 126976\n\t" \
    "s_nop 0\n\t" \
    "global_load_lds_dwordx4 v200, s[46:47]\n\t" \
    "s_add_u32 s46, s44, 4194304\n\t" \
    "s_addc_u32 s47, s45, 0\n\t" \
    "s_add_u32 m0, s82, 131072\n\t" \
    "s_nop 0\n\t" \
    "global_load_lds_dwordx4 v200, s[46:47]\n\t" \
    "s_add_u32 s46, s44, 4325376\n\t" \
    "s_addc_u32 s47, s45, 0\n\t" \
    "s_add_u32 m0, s82, 135168\n\t" \
    "s_nop 0\n\t" \
    "global_load_lds_dwordx4 v200, s[46:47]\n\t" \
    "s_add_u32 s46, s44, 4456448\n\t" \
    "s_addc_u32 s47, s45, 0\n\t" \
    "s_add_u32 m0, s82, 139264\n\t" \
    "s_nop 0\n\t" \
    "global_load_lds_dwordx4 v200, s[46:47]\n\t" \
    "s_add_u32 s46, s44, 4587520\n\t" \
    "s_addc_u32 s47, s45, 0\n\t" \
    "s_add_u32 m0, s82, 143360\n\t" \
    "s_nop 0\n\t" \
    "global_load_lds_dwordx4 v200, s[46:47]\n\t" \
    "global_load_dwordx4 v[120:123], v203, s[50:51]\n\t" \
    "global_load_dwordx4 v[124:127], v204, s[50:51]\n\t" \
    "global_load_dwordx4 v[128:131], v205, s[50:51]\n\t" \
    "global_load_dwordx4 v[132:135], v206, s[50:51]\n\t" \
    "global_load_dwordx4 v[136:139], v207, s[50:51]\n\t" \
    "global_load_dwordx4 v[140:143], v208, s[50:51]\n\t" \
    "global_load_dwordx4 v[144:147], v209, s[50:51]\n\t" \
    "global_load_dwordx4 v[148:151], v210, s[50:51]\n\t" \
    "global_load_dwordx4 v[152:155], v211, s[50:51]\n\t" \
    "s_add_u32 s52, s50, 294912\n\t" \
    "s_addc_u32 s53, s51, 0\n\t" \
    "global_load_dwordx4 v[72:75], v203, s[52:53]\n\t" \
    "global_load_dwordx4 v[76:79], v204, s[52:53]\n\t" \
    "global_load_dwordx4 v[80:83], v205, s[52:53]\n\t" \
    "global_load_dwordx4 v[84:87], v206, s[52:53]\n\t" \
    "global_load_dwordx4 v[88:91], v207, s[52:53]\n\t" \
    "global_load_dwordx4 v[92:95], v208, s[52:53]\n\t" \
    "global_load_dwordx4 v[96:99], v209, s[52:53]\n\t" \
    "global_load_dwordx4 v[100:103], v210, s[52:53]\n\t" \
    "global_load_dwordx4 v[104:107], v211, s[52:53]\n\t" \
    "s_waitcnt vmcnt(50) lgkmcnt(0)\n\t" \
    "s_barrier\n\t" \
    "ds_read_b64_tr_b16 a[0:1], v201 offset:0\n\t" \
    "ds_read_b64_tr_b16 a[2:3], v201 offset:1024\n\t" \
    "ds_read_b64_tr_b16 a[4:5], v201 offset:8192\n\t" \
    "ds_read_b64_tr_b16 a[6:7], v201 offset:9216\n\t" \
    "ds_read_b64_tr_b16 a[72:73], v202 offset:0\n\t" \
    "ds_read_b64_tr_b16 a[74:75], v202 offset:1024\n\t" \
    "ds_read_b64_tr_b16 a[76:77], v202 offset:8192\n\t" \
    "ds_read_b64_tr_b16 a[78:79], v202 offset:9216\n\t" \
    "s_waitcnt vmcnt(46) lgkmcnt(0)\n\t" \
    "s_barrier\n\t" \
    "ds_read_b64_tr_b16 a[8:9], v201 offset:16384\n\t" \
    "ds_read_b64_tr_b16 a[10:11], v201 offset:17408\n\t" \
    "ds_read_b64_tr_b16 a[12:13], v201 offset:24576\n\t" \
    "ds_read_b64_tr_b16 a[14:15], v201 offset:25600\n\t" \
    "ds_read_b64_tr_b16 a[80:81], v202 offset:16384\n\t" \
    "ds_read_b64_tr_b16 a[82:83], v202 offset:17408\n\t" \
    "ds_read_b64_tr_b16 a[84:85], v202 offset:24576\n\t" \
    "ds_read_b64_tr_b16 a[86:87], v202 offset:25600\n\t" \
    "s_waitcnt vmcnt(42) lgkmcnt(0)\n\t" \
    "s_barrier\n\t" \
    "ds_read_b64_tr_b16 a[16:17], v201 offset:32768\n\t" \
    "ds_read_b64_tr_b16 a[18:19], v201 offset:33792\n\t" \
    "ds_read_b64_tr_b16 a[20:21], v201 offset:40960\n\t" \
    "ds_read_b64_tr_b16 a[22:23], v201 offset:41984\n\t" \
    "ds_read_b64_tr_b16 a[88:89], v202 offset:32768\n\t" \
    "ds_read_b64_tr_b16 a[90:91], v202 offset:33792\n\t" \
    "ds_read_b64_tr_b16 a[92:93], v202 offset:40960\n\t" \
    "ds_read_b64_tr_b16 a[94:95], v202 offset:41984\n\t" \
    "s_waitcnt vmcnt(38) lgkmcnt(0)\n\t" \
    "s_barrier\n\t" \
    "ds_read_b64_tr_b16 a[24:25], v201 offset:49152\n\t" \
    "ds_read_b64_tr_b16 a[26:27], v201 offset:50176\n\t" \
    "ds_read_b64_tr_b16 a[28:29], v201 offset:57344\n\t" \
    "ds_read_b64_tr_b16 a[30:31], v201 offset:58368\n\t" \
    "ds_read_b64_tr_b16 a[96:97], v202 offset:49152\n\t" \
    "ds_read_b64_tr_b16 a[98:99], v202 offset:50176\n\t" \
    "ds_read_b64_tr_b16 a[100:101], v202 offset:57344\n\t" \
    "ds_read_b64_tr_b16 a[102:103], v202 offset:58368\n\t" \
    "s_waitcnt vmcnt(34) lgkmcnt(0)\n\t" \
    "s_barrier\n\t" \
    "v_add_u32 v201, 0x10000, v201\n\t" \
    "v_add_u32 v202, 0x10000, v202\n\t" \
    "ds_read_b64_tr_b16 a[32:33], v201 offset:0\n\t" \
    "ds_read_b64_tr_b16 a[34:35], v201 offset:1024\n\t" \
    "ds_read_b64_tr_b16 a[36:37], v201 offset:8192\n\t" \
    "ds_read_b64_tr_b16 a[38:39], v201 offset:9216\n\t" \
    "ds_read_b64_tr_b16 a[104:105], v202 offset:0\n\t" \
    "ds_read_b64_tr_b16 a[106:107], v202 offset:1024\n\t" \
    "ds_read_b64_tr_b16 a[108:109], v202 offset:8192\n\t" \
    "ds_read_b64_tr_b16 a[110:111], v202 offset:9216\n\t" \
    "s_waitcnt vmcnt(30) lgkmcnt(0)\n\t" \
    "s_barrier\n\t" \
    "ds_read_b64_tr_b16 a[40:41], v201 offset:16384\n\t" \
    "ds_read_b64_tr_b16 a[42:43], v201 offset:17408\n\t" \
    "ds_read_b64_tr_b16 a[44:45], v201 offset:24576\n\t" \
    "ds_read_b64_tr_b16 a[46:47], v201 offset:25600\n\t" \
    "ds_read_b64_tr_b16 a[112:113], v202 offset:16384\n\t" \
    "ds_read_b64_tr_b16 a[114:115], v202 offset:17408\n\t" \
    "ds_read_b64_tr_b16 a[116:117], v202 offset:24576\n\t" \
    "ds_read_b64_tr_b16 a[118:119], v202 offset:25600\n\t" \
    "s_waitcnt vmcnt(26) lgkmcnt(0)\n\t" \
    "s_barrier\n\t" \
    "ds_read_b64_tr_b16 a[48:49], v201 offset:32768\n\t" \
    "ds_read_b64_tr_b16 a[50:51], v201 offset:33792\n\t" \
    "ds_read_b64_tr_b16 a[52:53], v201 offset:40960\n\t" \
    "ds_read_b64_tr_b16 a[54:55], v201 offset:41984\n\t" \
    "ds_read_b64_tr_b16 a[120:121], v202 offset:32768\n\t" \
    "ds_read_b64_tr_b16 a[122:123], v202 offset:33792\n\t" \
    "ds_read_b64_tr_b16 a[124:125], v202 offset:40960\n\t" \
    "ds_read_b64_tr_b16 a[126:127], v202 offset:41984\n\t" \
    "s_waitcnt vmcnt(22) lgkmcnt(0)\n\t" \
    "s_barrier\n\t" \
    "ds_read_b64_tr_b16 a[56:57], v201 offset:49152\n\t" \
    "ds_read_b64_tr_b16 a[58:59], v201 offset:50176\n\t" \
    "ds_read_b64_tr_b16 a[60:61], v201 offset:57344\n\t" \
    "ds_read_b64_tr_b16 a[62:63], v201 offset:58368\n\t" \
    "ds_read_b64_tr_b16 a[128:129], v202 offset:49152\n\t" \
    "ds_read_b64_tr_b16 a[130:131], v202 offset:50176\n\t" \
    "ds_read_b64_tr_b16 a[132:133], v202 offset:57344\n\t" \
    "ds_read_b64_tr_b16 a[134:135], v202 offset:58368\n\t" \
    "s_waitcnt vmcnt(18) lgkmcnt(0)\n\t" \
    "s_barrier\n\t" \
    "v_add_u32 v201, 0x10000, v201\n\t" \
    "v_add_u32 v202, 0x10000, v202\n\t" \
    "ds_read_b64_tr_b16 a[64:65], v201 offset:0\n\t" \
    "ds_read_b64_tr_b16 a[66:67], v201 offset:1024\n\t" \
    "ds_read_b64_tr_b16 a[68:69], v201 offset:8192\n\t" \
    "ds_read_b64_tr_b16 a[70:71], v201 offset:9216\n\t" \
    "ds_read_b64_tr_b16 a[136:137], v202 offset:0\n\t" \
    "ds_read_b64_tr_b16 a[138:139], v202 offset:1024\n\t" \
    "ds_read_b64_tr_b16 a[140:141], v202 offset:8192\n\t" \
    "ds_read_b64_tr_b16 a[142:143], v202 offset:9216\n\t" \
    "v_add_u32 v201, 0xfffe0000, v201\n\t" \
    "v_add_u32 v202, 0xfffe0000, v202\n\t" \
    "s_waitcnt lgkmcnt(0)\n\t" \
    "s_barrier\n\t" \
    "s_waitcnt vmcnt(0)\n\t" \
    "ds_write_b128 v3, v[120:123] offset:0\n\t" \
    "ds_write_b128 v3, v[124:127] offset:1024\n\t" \
    "ds_write_b128 v3, v[128:131] offset:2048\n\t" \
    "ds_write_b128 v3, v[132:135] offset:3072\n\t" \
    "ds_write_b128 v3, v[136:139] offset:4096\n\t" \
    "ds_write_b128 v3, v[140:143] offset:5120\n\t" \
    "ds_write_b128 v3, v[144:147] offset:6144\n\t" \
    "ds_write_b128 v3, v[148:151] offset:7168\n\t" \
    "ds_write_b128 v3, v[152:155] offset:8192\n\t" \
    "ds_write_b128 v3, v[72:75] offset:36864\n\t" \
    "ds_write_b128 v3, v[76:79] offset:37888\n\t" \
    "ds_write_b128 v3, v[80:83] offset:38912\n\t" \
    "ds_write_b128 v3, v[84:87] offset:39936\n\t" \
    "ds_write_b128 v3, v[88:91] offset:40960\n\t" \
    "ds_write_b128 v3, v[92:95] offset:41984\n\t" \
    "ds_write_b128 v3, v[96:99] offset:43008\n\t" \
    "ds_write_b128 v3, v[100:103] offset:44032\n\t" \
    "ds_write_b128 v3, v[104:107] offset:45056\n\t" \
    "s_mov_b32 s65, 0\n\t" \
    "v_mov_b32 v1, v0\n\t" \
    "v_xor_b32 v2, 64, v0\n\t" \
    "s_mov_b32 s90, 0\n\t" \
    "s_add_u32 s91, s89, 0x12000\n\t" \
    "s_add_u32 s85, s91, s89\n\t" \
    "s_add_u32 s52, s50, 36864\n\t" \
    "s_addc_u32 s53, s51, 0\n\t" \
    "s_mul_i32 s69, s67, 8192\n\t" \
    "s_add_u32 s69, s69, 175112192\n\t" \
    "s_add_u32 s42, s36, s69\n\t" \
    "s_addc_u32 s43, s37, 0\n\t" \
    "s_mov_b64 s[40:41], s[42:43]\n\t" \
    "s_waitcnt lgkmcnt(0)\n\t" \
    "s_barrier\n\t" \
    "ds_read_b128 v[24:27], v1 offset:0\n\t" \
    "ds_read_b128 v[28:31], v1 offset:18432\n\t" \
    "ds_read_b128 v[32:35], v2 offset:0\n\t" \
    "ds_read_b128 v[36:39], v2 offset:18432\n\t" \
    "ds_read_b128 v[40:43], v1 offset:128\n\t" \
    "ds_read_b128 v[44:47], v1 offset:18560\n\t" \
    ".p2align 6\n\t" \
    "s_nop 0\n\t" \
    "s_nop 0\n\t" \
    "gx_l27_%=:\n\t" \
    "s_waitcnt lgkmcnt(5)\n\t" \
    "v_mfma_f32_16x16x32_bf16 v[88:91], a[0:3], v[24:27], 0\n\t" \
    "ds_read_b128 v[48:51], v2 offset:128\n\t" \
    "s_waitcnt lgkmcnt(5)\n\t" \
    "v_mfma_f32_16x16x32_bf16 v[92:95], a[0:3], v[28:31], 0\n\t" \
    "ds_read_b128 v[52:55], v2 offset:18560\n\t" \
    "v_mfma_f32_16x16x32_bf16 v[96:99], a[72:75], v[24:27], 0\n\t" \
    "s_add_u32 m0, s91, 0\n\t" \
    "v_mfma_f32_16x16x32_bf16 v[100:103], a[72:75], v[28:31], 0\n\t" \
    "global_load_lds_dwordx4 v203, s[52:53]\n\t" \
    "s_waitcnt lgkmcnt(5)\n\t" \
    "v_mfma_f32_16x16x32_bf16 v[88:91], a[4:7], v[32:35], v[88:91]\n\t" \
    "ds_read_b128 v[56:59], v1 offset:256\n\t" \
    "s_waitcnt lgkmcnt(5)\n\t" \
    "v_mfma_f32_16x16x32_bf16 v[92:95], a[4:7], v[36:39], v[92:95]\n\t" \
    "ds_read_b128 v[60:63], v1 offset:18688\n\t" \
    "v_mfma_f32_16x16x32_bf16 v[96:99], a[76:79], v[32:35], v[96:99]\n\t" \
    "s_add_u32 m0, s91, 1024\n\t" \
    "v_mfma_f32_16x16x32_bf16 v[100:103], a[76:79], v[36:39], v[100:103]\n\t" \
    "global_load_lds_dwordx4 v204, s[52:53]\n\t" \
    "s_waitcnt lgkmcnt(5)\n\t" \
    "v_mfma_f32_16x16x32_bf16 v[88:91], a[8:11], v[40:43], v[88:91]\n\t" \
    "ds_read_b128 v[64:67], v2 offset:256\n\t" \
    "s_waitcnt lgkmcnt(5)\n\t" \
    "v_mfma_f32_16x16x32_bf16 v[92:95], a[8:11], v[44:47], v[92:95]\n\t" \
    "ds_read_b128 v[68:71], v2 offset:18688\n\t" \
    "v_cvt_pk_bf16_f32 v104, v72, v73\n\t" \
    "v_mfma_f32_16x16x32_bf16 v[96:99], a[80:83], v[40:43], v[96:99]\n\t" \
    "s_add_u32 m0, s91, 2048\n\t" \
    "v_cvt_pk_bf16_f32 v105, v74, v75\n\t" \
    "v_mfma_f32_16x16x32_bf16 v[100:103], a[80:83], v[44:47], v[100:103]\n\t" \
    "global_load_lds_dwordx4 v205, s[52:53]\n\t" \
    "s_waitcnt lgkmcnt(5)\n\t" \
    "v_mfma_f32_16x16x32_bf16 v[88:91], a[12:15], v[48:51], v[88:91]\n\t" \
    "ds_read_b128 v[24:27], v1 offset:384\n\t" \
    "s_waitcnt lgkmcnt(5)\n\t" \
    "v_mfma_f32_16x16x32_bf16 v[92:95], a[12:15], v[52:55], v[92:95]\n\t" \
    "ds_read_b128 v[28:31], v1 offset:18816\n\t" \
    "v_cvt_pk_bf16_f32 v106, v80, v81\n\t" \
    "v_mfma_f32_16x16x32_bf16 v[96:99], a[84:87], v[48:51], v[96:99]\n\t" \
    "s_add_u32 m0, s91, 3072\n\t" \
    "v_cvt_pk_bf16_f32 v107, v82, v83\n\t" \
    "v_mfma_f32_16x16x32_bf16 v[100:103], a[84:87], v[52:55], v[100:103]\n\t" \
    "global_load_lds_dwordx4 v206, s[52:53]\n\t" \
    "s_waitcnt lgkmcnt(5)\n\t" \
    "v_mfma_f32_16x16x32_bf16 v[88:91], a[16:19], v[56:59], v[88:91]\n\t" \
    "ds_read_b128 v[32:35], v2 offset:384\n\t" \
    "s_waitcnt lgkmcnt(5)\n\t" \
    "v_mfma_f32_16x16x32_bf16 v[92:95], a[16:19], v[60:63], v[92:95]\n\t" \
    "ds_read_b128 v[36:39], v2 offset:18816\n\t" \
    "global_store_dwordx4 v8, v[104:107], s[40:41] offset:0 sc1\n\t" \
    "v_mfma_f32_16x16x32_bf16 v[96:99], a[88:91], v[56:59], v[96:99]\n\t" \
    "s_add_u32 m0, s91, 4096\n\t" \
    "v_cvt_pk_bf16_f32 v108, v76, v77\n\t" \
    "v_mfma_f32_16x16x32_bf16 v[100:103], a[88:91], v[60:63], v[100:103]\n\t" \
    "global_load_lds_dwordx4 v207, s[52:53]\n\t" \
    "s_waitcnt lgkmcnt(5)\n\t" \
    "v_mfma_f32_16x16x32_bf16 v[88:91], a[20:23], v[64:67], v[88:91]\n\t" \
    "ds_read_b128 v[40:43], v1 offset:512\n\t" \
    "s_waitcnt lgkmcnt(5)\n\t" \
    "v_mfma_f32_16x16x32_bf16 v[92:95], a[20:23], v[68:71], v[92:95]\n\t" \
    "ds_read_b128 v[44:47], v1 offset:18944\n\t" \
    "v_cvt_pk_bf16_f32 v109, v78, v79\n\t" \
    "v_mfma_f32_16x16x32_bf16 v[96:99], a[92:95], v[64:67], v[96:99]\n\t" \
    "s_add_u32 m0, s91, 5120\n\t" \
    "v_cvt_pk_bf16_f32 v110, v84, v85\n\t" \
    "v_mfma_f32_16x16x32_bf16 v[100:103], a[92:95], v[68:71], v[100:103]\n\t" \
    "global_load_lds_dwordx4 v208, s[52:53]\n\t" \
    "s_waitcnt lgkmcnt(5)\n\t" \
    "v_mfma_f32_16x16x32_bf16 v[88:91], a[24:27], v[24:27], v[88:91]\n\t" \
    "ds_read_b128 v[48:51], v2 offset:512\n\t" \
    "s_waitcnt lgkmcnt(5)\n\t" \
    "v_mfma_f32_16x16x32_bf16 v[92:95], a[24:27], v[28:31], v[92:95]\n\t" \
    "ds_read_b128 v[52:55], v2 offset:18944\n\t" \
    "v_cvt_pk_bf16_f32 v111, v86, v87\n\t" \
    "v_mfma_f32_16x16x32_bf16 v[96:99], a[96:99], v[24:27], v[96:99]\n\t" \
    "s_add_u32 m0, s91, 6144\n\t" \
    "global_store_dwordx4 v8, v[108:111], s[40:41] offset:1024 sc1\n\t" \
    "v_mfma_f32_16x16x32_bf16 v[100:103], a[96:99], v[28:31], v[100:103]\n\t" \
    "global_load_lds_dwordx4 v209, s[52:53]\n\t" \
    "s_waitcnt lgkmcnt(5)\n\t" \
    "v_mfma_f32_16x16x32_bf16 v[88:91], a[28:31], v[32:35], v[88:91]\n\t" \
    "ds_read_b128 v[56:59], v1 offset:640\n\t" \
    "s_waitcnt lgkmcnt(5)\n\t" \
    "v_mfma_f32_16x16x32_bf16 v[92:95], a[28:31], v[36:39], v[92:95]\n\t" \
    "ds_read_b128 v[60:63], v1 offset:19072\n\t" \
    "v_mfma_f32_16x16x32_bf16 v[96:99], a[100:103], v[32:35], v[96:99]\n\t" \
    "s_add_u32 m0, s91, 7168\n\t" \
    "v_mfma_f32_16x16x32_bf16 v[100:103], a[100:103], v[36:39], v[100:103]\n\t" \
    "global_load_lds_dwordx4 v210, s[52:53]\n\t" \
    "s_waitcnt lgkmcnt(5)\n\t" \
    "v_mfma_f32_16x16x32_bf16 v[88:91], a[32:35], v[40:43], v[88:91]\n\t" \
    "ds_read_b128 v[64:67], v2 offset:640\n\t" \
    "s_waitcnt lgkmcnt(5)\n\t" \
    "v_mfma_f32_16x16x32_bf16 v[92:95], a[32:35], v[44:47], v[92:95]\n\t" \
    "ds_read_b128 v[68:71], v2 offset:19072\n\t" \
    "v_mfma_f32_16x16x32_bf16 v[96:99], a[104:107], v[40:43], v[96:99]\n\t" \
    "s_add_u32 m0, s91, 8192\n\t" \
    "v_mfma_f32_16x16x32_bf16 v[100:103], a[104:107], v[44:47], v[100:103]\n\t" \
    "global_load_lds_dwordx4 v211, s[52:53]\n\t" \
    "s_waitcnt lgkmcnt(5)\n\t" \
    "v_mfma_f32_16x16x32_bf16 v[88:91], a[36:39], v[48:51], v[88:91]\n\t" \
    "ds_read_b128 v[24:27], v1 offset:768\n\t" \
    "s_waitcnt lgkmcnt(5)\n\t" \
    "v_mfma_f32_16x16x32_bf16 v[92:95], a[36:39], v[52:55], v[92:95]\n\t" \
    "ds_read_b128 v[28:31], v1 offset:19200\n\t" \
    "v_mfma_f32_16x16x32_bf16 v[96:99], a[108:111], v[48:51], v[96:99]\n\t" \
    "v_mfma_f32_16x16x32_bf16 v[100:103], a[108:111], v[52:55], v[100:103]\n\t" \
    "s_waitcnt lgkmcnt(5)\n\t" \
    "v_mfma_f32_16x16x32_bf16 v[88:91], a[40:43], v[56:59], v[88:91]\n\t" \
    "ds_read_b128 v[32:35], v2 offset:768\n\t" \
    "s_waitcnt lgkmcnt(5)\n\t" \
    "v_mfma_f32_16x16x32_bf16 v[92:95], a[40:43], v[60:63], v[92:95]\n\t" \
    "ds_read_b128 v[36:39], v2 offset:19200\n\t" \
    "v_mfma_f32_16x16x32_bf16 v[96:99], a[112:115], v[56:59], v[96:99]\n\t" \
    "s_waitcnt vmcnt(11)\n\t" \
    "s_barrier\n\t" \
    "v_mfma_f32_16x16x32_bf16 v[100:103], a[112:115], v[60:63], v[100:103]\n\t" \
    "s_cmp_lg_u32 s66, 0\n\t" \
    "s_cbranch_scc1 gx_SF13_%=\n\t" \
    "s_lshl_b32 s69, s67, 7\n\t" \
    "s_add_u32 s69, s69, 3211264\n\t" \
    "s_add_u32 s70, s65, -1\n\t" \
    "v_mov_b32 v156, s69\n\t" \
    "v_mov_b32 v157, s70\n\t" \
    "s_mov_b64 exec, 1\n\t" \
    "global_store_dword v156, v157, s[100:101] sc1\n\t" \
    "s_mov_b64 exec, -1\n\t" \
    "gx_SF13_%=:\n\t" \
    "s_waitcnt lgkmcnt(5)\n\t" \
    "v_mfma_f32_16x16x32_bf16 v[88:91], a[44:47], v[64:67], v[88:91]\n\t" \
    "ds_read_b128 v[40:43], v1 offset:896\n\t" \
    "s_waitcnt lgkmcnt(5)\n\t" \
    "v_mfma_f32_16x16x32_bf16 v[92:95], a[44:47], v[68:71], v[92:95]\n\t" \
    "ds_read_b128 v[44:47], v1 offset:19328\n\t" \
    "v_mfma_f32_16x16x32_bf16 v[96:99], a[116:119], v[64:67], v[96:99]\n\t" \
    "v_add_u32 v22, 0x9000, v1\n\t" \
    "v_add_u32 v23, 0x9000, v2\n\t" \
    "v_mfma_f32_16x16x32_bf16 v[100:103], a[116:119], v[68:71], v[100:103]\n\t" \
    "s_cmp_lt_u32 s65, 55\n\t" \
    "s_cselect_b32 s84, 9, 2\n\t" \
    "s_waitcnt lgkmcnt(5)\n\t" \
    "v_mfma_f32_16x16x32_bf16 v[88:91], a[48:51], v[24:27], v[88:91]\n\t" \
    "ds_read_b128 v[48:51], v2 offset:896\n\t" \
    "s_waitcnt lgkmcnt(5)\n\t" \
    "v_mfma_f32_16x16x32_bf16 v[92:95], a[48:51], v[28:31], v[92:95]\n\t" \
    "ds_read_b128 v[52:55], v2 offset:19328\n\t" \
    "v_mfma_f32_16x16x32_bf16 v[96:99], a[120:123], v[24:27], v[96:99]\n\t" \
    "s_add_u32 s84, s65, s84\n\t" \
    "s_min_u32 s69, s84, 63\n\t" \
    "v_mfma_f32_16x16x32_bf16 v[100:103], a[120:123], v[28:31], v[100:103]\n\t" \
    "s_mul_i32 s69, s69, 36864\n\t" \
    "s_add_u32 s48, s50, s69\n\t" \
    "s_waitcnt lgkmcnt(5)\n\t" \
    "v_mfma_f32_16x16x32_bf16 v[88:91], a[52:55], v[32:35], v[88:91]\n\t" \
    "ds_read_b128 v[56:59], v1 offset:1024\n\t" \
    "s_waitcnt lgkmcnt(5)\n\t" \
    "v_mfma_f32_16x16x32_bf16 v[92:95], a[52:55], v[36:39], v[92:95]\n\t" \
    "ds_read_b128 v[60:63], v1 offset:19456\n\t" \
    "v_mfma_f32_16x16x32_bf16 v[96:99], a[124:127], v[32:35], v[96:99]\n\t" \
    "s_addc_u32 s49, s51, 0\n\t" \
    "s_mul_i32 s70, s67, 7\n\t" \
    "v_mfma_f32_16x16x32_bf16 v[100:103], a[124:127], v[36:39], v[100:103]\n\t" \
    "s_add_u32 s69, s65, 0\n\t" \
    "s_xor_b32 s69, s69, s70\n\t" \
    "s_waitcnt lgkmcnt(5)\n\t" \
    "v_mfma_f32_16x16x32_bf16 v[88:91], a[56:59], v[40:43], v[88:91]\n\t" \
    "ds_read_b128 v[64:67], v2 offset:1024\n\t" \
    "s_waitcnt lgkmcnt(5)\n\t" \
    "v_mfma_f32_16x16x32_bf16 v[92:95], a[56:59], v[44:47], v[92:95]\n\t" \
    "ds_read_b128 v[68:71], v2 offset:19456\n\t" \
    "v_mfma_f32_16x16x32_bf16 v[96:99], a[128:131], v[40:43], v[96:99]\n\t" \
    "s_and_b32 s69, s69, 63\n\t" \
    "s_lshl_b32 s69, s69, 13\n\t" \
    "v_mfma_f32_16x16x32_bf16 v[100:103], a[128:131], v[44:47], v[100:103]\n\t" \
    "s_mul_i32 s70, s67, 524288\n\t" \
    "s_add_u32 s69, s69, s70\n\t" \
    "s_waitcnt lgkmcnt(5)\n\t" \
    "v_mfma_f32_16x16x32_bf16 v[88:91], a[60:63], v[48:51], v[88:91]\n\t" \
    "ds_read_b128 v[24:27], v22 offset:0\n\t" \
    "s_waitcnt lgkmcnt(5)\n\t" \
    "v_mfma_f32_16x16x32_bf16 v[92:95], a[60:63], v[52:55], v[92:95]\n\t" \
    "ds_read_b128 v[28:31], v22 offset:18432\n\t" \
    "v_mfma_f32_16x16x32_bf16 v[96:99], a[132:135], v[48:51], v[96:99]\n\t" \
    "s_add_u32 s69, s69, 6291456\n\t" \
    "s_add_u32 s38, s36, s69\n\t" \
    "v_mfma_f32_16x16x32_bf16 v[100:103], a[132:135], v[52:55], v[100:103]\n\t" \
    "s_addc_u32 s39, s37, 0\n\t" \
    "s_waitcnt lgkmcnt(5)\n\t" \
    "v_mfma_f32_16x16x32_bf16 v[88:91], a[64:67], v[56:59], v[88:91]\n\t" \
    "ds_read_b128 v[32:35], v23 offset:0\n\t" \
    "s_waitcnt lgkmcnt(5)\n\t" \
    "v_mfma_f32_16x16x32_bf16 v[92:95], a[64:67], v[60:63], v[92:95]\n\t" \
    "ds_read_b128 v[36:39], v23 offset:18432\n\t" \
    "v_mfma_f32_16x16x32_bf16 v[96:99], a[136:139], v[56:59], v[96:99]\n\t" \
    "v_mfma_f32_16x16x32_bf16 v[100:103], a[136:139], v[60:63], v[100:103]\n\t" \
    "s_waitcnt lgkmcnt(5)\n\t" \
    "v_mfma_f32_16x16x32_bf16 v[88:91], a[68:71], v[64:67], v[88:91]\n\t" \
    "ds_read_b128 v[40:43], v22 offset:128\n\t" \
    "s_waitcnt lgkmcnt(5)\n\t" \
    "v_mfma_f32_16x16x32_bf16 v[92:95], a[68:71], v[68:71], v[92:95]\n\t" \
    "ds_read_b128 v[44:47], v22 offset:18560\n\t" \
    "v_mfma_f32_16x16x32_bf16 v[96:99], a[140:143], v[64:67], v[96:99]\n\t" \
    "v_mfma_f32_16x16x32_bf16 v[100:103], a[140:143], v[68:71], v[100:103]\n\t" \
    "s_waitcnt lgkmcnt(5)\n\t" \
    "v_mfma_f32_16x16x32_bf16 v[72:75], a[144:147], v[24:27], 0\n\t" \
    "ds_read_b128 v[48:51], v23 offset:128\n\t" \
    "s_waitcnt lgkmcnt(5)\n\t" \
    "v_mfma_f32_16x16x32_bf16 v[76:79], a[144:147], v[28:31], 0\n\t" \
    "ds_read_b128 v[52:55], v23 offset:18560\n\t" \
    "v_mfma_f32_16x16x32_bf16 v[80:83], a[216:219], v[24:27], 0\n\t" \
    "s_add_u32 m0, s91, 36864\n\t" \
    "v_mfma_f32_16x16x32_bf16 v[84:87], a[216:219], v[28:31], 0\n\t" \
    "global_load_lds_dwordx4 v203, s[48:49]\n\t" \
    "s_waitcnt lgkmcnt(5)\n\t" \
    "v_mfma_f32_16x16x32_bf16 v[72:75], a[148:151], v[32:35], v[72:75]\n\t" \
    "ds_read_b128 v[56:59], v22 offset:256\n\t" \
    "s_waitcnt lgkmcnt(5)\n\t" \
    "v_mfma_f32_16x16x32_bf16 v[76:79], a[148:151], v[36:39], v[76:79]\n\t" \
    "ds_read_b128 v[60:63], v22 offset:18688\n\t" \
    "v_mfma_f32_16x16x32_bf16 v[80:83], a[220:223], v[32:35], v[80:83]\n\t" \
    "s_add_u32 m0, s91, 37888\n\t" \
    "v_mfma_f32_16x16x32_bf16 v[84:87], a[220:223], v[36:39], v[84:87]\n\t" \
    "global_load_lds_dwordx4 v204, s[48:49]\n\t" \
    "s_waitcnt lgkmcnt(5)\n\t" \
    "v_mfma_f32_16x16x32_bf16 v[72:75], a[152:155], v[40:43], v[72:75]\n\t" \
    "ds_read_b128 v[64:67], v23 offset:256\n\t" \
    "s_waitcnt lgkmcnt(5)\n\t" \
    "v_mfma_f32_16x16x32_bf16 v[76:79], a[152:155], v[44:47], v[76:79]\n\t" \
    "ds_read_b128 v[68:71], v23 offset:18688\n\t" \
    "v_cvt_pk_bf16_f32 v104, v88, v89\n\t" \
    "v_mfma_f32_16x16x32_bf16 v[80:83], a[224:227], v[40:43], v[80:83]\n\t" \
    "s_add_u32 m0, s91, 38912\n\t" \
    "v_cvt_pk_bf16_f32 v105, v90, v91\n\t" \
    "v_mfma_f32_16x16x32_bf16 v[84:87], a[224:227], v[44:47], v[84:87]\n\t" \
    "global_load_lds_dwordx4 v205, s[48:49]\n\t" \
    "s_waitcnt lgkmcnt(5)\n\t" \
    "v_mfma_f32_16x16x32_bf16 v[72:75], a[156:159], v[48:51], v[72:75]\n\t" \
    "ds_read_b128 v[24:27], v22 offset:384\n\t" \
    "s_waitcnt lgkmcnt(5)\n\t" \
    "v_mfma_f32_16x16x32_bf16 v[76:79], a[156:159], v[52:55], v[76:79]\n\t" \
    "ds_read_b128 v[28:31], v22 offset:18816\n\t" \
    "v_cvt_pk_bf16_f32 v106, v96, v97\n\t" \
    "v_mfma_f32_16x16x32_bf16 v[80:83], a[228:231], v[48:51], v[80:83]\n\t" \
    "s_add_u32 m0, s91, 39936\n\t" \
    "v_cvt_pk_bf16_f32 v107, v98, v99\n\t" \
    "v_mfma_f32_16x16x32_bf16 v[84:87], a[228:231], v[52:55], v[84:87]\n\t" \
    "global_load_lds_dwordx4 v206, s[48:49]\n\t" \
    "s_waitcnt lgkmcnt(5)\n\t" \
    "v_mfma_f32_16x16x32_bf16 v[72:75], a[160:163], v[56:59], v[72:75]\n\t" \
    "ds_read_b128 v[32:35], v23 offset:384\n\t" \
    "s_waitcnt lgkmcnt(5)\n\t" \
    "v_mfma_f32_16x16x32_bf16 v[76:79], a[160:163], v[60:63], v[76:79]\n\t" \
    "ds_read_b128 v[36:39], v23 offset:18816\n\t" \
    "global_store_dwordx4 v8, v[104:107], s[38:39] offset:0 sc1\n\t" \
    "v_mfma_f32_16x16x32_bf16 v[80:83], a[232:235], v[56:59], v[80:83]\n\t" \
    "s_add_u32 m0, s91, 40960\n\t" \
    "v_cvt_pk_bf16_f32 v108, v92, v93\n\t" \
    "v_mfma_f32_16x16x32_bf16 v[84:87], a[232:235], v[60:63], v[84:87]\n\t" \
    "global_load_lds_dwordx4 v207, s[48:49]\n\t" \
    "s_waitcnt lgkmcnt(5)\n\t" \
    "v_mfma_f32_16x16x32_bf16 v[72:75], a[164:167], v[64:67], v[72:75]\n\t" \
    "ds_read_b128 v[40:43], v22 offset:512\n\t" \
    "s_waitcnt lgkmcnt(5)\n\t" \
    "v_mfma_f32_16x16x32_bf16 v[76:79], a[164:167], v[68:71], v[76:79]\n\t" \
    "ds_read_b128 v[44:47], v22 offset:18944\n\t" \
    "v_cvt_pk_bf16_f32 v109, v94, v95\n\t" \
    "v_mfma_f32_16x16x32_bf16 v[80:83], a[236:239], v[64:67], v[80:83]\n\t" \
    "s_add_u32 m0, s91, 41984\n\t" \
    "v_cvt_pk_bf16_f32 v110, v100, v101\n\t" \
    "v_mfma_f32_16x16x32_bf16 v[84:87], a[236:239], v[68:71], v[84:87]\n\t" \
    "global_load_lds_dwordx4 v208, s[48:49]\n\t" \
    "s_waitcnt lgkmcnt(5)\n\t" \
    "v_mfma_f32_16x16x32_bf16 v[72:75], a[168:171], v[24:27], v[72:75]\n\t" \
    "ds_read_b128 v[48:51], v23 offset:512\n\t" \
    "s_waitcnt lgkmcnt(5)\n\t" \
    "v_mfma_f32_16x16x32_bf16 v[76:79], a[168:171], v[28:31], v[76:79]\n\t" \
    "ds_read_b128 v[52:55], v23 offset:18944\n\t" \
    "v_cvt_pk_bf16_f32 v111, v102, v103\n\t" \
    "v_mfma_f32_16x16x32_bf16 v[80:83], a[240:243], v[24:27], v[80:83]\n\t" \
    "s_add_u32 m0, s91, 43008\n\t" \
    "global_store_dwordx4 v8, v[108:111], s[38:39] offset:1024 sc1\n\t" \
    "v_mfma_f32_16x16x32_bf16 v[84:87], a[240:243], v[28:31], v[84:87]\n\t" \
    "global_load_lds_dwordx4 v209, s[48:49]\n\t" \
    "s_waitcnt lgkmcnt(5)\n\t" \
    "v_mfma_f32_16x16x32_bf16 v[72:75], a[172:175], v[32:35], v[72:75]\n\t" \
    "ds_read_b128 v[56:59], v22 offset:640\n\t" \
    "s_waitcnt lgkmcnt(5)\n\t" \
    "v_mfma_f32_16x16x32_bf16 v[76:79], a[172:175], v[36:39], v[76:79]\n\t" \
    "ds_read_b128 v[60:63], v22 offset:19072\n\t" \
    "v_mfma_f32_16x16x32_bf16 v[80:83], a[244:247], v[32:35], v[80:83]\n\t" \
    "s_add_u32 m0, s91, 44032\n\t" \
    "v_mfma_f32_16x16x32_bf16 v[84:87], a[244:247], v[36:39], v[84:87]\n\t" \
    "global_load_lds_dwordx4 v210, s[48:49]\n\t" \
    "s_waitcnt lgkmcnt(5)\n\t" \
    "v_mfma_f32_16x16x32_bf16 v[72:75], a[176:179], v[40:43], v[72:75]\n\t" \
    "ds_read_b128 v[64:67], v23 offset:640\n\t" \
    "s_waitcnt lgkmcnt(5)\n\t" \
    "v_mfma_f32_16x16x32_bf16 v[76:79], a[176:179], v[44:47], v[76:79]\n\t" \
    "ds_read_b128 v[68:71], v23 offset:19072\n\t" \
    "v_mfma_f32_16x16x32_bf16 v[80:83], a[248:251], v[40:43], v[80:83]\n\t" \
    "s_add_u32 m0, s91, 45056\n\t" \
    "v_mfma_f32_16x16x32_bf16 v[84:87], a[248:251], v[44:47], v[84:87]\n\t" \
    "global_load_lds_dwordx4 v211, s[48:49]\n\t" \
    "s_waitcnt lgkmcnt(5)\n\t" \
    "v_mfma_f32_16x16x32_bf16 v[72:75], a[180:183], v[48:51], v[72:75]\n\t" \
    "ds_read_b128 v[24:27], v22 offset:768\n\t" \
    "s_waitcnt lgkmcnt(5)\n\t" \
    "v_mfma_f32_16x16x32_bf16 v[76:79], a[180:183], v[52:55], v[76:79]\n\t" \
    "ds_read_b128 v[28:31], v22 offset:19200\n\t" \
    "v_mfma_f32_16x16x32_bf16 v[80:83], a[252:255], v[48:51], v[80:83]\n\t" \
    "v_mfma_f32_16x16x32_bf16 v[84:87], a[252:255], v[52:55], v[84:87]\n\t" \
    "s_waitcnt lgkmcnt(5)\n\t" \
    "v_mfma_f32_16x16x32_bf16 v[72:75], a[184:187], v[56:59], v[72:75]\n\t" \
    "ds_read_b128 v[32:35], v23 offset:768\n\t" \
    "s_waitcnt lgkmcnt(5)\n\t" \
    "v_mfma_f32_16x16x32_bf16 v[76:79], a[184:187], v[60:63], v[76:79]\n\t" \
    "ds_read_b128 v[36:39], v23 offset:19200\n\t" \
    "v_mfma_f32_16x16x32_bf16 v[80:83], v[224:227], v[56:59], v[80:83]\n\t" \
    "s_waitcnt vmcnt(11)\n\t" \
    "s_barrier\n\t" \
    "v_mfma_f32_16x16x32_bf16 v[84:87], v[224:227], v[60:63], v[84:87]\n\t" \
    "s_add_u32 s71, s65, 7\n\t" \
    "s_cmp_lg_u32 s66, 0\n\t" \
    "s_cbranch_scc1 gx_SF14_%=\n\t" \
    "s_lshl_b32 s69, s67, 7\n\t" \
    "s_add_u32 s69, s69, 3149824\n\t" \
    "s_add_u32 s70, s71, 0\n\t" \
    "v_mov_b32 v156, s69\n\t" \
    "v_mov_b32 v157, s70\n\t" \
    "s_mov_b64 exec, 1\n\t" \
    "global_store_dword v156, v157, s[100:101] sc1\n\t" \
    "s_mov_b64 exec, -1\n\t" \
    "gx_SF14_%=:\n\t" \
    "s_waitcnt lgkmcnt(5)\n\t" \
    "v_mfma_f32_16x16x32_bf16 v[72:75], a[188:191], v[64:67], v[72:75]\n\t" \
    "ds_read_b128 v[40:43], v22 offset:896\n\t" \
    "s_waitcnt lgkmcnt(5)\n\t" \
    "v_mfma_f32_16x16x32_bf16 v[76:79], a[188:191], v[68:71], v[76:79]\n\t" \
    "ds_read_b128 v[44:47], v22 offset:19328\n\t" \
    "v_mfma_f32_16x16x32_bf16 v[80:83], v[228:231], v[64:67], v[80:83]\n\t" \
    "s_xor_b32 s90, s90, 0x12000\n\t" \
    "v_add_u32 v1, s90, v0\n\t" \
    "v_mfma_f32_16x16x32_bf16 v[84:87], v[228:231], v[68:71], v[84:87]\n\t" \
    "v_xor_b32 v2, 64, v1\n\t" \
    "s_add_u32 s84, s65, 2\n\t" \
    "s_waitcnt lgkmcnt(5)\n\t" \
    "v_mfma_f32_16x16x32_bf16 v[72:75], a[192:195], v[24:27], v[72:75]\n\t" \
    "ds_read_b128 v[48:51], v23 offset:896\n\t" \
    "s_waitcnt lgkmcnt(5)\n\t" \
    "v_mfma_f32_16x16x32_bf16 v[76:79], a[192:195], v[28:31], v[76:79]\n\t" \
    "ds_read_b128 v[52:55], v23 offset:19328\n\t" \
    "v_mfma_f32_16x16x32_bf16 v[80:83], v[232:235], v[24:27], v[80:83]\n\t" \
    "s_min_u32 s69, s84, 63\n\t" \
    "s_mul_i32 s69, s69, 36864\n\t" \
    "v_mfma_f32_16x16x32_bf16 v[84:87], v[232:235], v[28:31], v[84:87]\n\t" \
    "s_add_u32 s52, s50, s69\n\t" \
    "s_addc_u32 s53, s51, 0\n\t" \
    "s_waitcnt lgkmcnt(5)\n\t" \
    "v_mfma_f32_16x16x32_bf16 v[72:75], a[196:199], v[32:35], v[72:75]\n\t" \
    "ds_read_b128 v[56:59], v22 offset:1024\n\t" \
    "s_waitcnt lgkmcnt(5)\n\t" \
    "v_mfma_f32_16x16x32_bf16 v[76:79], a[196:199], v[36:39], v[76:79]\n\t" \
    "ds_read_b128 v[60:63], v22 offset:19456\n\t" \
    "v_mfma_f32_16x16x32_bf16 v[80:83], v[236:239], v[32:35], v[80:83]\n\t" \
    "s_mul_i32 s70, s67, 7\n\t" \
    "s_add_u32 s69, s65, 8\n\t" \
    "v_mfma_f32_16x16x32_bf16 v[84:87], v[236:239], v[36:39], v[84:87]\n\t" \
    "s_xor_b32 s69, s69, s70\n\t" \
    "s_and_b32 s69, s69, 63\n\t" \
    "s_waitcnt lgkmcnt(5)\n\t" \
    "v_mfma_f32_16x16x32_bf16 v[72:75], a[200:203], v[40:43], v[72:75]\n\t" \
    "ds_read_b128 v[64:67], v23 offset:1024\n\t" \
    "s_waitcnt lgkmcnt(5)\n\t" \
    "v_mfma_f32_16x16x32_bf16 v[76:79], a[200:203], v[44:47], v[76:79]\n\t" \
    "ds_read_b128 v[68:71], v23 offset:19456\n\t" \
    "v_mfma_f32_16x16x32_bf16 v[80:83], v[240:243], v[40:43], v[80:83]\n\t" \
    "s_lshl_b32 s69, s69, 13\n\t" \
    "s_mul_i32 s70, s67, 524288\n\t" \
    "v_mfma_f32_16x16x32_bf16 v[84:87], v[240:243], v[44:47], v[84:87]\n\t" \
    "s_add_u32 s69, s69, s70\n\t" \
    "s_add_u32 s69, s69, 23068672\n\t" \
    "s_waitcnt lgkmcnt(5)\n\t" \
    "v_mfma_f32_16x16x32_bf16 v[72:75], a[204:207], v[48:51], v[72:75]\n\t" \
    "ds_read_b128 v[24:27], v1 offset:0\n\t" \
    "s_waitcnt lgkmcnt(5)\n\t" \
    "v_mfma_f32_16x16x32_bf16 v[76:79], a[204:207], v[52:55], v[76:79]\n\t" \
    "ds_read_b128 v[28:31], v1 offset:18432\n\t" \
    "v_mfma_f32_16x16x32_bf16 v[80:83], v[244:247], v[48:51], v[80:83]\n\t" \
    "s_add_u32 s40, s36, s69\n\t" \
    "s_addc_u32 s41, s37, 0\n\t" \
    "v_mfma_f32_16x16x32_bf16 v[84:87], v[244:247], v[52:55], v[84:87]\n\t" \
    "s_sub_u32 s91, s85, s91\n\t" \
    "s_add_u32 s65, s65, 1\n\t" \
    "s_waitcnt lgkmcnt(5)\n\t" \
    "v_mfma_f32_16x16x32_bf16 v[72:75], a[208:211], v[56:59], v[72:75]\n\t" \
    "ds_read_b128 v[32:35], v2 offset:0\n\t" \
    "s_waitcnt lgkmcnt(5)\n\t" \
    "v_mfma_f32_16x16x32_bf16 v[76:79], a[208:211], v[60:63], v[76:79]\n\t" \
    "ds_read_b128 v[36:39], v2 offset:18432\n\t" \
    "v_mfma_f32_16x16x32_bf16 v[80:83], v[248:251], v[56:59], v[80:83]\n\t" \
    "v_mfma_f32_16x16x32_bf16 v[84:87], v[248:251], v[60:63], v[84:87]\n\t" \
    "s_waitcnt lgkmcnt(5)\n\t" \
    "v_mfma_f32_16x16x32_bf16 v[72:75], a[212:215], v[64:67], v[72:75]\n\t" \
    "ds_read_b128 v[40:43], v1 offset:128\n\t" \
    "s_waitcnt lgkmcnt(5)\n\t" \
    "v_mfma_f32_16x16x32_bf16 v[76:79], a[212:215], v[68:71], v[76:79]\n\t" \
    "ds_read_b128 v[44:47], v1 offset:18560\n\t" \
    "v_mfma_f32_16x16x32_bf16 v[80:83], v[252:255], v[64:67], v[80:83]\n\t" \
    "v_mfma_f32_16x16x32_bf16 v[84:87], v[252:255], v[68:71], v[84:87]\n\t" \
    "s_cmp_lt_u32 s65, 56\n\t" \
    "s_cbranch_scc1 gx_l27_%=\n\t" \
    "v_cvt_pk_bf16_f32 v104, v72, v73\n\t" \
    "v_cvt_pk_bf16_f32 v105, v74, v75\n\t" \
    "s_nop 2\n\t" \
    "v_cvt_pk_bf16_f32 v106, v80, v81\n\t" \
    "v_cvt_pk_bf16_f32 v107, v82, v83\n\t" \
    "global_store_dwordx4 v8, v[104:107], s[40:41] offset:0 sc1\n\t" \
    "v_cvt_pk_bf16_f32 v108, v76, v77\n\t" \
    "v_cvt_pk_bf16_f32 v109, v78, v79\n\t" \
    "v_cvt_pk_bf16_f32 v110, v84, v85\n\t" \
    "v_cvt_pk_bf16_f32 v111, v86, v87\n\t" \
    "global_store_dwordx4 v8, v[108:111], s[40:41] offset:1024 sc1\n\t" \
    "gx_l315_%=:\n\t" \
    "s_add_u32 s84, s65, 2\n\t" \
    "s_min_u32 s69, s84, 63\n\t" \
    "s_mul_i32 s69, s69, 36864\n\t" \
    "s_add_u32 s52, s50, s69\n\t" \
    "s_addc_u32 s53, s51, 0\n\t" \
    "s_add_u32 s90, s90, 36864\n\t" \
    "s_cmp_eq_u32 s90, 147456\n\t" \
    "s_cselect_b32 s90, 0, s90\n\t" \
    "s_add_u32 s91, s90, 36864\n\t" \
    "s_cmp_eq_u32 s91, 147456\n\t" \
    "s_cselect_b32 s91, 0, s91\n\t" \
    "s_add_u32 s91, s91, s89\n\t" \
    "v_add_u32 v22, s90, v0\n\t" \
    "v_xor_b32 v23, 64, v22\n\t" \
    "s_mul_i32 s70, s67, 7\n\t" \
    "s_add_u32 s69, s65, 0\n\t" \
    "s_xor_b32 s69, s69, s70\n\t" \
    "s_and_b32 s69, s69, 63\n\t" \
    "s_lshl_b32 s69, s69, 13\n\t" \
    "s_mul_i32 s70, s67, 524288\n\t" \
    "s_add_u32 s69, s69, s70\n\t" \
    "s_add_u32 s69, s69, 6291456\n\t" \
    "s_add_u32 s38, s36, s69\n\t" \
    "s_addc_u32 s39, s37, 0\n\t" \
    "s_waitcnt lgkmcnt(5)\n\t" \
    "v_mfma_f32_16x16x32_bf16 v[88:91], a[0:3], v[24:27], 0\n\t" \
    "ds_read_b128 v[48:51], v2 offset:128\n\t" \
    "s_waitcnt lgkmcnt(5)\n\t" \
    "v_mfma_f32_16x16x32_bf16 v[92:95], a[0:3], v[28:31], 0\n\t" \
    "ds_read_b128 v[52:55], v2 offset:18560\n\t" \
    "v_mfma_f32_16x16x32_bf16 v[96:99], a[72:75], v[24:27], 0\n\t" \
    "s_add_u32 m0, s91, 0\n\t" \
    "v_mfma_f32_16x16x32_bf16 v[100:103], a[72:75], v[28:31], 0\n\t" \
    "global_load_lds_dwordx4 v203, s[52:53]\n\t" \
    "s_waitcnt lgkmcnt(5)\n\t" \
    "v_mfma_f32_16x16x32_bf16 v[88:91], a[4:7], v[32:35], v[88:91]\n\t" \
    "ds_read_b128 v[56:59], v1 offset:256\n\t" \
    "s_waitcnt lgkmcnt(5)\n\t" \
    "v_mfma_f32_16x16x32_bf16 v[92:95], a[4:7], v[36:39], v[92:95]\n\t" \
    "ds_read_b128 v[60:63], v1 offset:18688\n\t" \
    "v_mfma_f32_16x16x32_bf16 v[96:99], a[76:79], v[32:35], v[96:99]\n\t" \
    "s_add_u32 m0, s91, 1024\n\t" \
    "v_mfma_f32_16x16x32_bf16 v[100:103], a[76:79], v[36:39], v[100:103]\n\t" \
    "global_load_lds_dwordx4 v204, s[52:53]\n\t" \
    "s_waitcnt lgkmcnt(5)\n\t" \
    "v_mfma_f32_16x16x32_bf16 v[88:91], a[8:11], v[40:43], v[88:91]\n\t" \
    "ds_read_b128 v[64:67], v2 offset:256\n\t" \
    "s_waitcnt lgkmcnt(5)\n\t" \
    "v_mfma_f32_16x16x32_bf16 v[92:95], a[8:11], v[44:47], v[92:95]\n\t" \
    "ds_read_b128 v[68:71], v2 offset:18688\n\t" \
    "v_mfma_f32_16x16x32_bf16 v[96:99], a[80:83], v[40:43], v[96:99]\n\t" \
    "s_add_u32 m0, s91, 2048\n\t" \
    "v_mfma_f32_16x16x32_bf16 v[100:103], a[80:83], v[44:47], v[100:103]\n\t" \
    "global_load_lds_dwordx4 v205, s[52:53]\n\t" \
    "s_waitcnt lgkmcnt(5)\n\t" \
    "v_mfma_f32_16x16x32_bf16 v[88:91], a[12:15], v[48:51], v[88:91]\n\t" \
    "ds_read_b128 v[24:27], v1 offset:384\n\t" \
    "s_waitcnt lgkmcnt(5)\n\t" \
    "v_mfma_f32_16x16x32_bf16 v[92:95], a[12:15], v[52:55], v[92:95]\n\t" \
    "ds_read_b128 v[28:31], v1 offset:18816\n\t" \
    "v_mfma_f32_16x16x32_bf16 v[96:99], a[84:87], v[48:51], v[96:99]\n\t" \
    "s_add_u32 m0, s91, 3072\n\t" \
    "v_mfma_f32_16x16x32_bf16 v[100:103], a[84:87], v[52:55], v[100:103]\n\t" \
    "global_load_lds_dwordx4 v206, s[52:53]\n\t" \
    "s_waitcnt lgkmcnt(5)\n\t" \
    "v_mfma_f32_16x16x32_bf16 v[88:91], a[16:19], v[56:59], v[88:91]\n\t" \
    "ds_read_b128 v[32:35], v2 offset:384\n\t" \
    "s_waitcnt lgkmcnt(5)\n\t" \
    "v_mfma_f32_16x16x32_bf16 v[92:95], a[16:19], v[60:63], v[92:95]\n\t" \
    "ds_read_b128 v[36:39], v2 offset:18816\n\t" \
    "v_mfma_f32_16x16x32_bf16 v[96:99], a[88:91], v[56:59], v[96:99]\n\t" \
    "s_add_u32 m0, s91, 4096\n\t" \
    "v_mfma_f32_16x16x32_bf16 v[100:103], a[88:91], v[60:63], v[100:103]\n\t" \
    "global_load_lds_dwordx4 v207, s[52:53]\n\t" \
    "s_waitcnt lgkmcnt(5)\n\t" \
    "v_mfma_f32_16x16x32_bf16 v[88:91], a[20:23], v[64:67], v[88:91]\n\t" \
    "ds_read_b128 v[40:43], v1 offset:512\n\t" \
    "s_waitcnt lgkmcnt(5)\n\t" \
    "v_mfma_f32_16x16x32_bf16 v[92:95], a[20:23], v[68:71], v[92:95]\n\t" \
    "ds_read_b128 v[44:47], v1 offset:18944\n\t" \
    "v_mfma_f32_16x16x32_bf16 v[96:99], a[92:95], v[64:67], v[96:99]\n\t" \
    "s_add_u32 m0, s91, 5120\n\t" \
    "v_mfma_f32_16x16x32_bf16 v[100:103], a[92:95], v[68:71], v[100:103]\n\t" \
    "global_load_lds_dwordx4 v208, s[52:53]\n\t" \
    "s_waitcnt lgkmcnt(5)\n\t" \
    "v_mfma_f32_16x16x32_bf16 v[88:91], a[24:27], v[24:27], v[88:91]\n\t" \
    "ds_read_b128 v[48:51], v2 offset:512\n\t" \
    "s_waitcnt lgkmcnt(5)\n\t" \
    "v_mfma_f32_16x16x32_bf16 v[92:95], a[24:27], v[28:31], v[92:95]\n\t" \
    "ds_read_b128 v[52:55], v2 offset:18944\n\t" \
    "v_mfma_f32_16x16x32_bf16 v[96:99], a[96:99], v[24:27], v[96:99]\n\t" \
    "s_add_u32 m0, s91, 6144\n\t" \
    "v_mfma_f32_16x16x32_bf16 v[100:103], a[96:99], v[28:31], v[100:103]\n\t" \
    "global_load_lds_dwordx4 v209, s[52:53]\n\t" \
    "s_waitcnt lgkmcnt(5)\n\t" \
    "v_mfma_f32_16x16x32_bf16 v[88:91], a[28:31], v[32:35], v[88:91]\n\t" \
    "ds_read_b128 v[56:59], v1 offset:640\n\t" \
    "s_waitcnt lgkmcnt(5)\n\t" \
    "v_mfma_f32_16x16x32_bf16 v[92:95], a[28:31], v[36:39], v[92:95]\n\t" \
    "ds_read_b128 v[60:63], v1 offset:19072\n\t" \
    "v_mfma_f32_16x16x32_bf16 v[96:99], a[100:103], v[32:35], v[96:99]\n\t" \
    "s_add_u32 m0, s91, 7168\n\t" \
    "v_mfma_f32_16x16x32_bf16 v[100:103], a[100:103], v[36:39], v[100:103]\n\t" \
    "global_load_lds_dwordx4 v210, s[52:53]\n\t" \
    "s_waitcnt lgkmcnt(5)\n\t" \
    "v_mfma_f32_16x16x32_bf16 v[88:91], a[32:35], v[40:43], v[88:91]\n\t" \
    "ds_read_b128 v[64:67], v2 offset:640\n\t" \
    "s_waitcnt lgkmcnt(5)\n\t" \
    "v_mfma_f32_16x16x32_bf16 v[92:95], a[32:35], v[44:47], v[92:95]\n\t" \
    "ds_read_b128 v[68:71], v2 offset:19072\n\t" \
    "v_mfma_f32_16x16x32_bf16 v[96:99], a[104:107], v[40:43], v[96:99]\n\t" \
    "s_add_u32 m0, s91, 8192\n\t" \
    "v_mfma_f32_16x16x32_bf16 v[100:103], a[104:107], v[44:47], v[100:103]\n\t" \
    "global_load_lds_dwordx4 v211, s[52:53]\n\t" \
    "s_waitcnt lgkmcnt(5)\n\t" \
    "v_mfma_f32_16x16x32_bf16 v[88:91], a[36:39], v[48:51], v[88:91]\n\t" \
    "ds_read_b128 v[24:27], v1 offset:768\n\t" \
    "s_waitcnt lgkmcnt(5)\n\t" \
    "v_mfma_f32_16x16x32_bf16 v[92:95], a[36:39], v[52:55], v[92:95]\n\t" \
    "ds_read_b128 v[28:31], v1 offset:19200\n\t" \
    "v_mfma_f32_16x16x32_bf16 v[96:99], a[108:111], v[48:51], v[96:99]\n\t" \
    "v_mfma_f32_16x16x32_bf16 v[100:103], a[108:111], v[52:55], v[100:103]\n\t" \
    "s_waitcnt lgkmcnt(5)\n\t" \
    "v_mfma_f32_16x16x32_bf16 v[88:91], a[40:43], v[56:59], v[88:91]\n\t" \
    "ds_read_b128 v[32:35], v2 offset:768\n\t" \
    "s_waitcnt lgkmcnt(5)\n\t" \
    "v_mfma_f32_16x16x32_bf16 v[92:95], a[40:43], v[60:63], v[92:95]\n\t" \
    "ds_read_b128 v[36:39], v2 offset:19200\n\t" \
    "v_mfma_f32_16x16x32_bf16 v[96:99], a[112:115], v[56:59], v[96:99]\n\t" \
    "s_waitcnt vmcnt(9)\n\t" \
    "s_barrier\n\t" \
    "v_mfma_f32_16x16x32_bf16 v[100:103], a[112:115], v[60:63], v[100:103]\n\t" \
    "s_cmp_lg_u32 s66, 0\n\t" \
    "s_cbranch_scc1 gx_SF16_%=\n\t" \
    "s_lshl_b32 s69, s67, 7\n\t" \
    "s_add_u32 s69, s69, 3211264\n\t" \
    "s_add_u32 s70, s65, -1\n\t" \
    "v_mov_b32 v156, s69\n\t" \
    "v_mov_b32 v157, s70\n\t" \
    "s_mov_b64 exec, 1\n\t" \
    "global_store_dword v156, v157, s[100:101] sc1\n\t" \
    "s_mov_b64 exec, -1\n\t" \
    "gx_SF16_%=:\n\t" \
    "s_cmp_lg_u32 s66, 1\n\t" \
    "s_cbranch_scc1 gx_SF17_%=\n\t" \
    "s_lshl_b32 s69, s67, 7\n\t" \
    "s_add_u32 s69, s69, 3149824\n\t" \
    "s_add_u32 s70, s65, 40\n\t" \
    "v_mov_b32 v156, s69\n\t" \
    "v_mov_b32 v157, s70\n\t" \
    "s_mov_b64 exec, 1\n\t" \
    "global_store_dword v156, v157, s[100:101] sc1\n\t" \
    "s_mov_b64 exec, -1\n\t" \
    "gx_SF17_%=:\n\t" \
    "s_waitcnt lgkmcnt(5)\n\t" \
    "v_mfma_f32_16x16x32_bf16 v[88:91], a[44:47], v[64:67], v[88:91]\n\t" \
    "ds_read_b128 v[40:43], v1 offset:896\n\t" \
    "s_waitcnt lgkmcnt(5)\n\t" \
    "v_mfma_f32_16x16x32_bf16 v[92:95], a[44:47], v[68:71], v[92:95]\n\t" \
    "ds_read_b128 v[44:47], v1 offset:19328\n\t" \
    "v_mfma_f32_16x16x32_bf16 v[96:99], a[116:119], v[64:67], v[96:99]\n\t" \
    "v_mfma_f32_16x16x32_bf16 v[100:103], a[116:119], v[68:71], v[100:103]\n\t" \
    "s_waitcnt lgkmcnt(5)\n\t" \
    "v_mfma_f32_16x16x32_bf16 v[88:91], a[48:51], v[24:27], v[88:91]\n\t" \
    "ds_read_b128 v[48:51], v2 offset:896\n\t" \
    "s_waitcnt lgkmcnt(5)\n\t" \
    "v_mfma_f32_16x16x32_bf16 v[92:95], a[48:51], v[28:31], v[92:95]\n\t" \
    "ds_read_b128 v[52:55], v2 offset:19328\n\t" \
    "v_mfma_f32_16x16x32_bf16 v[96:99], a[120:123], v[24:27], v[96:99]\n\t" \
    "v_mfma_f32_16x16x32_bf16 v[100:103], a[120:123], v[28:31], v[100:103]\n\t" \
    "s_waitcnt lgkmcnt(5)\n\t" \
    "v_mfma_f32_16x16x32_bf16 v[88:91], a[52:55], v[32:35], v[88:91]\n\t" \
    "ds_read_b128 v[56:59], v1 offset:1024\n\t" \
    "s_waitcnt lgkmcnt(5)\n\t" \
    "v_mfma_f32_16x16x32_bf16 v[92:95], a[52:55], v[36:39], v[92:95]\n\t" \
    "ds_read_b128 v[60:63], v1 offset:19456\n\t" \
    "v_mfma_f32_16x16x32_bf16 v[96:99], a[124:127], v[32:35], v[96:99]\n\t" \
    "v_mfma_f32_16x16x32_bf16 v[100:103], a[124:127], v[36:39], v[100:103]\n\t" \
    "s_waitcnt lgkmcnt(5)\n\t" \
    "v_mfma_f32_16x16x32_bf16 v[88:91], a[56:59], v[40:43], v[88:91]\n\t" \
    "ds_read_b128 v[64:67], v2 offset:1024\n\t" \
    "s_waitcnt lgkmcnt(5)\n\t" \
    "v_mfma_f32_16x16x32_bf16 v[92:95], a[56:59], v[44:47], v[92:95]\n\t" \
    "ds_read_b128 v[68:71], v2 offset:19456\n\t" \
    "v_mfma_f32_16x16x32_bf16 v[96:99], a[128:131], v[40:43], v[96:99]\n\t" \
    "v_mfma_f32_16x16x32_bf16 v[100:103], a[128:131], v[44:47], v[100:103]\n\t" \
    "s_waitcnt lgkmcnt(5)\n\t" \
    "v_mfma_f32_16x16x32_bf16 v[88:91], a[60:63], v[48:51], v[88:91]\n\t" \
    "ds_read_b128 v[24:27], v22 offset:0\n\t" \
    "s_waitcnt lgkmcnt(5)\n\t" \
    "v_mfma_f32_16x16x32_bf16 v[92:95], a[60:63], v[52:55], v[92:95]\n\t" \
    "ds_read_b128 v[28:31], v22 offset:18432\n\t" \
    "v_mfma_f32_16x16x32_bf16 v[96:99], a[132:135], v[48:51], v[96:99]\n\t" \
    "v_mfma_f32_16x16x32_bf16 v[100:103], a[132:135], v[52:55], v[100:103]\n\t" \
    "s_waitcnt lgkmcnt(5)\n\t" \
    "v_mfma_f32_16x16x32_bf16 v[88:91], a[64:67], v[56:59], v[88:91]\n\t" \
    "ds_read_b128 v[32:35], v23 offset:0\n\t" \
    "s_waitcnt lgkmcnt(5)\n\t" \
    "v_mfma_f32_16x16x32_bf16 v[92:95], a[64:67], v[60:63], v[92:95]\n\t" \
    "ds_read_b128 v[36:39], v23 offset:18432\n\t" \
    "v_mfma_f32_16x16x32_bf16 v[96:99], a[136:139], v[56:59], v[96:99]\n\t" \
    "v_mfma_f32_16x16x32_bf16 v[100:103], a[136:139], v[60:63], v[100:103]\n\t" \
    "s_waitcnt lgkmcnt(5)\n\t" \
    "v_mfma_f32_16x16x32_bf16 v[88:91], a[68:71], v[64:67], v[88:91]\n\t" \
    "ds_read_b128 v[40:43], v22 offset:128\n\t" \
    "s_waitcnt lgkmcnt(5)\n\t" \
    "v_mfma_f32_16x16x32_bf16 v[92:95], a[68:71], v[68:71], v[92:95]\n\t" \
    "ds_read_b128 v[44:47], v22 offset:18560\n\t" \
    "v_mfma_f32_16x16x32_bf16 v[96:99], a[140:143], v[64:67], v[96:99]\n\t" \
    "v_mfma_f32_16x16x32_bf16 v[100:103], a[140:143], v[68:71], v[100:103]\n\t" \
    "v_mov_b32 v1, v22\n\t" \
    "v_mov_b32 v2, v23\n\t" \
    "v_cvt_pk_bf16_f32 v104, v88, v89\n\t" \
    "v_cvt_pk_bf16_f32 v105, v90, v91\n\t" \
    "s_nop 2\n\t" \
    "v_cvt_pk_bf16_f32 v106, v96, v97\n\t" \
    "v_cvt_pk_bf16_f32 v107, v98, v99\n\t" \
    "global_store_dwordx4 v8, v[104:107], s[38:39] offset:0 sc1\n\t" \
    "v_cvt_pk_bf16_f32 v108, v92, v93\n\t" \
    "v_cvt_pk_bf16_f32 v109, v94, v95\n\t" \
    "v_cvt_pk_bf16_f32 v110, v100, v101\n\t" \
    "v_cvt_pk_bf16_f32 v111, v102, v103\n\t" \
    "global_store_dwordx4 v8, v[108:111], s[38:39] offset:1024 sc1\n\t" \
    "s_add_u32 s65, s65, 1\n\t" \
    "s_cmp_lt_u32 s65, 64\n\t" \
    "s_cbranch_scc1 gx_l315_%=\n\t" \
    "s_waitcnt vmcnt(0) lgkmcnt(0)\n\t" \
    "s_barrier\n\t" \
    "s_cmp_lg_u32 s66, 0\n\t" \
    "s_cbranch_scc1 gx_SF18_%=\n\t" \
    "s_lshl_b32 s69, s67, 7\n\t" \
    "s_add_u32 s69, s69, 3211264\n\t" \
    "s_add_u32 s70, s65, -1\n\t" \
    "v_mov_b32 v156, s69\n\t" \
    "v_mov_b32 v157, s70\n\t" \
    "s_mov_b64 exec, 1\n\t" \
    "global_store_dword v156, v157, s[100:101] sc1\n\t" \
    "s_mov_b64 exec, -1\n\t" \
    "gx_SF18_%=:\n\t" \
    "gx_last_%=:\n\t" \
    ""
#define ASM_GV \
    "s_load_dwordx2 s[42:43], %3, 56\n\t" \
    "s_load_dwordx2 s[44:45], %3, 64\n\t" \
    "s_load_dwordx2 s[36:37], %3, 0x58\n\t" \
    "s_load_dwordx2 s[50:51], %3, 0x48\n\t" \
    "s_mov_b32 s66, %4\n\t" \
    "s_mov_b32 s67, %5\n\t" \
    "s_mov_b32 s68, 262144\n\t" \
    "s_mov_b32 s72, 0\n\t" \
    "s_mov_b32 s83, -1\n\t" \
    "s_mov_b32 s65, 0\n\t" \
    "s_lshl_b32 s82, s66, 10\n\t" \
    "s_waitcnt lgkmcnt(0)\n\t" \
    "s_lshr_b32 s65, s67, 5\n\t" \
    "s_cselect_b32 s88, 24, 64\n\t" \
    "s_cmp_lt_u32 s65, 24\n\t" \
    "s_addc_u32 s84, s65, 1\n\t" \
    "s_cmp_lt_u32 s84, 24\n\t" \
    "s_addc_u32 s85, s84, 1\n\t" \
    "s_mov_b32 s86, -1\n\t" \
    "s_mov_b32 s87, -1\n\t" \
    "s_add_u32 s38, s36, 48234496\n\t" \
    "s_addc_u32 s39, s37, 0\n\t" \
    "s_and_b32 s69, s67, 31\n\t" \
    "s_mul_i32 s69, s69, 147456\n\t" \
    "s_add_u32 s44, s44, s69\n\t" \
    "s_addc_u32 s45, s45, 0\n\t" \
    "s_mul_i32 s89, s66, 9216\n\t" \
    "s_mov_b32 s90, 0\n\t" \
    "v_mov_b32 v196, -1\n\t" \
    "v_mov_b32 v197, -1\n\t" \
    "v_mov_b32 v198, -1\n\t" \
    "v_mov_b32 v199, -1\n\t" \
    "s_add_u32 s46, s42, 2097152\n\t" \
    "s_addc_u32 s47, s43, 0\n\t" \
    "s_add_u32 m0, s82, 65536\n\t" \
    "s_nop 0\n\t" \
    "global_load_lds_dwordx4 v200, s[46:47]\n\t" \
    "s_add_u32 s46, s42, 2228224\n\t" \
    "s_addc_u32 s47, s43, 0\n\t" \
    "s_add_u32 m0, s82, 69632\n\t" \
    "s_nop 0\n\t" \
    "global_load_lds_dwordx4 v200, s[46:47]\n\t" \
    "s_add_u32 s46, s42, 2359296\n\t" \
    "s_addc_u32 s47, s43, 0\n\t" \
    "s_add_u32 m0, s82, 73728\n\t" \
    "s_nop 0\n\t" \
    "global_load_lds_dwordx4 v200, s[46:47]\n\t" \
    "s_add_u32 s46, s42, 2490368\n\t" \
    "s_addc_u32 s47, s43, 0\n\t" \
    "s_add_u32 m0, s82, 77824\n\t" \
    "s_nop 0\n\t" \
    "global_load_lds_dwordx4 v200, s[46:47]\n\t" \
    "s_add_u32 s46, s42, 2621440\n\t" \
    "s_addc_u32 s47, s43, 0\n\t" \
    "s_add_u32 m0, s82, 81920\n\t" \
    "s_nop 0\n\t" \
    "global_load_lds_dwordx4 v200, s[46:47]\n\t" \
    "s_add_u32 s46, s42, 2752512\n\t" \
    "s_addc_u32 s47, s43, 0\n\t" \
    "s_add_u32 m0, s82, 86016\n\t" \
    "s_nop 0\n\t" \
    "global_load_lds_dwordx4 v200, s[46:47]\n\t" \
    "s_add_u32 s46, s42, 2883584\n\t" \
    "s_addc_u32 s47, s43, 0\n\t" \
    "s_add_u32 m0, s82, 90112\n\t" \
    "s_nop 0\n\t" \
    "global_load_lds_dwordx4 v200, s[46:47]\n\t" \
    "s_add_u32 s46, s42, 3014656\n\t" \
    "s_addc_u32 s47, s43, 0\n\t" \
    "s_add_u32 m0, s82, 94208\n\t" \
    "s_nop 0\n\t" \
    "global_load_lds_dwordx4 v200, s[46:47]\n\t" \
    "s_add_u32 s46, s42, 3145728\n\t" \
    "s_addc_u32 s47, s43, 0\n\t" \
    "s_add_u32 m0, s82, 98304\n\t" \
    "s_nop 0\n\t" \
    "global_load_lds_dwordx4 v200, s[46:47]\n\t" \
    "s_add_u32 s46, s42, 3276800\n\t" \
    "s_addc_u32 s47, s43, 0\n\t" \
    "s_add_u32 m0, s82, 102400\n\t" \
    "s_nop 0\n\t" \
    "global_load_lds_dwordx4 v200, s[46:47]\n\t" \
    "s_add_u32 s46, s42, 3407872\n\t" \
    "s_addc_u32 s47, s43, 0\n\t" \
    "s_add_u32 m0, s82, 106496\n\t" \
    "s_nop 0\n\t" \
    "global_load_lds_dwordx4 v200, s[46:47]\n\t" \
    "s_add_u32 s46, s42, 3538944\n\t" \
    "s_addc_u32 s47, s43, 0\n\t" \
    "s_add_u32 m0, s82, 110592\n\t" \
    "s_nop 0\n\t" \
    "global_load_lds_dwordx4 v200, s[46:47]\n\t" \
    "s_add_u32 s46, s42, 3670016\n\t" \
    "s_addc_u32 s47, s43, 0\n\t" \
    "s_add_u32 m0, s82, 114688\n\t" \
    "s_nop 0\n\t" \
    "global_load_lds_dwordx4 v200, s[46:47]\n\t" \
    "s_add_u32 s46, s42, 3801088\n\t" \
    "s_addc_u32 s47, s43, 0\n\t" \
    "s_add_u32 m0, s82, 118784\n\t" \
    "s_nop 0\n\t" \
    "global_load_lds_dwordx4 v200, s[46:47]\n\t" \
    "s_add_u32 s46, s42, 3932160\n\t" \
    "s_addc_u32 s47, s43, 0\n\t" \
    "s_add_u32 m0, s82, 122880\n\t" \
    "s_nop 0\n\t" \
    "global_load_lds_dwordx4 v200, s[46:47]\n\t" \
    "s_add_u32 s46, s42, 4063232\n\t" \
    "s_addc_u32 s47, s43, 0\n\t" \
    "s_add_u32 m0, s82, 126976\n\t" \
    "s_nop 0\n\t" \
    "global_load_lds_dwordx4 v200, s[46:47]\n\t" \
    "s_add_u32 s46, s42, 4194304\n\t" \
    "s_addc_u32 s47, s43, 0\n\t" \
    "s_add_u32 m0, s82, 131072\n\t" \
    "s_nop 0\n\t" \
    "global_load_lds_dwordx4 v200, s[46:47]\n\t" \
    "s_add_u32 s46, s42, 4325376\n\t" \
    "s_addc_u32 s47, s43, 0\n\t" \
    "s_add_u32 m0, s82, 135168\n\t" \
    "s_nop 0\n\t" \
    "global_load_lds_dwordx4 v200, s[46:47]\n\t" \
    "s_add_u32 s46, s42, 4456448\n\t" \
    "s_addc_u32 s47, s43, 0\n\t" \
    "s_add_u32 m0, s82, 139264\n\t" \
    "s_nop 0\n\t" \
    "global_load_lds_dwordx4 v200, s[46:47]\n\t" \
    "s_add_u32 s46, s42, 4587520\n\t" \
    "s_addc_u32 s47, s43, 0\n\t" \
    "s_add_u32 m0, s82, 143360\n\t" \
    "s_nop 0\n\t" \
    "global_load_lds_dwordx4 v200, s[46:47]\n\t" \
    "s_waitcnt vmcnt(32) lgkmcnt(0)\n\t" \
    "s_barrier\n\t" \
    "ds_read_b64_tr_b16 a[0:1], v201 offset:0\n\t" \
    "ds_read_b64_tr_b16 a[2:3], v201 offset:1024\n\t" \
    "ds_read_b64_tr_b16 a[4:5], v201 offset:8192\n\t" \
    "ds_read_b64_tr_b16 a[6:7], v201 offset:9216\n\t" \
    "ds_read_b64_tr_b16 a[72:73], v202 offset:0\n\t" \
    "ds_read_b64_tr_b16 a[74:75], v202 offset:1024\n\t" \
    "ds_read_b64_tr_b16 a[76:77], v202 offset:8192\n\t" \
    "ds_read_b64_tr_b16 a[78:79], v202 offset:9216\n\t" \
    "s_waitcnt vmcnt(28) lgkmcnt(0)\n\t" \
    "s_barrier\n\t" \
    "ds_read_b64_tr_b16 a[8:9], v201 offset:16384\n\t" \
    "ds_read_b64_tr_b16 a[10:11], v201 offset:17408\n\t" \
    "ds_read_b64_tr_b16 a[12:13], v201 offset:24576\n\t" \
    "ds_read_b64_tr_b16 a[14:15], v201 offset:25600\n\t" \
    "ds_read_b64_tr_b16 a[80:81], v202 offset:16384\n\t" \
    "ds_read_b64_tr_b16 a[82:83], v202 offset:17408\n\t" \
    "ds_read_b64_tr_b16 a[84:85], v202 offset:24576\n\t" \
    "ds_read_b64_tr_b16 a[86:87], v202 offset:25600\n\t" \
    "s_waitcnt vmcnt(24) lgkmcnt(0)\n\t" \
    "s_barrier\n\t" \
    "ds_read_b64_tr_b16 a[16:17], v201 offset:32768\n\t" \
    "ds_read_b64_tr_b16 a[18:19], v201 offset:33792\n\t" \
    "ds_read_b64_tr_b16 a[20:21], v201 offset:40960\n\t" \
    "ds_read_b64_tr_b16 a[22:23], v201 offset:41984\n\t" \
    "ds_read_b64_tr_b16 a[88:89], v202 offset:32768\n\t" \
    "ds_read_b64_tr_b16 a[90:91], v202 offset:33792\n\t" \
    "ds_read_b64_tr_b16 a[92:93], v202 offset:40960\n\t" \
    "ds_read_b64_tr_b16 a[94:95], v202 offset:41984\n\t" \
    "s_waitcnt vmcnt(20) lgkmcnt(0)\n\t" \
    "s_barrier\n\t" \
    "ds_read_b64_tr_b16 a[24:25], v201 offset:49152\n\t" \
    "ds_read_b64_tr_b16 a[26:27], v201 offset:50176\n\t" \
    "ds_read_b64_tr_b16 a[28:29], v201 offset:57344\n\t" \
    "ds_read_b64_tr_b16 a[30:31], v201 offset:58368\n\t" \
    "ds_read_b64_tr_b16 a[96:97], v202 offset:49152\n\t" \
    "ds_read_b64_tr_b16 a[98:99], v202 offset:50176\n\t" \
    "ds_read_b64_tr_b16 a[100:101], v202 offset:57344\n\t" \
    "ds_read_b64_tr_b16 a[102:103], v202 offset:58368\n\t" \
    "s_add_u32 s46, s44, 0\n\t" \
    "s_addc_u32 s47, s45, 0\n\t" \
    "s_mul_i32 s69, s82, 9\n\t" \
    "s_add_u32 m0, s69, 0\n\t" \
    "s_nop 0\n\t" \
    "global_load_lds_dwordx4 v203, s[46:47]\n\t" \
    "s_add_u32 m0, s69, 1024\n\t" \
    "s_nop 0\n\t" \
    "global_load_lds_dwordx4 v204, s[46:47]\n\t" \
    "s_add_u32 m0, s69, 2048\n\t" \
    "s_nop 0\n\t" \
    "global_load_lds_dwordx4 v205, s[46:47]\n\t" \
    "s_add_u32 m0, s69, 3072\n\t" \
    "s_nop 0\n\t" \
    "global_load_lds_dwordx4 v206, s[46:47]\n\t" \
    "s_add_u32 m0, s69, 4096\n\t" \
    "s_nop 0\n\t" \
    "global_load_lds_dwordx4 v207, s[46:47]\n\t" \
    "s_add_u32 m0, s69, 5120\n\t" \
    "s_nop 0\n\t" \
    "global_load_lds_dwordx4 v208, s[46:47]\n\t" \
    "s_add_u32 m0, s69, 6144\n\t" \
    "s_nop 0\n\t" \
    "global_load_lds_dwordx4 v209, s[46:47]\n\t" \
    "s_add_u32 m0, s69, 7168\n\t" \
    "s_nop 0\n\t" \
    "global_load_lds_dwordx4 v210, s[46:47]\n\t" \
    "s_add_u32 m0, s69, 8192\n\t" \
    "s_nop 0\n\t" \
    "global_load_lds_dwordx4 v211, s[46:47]\n\t" \
    "s_min_u32 s69, s65, 63\n\t" \
    "s_mul_i32 s69, s69, 36864\n\t" \
    "s_add_u32 s52, s50, s69\n\t" \
    "s_addc_u32 s53, s51, 0\n\t" \
    "global_load_dwordx4 v[120:123], v203, s[52:53]\n\t" \
    "global_load_dwordx4 v[124:127], v204, s[52:53]\n\t" \
    "global_load_dwordx4 v[128:131], v205, s[52:53]\n\t" \
    "global_load_dwordx4 v[132:135], v206, s[52:53]\n\t" \
    "global_load_dwordx4 v[136:139], v207, s[52:53]\n\t" \
    "global_load_dwordx4 v[140:143], v208, s[52:53]\n\t" \
    "global_load_dwordx4 v[144:147], v209, s[52:53]\n\t" \
    "global_load_dwordx4 v[148:151], v210, s[52:53]\n\t" \
    "global_load_dwordx4 v[152:155], v211, s[52:53]\n\t" \
    "s_waitcnt vmcnt(34) lgkmcnt(0)\n\t" \
    "s_barrier\n\t" \
    "v_add_u32 v201, 0x10000, v201\n\t" \
    "v_add_u32 v202, 0x10000, v202\n\t" \
    "ds_read_b64_tr_b16 a[32:33], v201 offset:0\n\t" \
    "ds_read_b64_tr_b16 a[34:35], v201 offset:1024\n\t" \
    "ds_read_b64_tr_b16 a[36:37], v201 offset:8192\n\t" \
    "ds_read_b64_tr_b16 a[38:39], v201 offset:9216\n\t" \
    "ds_read_b64_tr_b16 a[104:105], v202 offset:0\n\t" \
    "ds_read_b64_tr_b16 a[106:107], v202 offset:1024\n\t" \
    "ds_read_b64_tr_b16 a[108:109], v202 offset:8192\n\t" \
    "ds_read_b64_tr_b16 a[110:111], v202 offset:9216\n\t" \
    "s_waitcnt vmcnt(30) lgkmcnt(0)\n\t" \
    "s_barrier\n\t" \
    "ds_read_b64_tr_b16 a[40:41], v201 offset:16384\n\t" \
    "ds_read_b64_tr_b16 a[42:43], v201 offset:17408\n\t" \
    "ds_read_b64_tr_b16 a[44:45], v201 offset:24576\n\t" \
    "ds_read_b64_tr_b16 a[46:47], v201 offset:25600\n\t" \
    "ds_read_b64_tr_b16 a[112:113], v202 offset:16384\n\t" \
    "ds_read_b64_tr_b16 a[114:115], v202 offset:17408\n\t" \
    "ds_read_b64_tr_b16 a[116:117], v202 offset:24576\n\t" \
    "ds_read_b64_tr_b16 a[118:119], v202 offset:25600\n\t" \
    "s_add_u32 s46, s44, 36864\n\t" \
    "s_addc_u32 s47, s45, 0\n\t" \
    "s_mul_i32 s69, s82, 9\n\t" \
    "s_add_u32 m0, s69, 36864\n\t" \
    "s_nop 0\n\t" \
    "global_load_lds_dwordx4 v203, s[46:47]\n\t" \
    "s_add_u32 m0, s69, 37888\n\t" \
    "s_nop 0\n\t" \
    "global_load_lds_dwordx4 v204, s[46:47]\n\t" \
    "s_add_u32 m0, s69, 38912\n\t" \
    "s_nop 0\n\t" \
    "global_load_lds_dwordx4 v205, s[46:47]\n\t" \
    "s_add_u32 m0, s69, 39936\n\t" \
    "s_nop 0\n\t" \
    "global_load_lds_dwordx4 v206, s[46:47]\n\t" \
    "s_add_u32 m0, s69, 40960\n\t" \
    "s_nop 0\n\t" \
    "global_load_lds_dwordx4 v207, s[46:47]\n\t" \
    "s_add_u32 m0, s69, 41984\n\t" \
    "s_nop 0\n\t" \
    "global_load_lds_dwordx4 v208, s[46:47]\n\t" \
    "s_add_u32 m0, s69, 43008\n\t" \
    "s_nop 0\n\t" \
    "global_load_lds_dwordx4 v209, s[46:47]\n\t" \
    "s_add_u32 m0, s69, 44032\n\t" \
    "s_nop 0\n\t" \
    "global_load_lds_dwordx4 v210, s[46:47]\n\t" \
    "s_add_u32 m0, s69, 45056\n\t" \
    "s_nop 0\n\t" \
    "global_load_lds_dwordx4 v211, s[46:47]\n\t" \
    "s_waitcnt vmcnt(35) lgkmcnt(0)\n\t" \
    "s_barrier\n\t" \
    "ds_read_b64_tr_b16 a[48:49], v201 offset:32768\n\t" \
    "ds_read_b64_tr_b16 a[50:51], v201 offset:33792\n\t" \
    "ds_read_b64_tr_b16 a[52:53], v201 offset:40960\n\t" \
    "ds_read_b64_tr_b16 a[54:55], v201 offset:41984\n\t" \
    "ds_read_b64_tr_b16 a[120:121], v202 offset:32768\n\t" \
    "ds_read_b64_tr_b16 a[122:123], v202 offset:33792\n\t" \
    "ds_read_b64_tr_b16 a[124:125], v202 offset:40960\n\t" \
    "ds_read_b64_tr_b16 a[126:127], v202 offset:41984\n\t" \
    "s_waitcnt vmcnt(31) lgkmcnt(0)\n\t" \
    "s_barrier\n\t" \
    "ds_read_b64_tr_b16 a[56:57], v201 offset:49152\n\t" \
    "ds_read_b64_tr_b16 a[58:59], v201 offset:50176\n\t" \
    "ds_read_b64_tr_b16 a[60:61], v201 offset:57344\n\t" \
    "ds_read_b64_tr_b16 a[62:63], v201 offset:58368\n\t" \
    "ds_read_b64_tr_b16 a[128:129], v202 offset:49152\n\t" \
    "ds_read_b64_tr_b16 a[130:131], v202 offset:50176\n\t" \
    "ds_read_b64_tr_b16 a[132:133], v202 offset:57344\n\t" \
    "ds_read_b64_tr_b16 a[134:135], v202 offset:58368\n\t" \
    "s_add_u32 s46, s44, 73728\n\t" \
    "s_addc_u32 s47, s45, 0\n\t" \
    "s_mul_i32 s69, s82, 9\n\t" \
    "s_add_u32 m0, s69, 73728\n\t" \
    "s_nop 0\n\t" \
    "global_load_lds_dwordx4 v203, s[46:47]\n\t" \
    "s_add_u32 m0, s69, 74752\n\t" \
    "s_nop 0\n\t" \
    "global_load_lds_dwordx4 v204, s[46:47]\n\t" \
    "s_add_u32 m0, s69, 75776\n\t" \
    "s_nop 0\n\t" \
    "global_load_lds_dwordx4 v205, s[46:47]\n\t" \
    "s_add_u32 m0, s69, 76800\n\t" \
    "s_nop 0\n\t" \
    "global_load_lds_dwordx4 v206, s[46:47]\n\t" \
    "s_add_u32 m0, s69, 77824\n\t" \
    "s_nop 0\n\t" \
    "global_load_lds_dwordx4 v207, s[46:47]\n\t" \
    "s_add_u32 m0, s69, 78848\n\t" \
    "s_nop 0\n\t" \
    "global_load_lds_dwordx4 v208, s[46:47]\n\t" \
    "s_add_u32 m0, s69, 79872\n\t" \
    "s_nop 0\n\t" \
    "global_load_lds_dwordx4 v209, s[46:47]\n\t" \
    "s_add_u32 m0, s69, 80896\n\t" \
    "s_nop 0\n\t" \
    "global_load_lds_dwordx4 v210, s[46:47]\n\t" \
    "s_add_u32 m0, s69, 81920\n\t" \
    "s_nop 0\n\t" \
    "global_load_lds_dwordx4 v211, s[46:47]\n\t" \
    "v_mbcnt_lo_u32_b32 v164, -1, 0\n\t" \
    "v_mbcnt_hi_u32_b32 v164, -1, v164\n\t" \
    "v_lshlrev_b32 v164, 6, v164\n\t" \
    "v_add_u32 v164, 6225920, v164\n\t" \
    "v_mov_b32 v165, 6234112\n\t" \
    "global_load_dword v165, v165, s[36:37] sc1\n\t" \
    "global_load_dwordx2 v[166:167], v164, s[36:37] sc1\n\t" \
    "global_load_dword v168, v164, s[36:37] offset:2048 sc1\n\t" \
    "s_waitcnt vmcnt(39) lgkmcnt(0)\n\t" \
    "s_barrier\n\t" \
    "v_add_u32 v201, 0x10000, v201\n\t" \
    "v_add_u32 v202, 0x10000, v202\n\t" \
    "ds_read_b64_tr_b16 a[64:65], v201 offset:0\n\t" \
    "ds_read_b64_tr_b16 a[66:67], v201 offset:1024\n\t" \
    "ds_read_b64_tr_b16 a[68:69], v201 offset:8192\n\t" \
    "ds_read_b64_tr_b16 a[70:71], v201 offset:9216\n\t" \
    "ds_read_b64_tr_b16 a[136:137], v202 offset:0\n\t" \
    "ds_read_b64_tr_b16 a[138:139], v202 offset:1024\n\t" \
    "ds_read_b64_tr_b16 a[140:141], v202 offset:8192\n\t" \
    "ds_read_b64_tr_b16 a[142:143], v202 offset:9216\n\t" \
    "v_add_u32 v201, 0xfffe0000, v201\n\t" \
    "v_add_u32 v202, 0xfffe0000, v202\n\t" \
    "gv_jn2_%=:\n\t" \
    "s_waitcnt lgkmcnt(0)\n\t" \
    "s_barrier\n\t" \
    "s_add_u32 s46, s44, 110592\n\t" \
    "s_addc_u32 s47, s45, 0\n\t" \
    "s_mul_i32 s69, s82, 9\n\t" \
    "s_add_u32 m0, s69, 110592\n\t" \
    "s_nop 0\n\t" \
    "global_load_lds_dwordx4 v203, s[46:47]\n\t" \
    "s_add_u32 m0, s69, 111616\n\t" \
    "s_nop 0\n\t" \
    "global_load_lds_dwordx4 v204, s[46:47]\n\t" \
    "s_add_u32 m0, s69, 112640\n\t" \
    "s_nop 0\n\t" \
    "global_load_lds_dwordx4 v205, s[46:47]\n\t" \
    "s_add_u32 m0, s69, 113664\n\t" \
    "s_nop 0\n\t" \
    "global_load_lds_dwordx4 v206, s[46:47]\n\t" \
    "s_add_u32 m0, s69, 114688\n\t" \
    "s_nop 0\n\t" \
    "global_load_lds_dwordx4 v207, s[46:47]\n\t" \
    "s_add_u32 m0, s69, 115712\n\t" \
    "s_nop 0\n\t" \
    "global_load_lds_dwordx4 v208, s[46:47]\n\t" \
    "s_add_u32 m0, s69, 116736\n\t" \
    "s_nop 0\n\t" \
    "global_load_lds_dwordx4 v209, s[46:47]\n\t" \
    "s_add_u32 m0, s69, 117760\n\t" \
    "s_nop 0\n\t" \
    "global_load_lds_dwordx4 v210, s[46:47]\n\t" \
    "s_add_u32 m0, s69, 118784\n\t" \
    "s_nop 0\n\t" \
    "global_load_lds_dwordx4 v211, s[46:47]\n\t" \
    "s_waitcnt vmcnt(39) lgkmcnt(0)\n\t" \
    "s_barrier\n\t" \
    "ds_read_b64_tr_b16 a[144:145], v212 offset:0\n\t" \
    "ds_read_b64_tr_b16 a[146:147], v212 offset:18432\n\t" \
    "ds_read_b64_tr_b16 a[160:161], v213 offset:0\n\t" \
    "ds_read_b64_tr_b16 a[162:163], v213 offset:18432\n\t" \
    "ds_read_b64_tr_b16 a[176:177], v214 offset:0\n\t" \
    "ds_read_b64_tr_b16 a[178:179], v214 offset:18432\n\t" \
    "s_waitcnt lgkmcnt(6)\n\t" \
    "ds_read_b64_tr_b16 a[192:193], v215 offset:0\n\t" \
    "ds_read_b64_tr_b16 a[194:195], v215 offset:18432\n\t" \
    "ds_read_b64_tr_b16 a[208:209], v212 offset:128\n\t" \
    "ds_read_b64_tr_b16 a[210:211], v212 offset:18560\n\t" \
    "ds_read_b64_tr_b16 a[224:225], v213 offset:128\n\t" \
    "ds_read_b64_tr_b16 a[226:227], v213 offset:18560\n\t" \
    "s_waitcnt lgkmcnt(6)\n\t" \
    "ds_read_b64_tr_b16 a[240:241], v214 offset:128\n\t" \
    "ds_read_b64_tr_b16 a[242:243], v214 offset:18560\n\t" \
    "ds_read_b64_tr_b16 v[224:225], v215 offset:128\n\t" \
    "ds_read_b64_tr_b16 v[226:227], v215 offset:18560\n\t" \
    "ds_read_b64_tr_b16 v[240:241], v212 offset:256\n\t" \
    "ds_read_b64_tr_b16 v[242:243], v212 offset:18688\n\t" \
    "s_waitcnt lgkmcnt(6)\n\t" \
    "v_add_u32 v212, 0x9000, v212\n\t" \
    "v_add_u32 v213, 0x9000, v213\n\t" \
    "v_add_u32 v214, 0x9000, v214\n\t" \
    "v_add_u32 v215, 0x9000, v215\n\t" \
    "s_waitcnt vmcnt(21) lgkmcnt(0)\n\t" \
    "s_barrier\n\t" \
    "ds_read_b64_tr_b16 a[148:149], v212 offset:0\n\t" \
    "ds_read_b64_tr_b16 a[150:151], v212 offset:18432\n\t" \
    "ds_read_b64_tr_b16 a[164:165], v213 offset:0\n\t" \
    "ds_read_b64_tr_b16 a[166:167], v213 offset:18432\n\t" \
    "ds_read_b64_tr_b16 a[180:181], v214 offset:0\n\t" \
    "ds_read_b64_tr_b16 a[182:183], v214 offset:18432\n\t" \
    "s_waitcnt lgkmcnt(6)\n\t" \
    "ds_read_b64_tr_b16 a[196:197], v215 offset:0\n\t" \
    "ds_read_b64_tr_b16 a[198:199], v215 offset:18432\n\t" \
    "ds_read_b64_tr_b16 a[212:213], v212 offset:128\n\t" \
    "ds_read_b64_tr_b16 a[214:215], v212 offset:18560\n\t" \
    "ds_read_b64_tr_b16 a[228:229], v213 offset:128\n\t" \
    "ds_read_b64_tr_b16 a[230:231], v213 offset:18560\n\t" \
    "s_waitcnt lgkmcnt(6)\n\t" \
    "ds_read_b64_tr_b16 a[244:245], v214 offset:128\n\t" \
    "ds_read_b64_tr_b16 a[246:247], v214 offset:18560\n\t" \
    "ds_read_b64_tr_b16 v[228:229], v215 offset:128\n\t" \
    "ds_read_b64_tr_b16 v[230:231], v215 offset:18560\n\t" \
    "ds_read_b64_tr_b16 v[244:245], v212 offset:256\n\t" \
    "ds_read_b64_tr_b16 v[246:247], v212 offset:18688\n\t" \
    "s_waitcnt lgkmcnt(6)\n\t" \
    "v_add_u32 v212, 0x9000, v212\n\t" \
    "v_add_u32 v213, 0x9000, v213\n\t" \
    "v_add_u32 v214, 0x9000, v214\n\t" \
    "v_add_u32 v215, 0x9000, v215\n\t" \
    "ds_write_b128 v3, v[120:123] offset:0\n\t" \
    "ds_write_b128 v3, v[124:127] offset:1024\n\t" \
    "ds_write_b128 v3, v[128:131] offset:2048\n\t" \
    "ds_write_b128 v3, v[132:135] offset:3072\n\t" \
    "ds_write_b128 v3, v[136:139] offset:4096\n\t" \
    "ds_write_b128 v3, v[140:143] offset:5120\n\t" \
    "ds_write_b128 v3, v[144:147] offset:6144\n\t" \
    "ds_write_b128 v3, v[148:151] offset:7168\n\t" \
    "ds_write_b128 v3, v[152:155] offset:8192\n\t" \
    "s_waitcnt vmcnt(12) lgkmcnt(0)\n\t" \
    "s_barrier\n\t" \
    "ds_read_b64_tr_b16 a[152:153], v212 offset:0\n\t" \
    "ds_read_b64_tr_b16 a[154:155], v212 offset:18432\n\t" \
    "ds_read_b64_tr_b16 a[168:169], v213 offset:0\n\t" \
    "ds_read_b64_tr_b16 a[170:171], v213 offset:18432\n\t" \
    "ds_read_b64_tr_b16 a[184:185], v214 offset:0\n\t" \
    "ds_read_b64_tr_b16 a[186:187], v214 offset:18432\n\t" \
    "s_waitcnt lgkmcnt(6)\n\t" \
    "ds_read_b64_tr_b16 a[200:201], v215 offset:0\n\t" \
    "ds_read_b64_tr_b16 a[202:203], v215 offset:18432\n\t" \
    "ds_read_b64_tr_b16 a[216:217], v212 offset:128\n\t" \
    "ds_read_b64_tr_b16 a[218:219], v212 offset:18560\n\t" \
    "ds_read_b64_tr_b16 a[232:233], v213 offset:128\n\t" \
    "ds_read_b64_tr_b16 a[234:235], v213 offset:18560\n\t" \
    "s_waitcnt lgkmcnt(6)\n\t" \
    "ds_read_b64_tr_b16 a[248:249], v214 offset:128\n\t" \
    "ds_read_b64_tr_b16 a[250:251], v214 offset:18560\n\t" \
    "ds_read_b64_tr_b16 v[232:233], v215 offset:128\n\t" \
    "ds_read_b64_tr_b16 v[234:235], v215 offset:18560\n\t" \
    "ds_read_b64_tr_b16 v[248:249], v212 offset:256\n\t" \
    "ds_read_b64_tr_b16 v[250:251], v212 offset:18688\n\t" \
    "s_waitcnt lgkmcnt(6)\n\t" \
    "v_add_u32 v212, 0x9000, v212\n\t" \
    "v_add_u32 v213, 0x9000, v213\n\t" \
    "v_add_u32 v214, 0x9000, v214\n\t" \
    "v_add_u32 v215, 0x9000, v215\n\t" \
    "s_min_u32 s69, s84, 63\n\t" \
    "s_mul_i32 s69, s69, 36864\n\t" \
    "s_add_u32 s52, s50, s69\n\t" \
    "s_addc_u32 s53, s51, 0\n\t" \
    "s_add_u32 m0, s89, 36864\n\t" \
    "s_nop 0\n\t" \
    "global_load_lds_dwordx4 v203, s[52:53]\n\t" \
    "s_add_u32 m0, s89, 37888\n\t" \
    "s_nop 0\n\t" \
    "global_load_lds_dwordx4 v204, s[52:53]\n\t" \
    "s_add_u32 m0, s89, 38912\n\t" \
    "s_nop 0\n\t" \
    "global_load_lds_dwordx4 v205, s[52:53]\n\t" \
    "s_add_u32 m0, s89, 39936\n\t" \
    "s_nop 0\n\t" \
    "global_load_lds_dwordx4 v206, s[52:53]\n\t" \
    "s_add_u32 m0, s89, 40960\n\t" \
    "s_nop 0\n\t" \
    "global_load_lds_dwordx4 v207, s[52:53]\n\t" \
    "s_add_u32 m0, s89, 41984\n\t" \
    "s_nop 0\n\t" \
    "global_load_lds_dwordx4 v208, s[52:53]\n\t" \
    "s_add_u32 m0, s89, 43008\n\t" \
    "s_nop 0\n\t" \
    "global_load_lds_dwordx4 v209, s[52:53]\n\t" \
    "s_add_u32 m0, s89, 44032\n\t" \
    "s_nop 0\n\t" \
    "global_load_lds_dwordx4 v210, s[52:53]\n\t" \
    "s_add_u32 m0, s89, 45056\n\t" \
    "s_nop 0\n\t" \
    "global_load_lds_dwordx4 v211, s[52:53]\n\t" \
    "s_waitcnt vmcnt(18)\n\t" \
    "gv_C3_%=:\n\t" \
    "v_cmp_ne_u32 s[54:55], v166, v168\n\t" \
    "v_sub_u32 v166, v166, v165\n\t" \
    "v_cmp_ne_u32 vcc, 1, v166\n\t" \
    "s_nop 1\n\t" \
    "s_or_b64 vcc, vcc, s[54:55]\n\t" \
    "s_cbranch_scc0 gv_Cok4_%=\n\t" \
    "s_cmp_eq_u32 s68, 0\n\t" \
    "s_cbranch_scc1 gv_F6_%=\n\t" \
    "s_sub_u32 s68, s68, 1\n\t" \
    "s_sleep 6\n\t" \
    "s_branch gv_Cag5_%=\n\t" \
    "gv_F6_%=:\n\t" \
    "v_mov_b32 v168, 5767424\n\t" \
    "v_mov_b32 v169, 4\n\t" \
    "global_store_dword v168, v169, s[36:37] sc1\n\t" \
    "s_waitcnt vmcnt(0)\n\t" \
    "s_branch gv_Cok4_%=\n\t" \
    "gv_Cag5_%=:\n\t" \
    "global_load_dwordx2 v[166:167], v164, s[36:37] sc1\n\t" \
    "global_load_dword v168, v164, s[36:37] offset:2048 sc1\n\t" \
    "s_waitcnt vmcnt(0)\n\t" \
    "s_branch gv_C3_%=\n\t" \
    "gv_Cok4_%=:\n\t" \
    "v_readlane_b32 s69, v167, 2\n\t" \
    "v_readlane_b32 s70, v167, 3\n\t" \
    "s_nop 1\n\t" \
    "s_cmp_lt_u32 s70, s69\n\t" \
    "s_cselect_b32 s69, 0, 8192\n\t" \
    "s_add_u32 s100, s36, s69\n\t" \
    "s_addc_u32 s101, s37, 0\n\t" \
    "s_cmp_lg_u32 s66, 0\n\t" \
    "s_cbranch_scc1 gv_Ca7_%=\n\t" \
    "v_mov_b32 v164, 5768192\n\t" \
    "s_mov_b64 exec, 1\n\t" \
    "global_atomic_add v164, v166, s[36:37] sc1\n\t" \
    "s_mov_b64 exec, -1\n\t" \
    "gv_Ca7_%=:\n\t" \
    "v_mov_b32_dpp v164, v167 quad_perm:[1,0,3,2] row_mask:0xf bank_mask:0xf\n\t" \
    "v_cmp_lt_u32 s[44:45], v164, v167\n\t" \
    "v_min_u32 v164, v164, v167\n\t" \
    "v_and_b32 v165, 1, v164\n\t" \
    "v_cmp_ne_u32 vcc, 0, v165\n\t" \
    "s_nop 1\n\t" \
    "s_and_b32 s44, s44, 0x55555555\n\t" \
    "s_and_b32 s69, vcc_lo, 0xaaaaaaaa\n\t" \
    "s_or_b32 s44, s44, s69\n\t" \
    "s_and_b32 s45, s45, 0x55555555\n\t" \
    "s_and_b32 s69, vcc_hi, 0xaaaaaaaa\n\t" \
    "s_or_b32 s45, s45, s69\n\t" \
    "s_waitcnt lgkmcnt(0)\n\t" \
    "s_barrier\n\t" \
    "ds_read_b128 v[24:27], v1 offset:0\n\t" \
    "ds_read_b128 v[28:31], v1 offset:18432\n\t" \
    "ds_read_b128 v[32:35], v2 offset:0\n\t" \
    "ds_read_b128 v[36:39], v2 offset:18432\n\t" \
    "ds_read_b128 v[40:43], v1 offset:128\n\t" \
    "ds_read_b128 v[44:47], v1 offset:18560\n\t" \
    "s_mov_b64 vcc, 0\n\t" \
    "s_min_u32 s69, s85, 63\n\t" \
    "s_mul_i32 s69, s69, 36864\n\t" \
    "s_add_u32 s52, s50, s69\n\t" \
    "s_addc_u32 s53, s51, 0\n\t" \
    "s_add_u32 s90, s90, 36864\n\t" \
    "s_cmp_eq_u32 s90, 110592\n\t" \
    "s_cselect_b32 s90, 0, s90\n\t" \
    "s_add_u32 s91, s90, 36864\n\t" \
    "s_cmp_eq_u32 s91, 110592\n\t" \
    "s_cselect_b32 s91, 0, s91\n\t" \
    "s_add_u32 s91, s91, s89\n\t" \
    ".p2align 6\n\t" \
    "s_nop 0\n\t" \
    "s_nop 0\n\t" \
    "gv_loop8_%=:\n\t" \
    "s_waitcnt lgkmcnt(5)\n\t" \
    "v_mfma_f32_16x16x32_bf16 v[72:75], a[0:3], v[24:27], 0\n\t" \
    "ds_read_b128 v[48:51], v2 offset:128\n\t" \
    "s_lshl_b32 s40, s65, 15\n\t" \
    "s_cselect_b32 s70, s65, 64\n\t" \
    "s_waitcnt lgkmcnt(5)\n\t" \
    "v_mfma_f32_16x16x32_bf16 v[76:79], a[0:3], v[28:31], 0\n\t" \
    "ds_read_b128 v[52:55], v2 offset:18560\n\t" \
    "s_mul_i32 s69, s70, 171\n\t" \
    "v_mfma_f32_16x16x32_bf16 v[80:83], a[72:75], v[24:27], 0\n\t" \
    "s_add_u32 m0, s91, 0\n\t" \
    "s_lshr_b32 s69, s69, 9\n\t" \
    "v_mfma_f32_16x16x32_bf16 v[84:87], a[72:75], v[28:31], 0\n\t" \
    "global_load_lds_dwordx4 v203, s[52:53]\n\t" \
    "s_waitcnt lgkmcnt(5)\n\t" \
    "v_mfma_f32_16x16x32_bf16 v[72:75], a[4:7], v[32:35], v[72:75]\n\t" \
    "ds_read_b128 v[56:59], v1 offset:256\n\t" \
    "s_mul_i32 s71, s69, 3\n\t" \
    "s_waitcnt lgkmcnt(5)\n\t" \
    "v_mfma_f32_16x16x32_bf16 v[76:79], a[4:7], v[36:39], v[76:79]\n\t" \
    "ds_read_b128 v[60:63], v1 offset:18688\n\t" \
    "s_sub_u32 s70, s70, s71\n\t" \
    "v_mfma_f32_16x16x32_bf16 v[80:83], a[76:79], v[32:35], v[80:83]\n\t" \
    "s_add_u32 m0, s91, 1024\n\t" \
    "s_mul_i32 s70, s70, 589824\n\t" \
    "v_mfma_f32_16x16x32_bf16 v[84:87], a[76:79], v[36:39], v[84:87]\n\t" \
    "global_load_lds_dwordx4 v204, s[52:53]\n\t" \
    "s_waitcnt lgkmcnt(5)\n\t" \
    "v_mfma_f32_16x16x32_bf16 v[72:75], a[8:11], v[40:43], v[72:75]\n\t" \
    "ds_read_b128 v[64:67], v2 offset:256\n\t" \
    "s_lshl1_add_u32 s71, s69, 4\n\t" \
    "s_waitcnt lgkmcnt(5)\n\t" \
    "v_mfma_f32_16x16x32_bf16 v[76:79], a[8:11], v[44:47], v[76:79]\n\t" \
    "ds_read_b128 v[68:71], v2 offset:18688\n\t" \
    "s_lshr_b64 s[48:49], s[44:45], s71\n\t" \
    "v_mfma_f32_16x16x32_bf16 v[80:83], a[80:83], v[40:43], v[80:83]\n\t" \
    "s_add_u32 m0, s91, 2048\n\t" \
    "s_lshl_b32 s71, s66, 1\n\t" \
    "v_mfma_f32_16x16x32_bf16 v[84:87], a[80:83], v[44:47], v[84:87]\n\t" \
    "global_load_lds_dwordx4 v205, s[52:53]\n\t" \
    "s_waitcnt lgkmcnt(5)\n\t" \
    "v_mfma_f32_16x16x32_bf16 v[72:75], a[12:15], v[48:51], v[72:75]\n\t" \
    "ds_read_b128 v[24:27], v1 offset:384\n\t" \
    "s_xor_b32 s71, s71, s48\n\t" \
    "s_waitcnt lgkmcnt(5)\n\t" \
    "v_mfma_f32_16x16x32_bf16 v[76:79], a[12:15], v[52:55], v[76:79]\n\t" \
    "ds_read_b128 v[28:31], v1 offset:18816\n\t" \
    "s_and_b32 s71, s71, 3\n\t" \
    "v_mfma_f32_16x16x32_bf16 v[80:83], a[84:87], v[48:51], v[80:83]\n\t" \
    "s_add_u32 m0, s91, 3072\n\t" \
    "s_lshl_b32 s71, s71, 13\n\t" \
    "v_mfma_f32_16x16x32_bf16 v[84:87], a[84:87], v[52:55], v[84:87]\n\t" \
    "global_load_lds_dwordx4 v206, s[52:53]\n\t" \
    "s_waitcnt lgkmcnt(5)\n\t" \
    "v_mfma_f32_16x16x32_bf16 v[72:75], a[16:19], v[56:59], v[72:75]\n\t" \
    "ds_read_b128 v[32:35], v2 offset:384\n\t" \
    "s_add_u32 s70, s70, s71\n\t" \
    "s_waitcnt lgkmcnt(5)\n\t" \
    "v_mfma_f32_16x16x32_bf16 v[76:79], a[16:19], v[60:63], v[76:79]\n\t" \
    "ds_read_b128 v[36:39], v2 offset:18816\n\t" \
    "s_lshl_b32 s69, s69, 21\n\t" \
    "v_mfma_f32_16x16x32_bf16 v[80:83], a[88:91], v[56:59], v[80:83]\n\t" \
    "s_add_u32 m0, s91, 4096\n\t" \
    "s_add_u32 s69, s69, 123731968\n\t" \
    "v_mfma_f32_16x16x32_bf16 v[84:87], a[88:91], v[60:63], v[84:87]\n\t" \
    "global_load_lds_dwordx4 v207, s[52:53]\n\t" \
    "s_waitcnt lgkmcnt(5)\n\t" \
    "v_mfma_f32_16x16x32_bf16 v[72:75], a[20:23], v[64:67], v[72:75]\n\t" \
    "ds_read_b128 v[40:43], v1 offset:512\n\t" \
    "v_add_u32 v153, s70, v19\n\t" \
    "s_waitcnt lgkmcnt(5)\n\t" \
    "v_mfma_f32_16x16x32_bf16 v[76:79], a[20:23], v[68:71], v[76:79]\n\t" \
    "ds_read_b128 v[44:47], v1 offset:18944\n\t" \
    "v_and_b32 v152, 0x80000, v153\n\t" \
    "v_mfma_f32_16x16x32_bf16 v[80:83], a[92:95], v[64:67], v[80:83]\n\t" \
    "s_add_u32 m0, s91, 5120\n\t" \
    "v_lshrrev_b32 v152, 5, v152\n\t" \
    "v_mfma_f32_16x16x32_bf16 v[84:87], a[92:95], v[68:71], v[84:87]\n\t" \
    "global_load_lds_dwordx4 v208, s[52:53]\n\t" \
    "s_waitcnt lgkmcnt(5)\n\t" \
    "v_mfma_f32_16x16x32_bf16 v[72:75], a[24:27], v[24:27], v[72:75]\n\t" \
    "ds_read_b128 v[48:51], v2 offset:512\n\t" \
    "v_xor_b32 v153, v153, v152\n\t" \
    "s_waitcnt lgkmcnt(5)\n\t" \
    "v_mfma_f32_16x16x32_bf16 v[76:79], a[24:27], v[28:31], v[76:79]\n\t" \
    "ds_read_b128 v[52:55], v2 offset:18944\n\t" \
    "v_add_u32 v154, s70, v20\n\t" \
    "v_mfma_f32_16x16x32_bf16 v[80:83], a[96:99], v[24:27], v[80:83]\n\t" \
    "s_add_u32 m0, s91, 6144\n\t" \
    "v_and_b32 v152, 0x80000, v154\n\t" \
    "v_mfma_f32_16x16x32_bf16 v[84:87], a[96:99], v[28:31], v[84:87]\n\t" \
    "global_load_lds_dwordx4 v209, s[52:53]\n\t" \
    "s_waitcnt lgkmcnt(5)\n\t" \
    "v_mfma_f32_16x16x32_bf16 v[72:75], a[28:31], v[32:35], v[72:75]\n\t" \
    "ds_read_b128 v[56:59], v1 offset:640\n\t" \
    "v_lshrrev_b32 v152, 5, v152\n\t" \
    "s_waitcnt lgkmcnt(5)\n\t" \
    "v_mfma_f32_16x16x32_bf16 v[76:79], a[28:31], v[36:39], v[76:79]\n\t" \
    "ds_read_b128 v[60:63], v1 offset:19072\n\t" \
    "v_xor_b32 v154, v154, v152\n\t" \
    "v_mfma_f32_16x16x32_bf16 v[80:83], a[100:103], v[32:35], v[80:83]\n\t" \
    "s_add_u32 m0, s91, 7168\n\t" \
    "v_add_u32 v155, s70, v21\n\t" \
    "v_mfma_f32_16x16x32_bf16 v[84:87], a[100:103], v[36:39], v[84:87]\n\t" \
    "global_load_lds_dwordx4 v210, s[52:53]\n\t" \
    "s_waitcnt lgkmcnt(5)\n\t" \
    "v_mfma_f32_16x16x32_bf16 v[72:75], a[32:35], v[40:43], v[72:75]\n\t" \
    "ds_read_b128 v[64:67], v2 offset:640\n\t" \
    "v_and_b32 v152, 0x80000, v155\n\t" \
    "s_waitcnt lgkmcnt(5)\n\t" \
    "v_mfma_f32_16x16x32_bf16 v[76:79], a[32:35], v[44:47], v[76:79]\n\t" \
    "ds_read_b128 v[68:71], v2 offset:19072\n\t" \
    "v_lshrrev_b32 v152, 5, v152\n\t" \
    "v_mfma_f32_16x16x32_bf16 v[80:83], a[104:107], v[40:43], v[80:83]\n\t" \
    "s_add_u32 m0, s91, 8192\n\t" \
    "v_xor_b32 v155, v155, v152\n\t" \
    "v_mfma_f32_16x16x32_bf16 v[84:87], a[104:107], v[44:47], v[84:87]\n\t" \
    "global_load_lds_dwordx4 v211, s[52:53]\n\t" \
    "s_waitcnt lgkmcnt(5)\n\t" \
    "v_mfma_f32_16x16x32_bf16 v[72:75], a[36:39], v[48:51], v[72:75]\n\t" \
    "ds_read_b128 v[24:27], v1 offset:768\n\t" \
    "s_add_u32 s48, s36, s69\n\t" \
    "s_addc_u32 s49, s37, 0\n\t" \
    "s_waitcnt lgkmcnt(5)\n\t" \
    "v_mfma_f32_16x16x32_bf16 v[76:79], a[36:39], v[52:55], v[76:79]\n\t" \
    "ds_read_b128 v[28:31], v1 offset:19200\n\t" \
    "v_add_u32 v22, s90, v0\n\t" \
    "v_mfma_f32_16x16x32_bf16 v[80:83], a[108:111], v[48:51], v[80:83]\n\t" \
    "v_xor_b32 v23, 64, v22\n\t" \
    "v_mfma_f32_16x16x32_bf16 v[84:87], a[108:111], v[52:55], v[84:87]\n\t" \
    "s_waitcnt lgkmcnt(5)\n\t" \
    "v_mfma_f32_16x16x32_bf16 v[72:75], a[40:43], v[56:59], v[72:75]\n\t" \
    "ds_read_b128 v[32:35], v2 offset:768\n\t" \
    "s_waitcnt lgkmcnt(5)\n\t" \
    "v_mfma_f32_16x16x32_bf16 v[76:79], a[40:43], v[60:63], v[76:79]\n\t" \
    "ds_read_b128 v[36:39], v2 offset:19200\n\t" \
    "v_mfma_f32_16x16x32_bf16 v[80:83], a[112:115], v[56:59], v[80:83]\n\t" \
    "v_mfma_f32_16x16x32_bf16 v[84:87], a[112:115], v[60:63], v[84:87]\n\t" \
    "s_waitcnt lgkmcnt(5)\n\t" \
    "v_mfma_f32_16x16x32_bf16 v[72:75], a[44:47], v[64:67], v[72:75]\n\t" \
    "ds_read_b128 v[40:43], v1 offset:896\n\t" \
    "s_waitcnt lgkmcnt(5)\n\t" \
    "v_mfma_f32_16x16x32_bf16 v[76:79], a[44:47], v[68:71], v[76:79]\n\t" \
    "ds_read_b128 v[44:47], v1 offset:19328\n\t" \
    "v_mfma_f32_16x16x32_bf16 v[80:83], a[116:119], v[64:67], v[80:83]\n\t" \
    "v_mfma_f32_16x16x32_bf16 v[84:87], a[116:119], v[68:71], v[84:87]\n\t" \
    "global_store_dwordx4 v153, v[196:199], s[48:49] sc1\n\t" \
    "s_waitcnt lgkmcnt(5)\n\t" \
    "v_mfma_f32_16x16x32_bf16 v[72:75], a[48:51], v[24:27], v[72:75]\n\t" \
    "ds_read_b128 v[48:51], v2 offset:896\n\t" \
    "s_waitcnt lgkmcnt(5)\n\t" \
    "v_mfma_f32_16x16x32_bf16 v[76:79], a[48:51], v[28:31], v[76:79]\n\t" \
    "ds_read_b128 v[52:55], v2 offset:19328\n\t" \
    "v_mfma_f32_16x16x32_bf16 v[80:83], a[120:123], v[24:27], v[80:83]\n\t" \
    "v_mfma_f32_16x16x32_bf16 v[84:87], a[120:123], v[28:31], v[84:87]\n\t" \
    "s_waitcnt lgkmcnt(5)\n\t" \
    "v_mfma_f32_16x16x32_bf16 v[72:75], a[52:55], v[32:35], v[72:75]\n\t" \
    "ds_read_b128 v[56:59], v1 offset:1024\n\t" \
    "s_waitcnt lgkmcnt(5)\n\t" \
    "v_mfma_f32_16x16x32_bf16 v[76:79], a[52:55], v[36:39], v[76:79]\n\t" \
    "ds_read_b128 v[60:63], v1 offset:19456\n\t" \
    "v_mfma_f32_16x16x32_bf16 v[80:83], a[124:127], v[32:35], v[80:83]\n\t" \
    "v_mfma_f32_16x16x32_bf16 v[84:87], a[124:127], v[36:39], v[84:87]\n\t" \
    "s_waitcnt lgkmcnt(5)\n\t" \
    "v_mfma_f32_16x16x32_bf16 v[72:75], a[56:59], v[40:43], v[72:75]\n\t" \
    "ds_read_b128 v[64:67], v2 offset:1024\n\t" \
    "s_waitcnt lgkmcnt(5)\n\t" \
    "v_mfma_f32_16x16x32_bf16 v[76:79], a[56:59], v[44:47], v[76:79]\n\t" \
    "ds_read_b128 v[68:71], v2 offset:19456\n\t" \
    "v_mfma_f32_16x16x32_bf16 v[80:83], a[128:131], v[40:43], v[80:83]\n\t" \
    "v_mfma_f32_16x16x32_bf16 v[84:87], a[128:131], v[44:47], v[84:87]\n\t" \
    "global_store_dwordx4 v154, v[196:199], s[48:49] sc1\n\t" \
    "s_waitcnt lgkmcnt(5)\n\t" \
    "v_mfma_f32_16x16x32_bf16 v[72:75], a[60:63], v[48:51], v[72:75]\n\t" \
    "s_waitcnt lgkmcnt(4)\n\t" \
    "v_mfma_f32_16x16x32_bf16 v[76:79], a[60:63], v[52:55], v[76:79]\n\t" \
    "v_mfma_f32_16x16x32_bf16 v[80:83], a[132:135], v[48:51], v[80:83]\n\t" \
    "v_mfma_f32_16x16x32_bf16 v[84:87], a[132:135], v[52:55], v[84:87]\n\t" \
    "s_waitcnt lgkmcnt(3)\n\t" \
    "v_mfma_f32_16x16x32_bf16 v[72:75], a[64:67], v[56:59], v[72:75]\n\t" \
    "s_waitcnt lgkmcnt(2)\n\t" \
    "v_mfma_f32_16x16x32_bf16 v[76:79], a[64:67], v[60:63], v[76:79]\n\t" \
    "v_mfma_f32_16x16x32_bf16 v[80:83], a[136:139], v[56:59], v[80:83]\n\t" \
    "v_mfma_f32_16x16x32_bf16 v[84:87], a[136:139], v[60:63], v[84:87]\n\t" \
    "s_waitcnt lgkmcnt(1)\n\t" \
    "v_mfma_f32_16x16x32_bf16 v[72:75], a[68:71], v[64:67], v[72:75]\n\t" \
    "s_waitcnt lgkmcnt(0)\n\t" \
    "v_mfma_f32_16x16x32_bf16 v[76:79], a[68:71], v[68:71], v[76:79]\n\t" \
    "v_mfma_f32_16x16x32_bf16 v[80:83], a[140:143], v[64:67], v[80:83]\n\t" \
    "v_mfma_f32_16x16x32_bf16 v[84:87], a[140:143], v[68:71], v[84:87]\n\t" \
    "global_store_dword v155, v196, s[48:49] sc1\n\t" \
    "v_mov_b32 v1, v22\n\t" \
    "v_mov_b32 v2, v23\n\t" \
    "s_nop 0\n\t" \
    "v_cvt_pk_bf16_f32 v104, v72, v73\n\t" \
    "v_cvt_pk_bf16_f32 v105, v74, v75\n\t" \
    "s_nop 0\n\t" \
    "v_cvt_pk_bf16_f32 v106, v80, v81\n\t" \
    "v_cvt_pk_bf16_f32 v107, v82, v83\n\t" \
    "ds_write_b128 v4, v[104:107] offset:0\n\t" \
    "v_cvt_pk_bf16_f32 v108, v76, v77\n\t" \
    "v_cvt_pk_bf16_f32 v109, v78, v79\n\t" \
    "v_cvt_pk_bf16_f32 v110, v84, v85\n\t" \
    "v_cvt_pk_bf16_f32 v111, v86, v87\n\t" \
    "ds_write_b128 v4, v[108:111] offset:1024\n\t" \
    "s_cmp_lt_i32 s86, 16\n\t" \
    "s_cbranch_scc1 gv_rest10_%=\n\t" \
    "gv_back11_%=:\n\t" \
    "s_waitcnt vmcnt(21) lgkmcnt(0)\n\t" \
    "s_barrier\n\t" \
    "ds_read_b128 v[164:167], v5 offset:0\n\t" \
    "ds_read_b128 v[168:171], v5 offset:1024\n\t" \
    "ds_read_b128 v[172:175], v5 offset:2048\n\t" \
    "ds_read_b128 v[176:179], v5 offset:3072\n\t" \
    "ds_read_b128 v[180:183], v5 offset:4096\n\t" \
    "ds_read_b128 v[184:187], v5 offset:5120\n\t" \
    "ds_read_b128 v[188:191], v5 offset:6144\n\t" \
    "ds_read_b128 v[192:195], v5 offset:7168\n\t" \
    "s_cmp_lt_i32 s86, 16\n\t" \
    "s_cselect_b32 s71, s86, s87\n\t" \
    "s_cmp_lg_u32 s66, 0\n\t" \
    "s_cbranch_scc1 gv_SF17_%=\n\t" \
    "s_lshl_b32 s69, s67, 7\n\t" \
    "s_add_u32 s69, s69, 3162112\n\t" \
    "s_add_u32 s70, s71, 0\n\t" \
    "v_mov_b32 v156, s69\n\t" \
    "v_mov_b32 v157, s70\n\t" \
    "s_mov_b64 exec, 1\n\t" \
    "global_store_dword v156, v157, s[100:101] sc1\n\t" \
    "s_mov_b64 exec, -1\n\t" \
    "gv_SF17_%=:\n\t" \
    "s_cmp_ge_u32 s86, 10\n\t" \
    "s_cbranch_scc1 gv_ef18_%=\n\t" \
    "s_cmp_lg_u32 s66, 2\n\t" \
    "s_cbranch_scc1 gv_ef18_%=\n\t" \
    "s_cmp_lt_u32 s86, 4\n\t" \
    "s_cbranch_scc1 gv_ef18_%=\n\t" \
    "s_lshr_b32 s69, s86, 1\n\t" \
    "s_lshl_b32 s69, s69, 6\n\t" \
    "s_add_u32 s69, s69, s67\n\t" \
    "s_lshl_b32 s69, s69, 6\n\t" \
    "s_add_u32 s69, s69, 5799936\n\t" \
    "v_mov_b32 v156, s69\n\t" \
    "v_mov_b32 v157, s86\n\t" \
    "global_store_dword v156, v157, s[36:37] sc1\n\t" \
    "gv_ef18_%=:\n\t" \
    "v_xor_b32 v4, 0x2000, v4\n\t" \
    "v_xor_b32 v5, 0x2000, v5\n\t" \
    "s_waitcnt lgkmcnt(7)\n\t" \
    "v_mfma_f32_16x16x32_bf16 v[72:75], v[164:167], a[144:147], 0\n\t" \
    "s_waitcnt lgkmcnt(6)\n\t" \
    "v_mfma_f32_16x16x32_bf16 v[76:79], v[168:171], a[144:147], 0\n\t" \
    "s_waitcnt lgkmcnt(5)\n\t" \
    "v_mfma_f32_16x16x32_bf16 v[72:75], v[172:175], a[148:151], v[72:75]\n\t" \
    "s_mov_b32 s87, s86\n\t" \
    "s_mov_b32 s86, s65\n\t" \
    "s_waitcnt lgkmcnt(4)\n\t" \
    "v_mfma_f32_16x16x32_bf16 v[76:79], v[176:179], a[148:151], v[76:79]\n\t" \
    "s_mov_b32 s65, s84\n\t" \
    "s_mov_b32 s84, s85\n\t" \
    "s_waitcnt lgkmcnt(3)\n\t" \
    "v_mfma_f32_16x16x32_bf16 v[72:75], v[180:183], a[152:155], v[72:75]\n\t" \
    "s_cmp_lt_u32 s84, 24\n\t" \
    "s_addc_u32 s85, s84, 1\n\t" \
    "s_waitcnt lgkmcnt(2)\n\t" \
    "v_mfma_f32_16x16x32_bf16 v[76:79], v[184:187], a[152:155], v[76:79]\n\t" \
    "s_min_u32 s69, s85, 63\n\t" \
    "s_mul_i32 s69, s69, 36864\n\t" \
    "s_waitcnt lgkmcnt(1)\n\t" \
    "v_mfma_f32_16x16x32_bf16 v[72:75], v[188:191], a[156:159], v[72:75]\n\t" \
    "s_add_u32 s52, s50, s69\n\t" \
    "s_addc_u32 s53, s51, 0\n\t" \
    "s_waitcnt lgkmcnt(0)\n\t" \
    "v_mfma_f32_16x16x32_bf16 v[76:79], v[192:195], a[156:159], v[76:79]\n\t" \
    "s_add_u32 s90, s90, 36864\n\t" \
    "s_cmp_eq_u32 s90, 110592\n\t" \
    "s_cselect_b32 s90, 0, s90\n\t" \
    "v_mfma_f32_16x16x32_bf16 v[80:83], v[164:167], a[160:163], 0\n\t" \
    "s_add_u32 s91, s90, 36864\n\t" \
    "s_cmp_eq_u32 s91, 110592\n\t" \
    "s_cselect_b32 s91, 0, s91\n\t" \
    "v_mfma_f32_16x16x32_bf16 v[84:87], v[168:171], a[160:163], 0\n\t" \
    "s_add_u32 s91, s91, s89\n\t" \
    "v_mfma_f32_16x16x32_bf16 v[80:83], v[172:175], a[164:167], v[80:83]\n\t" \
    "v_mfma_f32_16x16x32_bf16 v[84:87], v[176:179], a[164:167], v[84:87]\n\t" \
    "v_mfma_f32_16x16x32_bf16 v[80:83], v[180:183], a[168:171], v[80:83]\n\t" \
    "v_mfma_f32_16x16x32_bf16 v[84:87], v[184:187], a[168:171], v[84:87]\n\t" \
    "v_cvt_pk_bf16_f32 v104, v72, v73\n\t" \
    "v_mfma_f32_16x16x32_bf16 v[80:83], v[188:191], a[172:175], v[80:83]\n\t" \
    "v_cvt_pk_bf16_f32 v105, v74, v75\n\t" \
    "v_mfma_f32_16x16x32_bf16 v[84:87], v[192:195], a[172:175], v[84:87]\n\t" \
    "v_cvt_pk_bf16_f32 v106, v76, v77\n\t" \
    "v_mfma_f32_16x16x32_bf16 v[88:91], v[164:167], a[176:179], 0\n\t" \
    "v_cvt_pk_bf16_f32 v107, v78, v79\n\t" \
    "v_mfma_f32_16x16x32_bf16 v[92:95], v[168:171], a[176:179], 0\n\t" \
    "v_xor_b32 v18, s40, v8\n\t" \
    "v_mfma_f32_16x16x32_bf16 v[88:91], v[172:175], a[180:183], v[88:91]\n\t" \
    "global_store_dwordx4 v18, v[104:107], s[38:39] sc1\n\t" \
    "v_mfma_f32_16x16x32_bf16 v[92:95], v[176:179], a[180:183], v[92:95]\n\t" \
    "v_mfma_f32_16x16x32_bf16 v[88:91], v[180:183], a[184:187], v[88:91]\n\t" \
    "v_mfma_f32_16x16x32_bf16 v[92:95], v[184:187], a[184:187], v[92:95]\n\t" \
    "v_cvt_pk_bf16_f32 v108, v80, v81\n\t" \
    "v_mfma_f32_16x16x32_bf16 v[88:91], v[188:191], a[188:191], v[88:91]\n\t" \
    "v_cvt_pk_bf16_f32 v109, v82, v83\n\t" \
    "v_mfma_f32_16x16x32_bf16 v[92:95], v[192:195], a[188:191], v[92:95]\n\t" \
    "v_cvt_pk_bf16_f32 v110, v84, v85\n\t" \
    "v_mfma_f32_16x16x32_bf16 v[72:75], v[164:167], a[192:195], 0\n\t" \
    "v_cvt_pk_bf16_f32 v111, v86, v87\n\t" \
    "v_mfma_f32_16x16x32_bf16 v[76:79], v[168:171], a[192:195], 0\n\t" \
    "v_xor_b32 v18, s40, v9\n\t" \
    "v_mfma_f32_16x16x32_bf16 v[72:75], v[172:175], a[196:199], v[72:75]\n\t" \
    "global_store_dwordx4 v18, v[108:111], s[38:39] sc1\n\t" \
    "v_mfma_f32_16x16x32_bf16 v[76:79], v[176:179], a[196:199], v[76:79]\n\t" \
    "v_mfma_f32_16x16x32_bf16 v[72:75], v[180:183], a[200:203], v[72:75]\n\t" \
    "v_mfma_f32_16x16x32_bf16 v[76:79], v[184:187], a[200:203], v[76:79]\n\t" \
    "v_cvt_pk_bf16_f32 v104, v88, v89\n\t" \
    "v_mfma_f32_16x16x32_bf16 v[72:75], v[188:191], a[204:207], v[72:75]\n\t" \
    "v_cvt_pk_bf16_f32 v105, v90, v91\n\t" \
    "v_mfma_f32_16x16x32_bf16 v[76:79], v[192:195], a[204:207], v[76:79]\n\t" \
    "v_cvt_pk_bf16_f32 v106, v92, v93\n\t" \
    "v_mfma_f32_16x16x32_bf16 v[80:83], v[164:167], a[208:211], 0\n\t" \
    "v_cvt_pk_bf16_f32 v107, v94, v95\n\t" \
    "v_mfma_f32_16x16x32_bf16 v[84:87], v[168:171], a[208:211], 0\n\t" \
    "v_xor_b32 v18, s40, v10\n\t" \
    "v_mfma_f32_16x16x32_bf16 v[80:83], v[172:175], a[212:215], v[80:83]\n\t" \
    "global_store_dwordx4 v18, v[104:107], s[38:39] sc1\n\t" \
    "v_mfma_f32_16x16x32_bf16 v[84:87], v[176:179], a[212:215], v[84:87]\n\t" \
    "v_mfma_f32_16x16x32_bf16 v[80:83], v[180:183], a[216:219], v[80:83]\n\t" \
    "v_mfma_f32_16x16x32_bf16 v[84:87], v[184:187], a[216:219], v[84:87]\n\t" \
    "v_cvt_pk_bf16_f32 v108, v72, v73\n\t" \
    "v_mfma_f32_16x16x32_bf16 v[80:83], v[188:191], a[220:223], v[80:83]\n\t" \
    "v_cvt_pk_bf16_f32 v109, v74, v75\n\t" \
    "v_mfma_f32_16x16x32_bf16 v[84:87], v[192:195], a[220:223], v[84:87]\n\t" \
    "v_cvt_pk_bf16_f32 v110, v76, v77\n\t" \
    "v_mfma_f32_16x16x32_bf16 v[88:91], v[164:167], a[224:227], 0\n\t" \
    "v_cvt_pk_bf16_f32 v111, v78, v79\n\t" \
    "v_mfma_f32_16x16x32_bf16 v[92:95], v[168:171], a[224:227], 0\n\t" \
    "v_xor_b32 v18, s40, v11\n\t" \
    "v_mfma_f32_16x16x32_bf16 v[88:91], v[172:175], a[228:231], v[88:91]\n\t" \
    "global_store_dwordx4 v18, v[108:111], s[38:39] sc1\n\t" \
    "v_mfma_f32_16x16x32_bf16 v[92:95], v[176:179], a[228:231], v[92:95]\n\t" \
    "v_mfma_f32_16x16x32_bf16 v[88:91], v[180:183], a[232:235], v[88:91]\n\t" \
    "v_mfma_f32_16x16x32_bf16 v[92:95], v[184:187], a[232:235], v[92:95]\n\t" \
    "v_cvt_pk_bf16_f32 v104, v80, v81\n\t" \
    "v_mfma_f32_16x16x32_bf16 v[88:91], v[188:191], a[236:239], v[88:91]\n\t" \
    "v_cvt_pk_bf16_f32 v105, v82, v83\n\t" \
    "v_mfma_f32_16x16x32_bf16 v[92:95], v[192:195], a[236:239], v[92:95]\n\t" \
    "v_cvt_pk_bf16_f32 v106, v84, v85\n\t" \
    "v_mfma_f32_16x16x32_bf16 v[72:75], v[164:167], a[240:243], 0\n\t" \
    "v_cvt_pk_bf16_f32 v107, v86, v87\n\t" \
    "v_mfma_f32_16x16x32_bf16 v[76:79], v[168:171], a[240:243], 0\n\t" \
    "v_xor_b32 v18, s40, v12\n\t" \
    "v_mfma_f32_16x16x32_bf16 v[72:75], v[172:175], a[244:247], v[72:75]\n\t" \
    "global_store_dwordx4 v18, v[104:107], s[38:39] sc1\n\t" \
    "v_mfma_f32_16x16x32_bf16 v[76:79], v[176:179], a[244:247], v[76:79]\n\t" \
    "s_cbranch_vccnz gv_gol12_%=\n\t" \
    "gv_gob13_%=:\n\t" \
    "v_mfma_f32_16x16x32_bf16 v[72:75], v[180:183], a[248:251], v[72:75]\n\t" \
    "v_mfma_f32_16x16x32_bf16 v[76:79], v[184:187], a[248:251], v[76:79]\n\t" \
    "v_cvt_pk_bf16_f32 v108, v88, v89\n\t" \
    "v_mfma_f32_16x16x32_bf16 v[72:75], v[188:191], a[252:255], v[72:75]\n\t" \
    "v_cvt_pk_bf16_f32 v109, v90, v91\n\t" \
    "v_mfma_f32_16x16x32_bf16 v[76:79], v[192:195], a[252:255], v[76:79]\n\t" \
    "v_cvt_pk_bf16_f32 v110, v92, v93\n\t" \
    "v_mfma_f32_16x16x32_bf16 v[80:83], v[164:167], v[224:227], 0\n\t" \
    "v_cvt_pk_bf16_f32 v111, v94, v95\n\t" \
    "v_mfma_f32_16x16x32_bf16 v[84:87], v[168:171], v[224:227], 0\n\t" \
    "v_xor_b32 v18, s40, v13\n\t" \
    "v_mfma_f32_16x16x32_bf16 v[80:83], v[172:175], v[228:231], v[80:83]\n\t" \
    "global_store_dwordx4 v18, v[108:111], s[38:39] sc1\n\t" \
    "v_mfma_f32_16x16x32_bf16 v[84:87], v[176:179], v[228:231], v[84:87]\n\t" \
    "v_mfma_f32_16x16x32_bf16 v[80:83], v[180:183], v[232:235], v[80:83]\n\t" \
    "v_mfma_f32_16x16x32_bf16 v[84:87], v[184:187], v[232:235], v[84:87]\n\t" \
    "v_cvt_pk_bf16_f32 v104, v72, v73\n\t" \
    "v_mfma_f32_16x16x32_bf16 v[80:83], v[188:191], v[236:239], v[80:83]\n\t" \
    "v_cvt_pk_bf16_f32 v105, v74, v75\n\t" \
    "v_mfma_f32_16x16x32_bf16 v[84:87], v[192:195], v[236:239], v[84:87]\n\t" \
    "v_cvt_pk_bf16_f32 v106, v76, v77\n\t" \
    "v_mfma_f32_16x16x32_bf16 v[88:91], v[164:167], v[240:243], 0\n\t" \
    "v_cvt_pk_bf16_f32 v107, v78, v79\n\t" \
    "ds_read_b128 v[24:27], v22 offset:0\n\t" \
    "v_mfma_f32_16x16x32_bf16 v[92:95], v[168:171], v[240:243], 0\n\t" \
    "v_xor_b32 v18, s40, v14\n\t" \
    "ds_read_b128 v[28:31], v22 offset:18432\n\t" \
    "v_mfma_f32_16x16x32_bf16 v[88:91], v[172:175], v[244:247], v[88:91]\n\t" \
    "global_store_dwordx4 v18, v[104:107], s[38:39] sc1\n\t" \
    "ds_read_b128 v[32:35], v23 offset:0\n\t" \
    "v_mfma_f32_16x16x32_bf16 v[92:95], v[176:179], v[244:247], v[92:95]\n\t" \
    "ds_read_b128 v[36:39], v23 offset:18432\n\t" \
    "v_mfma_f32_16x16x32_bf16 v[88:91], v[180:183], v[248:251], v[88:91]\n\t" \
    "ds_read_b128 v[40:43], v22 offset:128\n\t" \
    "v_mfma_f32_16x16x32_bf16 v[92:95], v[184:187], v[248:251], v[92:95]\n\t" \
    "v_cvt_pk_bf16_f32 v108, v80, v81\n\t" \
    "ds_read_b128 v[44:47], v22 offset:18560\n\t" \
    "v_mfma_f32_16x16x32_bf16 v[88:91], v[188:191], v[252:255], v[88:91]\n\t" \
    "v_cvt_pk_bf16_f32 v109, v82, v83\n\t" \
    "v_mfma_f32_16x16x32_bf16 v[92:95], v[192:195], v[252:255], v[92:95]\n\t" \
    "v_cvt_pk_bf16_f32 v110, v84, v85\n\t" \
    "v_cvt_pk_bf16_f32 v111, v86, v87\n\t" \
    "v_xor_b32 v18, s40, v15\n\t" \
    "global_store_dwordx4 v18, v[108:111], s[38:39] sc1\n\t" \
    "s_nop 1\n\t" \
    "v_cvt_pk_bf16_f32 v104, v88, v89\n\t" \
    "v_cvt_pk_bf16_f32 v105, v90, v91\n\t" \
    "v_cvt_pk_bf16_f32 v106, v92, v93\n\t" \
    "v_cvt_pk_bf16_f32 v107, v94, v95\n\t" \
    "v_xor_b32 v18, s40, v16\n\t" \
    "global_store_dwordx4 v18, v[104:107], s[38:39] sc1\n\t" \
    "s_cmp_ge_u32 s86, 4\n\t" \
    "s_cbranch_scc1 gv_nofast9_%=\n\t" \
    "s_waitcnt vmcnt(0)\n\t" \
    "s_barrier\n\t" \
    "s_cmp_lg_u32 s66, 0\n\t" \
    "s_cbranch_scc1 gv_SF19_%=\n\t" \
    "s_lshl_b32 s69, s67, 7\n\t" \
    "s_add_u32 s69, s69, 3162112\n\t" \
    "s_add_u32 s70, s86, 0\n\t" \
    "v_mov_b32 v156, s69\n\t" \
    "v_mov_b32 v157, s70\n\t" \
    "s_mov_b64 exec, 1\n\t" \
    "global_store_dword v156, v157, s[100:101] sc1\n\t" \
    "s_mov_b64 exec, -1\n\t" \
    "gv_SF19_%=:\n\t" \
    "s_cmp_ge_u32 s86, 10\n\t" \
    "s_cbranch_scc1 gv_ef20_%=\n\t" \
    "s_cmp_lg_u32 s66, 2\n\t" \
    "s_cbranch_scc1 gv_ef20_%=\n\t" \
    "s_lshr_b32 s69, s86, 1\n\t" \
    "s_lshl_b32 s69, s69, 6\n\t" \
    "s_add_u32 s69, s69, s67\n\t" \
    "s_lshl_b32 s69, s69, 6\n\t" \
    "s_add_u32 s69, s69, 5799936\n\t" \
    "v_mov_b32 v156, s69\n\t" \
    "v_mov_b32 v157, s86\n\t" \
    "global_store_dword v156, v157, s[36:37] sc1\n\t" \
    "gv_ef20_%=:\n\t" \
    "gv_nofast9_%=:\n\t" \
    "s_cmp_lt_u32 s65, s88\n\t" \
    "s_cbranch_scc1 gv_loop8_%=\n\t" \
    "s_waitcnt vmcnt(0) lgkmcnt(0)\n\t" \
    "s_barrier\n\t" \
    "s_mov_b32 s65, 99\n\t" \
    "s_cmp_lg_u32 s66, 0\n\t" \
    "s_cbranch_scc1 gv_SF21_%=\n\t" \
    "s_lshl_b32 s69, s67, 7\n\t" \
    "s_add_u32 s69, s69, 3162112\n\t" \
    "s_add_u32 s70, s65, 0\n\t" \
    "v_mov_b32 v156, s69\n\t" \
    "v_mov_b32 v157, s70\n\t" \
    "s_mov_b64 exec, 1\n\t" \
    "global_store_dword v156, v157, s[100:101] sc1\n\t" \
    "s_mov_b64 exec, -1\n\t" \
    "gv_SF21_%=:\n\t" \
    "s_branch gv_end22_%=\n\t" \
    "gv_gol12_%=:\n\t" \
    "s_mov_b64 vcc, 0\n\t" \
    "v_mbcnt_lo_u32_b32 v120, -1, 0\n\t" \
    "v_and_b32 v121, 1, v120\n\t" \
    "v_lshlrev_b32 v121, 14, v121\n\t" \
    "v_bfe_u32 v123, v120, 1, 1\n\t" \
    "v_lshl_add_u32 v121, v123, 17, v121\n\t" \
    "v_bfe_u32 v123, v120, 2, 2\n\t" \
    "v_lshl_add_u32 v121, v123, 11, v121\n\t" \
    "v_bfe_u32 v123, v120, 4, 1\n\t" \
    "v_lshl_add_u32 v121, v123, 15, v121\n\t" \
    "s_bitcmp1_b32 s44, 2\n\t" \
    "s_cselect_b32 s63, 8192, 0\n\t" \
    "s_or_b32 s63, s63, 0x200000\n\t" \
    "v_add_u32 v121, s63, v121\n\t" \
    "s_waitcnt vmcnt(5)\n\t" \
    "v_readfirstlane_b32 s69, v122\n\t" \
    "s_nop 1\n\t" \
    "s_cmp_lg_u32 s69, 126\n\t" \
    "s_cbranch_scc1 gv_go23_%=\n\t" \
    "s_memrealtime s[56:57]\n\t" \
    "s_waitcnt lgkmcnt(0)\n\t" \
    "s_lshl_b32 s69, s56, 4\n\t" \
    "s_add_u32 s69, s69, 4160\n\t" \
    "s_add_u32 s70, s56, 24\n\t" \
    "s_lshr_b32 s70, s70, 4\n\t" \
    "v_lshrrev_b32 v123, 2, v120\n\t" \
    "v_add_u32 v123, s70, v123\n\t" \
    "v_and_b32 v123, 63, v123\n\t" \
    "v_lshlrev_b32 v123, 6, v123\n\t" \
    "v_and_b32 v122, 3, v120\n\t" \
    "v_lshl_add_u32 v123, v122, 12, v123\n\t" \
    "v_add_u32 v123, 5783552, v123\n\t" \
    "v_mov_b32 v120, s69\n\t" \
    "s_mov_b32 exec_hi, 0\n\t" \
    "global_store_dword v123, v120, s[36:37] sc1\n\t" \
    "global_store_dword v121, v120, s[36:37] sc1\n\t" \
    "s_mov_b32 exec_hi, -1\n\t" \
    "gv_go23_%=:\n\t" \
    "s_branch gv_gob13_%=\n\t" \
    "gv_rest10_%=:\n\t" \
    "s_waitcnt vmcnt(12) lgkmcnt(0)\n\t" \
    "s_cmp_ge_i32 s86, 0\n\t" \
    "s_cbranch_scc1 gv_back11_%=\n\t" \
    "s_barrier\n\t" \
    "s_cmp_lg_u32 s66, 1\n\t" \
    "s_cbranch_scc1 gv_nw124_%=\n\t" \
    "v_mov_b32 v120, 5769216\n\t" \
    "v_mov_b32 v121, 1\n\t" \
    "s_mov_b64 exec, 1\n\t" \
    "global_atomic_add v122, v120, v121, s[36:37] sc0 sc1\n\t" \
    "s_mov_b64 exec, -1\n\t" \
    "s_mov_b64 vcc, -1\n\t" \
    "gv_nw124_%=:\n\t" \
    "ds_read_b64_tr_b16 a[156:157], v212 offset:0\n\t" \
    "ds_read_b64_tr_b16 a[158:159], v212 offset:18432\n\t" \
    "ds_read_b64_tr_b16 a[172:173], v213 offset:0\n\t" \
    "ds_read_b64_tr_b16 a[174:175], v213 offset:18432\n\t" \
    "ds_read_b64_tr_b16 a[188:189], v214 offset:0\n\t" \
    "ds_read_b64_tr_b16 a[190:191], v214 offset:18432\n\t" \
    "s_waitcnt lgkmcnt(6)\n\t" \
    "ds_read_b64_tr_b16 a[204:205], v215 offset:0\n\t" \
    "ds_read_b64_tr_b16 a[206:207], v215 offset:18432\n\t" \
    "ds_read_b64_tr_b16 a[220:221], v212 offset:128\n\t" \
    "ds_read_b64_tr_b16 a[222:223], v212 offset:18560\n\t" \
    "ds_read_b64_tr_b16 a[236:237], v213 offset:128\n\t" \
    "ds_read_b64_tr_b16 a[238:239], v213 offset:18560\n\t" \
    "s_waitcnt lgkmcnt(6)\n\t" \
    "ds_read_b64_tr_b16 a[252:253], v214 offset:128\n\t" \
    "ds_read_b64_tr_b16 a[254:255], v214 offset:18560\n\t" \
    "ds_read_b64_tr_b16 v[236:237], v215 offset:128\n\t" \
    "ds_read_b64_tr_b16 v[238:239], v215 offset:18560\n\t" \
    "ds_read_b64_tr_b16 v[252:253], v212 offset:256\n\t" \
    "ds_read_b64_tr_b16 v[254:255], v212 offset:18688\n\t" \
    "s_waitcnt lgkmcnt(6)\n\t" \
    "v_add_u32 v212, 0x9000, v212\n\t" \
    "v_add_u32 v213, 0x9000, v213\n\t" \
    "v_add_u32 v214, 0x9000, v214\n\t" \
    "v_add_u32 v215, 0x9000, v215\n\t" \
    "s_branch gv_back11_%=\n\t" \
    "gv_end22_%=:\n\t" \
    "gv_last_%=:\n\t" \
    ""
#define ASM_CV \
    "v_mov_b32 v0, %0\n\t" \
    "v_mov_b32 v2, %2\n\t" \
    "v_subrev_u32 v1, 768, %1\n\t" \
    "v_mov_b32 v4, 0\n\t" \
    "v_mov_b32 v5, 0\n\t" \
    "v_mov_b32 v6, 0\n\t" \
    "v_mov_b32 v7, 0\n\t" \
    "v_mov_b32 v8, 0\n\t" \
    "v_mov_b32 v9, 0\n\t" \
    "v_mov_b32 v10, 0\n\t" \
    "v_mov_b32 v11, 0\n\t" \
    "v_mov_b32 v12, 0\n\t" \
    "v_mov_b32 v13, 0\n\t" \
    "v_mov_b32 v14, 0\n\t" \
    "v_mov_b32 v15, 0\n\t" \
    "v_mov_b32 v16, 0\n\t" \
    "v_mov_b32 v17, 0\n\t" \
    "v_mov_b32 v18, 0\n\t" \
    "v_mov_b32 v19, 0\n\t" \
    "v_mov_b32 v20, 0\n\t" \
    "v_mov_b32 v21, 0\n\t" \
    "v_mov_b32 v22, 0\n\t" \
    "v_mov_b32 v23, 0\n\t" \
    "v_mov_b32 v24, 0\n\t" \
    "v_mov_b32 v25, 0\n\t" \
    "v_mov_b32 v26, 0\n\t" \
    "v_mov_b32 v27, 0\n\t" \
    "v_mov_b32 v28, 0\n\t" \
    "v_mov_b32 v29, 0\n\t" \
    "v_mov_b32 v30, 0\n\t" \
    "v_mov_b32 v31, 0\n\t" \
    "v_mov_b32 v32, 0\n\t" \
    "v_mov_b32 v33, 0\n\t" \
    "v_mov_b32 v34, 0\n\t" \
    "v_mov_b32 v35, 0\n\t" \
    "s_waitcnt lgkmcnt(0)\n\t" \
    "ds_read_b64 v[36:37], v0 offset:0\n\t" \
    "ds_read_b64 v[38:39], v0 offset:8\n\t" \
    "ds_read_b64 v[40:41], v0 offset:32\n\t" \
    "ds_read_b64 v[42:43], v0 offset:40\n\t" \
    "ds_read_b128 v[44:47], v1 offset:768\n\t" \
    "ds_read_b128 v[48:51], v1 offset:2304\n\t" \
    "ds_read_b128 v[52:55], v1 offset:3840\n\t" \
    "ds_read_b128 v[56:59], v1 offset:5376\n\t" \
    "s_mov_b32 s69, 8\n\t" \
    "cv_c1_%=:\n\t" \
    "s_waitcnt lgkmcnt(3)\n\t" \
    "v_mfma_f32_16x16x32_bf16 v[4:7], v[36:39], v[44:47], v[4:7]\n\t" \
    "ds_read_b64 v[76:77], v0 offset:64\n\t" \
    "ds_read_b64 v[78:79], v0 offset:72\n\t" \
    "ds_read_b64 v[80:81], v0 offset:96\n\t" \
    "v_mfma_f32_16x16x32_bf16 v[20:23], v[40:43], v[44:47], v[20:23]\n\t" \
    "ds_read_b64 v[82:83], v0 offset:104\n\t" \
    "ds_read_b128 v[84:87], v1 offset:672\n\t" \
    "ds_read_b128 v[88:91], v1 offset:2208\n\t" \
    "s_waitcnt lgkmcnt(8)\n\t" \
    "v_mfma_f32_16x16x32_bf16 v[8:11], v[36:39], v[48:51], v[8:11]\n\t" \
    "ds_read_b128 v[92:95], v1 offset:3744\n\t" \
    "ds_read_b128 v[96:99], v1 offset:5280\n\t" \
    "v_mfma_f32_16x16x32_bf16 v[24:27], v[40:43], v[48:51], v[24:27]\n\t" \
    "s_waitcnt lgkmcnt(9)\n\t" \
    "v_mfma_f32_16x16x32_bf16 v[12:15], v[36:39], v[52:55], v[12:15]\n\t" \
    "v_mfma_f32_16x16x32_bf16 v[28:31], v[40:43], v[52:55], v[28:31]\n\t" \
    "s_waitcnt lgkmcnt(8)\n\t" \
    "v_mfma_f32_16x16x32_bf16 v[16:19], v[36:39], v[56:59], v[16:19]\n\t" \
    "v_mfma_f32_16x16x32_bf16 v[32:35], v[40:43], v[56:59], v[32:35]\n\t" \
    "s_waitcnt lgkmcnt(3)\n\t" \
    "v_mfma_f32_16x16x32_bf16 v[4:7], v[76:79], v[84:87], v[4:7]\n\t" \
    "ds_read_b64 v[36:37], v0 offset:128\n\t" \
    "ds_read_b64 v[38:39], v0 offset:136\n\t" \
    "ds_read_b64 v[40:41], v0 offset:160\n\t" \
    "v_mfma_f32_16x16x32_bf16 v[20:23], v[80:83], v[84:87], v[20:23]\n\t" \
    "ds_read_b64 v[42:43], v0 offset:168\n\t" \
    "ds_read_b128 v[44:47], v1 offset:576\n\t" \
    "ds_read_b128 v[48:51], v1 offset:2112\n\t" \
    "s_waitcnt lgkmcnt(8)\n\t" \
    "v_mfma_f32_16x16x32_bf16 v[8:11], v[76:79], v[88:91], v[8:11]\n\t" \
    "ds_read_b128 v[52:55], v1 offset:3648\n\t" \
    "ds_read_b128 v[56:59], v1 offset:5184\n\t" \
    "v_mfma_f32_16x16x32_bf16 v[24:27], v[80:83], v[88:91], v[24:27]\n\t" \
    "s_waitcnt lgkmcnt(9)\n\t" \
    "v_mfma_f32_16x16x32_bf16 v[12:15], v[76:79], v[92:95], v[12:15]\n\t" \
    "v_mfma_f32_16x16x32_bf16 v[28:31], v[80:83], v[92:95], v[28:31]\n\t" \
    "s_waitcnt lgkmcnt(8)\n\t" \
    "v_mfma_f32_16x16x32_bf16 v[16:19], v[76:79], v[96:99], v[16:19]\n\t" \
    "v_mfma_f32_16x16x32_bf16 v[32:35], v[80:83], v[96:99], v[32:35]\n\t" \
    "v_add_u32 v0, 128, v0\n\t" \
    "v_subrev_u32 v1, 192, v1\n\t" \
    "s_sub_u32 s69, s69, 1\n\t" \
    "s_cmp_lg_u32 s69, 0\n\t" \
    "s_cbranch_scc1 cv_c1_%=\n\t" \
    "s_waitcnt lgkmcnt(0)\n\t" \
    "ds_read_b64 v[36:37], v0 offset:0\n\t" \
    "ds_read_b64 v[38:39], v0 offset:8\n\t" \
    "ds_read_b64 v[40:41], v0 offset:32\n\t" \
    "ds_read_b64 v[42:43], v0 offset:40\n\t" \
    "ds_read_b128 v[48:51], v1 offset:2304\n\t" \
    "ds_read_b128 v[52:55], v1 offset:3840\n\t" \
    "ds_read_b128 v[56:59], v1 offset:5376\n\t" \
    "ds_read_b64 v[76:77], v0 offset:64\n\t" \
    "ds_read_b64 v[78:79], v0 offset:72\n\t" \
    "ds_read_b64 v[80:81], v0 offset:96\n\t" \
    "ds_read_b64 v[82:83], v0 offset:104\n\t" \
    "ds_read_b128 v[88:91], v1 offset:2208\n\t" \
    "ds_read_b128 v[92:95], v1 offset:3744\n\t" \
    "ds_read_b128 v[96:99], v1 offset:5280\n\t" \
    "s_mov_b32 s69, 4\n\t" \
    "cv_c2_%=:\n\t" \
    "s_waitcnt lgkmcnt(9)\n\t" \
    "v_mfma_f32_16x16x32_bf16 v[8:11], v[36:39], v[48:51], v[8:11]\n\t" \
    "ds_read_b64 v[116:117], v0 offset:128\n\t" \
    "ds_read_b64 v[118:119], v0 offset:136\n\t" \
    "ds_read_b64 v[120:121], v0 offset:160\n\t" \
    "v_mfma_f32_16x16x32_bf16 v[24:27], v[40:43], v[48:51], v[24:27]\n\t" \
    "ds_read_b64 v[122:123], v0 offset:168\n\t" \
    "ds_read_b128 v[128:131], v1 offset:2112\n\t" \
    "ds_read_b128 v[132:135], v1 offset:3648\n\t" \
    "s_waitcnt lgkmcnt(14)\n\t" \
    "v_mfma_f32_16x16x32_bf16 v[12:15], v[36:39], v[52:55], v[12:15]\n\t" \
    "ds_read_b128 v[136:139], v1 offset:5184\n\t" \
    "v_mfma_f32_16x16x32_bf16 v[28:31], v[40:43], v[52:55], v[28:31]\n\t" \
    "s_waitcnt lgkmcnt(14)\n\t" \
    "v_mfma_f32_16x16x32_bf16 v[16:19], v[36:39], v[56:59], v[16:19]\n\t" \
    "ds_read_b64 v[156:157], v0 offset:192\n\t" \
    "v_mfma_f32_16x16x32_bf16 v[32:35], v[40:43], v[56:59], v[32:35]\n\t" \
    "s_waitcnt lgkmcnt(10)\n\t" \
    "v_mfma_f32_16x16x32_bf16 v[8:11], v[76:79], v[88:91], v[8:11]\n\t" \
    "ds_read_b64 v[158:159], v0 offset:200\n\t" \
    "ds_read_b64 v[160:161], v0 offset:224\n\t" \
    "ds_read_b64 v[162:163], v0 offset:232\n\t" \
    "v_mfma_f32_16x16x32_bf16 v[24:27], v[80:83], v[88:91], v[24:27]\n\t" \
    "ds_read_b128 v[168:171], v1 offset:2016\n\t" \
    "ds_read_b128 v[172:175], v1 offset:3552\n\t" \
    "s_waitcnt lgkmcnt(14)\n\t" \
    "v_mfma_f32_16x16x32_bf16 v[12:15], v[76:79], v[92:95], v[12:15]\n\t" \
    "ds_read_b128 v[176:179], v1 offset:5088\n\t" \
    "v_mfma_f32_16x16x32_bf16 v[28:31], v[80:83], v[92:95], v[28:31]\n\t" \
    "s_waitcnt lgkmcnt(14)\n\t" \
    "v_mfma_f32_16x16x32_bf16 v[16:19], v[76:79], v[96:99], v[16:19]\n\t" \
    "v_mfma_f32_16x16x32_bf16 v[32:35], v[80:83], v[96:99], v[32:35]\n\t" \
    "s_waitcnt lgkmcnt(9)\n\t" \
    "v_mfma_f32_16x16x32_bf16 v[8:11], v[116:119], v[128:131], v[8:11]\n\t" \
    "ds_read_b64 v[36:37], v0 offset:256\n\t" \
    "ds_read_b64 v[38:39], v0 offset:264\n\t" \
    "ds_read_b64 v[40:41], v0 offset:288\n\t" \
    "v_mfma_f32_16x16x32_bf16 v[24:27], v[120:123], v[128:131], v[24:27]\n\t" \
    "ds_read_b64 v[42:43], v0 offset:296\n\t" \
    "ds_read_b128 v[48:51], v1 offset:1920\n\t" \
    "ds_read_b128 v[52:55], v1 offset:3456\n\t" \
    "s_waitcnt lgkmcnt(14)\n\t" \
    "v_mfma_f32_16x16x32_bf16 v[12:15], v[116:119], v[132:135], v[12:15]\n\t" \
    "ds_read_b128 v[56:59], v1 offset:4992\n\t" \
    "v_mfma_f32_16x16x32_bf16 v[28:31], v[120:123], v[132:135], v[28:31]\n\t" \
    "s_waitcnt lgkmcnt(14)\n\t" \
    "v_mfma_f32_16x16x32_bf16 v[16:19], v[116:119], v[136:139], v[16:19]\n\t" \
    "ds_read_b64 v[76:77], v0 offset:320\n\t" \
    "v_mfma_f32_16x16x32_bf16 v[32:35], v[120:123], v[136:139], v[32:35]\n\t" \
    "s_waitcnt lgkmcnt(10)\n\t" \
    "v_mfma_f32_16x16x32_bf16 v[8:11], v[156:159], v[168:171], v[8:11]\n\t" \
    "ds_read_b64 v[78:79], v0 offset:328\n\t" \
    "ds_read_b64 v[80:81], v0 offset:352\n\t" \
    "ds_read_b64 v[82:83], v0 offset:360\n\t" \
    "v_mfma_f32_16x16x32_bf16 v[24:27], v[160:163], v[168:171], v[24:27]\n\t" \
    "ds_read_b128 v[88:91], v1 offset:1824\n\t" \
    "ds_read_b128 v[92:95], v1 offset:3360\n\t" \
    "s_waitcnt lgkmcnt(14)\n\t" \
    "v_mfma_f32_16x16x32_bf16 v[12:15], v[156:159], v[172:175], v[12:15]\n\t" \
    "ds_read_b128 v[96:99], v1 offset:4896\n\t" \
    "v_mfma_f32_16x16x32_bf16 v[28:31], v[160:163], v[172:175], v[28:31]\n\t" \
    "s_waitcnt lgkmcnt(14)\n\t" \
    "v_mfma_f32_16x16x32_bf16 v[16:19], v[156:159], v[176:179], v[16:19]\n\t" \
    "v_mfma_f32_16x16x32_bf16 v[32:35], v[160:163], v[176:179], v[32:35]\n\t" \
    "v_add_u32 v0, 256, v0\n\t" \
    "v_subrev_u32 v1, 384, v1\n\t" \
    "s_sub_u32 s69, s69, 1\n\t" \
    "s_cmp_lg_u32 s69, 0\n\t" \
    "s_cbranch_scc1 cv_c2_%=\n\t" \
    "s_waitcnt lgkmcnt(0)\n\t" \
    "s_mov_b32 s69, 4\n\t" \
    "cv_c3_%=:\n\t" \
    "s_waitcnt lgkmcnt(7)\n\t" \
    "v_mfma_f32_16x16x32_bf16 v[12:15], v[36:39], v[52:55], v[12:15]\n\t" \
    "ds_read_b64 v[116:117], v0 offset:128\n\t" \
    "ds_read_b64 v[118:119], v0 offset:136\n\t" \
    "ds_read_b64 v[120:121], v0 offset:160\n\t" \
    "v_mfma_f32_16x16x32_bf16 v[28:31], v[40:43], v[52:55], v[28:31]\n\t" \
    "ds_read_b64 v[122:123], v0 offset:168\n\t" \
    "ds_read_b128 v[132:135], v1 offset:3648\n\t" \
    "ds_read_b128 v[136:139], v1 offset:5184\n\t" \
    "s_waitcnt lgkmcnt(12)\n\t" \
    "v_mfma_f32_16x16x32_bf16 v[16:19], v[36:39], v[56:59], v[16:19]\n\t" \
    "ds_read_b64 v[156:157], v0 offset:192\n\t" \
    "ds_read_b64 v[158:159], v0 offset:200\n\t" \
    "ds_read_b64 v[160:161], v0 offset:224\n\t" \
    "v_mfma_f32_16x16x32_bf16 v[32:35], v[40:43], v[56:59], v[32:35]\n\t" \
    "s_waitcnt lgkmcnt(10)\n\t" \
    "v_mfma_f32_16x16x32_bf16 v[12:15], v[76:79], v[92:95], v[12:15]\n\t" \
    "ds_read_b64 v[162:163], v0 offset:232\n\t" \
    "ds_read_b128 v[172:175], v1 offset:3552\n\t" \
    "ds_read_b128 v[176:179], v1 offset:5088\n\t" \
    "v_mfma_f32_16x16x32_bf16 v[28:31], v[80:83], v[92:95], v[28:31]\n\t" \
    "s_waitcnt lgkmcnt(12)\n\t" \
    "v_mfma_f32_16x16x32_bf16 v[16:19], v[76:79], v[96:99], v[16:19]\n\t" \
    "v_mfma_f32_16x16x32_bf16 v[32:35], v[80:83], v[96:99], v[32:35]\n\t" \
    "s_waitcnt lgkmcnt(7)\n\t" \
    "v_mfma_f32_16x16x32_bf16 v[12:15], v[116:119], v[132:135], v[12:15]\n\t" \
    "ds_read_b64 v[36:37], v0 offset:256\n\t" \
    "ds_read_b64 v[38:39], v0 offset:264\n\t" \
    "ds_read_b64 v[40:41], v0 offset:288\n\t" \
    "v_mfma_f32_16x16x32_bf16 v[28:31], v[120:123], v[132:135], v[28:31]\n\t" \
    "ds_read_b64 v[42:43], v0 offset:296\n\t" \
    "ds_read_b128 v[52:55], v1 offset:3456\n\t" \
    "ds_read_b128 v[56:59], v1 offset:4992\n\t" \
    "s_waitcnt lgkmcnt(12)\n\t" \
    "v_mfma_f32_16x16x32_bf16 v[16:19], v[116:119], v[136:139], v[16:19]\n\t" \
    "ds_read_b64 v[76:77], v0 offset:320\n\t" \
    "ds_read_b64 v[78:79], v0 offset:328\n\t" \
    "ds_read_b64 v[80:81], v0 offset:352\n\t" \
    "v_mfma_f32_16x16x32_bf16 v[32:35], v[120:123], v[136:139], v[32:35]\n\t" \
    "s_waitcnt lgkmcnt(10)\n\t" \
    "v_mfma_f32_16x16x32_bf16 v[12:15], v[156:159], v[172:175], v[12:15]\n\t" \
    "ds_read_b64 v[82:83], v0 offset:360\n\t" \
    "ds_read_b128 v[92:95], v1 offset:3360\n\t" \
    "ds_read_b128 v[96:99], v1 offset:4896\n\t" \
    "v_mfma_f32_16x16x32_bf16 v[28:31], v[160:163], v[172:175], v[28:31]\n\t" \
    "s_waitcnt lgkmcnt(12)\n\t" \
    "v_mfma_f32_16x16x32_bf16 v[16:19], v[156:159], v[176:179], v[16:19]\n\t" \
    "v_mfma_f32_16x16x32_bf16 v[32:35], v[160:163], v[176:179], v[32:35]\n\t" \
    "v_add_u32 v0, 256, v0\n\t" \
    "v_subrev_u32 v1, 384, v1\n\t" \
    "s_sub_u32 s69, s69, 1\n\t" \
    "s_cmp_lg_u32 s69, 0\n\t" \
    "s_cbranch_scc1 cv_c3_%=\n\t" \
    "s_waitcnt lgkmcnt(0)\n\t" \
    "s_mov_b32 s69, 4\n\t" \
    "cv_c4_%=:\n\t" \
    "s_waitcnt lgkmcnt(5)\n\t" \
    "v_mfma_f32_16x16x32_bf16 v[16:19], v[36:39], v[56:59], v[16:19]\n\t" \
    "ds_read_b64 v[116:117], v0 offset:128\n\t" \
    "ds_read_b64 v[118:119], v0 offset:136\n\t" \
    "ds_read_b64 v[120:121], v0 offset:160\n\t" \
    "v_mfma_f32_16x16x32_bf16 v[32:35], v[40:43], v[56:59], v[32:35]\n\t" \
    "ds_read_b64 v[122:123], v0 offset:168\n\t" \
    "ds_read_b128 v[136:139], v1 offset:5184\n\t" \
    "ds_read_b64 v[156:157], v0 offset:192\n\t" \
    "s_waitcnt lgkmcnt(6)\n\t" \
    "v_mfma_f32_16x16x32_bf16 v[16:19], v[76:79], v[96:99], v[16:19]\n\t" \
    "ds_read_b64 v[158:159], v0 offset:200\n\t" \
    "ds_read_b64 v[160:161], v0 offset:224\n\t" \
    "ds_read_b64 v[162:163], v0 offset:232\n\t" \
    "v_mfma_f32_16x16x32_bf16 v[32:35], v[80:83], v[96:99], v[32:35]\n\t" \
    "ds_read_b128 v[176:179], v1 offset:5088\n\t" \
    "s_waitcnt lgkmcnt(5)\n\t" \
    "v_mfma_f32_16x16x32_bf16 v[16:19], v[116:119], v[136:139], v[16:19]\n\t" \
    "ds_read_b64 v[36:37], v0 offset:256\n\t" \
    "ds_read_b64 v[38:39], v0 offset:264\n\t" \
    "ds_read_b64 v[40:41], v0 offset:288\n\t" \
    "v_mfma_f32_16x16x32_bf16 v[32:35], v[120:123], v[136:139], v[32:35]\n\t" \
    "ds_read_b64 v[42:43], v0 offset:296\n\t" \
    "ds_read_b128 v[56:59], v1 offset:4992\n\t" \
    "ds_read_b64 v[76:77], v0 offset:320\n\t" \
    "s_waitcnt lgkmcnt(6)\n\t" \
    "v_mfma_f32_16x16x32_bf16 v[16:19], v[156:159], v[176:179], v[16:19]\n\t" \
    "ds_read_b64 v[78:79], v0 offset:328\n\t" \
    "ds_read_b64 v[80:81], v0 offset:352\n\t" \
    "ds_read_b64 v[82:83], v0 offset:360\n\t" \
    "v_mfma_f32_16x16x32_bf16 v[32:35], v[160:163], v[176:179], v[32:35]\n\t" \
    "ds_read_b128 v[96:99], v1 offset:4896\n\t" \
    "v_add_u32 v0, 256, v0\n\t" \
    "v_subrev_u32 v1, 384, v1\n\t" \
    "s_sub_u32 s69, s69, 1\n\t" \
    "s_cmp_lg_u32 s69, 0\n\t" \
    "s_cbranch_scc1 cv_c4_%=\n\t" \
    "s_waitcnt lgkmcnt(0)\n\t" \
    "s_nop 7\n\t" \
    "ds_write_b128 v2, v[4:7] offset:0\n\t" \
    "ds_write_b128 v2, v[8:11] offset:1024\n\t" \
    "ds_write_b128 v2, v[12:15] offset:2048\n\t" \
    "ds_write_b128 v2, v[16:19] offset:3072\n\t" \
    "ds_write_b128 v2, v[20:23] offset:4096\n\t" \
    "ds_write_b128 v2, v[24:27] offset:5120\n\t" \
    "ds_write_b128 v2, v[28:31] offset:6144\n\t" \
    "ds_write_b128 v2, v[32:35] offset:7168\n\t" \
    "s_waitcnt lgkmcnt(0)\n\t" \
    "cv_last_%=:\n\t" \
    ""
#define ASM_EA \
    "s_bitcmp1_b32 %1, 2\n\t" \
    "s_cselect_b32 s40, 0x30, 0x28\n\t" \
    "s_cmp_lt_u32 %1, 64\n\t" \
    "s_cselect_b32 s40, s40, 0x38\n\t" \
    "s_load_dwordx2 s[42:43], %0, s40\n\t" \
    "s_load_dword s48, %0, 0x40\n\t" \
    "s_getpc_b64 s[44:45]\n\t" \
    "ea_pc_%=:\n\t" \
    "s_bfe_u32 s40, %1, 0x20003\n\t" \
    "s_lshl_b32 s40, s40, 15\n\t" \
    "v_lshl_add_u32 v1, %2, 7, s40\n\t" \
    "v_min_u32 v1, code_end_marker-ea_pc_%=, v1\n\t" \
    "s_mov_b32 m0, 155648\n\t" \
    "v_readfirstlane_b32 s41, %2\n\t" \
    "global_load_lds_dword v1, s[44:45]\n\t" \
    "s_bitcmp1_b32 %1, 2\n\t" \
    "s_cselect_b32 s40, 24, 16\n\t" \
    "s_lshr_b32 s47, %1, 3\n\t" \
    "s_cmp_ge_u32 s47, s40\n\t" \
    "s_cbranch_scc1 ea_done1_%=\n\t" \
    "s_mov_b32 s46, pad_qk-ea_pc_%=\n\t" \
    "s_mov_b32 s40, pad_vv-ea_pc_%=\n\t" \
    "s_bitcmp1_b32 %1, 2\n\t" \
    "s_cselect_b32 s46, pad_gx-ea_pc_%=, s46\n\t" \
    "s_cselect_b32 s40, pad_gv-ea_pc_%=, s40\n\t" \
    "s_cmp_lt_u32 %1, 64\n\t" \
    "s_cselect_b32 s40, s46, s40\n\t" \
    "s_add_u32 s44, s44, s40\n\t" \
    "s_addc_u32 s45, s45, 0\n\t" \
    "s_swappc_b64 s[46:47], s[44:45]\n\t" \
    "s_lshl_b32 m0, s41, 4\n\t" \
    "s_and_b32 s40, %1, 3\n\t" \
    "s_lshl_b32 s40, s40, 11\n\t" \
    "s_bfe_u32 s46, %1, 0x30003\n\t" \
    "s_lshl_b32 s46, s46, 8\n\t" \
    "s_add_u32 s40, s40, s46\n\t" \
    "s_and_b32 s41, s41, 0x80\n\t" \
    "s_xor_b32 s40, s40, s41\n\t" \
    "v_lshrrev_b32 v1, 3, %2\n\t" \
    "v_and_b32 v1, 6, v1\n\t" \
    "v_xor_b32 v1, v1, %2\n\t" \
    "v_and_b32 v1, 15, v1\n\t" \
    "v_lshlrev_b32 v1, 4, v1\n\t" \
    "v_xor_b32 v1, s40, v1\n\t" \
    "v_lshrrev_b32 v2, 4, %2\n\t" \
    "v_lshl_add_u32 v1, v2, 13, v1\n\t" \
    "s_mov_b32 s40, 16\n\t" \
    "s_waitcnt lgkmcnt(0)\n\t" \
    "ea_lp2_%=:\n\t" \
    "global_load_lds_dwordx4 v1, s[42:43]\n\t" \
    "v_add_u32 v1, 0x20000, v1\n\t" \
    "s_add_u32 m0, m0, 4096\n\t" \
    "s_sub_u32 s40, s40, 1\n\t" \
    "s_cmp_lg_u32 s40, 0\n\t" \
    "s_cbranch_scc1 ea_lp2_%=\n\t" \
    "ea_done1_%=:\n\t" \
    "s_waitcnt lgkmcnt(0)\n\t" \
    "ea_last_%=:\n\t" \
    ""
#define ASM_CLOB "v0", "v1", "v2", "v3", "v4", "v5", "v6", "v7", "v8", "v9", "v10", "v11", "v12", "v13", "v14", "v15", "v16", "v17", "v18", "v19", "v20", "v21", "v22", "v23", "v24", "v25", "v26", "v27", "v28", "v29", "v30", "v31", "v32", "v33", "v34", "v35", "v36", "v37", "v38", "v39", "v40", "v41", "v42", "v43", "v44", "v45", "v46", "v47", "v48", "v49", "v50", "v51", "v52", "v53", "v54", "v55", "v56", "v57", "v58", "v59", "v60", "v61", "v62", "v63", "v64", "v65", "v66", "v67", "v68", "v69", "v70", "v71", "v72", "v73", "v74", "v75", "v76", "v77", "v78", "v79", "v80", "v81", "v82", "v83", "v84", "v85", "v86", "v87", "v88", "v89", "v90", "v91", "v92", "v93", "v94", "v95", "v96", "v97", "v98", "v99", "v100", "v101", "v102", "v103", "v104", "v105", "v106", "v107", "v108", "v109", "v110", "v111", "v112", "v113", "v114", "v115", "v116", "v117", "v118", "v119", "v120", "v121", "v122", "v123", "v124", "v125", "v126", "v127", "v128", "v129", "v130", "v131", "v132", "v133", "v134", "v135", "v136", "v137", "v138", "v139", "v140", "v141", "v142", "v143", "v144", "v145", "v146", "v147", "v148", "v149", "v150", "v151", "v152", "v153", "v154", "v155", "v156", "v157", "v158", "v159", "v160", "v161", "v162", "v163", "v164", "v165", "v166", "v167", "v168", "v169", "v170", "v171", "v172", "v173", "v174", "v175", "v176", "v177", "v178", "v179", "v180", "v181", "v182", "v183", "v184", "v185", "v186", "v187", "v188", "v189", "v190", "v191", "v192", "v193", "v194", "v195", "v196", "v197", "v198", "v199", "v200", "v201", "v202", "v203", "v204", "v205", "v206", "v207", "v208", "v209", "v210", "v211", "v212", "v213", "v214", "v215", "v222", "v223", "v224", "v225", "v226", "v227", "v228", "v229", "v230", "v231", "v232", "v233", "v234", "v235", "v236", "v237", "v238", "v239", "v240", "v241", "v242", "v243", "v244", "v245", "v246", "v247", "v248", "v249", "v250", "v251", "v252", "v253", "v254", "v255", "a0", "a1", "a2", "a3", "a4", "a5", "a6", "a7", "a8", "a9", "a10", "a11", "a12", "a13", "a14", "a15", "a16", "a17", "a18", "a19", "a20", "a21", "a22", "a23", "a24", "a25", "a26", "a27", "a28", "a29", "a30", "a31", "a32", "a33", "a34", "a35", "a36", "a37", "a38", "a39", "a40", "a41", "a42", "a43", "a44", "a45", "a46", "a47", "a48", "a49", "a50", "a51", "a52", "a53", "a54", "a55", "a56", "a57", "a58", "a59", "a60", "a61", "a62", "a63", "a64", "a65", "a66", "a67", "a68", "a69", "a70", "a71", "a72", "a73", "a74", "a75", "a76", "a77", "a78", "a79", "a80", "a81", "a82", "a83", "a84", "a85", "a86", "a87", "a88", "a89", "a90", "a91", "a92", "a93", "a94", "a95", "a96", "a97", "a98", "a99", "a100", "a101", "a102", "a103", "a104", "a105", "a106", "a107", "a108", "a109", "a110", "a111", "a112", "a113", "a114", "a115", "a116", "a117", "a118", "a119", "a120", "a121", "a122", "a123", "a124", "a125", "a126", "a127", "a128", "a129", "a130", "a131", "a132", "a133", "a134", "a135", "a136", "a137", "a138", "a139", "a140", "a141", "a142", "a143", "a144", "a145", "a146", "a147", "a148", "a149", "a150", "a151", "a152", "a153", "a154", "a155", "a156", "a157", "a158", "a159", "a160", "a161", "a162", "a163", "a164", "a165", "a166", "a167", "a168", "a169", "a170", "a171", "a172", "a173", "a174", "a175", "a176", "a177", "a178", "a179", "a180", "a181", "a182", "a183", "a184", "a185", "a186", "a187", "a188", "a189", "a190", "a191", "a192", "a193", "a194", "a195", "a196", "a197", "a198", "a199", "a200", "a201", "a202", "a203", "a204", "a205", "a206", "a207", "a208", "a209", "a210", "a211", "a212", "a213", "a214", "a215", "a216", "a217", "a218", "a219", "a220", "a221", "a222", "a223", "a224", "a225", "a226", "a227", "a228", "a229", "a230", "a231", "a232", "a233", "a234", "a235", "a236", "a237", "a238", "a239", "a240", "a241", "a242", "a243", "a244", "a245", "a246", "a247", "a248", "a249", "a250", "a251", "a252", "a253", "a254", "a255", "s33", "s34", "s35", "s36", "s37", "s38", "s39", "s40", "s41", "s42", "s43", "s44", "s45", "s46", "s47", "s48", "s49", "s50", "s51", "s52", "s53", "s54", "s55", "s56", "s57", "s58", "s59", "s60", "s61", "s62", "s63", "s64", "s65", "s66", "s67", "s68", "s69", "s70", "s71", "s72", "s73", "s74", "s75", "s76", "s77", "s78", "s79", "s80", "s81", "s82", "s83", "s84", "s85", "s86", "s87", "s88", "s89", "s90", "s91", "s92", "s93", "s94", "s95", "s96", "s97", "s98", "s99", "s100", "s101", "vcc", "scc", "memory"
#define ASM_CLOBW "v32", "v33", "v34", "v35", "v36", "v37", "v38", "v39", "v40", "v41", "v42", "v43", "v44", "v45", "v46", "v47", "v48", "v49", "v50", "v51", "v52", "v53", "v54", "v55", "v56", "v57", "v58", "v59", "v60", "v61", "v62", "v63", "v64", "v65", "v66", "v67", "v68", "v69", "v70", "v71", "v72", "v73", "v74", "v75", "v76", "v77", "v78", "v79", "v80", "v81", "v82", "v83", "v84", "v85", "v86", "v87", "v88", "v89", "v90", "v91", "v92", "v93", "v94", "v95", "v96", "v97", "v98", "v99", "v100", "v101", "v102", "v103", "v104", "v105", "v106", "v107", "v108", "v109", "v110", "v111", "v112", "v113", "v114", "v115", "v116", "v117", "v118", "v119", "v120", "v121", "v122", "v123", "v124", "v125", "v126", "v127", "v128", "v129", "v130", "v131", "v132", "v133", "v134", "v135", "v136", "v137", "v138", "v139", "v140", "v141", "v142", "v143", "v144", "v145", "v146", "v147", "v148", "v149", "v150", "v151", "v152", "v153", "v154", "v155", "v156", "v157", "v158", "v159", "v160", "v161", "v162", "v163", "v164", "v165", "v166", "v167", "v168", "v169", "v170", "v171", "v172", "v173", "v174", "v175", "v176", "v177", "v178", "v179", "v180", "v181", "v182", "v183", "v184", "v185", "v186", "v187", "v188", "v189", "v190", "v191", "v192", "v193", "v194", "v195", "v196", "v197", "v198", "v199", "v222", "v223", "v224", "v225", "v226", "v227", "v228", "v229", "v230", "v231", "v232", "v233", "v234", "v235", "v236", "v237", "v238", "v239", "v240", "v241", "v242", "v243", "v244", "v245", "v246", "v247", "v248", "v249", "v250", "v251", "v252", "v253", "v254", "v255", "a0", "a1", "a2", "a3", "a4", "a5", "a6", "a7", "a8", "a9", "a10", "a11", "a12", "a13", "a14", "a15", "a16", "a17", "a18", "a19", "a20", "a21", "a22", "a23", "a24", "a25", "a26", "a27", "a28", "a29", "a30", "a31", "a32", "a33", "a34", "a35", "a36", "a37", "a38", "a39", "a40", "a41", "a42", "a43", "a44", "a45", "a46", "a47", "a48", "a49", "a50", "a51", "a52", "a53", "a54", "a55", "a56", "a57", "a58", "a59", "a60", "a61", "a62", "a63", "a64", "a65", "a66", "a67", "a68", "a69", "a70", "a71", "a72", "a73", "a74", "a75", "a76", "a77", "a78", "a79", "a80", "a81", "a82", "a83", "a84", "a85", "a86", "a87", "a88", "a89", "a90", "a91", "a92", "a93", "a94", "a95", "a96", "a97", "a98", "a99", "a100", "a101", "a102", "a103", "a104", "a105", "a106", "a107", "a108", "a109", "a110", "a111", "a112", "a113", "a114", "a115", "a116", "a117", "a118", "a119", "a120", "a121", "a122", "a123", "a124", "a125", "a126", "a127", "a128", "a129", "a130", "a131", "a132", "a133", "a134", "a135", "a136", "a137", "a138", "a139", "a140", "a141", "a142", "a143", "a144", "a145", "a146", "a147", "a148", "a149", "a150", "a151", "a152", "a153", "a154", "a155", "a156", "a157", "a158", "a159", "a160", "a161", "a162", "a163", "a164", "a165", "a166", "a167", "a168", "a169", "a170", "a171", "a172", "a173", "a174", "a175", "a176", "a177", "a178", "a179", "a180", "a181", "a182", "a183", "a184", "a185", "a186", "a187", "a188", "a189", "a190", "a191", "a192", "a193", "a194", "a195", "a196", "a197", "a198", "a199", "a200", "a201", "a202", "a203", "a204", "a205", "a206", "a207", "a208", "a209", "a210", "a211", "a212", "a213", "a214", "a215", "a216", "a217", "a218", "a219", "a220", "a221", "a222", "a223", "a224", "a225", "a226", "a227", "a228", "a229", "a230", "a231", "a232", "a233", "a234", "a235", "a236", "a237", "a238", "a239", "a240", "a241", "a242", "a243", "a244", "a245", "a246", "a247", "a248", "a249", "a250", "a251", "a252", "a253", "a254", "a255", "s33", "s34", "s35", "s36", "s37", "s38", "s39", "s40", "s41", "s42", "s43", "s44", "s45", "s46", "s47", "s48", "s49", "s50", "s51", "s52", "s53", "s54", "s55", "s56", "s57", "s58", "s59", "s60", "s61", "s62", "s63", "s64", "s65", "s66", "s67", "s68", "s69", "s70", "s71", "s72", "s73", "s74", "s75", "s76", "s77", "s78", "s79", "s80", "s81", "s82", "s83", "s84", "s85", "s86", "s87", "s88", "s89", "s90", "s91", "s92", "s93", "s94", "s95", "s96", "s97", "s98", "s99", "s100", "s101", "vcc", "scc", "memory"
#define CV_CLOB "v0", "v1", "v2", "v3", "v4", "v5", "v6", "v7", "v8", "v9", "v10", "v11", "v12", "v13", "v14", "v15", "v16", "v17", "v18", "v19", "v20", "v21", "v22", "v23", "v24", "v25", "v26", "v27", "v28", "v29", "v30", "v31", "v32", "v33", "v34", "v35", "v36", "v37", "v38", "v39", "v40", "v41", "v42", "v43", "v44", "v45", "v46", "v47", "v48", "v49", "v50", "v51", "v52", "v53", "v54", "v55", "v56", "v57", "v58", "v59", "v60", "v61", "v62", "v63", "v64", "v65", "v66", "v67", "v68", "v69", "v70", "v71", "v72", "v73", "v74", "v75", "v76", "v77", "v78", "v79", "v80", "v81", "v82", "v83", "v84", "v85", "v86", "v87", "v88", "v89", "v90", "v91", "v92", "v93", "v94", "v95", "v96", "v97", "v98", "v99", "v100", "v101", "v102", "v103", "v104", "v105", "v106", "v107", "v108", "v109", "v110", "v111", "v112", "v113", "v114", "v115", "v116", "v117", "v118", "v119", "v120", "v121", "v122", "v123", "v124", "v125", "v126", "v127", "v128", "v129", "v130", "v131", "v132", "v133", "v134", "v135", "v136", "v137", "v138", "v139", "v140", "v141", "v142", "v143", "v144", "v145", "v146", "v147", "v148", "v149", "v150", "v151", "v152", "v153", "v154", "v155", "v156", "v157", "v158", "v159", "v160", "v161", "v162", "v163", "v164", "v165", "v166", "v167", "v168", "v169", "v170", "v171", "v172", "v173", "v174", "v175", "v176", "v177", "v178", "v179", "v180", "v181", "v182", "v183", "v184", "v185", "v186", "v187", "v188", "v189", "v190", "v191", "v192", "v193", "v194", "v195", "s69", "scc", "memory"
static constexpr unsigned G_STEP = 0x10000;
static constexpr unsigned G_WS_CTL = 0x300000;
static constexpr unsigned G_WS_FIX = 0x580000;
static constexpr unsigned G_WS_DONE = 0x5f0000;
static constexpr unsigned G_WS_EP = 0x5f2000;
static constexpr unsigned G_WS_STAMPS = 0xc000000;
static constexpr unsigned G_C_TASK = 0x200;
static constexpr unsigned G_C_GX = 0x1000;
static constexpr unsigned G_C_GV = 0x4000;
static constexpr unsigned G_C_KM = 0x8000;
static constexpr unsigned G_C_UM = 0x9000;
static constexpr unsigned G_C_PC = 0xc000;
static constexpr unsigned G_C_YC = 0xd000;
static constexpr unsigned G_C_GQ = 0x10000;
static constexpr unsigned G_C_FX = 0x280000;
static constexpr unsigned G_C_ERR = 0x280100;
static constexpr unsigned G_C_ARR = 0x280400;
static constexpr unsigned G_C_GO = 0x280800;
static constexpr unsigned G_C_UT = 0x281000;
static constexpr unsigned G_C_RDY = 0x282000;
static constexpr unsigned G_C_GT = 0x284000;
static constexpr unsigned G_C_KG = 0x288000;
static constexpr unsigned G_C_EF = 0x288000;
static constexpr unsigned G_C_RW = 0x280000;
static constexpr unsigned G_WS_H = 0x0;
static constexpr unsigned G_HS8 = 0x80000;
static constexpr unsigned G_HS4 = 0x40000;
static constexpr unsigned G_UXS = 0x200000;
static constexpr unsigned G_UMS = 0x90000;
static constexpr unsigned G_PROBE = 0x1b9400;
static constexpr unsigned G_D_QX = 0x600000;
static constexpr unsigned G_D_KX = 0x1600000;
static constexpr unsigned G_D_KM = 0x2600000;
static constexpr unsigned G_D_UX = 0x2e00000;
static constexpr unsigned G_D_UM = 0x7600000;
static constexpr unsigned G_D_PX = 0xa200000;
static constexpr unsigned G_D_PM = 0xa600000;
static constexpr unsigned G_D_KT = 0xa700000;
static constexpr unsigned G_D_UT = 0xac00000;
static constexpr unsigned G_D_YB = 0xaf00000;
static constexpr unsigned G_D_END = 0xb200000;
static constexpr unsigned G_SKM = 0x7;
static constexpr unsigned G_S1 = 0x18;
static constexpr unsigned G_G2 = 0x0;
static constexpr unsigned G_S0 = 0x7;
static constexpr unsigned G_RSW = 0x0;
static constexpr unsigned G_GVN = 0x2;
static constexpr unsigned G_STG = 0x9000;
static constexpr unsigned G_KROW = 0x30;
static constexpr unsigned G_NHL = 0x1;
#define FLG(x_) (kDefault[5])
#define FLH(x_) (kDefault[7])
static constexpr int kDefault[8] = {PRM_DEFAULT};
#define MFMA32(a_, b_, c_) __builtin_amdgcn_mfma_f32_16x16x32_bf16(__builtin_bit_cast(bf16x8, a_), __builtin_bit_cast(bf16x8, b_), c_, 0, 0, 0)
#define MFMA16(a_, b_, c_) __builtin_amdgcn_mfma_f32_16x16x16bf16_1k(__builtin_bit_cast(s16x4, a_), __builtin_bit_cast(s16x4, b_), c_, 0, 0, 0)
#define WLD8(d_, voff_, base_, off_) asm volatile("s_nop 4\n\tglobal_load_dwordx2 %0, %1, %2 offset:%3 sc1" : "=v"(d_) : "v"(voff_), "s"(base_), "i"(off_) : "memory")
#define WLD16(d_, voff_, base_, off_) asm volatile("s_nop 4\n\tglobal_load_dwordx4 %0, %1, %2 offset:%3 sc1" : "=v"(d_) : "v"(voff_), "s"(base_), "i"(off_) : "memory")
#define WST16(voff_, d_, base_, off_) asm volatile("s_nop 4\n\tglobal_store_dwordx4 %0, %1, %2 offset:%3 sc1\n\ts_nop 1" :: "v"(voff_), "v"(d_), "s"(base_), "i"(off_) : "memory")
#define OMOD ""
#define OST8(p_, d_) asm volatile("global_store_dwordx2 %0, %1, off " OMOD :: "v"(p_), "v"(d_) : "memory")
#define OST16(p_, d_) asm volatile("global_store_dwordx4 %0, %1, off " OMOD "\n\ts_nop 1" :: "v"(p_), "v"(d_) : "memory")
#define OWT 0
#define OWM "sc1"
#define OST16W(p_, d_) asm volatile("global_store_dwordx4 %0, %1, off " OWM "\n\ts_nop 1" :: "v"(p_), "v"(d_) : "memory")
#define WLD16X4(d0, d1, d2, d3, voff_, base_) asm volatile("s_nop 4\n\tglobal_load_dwordx4 %0, %4, %5 sc1\n\tglobal_load_dwordx4 %1, %4, %5 offset:1024 sc1\n\t" \
    "global_load_dwordx4 %2, %4, %5 offset:2048 sc1\n\tglobal_load_dwordx4 %3, %4, %5 offset:3072 sc1" \
    : "=&v"(d0), "=&v"(d1), "=&v"(d2), "=&v"(d3) : "v"(voff_), "s"(base_) : "memory")

__device__ __forceinline__ u32x4 pack8(const f32x4& a, const f32x4& b) {
    return u32x4{pk2bf(a[0], a[1]), pk2bf(a[2], a[3]), pk2bf(b[0], b[1]), pk2bf(b[2], b[3])};
}
template <class T> __device__ __forceinline__ T* uni(T* q) {
    const u64 v = (u64)q;
    unsigned lo = __builtin_amdgcn_readfirstlane((unsigned)v), hi = __builtin_amdgcn_readfirstlane((unsigned)(v >> 32));
    asm volatile("s_nop 4" : "+s"(lo), "+s"(hi));
    return (T*)(((u64)hi << 32) | lo);
}
#define WC(p_) ((char*)(p_).X)
template <bool FAST = false>
__device__ __forceinline__ void wait_word(const MainP& p, const char* f, int want, unsigned code) {
    unsigned tries = 0;
#pragma unroll 1
    while (__any((int)__hip_atomic_load((const gu32*)f, RLX_AGENT) < want)) {
        if (++tries > kWaitCap) { __hip_atomic_store((gu32*)(p.ws + G_WS_CTL + G_C_ERR), code, RLX_AGENT); break; }
        if ((tries & 1023u) == 0u && __hip_atomic_load((const gu32*)(p.ws + G_WS_CTL + G_C_ERR), RLX_AGENT) != 0xffffffffu) break;
        if (FAST) __builtin_amdgcn_s_sleep(1); else __builtin_amdgcn_s_sleep(10);
    }
}

__device__ __forceinline__ int skew(int s, int id) { return (s ^ (G_SKM * id)) & 63; }
__device__ __forceinline__ unsigned um_base(int s, unsigned nm) { return G_D_UM + (s / 3) * 0x200000 + (s % 3) * G_UMS + ((nm >> (2 + s / 3)) & 1) * 8192; }
typedef GAS unsigned long long gu64;
#define IST(k_, slot_)
__device__ __forceinline__ void init_done(char* ws, int slot, unsigned val) {
    const unsigned e = __hip_atomic_load((const gu32*)(ws + G_WS_EP), RLX_AGENT);
    __hip_atomic_store((gu64*)(ws + G_WS_DONE + slot * 64), ((u64)val << 32) | (e + 1u), RLX_AGENT);
}
__device__ __forceinline__ void init_wait(char* ws, int w, int lane) {
    if (w == 0) {
        const unsigned e = __hip_atomic_load((const gu32*)(ws + G_WS_EP), RLX_AGENT);
        for (unsigned tries = 0; tries < kWaitCap; ++tries) {
            const unsigned d0 = __hip_atomic_load((const gu32*)(ws + G_WS_DONE + lane * 64), RLX_AGENT), d1 = __hip_atomic_load((const gu32*)(ws + G_WS_DONE + 2048 + lane * 64), RLX_AGENT);
            if (!__any((d0 != e + 1u) | (d1 != e + 1u))) break;
            __builtin_amdgcn_s_sleep(8);
        }
    }
    __syncthreads();
    if (w == 0 && lane == 0) __hip_atomic_fetch_add((gu32*)(ws + G_WS_CTL + G_C_ARR), 1u, RLX_AGENT);
}
__device__ __forceinline__ void fill_block(char* ws, int b, int tid, const u32x4& v0, const u32x4& v1) {
    const unsigned o = b * 8192 + tid * 16, o2 = o + 4096;
    asm volatile("global_store_dwordx4 %0, %1, %2 sc1\n\ts_nop 1" :: "v"(o), "v"(v0), "s"(ws) : "memory");
    if (b >= 256 || (b & 12) != 12 || tid < 64)
        asm volatile("global_store_dwordx4 %0, %1, %2 sc1\n\ts_nop 1" :: "v"(o2), "v"(v1), "s"(ws) : "memory");
}
__device__ __forceinline__ void init_fill(char* ws, const u16* mem0, int f, int tid) {
    const u32x4 sent = {0xffffffffu, 0xffffffffu, 0xffffffffu, 0xffffffffu};
    u32x4 v0 = sent, v1 = sent;
    if (f < 32 && (f & 8)) {
#pragma unroll
        for (int k = 0; k < 2; ++k) {
            const int q = tid + 256 * k + ((f & 4) ? 256 : 0), m = q & 15;
            if (!(f & 4) || (q >= 512 && q < 576)) {
                const unsigned* src = (const unsigned*)(mem0 + (2 * (m >> 2) + (m & 1)) * DM + 16 * (q >> 4) + (m & 2));
                const u32x4 o = {min(src[0], 0xfffffffeu), min(src[2], 0xfffffffeu), min(src[4], 0xfffffffeu), min(src[6], 0xfffffffeu)};
                if (k) v1 = o; else v0 = o;
            }
        }
    }
#pragma unroll 1
    for (int q = f; q < 185; q += 48) {
        if (q >= 136 && q < 152 && (tid & 127)) { v0 = u32x4{0u, 0u, 0u, 0u}; v1 = v0; }
        fill_block(ws, q < 64 ? q : q < 136 ? q - 64 + G_D_UM / 8192 : q < 168 ? 256 + ((q - 136) >> 3) * 16 + (q & 7) : q < 178 ? q - 168 + G_WS_CTL / 8192 : q - 178 + G_WS_FIX / 8192, tid, v0, v1);
        v0 = sent; v1 = sent;
    }
    const unsigned e = __hip_atomic_load((const gu32*)(ws + G_WS_EP), RLX_AGENT);
    asm volatile("s_waitcnt vmcnt(0)" :: "v"(e) : "memory");
    __syncthreads();
    if (tid == 0) __hip_atomic_store((gu64*)(ws + G_WS_DONE + (48 + f) * 64), (u64)(e + 1u), RLX_AGENT);
    if (tid == 0) IST(0, 48 + f);
}
__device__ __forceinline__ void init_cal(char* ws, int id, int odd, int tid, int n) {
    if (tid < 64) {
        const int pg = id >> 1;
        char* a = ws + (pg < 2 ? pg * 0x200000u : G_D_UM + (pg - 2) * 0x200000u) + G_PROBE + (id & 1) * 8192;
        const u32x4 z = {0u, 0u, 0u, 0u};
        asm volatile("global_store_dwordx4 %0, %2, off sc1\n\tglobal_store_dwordx4 %1, %2, off sc1\n\ts_waitcnt vmcnt(0)" :: "v"(a + tid * 16), "v"(a + 16384 + tid * 16), "v"(z) : "memory");
        if (tid == 0) {
            unsigned b0 = 0xffffu, b1 = 0xffffu;
            const unsigned ts1 = (unsigned)__builtin_amdgcn_s_memrealtime() << 4;
#pragma unroll 1
            for (int it = 0; it < n; ++it) {
                const unsigned c0 = (unsigned)__builtin_amdgcn_s_memtime();
                const unsigned v = __hip_atomic_load((const gu32*)(a + (it & 1) * 16384 + (it >> 1) * 128), RLX_AGENT);
                asm volatile("s_waitcnt vmcnt(0)" :: "v"(v) : "memory");
                const unsigned t = (unsigned)__builtin_amdgcn_s_memtime() - c0;
                if (it & 1) b1 = min(b1, t); else b0 = min(b0, t);
            }
            init_done(ws, id, 2 * min(b0, b1) + ((b1 < b0 ? 1 : 0) ^ odd));
            IST(0, id);
        }
    }
    __syncthreads();
}
__device__ __forceinline__ void near_mask(char* ws, int lane, int& nm, int& nc) {
    const gu32* q = (const gu32*)(ws + G_WS_DONE + (lane & 31) * 128 + 4);
    const unsigned v0 = __hip_atomic_load(q, RLX_AGENT), v1 = __hip_atomic_load(q + 16, RLX_AGENT);
    nm = (int)__ballot(v1 < v0);
    nc = (int)__ballot(min(v0, v1) & 1u);
}

#define TST(slot_, i_)
#define PIN8(a) asm volatile("" : "+v"(a[0]), "+v"(a[1]), "+v"(a[2]), "+v"(a[3]), "+v"(a[4]), "+v"(a[5]), "+v"(a[6]), "+v"(a[7]))
struct AOps { u32x4 q[4], k[8]; int fl; };
struct APtr { const char* q; const char* k; const char* m; unsigned voff, vkm, voffm, vfl; };
__device__ __forceinline__ void attn_batch(const MainP& p, AOps& o, const APtr& a) {
    const char* q2 = a.q + 4096; const char* k2 = a.k + 4096;
    WLD16(o.q[0], a.voff, a.q, 0); WLD16(o.q[1], a.voff, a.q, 2048); WLD16(o.q[2], a.voff, q2, 0); WLD16(o.q[3], a.voff, q2, 2048);
    WLD16X4(o.k[0], o.k[1], o.k[2], o.k[3], a.voff, a.k); WLD16X4(o.k[4], o.k[5], o.k[6], o.k[7], a.voff, k2);
    asm volatile("global_load_dword %0, %1, %2 sc1" : "=v"(o.fl) : "v"(a.vfl), "s"(WC(p)) : "memory");
}
__device__ __forceinline__ void attn_pin(AOps& o) {
    asm volatile("" : "+v"(o.fl), "+v"(o.q[0]), "+v"(o.q[1]), "+v"(o.q[2]), "+v"(o.q[3])); PIN8(o.k);
}
static constexpr int NFINE = 6;
static constexpr int NG1 = 64 - NFINE;
__device__ __forceinline__ void score_round4(float* r, const f32x4& x) {
    asm volatile("v_cvt_pk_bf16_f32 %0, 0, %4\n\tv_cvt_pk_bf16_f32 %1, 0, %5\n\tv_cvt_pk_bf16_f32 %2, 0, %6\n\tv_cvt_pk_bf16_f32 %3, 0, %7\n\t"
                 "v_mul_f32 %0, 0x3db504f3, %0\n\tv_mul_f32 %1, 0x3db504f3, %1\n\tv_mul_f32 %2, 0x3db504f3, %2\n\tv_mul_f32 %3, 0x3db504f3, %3\n\t"
                 "v_cvt_pk_bf16_f32 %0, 0, %0\n\tv_cvt_pk_bf16_f32 %1, 0, %1\n\tv_cvt_pk_bf16_f32 %2, 0, %2\n\tv_cvt_pk_bf16_f32 %3, 0, %3"
                 : "=&v"(r[0]), "=&v"(r[1]), "=&v"(r[2]), "=&v"(r[3]) : "v"(x[0]), "v"(x[1]), "v"(x[2]), "v"(x[3]));
}
__device__ __forceinline__ float vmax3(float a, float b, float c) { float r; asm("v_max3_f32 %0, %1, %2, %3" : "=v"(r) : "v"(a), "v"(b), "v"(c)); return r; }
template <int N>
__device__ __forceinline__ void attn_soft(float (&q)[12], u32x2& Pm, u32x4& Px) {
    float m = vmax3(q[0], q[1], q[2]);
#pragma unroll
    for (int i = 3; i < N; i += 2) m = vmax3(m, q[i], q[i + 1 < N ? i + 1 : i]);
    { auto a = __builtin_amdgcn_permlane16_swap(__float_as_uint(m), __float_as_uint(m), false, false); m = vmax3(m, __uint_as_float(a[0]), __uint_as_float(a[1])); }
    { auto a = __builtin_amdgcn_permlane32_swap(__float_as_uint(m), __float_as_uint(m), false, false); m = vmax3(m, __uint_as_float(a[0]), __uint_as_float(a[1])); }
    const float ml = -m * 1.4426950408889634f;
#pragma unroll
    for (int i = 0; i < N; ++i) q[i] = __builtin_amdgcn_exp2f(fmaf(q[i], 1.4426950408889634f, ml));
    float sum = 0.5f * ((q[0] + q[1]) + (q[2] + q[3])) + ((q[4] + q[5]) + (q[6] + q[7]));
    if (N > 8) sum += (q[8] + q[9]) + (q[10] + q[11]);
    sum = rowsum4(sum);
    const float inv = __builtin_amdgcn_rcpf(sum);
    Pm = u32x2{pk2bf(q[0] * inv, q[1] * inv), pk2bf(q[2] * inv, q[3] * inv)};
    Px = u32x4{pk2bf(q[4] * inv, q[5] * inv), pk2bf(q[6] * inv, q[7] * inv), N > 8 ? pk2bf(q[8] * inv, q[9] * inv) : 0u, N > 8 ? pk2bf(q[10] * inv, q[11] * inv) : 0u};
}
#define NASPLIT 12
#define KTOUCH 0
#define HSL 1
#define NAG 2
static constexpr int NA = NASPLIT;
__device__ __forceinline__ int attn_next(int s) { return s < NA ? s + 2 : s + NAG; }
__device__ __forceinline__ unsigned attn_flag(int s, int hq) { return G_WS_CTL + G_C_PC + (s < NA ? s & 1 : (s - NA) % NAG) * 1024 + hq * 128; }
struct BOps { u32x4 q[8], k[8]; };
#define WLD16X4A(d0, d1, d2, d3, voff_, base_) asm volatile("s_nop 4\n\tglobal_load_dwordx4 %0, %4, %5 sc1\n\tglobal_load_dwordx4 %1, %4, %5 offset:1024 sc1\n\t" \
    "global_load_dwordx4 %2, %4, %5 offset:2048 sc1\n\tglobal_load_dwordx4 %3, %4, %5 offset:3072 sc1" \
    : "=&a"(d0), "=&a"(d1), "=&a"(d2), "=&a"(d3) : "v"(voff_), "s"(base_) : "memory")
#define PIN8A(a) asm volatile("" : "+a"(a[0]), "+a"(a[1]), "+a"(a[2]), "+a"(a[3]), "+a"(a[4]), "+a"(a[5]), "+a"(a[6]), "+a"(a[7]))
#define MFMA_AV0(acc, a_, b_) asm volatile("v_mfma_f32_16x16x32_bf16 %0, %1, %2, 0" : "=&v"(acc) : "a"(a_), "v"(b_))
#define MFMA_AV(acc, a_, b_) asm volatile("v_mfma_f32_16x16x32_bf16 %0, %1, %2, %0" : "+v"(acc) : "a"(a_), "v"(b_))
__device__ __forceinline__ void attn_batch8(const MainP& p, BOps& o, const APtr& a) {
    const char* q2 = a.q + 4096; const char* k2 = a.k + 4096;
    WLD16X4(o.q[0], o.q[1], o.q[2], o.q[3], a.voff, a.q); WLD16X4(o.q[4], o.q[5], o.q[6], o.q[7], a.voff, q2);
    WLD16X4A(o.k[0], o.k[1], o.k[2], o.k[3], a.voff, a.k); WLD16X4A(o.k[4], o.k[5], o.k[6], o.k[7], a.voff, k2);
}
__device__ __forceinline__ void attn_step(const MainP& p, BOps& o, BOps& n1, BOps& n2, APtr& a, int& kg, int& kk, int s, int h, int w, int lane) {
    const int g = lane >> 4, c = lane & 15;
    const float scale = 0.08838834764831845f;
    TST(s * 64 + h * 2, 0);
    const int s2 = min(attn_next(attn_next(s)), NSEG - 1);
    if (kg < s2 || kk < s) {
        VMC0();
        if (s < 50) wait_word(p, WC(p) + a.vfl, (lane & 1) ? s : s2, 13u); else wait_word<true>(p, WC(p) + a.vfl, (lane & 1) ? s : s2, 13u);
    }
    TST(s * 64 + h * 2, 1);
    a.q = p.ws + G_D_QX + (size_t)h * G_HS8 + skew(s2, h) * 8192; a.k = a.q + (G_D_KX - G_D_QX); a.m = p.ws + G_D_KM + (size_t)h * G_HS4 + skew(s, h) * 4096;
    __builtin_amdgcn_sched_barrier(0);
    u32x4 km[4];
    int fl;
    WLD16X4A(km[0], km[1], km[2], km[3], a.vkm, a.m);
    asm volatile("global_load_dword %0, %1, %2 sc1" : "=v"(fl) : "v"(a.vfl), "s"(WC(p)) : "memory");
    attn_batch8(p, n2, a);
    f32x4 y[5];
#pragma unroll
    for (int wc = 0; wc < 4; ++wc)
#pragma unroll
        for (int i = 0; i < 3; ++i) { if (wc == 0) MFMA_AV0(y[i], o.k[i >> 1], o.q[(i + 1) >> 1]); else MFMA_AV(y[i], o.k[wc * 2 + (i >> 1)], o.q[wc * 2 + ((i + 1) >> 1)]); }
    asm volatile("s_nop 7" : "+v"(y[0]), "+v"(y[1]), "+v"(y[2]));
    float q0[12], q1[12];
    score_round4(q0 + 4, y[0]); score_round4(q1 + 4, y[1]); score_round4(q1 + 8, y[2]);
#pragma unroll
    for (int i = 0; i < 4; ++i) {
        const bool vis = 4 * g + i <= c;
        q0[4 + i] = vis ? q0[4 + i] : -INFINITY; q1[8 + i] = vis ? q1[8 + i] : -INFINITY;
    }
    __builtin_amdgcn_sched_barrier(0);
    asm volatile("s_waitcnt vmcnt(16)\n\ts_barrier" : "+a"(km[0]), "+a"(km[1]), "+a"(km[2]), "+a"(km[3]), "+v"(fl) :: "memory");
    PIN8(n1.q); PIN8A(n1.k);
    { const unsigned fo = attn_flag(s, h >> 2); const int fv = s - 1;
      asm volatile("s_cmp_lg_u32 %3, 0\n\ts_cbranch_scc1 1f\n\tglobal_store_dword %0, %1, %2 sc1\n1:" :: "v"(fo), "v"(fv), "s"(WC(p)), "s"(w) : "scc", "memory"); }
#pragma unroll
    for (int wc = 0; wc < 4; ++wc)
#pragma unroll
        for (int i = 0; i < 2; ++i) { if (wc == 0) MFMA_AV0(y[3 + i], km[0], o.q[i]); else MFMA_AV(y[3 + i], km[wc], o.q[wc * 2 + i]); }
    asm volatile("s_nop 7" : "+v"(y[3]), "+v"(y[4]));
    kg = __builtin_amdgcn_readlane(fl, 0); kk = __builtin_amdgcn_readlane(fl, 1);
    score_round4(q0, y[3]); score_round4(q1, y[4]);
    u32x2 Pm0, Pm1; u32x4 Px0, Px1;
    attn_soft<8>(q0, Pm0, Px0); attn_soft<12>(q1, Pm1, Px1);
    char* pxb = p.ws + G_D_PX + (size_t)(s * 32 + h) * 2048;
    char* pmb = p.ws + G_D_PM + (size_t)s * 16384 + h * 256; char* pm2 = pmb + 8192;
    WST16(a.voff, Px0, pxb, 0); WST16(a.voff, Px1, pxb, 1024);
    asm volatile("global_store_dwordx2 %0, %1, %3 sc1\n\tglobal_store_dwordx2 %0, %2, %4 sc1" :: "v"(a.voffm), "v"(Pm0), "v"(Pm1), "s"(pmb), "s"(pm2) : "memory");
    { const unsigned fo = attn_flag(s, h >> 2);
      asm volatile("s_cmp_lt_u32 %4, 46\n\ts_cbranch_scc1 1f\n\ts_waitcnt vmcnt(0)\n\ts_barrier\n\ts_cmp_lg_u32 %3, 0\n\ts_cbranch_scc1 1f\n\tglobal_store_dword %0, %1, %2 sc1\n1:"
                   :: "v"(fo), "v"(s), "s"(WC(p)), "s"(w), "s"(s) : "scc", "memory"); }
    TST(s * 64 + h * 2, 2);
}
__device__ __forceinline__ void attn_role(const MainP& p, int s, int h, int w, int lane) {
    APtr a;
    a.q = p.ws + G_D_QX + (size_t)h * G_HS8 + skew(s, h) * 8192; a.k = a.q + (G_D_KX - G_D_QX);
    a.voff = lane * 16; a.vkm = (lane & 55) * 16; a.voffm = (lane & 15) * 16 + ((lane >> 4) & 1) * 8;
    a.vfl = G_WS_CTL + ((lane & 1) ? G_C_KM : G_C_GQ) + h * 128;
    BOps o0, o1, o2;
    wait_word(p, WC(p) + G_WS_CTL + G_C_GQ + h * 128, attn_next(s), 10u);
    attn_batch8(p, o0, a);
    a.q = p.ws + G_D_QX + (size_t)h * G_HS8 + skew(attn_next(s), h) * 8192; a.k = a.q + (G_D_KX - G_D_QX);
    attn_batch8(p, o1, a);
    VMC0();
    PIN8(o0.q); PIN8A(o0.k); PIN8(o1.q); PIN8A(o1.k);
    int kg = -1, kk = -1;
    static_assert(NA % 2 == 0, "ranges");
#pragma unroll 1
    for (;;) {
        attn_step(p, o0, o1, o2, a, kg, kk, s, h, w, lane);
        if ((s = attn_next(s)) >= NG1) break;
        attn_step(p, o1, o2, o0, a, kg, kk, s, h, w, lane);
        if ((s = attn_next(s)) >= NG1) break;
        attn_step(p, o2, o0, o1, a, kg, kk, s, h, w, lane);
        if ((s = attn_next(s)) >= NG1) break;
    }
    VMC0();
}
__device__ __forceinline__ unsigned umax4(const u32x4& v) { return max(max(v[0], v[1]), max(v[2], v[3])); }
__device__ __forceinline__ bool give_up(const MainP& p, unsigned& tries, unsigned code) {
    if (++tries <= kWaitCap / 4) return false;
    __hip_atomic_store((gu32*)(p.ws + G_WS_CTL + G_C_ERR), code, RLX_AGENT);
    return true;
}
__device__ __forceinline__ void task_attn_tail(const MainP& p, int s, int h, int w, int lane) {
    const int g = lane >> 4, c = lane & 15, qt = w & 1;
    const float scale = 0.08838834764831845f;
    APtr a;
    a.q = p.ws + G_D_QX + (size_t)h * G_HS8 + skew(s, h) * 8192 + qt * 1024; a.k = p.ws + G_D_KX + (size_t)h * G_HS8 + skew(s, h) * 8192; a.m = p.ws + G_D_KM + (size_t)h * G_HS4 + skew(s, h) * 4096;
    a.voff = lane * 16; a.vkm = (lane & 55) * 16; a.voffm = c * 16 + (g & 1) * 8; a.vfl = G_WS_CTL;
    AOps o;
    wait_word(p, WC(p) + G_WS_CTL + G_C_GQ + h * 128, s, 14u);
    attn_batch(p, o, a);
    VMC0();
    attn_pin(o);
    float q[12];
    u32x4 km[4];
#pragma unroll
    for (int kt = 1; kt >= -1; --kt) {
        if (kt < 0) {
            TST(s * 64 + h * 2 + qt, 0);
            wait_word(p, WC(p) + G_WS_CTL + G_C_KM + h * 128, s - 2, 15u);
            if (FLG(p.prm[5]) & 2) wait_word<true>(p, WC(p) + G_WS_CTL + G_C_KM + h * 128, s - 1, 15u);
            for (int i = 0; i < ((FLH(p.prm[7]) >> 14) & 15); ++i) __builtin_amdgcn_s_sleep(1);
            for (unsigned tries = 0;;) {
                WLD16X4(km[0], km[1], km[2], km[3], a.vkm, a.m);
                asm volatile("s_waitcnt vmcnt(0)" : "+v"(km[0]), "+v"(km[1]), "+v"(km[2]), "+v"(km[3]) :: "memory");
                if (!__any(max(max(umax4(km[0]), umax4(km[1])), max(umax4(km[2]), umax4(km[3]))) == 0xffffffffu) || give_up(p, tries, 16u)) break;
            }
            TST(s * 64 + h * 2 + qt, 1);
        }
        f32x4 y = {0.f, 0.f, 0.f, 0.f};
#pragma unroll
        for (int wc = 0; wc < 4; ++wc) y = MFMA32(kt < 0 ? km[wc] : o.k[wc * 2 + (kt < 0 ? 0 : kt)], o.q[wc], y);
#pragma unroll
        for (int i = 0; i < 4; ++i) q[4 + 4 * kt + i] = (16 * kt + 4 * g + i <= 16 * qt + c) ? bfr(bfr(y[i]) * scale) : -INFINITY;
    }
    u32x2 Pm; u32x4 Px;
    attn_soft<12>(q, Pm, Px);
    char* pxb = p.ws + G_D_PX + (size_t)(s * 32 + h) * 2048 + qt * 1024;
    char* pmb = p.ws + G_D_PM + (size_t)s * 16384 + h * 256 + qt * 8192;
    WST16(a.voff, Px, pxb, 0);
    asm volatile("global_store_dwordx2 %0, %1, %2 sc1" :: "v"(a.voffm), "v"(Pm), "s"(pmb) : "memory");
    TST(s * 64 + h * 2 + qt, 2);
}

#define RLD(d_, voff_, base_, off_) asm volatile("global_load_dwordx4 %0, %1, %2 offset:%3 sc1" : "=v"(d_) : "v"(voff_), "s"(base_), "i"(off_) : "memory")
#define RLDA(d_, voff_, base_, off_) asm volatile("global_load_dwordx4 %0, %1, %2 offset:%3 sc1" : "=a"(d_) : "v"(voff_), "s"(base_), "i"(off_) : "memory")
#define MFMA_ACC_A(acc, a_, b_) asm volatile("v_mfma_f32_16x16x32_bf16 %0, %1, %2, %0" : "+v"(acc) : "a"(a_), "v"(b_))
#define VMW(n_) asm volatile("s_waitcnt vmcnt(%0)" :: "i"(n_) : "memory")
template <int NT, int DEPTH, bool TAIL, int NM = 2>
__device__ __forceinline__ void task_out(const MainP& p, char* smem, int s, int ct0, int w, int lane, int m0 = 0) {
    const int g = lane >> 4, c = lane & 15;
    const unsigned voff = lane * 16;
    f32x4 acc[NT][2];
#pragma unroll
    for (int t = 0; t < NT; ++t) { acc[t][0] = f32x4{0.f, 0.f, 0.f, 0.f}; acc[t][1] = acc[t][0]; asm volatile("" : "+v"(acc[t][0]), "+v"(acc[t][1])); }
    const char* ub[NT];
#pragma unroll
    for (int t = 0; t < NT; ++t) ub[t] = uni(p.ws + G_D_UX + (size_t)(ct0 + t) * G_UXS + skew(s, ct0 + t) * 32768 + w * 8192);
    const char* pb = uni(p.ws + G_D_PX + (size_t)(s * 32 + w * 8) * 2048 + m0 * 1024);
    const char* um = uni(p.ws + um_base(s, p.prm[0]));
    const int cbit = (p.prm[1] >> (2 + s / 3)) & 1;
    unsigned vu[NT];
#pragma unroll
    for (int t = 0; t < NT; ++t) { const int j = ct0 + t, xd = j / 9; const int pl = (j - 9 * ((xd + 1) >> 1)) * 32768; vu[t] = pl + ((xd ^ cbit ^ ((pl + (s % 3) * G_UMS) >> 19)) & 1) * 16384 + w * 2048 + voff; asm volatile("" : "+v"(vu[t])); }
    const char* pm = uni(p.ws + G_D_PM + (size_t)s * 16384 + w * 2048 + m0 * 8192);
    constexpr int NR = DEPTH + 1 > 10 ? 10 : DEPTH + 1;
    u32x4 a[NR][NT], b[10][2];
    unsigned vb = voff, va = voff;
    TST(4096 + (s * 36 + ct0) * 4 + w, 0);
    const int f0 = (int)__hip_atomic_load((const gu32*)(WC(p) + G_WS_CTL + (lane < 8 ? G_C_UM + (w * 8 + lane) * 128 : lane < 16 ? G_C_GV + ((s < (int)G_S1 ? s & 1 : 0) * 32 + w * 8 + lane - 8) * 128
                                                                                                                                : attn_flag(s, w * 2 + (lane & 1)) - G_WS_CTL)), RLX_AGENT);
    const bool early = !TAIL && !__any(f0 < s);
    if (!early) wait_word(p, WC(p) + G_WS_CTL + (lane < 8 ? G_C_GV + ((s < (int)G_S1 ? s & 1 : 0) * 32 + w * 8 + lane) * 128 : G_C_UM + w * 8 * 128), lane < 8 ? s : s - 3, 21u);
#define ALOAD(q) do { if ((q) < 8) { _Pragma("unroll") for (int t = 0; t < NT; ++t) RLDA(a[(q) % NR][t], va, ub[t], 0);  \
                                     asm volatile("v_add_u32 %0, 0x400, %0" : "+v"(va)); }                                            \
                      else { _Pragma("unroll") for (int t = 0; t < NT; ++t) RLDA(a[(q) % NR][t], vu[t], um, ((q) & 1) * 1024); } } while (0)
    constexpr int D0 = DEPTH < 8 ? DEPTH : 8;
    asm volatile("s_nop 4");
#pragma unroll
    for (int q = 0; q < D0; ++q) ALOAD(q);
    asm volatile("s_cmp_lg_u32 %0, 0\n\ts_cbranch_scc1 1f\n\ts_waitcnt vmcnt(0)\n1:\n\ts_waitcnt vmcnt(30)" :: "s"(__builtin_amdgcn_readfirstlane((int)early)) : "scc", "memory");
    unsigned tries = 0;
    if (TAIL) {
        const char* sp = pm + (lane & 7) * 256 + ((lane >> 3) & 1) * 8192;
        const int dl = (FLG(p.prm[5]) >> 26) & 31;
        if (dl) {
            wait_word<true>(p, WC(p) + G_WS_CTL + G_C_KM + ((ct0 >> 1) + w) * 128, s - 1, 25u);
            for (int i = 0; i < dl + (s < 63 ? 5 : 0); ++i) __builtin_amdgcn_s_sleep(2);
        } else
        while (__any(__hip_atomic_load((const gu32*)sp, RLX_AGENT) == 0xffffffffu) && !give_up(p, tries, 23u)) __builtin_amdgcn_s_sleep(1);
    } else if (!early) wait_word(p, WC(p) + G_WS_CTL + (lane < 8 ? G_C_UM + (w * 8 + lane) * 128 : attn_flag(s, w * 2 + (lane & 1)) - G_WS_CTL), s, 22u);
    if (!early) TST(4096 + (s * 36 + ct0) * 4 + w, 1);
    for (;;) {
    asm volatile("s_nop 4");
#pragma unroll
    for (int q = 0; q < 8; ++q) { RLD(b[q][0], vb, pb, 0); if (NM == 2) RLD(b[q][1], vb, pb, 1024); asm volatile("v_add_u32 %0, 0x800, %0" : "+v"(vb)); }
#pragma unroll
    for (int q = 8; q < 10; ++q) { RLD(b[q][0], voff, pm, (q & 1) * 1024); if (NM == 2) RLD(b[q][1], voff, pm + 8192, (q & 1) * 1024); }
#pragma unroll
    for (int q = D0; q < NR; ++q) ALOAD(q);
#pragma unroll
    for (int q = 0; q < 10; ++q) {
        const int issuedA = (NR + q < 10 ? NR + q : 10) - D0;
        if (q < D0) VMW(NM * (9 - q) + issuedA * NT); else VMW((issuedA - (q - D0 + 1)) * NT);
#pragma unroll
        for (int t = 0; t < NT; ++t) { MFMA_ACC_A(acc[t][0], a[q % NR][t], b[q][0]); if (NM == 2) MFMA_ACC_A(acc[t][1], a[q % NR][t], b[q][1]); }
        if (NR + q < 10) ALOAD(NR + q);
    }
    asm volatile("s_nop 7" ::: "memory");
#pragma unroll
    for (int t = 0; t < NT; ++t) asm volatile("" : "+v"(acc[t][0]), "+v"(acc[t][1]));
    if (!TAIL) break;
    f32x4 chk = acc[0][0];
#pragma unroll
    for (int t = 0; t < NT; ++t) chk += acc[t][1] + acc[t][0];
    const float ck = (chk[0] + chk[1]) + (chk[2] + chk[3]);
    if (!__any(ck != ck)) break;
    unsigned mx = 0;
#pragma unroll
    for (int q = 0; q < 10; ++q) { asm volatile("" : "+v"(b[q][0])); mx = max(mx, umax4(b[q][0])); if (NM == 2) { asm volatile("" : "+v"(b[q][1])); mx = max(mx, umax4(b[q][1])); } }
#pragma unroll
    for (int t = 0; t < NT; ++t) { asm volatile("" : "+a"(a[8 % NR][t]), "+a"(a[9 % NR][t])); mx = max(mx, max(umax4(a[8 % NR][t]), umax4(a[9 % NR][t]))); }
    if (!__any(mx == 0xffffffffu) || give_up(p, tries, 24u)) break;
    vb = voff;
#pragma unroll
    for (int t = 0; t < NT; ++t) { acc[t][0] = f32x4{0.f, 0.f, 0.f, 0.f}; acc[t][1] = acc[t][0]; asm volatile("" : "+v"(acc[t][0]), "+v"(acc[t][1])); }
    }
    TST(4096 + (s * 36 + ct0) * 4 + w, 2);
    f32x4* red = (f32x4*)smem;
#pragma unroll
    for (int t = 0; t < NT; ++t) { red[((w * NT + t) * 2 + 0) * 64 + lane] = acc[t][0]; if (NM == 2) red[((w * NT + t) * 2 + 1) * 64 + lane] = acc[t][1]; }
    __syncthreads();
    const int row = lane >> 2, pc = lane & 3;
#pragma unroll
    for (int it = 0; it < NT / 2 * NM; ++it)
        if ((it & 3) == w) {
            const int m = it % NM, t = 2 * (it / NM) + (pc >> 1);
            const f32x4* r0 = red + (t * 2 + m) * 64 + (pc & 1) * 32 + row;
            const f32x4 v0 = (r0[0] + r0[NT * 128]) + (r0[NT * 256] + r0[NT * 384]), v1 = (r0[16] + r0[NT * 128 + 16]) + (r0[NT * 256 + 16] + r0[NT * 384 + 16]);
            u16* o = p.out + (size_t)(32 * s + 16 * (m + m0) + row) * OUTW + 16 * ct0 + 32 * (it / NM) + 8 * pc;
            if (TAIL || s >= OWT) OST16W(o, pack8(v0, v1)); else OST16(o, pack8(v0, v1));
        }
}

__device__ __forceinline__ int index_of_pos(int pv) { return min(min(pv, 128 + (pv >> 1)), min(256 + (pv >> 2), 384 + (pv >> 3))); }
static constexpr int KROW = G_KROW;
static constexpr int KARR = 80 * KROW;
static constexpr int UCS = 2208;
struct BCh {
    u16 uc[4][UCS];
    f32x4 cst[NST];
    unsigned Kd[648];
    u16 Kh[KARR];
    u16 Kl[KARR];
};
static_assert(4 * sizeof(BCh) <= LDS_TOTAL - 32, "LDS");
__device__ __forceinline__ unsigned prm(unsigned hi, unsigned lo, unsigned sel) { return __builtin_amdgcn_perm(hi, lo, sel); }
#define BST(i) asm volatile("" ::: "memory")
#define UT_LOAD(p_) __builtin_nontemporal_load(p_)
template <class F>
__device__ __forceinline__ void task_ut(const MainP& p, char* smem, int rb, int w, int lane, F more) {
    const int tid = w * 64 + lane, g = lane >> 4, c = lane & 15;
    { char* ws = p.ws; if (tid == 0) IST(4, rb); }
    const u32x4* src = (const u32x4*)(p.U + (size_t)rb * 64 * DM);
    u32x4 v[18];
#pragma unroll
    for (int i = 0; i < 18; ++i) v[i] = UT_LOAD(src + tid + 256 * i);
#pragma unroll
    for (int i = 0; i < 18; ++i) { const int ch = tid + 256 * i, row = ch / 72; *(u32x4*)(smem + row * 1184 + (ch - 72 * row) * 16) = v[i]; }
    __syncthreads();
    { char* ws = p.ws; if (tid == 0) IST(3, rb); }
    const unsigned ad = (unsigned)(uintptr_t)smem + (16 * g + (c >> 2)) * 1184 + (w * 144 + 4 * (c & 3)) * 2;
    char* tw = smem + 77824 + w * 20736;
#pragma unroll
    for (int i = 0; i < 9; ++i) {
        u32x2 x0, x1, x2, x3;
        asm volatile("ds_read_b64_tr_b16 %0, %4 offset:%5\n\tds_read_b64_tr_b16 %1, %4 offset:%6\n\tds_read_b64_tr_b16 %2, %4 offset:%7\n\tds_read_b64_tr_b16 %3, %4 offset:%8\n\ts_waitcnt lgkmcnt(0)"
                     : "=&v"(x0), "=&v"(x1), "=&v"(x2), "=&v"(x3) : "v"(ad), "i"(i * 32), "i"(4 * 1184 + i * 32), "i"(8 * 1184 + i * 32), "i"(12 * 1184 + i * 32) : "memory");
        u32x4* o = (u32x4*)(tw + (16 * i + c) * 144 + g * 32);
        o[0] = u32x4{x0[0], x0[1], x1[0], x1[1]}; o[1] = u32x4{x2[0], x2[1], x3[0], x3[1]};
    }
    asm volatile("" ::: "memory");
    const unsigned vo = (w * 144 + (lane >> 3)) * (SL * 2) + rb * 128 + (lane & 7) * 16;
    const char* dst = p.ws + G_D_UT;
#pragma unroll
    for (int i = 0; i < 18; ++i) {
        const u32x4 a = *(const u32x4*)(tw + (8 * i + (lane >> 3)) * 144 + (lane & 7) * 16);
        const unsigned vi = vo + i * 8 * (SL * 2);
        WST16(vi, a, dst, 0);
    }
    VMC0();
    init_wait(p.ws, w, lane);
    if (tid == 0) __hip_atomic_store((gu32*)(p.ws + G_WS_CTL + G_C_UT + rb * 128), 1u, RLX_AGENT);
    { char* ws = p.ws; if (tid == 0) IST(2, rb); }
    more();
    VMC0();
    __syncthreads();
    if (tid == 0) __hip_atomic_store((gu32*)(p.ws + G_WS_CTL + G_C_UT + rb * 128), 2u, RLX_AGENT);
}
struct KPar { u16 ldt, dsk, pa[2][4]; };
__device__ __forceinline__ KPar kgen_par(const MainP& p, int d, int lane) {
    KPar k;
    k.ldt = p.logdt[d]; k.dsk = p.Dskip[d];
#pragma unroll
    for (int q = 0; q < 2; ++q) { const int i = d * NST + lane + 64 * q; k.pa[q][0] = p.logA[i]; k.pa[q][1] = p.Aim[i]; k.pa[q][2] = p.Cre[i]; k.pa[q][3] = p.Cim[i]; }
    return k;
}
__device__ __forceinline__ void task_s4d(const MainP& p, BCh& L, int d, int lane, bool first, int& nb, KPar& kp, int& pend) {
    const int g = lane >> 4, c = lane & 15;
    BST(0);
    const float dsk = bf2f(kp.dsk);
    const auto& pa = kp.pa;
    const float dt = bfr(expf(bf2f(kp.ldt)));
#pragma unroll
    for (int q = 0; q < 2; ++q) {
        const float ar = -bfr(expf(bf2f(pa[q][0])));
        const float cr = bf2f(pa[q][2]) * dt, ci = bf2f(pa[q][3]) * dt;
        L.cst[lane + 64 * q] = f32x4{ar * dt * 1.4426950408889634f, bf2f(pa[q][1]) * dt * 0.15915494309189535f,
                                     __log2f(hypotf(cr, ci)), atan2f(ci, cr) * 0.15915494309189535f};
    }
    const char* ut = uni(p.ws + G_D_UT + (size_t)d * 4096);
    const unsigned voff = lane * 16;
    u32x4 uu[4];
    unsigned tk = nb >= 0 ? 1u : 0u;
    __builtin_amdgcn_sched_barrier(0);
    asm volatile("s_mov_b64 exec, 1\n\tglobal_atomic_add %0, %1, %0, %2 sc0\n\ts_mov_b64 exec, -1" : "+v"(tk) : "v"(G_WS_CTL + G_C_TASK + 128), "s"(WC(p)) : "memory");
    const char* ue = uni(first ? (const char*)p.logA : ut);
    WLD16X4A(uu[0], uu[1], uu[2], uu[3], voff, ue);
    BST(1);
    {
        f32x4 acc[3];
#pragma unroll
        for (int v = 0; v < 3; ++v) acc[v] = f32x4{0.f, 0.f, 0.f, 0.f};
        auto base_of = [](int v, int r) { return v == 0 ? (r & 7) + 128 * (r >> 3) : v == 1 ? (r < 8 ? 256 + 2 * (r & 3) + 128 * (r >> 2) : 512 + 4 * (r & 1) + 128 * ((r - 8) >> 1))
                                                                                           : 1024 + 128 * min(r, 8); };
        const float b0 = (float)base_of(0, c), b1 = (float)base_of(1, c), b2 = (float)base_of(2, c), off = (float)(8 * c);
#define KG_POLAR(h_, l_, lg_, f_, a_, b_) do { const float m_ = __builtin_amdgcn_exp2f(fmaf(cc[0], b_, lg_)), p_ = __builtin_amdgcn_fractf(fmaf(a_, b_, f_)); \
            const float re_ = m_ * __builtin_amdgcn_cosf(p_), im_ = m_ * __builtin_amdgcn_sinf(p_);                                                            \
            h_ = pk2bf(re_, im_); l_ = pk2bf(re_ - lo2f(h_), im_ - hi2f(h_)); } while (0)
#pragma unroll 1
        for (int t = 0; t < NST / 16; ++t) {
            u32x4 fh, fl, ah[3], al[3];
#pragma unroll
            for (int j = 0; j < 4; ++j) {
                const f32x4 cc = L.cst[16 * t + 4 * g + j];
                KG_POLAR(fh[j], fl[j], 0.f, 0.f, -cc[1], off);
                KG_POLAR(ah[0][j], al[0][j], cc[2], cc[3], cc[1], b0); KG_POLAR(ah[1][j], al[1][j], cc[2], cc[3], cc[1], b1); KG_POLAR(ah[2][j], al[2][j], cc[2], cc[3], cc[1], b2);
            }
#pragma unroll
            for (int v = 0; v < 3; ++v) { acc[v] = MFMA32(al[v], fh, acc[v]); acc[v] = MFMA32(ah[v], fl, acc[v]); acc[v] = MFMA32(ah[v], fh, acc[v]); }
        }
        BST(2);
        auto split = [](float k) { const unsigned hi = pk2bf(k, 0.f) & 0xffffu; return hi | (pk2bf(0.f, k - __uint_as_float(hi << 16)) & 0xffff0000u); };
#pragma unroll
        for (int v = 0; v < 3; ++v)
#pragma unroll
            for (int i = 0; i < 4; ++i) {
                const int pos = base_of(v, 4 * g + i) + 8 * c;
                if (pos <= 2048) L.Kd[index_of_pos(pos)] = split(2.0f * acc[v][i]);
            }
    }
    __builtin_amdgcn_sched_barrier(0);
    asm volatile("s_waitcnt vmcnt(0)" : "+v"(tk), "+a"(uu[0]), "+a"(uu[1]), "+a"(uu[2]), "+a"(uu[3]) :: "memory");
    if (first) {
        wait_word(p, p.ws + G_WS_CTL + G_C_UT + (lane & 31) * 128, 1, 30u);
        WLD16X4A(uu[0], uu[1], uu[2], uu[3], voff, ut);
        asm volatile("s_waitcnt vmcnt(0)" : "+a"(uu[0]), "+a"(uu[1]), "+a"(uu[2]), "+a"(uu[3]) :: "memory");
    }
    if (pend >= 0 && lane == 0) __hip_atomic_fetch_add((gu32*)(WC(p) + G_WS_CTL + G_C_YC + pend * 128), 1u, RLX_AGENT);
    nb = __builtin_amdgcn_readfirstlane(tk) + 1;
    const u32x4 z4 = {0u, 0u, 0u, 0u};
    *(u32x4*)&L.Kh[8 * lane] = z4; *(u32x4*)&L.Kl[8 * lane] = z4;
    *(u32x4*)&L.Kh[512 + 8 * (lane & 31)] = z4; *(u32x4*)&L.Kl[512 + 8 * (lane & 31)] = z4;
    *(u32x4*)&L.uc[0][64 + SL] = z4;
    *(u32x4*)&L.uc[(lane >> 3) & 3][8 * (lane & 7)] = z4;
    asm volatile("" ::: "memory");
#pragma unroll
    for (int q = 0; q < 4; ++q) {
        const int e = lane + 64 * q, at = (16 + (e >> 2)) * KROW + 24 - 8 * (e & 3);
        unsigned k[8];
        const unsigned* kd = L.Kd + index_of_pos(8 * e);
        if (q == 0) {
            const bool two = lane >= 32;
#pragma unroll
            for (int r = 0; r < 8; ++r) k[7 - r] = kd[two ? (0x43222100 >> (4 * r)) & 7 : r];
        } else if (q == 1) {
            const unsigned k0 = kd[0], k1 = kd[1], k2 = kd[2];
            k[7] = k[6] = k[5] = k0; k[4] = k[3] = k[2] = k1; k[1] = k[0] = k2;
        } else {
            const unsigned k0 = kd[0], k1 = kd[1];
            k[7] = k[6] = k[5] = k[4] = k0; k[3] = (e & 1) ? k1 : k0; k[2] = k[1] = k[0] = k1;
        }
        u32x4 kh, kl;
#pragma unroll
        for (int j = 0; j < 4; ++j) { kh[j] = prm(k[2 * j + 1], k[2 * j], 0x05040100u); kl[j] = prm(k[2 * j + 1], k[2 * j], 0x07060302u); }
        *(u32x4*)&L.Kh[at] = kh;
        if (G_NHL == 2) *(u32x4*)&L.Kl[at] = kl;
    }
    BST(3);
#pragma unroll
    for (int q = 0; q < 4; ++q) *(u32x4*)&L.uc[0][64 + 8 * (lane + 64 * q)] = uu[q];
    asm volatile("" ::: "memory");
#pragma unroll
    for (int q = 0; q < 4; ++q) {
        const int e = lane + 64 * q;
        const int ue = 56 + 8 * e;
        const u32x4 a = *(const u32x4*)&L.uc[0][ue];
        const u32x2 b = *(const u32x2*)&L.uc[0][ue + 8];
        const unsigned s0 = __builtin_amdgcn_alignbit(a[1], a[0], 16), s1 = __builtin_amdgcn_alignbit(a[2], a[1], 16), s2 = __builtin_amdgcn_alignbit(a[3], a[2], 16),
                       s3 = __builtin_amdgcn_alignbit(b[0], a[3], 16), s4 = __builtin_amdgcn_alignbit(b[1], b[0], 16);
        *(u32x4*)&L.uc[1][ue] = u32x4{s0, s1, s2, s3};
        *(u32x4*)&L.uc[2][ue] = u32x4{a[1], a[2], a[3], b[0]};
        *(u32x4*)&L.uc[3][ue] = u32x4{s1, s2, s3, s4};
    }
    if (lane == 0) {
        const u32x4 a = *(const u32x4*)&L.uc[0][56 + SL];
        const unsigned s0 = __builtin_amdgcn_alignbit(a[1], a[0], 16), s1 = __builtin_amdgcn_alignbit(a[2], a[1], 16), s2 = __builtin_amdgcn_alignbit(a[3], a[2], 16), s3 = a[3] >> 16;
        *(u32x4*)&L.uc[1][56 + SL] = u32x4{s0, s1, s2, s3};
        *(u32x4*)&L.uc[2][56 + SL] = u32x4{a[1], a[2], a[3], 0u};
        *(u32x4*)&L.uc[3][56 + SL] = u32x4{s1, s2, s3, 0u};
    }
    kp = kgen_par(p, min(nb, DM - 1), lane);
    BST(4);
    {
        const int o = 33 + c + 8 * g;
        const unsigned ua = (unsigned)(uintptr_t)&L.uc[o & 3][o & ~3], ka = (unsigned)(uintptr_t)&L.Kh[(16 + c) * KROW + 8 * g];
        const f32x4* res = (const f32x4*)L.Kh + lane;
        asm volatile(ASM_CV :: "v"(ua), "v"(ka), "v"((unsigned)(uintptr_t)res) : CV_CLOB);
        asm volatile("" :: "v"(kp.ldt), "v"(kp.dsk), "v"(kp.pa[0][0]), "v"(kp.pa[0][1]), "v"(kp.pa[0][2]), "v"(kp.pa[0][3]), "v"(kp.pa[1][0]), "v"(kp.pa[1][1]), "v"(kp.pa[1][2]), "v"(kp.pa[1][3]));
        BST(5);
#pragma unroll
        for (int par = 0; par < 2; ++par)
#pragma unroll
            for (int t = 0; t < 4; ++t) {
                const int n = 16 * (par + 2 * (16 * t + c)) + 4 * g;
                const u32x2 uu = *(const u32x2*)&L.uc[0][64 + n];
                const f32x4 y4 = res[(par * 4 + t) * 64];
                const float r0 = bfr(y4[0]) + bfr(lo2f(uu[0]) * dsk), r1 = bfr(y4[1]) + bfr(hi2f(uu[0]) * dsk);
                const float r2 = bfr(y4[2]) + bfr(lo2f(uu[1]) * dsk), r3 = bfr(y4[3]) + bfr(hi2f(uu[1]) * dsk);
                *(u32x2*)&L.uc[1][n] = u32x2{pk2bf(r0, r1), pk2bf(r2, r3)};
            }
    }
    BST(6);
    char* yb = uni(p.ws + G_D_YB + (size_t)d * 4096);
#pragma unroll
    for (int q = 0; q < 4; ++q) { const u32x4 o = *(const u32x4*)&L.uc[1][8 * (lane + 64 * q)]; WST16(voff, o, yb, q * 1024); }
    pend = d >> 6;
    BST(7);
}
__device__ __forceinline__ void s4d_wave(const MainP& p, char* smem, int w, int lane, int quota) {
    int nb = 0, pend = -1;
    if (lane == 0) nb = (int)(__hip_atomic_fetch_add((gu32*)(WC(p) + G_WS_CTL + G_C_TASK + 128), 1u, RLX_AGENT) + 1u);
    nb = __builtin_amdgcn_readfirstlane(nb);
    KPar kp = kgen_par(p, min(nb, DM - 1), lane);
#pragma unroll 1
    for (int n = 0; n < quota; ++n) {
        const int d = nb;
        if (d >= DM) break;
        nb = n + 1 < quota ? 0 : -1;
        task_s4d(p, reinterpret_cast<BCh*>(smem)[w], d, lane, n == 0, nb, kp, pend);
    }
    VMC0();
    if (pend >= 0 && lane == 0) __hip_atomic_fetch_add((gu32*)(WC(p) + G_WS_CTL + G_C_YC + pend * 128), 1u, RLX_AGENT);
}
__device__ __forceinline__ void watch_role(const MainP& p, int lane) {
    const char* c = WC(p) + G_WS_CTL;
#pragma unroll 1
    for (int s = G_S0; s < NSEG; ++s) {
        const int n = min(s + 3, NSEG - 1);
        wait_word(p, c + G_C_GV + lane * 128, n - (n < (int)G_S1 ? (n ^ (lane >> 5)) & 1 : 0), 60u);
        wait_word(p, (lane < 32 ? c + G_C_GX : p.ws + G_WS_CTL + G_C_UT - 4096) + lane * 128, lane < 32 ? min(s + 1 + KTOUCH, NSEG - 1) : s < 40 ? -1 : 2, 61u);
        if (lane == 0) __hip_atomic_store((gu32*)(p.ws + G_WS_CTL + G_C_RDY + s * 128), 1u, RLX_AGENT);
    }
}
__device__ __forceinline__ void task_merge(const MainP& p, char* smem, int gr, int i, int w, int lane) {
    const int g = lane >> 4, c = lane & 15;
    wait_word(p, WC(p) + G_WS_CTL + G_C_YC + gr * 128, 63, 31u);
    const char* sb = uni(p.ws + G_D_YB + (size_t)gr * 262144 + i * 1024 + w * 256);
    const unsigned vt = g * 4096 + c * 16;
    u32x4 z[16];
#pragma unroll
    for (int j = 0; j < 16; ++j) { const char* sj = sb + j * 16384; WLD16(z[j], vt, sj, 0); }
    asm volatile("s_waitcnt vmcnt(0)" : "+v"(z[0]), "+v"(z[1]), "+v"(z[2]), "+v"(z[3]), "+v"(z[4]), "+v"(z[5]), "+v"(z[6]), "+v"(z[7]),
                 "+v"(z[8]), "+v"(z[9]), "+v"(z[10]), "+v"(z[11]), "+v"(z[12]), "+v"(z[13]), "+v"(z[14]), "+v"(z[15]) :: "memory");
    char* tw = smem + w * 17408;
#pragma unroll
    for (int j = 0; j < 16; ++j) *(u32x4*)(tw + (4 * j + g) * 272 + c * 16) = z[j];
    const unsigned ad = (unsigned)(uintptr_t)tw + (8 * g + (c >> 2)) * 272 + (c & 3) * 8;
    u16* orow = p.out + (size_t)(512 * i + 128 * w + c) * OUTW + DM + 64 * gr + 8 * g;
#pragma unroll
    for (int k = 0; k < 8; ++k) {
        u32x2 x0, x1, x2, x3;
        asm volatile("ds_read_b64_tr_b16 %0, %4 offset:%5\n\tds_read_b64_tr_b16 %1, %4 offset:%6\n\tds_read_b64_tr_b16 %2, %4 offset:%7\n\tds_read_b64_tr_b16 %3, %4 offset:%8\n\ts_waitcnt lgkmcnt(0)"
                     : "=&v"(x0), "=&v"(x1), "=&v"(x2), "=&v"(x3) : "v"(ad), "i"(k * 32), "i"(4 * 272 + k * 32), "i"(32 * 272 + k * 32), "i"(36 * 272 + k * 32) : "memory");
        OST16(orow + (size_t)16 * k * OUTW, (u32x4{x0[0], x0[1], x1[0], x1[1]}));
        OST16(orow + (size_t)16 * k * OUTW + 32, (u32x4{x2[0], x2[1], x3[0], x3[1]}));
    }
}

static constexpr int Q1 = NG1 * 6 + 36, Q2 = Q1 + (NFINE - 2) * 25;
static constexpr int NTASK = Q2 + 34 + 52;
__host__ __device__ __forceinline__ void task_of(int u, int m0, int& sg, int& r) {
    if (u < 6 * m0) { sg = u / 6; r = u % 6; }
    else if (u < 6 * m0 + 108) { sg = m0 + (u - 6 * m0) / 9; r = (u - 6 * m0) % 9; }
    else if (u < Q1) { sg = (u - 36) / 6; r = (u - 36) % 6; }
    else if (u < Q2) { sg = NG1 + (u - Q1) / 25; r = (u - Q1) % 25; }
    else { sg = u < Q2 + 34 ? 62 : 63; r = u < Q2 + 34 ? u - Q2 : u - Q2 - 34; }
}
__device__ __forceinline__ void worker_body(const MainP& p, char* smem, const int w, const int lane) {
    const int tid = w * 64 + lane;
    int* slot = (int*)(smem + LDS_TOTAL - 16);
    int nst = 0, nxt = -1;
    gu32* qa = (gu32*)(WC(p) + G_WS_CTL + G_C_TASK);
    const int m0 = (FLH(p.prm[7]) >> 4) & 63;
    for (;;) {
        if (tid == 0) {
            *slot = nxt >= 0 ? nxt : (int)(__hip_atomic_fetch_add(qa, 1u, RLX_AGENT) + 1u);
            nxt = -1;
        }
        __syncthreads();
        const int u = __builtin_amdgcn_readfirstlane(*slot);
        if (u >= NTASK) break;
        if (tid == 0 && u < Q1 - 140) nxt = (int)(__hip_atomic_fetch_add(qa, 1u, RLX_AGENT) + 1u);
        const bool last2 = u >= Q2;
        int sg, r;
        task_of(u, m0, sg, r);
        if (u < Q1) {
            if (r < 6) task_out<6, 6, false>(p, smem, sg, 6 * r, w, lane);
            else { const int m = 3 * (sg - m0) + r - 6; task_merge(p, smem, m >> 2, m & 3, w, lane); }
        }
        else if (r < 16) task_attn_tail(p, sg, 2 * r + (w >> 1), w, lane);
        else if (sg == 63) task_out<2, 9, true, 1>(p, smem, sg, (r - 16) & ~1, w, lane, r & 1);
        else if (last2) task_out<2, 9, true>(p, smem, sg, 2 * (r - 16), w, lane);
        else task_out<4, 9, true>(p, smem, sg, 4 * (r - 16), w, lane);
        if (last2) break;
        __syncthreads();
    }
}

__host__ __device__ constexpr unsigned phys(unsigned x) { return (x >> 13) * 32768 + (x & 8191); }
__host__ __device__ constexpr unsigned rep(int x) { return G_RSW ? (x >> 1) * 16384 + (x & 1) * 131072 : (x & 1) * 16384 + (x >> 1) * 131072; }
__host__ __device__ constexpr unsigned aMB(int f, int b) { return phys(f < 8 ? 16384 + f * 1024 + b : 28672 + b); }
__host__ __device__ constexpr unsigned aPX(int pr, int b) { return phys(pr * 1024 + b); }
__host__ __device__ constexpr unsigned aPM(int u, int b) { return phys(24576 + u * 1024 + b); }
static constexpr int L_KI = 8192, L_QI = 16384, L_VI = 147456;

__device__ __forceinline__ int lane_id() { int l; asm volatile("v_mbcnt_lo_u32_b32 %0, -1, 0\n\tv_mbcnt_hi_u32_b32 %0, -1, %0" : "=v"(l)); return l; }
typedef const __attribute__((address_space(4))) MainP* KArg;

__global__ __launch_bounds__(256, 1) void main_kernel(MainP p_) {
    __shared__ __attribute__((aligned(16))) char smem[LDS_TOTAL];
    KArg ka = (KArg)__builtin_amdgcn_kernarg_segment_ptr();
    static_assert(offsetof(MainP, W) == 0x28, "argument layout");
    asm volatile(ASM_EA :: "s"(ka), "s"(blockIdx.x), "v"(threadIdx.x) : "v1", "v2", "s40", "s41", "s42", "s43", "s44", "s45", "s46", "s47", "s48", "s49", "scc", "memory");
    asm volatile("" : "+s"(ka));
    const int b = blockIdx.x, x = b & 7, ix = b >> 3;
    const int w = __builtin_amdgcn_readfirstlane(threadIdx.x >> 6);
    const bool isr = x < 4 && ix >= 16;
    constexpr int NRF = 8 + 8 * G_GVN;
    if (__builtin_expect(ix < (x < 4 ? 16 : NRF), 1)) {
        const int h = (x & 3) * 8 + (ix & 7), id = h + (ix >> 4) * 32;
        const unsigned stb = 0;
#define UNR _Pragma("unroll")
#define CW(k, val) do { if ((k) < 16) ca[(k) & 15] = (unsigned)(val); else if ((k) < 32) cb[(k) & 15] = (unsigned)(val); else cl[((k) - 40) & 15] = (unsigned)(val); } while (0)
#define ROLEW(A_) asm volatile(A_ : "+{v[0:15]}"(ca), "+{v[16:31]}"(cb), "+{v[200:215]}"(cl) : "s"(ka), "s"(w), "s"(id), "s"(stb) : ASM_CLOBW)
#define PAD(n_) asm volatile("s_branch skip_" n_ "\npad_" n_ ":\n\ts_setpc_b64 s[46:47]\nskip_" n_ ":")
#define CW_ALL const int lane = lane_id(), g = lane >> 4, c = lane & 15, q = c >> 2; u32x16 ca = {}, cb = {}, cl = {};                                      \
        CW(0, lane * 16); CW(40, (4 * w + g) * 8192 + h * 256 + (c ^ (2 * g + 8 * (w >> 1))) * 16);                                                          \
        UNR for (int k = 0; k < 2; ++k) CW(41 + k, (8 * g + q) * 256 + ((4 * w + 2 * k + ((c >> 1) & 1)) ^ (2 * q + 8 * (g & 1))) * 16 + (c & 1) * 8)
#define CW_SRC(b_, key_) UNR for (int k = 0; k < 9; ++k) { const int pp = 64 * k + lane, r = pp / 72, pc = pp - 72 * r; CW(43 + k, (b_) + (8 * w + r) * 1152 + (pc ^ (key_)) * 16); }
#define UNIT(u_, key_) (((u_) >> 2) * 128 + (((u_) & 3) ^ (key_)) * 32 + (c & 3) * 8)
#define CW_NEAR CW(1, rep(x) + aMB(2 * w, lane * 16)); CW(2, rep(x) + aMB(8, w * 256 + lane * 4)); CW(3, 2 * w * 1024 + lane * 16);                            \
        UNR for (int i = 0; i < 6; ++i) CW(4 + i, ((2 * w + 2 + i) & 7) * 1024 + lane * 16);                                                                 \
        CW(10, (c & 2) ? 0xEEEE : 0x4444);                                                                                                                   \
        CW(20, rep(x) + 38912 + (lane & 31) * 64); CW(21, lane < 32 ? rep(lane & 3) + phys((lane >> 2) * 2048) + h * 64 : 65536 + h * 128);          \
        CW(22, lane * 4); CW(23, rep(x) + aMB(2 * w + 1, lane * 16))
#define CW_FAR CW(0, c * 1152 + (g ^ (c >> 1)) * 16); CW(1, ca[0]); CW(2, ca[0] ^ 64); CW(3, w * 9216 + lane * 16); CW_SRC(0, (r >> 1) + 4 * (w & 1))                                   \
        CW(4, L_VI + w * 2048 + lane * 16); CW(5, L_VI + lane * 16); CW(6, lane * 4)
        if (x >= 4 && ix >= 8) {
            PAD("gv"); CW_ALL; CW_FAR;
            UNR for (int t = 0; t < 9; ++t) CW(8 + t, (9 * w + t) * G_UXS + skew(0, 9 * w + t) * 32768 + h * 1024 + lane * 16);
            UNR for (int k = 0; k < 2; ++k) CW(19 + k, (9 * (w >> 1) + 4 * k + g) * 32768 + h * 256 + c * 16);
            CW(21, (9 * (w >> 1) + 8) * 32768 + h * 256 + lane * 4);
            UNR for (int k = 0; k < 4; ++k) CW(52 + k, (4 * g + q) * 1152 + ((9 * w + k) >> 2) * 128 + ((2 * ((9 * w + k) & 3) + ((c >> 1) & 1)) ^ (2 * g + (q >> 1))) * 16 + (c & 1) * 8);
            ROLEW(ASM_GV);
        } else if (x >= 4) {
            PAD("gx"); CW_ALL; CW_FAR;
            CW(8, w * 2048 + lane * 16);
            ROLEW(ASM_GX);
        } else if (ix < 8) {
            PAD("qk"); CW_ALL; CW_NEAR;
            CW(12, L_QI + (16 * w + (c & 13)) * 32 + 8 * g + 4 * ((c >> 1) & 1));
            CW(13, L_QI + (8 * g + q) * 32 + (c & 3) * 8);
            CW(14, G_WS_CTL + G_C_KM + h * 128); CW(15, lane * 16 + 4096);
            CW(16, L_KI + (32 * w + c) * 16 + 4 * g);
            CW(17, L_KI + (4 * g + q) * 16 + (c & 1) * 8);
            const int mm = 8 * (w & 1) + 4 * (g >> 1) + (g & 1);
            CW(18, rep(x ^ 3) + aPX(h >> 1, (h & 1) * 512 + (w & 1) * 256 + g * 64 + (c >> 3) * 32 + ((c >> 1) & 1) * 16 + (2 * (q & 1) + (c & 1)) * 4));
            CW(19, rep((c & 1) + 2 * (c >> 3)) + aPM(h >> 3, ((h >> 1) & 3) * 256 + (mm + 2 * ((c >> 1) & 1)) * 16 + (h & 1) * 8 + (q & 1) * 4));
            CW(24, G_WS_CTL + G_C_GX + h * 128);
            ROLEW(ASM_QK);
        } else {
            PAD("vv"); CW_ALL; CW_NEAR; CW_SRC(h * 147456, r & 4);
            const int tq = 9 * (w >> 1) + 2 * (g & 1) + (g >> 1);
            CW(14, tq * 32768 + h * 256 + c * 16);
            CW(15, (tq + 4) * 32768 + h * 256 + c * 16);
            CW(16, (9 * (w >> 1) + 8) * 32768 + h * 256 + c * 16 + (g & 1) * 8);
            CW(19, (c & 8) ? 0xEEEE : 0x4444);
            CW(12, L_QI + w * 576 + c * 16 + 4 * g);
            CW(13, L_QI + (8 * g + q) * 16 + (c & 1) * 8 + ((c >> 1) & 1) * 576);
            CW(25, L_QI + (8 * g + q) * 16 + (c & 1) * 8 + (1 - ((c >> 1) & 1)) * 576);
            UNR for (int k = 0; k < 4; ++k) CW(52 + k, ((k >> 1) ^ (q >> 1)) * 36864 + (4 * g + (q & 1)) * 1152 + UNIT(9 * w + 2 * (k & 1) + (q >> 1), 2 * (g & 1)));
            CW(24, (q >> 1) * 36864 + (4 * g + (q & 1)) * 1152 + UNIT(9 * w + 8, 2 * (g & 1)));
            ROLEW(ASM_V);
        }
    } else {
    if (isr && (ix < 25 || ix >= 29)) {
        const int id = x * 12 + (ix < 25 ? ix - 13 : ix - 29);
        init_cal(ka->ws, id, (x ^ (FLG(ka->prm[5]) >> 3)) & 1, threadIdx.x, (FLG(ka->prm[5]) >> 16) & 15);
        if (FLG(ka->prm[5]) & 4) {
            const unsigned o = (id * 384 + threadIdx.x) * 128, o2 = o + (threadIdx.x & 128 ? 16384 : 32768);
            const char* a = (const char*)ka->X + o; const char* b2 = (const char*)ka->U + o;
            const char* c2 = (const char*)(w < 2 ? ka->X : ka->U) + o2;
            const char* ps = (const char*)(w == 0 ? ka->logA : w == 1 ? ka->Aim : w == 2 ? ka->Cre : ka->Cim) + id * 3072 + min((int)(threadIdx.x & 63), 23) * 128;
            asm volatile("s_mov_b32 m0, %4\n\ts_nop 0\n\tglobal_load_lds_dword %0, off\n\tglobal_load_lds_dword %1, off\n\tglobal_load_lds_dword %2, off\n\tglobal_load_lds_dword %3, off"
                         :: "v"(a), "v"(c2), "v"(b2), "v"(ps), "s"(163072) : "memory");
        }
    }
    if (x >= 4 ? ix >= 24 : (ix >= 25 && ix < 29)) init_fill(ka->ws, ka->mem0, x >= 4 ? (ix - 24) * 4 + (x & 3) : 32 + (ix - 25) * 4 + x, threadIdx.x);
    if (x >= 4 && !(FLG(ka->prm[5]) & 1)) {
        const char* src = (const char*)ka->W[3] + (size_t)((x & 3) * 8 + (ix & 7)) * 147456 + threadIdx.x * 128;
#pragma unroll
        for (int i = 0; i < 5; ++i)
            if (i < 4 || w < 2) asm volatile("s_mov_b32 m0, %1\n\ts_nop 0\n\tglobal_load_lds_dword %0, off" :: "v"(src + i * 32768), "s"(155648) : "memory");
    }
    if (isr && ix >= 25) {
        for (int i = 0; i < (ix < 29 ? 3 : 0) + HSL; ++i) __builtin_amdgcn_s_sleep(40);
        init_wait(ka->ws, w, threadIdx.x & 63);
    }
    asm volatile("" : "+s"(ka));
    if (x < 4 && (ix < 25 || w == 0)) {
        const int lane = threadIdx.x & 63, g = lane >> 4, c = lane & 15;
        const unsigned lb = w * G_STG;
        unsigned* ct = (unsigned*)(smem + lb) + lane;
        asm volatile("" :: "v"(ct) : "memory");
#define CTW(k, val) ct[(k) * 64] = (unsigned)(val)
        const u64 ws = (u64)ka->ws, prm = (u64)ka;
        const u64 stb = 0;
        CTW(0, lane * 16);
        const int j = x * 9 + ix - 16;
        if (ix >= 25) {
            CTW(1, rep(x) + 38912 + (lane & 31) * 64);
            CTW(2, G_WS_CTL + (lane < 32 || !KTOUCH ? G_C_EF + 3 * 2048 + (lane & 31) * 64 : G_C_GX + (8 * x + (lane & 7)) * 128)); CTW(3, lane < 32 || !KTOUCH ? 2048 : 0);
            for (int k = 0; k < 7; ++k) {
                const int q = min(ix - 25 + 7 * k, KTOUCH ? 43 : 35), id = q < 36 ? 9 * x + (q >> 2) : 8 * x + q - 36;
                CTW(4 + k, (q < 36 ? G_D_UX + id * G_UXS + skew(0, id) * 32768 + (q & 3) * 8192 : G_D_KX + id * G_HS8 + skew(0, id) * 8192) + lane * 128);
                CTW(11 + k, q < 36 ? 32768 : 8192);
            }
        } else {
            const int rot = (FLG(ka->prm[5]) & 16) ? 0 : (x ^ 3) * 4;
            if (w >= 2) {
                return;
            }
            const int us = (FLH(ka->prm[7]) & 1) ? (ix + (x >> 1)) & 1 : 0;
            CTW(1, lane * 16 + 4096 * (1 - us)); CTW(3, lane * 16 + 4096 * us);
            CTW(2, rep(x) + aMB(j >> 2, (j & 3) * 256 + (4 * g + 2 * ((c >> 1) & 1) + (c & 1)) * 16 + (c >> 2) * 4));
            for (int k = 0; k < 6; ++k) { const int q = 9 * k + ix - 16; CTW(46 + k, rep(x) + lane * 16 + (q < 29 ? phys(q * 1024) : 0x200000 + phys((q < 45 ? q - 29 : q < 53 ? q - 21 : 20) * 1024))); }
            CTW(4, (c & 2) ? 0xEEEE : 0x4444); CTW(5, (c & 1) ? 0x03020706u : 0x05040100u);
            CTW(6, G_WS_CTL + G_C_GV + ((w & (G_GVN - 1)) * 32 + ((lane & 31) ^ (2 * rot))) * 128); CTW(7, rep(x) + (lane & 31) * 64); CTW(8, lane * 4); CTW(9, 131072);
            for (int k = 0; k < 16; ++k) { const int i = k ^ rot; CTW(10 + k, rep(x) + aPX(i, (g >> 1) * 512 + (2 * (c >> 2) + (c & 1)) * 64 + (g & 1) * 32 + ((c >> 1) & 1) * 16)); CTW(26 + k, i * 2048 + lane * 16); }
            for (int k = 0; k < 4; ++k) CTW(42 + k, rep(x) + aPM(k ^ (2 * us), lane * 16));
        }
        const int id = ix >= 25 ? ix - 25 : j;
#define ROLE(A_) asm volatile(A_ :: "s"(ws), "s"(ws), "s"(ws), "s"(w), "s"(id), "s"(ws), "s"(prm), "s"(lb), "s"(stb) : ASM_CLOB)
        if (ix >= 25) ROLE(ASM_H);
        else ROLE(ASM_R);
    }
    else if (x >= 4 && ix >= 24) {
        if (ix >= 25) for (int i = 0; i < ((FLG(ka->prm[5]) >> 5) & 7); ++i) __builtin_amdgcn_s_sleep(36);
        const char* xs = (const char*)ka->X + (size_t)((ix - 24) * 4 + (x & 3)) * 73728 + threadIdx.x * 128;
        const int f = (ix - 24) * 4 + (x & 3);
        const u16* ps = (w == 0 ? ka->logA : w == 1 ? ka->Aim : w == 2 ? ka->Cre : ka->Cim) + f * 2304 + min((int)(threadIdx.x & 63), 35) * 64;
        if (ix < 25 || !(FLG(ka->prm[5]) & 4))
        asm volatile("s_mov_b32 m0, %4\n\ts_nop 0\n\tglobal_load_lds_dword %0, off\n\tglobal_load_lds_dword %1, off\n\tglobal_load_lds_dword %2, off\n\tglobal_load_lds_dword %3, off"
                     :: "v"(xs), "v"(xs + 32768), "v"(xs + 65536 - w * 8192), "v"(ps), "s"(163072) : "memory");
        if (ix < 25) for (int i = 0; i < ((FLG(ka->prm[5]) >> 5) & 7); ++i) __builtin_amdgcn_s_sleep(36);
    }
    }
    asm volatile("" : "+s"(ka));
    MainP p;
    p.prm[5] = ka->prm[5]; p.prm[7] = ka->prm[7];
    p.U = ka->U; p.ws = ka->ws; p.logA = ka->logA; p.Aim = ka->Aim; p.Cre = ka->Cre; p.Cim = ka->Cim; p.logdt = ka->logdt; p.Dskip = ka->Dskip; p.out = ka->out;
    if (x >= 4 && ix >= 24) {
        const u32x4 sent = {0xffffffffu, 0xffffffffu, 0xffffffffu, 0xffffffffu};
        const int tid = w * 64 + lane_id(), h = (ix - 24) * 4 + (x & 3);
        task_ut(p, smem, h, w, lane_id(), [&] {
#pragma unroll 1
            for (int sg = NG1; sg < 64; ++sg) {
                const unsigned ok = G_D_KM + h * G_HS4 + skew(sg, h) * 4096 + tid * 16;
                const unsigned op = tid < 128 ? G_D_PX + (sg * 32 + h) * 2048 + tid * 16 : G_D_PM + sg * 16384 + ((tid >> 4) & 1) * 8192 + h * 256 + (tid & 15) * 16;
                asm volatile("global_store_dwordx4 %0, %2, %3 sc1\n\tglobal_store_dwordx4 %1, %2, %3 sc1\n\ts_nop 1" :: "v"(ok), "v"(op), "v"(sent), "s"(p.ws) : "memory");
            }
        });
    }
    near_mask(p.ws, lane_id(), p.prm[0], p.prm[1]);
    p.X = (const u16*)(p.ws + ((p.prm[0] & 2) ? 0 : 8192));
    {
        const int nq = x < 4 ? (w + ((FLH(p.prm[7]) >> 1) & 3) > 3 ? 0 : 1000) : ix < 8 ? (FLG(p.prm[5]) >> 8) & 3 : ix < 16 ? (FLG(p.prm[5]) >> 10) & 3 : (ix & 2) && ((31 - ix) >> 2) < NAG ? 0 : ix < 24 ? (ix < 16 + ((FLH(p.prm[7]) >> 10) & 15) ? (FLG(p.prm[5]) >> 12) & 3 : 0) : (FLG(p.prm[5]) >> 14) & 3;
        if (b == 239 && w == 0) watch_role(p, lane_id());
        else if (nq || b == 239) {
            for (int i = 0; i < (int)(((ix & 7) * 4 + w) * ((FLG(p.prm[5]) >> 23) & 7)); ++i) __builtin_amdgcn_s_sleep(3);
            s4d_wave(p, smem, w, lane_id(), nq == 3 || b == 239 ? 1000 : nq);
        }
        if (x < 4) return;
        __syncthreads();
    }
    if ((ix & 18) == 18 && ((31 - ix) >> 2) < NAG) { attn_role(p, ((31 - ix) >> 2) + (ix < 24 ? NA : 0), (x & 3) * 8 + (ix & 1) * 4 + w, w, lane_id());
        if (b == 255) {
            wait_word(p, p.ws + G_WS_CTL + G_C_ARR, 255, 50u);
            if (w == 0 && lane_id() == 0) __hip_atomic_fetch_add((gu32*)(p.ws + G_WS_EP), 1u, RLX_AGENT);
        }
        __syncthreads();
    }
    worker_body(p, smem, w, lane_id());
}

extern "C" __global__ void code_end_marker() {}

static constexpr int g_prm[8] = {PRM_DEFAULT};

extern "C" void kernel_launch(void* const* d_in, const int* in_sizes, int n_in, void* d_out, int out_size,
                              void* d_ws, size_t ws_size, hipStream_t stream) {
    char* ws = (char*)(((uintptr_t)d_ws + 0x1FFFFF) & ~(uintptr_t)0x1FFFFF);
    MainP p;
    for (int i = 0; i < 8; ++i) p.prm[i] = g_prm[i];
    p.mem0 = (const u16*)d_in[1];
    for (int i = 0; i < 4; ++i) p.W[i] = (const u16*)d_in[2 + i];
    p.X = (const u16*)d_in[0]; p.U = (const u16*)d_in[7]; p.ws = ws;
    p.logA = (const u16*)d_in[8]; p.Aim = (const u16*)d_in[9]; p.Cre = (const u16*)d_in[10]; p.Cim = (const u16*)d_in[11];
    p.logdt = (const u16*)d_in[12]; p.Dskip = (const u16*)d_in[13]; p.out = (u16*)d_out;
    main_kernel<<<256, 256, 0, stream>>>(p);
}
```

```cpp
#include <hip/hip_runtime.h>
#include <hip/hip_bf16.h>
#include <stdint.h>
#include <stddef.h>

typedef __attribute__((ext_vector_type(8))) __bf16 bf16x8;
typedef __attribute__((ext_vector_type(4))) short s16x4;
typedef __attribute__((ext_vector_type(4))) float f32x4;
typedef __attribute__((ext_vector_type(4))) unsigned u32x4;
typedef __attribute__((ext_vector_type(2))) unsigned u32x2;
typedef __attribute__((ext_vector_type(2))) float f32x2;
typedef __attribute__((ext_vector_type(16))) unsigned u32x16;
typedef unsigned short u16;
typedef unsigned long long u64;

#define GAS __attribute__((address_space(1)))
typedef GAS unsigned gu32;
#define RLX_AGENT __ATOMIC_RELAXED, __HIP_MEMORY_SCOPE_AGENT

static constexpr int DM = 576, NH = 32, DH = 128, MM = 8, SEG = 32, NSEG = 64, SL = 2048;
static constexpr int HD = NH * DH;
static constexpr int OUTW = 2 * DM;
static constexpr int NST = 128;

__device__ __forceinline__ float bf2f(u16 h) { return __uint_as_float(((unsigned)h) << 16); }
typedef __attribute__((ext_vector_type(2))) __bf16 bf16x2_t;
__device__ __forceinline__ unsigned pk2bf(float a, float b) {
    bf16x2_t v; v[0] = (__bf16)a; v[1] = (__bf16)b;
    return __builtin_bit_cast(unsigned, v);
}
__device__ __forceinline__ float bfr(float f) { return __uint_as_float(pk2bf(f, f) << 16); }
__device__ __forceinline__ float lo2f(unsigned u) { return __uint_as_float(u << 16); }
__device__ __forceinline__ float hi2f(unsigned u) { return __uint_as_float(u & 0xffff0000u); }
__device__ __forceinline__ float rowmax4(float x) {
    auto a = __builtin_amdgcn_permlane16_swap(__float_as_uint(x), __float_as_uint(x), false, false);
    x = fmaxf(__uint_as_float(a[0]), __uint_as_float(a[1]));
    auto b = __builtin_amdgcn_permlane32_swap(__float_as_uint(x), __float_as_uint(x), false, false);
    return fmaxf(__uint_as_float(b[0]), __uint_as_float(b[1]));
}
__device__ __forceinline__ float rowsum4(float x) {
    auto a = __builtin_amdgcn_permlane16_swap(__float_as_uint(x), __float_as_uint(x), false, false);
    x = __uint_as_float(a[0]) + __uint_as_float(a[1]);
    auto b = __builtin_amdgcn_permlane32_swap(__float_as_uint(x), __float_as_uint(x), false, false);
    return __uint_as_float(b[0]) + __uint_as_float(b[1]);
}

struct MainP {
    int prm[8];
    const u16* mem0; const u16* W[4]; const u16* X; const u16* U;
    char* ws;
    const u16* logA; const u16* Aim; const u16* Cre; const u16* Cim; const u16* logdt; const u16* Dskip;
    u16* out;
};
static constexpr int LDS_TOTAL = 163840;
static constexpr unsigned kWaitCap = 1u << 18;
#define VMC0() asm volatile("s_waitcnt vmcnt(0)" ::: "memory")
#define PRM_DEFAULT 1020, 600, 128, 640, 736, 1434873899, 3145976, 52930
#define ASM_QK \
    "s_load_dwordx2 s[42:43], %3, 40\n\t" \
    "s_load_dwordx2 s[44:45], %3, 48\n\t" \
    "s_load_dwordx2 s[36:37], %3, 0x58\n\t" \
    "s_load_dwordx2 s[50:51], %3, 0x48\n\t" \
    "s_load_dwordx8 s[84:91], %3, 0\n\t" \
    "s_mov_b32 s66, %4\n\t" \
    "s_mov_b32 s67, %5\n\t" \
    "s_mov_b32 s68, 262144\n\t" \
    "s_mov_b32 s72, 0\n\t" \
    "s_mov_b32 s83, -1\n\t" \
    "s_mov_b32 s65, 0\n\t" \
    "s_lshl_b32 s82, s66, 10\n\t" \
    "s_waitcnt lgkmcnt(0)\n\t" \
    "s_add_u32 s46, s42, 2097152\n\t" \
    "s_addc_u32 s47, s43, 0\n\t" \
    "s_add_u32 m0, s82, 65536\n\t" \
    "s_nop 0\n\t" \
    "global_load_lds_dwordx4 v200, s[46:47]\n\t" \
    "s_add_u32 s46, s42, 2228224\n\t" \
    "s_addc_u32 s47, s43, 0\n\t" \
    "s_add_u32 m0, s82, 69632\n\t" \
    "s_nop 0\n\t" \
    "global_load_lds_dwordx4 v200, s[46:47]\n\t" \
    "s_add_u32 s46, s42, 2359296\n\t" \
    "s_addc_u32 s47, s43, 0\n\t" \
    "s_add_u32 m0, s82, 73728\n\t" \
    "s_nop 0\n\t" \
    "global_load_lds_dwordx4 v200, s[46:47]\n\t" \
    "s_add_u32 s46, s42, 2490368\n\t" \
    "s_addc_u32 s47, s43, 0\n\t" \
    "s_add_u32 m0, s82, 77824\n\t" \
    "s_nop 0\n\t" \
    "global_load_lds_dwordx4 v200, s[46:47]\n\t" \
    "s_add_u32 s46, s42, 2621440\n\t" \
    "s_addc_u32 s47, s43, 0\n\t" \
    "s_add_u32 m0, s82, 81920\n\t" \
    "s_nop 0\n\t" \
    "global_load_lds_dwordx4 v200, s[46:47]\n\t" \
    "s_add_u32 s46, s42, 2752512\n\t" \
    "s_addc_u32 s47, s43, 0\n\t" \
    "s_add_u32 m0, s82, 86016\n\t" \
    "s_nop 0\n\t" \
    "global_load_lds_dwordx4 v200, s[46:47]\n\t" \
    "s_add_u32 s46, s42, 2883584\n\t" \
    "s_addc_u32 s47, s43, 0\n\t" \
    "s_add_u32 m0, s82, 90112\n\t" \
    "s_nop 0\n\t" \
    "global_load_lds_dwordx4 v200, s[46:47]\n\t" \
    "s_add_u32 s46, s42, 3014656\n\t" \
    "s_addc_u32 s47, s43, 0\n\t" \
    "s_add_u32 m0, s82, 94208\n\t" \
    "s_nop 0\n\t" \
    "global_load_lds_dwordx4 v200, s[46:47]\n\t" \
    "s_add_u32 s46, s42, 3145728\n\t" \
    "s_addc_u32 s47, s43, 0\n\t" \
    "s_add_u32 m0, s82, 98304\n\t" \
    "s_nop 0\n\t" \
    "global_load_lds_dwordx4 v200, s[46:47]\n\t" \
    "s_add_u32 s46, s42, 3276800\n\t" \
    "s_addc_u32 s47, s43, 0\n\t" \
    "s_add_u32 m0, s82, 102400\n\t" \
    "s_nop 0\n\t" \
    "global_load_lds_dwordx4 v200, s[46:47]\n\t" \
    "s_add_u32 s46, s42, 3407872\n\t" \
    "s_addc_u32 s47, s43, 0\n\t" \
    "s_add_u32 m0, s82, 106496\n\t" \
    "s_nop 0\n\t" \
    "global_load_lds_dwordx4 v200, s[46:47]\n\t" \
    "s_add_u32 s46, s42, 3538944\n\t" \
    "s_addc_u32 s47, s43, 0\n\t" \
    "s_add_u32 m0, s82, 110592\n\t" \
    "s_nop 0\n\t" \
    "global_load_lds_dwordx4 v200, s[46:47]\n\t" \
    "s_add_u32 s46, s42, 3670016\n\t" \
    "s_addc_u32 s47, s43, 0\n\t" \
    "s_add_u32 m0, s82, 114688\n\t" \
    "s_nop 0\n\t" \
    "global_load_lds_dwordx4 v200, s[46:47]\n\t" \
    "s_add_u32 s46, s42, 3801088\n\t" \
    "s_addc_u32 s47, s43, 0\n\t" \
    "s_add_u32 m0, s82, 118784\n\t" \
    "s_nop 0\n\t" \
    "global_load_lds_dwordx4 v200, s[46:47]\n\t" \
    "s_add_u32 s46, s42, 3932160\n\t" \
    "s_addc_u32 s47, s43, 0\n\t" \
    "s_add_u32 m0, s82, 122880\n\t" \
    "s_nop 0\n\t" \
    "global_load_lds_dwordx4 v200, s[46:47]\n\t" \
    "s_add_u32 s46, s42, 4063232\n\t" \
    "s_addc_u32 s47, s43, 0\n\t" \
    "s_add_u32 m0, s82, 126976\n\t" \
    "s_nop 0\n\t" \
    "global_load_lds_dwordx4 v200, s[46:47]\n\t" \
    "s_add_u32 s46, s42, 4194304\n\t" \
    "s_addc_u32 s47, s43, 0\n\t" \
    "s_add_u32 m0, s82, 131072\n\t" \
    "s_nop 0\n\t" \
    "global_load_lds_dwordx4 v200, s[46:47]\n\t" \
    "s_add_u32 s46, s42, 4325376\n\t" \
    "s_addc_u32 s47, s43, 0\n\t" \
    "s_add_u32 m0, s82, 135168\n\t" \
    "s_nop 0\n\t" \
    "global_load_lds_dwordx4 v200, s[46:47]\n\t" \
    "s_add_u32 s46, s42, 4456448\n\t" \
    "s_addc_u32 s47, s43, 0\n\t" \
    "s_add_u32 m0, s82, 139264\n\t" \
    "s_nop 0\n\t" \
    "global_load_lds_dwordx4 v200, s[46:47]\n\t" \
    "s_add_u32 s46, s42, 4587520\n\t" \
    "s_addc_u32 s47, s43, 0\n\t" \
    "s_add_u32 m0, s82, 143360\n\t" \
    "s_nop 0\n\t" \
    "global_load_lds_dwordx4 v200, s[46:47]\n\t" \
    "s_mov_b32 s77, 3019898880\n\t" \
    "s_mov_b32 s78, 3054198966\n\t" \
    "s_waitcnt lgkmcnt(0)\n\t" \
    "s_mov_b32 s70, 116\n\t" \
    "qk_E1_%=:\n\t" \
    "s_memtime s[56:57]\n\t" \
    "s_memrealtime s[2:3]\n\t" \
    "s_nop 0\n\t" \
    "s_memrealtime s[4:5]\n\t" \
    "s_nop 0\n\t" \
    "s_memrealtime s[6:7]\n\t" \
    "s_nop 0\n\t" \
    "s_memrealtime s[8:9]\n\t" \
    "s_nop 0\n\t" \
    "s_memrealtime s[10:11]\n\t" \
    "s_nop 0\n\t" \
    "s_memrealtime s[12:13]\n\t" \
    "s_nop 0\n\t" \
    "s_memrealtime s[14:15]\n\t" \
    "s_nop 0\n\t" \
    "s_memrealtime s[16:17]\n\t" \
    "s_nop 0\n\t" \
    "s_memrealtime s[18:19]\n\t" \
    "s_nop 0\n\t" \
    "s_memrealtime s[20:21]\n\t" \
    "s_nop 0\n\t" \
    "s_memrealtime s[22:23]\n\t" \
    "s_nop 0\n\t" \
    "s_memrealtime s[24:25]\n\t" \
    "s_nop 0\n\t" \
    "s_memrealtime s[26:27]\n\t" \
    "s_nop 0\n\t" \
    "s_memrealtime s[28:29]\n\t" \
    "s_memtime s[54:55]\n\t" \
    "s_add_u32 s70, s70, 8\n\t" \
    "s_waitcnt lgkmcnt(0)\n\t" \
    "s_sub_u32 s69, s54, s56\n\t" \
    "s_cmp_gt_u32 s69, s70\n\t" \
    "s_cbranch_scc1 qk_E1_%=\n\t" \
    "s_add_u32 s2, s2, s4\n\t" \
    "s_add_u32 s6, s6, s8\n\t" \
    "s_add_u32 s10, s10, s12\n\t" \
    "s_add_u32 s14, s14, s16\n\t" \
    "s_add_u32 s18, s18, s20\n\t" \
    "s_add_u32 s22, s22, s24\n\t" \
    "s_add_u32 s2, s2, s6\n\t" \
    "s_add_u32 s10, s10, s14\n\t" \
    "s_add_u32 s18, s18, s22\n\t" \
    "s_add_u32 s2, s2, s26\n\t" \
    "s_add_u32 s10, s10, s18\n\t" \
    "s_add_u32 s2, s2, s10\n\t" \
    "s_mul_i32 s70, s28, 13\n\t" \
    "s_sub_u32 s70, s70, s2\n\t" \
    "s_min_u32 s70, s70, 52\n\t" \
    "s_mul_i32 s69, s69, s70\n\t" \
    "s_mul_hi_u32 s69, s69, 76695844\n\t" \
    "s_mov_b32 s75, s73\n\t" \
    "s_mov_b32 s76, s74\n\t" \
    "s_add_u32 s73, s56, s69\n\t" \
    "s_lshl_b32 s74, s28, 4\n\t" \
    "s_waitcnt vmcnt(32) lgkmcnt(0)\n\t" \
    "s_barrier\n\t" \
    "ds_read_b64_tr_b16 a[0:1], v201 offset:0\n\t" \
    "ds_read_b64_tr_b16 a[2:3], v201 offset:1024\n\t" \
    "ds_read_b64_tr_b16 a[4:5], v201 offset:8192\n\t" \
    "ds_read_b64_tr_b16 a[6:7], v201 offset:9216\n\t" \
    "ds_read_b64_tr_b16 a[72:73], v202 offset:0\n\t" \
    "ds_read_b64_tr_b16 a[74:75], v202 offset:1024\n\t" \
    "ds_read_b64_tr_b16 a[76:77], v202 offset:8192\n\t" \
    "ds_read_b64_tr_b16 a[78:79], v202 offset:9216\n\t" \
    "s_waitcnt vmcnt(28) lgkmcnt(0)\n\t" \
    "s_barrier\n\t" \
    "ds_read_b64_tr_b16 a[8:9], v201 offset:16384\n\t" \
    "ds_read_b64_tr_b16 a[10:11], v201 offset:17408\n\t" \
    "ds_read_b64_tr_b16 a[12:13], v201 offset:24576\n\t" \
    "ds_read_b64_tr_b16 a[14:15], v201 offset:25600\n\t" \
    "ds_read_b64_tr_b16 a[80:81], v202 offset:16384\n\t" \
    "ds_read_b64_tr_b16 a[82:83], v202 offset:17408\n\t" \
    "ds_read_b64_tr_b16 a[84:85], v202 offset:24576\n\t" \
    "ds_read_b64_tr_b16 a[86:87], v202 offset:25600\n\t" \
    "s_add_u32 s46, s44, 0\n\t" \
    "s_addc_u32 s47, s45, 0\n\t" \
    "s_add_u32 m0, s82, 0\n\t" \
    "s_nop 0\n\t" \
    "global_load_lds_dwordx4 v200, s[46:47]\n\t" \
    "s_add_u32 s46, s44, 131072\n\t" \
    "s_addc_u32 s47, s45, 0\n\t" \
    "s_add_u32 m0, s82, 4096\n\t" \
    "s_nop 0\n\t" \
    "global_load_lds_dwordx4 v200, s[46:47]\n\t" \
    "s_add_u32 s46, s44, 262144\n\t" \
    "s_addc_u32 s47, s45, 0\n\t" \
    "s_add_u32 m0, s82, 8192\n\t" \
    "s_nop 0\n\t" \
    "global_load_lds_dwordx4 v200, s[46:47]\n\t" \
    "s_add_u32 s46, s44, 393216\n\t" \
    "s_addc_u32 s47, s45, 0\n\t" \
    "s_add_u32 m0, s82, 12288\n\t" \
    "s_nop 0\n\t" \
    "global_load_lds_dwordx4 v200, s[46:47]\n\t" \
    "v_mov_b32 v160, 1809408\n\t" \
    "s_mov_b32 s69, 3906560\n\t" \
    "v_writelane_b32 v160, s69, 1\n\t" \
    "s_mov_b32 s69, 6238208\n\t" \
    "v_writelane_b32 v160, s69, 2\n\t" \
    "s_mov_b32 s69, 125541376\n\t" \
    "v_writelane_b32 v160, s69, 3\n\t" \
    "s_mov_b32 m0, 155648\n\t" \
    "s_nop 0\n\t" \
    "global_load_lds_dword v160, s[36:37]\n\t" \
    "s_lshl_b32 s70, s66, 8\n\t" \
    "s_mul_i32 s69, s67, 262144\n\t" \
    "s_add_u32 s69, s69, 39846016\n\t" \
    "s_add_u32 s69, s69, s70\n\t" \
    "v_writelane_b32 v160, s69, 0\n\t" \
    "s_mul_i32 s69, s67, 524288\n\t" \
    "s_add_u32 s69, s69, 23069696\n\t" \
    "s_add_u32 s69, s69, s70\n\t" \
    "v_writelane_b32 v160, s69, 1\n\t" \
    "v_mov_b32 v161, 0\n\t" \
    "s_mov_b64 exec, 3\n\t" \
    "global_atomic_or v160, v161, s[36:37] sc1\n\t" \
    "s_mov_b64 exec, -1\n\t" \
    "s_waitcnt vmcnt(30) lgkmcnt(0)\n\t" \
    "s_barrier\n\t" \
    "ds_read_b64_tr_b16 a[16:17], v201 offset:32768\n\t" \
    "ds_read_b64_tr_b16 a[18:19], v201 offset:33792\n\t" \
    "ds_read_b64_tr_b16 a[20:21], v201 offset:40960\n\t" \
    "ds_read_b64_tr_b16 a[22:23], v201 offset:41984\n\t" \
    "ds_read_b64_tr_b16 a[88:89], v202 offset:32768\n\t" \
    "ds_read_b64_tr_b16 a[90:91], v202 offset:33792\n\t" \
    "ds_read_b64_tr_b16 a[92:93], v202 offset:40960\n\t" \
    "ds_read_b64_tr_b16 a[94:95], v202 offset:41984\n\t" \
    "s_add_u32 s46, s44, 524288\n\t" \
    "s_addc_u32 s47, s45, 0\n\t" \
    "s_add_u32 m0, s82, 16384\n\t" \
    "s_nop 0\n\t" \
    "global_load_lds_dwordx4 v200, s[46:47]\n\t" \
    "s_add_u32 s46, s44, 655360\n\t" \
    "s_addc_u32 s47, s45, 0\n\t" \
    "s_add_u32 m0, s82, 20480\n\t" \
    "s_nop 0\n\t" \
    "global_load_lds_dwordx4 v200, s[46:47]\n\t" \
    "s_add_u32 s46, s44, 786432\n\t" \
    "s_addc_u32 s47, s45, 0\n\t" \
    "s_add_u32 m0, s82, 24576\n\t" \
    "s_nop 0\n\t" \
    "global_load_lds_dwordx4 v200, s[46:47]\n\t" \
    "s_add_u32 s46, s44, 917504\n\t" \
    "s_addc_u32 s47, s45, 0\n\t" \
    "s_add_u32 m0, s82, 28672\n\t" \
    "s_nop 0\n\t" \
    "global_load_lds_dwordx4 v200, s[46:47]\n\t" \
    "s_waitcnt vmcnt(30) lgkmcnt(0)\n\t" \
    "s_barrier\n\t" \
    "ds_read_b64_tr_b16 a[24:25], v201 offset:49152\n\t" \
    "ds_read_b64_tr_b16 a[26:27], v201 offset:50176\n\t" \
    "ds_read_b64_tr_b16 a[28:29], v201 offset:57344\n\t" \
    "ds_read_b64_tr_b16 a[30:31], v201 offset:58368\n\t" \
    "ds_read_b64_tr_b16 a[96:97], v202 offset:49152\n\t" \
    "ds_read_b64_tr_b16 a[98:99], v202 offset:50176\n\t" \
    "ds_read_b64_tr_b16 a[100:101], v202 offset:57344\n\t" \
    "ds_read_b64_tr_b16 a[102:103], v202 offset:58368\n\t" \
    "s_add_u32 s46, s44, 1048576\n\t" \
    "s_addc_u32 s47, s45, 0\n\t" \
    "s_add_u32 m0, s82, 32768\n\t" \
    "s_nop 0\n\t" \
    "global_load_lds_dwordx4 v200, s[46:47]\n\t" \
    "s_add_u32 s46, s44, 1179648\n\t" \
    "s_addc_u32 s47, s45, 0\n\t" \
    "s_add_u32 m0, s82, 36864\n\t" \
    "s_nop 0\n\t" \
    "global_load_lds_dwordx4 v200, s[46:47]\n\t" \
    "s_add_u32 s46, s44, 1310720\n\t" \
    "s_addc_u32 s47, s45, 0\n\t" \
    "s_add_u32 m0, s82, 40960\n\t" \
    "s_nop 0\n\t" \
    "global_load_lds_dwordx4 v200, s[46:47]\n\t" \
    "s_add_u32 s46, s44, 1441792\n\t" \
    "s_addc_u32 s47, s45, 0\n\t" \
    "s_add_u32 m0, s82, 45056\n\t" \
    "s_nop 0\n\t" \
    "global_load_lds_dwordx4 v200, s[46:47]\n\t" \
    "s_waitcnt vmcnt(30) lgkmcnt(0)\n\t" \
    "s_barrier\n\t" \
    "v_add_u32 v201, 0x10000, v201\n\t" \
    "v_add_u32 v202, 0x10000, v202\n\t" \
    "ds_read_b64_tr_b16 a[32:33], v201 offset:0\n\t" \
    "ds_read_b64_tr_b16 a[34:35], v201 offset:1024\n\t" \
    "ds_read_b64_tr_b16 a[36:37], v201 offset:8192\n\t" \
    "ds_read_b64_tr_b16 a[38:39], v201 offset:9216\n\t" \
    "ds_read_b64_tr_b16 a[104:105], v202 offset:0\n\t" \
    "ds_read_b64_tr_b16 a[106:107], v202 offset:1024\n\t" \
    "ds_read_b64_tr_b16 a[108:109], v202 offset:8192\n\t" \
    "ds_read_b64_tr_b16 a[110:111], v202 offset:9216\n\t" \
    "s_add_u32 s46, s44, 1572864\n\t" \
    "s_addc_u32 s47, s45, 0\n\t" \
    "s_add_u32 m0, s82, 49152\n\t" \
    "s_nop 0\n\t" \
    "global_load_lds_dwordx4 v200, s[46:47]\n\t" \
    "s_add_u32 s46, s44, 1703936\n\t" \
    "s_addc_u32 s47, s45, 0\n\t" \
    "s_add_u32 m0, s82, 53248\n\t" \
    "s_nop 0\n\t" \
    "global_load_lds_dwordx4 v200, s[46:47]\n\t" \
    "s_add_u32 s46, s44, 1835008\n\t" \
    "s_addc_u32 s47, s45, 0\n\t" \
    "s_add_u32 m0, s82, 57344\n\t" \
    "s_nop 0\n\t" \
    "global_load_lds_dwordx4 v200, s[46:47]\n\t" \
    "s_add_u32 s46, s44, 1966080\n\t" \
    "s_addc_u32 s47, s45, 0\n\t" \
    "s_add_u32 m0, s82, 61440\n\t" \
    "s_nop 0\n\t" \
    "global_load_lds_dwordx4 v200, s[46:47]\n\t" \
    "s_waitcnt vmcnt(30) lgkmcnt(0)\n\t" \
    "s_barrier\n\t" \
    "ds_read_b64_tr_b16 a[40:41], v201 offset:16384\n\t" \
    "ds_read_b64_tr_b16 a[42:43], v201 offset:17408\n\t" \
    "ds_read_b64_tr_b16 a[44:45], v201 offset:24576\n\t" \
    "ds_read_b64_tr_b16 a[46:47], v201 offset:25600\n\t" \
    "ds_read_b64_tr_b16 a[112:113], v202 offset:16384\n\t" \
    "ds_read_b64_tr_b16 a[114:115], v202 offset:17408\n\t" \
    "ds_read_b64_tr_b16 a[116:117], v202 offset:24576\n\t" \
    "ds_read_b64_tr_b16 a[118:119], v202 offset:25600\n\t" \
    "s_add_u32 s46, s44, 2097152\n\t" \
    "s_addc_u32 s47, s45, 0\n\t" \
    "s_add_u32 m0, s82, 65536\n\t" \
    "s_nop 0\n\t" \
    "global_load_lds_dwordx4 v200, s[46:47]\n\t" \
    "s_add_u32 s46, s44, 2228224\n\t" \
    "s_addc_u32 s47, s45, 0\n\t" \
    "s_add_u32 m0, s82, 69632\n\t" \
    "s_nop 0\n\t" \
    "global_load_lds_dwordx4 v200, s[46:47]\n\t" \
    "s_add_u32 s46, s44, 2359296\n\t" \
    "s_addc_u32 s47, s45, 0\n\t" \
    "s_add_u32 m0, s82, 73728\n\t" \
    "s_nop 0\n\t" \
    "global_load_lds_dwordx4 v200, s[46:47]\n\t" \
    "s_add_u32 s46, s44, 2490368\n\t" \
    "s_addc_u32 s47, s45, 0\n\t" \
    "s_add_u32 m0, s82, 77824\n\t" \
    "s_nop 0\n\t" \
    "global_load_lds_dwordx4 v200, s[46:47]\n\t" \
    "s_waitcnt vmcnt(30) lgkmcnt(0)\n\t" \
    "s_barrier\n\t" \
    "ds_read_b64_tr_b16 a[48:49], v201 offset:32768\n\t" \
    "ds_read_b64_tr_b16 a[50:51], v201 offset:33792\n\t" \
    "ds_read_b64_tr_b16 a[52:53], v201 offset:40960\n\t" \
    "ds_read_b64_tr_b16 a[54:55], v201 offset:41984\n\t" \
    "ds_read_b64_tr_b16 a[120:121], v202 offset:32768\n\t" \
    "ds_read_b64_tr_b16 a[122:123], v202 offset:33792\n\t" \
    "ds_read_b64_tr_b16 a[124:125], v202 offset:40960\n\t" \
    "ds_read_b64_tr_b16 a[126:127], v202 offset:41984\n\t" \
    "s_add_u32 s46, s44, 2621440\n\t" \
    "s_addc_u32 s47, s45, 0\n\t" \
    "s_add_u32 m0, s82, 81920\n\t" \
    "s_nop 0\n\t" \
    "global_load_lds_dwordx4 v200, s[46:47]\n\t" \
    "s_add_u32 s46, s44, 2752512\n\t" \
    "s_addc_u32 s47, s45, 0\n\t" \
    "s_add_u32 m0, s82, 86016\n\t" \
    "s_nop 0\n\t" \
    "global_load_lds_dwordx4 v200, s[46:47]\n\t" \
    "s_add_u32 s46, s44, 2883584\n\t" \
    "s_addc_u32 s47, s45, 0\n\t" \
    "s_add_u32 m0, s82, 90112\n\t" \
    "s_nop 0\n\t" \
    "global_load_lds_dwordx4 v200, s[46:47]\n\t" \
    "s_add_u32 s46, s44, 3014656\n\t" \
    "s_addc_u32 s47, s45, 0\n\t" \
    "s_add_u32 m0, s82, 94208\n\t" \
    "s_nop 0\n\t" \
    "global_load_lds_dwordx4 v200, s[46:47]\n\t" \
    "s_waitcnt vmcnt(30) lgkmcnt(0)\n\t" \
    "s_barrier\n\t" \
    "ds_read_b64_tr_b16 a[56:57], v201 offset:49152\n\t" \
    "ds_read_b64_tr_b16 a[58:59], v201 offset:50176\n\t" \
    "ds_read_b64_tr_b16 a[60:61], v201 offset:57344\n\t" \
    "ds_read_b64_tr_b16 a[62:63], v201 offset:58368\n\t" \
    "ds_read_b64_tr_b16 a[128:129], v202 offset:49152\n\t" \
    "ds_read_b64_tr_b16 a[130:131], v202 offset:50176\n\t" \
    "ds_read_b64_tr_b16 a[132:133], v202 offset:57344\n\t" \
    "ds_read_b64_tr_b16 a[134:135], v202 offset:58368\n\t" \
    "s_add_u32 s46, s44, 3145728\n\t" \
    "s_addc_u32 s47, s45, 0\n\t" \
    "s_add_u32 m0, s82, 98304\n\t" \
    "s_nop 0\n\t" \
    "global_load_lds_dwordx4 v200, s[46:47]\n\t" \
    "s_add_u32 s46, s44, 3276800\n\t" \
    "s_addc_u32 s47, s45, 0\n\t" \
    "s_add_u32 m0, s82, 102400\n\t" \
    "s_nop 0\n\t" \
    "global_load_lds_dwordx4 v200, s[46:47]\n\t" \
    "s_add_u32 s46, s44, 3407872\n\t" \
    "s_addc_u32 s47, s45, 0\n\t" \
    "s_add_u32 m0, s82, 106496\n\t" \
    "s_nop 0\n\t" \
    "global_load_lds_dwordx4 v200, s[46:47]\n\t" \
    "s_add_u32 s46, s44, 3538944\n\t" \
    "s_addc_u32 s47, s45, 0\n\t" \
    "s_add_u32 m0, s82, 110592\n\t" \
    "s_nop 0\n\t" \
    "global_load_lds_dwordx4 v200, s[46:47]\n\t" \
    "v_mbcnt_lo_u32_b32 v160, -1, 0\n\t" \
    "v_mbcnt_hi_u32_b32 v160, -1, v160\n\t" \
    "v_lshlrev_b32 v160, 6, v160\n\t" \
    "v_add_u32 v160, 6225920, v160\n\t" \
    "v_mov_b32 v161, 6234112\n\t" \
    "global_load_dword v161, v161, s[36:37] sc1\n\t" \
    "global_load_dwordx2 v[162:163], v160, s[36:37] sc1\n\t" \
    "global_load_dword v164, v160, s[36:37] offset:2048 sc1\n\t" \
    "s_waitcnt vmcnt(33) lgkmcnt(0)\n\t" \
    "s_barrier\n\t" \
    "v_add_u32 v201, 0x10000, v201\n\t" \
    "v_add_u32 v202, 0x10000, v202\n\t" \
    "ds_read_b64_tr_b16 a[64:65], v201 offset:0\n\t" \
    "ds_read_b64_tr_b16 a[66:67], v201 offset:1024\n\t" \
    "ds_read_b64_tr_b16 a[68:69], v201 offset:8192\n\t" \
    "ds_read_b64_tr_b16 a[70:71], v201 offset:9216\n\t" \
    "ds_read_b64_tr_b16 a[136:137], v202 offset:0\n\t" \
    "ds_read_b64_tr_b16 a[138:139], v202 offset:1024\n\t" \
    "ds_read_b64_tr_b16 a[140:141], v202 offset:8192\n\t" \
    "ds_read_b64_tr_b16 a[142:143], v202 offset:9216\n\t" \
    "v_add_u32 v201, 0xfffe0000, v201\n\t" \
    "v_add_u32 v202, 0xfffe0000, v202\n\t" \
    "s_add_u32 s46, s44, 3670016\n\t" \
    "s_addc_u32 s47, s45, 0\n\t" \
    "s_add_u32 m0, s82, 114688\n\t" \
    "s_nop 0\n\t" \
    "global_load_lds_dwordx4 v200, s[46:47]\n\t" \
    "s_add_u32 s46, s44, 3801088\n\t" \
    "s_addc_u32 s47, s45, 0\n\t" \
    "s_add_u32 m0, s82, 118784\n\t" \
    "s_nop 0\n\t" \
    "global_load_lds_dwordx4 v200, s[46:47]\n\t" \
    "s_add_u32 s46, s44, 3932160\n\t" \
    "s_addc_u32 s47, s45, 0\n\t" \
    "s_add_u32 m0, s82, 122880\n\t" \
    "s_nop 0\n\t" \
    "global_load_lds_dwordx4 v200, s[46:47]\n\t" \
    "s_add_u32 s46, s44, 4063232\n\t" \
    "s_addc_u32 s47, s45, 0\n\t" \
    "s_add_u32 m0, s82, 126976\n\t" \
    "s_nop 0\n\t" \
    "global_load_lds_dwordx4 v200, s[46:47]\n\t" \
    "s_waitcnt lgkmcnt(0)\n\t" \
    "s_barrier\n\t" \
    "s_add_u32 s46, s44, 4194304\n\t" \
    "s_addc_u32 s47, s45, 0\n\t" \
    "s_add_u32 m0, s82, 131072\n\t" \
    "s_nop 0\n\t" \
    "global_load_lds_dwordx4 v200, s[46:47]\n\t" \
    "s_add_u32 s46, s44, 4325376\n\t" \
    "s_addc_u32 s47, s45, 0\n\t" \
    "s_add_u32 m0, s82, 135168\n\t" \
    "s_nop 0\n\t" \
    "global_load_lds_dwordx4 v200, s[46:47]\n\t" \
    "s_add_u32 s46, s44, 4456448\n\t" \
    "s_addc_u32 s47, s45, 0\n\t" \
    "s_add_u32 m0, s82, 139264\n\t" \
    "s_nop 0\n\t" \
    "global_load_lds_dwordx4 v200, s[46:47]\n\t" \
    "s_add_u32 s46, s44, 4587520\n\t" \
    "s_addc_u32 s47, s45, 0\n\t" \
    "s_add_u32 m0, s82, 143360\n\t" \
    "s_nop 0\n\t" \
    "global_load_lds_dwordx4 v200, s[46:47]\n\t" \
    "s_waitcnt vmcnt(37) lgkmcnt(0)\n\t" \
    "s_barrier\n\t" \
    "ds_read_b64_tr_b16 a[144:145], v201 offset:0\n\t" \
    "ds_read_b64_tr_b16 a[146:147], v201 offset:1024\n\t" \
    "ds_read_b64_tr_b16 a[148:149], v201 offset:8192\n\t" \
    "ds_read_b64_tr_b16 a[150:151], v201 offset:9216\n\t" \
    "ds_read_b64_tr_b16 a[216:217], v202 offset:0\n\t" \
    "ds_read_b64_tr_b16 a[218:219], v202 offset:1024\n\t" \
    "ds_read_b64_tr_b16 a[220:221], v202 offset:8192\n\t" \
    "ds_read_b64_tr_b16 a[222:223], v202 offset:9216\n\t" \
    "s_waitcnt vmcnt(31) lgkmcnt(0)\n\t" \
    "s_barrier\n\t" \
    "ds_read_b64_tr_b16 a[152:153], v201 offset:16384\n\t" \
    "ds_read_b64_tr_b16 a[154:155], v201 offset:17408\n\t" \
    "ds_read_b64_tr_b16 a[156:157], v201 offset:24576\n\t" \
    "ds_read_b64_tr_b16 a[158:159], v201 offset:25600\n\t" \
    "ds_read_b64_tr_b16 a[224:225], v202 offset:16384\n\t" \
    "ds_read_b64_tr_b16 a[226:227], v202 offset:17408\n\t" \
    "ds_read_b64_tr_b16 a[228:229], v202 offset:24576\n\t" \
    "ds_read_b64_tr_b16 a[230:231], v202 offset:25600\n\t" \
    "s_waitcnt vmcnt(27) lgkmcnt(0)\n\t" \
    "s_barrier\n\t" \
    "ds_read_b64_tr_b16 a[160:161], v201 offset:32768\n\t" \
    "ds_read_b64_tr_b16 a[162:163], v201 offset:33792\n\t" \
    "ds_read_b64_tr_b16 a[164:165], v201 offset:40960\n\t" \
    "ds_read_b64_tr_b16 a[166:167], v201 offset:41984\n\t" \
    "ds_read_b64_tr_b16 a[232:233], v202 offset:32768\n\t" \
    "ds_read_b64_tr_b16 a[234:235], v202 offset:33792\n\t" \
    "ds_read_b64_tr_b16 a[236:237], v202 offset:40960\n\t" \
    "ds_read_b64_tr_b16 a[238:239], v202 offset:41984\n\t" \
    "s_waitcnt vmcnt(23) lgkmcnt(0)\n\t" \
    "s_barrier\n\t" \
    "ds_read_b64_tr_b16 a[168:169], v201 offset:49152\n\t" \
    "ds_read_b64_tr_b16 a[170:171], v201 offset:50176\n\t" \
    "ds_read_b64_tr_b16 a[172:173], v201 offset:57344\n\t" \
    "ds_read_b64_tr_b16 a[174:175], v201 offset:58368\n\t" \
    "ds_read_b64_tr_b16 a[240:241], v202 offset:49152\n\t" \
    "ds_read_b64_tr_b16 a[242:243], v202 offset:50176\n\t" \
    "ds_read_b64_tr_b16 a[244:245], v202 offset:57344\n\t" \
    "ds_read_b64_tr_b16 a[246:247], v202 offset:58368\n\t" \
    "s_waitcnt vmcnt(19) lgkmcnt(0)\n\t" \
    "s_barrier\n\t" \
    "v_add_u32 v201, 0x10000, v201\n\t" \
    "v_add_u32 v202, 0x10000, v202\n\t" \
    "ds_read_b64_tr_b16 a[176:177], v201 offset:0\n\t" \
    "ds_read_b64_tr_b16 a[178:179], v201 offset:1024\n\t" \
    "ds_read_b64_tr_b16 a[180:181], v201 offset:8192\n\t" \
    "ds_read_b64_tr_b16 a[182:183], v201 offset:9216\n\t" \
    "ds_read_b64_tr_b16 a[248:249], v202 offset:0\n\t" \
    "ds_read_b64_tr_b16 a[250:251], v202 offset:1024\n\t" \
    "ds_read_b64_tr_b16 a[252:253], v202 offset:8192\n\t" \
    "ds_read_b64_tr_b16 a[254:255], v202 offset:9216\n\t" \
    "s_waitcnt vmcnt(15) lgkmcnt(0)\n\t" \
    "s_barrier\n\t" \
    "ds_read_b64_tr_b16 a[184:185], v201 offset:16384\n\t" \
    "ds_read_b64_tr_b16 a[186:187], v201 offset:17408\n\t" \
    "ds_read_b64_tr_b16 a[188:189], v201 offset:24576\n\t" \
    "ds_read_b64_tr_b16 a[190:191], v201 offset:25600\n\t" \
    "ds_read_b64_tr_b16 v[224:225], v202 offset:16384\n\t" \
    "ds_read_b64_tr_b16 v[226:227], v202 offset:17408\n\t" \
    "ds_read_b64_tr_b16 v[228:229], v202 offset:24576\n\t" \
    "ds_read_b64_tr_b16 v[230:231], v202 offset:25600\n\t" \
    "s_waitcnt vmcnt(11) lgkmcnt(0)\n\t" \
    "s_barrier\n\t" \
    "ds_read_b64_tr_b16 a[192:193], v201 offset:32768\n\t" \
    "ds_read_b64_tr_b16 a[194:195], v201 offset:33792\n\t" \
    "ds_read_b64_tr_b16 a[196:197], v201 offset:40960\n\t" \
    "ds_read_b64_tr_b16 a[198:199], v201 offset:41984\n\t" \
    "ds_read_b64_tr_b16 v[232:233], v202 offset:32768\n\t" \
    "ds_read_b64_tr_b16 v[234:235], v202 offset:33792\n\t" \
    "ds_read_b64_tr_b16 v[236:237], v202 offset:40960\n\t" \
    "ds_read_b64_tr_b16 v[238:239], v202 offset:41984\n\t" \
    "s_waitcnt vmcnt(8)\n\t" \
    "qk_C2_%=:\n\t" \
    "v_cmp_ne_u32 s[54:55], v162, v164\n\t" \
    "v_sub_u32 v162, v162, v161\n\t" \
    "v_cmp_ne_u32 vcc, 1, v162\n\t" \
    "s_nop 1\n\t" \
    "s_or_b64 vcc, vcc, s[54:55]\n\t" \
    "s_cbranch_scc0 qk_Cok3_%=\n\t" \
    "s_cmp_eq_u32 s68, 0\n\t" \
    "s_cbranch_scc1 qk_F5_%=\n\t" \
    "s_sub_u32 s68, s68, 1\n\t" \
    "s_sleep 6\n\t" \
    "s_branch qk_Cag4_%=\n\t" \
    "qk_F5_%=:\n\t" \
    "v_mov_b32 v164, 5767424\n\t" \
    "v_mov_b32 v165, 4\n\t" \
    "global_store_dword v164, v165, s[36:37] sc1\n\t" \
    "s_waitcnt vmcnt(0)\n\t" \
    "s_branch qk_Cok3_%=\n\t" \
    "qk_Cag4_%=:\n\t" \
    "global_load_dwordx2 v[162:163], v160, s[36:37] sc1\n\t" \
    "global_load_dword v164, v160, s[36:37] offset:2048 sc1\n\t" \
    "s_waitcnt vmcnt(0)\n\t" \
    "s_branch qk_C2_%=\n\t" \
    "qk_Cok3_%=:\n\t" \
    "v_readlane_b32 s69, v163, 2\n\t" \
    "v_readlane_b32 s70, v163, 3\n\t" \
    "s_nop 1\n\t" \
    "s_cmp_lt_u32 s70, s69\n\t" \
    "s_cselect_b32 s69, 0, 8192\n\t" \
    "s_add_u32 s100, s36, s69\n\t" \
    "s_addc_u32 s101, s37, 0\n\t" \
    "s_cmp_lg_u32 s66, 0\n\t" \
    "s_cbranch_scc1 qk_Ca6_%=\n\t" \
    "v_mov_b32 v160, 5768192\n\t" \
    "s_mov_b64 exec, 1\n\t" \
    "global_atomic_add v160, v162, s[36:37] sc1\n\t" \
    "s_mov_b64 exec, -1\n\t" \
    "qk_Ca6_%=:\n\t" \
    "v_mov_b32_dpp v160, v163 quad_perm:[1,0,3,2] row_mask:0xf bank_mask:0xf\n\t" \
    "v_cmp_lt_u32 s[44:45], v160, v163\n\t" \
    "v_min_u32 v160, v160, v163\n\t" \
    "v_and_b32 v161, 1, v160\n\t" \
    "v_cmp_ne_u32 vcc, 0, v161\n\t" \
    "s_nop 1\n\t" \
    "s_and_b32 s44, s44, 0x55555555\n\t" \
    "s_and_b32 s69, vcc_lo, 0xaaaaaaaa\n\t" \
    "s_or_b32 s44, s44, s69\n\t" \
    "s_and_b32 s45, s45, 0x55555555\n\t" \
    "s_and_b32 s69, vcc_hi, 0xaaaaaaaa\n\t" \
    "s_or_b32 s45, s45, s69\n\t" \
    "s_bitcmp1_b32 s44, 0\n\t" \
    "s_cselect_b32 s62, 8192, 0\n\t" \
    "s_bitcmp1_b32 s44, 2\n\t" \
    "s_cselect_b32 s63, 8192, 0\n\t" \
    "s_or_b32 s63, s63, 0x200000\n\t" \
    "v_cmp_lt_u32 vcc, 1320, v160\n\t" \
    "s_nop 1\n\t" \
    "s_bcnt1_i32_b64 s69, vcc\n\t" \
    "s_cmp_gt_u32 s69, 16\n\t" \
    "s_cselect_b32 s69, 1, 0\n\t" \
    "s_mul_i32 s70, s69, 160\n\t" \
    "s_add_u32 s86, s86, s70\n\t" \
    "s_mul_i32 s70, s69, 536\n\t" \
    "s_add_u32 s87, s87, s70\n\t" \
    "s_mul_i32 s70, s69, 256\n\t" \
    "s_add_u32 s88, s88, s70\n\t" \
    "global_load_dword v25, v24, s[100:101] sc1\n\t" \
    "s_waitcnt vmcnt(5) lgkmcnt(0)\n\t" \
    "s_barrier\n\t" \
    "ds_read_b64_tr_b16 a[200:201], v201 offset:49152\n\t" \
    "ds_read_b64_tr_b16 a[202:203], v201 offset:50176\n\t" \
    "ds_read_b64_tr_b16 a[204:205], v201 offset:57344\n\t" \
    "ds_read_b64_tr_b16 a[206:207], v201 offset:58368\n\t" \
    "ds_read_b64_tr_b16 v[240:241], v202 offset:49152\n\t" \
    "ds_read_b64_tr_b16 v[242:243], v202 offset:50176\n\t" \
    "ds_read_b64_tr_b16 v[244:245], v202 offset:57344\n\t" \
    "ds_read_b64_tr_b16 v[246:247], v202 offset:58368\n\t" \
    "s_waitcnt vmcnt(1) lgkmcnt(0)\n\t" \
    "s_barrier\n\t" \
    "v_add_u32 v201, 0x10000, v201\n\t" \
    "v_add_u32 v202, 0x10000, v202\n\t" \
    "ds_read_b64_tr_b16 a[208:209], v201 offset:0\n\t" \
    "ds_read_b64_tr_b16 a[210:211], v201 offset:1024\n\t" \
    "ds_read_b64_tr_b16 a[212:213], v201 offset:8192\n\t" \
    "ds_read_b64_tr_b16 a[214:215], v201 offset:9216\n\t" \
    "ds_read_b64_tr_b16 v[248:249], v202 offset:0\n\t" \
    "ds_read_b64_tr_b16 v[250:251], v202 offset:1024\n\t" \
    "ds_read_b64_tr_b16 v[252:253], v202 offset:8192\n\t" \
    "ds_read_b64_tr_b16 v[254:255], v202 offset:9216\n\t" \
    "v_add_u32 v201, 0xfffe0000, v201\n\t" \
    "v_add_u32 v202, 0xfffe0000, v202\n\t" \
    "s_waitcnt lgkmcnt(0)\n\t" \
    "s_barrier\n\t" \
    "s_cmp_lg_u32 s66, 3\n\t" \
    "s_cbranch_scc1 qk_na7_%=\n\t" \
    "v_mov_b32 v172, 5769216\n\t" \
    "v_mov_b32 v173, 1\n\t" \
    "s_mov_b64 exec, 1\n\t" \
    "global_atomic_add v174, v172, v173, s[36:37] sc0 sc1\n\t" \
    "s_mov_b64 exec, -1\n\t" \
    "v_mbcnt_lo_u32_b32 v172, -1, 0\n\t" \
    "v_and_b32 v173, 1, v172\n\t" \
    "v_lshlrev_b32 v173, 14, v173\n\t" \
    "v_bfe_u32 v175, v172, 1, 1\n\t" \
    "v_lshl_add_u32 v173, v175, 17, v173\n\t" \
    "v_bfe_u32 v175, v172, 2, 2\n\t" \
    "v_lshl_add_u32 v173, v175, 11, v173\n\t" \
    "v_bfe_u32 v175, v172, 4, 1\n\t" \
    "v_lshl_add_u32 v173, v175, 15, v173\n\t" \
    "v_add_u32 v173, s63, v173\n\t" \
    "s_waitcnt vmcnt(0)\n\t" \
    "v_readfirstlane_b32 s69, v174\n\t" \
    "s_nop 1\n\t" \
    "s_cmp_lg_u32 s69, 126\n\t" \
    "s_cbranch_scc1 qk_go8_%=\n\t" \
    "s_memrealtime s[56:57]\n\t" \
    "s_waitcnt lgkmcnt(0)\n\t" \
    "s_lshl_b32 s69, s56, 4\n\t" \
    "s_add_u32 s69, s69, 4160\n\t" \
    "s_add_u32 s70, s56, 24\n\t" \
    "s_lshr_b32 s70, s70, 4\n\t" \
    "v_lshrrev_b32 v175, 2, v172\n\t" \
    "v_add_u32 v175, s70, v175\n\t" \
    "v_and_b32 v175, 63, v175\n\t" \
    "v_lshlrev_b32 v175, 6, v175\n\t" \
    "v_and_b32 v174, 3, v172\n\t" \
    "v_lshl_add_u32 v175, v174, 12, v175\n\t" \
    "v_add_u32 v175, 5783552, v175\n\t" \
    "v_mov_b32 v172, s69\n\t" \
    "s_mov_b32 exec_hi, 0\n\t" \
    "global_store_dword v175, v172, s[36:37] sc1\n\t" \
    "global_store_dword v173, v172, s[36:37] sc1\n\t" \
    "s_mov_b32 exec_hi, -1\n\t" \
    "qk_go8_%=:\n\t" \
    "qk_na7_%=:\n\t" \
    "s_mul_i32 s70, s67, 7\n\t" \
    "s_add_u32 s69, s65, 0\n\t" \
    "s_xor_b32 s69, s69, s70\n\t" \
    "s_and_b32 s69, s69, 63\n\t" \
    "s_lshl_b32 s69, s69, 13\n\t" \
    "s_mul_i32 s70, s67, 524288\n\t" \
    "s_add_u32 s69, s69, s70\n\t" \
    "s_add_u32 s69, s69, 23068672\n\t" \
    "s_add_u32 s38, s36, s69\n\t" \
    "s_addc_u32 s39, s37, 0\n\t" \
    "v_mov_b32 v26, 0x3db504f3\n\t" \
    "v_mov_b32 v28, 0x3fb8aa3b\n\t" \
    "v_mov_b32 v27, 0x3db504f3\n\t" \
    "v_mov_b32 v29, 0x3fb8aa3b\n\t" \
    "s_bitcmp0_b32 s44, 1\n\t" \
    "s_cselect_b32 s69, 16384, 0\n\t" \
    "v_xor_b32 v1, s69, v1\n\t" \
    "v_xor_b32 v2, s69, v2\n\t" \
    "v_xor_b32 v23, s69, v23\n\t" \
    "v_xor_b32 v18, s69, v18\n\t" \
    "s_bitcmp1_b32 s44, 3\n\t" \
    "s_cselect_b32 s69, 16384, 0\n\t" \
    "v_xor_b32 v20, s69, v20\n\t" \
    "v_xor_b32 v180, 16384, v18\n\t" \
    "v_xor_b32 v181, 131072, v18\n\t" \
    "v_xor_b32 v182, 147456, v18\n\t" \
    "s_mov_b32 s48, 0xaaaaaaaa\n\t" \
    "s_mov_b32 s49, 0xaaaaaaaa\n\t" \
    "s_mov_b32 s58, 0x00ff00ff\n\t" \
    "s_mov_b32 s59, 0x00ff00ff\n\t" \
    "v_mov_b32 v31, -1\n\t" \
    "s_mov_b32 s91, -1\n\t" \
    "ds_write_b32 v12, v31 offset:0\n\t" \
    "ds_write_b32 v12, v31 offset:64\n\t" \
    "s_waitcnt vmcnt(0)\n\t" \
    "s_cmp_gt_u32 s66, 1\n\t" \
    "s_cbranch_scc1 qk_nk9_%=\n\t" \
    "v_readfirstlane_b32 s83, v25\n\t" \
    "s_nop 1\n\t" \
    "qk_WF10_%=:\n\t" \
    "s_cmp_ge_i32 s83, s65\n\t" \
    "s_cbranch_scc1 qk_WFok11_%=\n\t" \
    "global_load_dword v160, v24, s[100:101] sc1\n\t" \
    "s_waitcnt vmcnt(0)\n\t" \
    "v_readfirstlane_b32 s83, v160\n\t" \
    "s_nop 1\n\t" \
    "s_cmp_ge_i32 s83, s65\n\t" \
    "s_cbranch_scc1 qk_WFok11_%=\n\t" \
    "s_cmp_eq_u32 s68, 0\n\t" \
    "s_cbranch_scc1 qk_F12_%=\n\t" \
    "s_sub_u32 s68, s68, 1\n\t" \
    "s_sleep 2\n\t" \
    "s_branch qk_WF10_%=\n\t" \
    "qk_F12_%=:\n\t" \
    "v_mov_b32 v160, 5767424\n\t" \
    "v_mov_b32 v161, 8\n\t" \
    "global_store_dword v160, v161, s[36:37] sc1\n\t" \
    "s_waitcnt vmcnt(0)\n\t" \
    "s_branch qk_WFok11_%=\n\t" \
    "qk_WFok11_%=:\n\t" \
    "global_load_dwordx4 v[100:103], v0, s[38:39] offset:0 sc1\n\t" \
    "global_load_dwordx4 v[104:107], v0, s[38:39] offset:2048 sc1\n\t" \
    "global_load_dwordx4 v[108:111], v15, s[38:39] offset:0 sc1\n\t" \
    "global_load_dwordx4 v[112:115], v15, s[38:39] offset:2048 sc1\n\t" \
    "global_load_dwordx4 v[116:119], v0, s[38:39] offset:1024 sc1\n\t" \
    "global_load_dwordx4 v[120:123], v0, s[38:39] offset:3072 sc1\n\t" \
    "global_load_dwordx4 v[124:127], v15, s[38:39] offset:1024 sc1\n\t" \
    "global_load_dwordx4 v[128:131], v15, s[38:39] offset:3072 sc1\n\t" \
    "qk_nk9_%=:\n\t" \
    "v_mov_b32 v33, v21\n\t" \
    "v_readlane_b32 s69, v21, 63\n\t" \
    "s_nop 1\n\t" \
    "v_mov_b32 v21, s69\n\t" \
    "s_mov_b32 s79, s74\n\t" \
    "s_mov_b32 s80, s74\n\t" \
    "s_cmp_eq_u32 s66, 1\n\t" \
    "s_cbranch_scc1 qk_loop14_%=\n\t" \
    "s_cmp_eq_u32 s66, 2\n\t" \
    "s_cbranch_scc1 qk_loop15_%=\n\t" \
    "s_cmp_eq_u32 s66, 3\n\t" \
    "s_cbranch_scc1 qk_loop16_%=\n\t" \
    "qk_loop13_%=:\n\t" \
    "v_mov_b32 v72, 0\n\t" \
    "v_mov_b32 v73, 0\n\t" \
    "v_mov_b32 v74, 0\n\t" \
    "v_mov_b32 v75, 0\n\t" \
    "v_mov_b32 v76, 0\n\t" \
    "v_mov_b32 v77, 0\n\t" \
    "v_mov_b32 v78, 0\n\t" \
    "v_mov_b32 v79, 0\n\t" \
    "v_mov_b32 v80, 0\n\t" \
    "v_mov_b32 v81, 0\n\t" \
    "v_mov_b32 v82, 0\n\t" \
    "v_mov_b32 v83, 0\n\t" \
    "v_mov_b32 v84, 0\n\t" \
    "v_mov_b32 v85, 0\n\t" \
    "v_mov_b32 v86, 0\n\t" \
    "v_mov_b32 v87, 0\n\t" \
    "v_mov_b32 v88, 0\n\t" \
    "v_mov_b32 v89, 0\n\t" \
    "v_mov_b32 v90, 0\n\t" \
    "v_mov_b32 v91, 0\n\t" \
    "v_mov_b32 v92, 0\n\t" \
    "v_mov_b32 v93, 0\n\t" \
    "v_mov_b32 v94, 0\n\t" \
    "v_mov_b32 v95, 0\n\t" \
    "v_mov_b32 v96, 0\n\t" \
    "v_mov_b32 v97, 0\n\t" \
    "v_mov_b32 v98, 0\n\t" \
    "v_mov_b32 v99, 0\n\t" \
    "s_add_u32 s69, s65, 0\n\t" \
    "s_and_b32 s69, s69, 7\n\t" \
    "s_lshl_b32 s69, s69, 18\n\t" \
    "s_add_u32 s69, s69, s62\n\t" \
    "s_add_u32 s46, s36, s69\n\t" \
    "s_addc_u32 s47, s37, 0\n\t" \
    "s_bitcmp0_b32 s65, 0\n\t" \
    "s_cselect_b32 s69, 16384, 0\n\t" \
    "v_xor_b32 v1, s69, v1\n\t" \
    "v_xor_b32 v2, s69, v2\n\t" \
    "v_xor_b32 v23, s69, v23\n\t" \
    "v_xor_b32 v18, s69, v18\n\t" \
    "v_xor_b32 v180, s69, v180\n\t" \
    "v_xor_b32 v181, s69, v181\n\t" \
    "v_xor_b32 v182, s69, v182\n\t" \
    "s_sub_u32 s70, s79, s74\n\t" \
    "s_max_i32 s70, s70, 0\n\t" \
    "s_min_i32 s70, s70, 16000\n\t" \
    "s_lshl_b32 s70, s70, 1\n\t" \
    "s_mul_hi_u32 s70, s70, s77\n\t" \
    "s_add_u32 s70, s70, s73\n\t" \
    ".p2align 6\n\t" \
    "s_nop 0\n\t" \
    "s_nop 0\n\t" \
    "s_nop 0\n\t" \
    "s_nop 0\n\t" \
    "s_nop 0\n\t" \
    "s_nop 0\n\t" \
    "qk_go18_%=:\n\t" \
    "s_getpc_b64 s[30:31]\n\t" \
    "qk_Wp20_%=:\n\t" \
    "s_add_u32 s30, s30, qk_We21_%=-qk_Wp20_%=\n\t" \
    "s_addc_u32 s31, s31, 0\n\t" \
    "s_sub_u32 s70, s70, 96\n\t" \
    "qk_W19_%=:\n\t" \
    "s_memtime s[56:57]\n\t" \
    "s_waitcnt lgkmcnt(0)\n\t" \
    "s_sub_u32 s69, s70, s56\n\t" \
    "s_cmp_gt_i32 s69, 80\n\t" \
    "s_cbranch_scc1 qk_W19_%=\n\t" \
    "s_max_i32 s69, s69, 0\n\t" \
    "s_and_b32 s69, s69, -4\n\t" \
    "s_sub_u32 s30, s30, s69\n\t" \
    "s_subb_u32 s31, s31, 0\n\t" \
    "s_setpc_b64 s[30:31]\n\t" \
    "s_nop 0\n\t" \
    "s_nop 0\n\t" \
    "s_nop 0\n\t" \
    "s_nop 0\n\t" \
    "s_nop 0\n\t" \
    "s_nop 0\n\t" \
    "s_nop 0\n\t" \
    "s_nop 0\n\t" \
    "s_nop 0\n\t" \
    "s_nop 0\n\t" \
    "s_nop 0\n\t" \
    "s_nop 0\n\t" \
    "s_nop 0\n\t" \
    "s_nop 0\n\t" \
    "s_nop 0\n\t" \
    "s_nop 0\n\t" \
    "s_nop 0\n\t" \
    "s_nop 0\n\t" \
    "s_nop 0\n\t" \
    "s_nop 0\n\t" \
    "qk_We21_%=:\n\t" \
    "qk_G22_%=:\n\t" \
    "global_load_dwordx4 v[36:39], v1, s[46:47] sc1\n\t" \
    "global_load_dword v161, v2, s[46:47] sc1\n\t" \
    "global_load_dwordx4 v[40:43], v23, s[46:47] sc1\n\t" \
    "s_waitcnt vmcnt(2)\n\t" \
    "qk_Garr27_%=:\n\t" \
    "ds_write_b128 v3, v[36:39]\n\t" \
    "v_smfmac_f32_16x16x64_bf16 v[72:75], v[36:39], a[0:7], v10\n\t" \
    "s_waitcnt vmcnt(1)\n\t" \
    "ds_write_b32 v22, v161 offset:19456\n\t" \
    "v_smfmac_f32_16x16x64_bf16 v[76:79], v[36:39], a[72:79], v10\n\t" \
    "s_waitcnt vmcnt(0)\n\t" \
    "ds_write_b128 v3, v[40:43] offset:1024\n\t" \
    "v_max3_u32 v160, v36, v40, v161\n\t" \
    "v_cmp_eq_u32 vcc, -1, v160\n\t" \
    "v_smfmac_f32_16x16x64_bf16 v[80:83], v[36:39], a[144:151], v10\n\t" \
    "v_smfmac_f32_16x16x64_bf16 v[84:87], v[36:39], a[216:223], v10\n\t" \
    "v_smfmac_f32_16x16x64_bf16 v[72:75], v[40:43], a[8:15], v10\n\t" \
    "s_cbranch_vccnz qk_Gbad23_%=\n\t" \
    "qk_Gc24_%=:\n\t" \
    "s_waitcnt lgkmcnt(0)\n\t" \
    "s_barrier\n\t" \
    "ds_read_b128 v[48:51], v4\n\t" \
    "ds_read_b128 v[52:55], v5\n\t" \
    "v_smfmac_f32_16x16x64_bf16 v[76:79], v[40:43], a[80:87], v10\n\t" \
    "ds_read_b128 v[56:59], v6\n\t" \
    "ds_read_b128 v[60:63], v7\n\t" \
    "v_smfmac_f32_16x16x64_bf16 v[80:83], v[40:43], a[152:159], v10\n\t" \
    "ds_read_b128 v[64:67], v8\n\t" \
    "v_smfmac_f32_16x16x64_bf16 v[84:87], v[40:43], a[224:231], v10\n\t" \
    "ds_read_b128 v[68:71], v9\n\t" \
    "s_waitcnt lgkmcnt(5)\n\t" \
    "v_smfmac_f32_16x16x64_bf16 v[72:75], v[48:51], a[16:23], v10\n\t" \
    "ds_read_b128 v[44:47], v0 offset:19456\n\t" \
    "s_waitcnt lgkmcnt(5)\n\t" \
    "v_smfmac_f32_16x16x64_bf16 v[72:75], v[52:55], a[24:31], v10\n\t" \
    "s_waitcnt lgkmcnt(4)\n\t" \
    "v_smfmac_f32_16x16x64_bf16 v[72:75], v[56:59], a[32:39], v10\n\t" \
    "s_waitcnt lgkmcnt(3)\n\t" \
    "v_smfmac_f32_16x16x64_bf16 v[72:75], v[60:63], a[40:47], v10\n\t" \
    "s_waitcnt lgkmcnt(2)\n\t" \
    "v_smfmac_f32_16x16x64_bf16 v[72:75], v[64:67], a[48:55], v10\n\t" \
    "s_waitcnt lgkmcnt(1)\n\t" \
    "v_smfmac_f32_16x16x64_bf16 v[72:75], v[68:71], a[56:63], v10\n\t" \
    "s_waitcnt lgkmcnt(0)\n\t" \
    "v_smfmac_f32_16x16x64_bf16 v[72:75], v[44:47], a[64:71], v10\n\t" \
    "s_nop 2\n\t" \
    "v_smfmac_f32_16x16x64_bf16 v[76:79], v[48:51], a[88:95], v10\n\t" \
    "s_nop 2\n\t" \
    "v_smfmac_f32_16x16x64_bf16 v[76:79], v[52:55], a[96:103], v10\n\t" \
    "v_add_f32 v72, v72, v74\n\t" \
    "v_smfmac_f32_16x16x64_bf16 v[76:79], v[56:59], a[104:111], v10\n\t" \
    "v_add_f32 v73, v73, v75\n\t" \
    "v_smfmac_f32_16x16x64_bf16 v[76:79], v[60:63], a[112:119], v10\n\t" \
    "v_cvt_pk_bf16_f32 v156, v72, v73\n\t" \
    "v_smfmac_f32_16x16x64_bf16 v[76:79], v[64:67], a[120:127], v10\n\t" \
    "ds_write_b32 v12, v156 offset:0\n\t" \
    "v_smfmac_f32_16x16x64_bf16 v[76:79], v[68:71], a[128:135], v10\n\t" \
    "v_smfmac_f32_16x16x64_bf16 v[76:79], v[44:47], a[136:143], v10\n\t" \
    "s_nop 2\n\t" \
    "v_smfmac_f32_16x16x64_bf16 v[80:83], v[48:51], a[160:167], v10\n\t" \
    "s_nop 2\n\t" \
    "v_smfmac_f32_16x16x64_bf16 v[80:83], v[52:55], a[168:175], v10\n\t" \
    "v_add_f32 v76, v76, v78\n\t" \
    "v_smfmac_f32_16x16x64_bf16 v[80:83], v[56:59], a[176:183], v10\n\t" \
    "v_add_f32 v77, v77, v79\n\t" \
    "v_smfmac_f32_16x16x64_bf16 v[80:83], v[60:63], a[184:191], v10\n\t" \
    "v_cvt_pk_bf16_f32 v157, v76, v77\n\t" \
    "v_smfmac_f32_16x16x64_bf16 v[80:83], v[64:67], a[192:199], v10\n\t" \
    "ds_write_b32 v12, v157 offset:64\n\t" \
    "v_smfmac_f32_16x16x64_bf16 v[80:83], v[68:71], a[200:207], v10\n\t" \
    "v_smfmac_f32_16x16x64_bf16 v[80:83], v[44:47], a[208:215], v10\n\t" \
    "s_nop 2\n\t" \
    "v_smfmac_f32_16x16x64_bf16 v[84:87], v[48:51], a[232:239], v10\n\t" \
    "s_nop 2\n\t" \
    "v_smfmac_f32_16x16x64_bf16 v[84:87], v[52:55], a[240:247], v10\n\t" \
    "v_add_f32 v80, v80, v82\n\t" \
    "v_smfmac_f32_16x16x64_bf16 v[84:87], v[56:59], a[248:255], v10\n\t" \
    "ds_read_b64_tr_b16 v[132:133], v13 offset:0\n\t" \
    "v_add_f32 v81, v81, v83\n\t" \
    "v_smfmac_f32_16x16x64_bf16 v[84:87], v[60:63], v[224:231], v10\n\t" \
    "ds_read_b64_tr_b16 v[134:135], v13 offset:128\n\t" \
    "v_cvt_pk_bf16_f32 v158, v80, v81\n\t" \
    "v_smfmac_f32_16x16x64_bf16 v[84:87], v[64:67], v[232:239], v10\n\t" \
    "ds_write_b32 v16, v158 offset:0\n\t" \
    "v_smfmac_f32_16x16x64_bf16 v[84:87], v[68:71], v[240:247], v10\n\t" \
    "ds_read_b64_tr_b16 v[136:137], v13 offset:1024\n\t" \
    "ds_read_b64_tr_b16 v[138:139], v13 offset:1152\n\t" \
    "v_smfmac_f32_16x16x64_bf16 v[84:87], v[44:47], v[248:255], v10\n\t" \
    "s_waitcnt lgkmcnt(3)\n\t" \
    "v_smfmac_f32_16x16x64_bf16 v[88:91], v[132:135], v[100:107], v10\n\t" \
    "s_nop 4\n\t" \
    "v_cmp_u_f32 vcc, v72, v73\n\t" \
    "v_pk_add_f32 v[84:85], v[84:85], v[86:87]\n\t" \
    "v_cvt_pk_bf16_f32 v159, v84, v85\n\t" \
    "ds_write_b32 v16, v159 offset:256\n\t" \
    "v_smfmac_f32_16x16x64_bf16 v[92:95], v[132:135], v[116:123], v10\n\t" \
    "s_waitcnt lgkmcnt(1)\n\t" \
    "v_smfmac_f32_16x16x64_bf16 v[88:91], v[136:139], v[108:115], v10\n\t" \
    "s_cbranch_vccnz qk_Gslow25_%=\n\t" \
    "qk_Gback26_%=:\n\t" \
    "v_smfmac_f32_16x16x64_bf16 v[92:95], v[136:139], v[124:131], v10\n\t" \
    "s_waitcnt lgkmcnt(0)\n\t" \
    "s_barrier\n\t" \
    "ds_read_b64_tr_b16 v[140:141], v17 offset:0\n\t" \
    "ds_read_b64_tr_b16 v[142:143], v17 offset:256\n\t" \
    "ds_read_b64_tr_b16 v[144:145], v17 offset:512\n\t" \
    "ds_read_b64_tr_b16 v[146:147], v17 offset:768\n\t" \
    "ds_read_b64_tr_b16 v[148:149], v17 offset:1024\n\t" \
    "ds_read_b64_tr_b16 v[150:151], v17 offset:1280\n\t" \
    "ds_read_b64_tr_b16 v[152:153], v17 offset:1536\n\t" \
    "ds_read_b64_tr_b16 v[154:155], v17 offset:1792\n\t" \
    "qk_qb29_%=:\n\t" \
    "v_add_f32 v168, v88, v90\n\t" \
    "v_add_f32 v169, v92, v94\n\t" \
    "v_cmp_u_f32 vcc, v168, v169\n\t" \
    "v_cvt_pk_bf16_f32 v168, 0, v168\n\t" \
    "v_cvt_pk_bf16_f32 v169, 0, v169\n\t" \
    "s_waitcnt lgkmcnt(4)\n\t" \
    "v_smfmac_f32_16x16x64_bf16 v[96:99], v[132:135], v[140:147], v10\n\t" \
    "s_waitcnt lgkmcnt(0)\n\t" \
    "v_smfmac_f32_16x16x64_bf16 v[96:99], v[136:139], v[148:155], v10\n\t" \
    "s_cbranch_vccnz qk_qs28_%=\n\t" \
    "qk_qb230_%=:\n\t" \
    "v_mul_f32 v168, v168, v26\n\t" \
    "v_mul_f32 v169, v169, v26\n\t" \
    "v_cvt_pk_bf16_f32 v168, 0, v168\n\t" \
    "v_cvt_pk_bf16_f32 v169, 0, v169\n\t" \
    "v_max_f32 v178, v168, v169\n\t" \
    "v_mul_f32 v168, v168, v28\n\t" \
    "v_mul_f32 v169, v169, v28\n\t" \
    "v_max_f32_dpp v178, v178, v178 row_mirror row_mask:0xf bank_mask:0xf\n\t" \
    "v_add_f32 v172, v96, v98\n\t" \
    "v_cvt_pk_bf16_f32 v172, 0, v172\n\t" \
    "v_mul_f32 v172, v172, v26\n\t" \
    "v_cvt_pk_bf16_f32 v172, 0, v172\n\t" \
    "v_max_f32 v173, v178, v172\n\t" \
    "v_mul_f32 v172, v172, v28\n\t" \
    "s_nop 0\n\t" \
    "v_max_f32_dpp v173, v173, v173 quad_perm:[1,0,3,2] row_mask:0xf bank_mask:0xf\n\t" \
    "s_nop 1\n\t" \
    "v_max_f32_dpp v173, v173, v173 quad_perm:[2,3,0,1] row_mask:0xf bank_mask:0xf\n\t" \
    "s_nop 1\n\t" \
    "v_max_f32_dpp v173, v173, v173 row_half_mirror row_mask:0xf bank_mask:0xf\n\t" \
    "v_fma_f32 v172, -v173, v28, v172\n\t" \
    "v_fma_f32 v168, -v173, v28, v168\n\t" \
    "v_fma_f32 v169, -v173, v28, v169\n\t" \
    "v_exp_f32 v172, v172\n\t" \
    "v_exp_f32 v168, v168\n\t" \
    "v_exp_f32 v169, v169\n\t" \
    "v_fma_f32 v173, v172, 0.5, v168\n\t" \
    "v_add_f32 v173, v173, v169\n\t" \
    "v_mov_b32_dpp v174, v172 quad_perm:[0,0,2,2] row_mask:0xf bank_mask:0xf\n\t" \
    "v_mov_b32_dpp v175, v172 quad_perm:[1,1,3,3] row_mask:0xf bank_mask:0xf\n\t" \
    "v_add_f32_dpp v173, v173, v173 quad_perm:[1,0,3,2] row_mask:0xf bank_mask:0xf\n\t" \
    "v_mov_b32_dpp v176, v168 quad_perm:[1,0,3,2] row_mask:0xf bank_mask:0xf\n\t" \
    "v_mov_b32_dpp v177, v169 quad_perm:[1,0,3,2] row_mask:0xf bank_mask:0xf\n\t" \
    "v_add_f32_dpp v173, v173, v173 quad_perm:[2,3,0,1] row_mask:0xf bank_mask:0xf\n\t" \
    "v_cndmask_b32 v168, v168, v177, s[48:49]\n\t" \
    "v_cndmask_b32 v169, v176, v169, s[48:49]\n\t" \
    "v_add_f32_dpp v173, v173, v173 row_half_mirror row_mask:0xf bank_mask:0xf\n\t" \
    "s_nop 1\n\t" \
    "v_add_f32_dpp v173, v173, v173 row_mirror row_mask:0xf bank_mask:0xf\n\t" \
    "v_rcp_f32 v173, v173\n\t" \
    "s_nop 0\n\t" \
    "v_pk_mul_f32 v[168:169], v[168:169], v[172:173] op_sel:[0,1] op_sel_hi:[1,1]\n\t" \
    "v_cvt_pk_bf16_f32 v176, v168, v169\n\t" \
    "global_store_dword v18, v176, s[46:47] sc1\n\t" \
    "global_store_dword v180, v176, s[46:47] sc1\n\t" \
    "global_store_dword v181, v176, s[46:47] sc1\n\t" \
    "global_store_dword v182, v176, s[46:47] sc1\n\t" \
    "v_pk_mul_f32 v[174:175], v[174:175], v[172:173] op_sel:[0,1] op_sel_hi:[1,1]\n\t" \
    "v_cvt_pk_bf16_f32 v177, v174, v175\n\t" \
    "global_store_dword v19, v177, s[46:47] sc1\n\t" \
    "s_cmp_eq_u32 s65, 63\n\t" \
    "s_cbranch_scc1 qk_last17_%=\n\t" \
    "v_readfirstlane_b32 s69, v25\n\t" \
    "s_add_u32 s65, s65, 1\n\t" \
    "s_max_i32 s83, s83, s69\n\t" \
    "s_cmp_gt_u32 s65, 7\n\t" \
    "s_cselect_b32 s83, 99, s83\n\t" \
    "v_mov_b32 v72, 0\n\t" \
    "v_mov_b32 v73, 0\n\t" \
    "v_mov_b32 v74, 0\n\t" \
    "v_mov_b32 v75, 0\n\t" \
    "v_mov_b32 v76, 0\n\t" \
    "v_mov_b32 v77, 0\n\t" \
    "v_mov_b32 v78, 0\n\t" \
    "v_mov_b32 v79, 0\n\t" \
    "v_mov_b32 v80, 0\n\t" \
    "v_mov_b32 v81, 0\n\t" \
    "v_mov_b32 v82, 0\n\t" \
    "v_mov_b32 v83, 0\n\t" \
    "v_mov_b32 v84, 0\n\t" \
    "v_mov_b32 v85, 0\n\t" \
    "v_mov_b32 v86, 0\n\t" \
    "v_mov_b32 v87, 0\n\t" \
    "v_mov_b32 v88, 0\n\t" \
    "v_mov_b32 v89, 0\n\t" \
    "v_mov_b32 v90, 0\n\t" \
    "v_mov_b32 v91, 0\n\t" \
    "v_mov_b32 v92, 0\n\t" \
    "v_mov_b32 v93, 0\n\t" \
    "v_mov_b32 v94, 0\n\t" \
    "v_mov_b32 v95, 0\n\t" \
    "v_mov_b32 v96, 0\n\t" \
    "v_mov_b32 v97, 0\n\t" \
    "v_mov_b32 v98, 0\n\t" \
    "v_mov_b32 v99, 0\n\t" \
    "s_add_u32 s69, s65, 0\n\t" \
    "s_and_b32 s69, s69, 7\n\t" \
    "s_lshl_b32 s69, s69, 18\n\t" \
    "s_add_u32 s69, s69, s62\n\t" \
    "s_add_u32 s46, s36, s69\n\t" \
    "s_addc_u32 s47, s37, 0\n\t" \
    "s_bitcmp0_b32 s65, 0\n\t" \
    "s_cselect_b32 s69, 16384, 0\n\t" \
    "v_xor_b32 v1, s69, v1\n\t" \
    "v_xor_b32 v2, s69, v2\n\t" \
    "v_xor_b32 v23, s69, v23\n\t" \
    "v_xor_b32 v18, s69, v18\n\t" \
    "v_xor_b32 v180, s69, v180\n\t" \
    "v_xor_b32 v181, s69, v181\n\t" \
    "v_xor_b32 v182, s69, v182\n\t" \
    "s_mul_i32 s70, s67, 7\n\t" \
    "s_add_u32 s69, s65, 0\n\t" \
    "s_xor_b32 s69, s69, s70\n\t" \
    "s_and_b32 s69, s69, 63\n\t" \
    "s_lshl_b32 s69, s69, 13\n\t" \
    "s_mul_i32 s70, s67, 524288\n\t" \
    "s_add_u32 s69, s69, s70\n\t" \
    "s_add_u32 s69, s69, 23068672\n\t" \
    "s_add_u32 s38, s36, s69\n\t" \
    "s_addc_u32 s39, s37, 0\n\t" \
    "qk_WF31_%=:\n\t" \
    "s_cmp_ge_i32 s83, s65\n\t" \
    "s_cbranch_scc1 qk_WFok32_%=\n\t" \
    "global_load_dword v160, v24, s[100:101] sc1\n\t" \
    "s_waitcnt vmcnt(0)\n\t" \
    "v_readfirstlane_b32 s83, v160\n\t" \
    "s_nop 1\n\t" \
    "s_cmp_ge_i32 s83, s65\n\t" \
    "s_cbranch_scc1 qk_WFok32_%=\n\t" \
    "s_cmp_eq_u32 s68, 0\n\t" \
    "s_cbranch_scc1 qk_F33_%=\n\t" \
    "s_sub_u32 s68, s68, 1\n\t" \
    "s_sleep 2\n\t" \
    "s_branch qk_WF31_%=\n\t" \
    "qk_F33_%=:\n\t" \
    "v_mov_b32 v160, 5767424\n\t" \
    "v_mov_b32 v161, 9\n\t" \
    "global_store_dword v160, v161, s[36:37] sc1\n\t" \
    "s_waitcnt vmcnt(0)\n\t" \
    "s_branch qk_WFok32_%=\n\t" \
    "qk_WFok32_%=:\n\t" \
    "global_load_dwordx4 v[100:103], v0, s[38:39] offset:0 sc1\n\t" \
    "global_load_dwordx4 v[104:107], v0, s[38:39] offset:2048 sc1\n\t" \
    "global_load_dwordx4 v[108:111], v15, s[38:39] offset:0 sc1\n\t" \
    "global_load_dwordx4 v[112:115], v15, s[38:39] offset:2048 sc1\n\t" \
    "global_load_dwordx4 v[116:119], v0, s[38:39] offset:1024 sc1\n\t" \
    "global_load_dwordx4 v[120:123], v0, s[38:39] offset:3072 sc1\n\t" \
    "global_load_dwordx4 v[124:127], v15, s[38:39] offset:1024 sc1\n\t" \
    "global_load_dwordx4 v[128:131], v15, s[38:39] offset:3072 sc1\n\t" \
    "s_cmp_gt_i32 s83, s65\n\t" \
    "s_cbranch_scc1 qk_nf34_%=\n\t" \
    "global_load_dword v25, v24, s[100:101] sc1\n\t" \
    "qk_nf34_%=:\n\t" \
    "s_barrier\n\t" \
    "ds_read_b32 v168, v11 offset:20480\n\t" \
    "s_waitcnt lgkmcnt(0)\n\t" \
    "v_readfirstlane_b32 s70, v168\n\t" \
    "ds_write_b32 v12, v31 offset:0\n\t" \
    "ds_write_b32 v12, v31 offset:64\n\t" \
    "s_branch qk_go18_%=\n\t" \
    "qk_Gbad23_%=:\n\t" \
    "s_waitcnt vmcnt(0) lgkmcnt(0)\n\t" \
    "s_nop 7\n\t" \
    "v_mov_b32 v72, 0\n\t" \
    "v_mov_b32 v73, 0\n\t" \
    "v_mov_b32 v74, 0\n\t" \
    "v_mov_b32 v75, 0\n\t" \
    "v_mov_b32 v76, 0\n\t" \
    "v_mov_b32 v77, 0\n\t" \
    "v_mov_b32 v78, 0\n\t" \
    "v_mov_b32 v79, 0\n\t" \
    "v_mov_b32 v80, 0\n\t" \
    "v_mov_b32 v81, 0\n\t" \
    "v_mov_b32 v82, 0\n\t" \
    "v_mov_b32 v83, 0\n\t" \
    "v_mov_b32 v84, 0\n\t" \
    "v_mov_b32 v85, 0\n\t" \
    "v_mov_b32 v86, 0\n\t" \
    "v_mov_b32 v87, 0\n\t" \
    "s_cmp_eq_u32 s68, 0\n\t" \
    "s_cbranch_scc1 qk_F35_%=\n\t" \
    "s_sub_u32 s68, s68, 1\n\t" \
    "s_branch qk_G22_%=\n\t" \
    "qk_F35_%=:\n\t" \
    "v_mov_b32 v160, 5767424\n\t" \
    "v_mov_b32 v161, 1\n\t" \
    "global_store_dword v160, v161, s[36:37] sc1\n\t" \
    "s_waitcnt vmcnt(0)\n\t" \
    "s_branch qk_Gc24_%=\n\t" \
    "qk_Gslow25_%=:\n\t" \
    "v_max_u32 v160, v36, v37\n\t" \
    "v_max3_u32 v160, v160, v38, v39\n\t" \
    "v_max3_u32 v160, v160, v40, v41\n\t" \
    "v_max3_u32 v160, v160, v42, v43\n\t" \
    "v_max3_u32 v160, v160, v44, v45\n\t" \
    "v_max3_u32 v160, v160, v46, v47\n\t" \
    "v_max3_u32 v160, v160, v48, v49\n\t" \
    "v_max3_u32 v160, v160, v50, v51\n\t" \
    "v_max3_u32 v160, v160, v52, v53\n\t" \
    "v_max3_u32 v160, v160, v54, v55\n\t" \
    "v_max3_u32 v160, v160, v56, v57\n\t" \
    "v_max3_u32 v160, v160, v58, v59\n\t" \
    "v_max3_u32 v160, v160, v60, v61\n\t" \
    "v_max3_u32 v160, v160, v62, v63\n\t" \
    "v_max3_u32 v160, v160, v64, v65\n\t" \
    "v_max3_u32 v160, v160, v66, v67\n\t" \
    "v_max3_u32 v160, v160, v68, v69\n\t" \
    "v_max3_u32 v160, v160, v70, v71\n\t" \
    "v_cmp_eq_u32 vcc, -1, v160\n\t" \
    "s_cbranch_vccz qk_Gback26_%=\n\t" \
    "s_waitcnt lgkmcnt(0)\n\t" \
    "v_sub_u32 v160, v1, v3\n\t" \
    "s_nop 0\n\t" \
    "v_readfirstlane_b32 s69, v160\n\t" \
    "v_add_u32 v163, 36864, v0\n\t" \
    "s_nop 7\n\t" \
    "s_add_u32 s30, s46, s69\n\t" \
    "s_addc_u32 s31, s47, 0\n\t" \
    "qk_Gag36_%=:\n\t" \
    "global_load_dwordx4 v[36:39], v1, s[46:47] sc1\n\t" \
    "global_load_dwordx4 v[40:43], v23, s[46:47] sc1\n\t" \
    "global_load_dwordx4 v[44:47], v163, s[30:31] sc1\n\t" \
    "global_load_dwordx4 v[48:51], v4, s[30:31] sc1\n\t" \
    "global_load_dwordx4 v[52:55], v5, s[30:31] sc1\n\t" \
    "global_load_dwordx4 v[56:59], v6, s[30:31] sc1\n\t" \
    "global_load_dwordx4 v[60:63], v7, s[30:31] sc1\n\t" \
    "global_load_dwordx4 v[64:67], v8, s[30:31] sc1\n\t" \
    "global_load_dwordx4 v[68:71], v9, s[30:31] sc1\n\t" \
    "s_waitcnt vmcnt(0)\n\t" \
    "v_max_u32 v160, v36, v37\n\t" \
    "v_max3_u32 v160, v160, v38, v39\n\t" \
    "v_max3_u32 v160, v160, v40, v41\n\t" \
    "v_max3_u32 v160, v160, v42, v43\n\t" \
    "v_max3_u32 v160, v160, v44, v45\n\t" \
    "v_max3_u32 v160, v160, v46, v47\n\t" \
    "v_max3_u32 v160, v160, v48, v49\n\t" \
    "v_max3_u32 v160, v160, v50, v51\n\t" \
    "v_max3_u32 v160, v160, v52, v53\n\t" \
    "v_max3_u32 v160, v160, v54, v55\n\t" \
    "v_max3_u32 v160, v160, v56, v57\n\t" \
    "v_max3_u32 v160, v160, v58, v59\n\t" \
    "v_max3_u32 v160, v160, v60, v61\n\t" \
    "v_max3_u32 v160, v160, v62, v63\n\t" \
    "v_max3_u32 v160, v160, v64, v65\n\t" \
    "v_max3_u32 v160, v160, v66, v67\n\t" \
    "v_max3_u32 v160, v160, v68, v69\n\t" \
    "v_max3_u32 v160, v160, v70, v71\n\t" \
    "v_cmp_eq_u32 vcc, -1, v160\n\t" \
    "s_cbranch_vccz qk_Gredo37_%=\n\t" \
    "s_cmp_eq_u32 s68, 0\n\t" \
    "s_cbranch_scc1 qk_F38_%=\n\t" \
    "s_sub_u32 s68, s68, 1\n\t" \
    "s_sleep 1\n\t" \
    "s_branch qk_Gag36_%=\n\t" \
    "qk_F38_%=:\n\t" \
    "v_mov_b32 v160, 5767424\n\t" \
    "v_mov_b32 v161, 2\n\t" \
    "global_store_dword v160, v161, s[36:37] sc1\n\t" \
    "s_waitcnt vmcnt(0)\n\t" \
    "s_branch qk_Gredo37_%=\n\t" \
    "qk_Gredo37_%=:\n\t" \
    "v_mov_b32 v72, 0\n\t" \
    "v_mov_b32 v73, 0\n\t" \
    "v_mov_b32 v74, 0\n\t" \
    "v_mov_b32 v75, 0\n\t" \
    "v_mov_b32 v76, 0\n\t" \
    "v_mov_b32 v77, 0\n\t" \
    "v_mov_b32 v78, 0\n\t" \
    "v_mov_b32 v79, 0\n\t" \
    "v_mov_b32 v80, 0\n\t" \
    "v_mov_b32 v81, 0\n\t" \
    "v_mov_b32 v82, 0\n\t" \
    "v_mov_b32 v83, 0\n\t" \
    "v_mov_b32 v84, 0\n\t" \
    "v_mov_b32 v85, 0\n\t" \
    "v_mov_b32 v86, 0\n\t" \
    "v_mov_b32 v87, 0\n\t" \
    "v_smfmac_f32_16x16x64_bf16 v[72:75], v[36:39], a[0:7], v10\n\t" \
    "v_smfmac_f32_16x16x64_bf16 v[72:75], v[40:43], a[8:15], v10\n\t" \
    "v_smfmac_f32_16x16x64_bf16 v[72:75], v[48:51], a[16:23], v10\n\t" \
    "v_smfmac_f32_16x16x64_bf16 v[72:75], v[52:55], a[24:31], v10\n\t" \
    "v_smfmac_f32_16x16x64_bf16 v[72:75], v[56:59], a[32:39], v10\n\t" \
    "v_smfmac_f32_16x16x64_bf16 v[72:75], v[60:63], a[40:47], v10\n\t" \
    "v_smfmac_f32_16x16x64_bf16 v[72:75], v[64:67], a[48:55], v10\n\t" \
    "v_smfmac_f32_16x16x64_bf16 v[72:75], v[68:71], a[56:63], v10\n\t" \
    "v_smfmac_f32_16x16x64_bf16 v[72:75], v[44:47], a[64:71], v10\n\t" \
    "v_smfmac_f32_16x16x64_bf16 v[76:79], v[36:39], a[72:79], v10\n\t" \
    "v_smfmac_f32_16x16x64_bf16 v[76:79], v[40:43], a[80:87], v10\n\t" \
    "v_smfmac_f32_16x16x64_bf16 v[76:79], v[48:51], a[88:95], v10\n\t" \
    "v_smfmac_f32_16x16x64_bf16 v[76:79], v[52:55], a[96:103], v10\n\t" \
    "v_smfmac_f32_16x16x64_bf16 v[76:79], v[56:59], a[104:111], v10\n\t" \
    "v_smfmac_f32_16x16x64_bf16 v[76:79], v[60:63], a[112:119], v10\n\t" \
    "v_smfmac_f32_16x16x64_bf16 v[76:79], v[64:67], a[120:127], v10\n\t" \
    "v_smfmac_f32_16x16x64_bf16 v[76:79], v[68:71], a[128:135], v10\n\t" \
    "v_smfmac_f32_16x16x64_bf16 v[76:79], v[44:47], a[136:143], v10\n\t" \
    "v_smfmac_f32_16x16x64_bf16 v[80:83], v[36:39], a[144:151], v10\n\t" \
    "v_smfmac_f32_16x16x64_bf16 v[80:83], v[40:43], a[152:159], v10\n\t" \
    "v_smfmac_f32_16x16x64_bf16 v[80:83], v[48:51], a[160:167], v10\n\t" \
    "v_smfmac_f32_16x16x64_bf16 v[80:83], v[52:55], a[168:175], v10\n\t" \
    "v_smfmac_f32_16x16x64_bf16 v[80:83], v[56:59], a[176:183], v10\n\t" \
    "v_smfmac_f32_16x16x64_bf16 v[80:83], v[60:63], a[184:191], v10\n\t" \
    "v_smfmac_f32_16x16x64_bf16 v[80:83], v[64:67], a[192:199], v10\n\t" \
    "v_smfmac_f32_16x16x64_bf16 v[80:83], v[68:71], a[200:207], v10\n\t" \
    "v_smfmac_f32_16x16x64_bf16 v[80:83], v[44:47], a[208:215], v10\n\t" \
    "v_smfmac_f32_16x16x64_bf16 v[84:87], v[36:39], a[216:223], v10\n\t" \
    "v_smfmac_f32_16x16x64_bf16 v[84:87], v[40:43], a[224:231], v10\n\t" \
    "v_smfmac_f32_16x16x64_bf16 v[84:87], v[48:51], a[232:239], v10\n\t" \
    "v_smfmac_f32_16x16x64_bf16 v[84:87], v[52:55], a[240:247], v10\n\t" \
    "v_smfmac_f32_16x16x64_bf16 v[84:87], v[56:59], a[248:255], v10\n\t" \
    "v_smfmac_f32_16x16x64_bf16 v[84:87], v[60:63], v[224:231], v10\n\t" \
    "v_smfmac_f32_16x16x64_bf16 v[84:87], v[64:67], v[232:239], v10\n\t" \
    "v_smfmac_f32_16x16x64_bf16 v[84:87], v[68:71], v[240:247], v10\n\t" \
    "v_smfmac_f32_16x16x64_bf16 v[84:87], v[44:47], v[248:255], v10\n\t" \
    "v_add_f32 v72, v72, v74\n\t" \
    "v_add_f32 v73, v73, v75\n\t" \
    "v_cvt_pk_bf16_f32 v156, v72, v73\n\t" \
    "ds_write_b32 v12, v156 offset:0\n\t" \
    "v_add_f32 v76, v76, v78\n\t" \
    "v_add_f32 v77, v77, v79\n\t" \
    "v_cvt_pk_bf16_f32 v157, v76, v77\n\t" \
    "ds_write_b32 v12, v157 offset:64\n\t" \
    "v_add_f32 v80, v80, v82\n\t" \
    "v_add_f32 v81, v81, v83\n\t" \
    "v_cvt_pk_bf16_f32 v158, v80, v81\n\t" \
    "ds_write_b32 v16, v158 offset:0\n\t" \
    "v_pk_add_f32 v[84:85], v[84:85], v[86:87]\n\t" \
    "v_cvt_pk_bf16_f32 v159, v84, v85\n\t" \
    "ds_write_b32 v16, v159 offset:256\n\t" \
    "s_branch qk_Gback26_%=\n\t" \
    "qk_qs28_%=:\n\t" \
    "s_cmp_eq_u32 s91, s65\n\t" \
    "s_cbranch_scc1 qk_qb230_%=\n\t" \
    "s_mov_b32 s91, s65\n\t" \
    "s_nop 7\n\t" \
    "ds_read_b64_tr_b16 v[132:133], v13 offset:0\n\t" \
    "ds_read_b64_tr_b16 v[134:135], v13 offset:128\n\t" \
    "ds_read_b64_tr_b16 v[136:137], v13 offset:1024\n\t" \
    "ds_read_b64_tr_b16 v[138:139], v13 offset:1152\n\t" \
    "v_mov_b32 v88, 0\n\t" \
    "v_mov_b32 v89, 0\n\t" \
    "v_mov_b32 v90, 0\n\t" \
    "v_mov_b32 v91, 0\n\t" \
    "v_mov_b32 v92, 0\n\t" \
    "v_mov_b32 v93, 0\n\t" \
    "v_mov_b32 v94, 0\n\t" \
    "v_mov_b32 v95, 0\n\t" \
    "v_mov_b32 v96, 0\n\t" \
    "v_mov_b32 v97, 0\n\t" \
    "v_mov_b32 v98, 0\n\t" \
    "v_mov_b32 v99, 0\n\t" \
    "s_waitcnt lgkmcnt(0)\n\t" \
    "v_smfmac_f32_16x16x64_bf16 v[88:91], v[132:135], v[100:107], v10\n\t" \
    "v_smfmac_f32_16x16x64_bf16 v[92:95], v[132:135], v[116:123], v10\n\t" \
    "v_smfmac_f32_16x16x64_bf16 v[88:91], v[136:139], v[108:115], v10\n\t" \
    "v_smfmac_f32_16x16x64_bf16 v[92:95], v[136:139], v[124:131], v10\n\t" \
    "s_nop 7\n\t" \
    "s_branch qk_qb29_%=\n\t" \
    "qk_last17_%=:\n\t" \
    "s_endpgm\n\t" \
    "qk_loop14_%=:\n\t" \
    "v_mov_b32 v72, 0\n\t" \
    "v_mov_b32 v73, 0\n\t" \
    "v_mov_b32 v74, 0\n\t" \
    "v_mov_b32 v75, 0\n\t" \
    "v_mov_b32 v76, 0\n\t" \
    "v_mov_b32 v77, 0\n\t" \
    "v_mov_b32 v78, 0\n\t" \
    "v_mov_b32 v79, 0\n\t" \
    "v_mov_b32 v80, 0\n\t" \
    "v_mov_b32 v81, 0\n\t" \
    "v_mov_b32 v82, 0\n\t" \
    "v_mov_b32 v83, 0\n\t" \
    "v_mov_b32 v84, 0\n\t" \
    "v_mov_b32 v85, 0\n\t" \
    "v_mov_b32 v86, 0\n\t" \
    "v_mov_b32 v87, 0\n\t" \
    "v_mov_b32 v88, 0\n\t" \
    "v_mov_b32 v89, 0\n\t" \
    "v_mov_b32 v90, 0\n\t" \
    "v_mov_b32 v91, 0\n\t" \
    "v_mov_b32 v92, 0\n\t" \
    "v_mov_b32 v93, 0\n\t" \
    "v_mov_b32 v94, 0\n\t" \
    "v_mov_b32 v95, 0\n\t" \
    "v_mov_b32 v96, 0\n\t" \
    "v_mov_b32 v97, 0\n\t" \
    "v_mov_b32 v98, 0\n\t" \
    "v_mov_b32 v99, 0\n\t" \
    "s_add_u32 s69, s65, 0\n\t" \
    "s_and_b32 s69, s69, 7\n\t" \
    "s_lshl_b32 s69, s69, 18\n\t" \
    "s_add_u32 s69, s69, s62\n\t" \
    "s_add_u32 s46, s36, s69\n\t" \
    "s_addc_u32 s47, s37, 0\n\t" \
    "s_bitcmp0_b32 s65, 0\n\t" \
    "s_cselect_b32 s69, 16384, 0\n\t" \
    "v_xor_b32 v1, s69, v1\n\t" \
    "v_xor_b32 v2, s69, v2\n\t" \
    "v_xor_b32 v23, s69, v23\n\t" \
    "v_xor_b32 v18, s69, v18\n\t" \
    "v_xor_b32 v180, s69, v180\n\t" \
    "v_xor_b32 v181, s69, v181\n\t" \
    "v_xor_b32 v182, s69, v182\n\t" \
    "s_sub_u32 s70, s79, s74\n\t" \
    "s_max_i32 s70, s70, 0\n\t" \
    "s_min_i32 s70, s70, 16000\n\t" \
    "s_lshl_b32 s70, s70, 1\n\t" \
    "s_mul_hi_u32 s70, s70, s77\n\t" \
    "s_add_u32 s70, s70, s73\n\t" \
    ".p2align 6\n\t" \
    "s_nop 0\n\t" \
    "s_nop 0\n\t" \
    "s_nop 0\n\t" \
    "s_nop 0\n\t" \
    "s_nop 0\n\t" \
    "s_nop 0\n\t" \
    "qk_go40_%=:\n\t" \
    "s_getpc_b64 s[30:31]\n\t" \
    "qk_Wp42_%=:\n\t" \
    "s_add_u32 s30, s30, qk_We43_%=-qk_Wp42_%=\n\t" \
    "s_addc_u32 s31, s31, 0\n\t" \
    "s_sub_u32 s70, s70, 96\n\t" \
    "qk_W41_%=:\n\t" \
    "s_memtime s[56:57]\n\t" \
    "s_waitcnt lgkmcnt(0)\n\t" \
    "s_sub_u32 s69, s70, s56\n\t" \
    "s_cmp_gt_i32 s69, 80\n\t" \
    "s_cbranch_scc1 qk_W41_%=\n\t" \
    "s_max_i32 s69, s69, 0\n\t" \
    "s_and_b32 s69, s69, -4\n\t" \
    "s_sub_u32 s30, s30, s69\n\t" \
    "s_subb_u32 s31, s31, 0\n\t" \
    "s_setpc_b64 s[30:31]\n\t" \
    "s_nop 0\n\t" \
    "s_nop 0\n\t" \
    "s_nop 0\n\t" \
    "s_nop 0\n\t" \
    "s_nop 0\n\t" \
    "s_nop 0\n\t" \
    "s_nop 0\n\t" \
    "s_nop 0\n\t" \
    "s_nop 0\n\t" \
    "s_nop 0\n\t" \
    "s_nop 0\n\t" \
    "s_nop 0\n\t" \
    "s_nop 0\n\t" \
    "s_nop 0\n\t" \
    "s_nop 0\n\t" \
    "s_nop 0\n\t" \
    "s_nop 0\n\t" \
    "s_nop 0\n\t" \
    "s_nop 0\n\t" \
    "s_nop 0\n\t" \
    "qk_We43_%=:\n\t" \
    "qk_G44_%=:\n\t" \
    "global_load_dwordx4 v[36:39], v1, s[46:47] sc1\n\t" \
    "global_load_dword v161, v2, s[46:47] sc1\n\t" \
    "global_load_dwordx4 v[40:43], v23, s[46:47] sc1\n\t" \
    "s_waitcnt vmcnt(2)\n\t" \
    "qk_Garr49_%=:\n\t" \
    "ds_write_b128 v3, v[36:39]\n\t" \
    "v_smfmac_f32_16x16x64_bf16 v[72:75], v[36:39], a[16:23], v10\n\t" \
    "s_waitcnt vmcnt(1)\n\t" \
    "ds_write_b32 v22, v161 offset:19712\n\t" \
    "v_smfmac_f32_16x16x64_bf16 v[76:79], v[36:39], a[88:95], v10\n\t" \
    "s_waitcnt vmcnt(0)\n\t" \
    "ds_write_b128 v3, v[40:43] offset:1024\n\t" \
    "v_max3_u32 v160, v36, v40, v161\n\t" \
    "v_cmp_eq_u32 vcc, -1, v160\n\t" \
    "v_smfmac_f32_16x16x64_bf16 v[80:83], v[36:39], a[160:167], v10\n\t" \
    "v_smfmac_f32_16x16x64_bf16 v[84:87], v[36:39], a[232:239], v10\n\t" \
    "v_smfmac_f32_16x16x64_bf16 v[72:75], v[40:43], a[24:31], v10\n\t" \
    "s_cbranch_vccnz qk_Gbad45_%=\n\t" \
    "qk_Gc46_%=:\n\t" \
    "s_waitcnt lgkmcnt(0)\n\t" \
    "s_barrier\n\t" \
    "ds_read_b128 v[48:51], v4\n\t" \
    "ds_read_b128 v[52:55], v5\n\t" \
    "v_smfmac_f32_16x16x64_bf16 v[76:79], v[40:43], a[96:103], v10\n\t" \
    "ds_read_b128 v[56:59], v6\n\t" \
    "ds_read_b128 v[60:63], v7\n\t" \
    "v_smfmac_f32_16x16x64_bf16 v[80:83], v[40:43], a[168:175], v10\n\t" \
    "ds_read_b128 v[64:67], v8\n\t" \
    "v_smfmac_f32_16x16x64_bf16 v[84:87], v[40:43], a[240:247], v10\n\t" \
    "ds_read_b128 v[68:71], v9\n\t" \
    "s_waitcnt lgkmcnt(5)\n\t" \
    "v_smfmac_f32_16x16x64_bf16 v[72:75], v[48:51], a[32:39], v10\n\t" \
    "ds_read_b128 v[44:47], v0 offset:19456\n\t" \
    "s_waitcnt lgkmcnt(5)\n\t" \
    "v_smfmac_f32_16x16x64_bf16 v[72:75], v[52:55], a[40:47], v10\n\t" \
    "s_waitcnt lgkmcnt(4)\n\t" \
    "v_smfmac_f32_16x16x64_bf16 v[72:75], v[56:59], a[48:55], v10\n\t" \
    "s_waitcnt lgkmcnt(3)\n\t" \
    "v_smfmac_f32_16x16x64_bf16 v[72:75], v[60:63], a[56:63], v10\n\t" \
    "s_waitcnt lgkmcnt(2)\n\t" \
    "v_smfmac_f32_16x16x64_bf16 v[72:75], v[64:67], a[0:7], v10\n\t" \
    "s_waitcnt lgkmcnt(1)\n\t" \
    "v_smfmac_f32_16x16x64_bf16 v[72:75], v[68:71], a[8:15], v10\n\t" \
    "s_waitcnt lgkmcnt(0)\n\t" \
    "v_smfmac_f32_16x16x64_bf16 v[72:75], v[44:47], a[64:71], v10\n\t" \
    "s_nop 2\n\t" \
    "v_smfmac_f32_16x16x64_bf16 v[76:79], v[48:51], a[104:111], v10\n\t" \
    "s_nop 2\n\t" \
    "v_smfmac_f32_16x16x64_bf16 v[76:79], v[52:55], a[112:119], v10\n\t" \
    "v_add_f32 v72, v72, v74\n\t" \
    "v_smfmac_f32_16x16x64_bf16 v[76:79], v[56:59], a[120:127], v10\n\t" \
    "v_add_f32 v73, v73, v75\n\t" \
    "v_smfmac_f32_16x16x64_bf16 v[76:79], v[60:63], a[128:135], v10\n\t" \
    "v_cvt_pk_bf16_f32 v156, v72, v73\n\t" \
    "v_smfmac_f32_16x16x64_bf16 v[76:79], v[64:67], a[72:79], v10\n\t" \
    "ds_write_b32 v12, v156 offset:0\n\t" \
    "v_smfmac_f32_16x16x64_bf16 v[76:79], v[68:71], a[80:87], v10\n\t" \
    "v_smfmac_f32_16x16x64_bf16 v[76:79], v[44:47], a[136:143], v10\n\t" \
    "s_nop 2\n\t" \
    "v_smfmac_f32_16x16x64_bf16 v[80:83], v[48:51], a[176:183], v10\n\t" \
    "s_nop 2\n\t" \
    "v_smfmac_f32_16x16x64_bf16 v[80:83], v[52:55], a[184:191], v10\n\t" \
    "v_add_f32 v76, v76, v78\n\t" \
    "v_smfmac_f32_16x16x64_bf16 v[80:83], v[56:59], a[192:199], v10\n\t" \
    "v_add_f32 v77, v77, v79\n\t" \
    "v_smfmac_f32_16x16x64_bf16 v[80:83], v[60:63], a[200:207], v10\n\t" \
    "v_cvt_pk_bf16_f32 v157, v76, v77\n\t" \
    "v_smfmac_f32_16x16x64_bf16 v[80:83], v[64:67], a[144:151], v10\n\t" \
    "ds_write_b32 v12, v157 offset:64\n\t" \
    "v_smfmac_f32_16x16x64_bf16 v[80:83], v[68:71], a[152:159], v10\n\t" \
    "v_smfmac_f32_16x16x64_bf16 v[80:83], v[44:47], a[208:215], v10\n\t" \
    "s_nop 2\n\t" \
    "v_smfmac_f32_16x16x64_bf16 v[84:87], v[48:51], a[248:255], v10\n\t" \
    "s_nop 2\n\t" \
    "v_smfmac_f32_16x16x64_bf16 v[84:87], v[52:55], v[224:231], v10\n\t" \
    "v_add_f32 v80, v80, v82\n\t" \
    "v_smfmac_f32_16x16x64_bf16 v[84:87], v[56:59], v[232:239], v10\n\t" \
    "ds_read_b64_tr_b16 v[132:133], v13 offset:0\n\t" \
    "v_add_f32 v81, v81, v83\n\t" \
    "v_smfmac_f32_16x16x64_bf16 v[84:87], v[60:63], v[240:247], v10\n\t" \
    "ds_read_b64_tr_b16 v[134:135], v13 offset:128\n\t" \
    "v_cvt_pk_bf16_f32 v158, v80, v81\n\t" \
    "v_smfmac_f32_16x16x64_bf16 v[84:87], v[64:67], a[216:223], v10\n\t" \
    "ds_write_b32 v16, v158 offset:0\n\t" \
    "v_smfmac_f32_16x16x64_bf16 v[84:87], v[68:71], a[224:231], v10\n\t" \
    "ds_read_b64_tr_b16 v[136:137], v13 offset:1024\n\t" \
    "ds_read_b64_tr_b16 v[138:139], v13 offset:1152\n\t" \
    "v_smfmac_f32_16x16x64_bf16 v[84:87], v[44:47], v[248:255], v10\n\t" \
    "s_waitcnt lgkmcnt(3)\n\t" \
    "v_smfmac_f32_16x16x64_bf16 v[88:91], v[132:135], v[100:107], v10\n\t" \
    "s_nop 4\n\t" \
    "v_cmp_u_f32 vcc, v72, v73\n\t" \
    "v_pk_add_f32 v[84:85], v[84:85], v[86:87]\n\t" \
    "v_cvt_pk_bf16_f32 v159, v84, v85\n\t" \
    "ds_write_b32 v16, v159 offset:256\n\t" \
    "v_smfmac_f32_16x16x64_bf16 v[92:95], v[132:135], v[116:123], v10\n\t" \
    "s_waitcnt lgkmcnt(1)\n\t" \
    "v_smfmac_f32_16x16x64_bf16 v[88:91], v[136:139], v[108:115], v10\n\t" \
    "s_cbranch_vccnz qk_Gslow47_%=\n\t" \
    "qk_Gback48_%=:\n\t" \
    "v_smfmac_f32_16x16x64_bf16 v[92:95], v[136:139], v[124:131], v10\n\t" \
    "s_waitcnt lgkmcnt(0)\n\t" \
    "s_barrier\n\t" \
    "ds_read_b64_tr_b16 v[140:141], v17 offset:0\n\t" \
    "ds_read_b64_tr_b16 v[142:143], v17 offset:256\n\t" \
    "ds_read_b64_tr_b16 v[144:145], v17 offset:512\n\t" \
    "ds_read_b64_tr_b16 v[146:147], v17 offset:768\n\t" \
    "ds_read_b64_tr_b16 v[148:149], v17 offset:1024\n\t" \
    "ds_read_b64_tr_b16 v[150:151], v17 offset:1280\n\t" \
    "ds_read_b64_tr_b16 v[152:153], v17 offset:1536\n\t" \
    "ds_read_b64_tr_b16 v[154:155], v17 offset:1792\n\t" \
    "qk_qb51_%=:\n\t" \
    "v_add_f32 v168, v89, v91\n\t" \
    "v_add_f32 v169, v93, v95\n\t" \
    "v_cmp_u_f32 vcc, v168, v169\n\t" \
    "v_cvt_pk_bf16_f32 v168, 0, v168\n\t" \
    "v_cvt_pk_bf16_f32 v169, 0, v169\n\t" \
    "s_waitcnt lgkmcnt(4)\n\t" \
    "v_smfmac_f32_16x16x64_bf16 v[96:99], v[132:135], v[140:147], v10\n\t" \
    "s_waitcnt lgkmcnt(0)\n\t" \
    "v_smfmac_f32_16x16x64_bf16 v[96:99], v[136:139], v[148:155], v10\n\t" \
    "s_cbranch_vccnz qk_qs50_%=\n\t" \
    "qk_qb252_%=:\n\t" \
    "v_mul_f32 v168, v168, v26\n\t" \
    "v_mul_f32 v169, v169, v26\n\t" \
    "v_cvt_pk_bf16_f32 v168, 0, v168\n\t" \
    "v_cvt_pk_bf16_f32 v169, 0, v169\n\t" \
    "v_max_f32 v178, v168, v169\n\t" \
    "v_mul_f32 v168, v168, v28\n\t" \
    "v_mul_f32 v169, v169, v28\n\t" \
    "v_max_f32_dpp v178, v178, v178 row_mirror row_mask:0xf bank_mask:0xf\n\t" \
    "v_add_f32 v172, v97, v99\n\t" \
    "v_cvt_pk_bf16_f32 v172, 0, v172\n\t" \
    "v_mul_f32 v172, v172, v26\n\t" \
    "v_cvt_pk_bf16_f32 v172, 0, v172\n\t" \
    "v_max_f32 v173, v178, v172\n\t" \
    "v_mul_f32 v172, v172, v28\n\t" \
    "s_nop 0\n\t" \
    "v_max_f32_dpp v173, v173, v173 quad_perm:[1,0,3,2] row_mask:0xf bank_mask:0xf\n\t" \
    "s_nop 1\n\t" \
    "v_max_f32_dpp v173, v173, v173 quad_perm:[2,3,0,1] row_mask:0xf bank_mask:0xf\n\t" \
    "s_nop 1\n\t" \
    "v_max_f32_dpp v173, v173, v173 row_half_mirror row_mask:0xf bank_mask:0xf\n\t" \
    "v_fma_f32 v172, -v173, v28, v172\n\t" \
    "v_fma_f32 v168, -v173, v28, v168\n\t" \
    "v_fma_f32 v169, -v173, v28, v169\n\t" \
    "v_exp_f32 v172, v172\n\t" \
    "v_exp_f32 v168, v168\n\t" \
    "v_exp_f32 v169, v169\n\t" \
    "v_fma_f32 v173, v172, 0.5, v168\n\t" \
    "v_add_f32 v173, v173, v169\n\t" \
    "v_mov_b32_dpp v174, v172 quad_perm:[0,0,2,2] row_mask:0xf bank_mask:0xf\n\t" \
    "v_mov_b32_dpp v175, v172 quad_perm:[1,1,3,3] row_mask:0xf bank_mask:0xf\n\t" \
    "v_add_f32_dpp v173, v173, v173 quad_perm:[1,0,3,2] row_mask:0xf bank_mask:0xf\n\t" \
    "v_mov_b32_dpp v176, v168 quad_perm:[1,0,3,2] row_mask:0xf bank_mask:0xf\n\t" \
    "v_mov_b32_dpp v177, v169 quad_perm:[1,0,3,2] row_mask:0xf bank_mask:0xf\n\t" \
    "v_add_f32_dpp v173, v173, v173 quad_perm:[2,3,0,1] row_mask:0xf bank_mask:0xf\n\t" \
    "v_cndmask_b32 v168, v168, v177, s[48:49]\n\t" \
    "v_cndmask_b32 v169, v176, v169, s[48:49]\n\t" \
    "v_add_f32_dpp v173, v173, v173 row_half_mirror row_mask:0xf bank_mask:0xf\n\t" \
    "s_nop 1\n\t" \
    "v_add_f32_dpp v173, v173, v173 row_mirror row_mask:0xf bank_mask:0xf\n\t" \
    "v_rcp_f32 v173, v173\n\t" \
    "s_nop 0\n\t" \
    "v_pk_mul_f32 v[168:169], v[168:169], v[172:173] op_sel:[0,1] op_sel_hi:[1,1]\n\t" \
    "v_cvt_pk_bf16_f32 v176, v168, v169\n\t" \
    "global_store_dword v18, v176, s[46:47] sc1\n\t" \
    "global_store_dword v180, v176, s[46:47] sc1\n\t" \
    "global_store_dword v181, v176, s[46:47] sc1\n\t" \
    "global_store_dword v182, v176, s[46:47] sc1\n\t" \
    "v_pk_mul_f32 v[174:175], v[174:175], v[172:173] op_sel:[0,1] op_sel_hi:[1,1]\n\t" \
    "v_cvt_pk_bf16_f32 v177, v174, v175\n\t" \
    "global_store_dword v19, v177, s[46:47] sc1\n\t" \
    "s_cmp_eq_u32 s65, 63\n\t" \
    "s_cbranch_scc1 qk_last39_%=\n\t" \
    "v_readfirstlane_b32 s69, v25\n\t" \
    "s_add_u32 s65, s65, 1\n\t" \
    "s_max_i32 s83, s83, s69\n\t" \
    "s_cmp_gt_u32 s65, 7\n\t" \
    "s_cselect_b32 s83, 99, s83\n\t" \
    "v_mov_b32 v72, 0\n\t" \
    "v_mov_b32 v73, 0\n\t" \
    "v_mov_b32 v74, 0\n\t" \
    "v_mov_b32 v75, 0\n\t" \
    "v_mov_b32 v76, 0\n\t" \
    "v_mov_b32 v77, 0\n\t" \
    "v_mov_b32 v78, 0\n\t" \
    "v_mov_b32 v79, 0\n\t" \
    "v_mov_b32 v80, 0\n\t" \
    "v_mov_b32 v81, 0\n\t" \
    "v_mov_b32 v82, 0\n\t" \
    "v_mov_b32 v83, 0\n\t" \
    "v_mov_b32 v84, 0\n\t" \
    "v_mov_b32 v85, 0\n\t" \
    "v_mov_b32 v86, 0\n\t" \
    "v_mov_b32 v87, 0\n\t" \
    "v_mov_b32 v88, 0\n\t" \
    "v_mov_b32 v89, 0\n\t" \
    "v_mov_b32 v90, 0\n\t" \
    "v_mov_b32 v91, 0\n\t" \
    "v_mov_b32 v92, 0\n\t" \
    "v_mov_b32 v93, 0\n\t" \
    "v_mov_b32 v94, 0\n\t" \
    "v_mov_b32 v95, 0\n\t" \
    "v_mov_b32 v96, 0\n\t" \
    "v_mov_b32 v97, 0\n\t" \
    "v_mov_b32 v98, 0\n\t" \
    "v_mov_b32 v99, 0\n\t" \
    "s_add_u32 s69, s65, 0\n\t" \
    "s_and_b32 s69, s69, 7\n\t" \
    "s_lshl_b32 s69, s69, 18\n\t" \
    "s_add_u32 s69, s69, s62\n\t" \
    "s_add_u32 s46, s36, s69\n\t" \
    "s_addc_u32 s47, s37, 0\n\t" \
    "s_bitcmp0_b32 s65, 0\n\t" \
    "s_cselect_b32 s69, 16384, 0\n\t" \
    "v_xor_b32 v1, s69, v1\n\t" \
    "v_xor_b32 v2, s69, v2\n\t" \
    "v_xor_b32 v23, s69, v23\n\t" \
    "v_xor_b32 v18, s69, v18\n\t" \
    "v_xor_b32 v180, s69, v180\n\t" \
    "v_xor_b32 v181, s69, v181\n\t" \
    "v_xor_b32 v182, s69, v182\n\t" \
    "s_mul_i32 s70, s67, 7\n\t" \
    "s_add_u32 s69, s65, 0\n\t" \
    "s_xor_b32 s69, s69, s70\n\t" \
    "s_and_b32 s69, s69, 63\n\t" \
    "s_lshl_b32 s69, s69, 13\n\t" \
    "s_mul_i32 s70, s67, 524288\n\t" \
    "s_add_u32 s69, s69, s70\n\t" \
    "s_add_u32 s69, s69, 23068672\n\t" \
    "s_add_u32 s38, s36, s69\n\t" \
    "s_addc_u32 s39, s37, 0\n\t" \
    "qk_WF53_%=:\n\t" \
    "s_cmp_ge_i32 s83, s65\n\t" \
    "s_cbranch_scc1 qk_WFok54_%=\n\t" \
    "global_load_dword v160, v24, s[100:101] sc1\n\t" \
    "s_waitcnt vmcnt(0)\n\t" \
    "v_readfirstlane_b32 s83, v160\n\t" \
    "s_nop 1\n\t" \
    "s_cmp_ge_i32 s83, s65\n\t" \
    "s_cbranch_scc1 qk_WFok54_%=\n\t" \
    "s_cmp_eq_u32 s68, 0\n\t" \
    "s_cbranch_scc1 qk_F55_%=\n\t" \
    "s_sub_u32 s68, s68, 1\n\t" \
    "s_sleep 2\n\t" \
    "s_branch qk_WF53_%=\n\t" \
    "qk_F55_%=:\n\t" \
    "v_mov_b32 v160, 5767424\n\t" \
    "v_mov_b32 v161, 9\n\t" \
    "global_store_dword v160, v161, s[36:37] sc1\n\t" \
    "s_waitcnt vmcnt(0)\n\t" \
    "s_branch qk_WFok54_%=\n\t" \
    "qk_WFok54_%=:\n\t" \
    "global_load_dwordx4 v[100:103], v0, s[38:39] offset:0 sc1\n\t" \
    "global_load_dwordx4 v[104:107], v0, s[38:39] offset:2048 sc1\n\t" \
    "global_load_dwordx4 v[108:111], v15, s[38:39] offset:0 sc1\n\t" \
    "global_load_dwordx4 v[112:115], v15, s[38:39] offset:2048 sc1\n\t" \
    "global_load_dwordx4 v[116:119], v0, s[38:39] offset:1024 sc1\n\t" \
    "global_load_dwordx4 v[120:123], v0, s[38:39] offset:3072 sc1\n\t" \
    "global_load_dwordx4 v[124:127], v15, s[38:39] offset:1024 sc1\n\t" \
    "global_load_dwordx4 v[128:131], v15, s[38:39] offset:3072 sc1\n\t" \
    "s_cmp_gt_i32 s83, s65\n\t" \
    "s_cbranch_scc1 qk_nf56_%=\n\t" \
    "global_load_dword v25, v24, s[100:101] sc1\n\t" \
    "qk_nf56_%=:\n\t" \
    "s_barrier\n\t" \
    "ds_read_b32 v168, v11 offset:20480\n\t" \
    "s_waitcnt lgkmcnt(0)\n\t" \
    "v_readfirstlane_b32 s70, v168\n\t" \
    "ds_write_b32 v12, v31 offset:0\n\t" \
    "ds_write_b32 v12, v31 offset:64\n\t" \
    "s_branch qk_go40_%=\n\t" \
    "qk_Gbad45_%=:\n\t" \
    "s_waitcnt vmcnt(0) lgkmcnt(0)\n\t" \
    "s_nop 7\n\t" \
    "v_mov_b32 v72, 0\n\t" \
    "v_mov_b32 v73, 0\n\t" \
    "v_mov_b32 v74, 0\n\t" \
    "v_mov_b32 v75, 0\n\t" \
    "v_mov_b32 v76, 0\n\t" \
    "v_mov_b32 v77, 0\n\t" \
    "v_mov_b32 v78, 0\n\t" \
    "v_mov_b32 v79, 0\n\t" \
    "v_mov_b32 v80, 0\n\t" \
    "v_mov_b32 v81, 0\n\t" \
    "v_mov_b32 v82, 0\n\t" \
    "v_mov_b32 v83, 0\n\t" \
    "v_mov_b32 v84, 0\n\t" \
    "v_mov_b32 v85, 0\n\t" \
    "v_mov_b32 v86, 0\n\t" \
    "v_mov_b32 v87, 0\n\t" \
    "s_cmp_eq_u32 s68, 0\n\t" \
    "s_cbranch_scc1 qk_F57_%=\n\t" \
    "s_sub_u32 s68, s68, 1\n\t" \
    "s_branch qk_G44_%=\n\t" \
    "qk_F57_%=:\n\t" \
    "v_mov_b32 v160, 5767424\n\t" \
    "v_mov_b32 v161, 1\n\t" \
    "global_store_dword v160, v161, s[36:37] sc1\n\t" \
    "s_waitcnt vmcnt(0)\n\t" \
    "s_branch qk_Gc46_%=\n\t" \
    "qk_Gslow47_%=:\n\t" \
    "v_max_u32 v160, v36, v37\n\t" \
    "v_max3_u32 v160, v160, v38, v39\n\t" \
    "v_max3_u32 v160, v160, v40, v41\n\t" \
    "v_max3_u32 v160, v160, v42, v43\n\t" \
    "v_max3_u32 v160, v160, v44, v45\n\t" \
    "v_max3_u32 v160, v160, v46, v47\n\t" \
    "v_max3_u32 v160, v160, v48, v49\n\t" \
    "v_max3_u32 v160, v160, v50, v51\n\t" \
    "v_max3_u32 v160, v160, v52, v53\n\t" \
    "v_max3_u32 v160, v160, v54, v55\n\t" \
    "v_max3_u32 v160, v160, v56, v57\n\t" \
    "v_max3_u32 v160, v160, v58, v59\n\t" \
    "v_max3_u32 v160, v160, v60, v61\n\t" \
    "v_max3_u32 v160, v160, v62, v63\n\t" \
    "v_max3_u32 v160, v160, v64, v65\n\t" \
    "v_max3_u32 v160, v160, v66, v67\n\t" \
    "v_max3_u32 v160, v160, v68, v69\n\t" \
    "v_max3_u32 v160, v160, v70, v71\n\t" \
    "v_cmp_eq_u32 vcc, -1, v160\n\t" \
    "s_cbranch_vccz qk_Gback48_%=\n\t" \
    "s_waitcnt lgkmcnt(0)\n\t" \
    "v_sub_u32 v160, v1, v3\n\t" \
    "s_nop 0\n\t" \
    "v_readfirstlane_b32 s69, v160\n\t" \
    "v_add_u32 v163, 36864, v0\n\t" \
    "s_nop 7\n\t" \
    "s_add_u32 s30, s46, s69\n\t" \
    "s_addc_u32 s31, s47, 0\n\t" \
    "qk_Gag58_%=:\n\t" \
    "global_load_dwordx4 v[36:39], v1, s[46:47] sc1\n\t" \
    "global_load_dwordx4 v[40:43], v23, s[46:47] sc1\n\t" \
    "global_load_dwordx4 v[44:47], v163, s[30:31] sc1\n\t" \
    "global_load_dwordx4 v[48:51], v4, s[30:31] sc1\n\t" \
    "global_load_dwordx4 v[52:55], v5, s[30:31] sc1\n\t" \
    "global_load_dwordx4 v[56:59], v6, s[30:31] sc1\n\t" \
    "global_load_dwordx4 v[60:63], v7, s[30:31] sc1\n\t" \
    "global_load_dwordx4 v[64:67], v8, s[30:31] sc1\n\t" \
    "global_load_dwordx4 v[68:71], v9, s[30:31] sc1\n\t" \
    "s_waitcnt vmcnt(0)\n\t" \
    "v_max_u32 v160, v36, v37\n\t" \
    "v_max3_u32 v160, v160, v38, v39\n\t" \
    "v_max3_u32 v160, v160, v40, v41\n\t" \
    "v_max3_u32 v160, v160, v42, v43\n\t" \
    "v_max3_u32 v160, v160, v44, v45\n\t" \
    "v_max3_u32 v160, v160, v46, v47\n\t" \
    "v_max3_u32 v160, v160, v48, v49\n\t" \
    "v_max3_u32 v160, v160, v50, v51\n\t" \
    "v_max3_u32 v160, v160, v52, v53\n\t" \
    "v_max3_u32 v160, v160, v54, v55\n\t" \
    "v_max3_u32 v160, v160, v56, v57\n\t" \
    "v_max3_u32 v160, v160, v58, v59\n\t" \
    "v_max3_u32 v160, v160, v60, v61\n\t" \
    "v_max3_u32 v160, v160, v62, v63\n\t" \
    "v_max3_u32 v160, v160, v64, v65\n\t" \
    "v_max3_u32 v160, v160, v66, v67\n\t" \
    "v_max3_u32 v160, v160, v68, v69\n\t" \
    "v_max3_u32 v160, v160, v70, v71\n\t" \
    "v_cmp_eq_u32 vcc, -1, v160\n\t" \
    "s_cbranch_vccz qk_Gredo59_%=\n\t" \
    "s_cmp_eq_u32 s68, 0\n\t" \
    "s_cbranch_scc1 qk_F60_%=\n\t" \
    "s_sub_u32 s68, s68, 1\n\t" \
    "s_sleep 1\n\t" \
    "s_branch qk_Gag58_%=\n\t" \
    "qk_F60_%=:\n\t" \
    "v_mov_b32 v160, 5767424\n\t" \
    "v_mov_b32 v161, 2\n\t" \
    "global_store_dword v160, v161, s[36:37] sc1\n\t" \
    "s_waitcnt vmcnt(0)\n\t" \
    "s_branch qk_Gredo59_%=\n\t" \
    "qk_Gredo59_%=:\n\t" \
    "v_mov_b32 v72, 0\n\t" \
    "v_mov_b32 v73, 0\n\t" \
    "v_mov_b32 v74, 0\n\t" \
    "v_mov_b32 v75, 0\n\t" \
    "v_mov_b32 v76, 0\n\t" \
    "v_mov_b32 v77, 0\n\t" \
    "v_mov_b32 v78, 0\n\t" \
    "v_mov_b32 v79, 0\n\t" \
    "v_mov_b32 v80, 0\n\t" \
    "v_mov_b32 v81, 0\n\t" \
    "v_mov_b32 v82, 0\n\t" \
    "v_mov_b32 v83, 0\n\t" \
    "v_mov_b32 v84, 0\n\t" \
    "v_mov_b32 v85, 0\n\t" \
    "v_mov_b32 v86, 0\n\t" \
    "v_mov_b32 v87, 0\n\t" \
    "v_smfmac_f32_16x16x64_bf16 v[72:75], v[36:39], a[16:23], v10\n\t" \
    "v_smfmac_f32_16x16x64_bf16 v[72:75], v[40:43], a[24:31], v10\n\t" \
    "v_smfmac_f32_16x16x64_bf16 v[72:75], v[48:51], a[32:39], v10\n\t" \
    "v_smfmac_f32_16x16x64_bf16 v[72:75], v[52:55], a[40:47], v10\n\t" \
    "v_smfmac_f32_16x16x64_bf16 v[72:75], v[56:59], a[48:55], v10\n\t" \
    "v_smfmac_f32_16x16x64_bf16 v[72:75], v[60:63], a[56:63], v10\n\t" \
    "v_smfmac_f32_16x16x64_bf16 v[72:75], v[64:67], a[0:7], v10\n\t" \
    "v_smfmac_f32_16x16x64_bf16 v[72:75], v[68:71], a[8:15], v10\n\t" \
    "v_smfmac_f32_16x16x64_bf16 v[72:75], v[44:47], a[64:71], v10\n\t" \
    "v_smfmac_f32_16x16x64_bf16 v[76:79], v[36:39], a[88:95], v10\n\t" \
    "v_smfmac_f32_16x16x64_bf16 v[76:79], v[40:43], a[96:103], v10\n\t" \
    "v_smfmac_f32_16x16x64_bf16 v[76:79], v[48:51], a[104:111], v10\n\t" \
    "v_smfmac_f32_16x16x64_bf16 v[76:79], v[52:55], a[112:119], v10\n\t" \
    "v_smfmac_f32_16x16x64_bf16 v[76:79], v[56:59], a[120:127], v10\n\t" \
    "v_smfmac_f32_16x16x64_bf16 v[76:79], v[60:63], a[128:135], v10\n\t" \
    "v_smfmac_f32_16x16x64_bf16 v[76:79], v[64:67], a[72:79], v10\n\t" \
    "v_smfmac_f32_16x16x64_bf16 v[76:79], v[68:71], a[80:87], v10\n\t" \
    "v_smfmac_f32_16x16x64_bf16 v[76:79], v[44:47], a[136:143], v10\n\t" \
    "v_smfmac_f32_16x16x64_bf16 v[80:83], v[36:39], a[160:167], v10\n\t" \
    "v_smfmac_f32_16x16x64_bf16 v[80:83], v[40:43], a[168:175], v10\n\t" \
    "v_smfmac_f32_16x16x64_bf16 v[80:83], v[48:51], a[176:183], v10\n\t" \
    "v_smfmac_f32_16x16x64_bf16 v[80:83], v[52:55], a[184:191], v10\n\t" \
    "v_smfmac_f32_16x16x64_bf16 v[80:83], v[56:59], a[192:199], v10\n\t" \
    "v_smfmac_f32_16x16x64_bf16 v[80:83], v[60:63], a[200:207], v10\n\t" \
    "v_smfmac_f32_16x16x64_bf16 v[80:83], v[64:67], a[144:151], v10\n\t" \
    "v_smfmac_f32_16x16x64_bf16 v[80:83], v[68:71], a[152:159], v10\n\t" \
    "v_smfmac_f32_16x16x64_bf16 v[80:83], v[44:47], a[208:215], v10\n\t" \
    "v_smfmac_f32_16x16x64_bf16 v[84:87], v[36:39], a[232:239], v10\n\t" \
    "v_smfmac_f32_16x16x64_bf16 v[84:87], v[40:43], a[240:247], v10\n\t" \
    "v_smfmac_f32_16x16x64_bf16 v[84:87], v[48:51], a[248:255], v10\n\t" \
    "v_smfmac_f32_16x16x64_bf16 v[84:87], v[52:55], v[224:231], v10\n\t" \
    "v_smfmac_f32_16x16x64_bf16 v[84:87], v[56:59], v[232:239], v10\n\t" \
    "v_smfmac_f32_16x16x64_bf16 v[84:87], v[60:63], v[240:247], v10\n\t" \
    "v_smfmac_f32_16x16x64_bf16 v[84:87], v[64:67], a[216:223], v10\n\t" \
    "v_smfmac_f32_16x16x64_bf16 v[84:87], v[68:71], a[224:231], v10\n\t" \
    "v_smfmac_f32_16x16x64_bf16 v[84:87], v[44:47], v[248:255], v10\n\t" \
    "v_add_f32 v72, v72, v74\n\t" \
    "v_add_f32 v73, v73, v75\n\t" \
    "v_cvt_pk_bf16_f32 v156, v72, v73\n\t" \
    "ds_write_b32 v12, v156 offset:0\n\t" \
    "v_add_f32 v76, v76, v78\n\t" \
    "v_add_f32 v77, v77, v79\n\t" \
    "v_cvt_pk_bf16_f32 v157, v76, v77\n\t" \
    "ds_write_b32 v12, v157 offset:64\n\t" \
    "v_add_f32 v80, v80, v82\n\t" \
    "v_add_f32 v81, v81, v83\n\t" \
    "v_cvt_pk_bf16_f32 v158, v80, v81\n\t" \
    "ds_write_b32 v16, v158 offset:0\n\t" \
    "v_pk_add_f32 v[84:85], v[84:85], v[86:87]\n\t" \
    "v_cvt_pk_bf16_f32 v159, v84, v85\n\t" \
    "ds_write_b32 v16, v159 offset:256\n\t" \
    "s_branch qk_Gback48_%=\n\t" \
    "qk_qs50_%=:\n\t" \
    "s_cmp_eq_u32 s91, s65\n\t" \
    "s_cbranch_scc1 qk_qb252_%=\n\t" \
    "s_mov_b32 s91, s65\n\t" \
    "s_nop 7\n\t" \
    "ds_read_b64_tr_b16 v[132:133], v13 offset:0\n\t" \
    "ds_read_b64_tr_b16 v[134:135], v13 offset:128\n\t" \
    "ds_read_b64_tr_b16 v[136:137], v13 offset:1024\n\t" \
    "ds_read_b64_tr_b16 v[138:139], v13 offset:1152\n\t" \
    "v_mov_b32 v88, 0\n\t" \
    "v_mov_b32 v89, 0\n\t" \
    "v_mov_b32 v90, 0\n\t" \
    "v_mov_b32 v91, 0\n\t" \
    "v_mov_b32 v92, 0\n\t" \
    "v_mov_b32 v93, 0\n\t" \
    "v_mov_b32 v94, 0\n\t" \
    "v_mov_b32 v95, 0\n\t" \
    "v_mov_b32 v96, 0\n\t" \
    "v_mov_b32 v97, 0\n\t" \
    "v_mov_b32 v98, 0\n\t" \
    "v_mov_b32 v99, 0\n\t" \
    "s_waitcnt lgkmcnt(0)\n\t" \
    "v_smfmac_f32_16x16x64_bf16 v[88:91], v[132:135], v[100:107], v10\n\t" \
    "v_smfmac_f32_16x16x64_bf16 v[92:95], v[132:135], v[116:123], v10\n\t" \
    "v_smfmac_f32_16x16x64_bf16 v[88:91], v[136:139], v[108:115], v10\n\t" \
    "v_smfmac_f32_16x16x64_bf16 v[92:95], v[136:139], v[124:131], v10\n\t" \
    "s_nop 7\n\t" \
    "s_branch qk_qb51_%=\n\t" \
    "qk_last39_%=:\n\t" \
    "s_endpgm\n\t" \
    "qk_loop15_%=:\n\t" \
    "v_mov_b32 v72, 0\n\t" \
    "v_mov_b32 v73, 0\n\t" \
    "v_mov_b32 v74, 0\n\t" \
    "v_mov_b32 v75, 0\n\t" \
    "v_mov_b32 v76, 0\n\t" \
    "v_mov_b32 v77, 0\n\t" \
    "v_mov_b32 v78, 0\n\t" \
    "v_mov_b32 v79, 0\n\t" \
    "v_mov_b32 v80, 0\n\t" \
    "v_mov_b32 v81, 0\n\t" \
    "v_mov_b32 v82, 0\n\t" \
    "v_mov_b32 v83, 0\n\t" \
    "v_mov_b32 v84, 0\n\t" \
    "v_mov_b32 v85, 0\n\t" \
    "v_mov_b32 v86, 0\n\t" \
    "v_mov_b32 v87, 0\n\t" \
    "s_add_u32 s69, s65, 0\n\t" \
    "s_and_b32 s69, s69, 7\n\t" \
    "s_lshl_b32 s69, s69, 18\n\t" \
    "s_add_u32 s69, s69, s62\n\t" \
    "s_add_u32 s46, s36, s69\n\t" \
    "s_addc_u32 s47, s37, 0\n\t" \
    "s_bitcmp0_b32 s65, 0\n\t" \
    "s_cselect_b32 s69, 16384, 0\n\t" \
    "v_xor_b32 v1, s69, v1\n\t" \
    "v_xor_b32 v2, s69, v2\n\t" \
    "v_xor_b32 v23, s69, v23\n\t" \
    "s_mul_i32 s70, s67, 7\n\t" \
    "s_add_u32 s69, s65, 0\n\t" \
    "s_xor_b32 s69, s69, s70\n\t" \
    "s_and_b32 s69, s69, 63\n\t" \
    "s_lshl_b32 s69, s69, 12\n\t" \
    "s_mul_i32 s70, s67, 262144\n\t" \
    "s_add_u32 s69, s69, s70\n\t" \
    "s_add_u32 s69, s69, 39845888\n\t" \
    "s_add_u32 s40, s36, s69\n\t" \
    "s_addc_u32 s41, s37, 0\n\t" \
    "s_sub_u32 s91, s65, 1\n\t" \
    "v_mov_b32 v25, s91\n\t" \
    "s_sub_u32 s70, s79, s74\n\t" \
    "s_max_i32 s70, s70, 0\n\t" \
    "s_min_i32 s70, s70, 16000\n\t" \
    "s_lshl_b32 s70, s70, 1\n\t" \
    "s_mul_hi_u32 s70, s70, s77\n\t" \
    "s_add_u32 s70, s70, s73\n\t" \
    ".p2align 6\n\t" \
    "s_nop 0\n\t" \
    "s_nop 0\n\t" \
    "s_nop 0\n\t" \
    "s_nop 0\n\t" \
    "s_nop 0\n\t" \
    "s_nop 0\n\t" \
    "qk_go62_%=:\n\t" \
    "s_getpc_b64 s[30:31]\n\t" \
    "qk_Wp66_%=:\n\t" \
    "s_add_u32 s30, s30, qk_We67_%=-qk_Wp66_%=\n\t" \
    "s_addc_u32 s31, s31, 0\n\t" \
    "s_sub_u32 s70, s70, 96\n\t" \
    "qk_W65_%=:\n\t" \
    "s_memtime s[56:57]\n\t" \
    "s_waitcnt lgkmcnt(0)\n\t" \
    "s_sub_u32 s69, s70, s56\n\t" \
    "s_cmp_gt_i32 s69, 80\n\t" \
    "s_cbranch_scc1 qk_W65_%=\n\t" \
    "s_max_i32 s69, s69, 0\n\t" \
    "s_and_b32 s69, s69, -4\n\t" \
    "s_sub_u32 s30, s30, s69\n\t" \
    "s_subb_u32 s31, s31, 0\n\t" \
    "s_setpc_b64 s[30:31]\n\t" \
    "s_nop 0\n\t" \
    "s_nop 0\n\t" \
    "s_nop 0\n\t" \
    "s_nop 0\n\t" \
    "s_nop 0\n\t" \
    "s_nop 0\n\t" \
    "s_nop 0\n\t" \
    "s_nop 0\n\t" \
    "s_nop 0\n\t" \
    "s_nop 0\n\t" \
    "s_nop 0\n\t" \
    "s_nop 0\n\t" \
    "s_nop 0\n\t" \
    "s_nop 0\n\t" \
    "s_nop 0\n\t" \
    "s_nop 0\n\t" \
    "s_nop 0\n\t" \
    "s_nop 0\n\t" \
    "s_nop 0\n\t" \
    "s_nop 0\n\t" \
    "qk_We67_%=:\n\t" \
    "qk_G68_%=:\n\t" \
    "global_load_dwordx4 v[36:39], v1, s[46:47] sc1\n\t" \
    "global_load_dword v161, v2, s[46:47] sc1\n\t" \
    "global_load_dwordx4 v[40:43], v23, s[46:47] sc1\n\t" \
    "s_waitcnt vmcnt(2)\n\t" \
    "qk_Garr73_%=:\n\t" \
    "ds_write_b128 v3, v[36:39]\n\t" \
    "v_smfmac_f32_16x16x64_bf16 v[72:75], v[36:39], a[32:39], v10\n\t" \
    "s_waitcnt vmcnt(1)\n\t" \
    "ds_write_b32 v22, v161 offset:19968\n\t" \
    "v_smfmac_f32_16x16x64_bf16 v[76:79], v[36:39], a[104:111], v10\n\t" \
    "s_waitcnt vmcnt(0)\n\t" \
    "ds_write_b128 v3, v[40:43] offset:1024\n\t" \
    "v_max3_u32 v160, v36, v40, v161\n\t" \
    "v_cmp_eq_u32 vcc, -1, v160\n\t" \
    "v_smfmac_f32_16x16x64_bf16 v[80:83], v[36:39], a[176:183], v10\n\t" \
    "v_smfmac_f32_16x16x64_bf16 v[84:87], v[36:39], a[248:255], v10\n\t" \
    "v_smfmac_f32_16x16x64_bf16 v[72:75], v[40:43], a[40:47], v10\n\t" \
    "s_cbranch_vccnz qk_Gbad69_%=\n\t" \
    "qk_Gc70_%=:\n\t" \
    "s_cmp_ge_u32 s91, 57\n\t" \
    "s_cbranch_scc1 qk_kf63_%=\n\t" \
    "qk_kb64_%=:\n\t" \
    "s_waitcnt lgkmcnt(0)\n\t" \
    "s_barrier\n\t" \
    "ds_read_b128 v[48:51], v4\n\t" \
    "ds_read_b128 v[52:55], v5\n\t" \
    "v_smfmac_f32_16x16x64_bf16 v[76:79], v[40:43], a[112:119], v10\n\t" \
    "ds_read_b128 v[56:59], v6\n\t" \
    "ds_read_b128 v[60:63], v7\n\t" \
    "v_smfmac_f32_16x16x64_bf16 v[80:83], v[40:43], a[184:191], v10\n\t" \
    "ds_read_b128 v[64:67], v8\n\t" \
    "v_smfmac_f32_16x16x64_bf16 v[84:87], v[40:43], v[224:231], v10\n\t" \
    "ds_read_b128 v[68:71], v9\n\t" \
    "s_waitcnt lgkmcnt(5)\n\t" \
    "v_smfmac_f32_16x16x64_bf16 v[72:75], v[48:51], a[48:55], v10\n\t" \
    "ds_read_b128 v[44:47], v0 offset:19456\n\t" \
    "s_waitcnt lgkmcnt(5)\n\t" \
    "v_smfmac_f32_16x16x64_bf16 v[72:75], v[52:55], a[56:63], v10\n\t" \
    "s_waitcnt lgkmcnt(4)\n\t" \
    "v_smfmac_f32_16x16x64_bf16 v[72:75], v[56:59], a[0:7], v10\n\t" \
    "s_waitcnt lgkmcnt(3)\n\t" \
    "v_smfmac_f32_16x16x64_bf16 v[72:75], v[60:63], a[8:15], v10\n\t" \
    "s_waitcnt lgkmcnt(2)\n\t" \
    "v_smfmac_f32_16x16x64_bf16 v[72:75], v[64:67], a[16:23], v10\n\t" \
    "s_waitcnt lgkmcnt(1)\n\t" \
    "v_smfmac_f32_16x16x64_bf16 v[72:75], v[68:71], a[24:31], v10\n\t" \
    "s_waitcnt lgkmcnt(0)\n\t" \
    "v_smfmac_f32_16x16x64_bf16 v[72:75], v[44:47], a[64:71], v10\n\t" \
    "s_nop 2\n\t" \
    "v_smfmac_f32_16x16x64_bf16 v[76:79], v[48:51], a[120:127], v10\n\t" \
    "s_nop 2\n\t" \
    "v_smfmac_f32_16x16x64_bf16 v[76:79], v[52:55], a[128:135], v10\n\t" \
    "v_add_f32 v72, v72, v74\n\t" \
    "v_smfmac_f32_16x16x64_bf16 v[76:79], v[56:59], a[72:79], v10\n\t" \
    "v_add_f32 v73, v73, v75\n\t" \
    "v_smfmac_f32_16x16x64_bf16 v[76:79], v[60:63], a[80:87], v10\n\t" \
    "v_cvt_pk_bf16_f32 v156, v72, v73\n\t" \
    "v_smfmac_f32_16x16x64_bf16 v[76:79], v[64:67], a[88:95], v10\n\t" \
    "ds_write_b32 v12, v156 offset:0\n\t" \
    "v_smfmac_f32_16x16x64_bf16 v[76:79], v[68:71], a[96:103], v10\n\t" \
    "v_smfmac_f32_16x16x64_bf16 v[76:79], v[44:47], a[136:143], v10\n\t" \
    "s_nop 2\n\t" \
    "v_smfmac_f32_16x16x64_bf16 v[80:83], v[48:51], a[192:199], v10\n\t" \
    "s_nop 2\n\t" \
    "v_smfmac_f32_16x16x64_bf16 v[80:83], v[52:55], a[200:207], v10\n\t" \
    "v_add_f32 v76, v76, v78\n\t" \
    "v_smfmac_f32_16x16x64_bf16 v[80:83], v[56:59], a[144:151], v10\n\t" \
    "v_add_f32 v77, v77, v79\n\t" \
    "v_smfmac_f32_16x16x64_bf16 v[80:83], v[60:63], a[152:159], v10\n\t" \
    "v_cvt_pk_bf16_f32 v157, v76, v77\n\t" \
    "v_smfmac_f32_16x16x64_bf16 v[80:83], v[64:67], a[160:167], v10\n\t" \
    "ds_write_b32 v12, v157 offset:64\n\t" \
    "v_smfmac_f32_16x16x64_bf16 v[80:83], v[68:71], a[168:175], v10\n\t" \
    "v_smfmac_f32_16x16x64_bf16 v[80:83], v[44:47], a[208:215], v10\n\t" \
    "s_nop 2\n\t" \
    "v_smfmac_f32_16x16x64_bf16 v[84:87], v[48:51], v[232:239], v10\n\t" \
    "s_nop 2\n\t" \
    "v_smfmac_f32_16x16x64_bf16 v[84:87], v[52:55], v[240:247], v10\n\t" \
    "v_add_f32 v80, v80, v82\n\t" \
    "v_smfmac_f32_16x16x64_bf16 v[84:87], v[56:59], a[216:223], v10\n\t" \
    "v_add_f32 v81, v81, v83\n\t" \
    "v_smfmac_f32_16x16x64_bf16 v[84:87], v[60:63], a[224:231], v10\n\t" \
    "v_cvt_pk_bf16_f32 v158, v80, v81\n\t" \
    "v_smfmac_f32_16x16x64_bf16 v[84:87], v[64:67], a[232:239], v10\n\t" \
    "ds_write_b32 v16, v158 offset:0\n\t" \
    "v_smfmac_f32_16x16x64_bf16 v[84:87], v[68:71], a[240:247], v10\n\t" \
    "v_smfmac_f32_16x16x64_bf16 v[84:87], v[44:47], v[248:255], v10\n\t" \
    "s_nop 6\n\t" \
    "v_cmp_u_f32 vcc, v72, v73\n\t" \
    "v_pk_add_f32 v[84:85], v[84:85], v[86:87]\n\t" \
    "v_cvt_pk_bf16_f32 v159, v84, v85\n\t" \
    "ds_write_b32 v16, v159 offset:256\n\t" \
    "s_cbranch_vccnz qk_Gslow71_%=\n\t" \
    "qk_Gback72_%=:\n\t" \
    "s_waitcnt lgkmcnt(0)\n\t" \
    "s_barrier\n\t" \
    "s_sleep 1\n\t" \
    "ds_read_b64_tr_b16 v[140:141], v17 offset:0\n\t" \
    "ds_read_b64_tr_b16 v[142:143], v17 offset:256\n\t" \
    "ds_read_b64_tr_b16 v[144:145], v17 offset:512\n\t" \
    "ds_read_b64_tr_b16 v[146:147], v17 offset:768\n\t" \
    "ds_read_b64_tr_b16 v[148:149], v17 offset:1024\n\t" \
    "ds_read_b64_tr_b16 v[150:151], v17 offset:1280\n\t" \
    "ds_read_b64_tr_b16 v[152:153], v17 offset:1536\n\t" \
    "ds_read_b64_tr_b16 v[154:155], v17 offset:1792\n\t" \
    "s_waitcnt lgkmcnt(0)\n\t" \
    "s_cmp_eq_u32 s65, 63\n\t" \
    "s_cbranch_scc1 qk_last61_%=\n\t" \
    "s_cmp_ge_u32 s65, 58\n\t" \
    "s_cbranch_scc1 qk_nsl74_%=\n\t" \
    "s_sleep 6\n\t" \
    "global_store_dword v14, v25, s[100:101] sc1\n\t" \
    "qk_nsl74_%=:\n\t" \
    "s_mov_b64 exec, s[58:59]\n\t" \
    "global_store_dwordx4 v0, v[140:143], s[40:41] offset:0 sc1\n\t" \
    "global_store_dwordx4 v0, v[144:147], s[40:41] offset:1024 sc1\n\t" \
    "global_store_dwordx4 v0, v[148:151], s[40:41] offset:2048 sc1\n\t" \
    "global_store_dwordx4 v0, v[152:155], s[40:41] offset:3072 sc1\n\t" \
    "s_mov_b64 exec, -1\n\t" \
    "s_add_u32 s65, s65, 1\n\t" \
    "v_mov_b32 v72, 0\n\t" \
    "v_mov_b32 v73, 0\n\t" \
    "v_mov_b32 v74, 0\n\t" \
    "v_mov_b32 v75, 0\n\t" \
    "v_mov_b32 v76, 0\n\t" \
    "v_mov_b32 v77, 0\n\t" \
    "v_mov_b32 v78, 0\n\t" \
    "v_mov_b32 v79, 0\n\t" \
    "v_mov_b32 v80, 0\n\t" \
    "v_mov_b32 v81, 0\n\t" \
    "v_mov_b32 v82, 0\n\t" \
    "v_mov_b32 v83, 0\n\t" \
    "v_mov_b32 v84, 0\n\t" \
    "v_mov_b32 v85, 0\n\t" \
    "v_mov_b32 v86, 0\n\t" \
    "v_mov_b32 v87, 0\n\t" \
    "s_add_u32 s69, s65, 0\n\t" \
    "s_and_b32 s69, s69, 7\n\t" \
    "s_lshl_b32 s69, s69, 18\n\t" \
    "s_add_u32 s69, s69, s62\n\t" \
    "s_add_u32 s46, s36, s69\n\t" \
    "s_addc_u32 s47, s37, 0\n\t" \
    "s_bitcmp0_b32 s65, 0\n\t" \
    "s_cselect_b32 s69, 16384, 0\n\t" \
    "v_xor_b32 v1, s69, v1\n\t" \
    "v_xor_b32 v2, s69, v2\n\t" \
    "v_xor_b32 v23, s69, v23\n\t" \
    "s_mul_i32 s70, s67, 7\n\t" \
    "s_add_u32 s69, s65, 0\n\t" \
    "s_xor_b32 s69, s69, s70\n\t" \
    "s_and_b32 s69, s69, 63\n\t" \
    "s_lshl_b32 s69, s69, 12\n\t" \
    "s_mul_i32 s70, s67, 262144\n\t" \
    "s_add_u32 s69, s69, s70\n\t" \
    "s_add_u32 s69, s69, 39845888\n\t" \
    "s_add_u32 s40, s36, s69\n\t" \
    "s_addc_u32 s41, s37, 0\n\t" \
    "s_sub_u32 s91, s65, 1\n\t" \
    "v_mov_b32 v25, s91\n\t" \
    "s_barrier\n\t" \
    "ds_read_b32 v168, v11 offset:20480\n\t" \
    "s_waitcnt lgkmcnt(0)\n\t" \
    "v_readfirstlane_b32 s70, v168\n\t" \
    "ds_write_b32 v12, v31 offset:0\n\t" \
    "ds_write_b32 v12, v31 offset:64\n\t" \
    "s_branch qk_go62_%=\n\t" \
    "qk_Gbad69_%=:\n\t" \
    "s_waitcnt vmcnt(0) lgkmcnt(0)\n\t" \
    "s_nop 7\n\t" \
    "v_mov_b32 v72, 0\n\t" \
    "v_mov_b32 v73, 0\n\t" \
    "v_mov_b32 v74, 0\n\t" \
    "v_mov_b32 v75, 0\n\t" \
    "v_mov_b32 v76, 0\n\t" \
    "v_mov_b32 v77, 0\n\t" \
    "v_mov_b32 v78, 0\n\t" \
    "v_mov_b32 v79, 0\n\t" \
    "v_mov_b32 v80, 0\n\t" \
    "v_mov_b32 v81, 0\n\t" \
    "v_mov_b32 v82, 0\n\t" \
    "v_mov_b32 v83, 0\n\t" \
    "v_mov_b32 v84, 0\n\t" \
    "v_mov_b32 v85, 0\n\t" \
    "v_mov_b32 v86, 0\n\t" \
    "v_mov_b32 v87, 0\n\t" \
    "s_cmp_eq_u32 s68, 0\n\t" \
    "s_cbranch_scc1 qk_F75_%=\n\t" \
    "s_sub_u32 s68, s68, 1\n\t" \
    "s_branch qk_G68_%=\n\t" \
    "qk_F75_%=:\n\t" \
    "v_mov_b32 v160, 5767424\n\t" \
    "v_mov_b32 v161, 1\n\t" \
    "global_store_dword v160, v161, s[36:37] sc1\n\t" \
    "s_waitcnt vmcnt(0)\n\t" \
    "s_branch qk_Gc70_%=\n\t" \
    "qk_Gslow71_%=:\n\t" \
    "v_max_u32 v160, v36, v37\n\t" \
    "v_max3_u32 v160, v160, v38, v39\n\t" \
    "v_max3_u32 v160, v160, v40, v41\n\t" \
    "v_max3_u32 v160, v160, v42, v43\n\t" \
    "v_max3_u32 v160, v160, v44, v45\n\t" \
    "v_max3_u32 v160, v160, v46, v47\n\t" \
    "v_max3_u32 v160, v160, v48, v49\n\t" \
    "v_max3_u32 v160, v160, v50, v51\n\t" \
    "v_max3_u32 v160, v160, v52, v53\n\t" \
    "v_max3_u32 v160, v160, v54, v55\n\t" \
    "v_max3_u32 v160, v160, v56, v57\n\t" \
    "v_max3_u32 v160, v160, v58, v59\n\t" \
    "v_max3_u32 v160, v160, v60, v61\n\t" \
    "v_max3_u32 v160, v160, v62, v63\n\t" \
    "v_max3_u32 v160, v160, v64, v65\n\t" \
    "v_max3_u32 v160, v160, v66, v67\n\t" \
    "v_max3_u32 v160, v160, v68, v69\n\t" \
    "v_max3_u32 v160, v160, v70, v71\n\t" \
    "v_cmp_eq_u32 vcc, -1, v160\n\t" \
    "s_cbranch_vccz qk_Gback72_%=\n\t" \
    "s_waitcnt lgkmcnt(0)\n\t" \
    "v_sub_u32 v160, v1, v3\n\t" \
    "s_nop 0\n\t" \
    "v_readfirstlane_b32 s69, v160\n\t" \
    "v_add_u32 v163, 36864, v0\n\t" \
    "s_nop 7\n\t" \
    "s_add_u32 s30, s46, s69\n\t" \
    "s_addc_u32 s31, s47, 0\n\t" \
    "qk_Gag76_%=:\n\t" \
    "global_load_dwordx4 v[36:39], v1, s[46:47] sc1\n\t" \
    "global_load_dwordx4 v[40:43], v23, s[46:47] sc1\n\t" \
    "global_load_dwordx4 v[44:47], v163, s[30:31] sc1\n\t" \
    "global_load_dwordx4 v[48:51], v4, s[30:31] sc1\n\t" \
    "global_load_dwordx4 v[52:55], v5, s[30:31] sc1\n\t" \
    "global_load_dwordx4 v[56:59], v6, s[30:31] sc1\n\t" \
    "global_load_dwordx4 v[60:63], v7, s[30:31] sc1\n\t" \
    "global_load_dwordx4 v[64:67], v8, s[30:31] sc1\n\t" \
    "global_load_dwordx4 v[68:71], v9, s[30:31] sc1\n\t" \
    "s_waitcnt vmcnt(0)\n\t" \
    "v_max_u32 v160, v36, v37\n\t" \
    "v_max3_u32 v160, v160, v38, v39\n\t" \
    "v_max3_u32 v160, v160, v40, v41\n\t" \
    "v_max3_u32 v160, v160, v42, v43\n\t" \
    "v_max3_u32 v160, v160, v44, v45\n\t" \
    "v_max3_u32 v160, v160, v46, v47\n\t" \
    "v_max3_u32 v160, v160, v48, v49\n\t" \
    "v_max3_u32 v160, v160, v50, v51\n\t" \
    "v_max3_u32 v160, v160, v52, v53\n\t" \
    "v_max3_u32 v160, v160, v54, v55\n\t" \
    "v_max3_u32 v160, v160, v56, v57\n\t" \
    "v_max3_u32 v160, v160, v58, v59\n\t" \
    "v_max3_u32 v160, v160, v60, v61\n\t" \
    "v_max3_u32 v160, v160, v62, v63\n\t" \
    "v_max3_u32 v160, v160, v64, v65\n\t" \
    "v_max3_u32 v160, v160, v66, v67\n\t" \
    "v_max3_u32 v160, v160, v68, v69\n\t" \
    "v_max3_u32 v160, v160, v70, v71\n\t" \
    "v_cmp_eq_u32 vcc, -1, v160\n\t" \
    "s_cbranch_vccz qk_Gredo77_%=\n\t" \
    "s_cmp_eq_u32 s68, 0\n\t" \
    "s_cbranch_scc1 qk_F78_%=\n\t" \
    "s_sub_u32 s68, s68, 1\n\t" \
    "s_sleep 1\n\t" \
    "s_branch qk_Gag76_%=\n\t" \
    "qk_F78_%=:\n\t" \
    "v_mov_b32 v160, 5767424\n\t" \
    "v_mov_b32 v161, 2\n\t" \
    "global_store_dword v160, v161, s[36:37] sc1\n\t" \
    "s_waitcnt vmcnt(0)\n\t" \
    "s_branch qk_Gredo77_%=\n\t" \
    "qk_Gredo77_%=:\n\t" \
    "v_mov_b32 v72, 0\n\t" \
    "v_mov_b32 v73, 0\n\t" \
    "v_mov_b32 v74, 0\n\t" \
    "v_mov_b32 v75, 0\n\t" \
    "v_mov_b32 v76, 0\n\t" \
    "v_mov_b32 v77, 0\n\t" \
    "v_mov_b32 v78, 0\n\t" \
    "v_mov_b32 v79, 0\n\t" \
    "v_mov_b32 v80, 0\n\t" \
    "v_mov_b32 v81, 0\n\t" \
    "v_mov_b32 v82, 0\n\t" \
    "v_mov_b32 v83, 0\n\t" \
    "v_mov_b32 v84, 0\n\t" \
    "v_mov_b32 v85, 0\n\t" \
    "v_mov_b32 v86, 0\n\t" \
    "v_mov_b32 v87, 0\n\t" \
    "v_smfmac_f32_16x16x64_bf16 v[72:75], v[36:39], a[32:39], v10\n\t" \
    "v_smfmac_f32_16x16x64_bf16 v[72:75], v[40:43], a[40:47], v10\n\t" \
    "v_smfmac_f32_16x16x64_bf16 v[72:75], v[48:51], a[48:55], v10\n\t" \
    "v_smfmac_f32_16x16x64_bf16 v[72:75], v[52:55], a[56:63], v10\n\t" \
    "v_smfmac_f32_16x16x64_bf16 v[72:75], v[56:59], a[0:7], v10\n\t" \
    "v_smfmac_f32_16x16x64_bf16 v[72:75], v[60:63], a[8:15], v10\n\t" \
    "v_smfmac_f32_16x16x64_bf16 v[72:75], v[64:67], a[16:23], v10\n\t" \
    "v_smfmac_f32_16x16x64_bf16 v[72:75], v[68:71], a[24:31], v10\n\t" \
    "v_smfmac_f32_16x16x64_bf16 v[72:75], v[44:47], a[64:71], v10\n\t" \
    "v_smfmac_f32_16x16x64_bf16 v[76:79], v[36:39], a[104:111], v10\n\t" \
    "v_smfmac_f32_16x16x64_bf16 v[76:79], v[40:43], a[112:119], v10\n\t" \
    "v_smfmac_f32_16x16x64_bf16 v[76:79], v[48:51], a[120:127], v10\n\t" \
    "v_smfmac_f32_16x16x64_bf16 v[76:79], v[52:55], a[128:135], v10\n\t" \
    "v_smfmac_f32_16x16x64_bf16 v[76:79], v[56:59], a[72:79], v10\n\t" \
    "v_smfmac_f32_16x16x64_bf16 v[76:79], v[60:63], a[80:87], v10\n\t" \
    "v_smfmac_f32_16x16x64_bf16 v[76:79], v[64:67], a[88:95], v10\n\t" \
    "v_smfmac_f32_16x16x64_bf16 v[76:79], v[68:71], a[96:103], v10\n\t" \
    "v_smfmac_f32_16x16x64_bf16 v[76:79], v[44:47], a[136:143], v10\n\t" \
    "v_smfmac_f32_16x16x64_bf16 v[80:83], v[36:39], a[176:183], v10\n\t" \
    "v_smfmac_f32_16x16x64_bf16 v[80:83], v[40:43], a[184:191], v10\n\t" \
    "v_smfmac_f32_16x16x64_bf16 v[80:83], v[48:51], a[192:199], v10\n\t" \
    "v_smfmac_f32_16x16x64_bf16 v[80:83], v[52:55], a[200:207], v10\n\t" \
    "v_smfmac_f32_16x16x64_bf16 v[80:83], v[56:59], a[144:151], v10\n\t" \
    "v_smfmac_f32_16x16x64_bf16 v[80:83], v[60:63], a[152:159], v10\n\t" \
    "v_smfmac_f32_16x16x64_bf16 v[80:83], v[64:67], a[160:167], v10\n\t" \
    "v_smfmac_f32_16x16x64_bf16 v[80:83], v[68:71], a[168:175], v10\n\t" \
    "v_smfmac_f32_16x16x64_bf16 v[80:83], v[44:47], a[208:215], v10\n\t" \
    "v_smfmac_f32_16x16x64_bf16 v[84:87], v[36:39], a[248:255], v10\n\t" \
    "v_smfmac_f32_16x16x64_bf16 v[84:87], v[40:43], v[224:231], v10\n\t" \
    "v_smfmac_f32_16x16x64_bf16 v[84:87], v[48:51], v[232:239], v10\n\t" \
    "v_smfmac_f32_16x16x64_bf16 v[84:87], v[52:55], v[240:247], v10\n\t" \
    "v_smfmac_f32_16x16x64_bf16 v[84:87], v[56:59], a[216:223], v10\n\t" \
    "v_smfmac_f32_16x16x64_bf16 v[84:87], v[60:63], a[224:231], v10\n\t" \
    "v_smfmac_f32_16x16x64_bf16 v[84:87], v[64:67], a[232:239], v10\n\t" \
    "v_smfmac_f32_16x16x64_bf16 v[84:87], v[68:71], a[240:247], v10\n\t" \
    "v_smfmac_f32_16x16x64_bf16 v[84:87], v[44:47], v[248:255], v10\n\t" \
    "v_add_f32 v72, v72, v74\n\t" \
    "v_add_f32 v73, v73, v75\n\t" \
    "v_cvt_pk_bf16_f32 v156, v72, v73\n\t" \
    "ds_write_b32 v12, v156 offset:0\n\t" \
    "v_add_f32 v76, v76, v78\n\t" \
    "v_add_f32 v77, v77, v79\n\t" \
    "v_cvt_pk_bf16_f32 v157, v76, v77\n\t" \
    "ds_write_b32 v12, v157 offset:64\n\t" \
    "v_add_f32 v80, v80, v82\n\t" \
    "v_add_f32 v81, v81, v83\n\t" \
    "v_cvt_pk_bf16_f32 v158, v80, v81\n\t" \
    "ds_write_b32 v16, v158 offset:0\n\t" \
    "v_pk_add_f32 v[84:85], v[84:85], v[86:87]\n\t" \
    "v_cvt_pk_bf16_f32 v159, v84, v85\n\t" \
    "ds_write_b32 v16, v159 offset:256\n\t" \
    "s_branch qk_Gback72_%=\n\t" \
    "qk_kf63_%=:\n\t" \
    "global_store_dword v14, v25, s[100:101] sc1\n\t" \
    "s_branch qk_kb64_%=\n\t" \
    "qk_last61_%=:\n\t" \
    "s_mov_b64 exec, s[58:59]\n\t" \
    "global_store_dwordx4 v0, v[140:143], s[40:41] offset:0 sc1\n\t" \
    "global_store_dwordx4 v0, v[144:147], s[40:41] offset:1024 sc1\n\t" \
    "global_store_dwordx4 v0, v[148:151], s[40:41] offset:2048 sc1\n\t" \
    "global_store_dwordx4 v0, v[152:155], s[40:41] offset:3072 sc1\n\t" \
    "s_mov_b64 exec, -1\n\t" \
    "s_endpgm\n\t" \
    "qk_loop16_%=:\n\t" \
    "s_mov_b32 s38, 0x1b8d00\n\t" \
    "s_mov_b32 s39, 0x0\n\t" \
    "v_mov_b32 v72, 0\n\t" \
    "v_mov_b32 v73, 0\n\t" \
    "v_mov_b32 v74, 0\n\t" \
    "v_mov_b32 v75, 0\n\t" \
    "v_mov_b32 v76, 0\n\t" \
    "v_mov_b32 v77, 0\n\t" \
    "v_mov_b32 v78, 0\n\t" \
    "v_mov_b32 v79, 0\n\t" \
    "v_mov_b32 v80, 0\n\t" \
    "v_mov_b32 v81, 0\n\t" \
    "v_mov_b32 v82, 0\n\t" \
    "v_mov_b32 v83, 0\n\t" \
    "v_mov_b32 v84, 0\n\t" \
    "v_mov_b32 v85, 0\n\t" \
    "v_mov_b32 v86, 0\n\t" \
    "v_mov_b32 v87, 0\n\t" \
    "s_add_u32 s69, s65, 0\n\t" \
    "s_and_b32 s69, s69, 7\n\t" \
    "s_lshl_b32 s69, s69, 18\n\t" \
    "s_add_u32 s69, s69, s62\n\t" \
    "s_add_u32 s46, s36, s69\n\t" \
    "s_addc_u32 s47, s37, 0\n\t" \
    "s_bitcmp0_b32 s65, 0\n\t" \
    "s_cselect_b32 s69, 16384, 0\n\t" \
    "v_xor_b32 v1, s69, v1\n\t" \
    "v_xor_b32 v2, s69, v2\n\t" \
    "v_xor_b32 v23, s69, v23\n\t" \
    "s_lshr_b64 s[38:39], s[38:39], 4\n\t" \
    "s_and_b32 s97, s38, 15\n\t" \
    "s_lshl_b32 s97, s97, 6\n\t" \
    "s_and_b32 s69, s65, 7\n\t" \
    "s_lshl_b32 s69, s69, 18\n\t" \
    "s_add_u32 s69, s69, s63\n\t" \
    "s_add_u32 s52, s36, s69\n\t" \
    "s_addc_u32 s53, s37, 0\n\t" \
    "s_sub_u32 s70, s79, s74\n\t" \
    "s_max_i32 s70, s70, 0\n\t" \
    "s_min_i32 s70, s70, 16000\n\t" \
    "s_lshl_b32 s70, s70, 1\n\t" \
    "s_mul_hi_u32 s70, s70, s77\n\t" \
    "s_add_u32 s70, s70, s73\n\t" \
    ".p2align 6\n\t" \
    "s_nop 0\n\t" \
    "s_nop 0\n\t" \
    "s_nop 0\n\t" \
    "s_nop 0\n\t" \
    "s_nop 0\n\t" \
    "s_nop 0\n\t" \
    "qk_go80_%=:\n\t" \
    "s_getpc_b64 s[30:31]\n\t" \
    "qk_Wp82_%=:\n\t" \
    "s_add_u32 s30, s30, qk_We83_%=-qk_Wp82_%=\n\t" \
    "s_addc_u32 s31, s31, 0\n\t" \
    "s_sub_u32 s70, s70, 96\n\t" \
    "qk_W81_%=:\n\t" \
    "s_memtime s[56:57]\n\t" \
    "s_waitcnt lgkmcnt(0)\n\t" \
    "s_sub_u32 s69, s70, s56\n\t" \
    "s_cmp_gt_i32 s69, 80\n\t" \
    "s_cbranch_scc1 qk_W81_%=\n\t" \
    "s_max_i32 s69, s69, 0\n\t" \
    "s_and_b32 s69, s69, -4\n\t" \
    "s_sub_u32 s30, s30, s69\n\t" \
    "s_subb_u32 s31, s31, 0\n\t" \
    "s_setpc_b64 s[30:31]\n\t" \
    "s_nop 0\n\t" \
    "s_nop 0\n\t" \
    "s_nop 0\n\t" \
    "s_nop 0\n\t" \
    "s_nop 0\n\t" \
    "s_nop 0\n\t" \
    "s_nop 0\n\t" \
    "s_nop 0\n\t" \
    "s_nop 0\n\t" \
    "s_nop 0\n\t" \
    "s_nop 0\n\t" \
    "s_nop 0\n\t" \
    "s_nop 0\n\t" \
    "s_nop 0\n\t" \
    "s_nop 0\n\t" \
    "s_nop 0\n\t" \
    "s_nop 0\n\t" \
    "s_nop 0\n\t" \
    "s_nop 0\n\t" \
    "s_nop 0\n\t" \
    "qk_We83_%=:\n\t" \
    "qk_G84_%=:\n\t" \
    "global_load_dwordx4 v[36:39], v1, s[46:47] sc1\n\t" \
    "global_load_dword v161, v2, s[46:47] sc1\n\t" \
    "global_load_dwordx4 v[40:43], v23, s[46:47] sc1\n\t" \
    "s_mul_hi_u32 s69, s84, s78\n\t" \
    "s_add_u32 s33, s74, s69\n\t" \
    "s_add_u32 s33, s33, s97\n\t" \
    "s_lshr_b32 s69, s90, 16\n\t" \
    "s_add_u32 s34, s33, s69\n\t" \
    "s_and_b32 s69, s90, 0xffff\n\t" \
    "s_sub_u32 s35, s80, s74\n\t" \
    "s_sub_u32 s35, s35, s69\n\t" \
    "s_add_u32 s99, s35, 240\n\t" \
    "s_add_u32 s98, s33, 448\n\t" \
    "s_sub_u32 s96, s79, s74\n\t" \
    "s_cmp_le_u32 s65, 0\n\t" \
    "s_cselect_b32 s69, 112, 0\n\t" \
    "s_add_u32 s69, s69, 448\n\t" \
    "s_add_u32 s96, s96, s69\n\t" \
    "s_max_i32 s35, s35, s96\n\t" \
    "s_add_u32 s99, s35, 716\n\t" \
    "s_mul_i32 s69, s35, 10\n\t" \
    "s_ashr_i32 s69, s69, 4\n\t" \
    "s_sub_u32 s69, s33, s69\n\t" \
    "v_lshrrev_b32_e64 v163, 1, s69\n\t" \
    "s_waitcnt vmcnt(2)\n\t" \
    "qk_Garr89_%=:\n\t" \
    "ds_write_b128 v3, v[36:39]\n\t" \
    "v_smfmac_f32_16x16x64_bf16 v[72:75], v[36:39], a[48:55], v10\n\t" \
    "s_waitcnt vmcnt(1)\n\t" \
    "ds_write_b32 v22, v161 offset:20224\n\t" \
    "v_smfmac_f32_16x16x64_bf16 v[76:79], v[36:39], a[120:127], v10\n\t" \
    "s_waitcnt vmcnt(0)\n\t" \
    "ds_write_b128 v3, v[40:43] offset:1024\n\t" \
    "v_max3_u32 v160, v36, v40, v161\n\t" \
    "v_cmp_eq_u32 vcc, -1, v160\n\t" \
    "v_smfmac_f32_16x16x64_bf16 v[80:83], v[36:39], a[192:199], v10\n\t" \
    "v_smfmac_f32_16x16x64_bf16 v[84:87], v[36:39], v[232:239], v10\n\t" \
    "v_smfmac_f32_16x16x64_bf16 v[72:75], v[40:43], a[56:63], v10\n\t" \
    "s_cbranch_vccnz qk_Gbad85_%=\n\t" \
    "qk_Gc86_%=:\n\t" \
    "s_waitcnt lgkmcnt(0)\n\t" \
    "s_barrier\n\t" \
    "s_memtime s[56:57]\n\t" \
    "ds_read_b128 v[48:51], v4\n\t" \
    "ds_read_b128 v[52:55], v5\n\t" \
    "v_smfmac_f32_16x16x64_bf16 v[76:79], v[40:43], a[128:135], v10\n\t" \
    "ds_read_b128 v[56:59], v6\n\t" \
    "ds_read_b128 v[60:63], v7\n\t" \
    "v_smfmac_f32_16x16x64_bf16 v[80:83], v[40:43], a[200:207], v10\n\t" \
    "ds_read_b128 v[64:67], v8\n\t" \
    "ds_read_b128 v[68:71], v9\n\t" \
    "v_smfmac_f32_16x16x64_bf16 v[84:87], v[40:43], v[240:247], v10\n\t" \
    "ds_read_b128 v[44:47], v0 offset:19456\n\t" \
    "s_waitcnt lgkmcnt(6)\n\t" \
    "v_smfmac_f32_16x16x64_bf16 v[72:75], v[48:51], a[0:7], v10\n\t" \
    "s_sub_u32 s69, s56, s73\n\t" \
    "s_mul_hi_u32 s69, s69, s78\n\t" \
    "s_waitcnt lgkmcnt(5)\n\t" \
    "v_smfmac_f32_16x16x64_bf16 v[72:75], v[52:55], a[8:15], v10\n\t" \
    "s_max_i32 s69, s69, s96\n\t" \
    "s_max_i32 s70, s69, s35\n\t" \
    "s_waitcnt lgkmcnt(4)\n\t" \
    "v_smfmac_f32_16x16x64_bf16 v[72:75], v[56:59], a[16:23], v10\n\t" \
    "s_min_i32 s70, s70, s99\n\t" \
    "s_mul_i32 s70, s70, 10\n\t" \
    "s_waitcnt lgkmcnt(3)\n\t" \
    "v_smfmac_f32_16x16x64_bf16 v[72:75], v[60:63], a[24:31], v10\n\t" \
    "s_lshl4_add_u32 s70, s69, s70\n\t" \
    "s_ashr_i32 s70, s70, 5\n\t" \
    "v_add_lshl_u32 v162, s70, v163, 1\n\t" \
    "s_waitcnt lgkmcnt(2)\n\t" \
    "v_smfmac_f32_16x16x64_bf16 v[72:75], v[64:67], a[32:39], v10\n\t" \
    "global_store_dword v21, v162, s[52:53] sc1\n\t" \
    "s_waitcnt lgkmcnt(1)\n\t" \
    "v_smfmac_f32_16x16x64_bf16 v[72:75], v[68:71], a[40:47], v10\n\t" \
    "s_waitcnt lgkmcnt(0)\n\t" \
    "v_smfmac_f32_16x16x64_bf16 v[72:75], v[44:47], a[64:71], v10\n\t" \
    "s_nop 2\n\t" \
    "v_smfmac_f32_16x16x64_bf16 v[76:79], v[48:51], a[72:79], v10\n\t" \
    "s_nop 2\n\t" \
    "v_smfmac_f32_16x16x64_bf16 v[76:79], v[52:55], a[80:87], v10\n\t" \
    "v_add_f32 v72, v72, v74\n\t" \
    "v_smfmac_f32_16x16x64_bf16 v[76:79], v[56:59], a[88:95], v10\n\t" \
    "v_add_f32 v73, v73, v75\n\t" \
    "v_smfmac_f32_16x16x64_bf16 v[76:79], v[60:63], a[96:103], v10\n\t" \
    "v_cvt_pk_bf16_f32 v156, v72, v73\n\t" \
    "v_smfmac_f32_16x16x64_bf16 v[76:79], v[64:67], a[104:111], v10\n\t" \
    "ds_write_b32 v12, v156 offset:0\n\t" \
    "v_smfmac_f32_16x16x64_bf16 v[76:79], v[68:71], a[112:119], v10\n\t" \
    "v_smfmac_f32_16x16x64_bf16 v[76:79], v[44:47], a[136:143], v10\n\t" \
    "s_nop 2\n\t" \
    "v_smfmac_f32_16x16x64_bf16 v[80:83], v[48:51], a[144:151], v10\n\t" \
    "s_nop 2\n\t" \
    "v_smfmac_f32_16x16x64_bf16 v[80:83], v[52:55], a[152:159], v10\n\t" \
    "v_add_f32 v76, v76, v78\n\t" \
    "v_smfmac_f32_16x16x64_bf16 v[80:83], v[56:59], a[160:167], v10\n\t" \
    "v_add_f32 v77, v77, v79\n\t" \
    "v_smfmac_f32_16x16x64_bf16 v[80:83], v[60:63], a[168:175], v10\n\t" \
    "v_cvt_pk_bf16_f32 v157, v76, v77\n\t" \
    "v_smfmac_f32_16x16x64_bf16 v[80:83], v[64:67], a[176:183], v10\n\t" \
    "ds_write_b32 v12, v157 offset:64\n\t" \
    "v_smfmac_f32_16x16x64_bf16 v[80:83], v[68:71], a[184:191], v10\n\t" \
    "v_smfmac_f32_16x16x64_bf16 v[80:83], v[44:47], a[208:215], v10\n\t" \
    "s_nop 2\n\t" \
    "v_smfmac_f32_16x16x64_bf16 v[84:87], v[48:51], a[216:223], v10\n\t" \
    "s_nop 2\n\t" \
    "v_smfmac_f32_16x16x64_bf16 v[84:87], v[52:55], a[224:231], v10\n\t" \
    "v_add_f32 v80, v80, v82\n\t" \
    "v_smfmac_f32_16x16x64_bf16 v[84:87], v[56:59], a[232:239], v10\n\t" \
    "v_add_f32 v81, v81, v83\n\t" \
    "v_smfmac_f32_16x16x64_bf16 v[84:87], v[60:63], a[240:247], v10\n\t" \
    "v_cvt_pk_bf16_f32 v158, v80, v81\n\t" \
    "v_smfmac_f32_16x16x64_bf16 v[84:87], v[64:67], a[248:255], v10\n\t" \
    "ds_write_b32 v16, v158 offset:0\n\t" \
    "v_smfmac_f32_16x16x64_bf16 v[84:87], v[68:71], v[224:231], v10\n\t" \
    "v_smfmac_f32_16x16x64_bf16 v[84:87], v[44:47], v[248:255], v10\n\t" \
    "s_nop 6\n\t" \
    "v_cmp_u_f32 vcc, v72, v73\n\t" \
    "v_pk_add_f32 v[84:85], v[84:85], v[86:87]\n\t" \
    "v_cvt_pk_bf16_f32 v159, v84, v85\n\t" \
    "ds_write_b32 v16, v159 offset:256\n\t" \
    "s_cbranch_vccnz qk_Gslow87_%=\n\t" \
    "qk_Gback88_%=:\n\t" \
    "s_waitcnt lgkmcnt(0)\n\t" \
    "s_barrier\n\t" \
    "s_cmp_eq_u32 s65, 63\n\t" \
    "s_cbranch_scc1 qk_last79_%=\n\t" \
    "s_cmp_lg_u32 s65, 0\n\t" \
    "s_cbranch_scc1 qk_ng90_%=\n\t" \
    "v_mov_b32 v21, v33\n\t" \
    "qk_ng90_%=:\n\t" \
    "s_sub_u32 s70, s80, s74\n\t" \
    "s_max_i32 s70, s70, 0\n\t" \
    "s_min_i32 s70, s70, 16000\n\t" \
    "s_lshl_b32 s70, s70, 1\n\t" \
    "s_mul_hi_u32 s70, s70, s77\n\t" \
    "s_add_u32 s70, s70, s73\n\t" \
    "qk_W91_%=:\n\t" \
    "s_memtime s[56:57]\n\t" \
    "s_waitcnt lgkmcnt(0)\n\t" \
    "s_sub_u32 s69, s70, s56\n\t" \
    "s_cmp_gt_i32 s69, 24\n\t" \
    "s_cbranch_scc1 qk_W91_%=\n\t" \
    "global_load_dword v160, v20, s[52:53] sc1\n\t" \
    "s_waitcnt lgkmcnt(0)\n\t" \
    "s_mov_b32 s70, 116\n\t" \
    "qk_E95_%=:\n\t" \
    "s_memtime s[56:57]\n\t" \
    "s_memrealtime s[2:3]\n\t" \
    "s_nop 0\n\t" \
    "s_memrealtime s[4:5]\n\t" \
    "s_nop 0\n\t" \
    "s_memrealtime s[6:7]\n\t" \
    "s_nop 0\n\t" \
    "s_memrealtime s[8:9]\n\t" \
    "s_nop 0\n\t" \
    "s_memrealtime s[10:11]\n\t" \
    "s_nop 0\n\t" \
    "s_memrealtime s[12:13]\n\t" \
    "s_nop 0\n\t" \
    "s_memrealtime s[14:15]\n\t" \
    "s_nop 0\n\t" \
    "s_memrealtime s[16:17]\n\t" \
    "s_nop 0\n\t" \
    "s_memrealtime s[18:19]\n\t" \
    "s_nop 0\n\t" \
    "s_memrealtime s[20:21]\n\t" \
    "s_nop 0\n\t" \
    "s_memrealtime s[22:23]\n\t" \
    "s_nop 0\n\t" \
    "s_memrealtime s[24:25]\n\t" \
    "s_nop 0\n\t" \
    "s_memrealtime s[26:27]\n\t" \
    "s_nop 0\n\t" \
    "s_memrealtime s[28:29]\n\t" \
    "s_memtime s[54:55]\n\t" \
    "s_add_u32 s70, s70, 8\n\t" \
    "s_waitcnt lgkmcnt(0)\n\t" \
    "s_sub_u32 s69, s54, s56\n\t" \
    "s_cmp_gt_u32 s69, s70\n\t" \
    "s_cbranch_scc1 qk_E95_%=\n\t" \
    "s_add_u32 s2, s2, s4\n\t" \
    "s_add_u32 s6, s6, s8\n\t" \
    "s_add_u32 s10, s10, s12\n\t" \
    "s_add_u32 s14, s14, s16\n\t" \
    "s_add_u32 s18, s18, s20\n\t" \
    "s_add_u32 s22, s22, s24\n\t" \
    "s_add_u32 s2, s2, s6\n\t" \
    "s_add_u32 s10, s10, s14\n\t" \
    "s_add_u32 s18, s18, s22\n\t" \
    "s_add_u32 s2, s2, s26\n\t" \
    "s_add_u32 s10, s10, s18\n\t" \
    "s_add_u32 s2, s2, s10\n\t" \
    "s_mul_i32 s70, s28, 13\n\t" \
    "s_sub_u32 s70, s70, s2\n\t" \
    "s_min_u32 s70, s70, 52\n\t" \
    "s_mul_i32 s69, s69, s70\n\t" \
    "s_mul_hi_u32 s69, s69, 76695844\n\t" \
    "s_mov_b32 s75, s73\n\t" \
    "s_mov_b32 s76, s74\n\t" \
    "s_add_u32 s73, s56, s69\n\t" \
    "s_lshl_b32 s74, s28, 4\n\t" \
    "s_sub_u32 s69, s73, s75\n\t" \
    "s_sub_u32 s70, s74, s76\n\t" \
    "s_sub_u32 s71, s70, 1600\n\t" \
    "s_cmp_lt_u32 s71, 400000\n\t" \
    "s_cbranch_scc0 qk_K96_%=\n\t" \
    "v_cvt_f32_u32 v168, s69\n\t" \
    "v_cvt_f32_u32 v169, s70\n\t" \
    "v_rcp_f32 v170, v168\n\t" \
    "v_rcp_f32 v171, v169\n\t" \
    "v_mul_f32 v170, v170, v169\n\t" \
    "v_mul_f32 v171, v171, v168\n\t" \
    "v_mul_f32 v170, 0x4f800000, v170\n\t" \
    "v_mul_f32 v171, 0x4f000000, v171\n\t" \
    "v_cvt_u32_f32 v170, v170\n\t" \
    "v_cvt_u32_f32 v171, v171\n\t" \
    "v_readfirstlane_b32 s69, v170\n\t" \
    "v_readfirstlane_b32 s70, v171\n\t" \
    "s_nop 1\n\t" \
    "s_min_u32 s71, s65, 1\n\t" \
    "s_sub_u32 s69, s69, s78\n\t" \
    "s_ashr_i32 s69, s69, s71\n\t" \
    "s_add_u32 s78, s78, s69\n\t" \
    "s_sub_u32 s70, s70, s77\n\t" \
    "s_ashr_i32 s70, s70, s71\n\t" \
    "s_add_u32 s77, s77, s70\n\t" \
    "qk_K96_%=:\n\t" \
    "s_add_u32 s65, s65, 1\n\t" \
    "v_mov_b32 v72, 0\n\t" \
    "v_mov_b32 v73, 0\n\t" \
    "v_mov_b32 v74, 0\n\t" \
    "v_mov_b32 v75, 0\n\t" \
    "v_mov_b32 v76, 0\n\t" \
    "v_mov_b32 v77, 0\n\t" \
    "v_mov_b32 v78, 0\n\t" \
    "v_mov_b32 v79, 0\n\t" \
    "v_mov_b32 v80, 0\n\t" \
    "v_mov_b32 v81, 0\n\t" \
    "v_mov_b32 v82, 0\n\t" \
    "v_mov_b32 v83, 0\n\t" \
    "v_mov_b32 v84, 0\n\t" \
    "v_mov_b32 v85, 0\n\t" \
    "v_mov_b32 v86, 0\n\t" \
    "v_mov_b32 v87, 0\n\t" \
    "s_add_u32 s69, s65, 0\n\t" \
    "s_and_b32 s69, s69, 7\n\t" \
    "s_lshl_b32 s69, s69, 18\n\t" \
    "s_add_u32 s69, s69, s62\n\t" \
    "s_add_u32 s46, s36, s69\n\t" \
    "s_addc_u32 s47, s37, 0\n\t" \
    "s_bitcmp0_b32 s65, 0\n\t" \
    "s_cselect_b32 s69, 16384, 0\n\t" \
    "v_xor_b32 v1, s69, v1\n\t" \
    "v_xor_b32 v2, s69, v2\n\t" \
    "v_xor_b32 v23, s69, v23\n\t" \
    "s_lshr_b64 s[38:39], s[38:39], 4\n\t" \
    "s_and_b32 s97, s38, 15\n\t" \
    "s_lshl_b32 s97, s97, 6\n\t" \
    "s_waitcnt vmcnt(0)\n\t" \
    "v_cmp_eq_u32 vcc, -1, v160\n\t" \
    "s_cbranch_vccz qk_Hok93_%=\n\t" \
    "qk_H92_%=:\n\t" \
    "global_load_dword v160, v20, s[52:53] sc1\n\t" \
    "s_waitcnt vmcnt(0)\n\t" \
    "v_cmp_eq_u32 vcc, -1, v160\n\t" \
    "s_cbranch_vccz qk_Hc94_%=\n\t" \
    "s_cmp_eq_u32 s68, 0\n\t" \
    "s_cbranch_scc1 qk_F97_%=\n\t" \
    "s_sub_u32 s68, s68, 1\n\t" \
    "s_sleep 2\n\t" \
    "s_branch qk_H92_%=\n\t" \
    "qk_F97_%=:\n\t" \
    "v_mov_b32 v161, 5767424\n\t" \
    "v_mov_b32 v162, 5\n\t" \
    "global_store_dword v161, v162, s[36:37] sc1\n\t" \
    "s_waitcnt vmcnt(0)\n\t" \
    "s_branch qk_Hc94_%=\n\t" \
    "qk_Hc94_%=:\n\t" \
    "s_memtime s[56:57]\n\t" \
    "s_waitcnt lgkmcnt(0)\n\t" \
    "s_sub_u32 s69, s56, s73\n\t" \
    "s_cmp_lt_u32 s69, 3000\n\t" \
    "s_cbranch_scc1 qk_St98_%=\n\t" \
    "s_waitcnt lgkmcnt(0)\n\t" \
    "s_mov_b32 s70, 116\n\t" \
    "qk_E99_%=:\n\t" \
    "s_memtime s[56:57]\n\t" \
    "s_memrealtime s[2:3]\n\t" \
    "s_nop 0\n\t" \
    "s_memrealtime s[4:5]\n\t" \
    "s_nop 0\n\t" \
    "s_memrealtime s[6:7]\n\t" \
    "s_nop 0\n\t" \
    "s_memrealtime s[8:9]\n\t" \
    "s_nop 0\n\t" \
    "s_memrealtime s[10:11]\n\t" \
    "s_nop 0\n\t" \
    "s_memrealtime s[12:13]\n\t" \
    "s_nop 0\n\t" \
    "s_memrealtime s[14:15]\n\t" \
    "s_nop 0\n\t" \
    "s_memrealtime s[16:17]\n\t" \
    "s_nop 0\n\t" \
    "s_memrealtime s[18:19]\n\t" \
    "s_nop 0\n\t" \
    "s_memrealtime s[20:21]\n\t" \
    "s_nop 0\n\t" \
    "s_memrealtime s[22:23]\n\t" \
    "s_nop 0\n\t" \
    "s_memrealtime s[24:25]\n\t" \
    "s_nop 0\n\t" \
    "s_memrealtime s[26:27]\n\t" \
    "s_nop 0\n\t" \
    "s_memrealtime s[28:29]\n\t" \
    "s_memtime s[54:55]\n\t" \
    "s_add_u32 s70, s70, 8\n\t" \
    "s_waitcnt lgkmcnt(0)\n\t" \
    "s_sub_u32 s69, s54, s56\n\t" \
    "s_cmp_gt_u32 s69, s70\n\t" \
    "s_cbranch_scc1 qk_E99_%=\n\t" \
    "s_add_u32 s2, s2, s4\n\t" \
    "s_add_u32 s6, s6, s8\n\t" \
    "s_add_u32 s10, s10, s12\n\t" \
    "s_add_u32 s14, s14, s16\n\t" \
    "s_add_u32 s18, s18, s20\n\t" \
    "s_add_u32 s22, s22, s24\n\t" \
    "s_add_u32 s2, s2, s6\n\t" \
    "s_add_u32 s10, s10, s14\n\t" \
    "s_add_u32 s18, s18, s22\n\t" \
    "s_add_u32 s2, s2, s26\n\t" \
    "s_add_u32 s10, s10, s18\n\t" \
    "s_add_u32 s2, s2, s10\n\t" \
    "s_mul_i32 s70, s28, 13\n\t" \
    "s_sub_u32 s70, s70, s2\n\t" \
    "s_min_u32 s70, s70, 52\n\t" \
    "s_mul_i32 s69, s69, s70\n\t" \
    "s_mul_hi_u32 s69, s69, 76695844\n\t" \
    "s_mov_b32 s75, s73\n\t" \
    "s_mov_b32 s76, s74\n\t" \
    "s_add_u32 s73, s56, s69\n\t" \
    "s_lshl_b32 s74, s28, 4\n\t" \
    "qk_St98_%=:\n\t" \
    "qk_Hok93_%=:\n\t" \
    "v_max_u32_dpp v160, v160, v160 row_ror:1 row_mask:0xf bank_mask:0xf\n\t" \
    "s_nop 1\n\t" \
    "v_max_u32_dpp v160, v160, v160 row_ror:2 row_mask:0xf bank_mask:0xf\n\t" \
    "s_nop 1\n\t" \
    "v_max_u32_dpp v160, v160, v160 row_ror:4 row_mask:0xf bank_mask:0xf\n\t" \
    "s_nop 1\n\t" \
    "v_max_u32_dpp v160, v160, v160 row_ror:8 row_mask:0xf bank_mask:0xf\n\t" \
    "s_nop 0\n\t" \
    "v_readlane_b32 s69, v160, 0\n\t" \
    "v_readlane_b32 s71, v160, 16\n\t" \
    "s_nop 1\n\t" \
    "s_max_u32 s81, s69, s71\n\t" \
    "s_mul_hi_u32 s69, s85, s78\n\t" \
    "s_add_u32 s79, s81, s69\n\t" \
    "s_add_u32 s79, s79, s87\n\t" \
    "s_cmp_lt_u32 s65, 6\n\t" \
    "s_cselect_b32 s69, 48, 0\n\t" \
    "s_add_u32 s79, s79, s69\n\t" \
    "s_add_u32 s80, s79, s88\n\t" \
    "s_sub_u32 s70, s79, s74\n\t" \
    "s_max_i32 s70, s70, 0\n\t" \
    "s_min_i32 s70, s70, 16000\n\t" \
    "s_lshl_b32 s70, s70, 1\n\t" \
    "s_mul_hi_u32 s70, s70, s77\n\t" \
    "s_add_u32 s70, s70, s73\n\t" \
    "v_mov_b32 v168, s70\n\t" \
    "ds_write_b32 v11, v168 offset:20480\n\t" \
    "s_and_b32 s69, s65, 7\n\t" \
    "s_lshl_b32 s69, s69, 18\n\t" \
    "s_add_u32 s69, s69, s63\n\t" \
    "s_add_u32 s52, s36, s69\n\t" \
    "s_addc_u32 s53, s37, 0\n\t" \
    "s_waitcnt lgkmcnt(0)\n\t" \
    "s_barrier\n\t" \
    "ds_write_b32 v12, v31 offset:0\n\t" \
    "ds_write_b32 v12, v31 offset:64\n\t" \
    "s_branch qk_go80_%=\n\t" \
    "qk_Gbad85_%=:\n\t" \
    "s_waitcnt vmcnt(0) lgkmcnt(0)\n\t" \
    "s_nop 7\n\t" \
    "v_mov_b32 v72, 0\n\t" \
    "v_mov_b32 v73, 0\n\t" \
    "v_mov_b32 v74, 0\n\t" \
    "v_mov_b32 v75, 0\n\t" \
    "v_mov_b32 v76, 0\n\t" \
    "v_mov_b32 v77, 0\n\t" \
    "v_mov_b32 v78, 0\n\t" \
    "v_mov_b32 v79, 0\n\t" \
    "v_mov_b32 v80, 0\n\t" \
    "v_mov_b32 v81, 0\n\t" \
    "v_mov_b32 v82, 0\n\t" \
    "v_mov_b32 v83, 0\n\t" \
    "v_mov_b32 v84, 0\n\t" \
    "v_mov_b32 v85, 0\n\t" \
    "v_mov_b32 v86, 0\n\t" \
    "v_mov_b32 v87, 0\n\t" \
    "s_cmp_eq_u32 s68, 0\n\t" \
    "s_cbranch_scc1 qk_F100_%=\n\t" \
    "s_sub_u32 s68, s68, 1\n\t" \
    "s_branch qk_G84_%=\n\t" \
    "qk_F100_%=:\n\t" \
    "v_mov_b32 v160, 5767424\n\t" \
    "v_mov_b32 v161, 1\n\t" \
    "global_store_dword v160, v161, s[36:37] sc1\n\t" \
    "s_waitcnt vmcnt(0)\n\t" \
    "s_branch qk_Gc86_%=\n\t" \
    "qk_Gslow87_%=:\n\t" \
    "v_max_u32 v160, v36, v37\n\t" \
    "v_max3_u32 v160, v160, v38, v39\n\t" \
    "v_max3_u32 v160, v160, v40, v41\n\t" \
    "v_max3_u32 v160, v160, v42, v43\n\t" \
    "v_max3_u32 v160, v160, v44, v45\n\t" \
    "v_max3_u32 v160, v160, v46, v47\n\t" \
    "v_max3_u32 v160, v160, v48, v49\n\t" \
    "v_max3_u32 v160, v160, v50, v51\n\t" \
    "v_max3_u32 v160, v160, v52, v53\n\t" \
    "v_max3_u32 v160, v160, v54, v55\n\t" \
    "v_max3_u32 v160, v160, v56, v57\n\t" \
    "v_max3_u32 v160, v160, v58, v59\n\t" \
    "v_max3_u32 v160, v160, v60, v61\n\t" \
    "v_max3_u32 v160, v160, v62, v63\n\t" \
    "v_max3_u32 v160, v160, v64, v65\n\t" \
    "v_max3_u32 v160, v160, v66, v67\n\t" \
    "v_max3_u32 v160, v160, v68, v69\n\t" \
    "v_max3_u32 v160, v160, v70, v71\n\t" \
    "v_cmp_eq_u32 vcc, -1, v160\n\t" \
    "s_cbranch_vccz qk_Gback88_%=\n\t" \
    "s_waitcnt lgkmcnt(0)\n\t" \
    "v_sub_u32 v160, v1, v3\n\t" \
    "s_nop 0\n\t" \
    "v_readfirstlane_b32 s69, v160\n\t" \
    "v_add_u32 v163, 36864, v0\n\t" \
    "s_nop 7\n\t" \
    "s_add_u32 s30, s46, s69\n\t" \
    "s_addc_u32 s31, s47, 0\n\t" \
    "qk_Gag101_%=:\n\t" \
    "global_load_dwordx4 v[36:39], v1, s[46:47] sc1\n\t" \
    "global_load_dwordx4 v[40:43], v23, s[46:47] sc1\n\t" \
    "global_load_dwordx4 v[44:47], v163, s[30:31] sc1\n\t" \
    "global_load_dwordx4 v[48:51], v4, s[30:31] sc1\n\t" \
    "global_load_dwordx4 v[52:55], v5, s[30:31] sc1\n\t" \
    "global_load_dwordx4 v[56:59], v6, s[30:31] sc1\n\t" \
    "global_load_dwordx4 v[60:63], v7, s[30:31] sc1\n\t" \
    "global_load_dwordx4 v[64:67], v8, s[30:31] sc1\n\t" \
    "global_load_dwordx4 v[68:71], v9, s[30:31] sc1\n\t" \
    "s_waitcnt vmcnt(0)\n\t" \
    "v_max_u32 v160, v36, v37\n\t" \
    "v_max3_u32 v160, v160, v38, v39\n\t" \
    "v_max3_u32 v160, v160, v40, v41\n\t" \
    "v_max3_u32 v160, v160, v42, v43\n\t" \
    "v_max3_u32 v160, v160, v44, v45\n\t" \
    "v_max3_u32 v160, v160, v46, v47\n\t" \
    "v_max3_u32 v160, v160, v48, v49\n\t" \
    "v_max3_u32 v160, v160, v50, v51\n\t" \
    "v_max3_u32 v160, v160, v52, v53\n\t" \
    "v_max3_u32 v160, v160, v54, v55\n\t" \
    "v_max3_u32 v160, v160, v56, v57\n\t" \
    "v_max3_u32 v160, v160, v58, v59\n\t" \
    "v_max3_u32 v160, v160, v60, v61\n\t" \
    "v_max3_u32 v160, v160, v62, v63\n\t" \
    "v_max3_u32 v160, v160, v64, v65\n\t" \
    "v_max3_u32 v160, v160, v66, v67\n\t" \
    "v_max3_u32 v160, v160, v68, v69\n\t" \
    "v_max3_u32 v160, v160, v70, v71\n\t" \
    "v_cmp_eq_u32 vcc, -1, v160\n\t" \
    "s_cbranch_vccz qk_Gredo102_%=\n\t" \
    "s_cmp_eq_u32 s68, 0\n\t" \
    "s_cbranch_scc1 qk_F103_%=\n\t" \
    "s_sub_u32 s68, s68, 1\n\t" \
    "s_sleep 1\n\t" \
    "s_branch qk_Gag101_%=\n\t" \
    "qk_F103_%=:\n\t" \
    "v_mov_b32 v160, 5767424\n\t" \
    "v_mov_b32 v161, 2\n\t" \
    "global_store_dword v160, v161, s[36:37] sc1\n\t" \
    "s_waitcnt vmcnt(0)\n\t" \
    "s_branch qk_Gredo102_%=\n\t" \
    "qk_Gredo102_%=:\n\t" \
    "v_mov_b32 v72, 0\n\t" \
    "v_mov_b32 v73, 0\n\t" \
    "v_mov_b32 v74, 0\n\t" \
    "v_mov_b32 v75, 0\n\t" \
    "v_mov_b32 v76, 0\n\t" \
    "v_mov_b32 v77, 0\n\t" \
    "v_mov_b32 v78, 0\n\t" \
    "v_mov_b32 v79, 0\n\t" \
    "v_mov_b32 v80, 0\n\t" \
    "v_mov_b32 v81, 0\n\t" \
    "v_mov_b32 v82, 0\n\t" \
    "v_mov_b32 v83, 0\n\t" \
    "v_mov_b32 v84, 0\n\t" \
    "v_mov_b32 v85, 0\n\t" \
    "v_mov_b32 v86, 0\n\t" \
    "v_mov_b32 v87, 0\n\t" \
    "v_smfmac_f32_16x16x64_bf16 v[72:75], v[36:39], a[48:55], v10\n\t" \
    "v_smfmac_f32_16x16x64_bf16 v[72:75], v[40:43], a[56:63], v10\n\t" \
    "v_smfmac_f32_16x16x64_bf16 v[72:75], v[48:51], a[0:7], v10\n\t" \
    "v_smfmac_f32_16x16x64_bf16 v[72:75], v[52:55], a[8:15], v10\n\t" \
    "v_smfmac_f32_16x16x64_bf16 v[72:75], v[56:59], a[16:23], v10\n\t" \
    "v_smfmac_f32_16x16x64_bf16 v[72:75], v[60:63], a[24:31], v10\n\t" \
    "v_smfmac_f32_16x16x64_bf16 v[72:75], v[64:67], a[32:39], v10\n\t" \
    "v_smfmac_f32_16x16x64_bf16 v[72:75], v[68:71], a[40:47], v10\n\t" \
    "v_smfmac_f32_16x16x64_bf16 v[72:75], v[44:47], a[64:71], v10\n\t" \
    "v_smfmac_f32_16x16x64_bf16 v[76:79], v[36:39], a[120:127], v10\n\t" \
    "v_smfmac_f32_16x16x64_bf16 v[76:79], v[40:43], a[128:135], v10\n\t" \
    "v_smfmac_f32_16x16x64_bf16 v[76:79], v[48:51], a[72:79], v10\n\t" \
    "v_smfmac_f32_16x16x64_bf16 v[76:79], v[52:55], a[80:87], v10\n\t" \
    "v_smfmac_f32_16x16x64_bf16 v[76:79], v[56:59], a[88:95], v10\n\t" \
    "v_smfmac_f32_16x16x64_bf16 v[76:79], v[60:63], a[96:103], v10\n\t" \
    "v_smfmac_f32_16x16x64_bf16 v[76:79], v[64:67], a[104:111], v10\n\t" \
    "v_smfmac_f32_16x16x64_bf16 v[76:79], v[68:71], a[112:119], v10\n\t" \
    "v_smfmac_f32_16x16x64_bf16 v[76:79], v[44:47], a[136:143], v10\n\t" \
    "v_smfmac_f32_16x16x64_bf16 v[80:83], v[36:39], a[192:199], v10\n\t" \
    "v_smfmac_f32_16x16x64_bf16 v[80:83], v[40:43], a[200:207], v10\n\t" \
    "v_smfmac_f32_16x16x64_bf16 v[80:83], v[48:51], a[144:151], v10\n\t" \
    "v_smfmac_f32_16x16x64_bf16 v[80:83], v[52:55], a[152:159], v10\n\t" \
    "v_smfmac_f32_16x16x64_bf16 v[80:83], v[56:59], a[160:167], v10\n\t" \
    "v_smfmac_f32_16x16x64_bf16 v[80:83], v[60:63], a[168:175], v10\n\t" \
    "v_smfmac_f32_16x16x64_bf16 v[80:83], v[64:67], a[176:183], v10\n\t" \
    "v_smfmac_f32_16x16x64_bf16 v[80:83], v[68:71], a[184:191], v10\n\t" \
    "v_smfmac_f32_16x16x64_bf16 v[80:83], v[44:47], a[208:215], v10\n\t" \
    "v_smfmac_f32_16x16x64_bf16 v[84:87], v[36:39], v[232:239], v10\n\t" \
    "v_smfmac_f32_16x16x64_bf16 v[84:87], v[40:43], v[240:247], v10\n\t" \
    "v_smfmac_f32_16x16x64_bf16 v[84:87], v[48:51], a[216:223], v10\n\t" \
    "v_smfmac_f32_16x16x64_bf16 v[84:87], v[52:55], a[224:231], v10\n\t" \
    "v_smfmac_f32_16x16x64_bf16 v[84:87], v[56:59], a[232:239], v10\n\t" \
    "v_smfmac_f32_16x16x64_bf16 v[84:87], v[60:63], a[240:247], v10\n\t" \
    "v_smfmac_f32_16x16x64_bf16 v[84:87], v[64:67], a[248:255], v10\n\t" \
    "v_smfmac_f32_16x16x64_bf16 v[84:87], v[68:71], v[224:231], v10\n\t" \
    "v_smfmac_f32_16x16x64_bf16 v[84:87], v[44:47], v[248:255], v10\n\t" \
    "v_add_f32 v72, v72, v74\n\t" \
    "v_add_f32 v73, v73, v75\n\t" \
    "v_cvt_pk_bf16_f32 v156, v72, v73\n\t" \
    "ds_write_b32 v12, v156 offset:0\n\t" \
    "v_add_f32 v76, v76, v78\n\t" \
    "v_add_f32 v77, v77, v79\n\t" \
    "v_cvt_pk_bf16_f32 v157, v76, v77\n\t" \
    "ds_write_b32 v12, v157 offset:64\n\t" \
    "v_add_f32 v80, v80, v82\n\t" \
    "v_add_f32 v81, v81, v83\n\t" \
    "v_cvt_pk_bf16_f32 v158, v80, v81\n\t" \
    "ds_write_b32 v16, v158 offset:0\n\t" \
    "v_pk_add_f32 v[84:85], v[84:85], v[86:87]\n\t" \
    "v_cvt_pk_bf16_f32 v159, v84, v85\n\t" \
    "ds_write_b32 v16, v159 offset:256\n\t" \
    "s_branch qk_Gback88_%=\n\t" \
    "qk_last79_%=:\n\t" \
    "s_endpgm\n\t" \
    "qk_last_%=:\n\t" \
    ""
#define ASM_V \
    "s_load_dwordx2 s[42:43], %3, 56\n\t" \
    "s_load_dwordx2 s[44:45], %3, 64\n\t" \
    "s_load_dwordx2 s[36:37], %3, 0x58\n\t" \
    "s_load_dwordx2 s[50:51], %3, 0x48\n\t" \
    "s_load_dwordx8 s[84:91], %3, 0\n\t" \
    "s_mov_b32 s66, %4\n\t" \
    "s_mov_b32 s67, %5\n\t" \
    "s_mov_b32 s68, 262144\n\t" \
    "s_mov_b32 s72, 0\n\t" \
    "s_mov_b32 s83, -1\n\t" \
    "s_mov_b32 s65, 0\n\t" \
    "s_lshl_b32 s82, s66, 10\n\t" \
    "s_waitcnt lgkmcnt(0)\n\t" \
    "s_add_u32 s46, s42, 2097152\n\t" \
    "s_addc_u32 s47, s43, 0\n\t" \
    "s_add_u32 m0, s82, 65536\n\t" \
    "s_nop 0\n\t" \
    "global_load_lds_dwordx4 v200, s[46:47]\n\t" \
    "s_add_u32 s46, s42, 2228224\n\t" \
    "s_addc_u32 s47, s43, 0\n\t" \
    "s_add_u32 m0, s82, 69632\n\t" \
    "s_nop 0\n\t" \
    "global_load_lds_dwordx4 v200, s[46:47]\n\t" \
    "s_add_u32 s46, s42, 2359296\n\t" \
    "s_addc_u32 s47, s43, 0\n\t" \
    "s_add_u32 m0, s82, 73728\n\t" \
    "s_nop 0\n\t" \
    "global_load_lds_dwordx4 v200, s[46:47]\n\t" \
    "s_add_u32 s46, s42, 2490368\n\t" \
    "s_addc_u32 s47, s43, 0\n\t" \
    "s_add_u32 m0, s82, 77824\n\t" \
    "s_nop 0\n\t" \
    "global_load_lds_dwordx4 v200, s[46:47]\n\t" \
    "s_add_u32 s46, s42, 2621440\n\t" \
    "s_addc_u32 s47, s43, 0\n\t" \
    "s_add_u32 m0, s82, 81920\n\t" \
    "s_nop 0\n\t" \
    "global_load_lds_dwordx4 v200, s[46:47]\n\t" \
    "s_add_u32 s46, s42, 2752512\n\t" \
    "s_addc_u32 s47, s43, 0\n\t" \
    "s_add_u32 m0, s82, 86016\n\t" \
    "s_nop 0\n\t" \
    "global_load_lds_dwordx4 v200, s[46:47]\n\t" \
    "s_add_u32 s46, s42, 2883584\n\t" \
    "s_addc_u32 s47, s43, 0\n\t" \
    "s_add_u32 m0, s82, 90112\n\t" \
    "s_nop 0\n\t" \
    "global_load_lds_dwordx4 v200, s[46:47]\n\t" \
    "s_add_u32 s46, s42, 3014656\n\t" \
    "s_addc_u32 s47, s43, 0\n\t" \
    "s_add_u32 m0, s82, 94208\n\t" \
    "s_nop 0\n\t" \
    "global_load_lds_dwordx4 v200, s[46:47]\n\t" \
    "s_add_u32 s46, s42, 3145728\n\t" \
    "s_addc_u32 s47, s43, 0\n\t" \
    "s_add_u32 m0, s82, 98304\n\t" \
    "s_nop 0\n\t" \
    "global_load_lds_dwordx4 v200, s[46:47]\n\t" \
    "s_add_u32 s46, s42, 3276800\n\t" \
    "s_addc_u32 s47, s43, 0\n\t" \
    "s_add_u32 m0, s82, 102400\n\t" \
    "s_nop 0\n\t" \
    "global_load_lds_dwordx4 v200, s[46:47]\n\t" \
    "s_add_u32 s46, s42, 3407872\n\t" \
    "s_addc_u32 s47, s43, 0\n\t" \
    "s_add_u32 m0, s82, 106496\n\t" \
    "s_nop 0\n\t" \
    "global_load_lds_dwordx4 v200, s[46:47]\n\t" \
    "s_add_u32 s46, s42, 3538944\n\t" \
    "s_addc_u32 s47, s43, 0\n\t" \
    "s_add_u32 m0, s82, 110592\n\t" \
    "s_nop 0\n\t" \
    "global_load_lds_dwordx4 v200, s[46:47]\n\t" \
    "s_add_u32 s46, s42, 3670016\n\t" \
    "s_addc_u32 s47, s43, 0\n\t" \
    "s_add_u32 m0, s82, 114688\n\t" \
    "s_nop 0\n\t" \
    "global_load_lds_dwordx4 v200, s[46:47]\n\t" \
    "s_add_u32 s46, s42, 3801088\n\t" \
    "s_addc_u32 s47, s43, 0\n\t" \
    "s_add_u32 m0, s82, 118784\n\t" \
    "s_nop 0\n\t" \
    "global_load_lds_dwordx4 v200, s[46:47]\n\t" \
    "s_add_u32 s46, s42, 3932160\n\t" \
    "s_addc_u32 s47, s43, 0\n\t" \
    "s_add_u32 m0, s82, 122880\n\t" \
    "s_nop 0\n\t" \
    "global_load_lds_dwordx4 v200, s[46:47]\n\t" \
    "s_add_u32 s46, s42, 4063232\n\t" \
    "s_addc_u32 s47, s43, 0\n\t" \
    "s_add_u32 m0, s82, 126976\n\t" \
    "s_nop 0\n\t" \
    "global_load_lds_dwordx4 v200, s[46:47]\n\t" \
    "s_add_u32 s46, s42, 4194304\n\t" \
    "s_addc_u32 s47, s43, 0\n\t" \
    "s_add_u32 m0, s82, 131072\n\t" \
    "s_nop 0\n\t" \
    "global_load_lds_dwordx4 v200, s[46:47]\n\t" \
    "s_add_u32 s46, s42, 4325376\n\t" \
    "s_addc_u32 s47, s43, 0\n\t" \
    "s_add_u32 m0, s82, 135168\n\t" \
    "s_nop 0\n\t" \
    "global_load_lds_dwordx4 v200, s[46:47]\n\t" \
    "s_add_u32 s46, s42, 4456448\n\t" \
    "s_addc_u32 s47, s43, 0\n\t" \
    "s_add_u32 m0, s82, 139264\n\t" \
    "s_nop 0\n\t" \
    "global_load_lds_dwordx4 v200, s[46:47]\n\t" \
    "s_add_u32 s46, s42, 4587520\n\t" \
    "s_addc_u32 s47, s43, 0\n\t" \
    "s_add_u32 m0, s82, 143360\n\t" \
    "s_nop 0\n\t" \
    "global_load_lds_dwordx4 v200, s[46:47]\n\t" \
    "s_mov_b32 s77, 3019898880\n\t" \
    "s_mov_b32 s78, 3054198966\n\t" \
    "s_waitcnt lgkmcnt(0)\n\t" \
    "s_mov_b32 s70, 116\n\t" \
    "vv_E1_%=:\n\t" \
    "s_memtime s[56:57]\n\t" \
    "s_memrealtime s[2:3]\n\t" \
    "s_nop 0\n\t" \
    "s_memrealtime s[4:5]\n\t" \
    "s_nop 0\n\t" \
    "s_memrealtime s[6:7]\n\t" \
    "s_nop 0\n\t" \
    "s_memrealtime s[8:9]\n\t" \
    "s_nop 0\n\t" \
    "s_memrealtime s[10:11]\n\t" \
    "s_nop 0\n\t" \
    "s_memrealtime s[12:13]\n\t" \
    "s_nop 0\n\t" \
    "s_memrealtime s[14:15]\n\t" \
    "s_nop 0\n\t" \
    "s_memrealtime s[16:17]\n\t" \
    "s_nop 0\n\t" \
    "s_memrealtime s[18:19]\n\t" \
    "s_nop 0\n\t" \
    "s_memrealtime s[20:21]\n\t" \
    "s_nop 0\n\t" \
    "s_memrealtime s[22:23]\n\t" \
    "s_nop 0\n\t" \
    "s_memrealtime s[24:25]\n\t" \
    "s_nop 0\n\t" \
    "s_memrealtime s[26:27]\n\t" \
    "s_nop 0\n\t" \
    "s_memrealtime s[28:29]\n\t" \
    "s_memtime s[54:55]\n\t" \
    "s_add_u32 s70, s70, 8\n\t" \
    "s_waitcnt lgkmcnt(0)\n\t" \
    "s_sub_u32 s69, s54, s56\n\t" \
    "s_cmp_gt_u32 s69, s70\n\t" \
    "s_cbranch_scc1 vv_E1_%=\n\t" \
    "s_add_u32 s2, s2, s4\n\t" \
    "s_add_u32 s6, s6, s8\n\t" \
    "s_add_u32 s10, s10, s12\n\t" \
    "s_add_u32 s14, s14, s16\n\t" \
    "s_add_u32 s18, s18, s20\n\t" \
    "s_add_u32 s22, s22, s24\n\t" \
    "s_add_u32 s2, s2, s6\n\t" \
    "s_add_u32 s10, s10, s14\n\t" \
    "s_add_u32 s18, s18, s22\n\t" \
    "s_add_u32 s2, s2, s26\n\t" \
    "s_add_u32 s10, s10, s18\n\t" \
    "s_add_u32 s2, s2, s10\n\t" \
    "s_mul_i32 s70, s28, 13\n\t" \
    "s_sub_u32 s70, s70, s2\n\t" \
    "s_min_u32 s70, s70, 52\n\t" \
    "s_mul_i32 s69, s69, s70\n\t" \
    "s_mul_hi_u32 s69, s69, 76695844\n\t" \
    "s_mov_b32 s75, s73\n\t" \
    "s_mov_b32 s76, s74\n\t" \
    "s_add_u32 s73, s56, s69\n\t" \
    "s_lshl_b32 s74, s28, 4\n\t" \
    "s_waitcnt vmcnt(32) lgkmcnt(0)\n\t" \
    "s_barrier\n\t" \
    "ds_read_b64_tr_b16 a[0:1], v201 offset:0\n\t" \
    "ds_read_b64_tr_b16 a[2:3], v201 offset:1024\n\t" \
    "ds_read_b64_tr_b16 a[4:5], v201 offset:8192\n\t" \
    "ds_read_b64_tr_b16 a[6:7], v201 offset:9216\n\t" \
    "ds_read_b64_tr_b16 a[72:73], v202 offset:0\n\t" \
    "ds_read_b64_tr_b16 a[74:75], v202 offset:1024\n\t" \
    "ds_read_b64_tr_b16 a[76:77], v202 offset:8192\n\t" \
    "ds_read_b64_tr_b16 a[78:79], v202 offset:9216\n\t" \
    "s_waitcnt vmcnt(28) lgkmcnt(0)\n\t" \
    "s_barrier\n\t" \
    "ds_read_b64_tr_b16 a[8:9], v201 offset:16384\n\t" \
    "ds_read_b64_tr_b16 a[10:11], v201 offset:17408\n\t" \
    "ds_read_b64_tr_b16 a[12:13], v201 offset:24576\n\t" \
    "ds_read_b64_tr_b16 a[14:15], v201 offset:25600\n\t" \
    "ds_read_b64_tr_b16 a[80:81], v202 offset:16384\n\t" \
    "ds_read_b64_tr_b16 a[82:83], v202 offset:17408\n\t" \
    "ds_read_b64_tr_b16 a[84:85], v202 offset:24576\n\t" \
    "ds_read_b64_tr_b16 a[86:87], v202 offset:25600\n\t" \
    "v_mov_b32 v140, 1809408\n\t" \
    "s_mov_b32 s69, 3906560\n\t" \
    "v_writelane_b32 v140, s69, 1\n\t" \
    "s_mov_b32 s69, 6238208\n\t" \
    "v_writelane_b32 v140, s69, 2\n\t" \
    "s_mov_b32 s69, 125541376\n\t" \
    "v_writelane_b32 v140, s69, 3\n\t" \
    "s_mov_b32 m0, 155648\n\t" \
    "s_nop 0\n\t" \
    "global_load_lds_dword v140, s[36:37]\n\t" \
    "s_waitcnt vmcnt(25) lgkmcnt(0)\n\t" \
    "s_barrier\n\t" \
    "ds_read_b64_tr_b16 a[16:17], v201 offset:32768\n\t" \
    "ds_read_b64_tr_b16 a[18:19], v201 offset:33792\n\t" \
    "ds_read_b64_tr_b16 a[20:21], v201 offset:40960\n\t" \
    "ds_read_b64_tr_b16 a[22:23], v201 offset:41984\n\t" \
    "ds_read_b64_tr_b16 a[88:89], v202 offset:32768\n\t" \
    "ds_read_b64_tr_b16 a[90:91], v202 offset:33792\n\t" \
    "ds_read_b64_tr_b16 a[92:93], v202 offset:40960\n\t" \
    "ds_read_b64_tr_b16 a[94:95], v202 offset:41984\n\t" \
    "s_waitcnt vmcnt(21) lgkmcnt(0)\n\t" \
    "s_barrier\n\t" \
    "ds_read_b64_tr_b16 a[24:25], v201 offset:49152\n\t" \
    "ds_read_b64_tr_b16 a[26:27], v201 offset:50176\n\t" \
    "ds_read_b64_tr_b16 a[28:29], v201 offset:57344\n\t" \
    "ds_read_b64_tr_b16 a[30:31], v201 offset:58368\n\t" \
    "ds_read_b64_tr_b16 a[96:97], v202 offset:49152\n\t" \
    "ds_read_b64_tr_b16 a[98:99], v202 offset:50176\n\t" \
    "ds_read_b64_tr_b16 a[100:101], v202 offset:57344\n\t" \
    "ds_read_b64_tr_b16 a[102:103], v202 offset:58368\n\t" \
    "s_add_u32 s46, s44, 0\n\t" \
    "s_addc_u32 s47, s45, 0\n\t" \
    "s_mul_i32 s69, s82, 9\n\t" \
    "s_add_u32 m0, s69, 0\n\t" \
    "s_nop 0\n\t" \
    "global_load_lds_dwordx4 v203, s[46:47]\n\t" \
    "s_add_u32 m0, s69, 1024\n\t" \
    "s_nop 0\n\t" \
    "global_load_lds_dwordx4 v204, s[46:47]\n\t" \
    "s_add_u32 m0, s69, 2048\n\t" \
    "s_nop 0\n\t" \
    "global_load_lds_dwordx4 v205, s[46:47]\n\t" \
    "s_add_u32 m0, s69, 3072\n\t" \
    "s_nop 0\n\t" \
    "global_load_lds_dwordx4 v206, s[46:47]\n\t" \
    "s_add_u32 m0, s69, 4096\n\t" \
    "s_nop 0\n\t" \
    "global_load_lds_dwordx4 v207, s[46:47]\n\t" \
    "s_add_u32 m0, s69, 5120\n\t" \
    "s_nop 0\n\t" \
    "global_load_lds_dwordx4 v208, s[46:47]\n\t" \
    "s_add_u32 m0, s69, 6144\n\t" \
    "s_nop 0\n\t" \
    "global_load_lds_dwordx4 v209, s[46:47]\n\t" \
    "s_add_u32 m0, s69, 7168\n\t" \
    "s_nop 0\n\t" \
    "global_load_lds_dwordx4 v210, s[46:47]\n\t" \
    "s_add_u32 m0, s69, 8192\n\t" \
    "s_nop 0\n\t" \
    "global_load_lds_dwordx4 v211, s[46:47]\n\t" \
    "s_waitcnt vmcnt(26) lgkmcnt(0)\n\t" \
    "s_barrier\n\t" \
    "v_add_u32 v201, 0x10000, v201\n\t" \
    "v_add_u32 v202, 0x10000, v202\n\t" \
    "ds_read_b64_tr_b16 a[32:33], v201 offset:0\n\t" \
    "ds_read_b64_tr_b16 a[34:35], v201 offset:1024\n\t" \
    "ds_read_b64_tr_b16 a[36:37], v201 offset:8192\n\t" \
    "ds_read_b64_tr_b16 a[38:39], v201 offset:9216\n\t" \
    "ds_read_b64_tr_b16 a[104:105], v202 offset:0\n\t" \
    "ds_read_b64_tr_b16 a[106:107], v202 offset:1024\n\t" \
    "ds_read_b64_tr_b16 a[108:109], v202 offset:8192\n\t" \
    "ds_read_b64_tr_b16 a[110:111], v202 offset:9216\n\t" \
    "s_waitcnt vmcnt(22) lgkmcnt(0)\n\t" \
    "s_barrier\n\t" \
    "ds_read_b64_tr_b16 a[40:41], v201 offset:16384\n\t" \
    "ds_read_b64_tr_b16 a[42:43], v201 offset:17408\n\t" \
    "ds_read_b64_tr_b16 a[44:45], v201 offset:24576\n\t" \
    "ds_read_b64_tr_b16 a[46:47], v201 offset:25600\n\t" \
    "ds_read_b64_tr_b16 a[112:113], v202 offset:16384\n\t" \
    "ds_read_b64_tr_b16 a[114:115], v202 offset:17408\n\t" \
    "ds_read_b64_tr_b16 a[116:117], v202 offset:24576\n\t" \
    "ds_read_b64_tr_b16 a[118:119], v202 offset:25600\n\t" \
    "s_add_u32 s46, s44, 36864\n\t" \
    "s_addc_u32 s47, s45, 0\n\t" \
    "s_mul_i32 s69, s82, 9\n\t" \
    "s_add_u32 m0, s69, 36864\n\t" \
    "s_nop 0\n\t" \
    "global_load_lds_dwordx4 v203, s[46:47]\n\t" \
    "s_add_u32 m0, s69, 37888\n\t" \
    "s_nop 0\n\t" \
    "global_load_lds_dwordx4 v204, s[46:47]\n\t" \
    "s_add_u32 m0, s69, 38912\n\t" \
    "s_nop 0\n\t" \
    "global_load_lds_dwordx4 v205, s[46:47]\n\t" \
    "s_add_u32 m0, s69, 39936\n\t" \
    "s_nop 0\n\t" \
    "global_load_lds_dwordx4 v206, s[46:47]\n\t" \
    "s_add_u32 m0, s69, 40960\n\t" \
    "s_nop 0\n\t" \
    "global_load_lds_dwordx4 v207, s[46:47]\n\t" \
    "s_add_u32 m0, s69, 41984\n\t" \
    "s_nop 0\n\t" \
    "global_load_lds_dwordx4 v208, s[46:47]\n\t" \
    "s_add_u32 m0, s69, 43008\n\t" \
    "s_nop 0\n\t" \
    "global_load_lds_dwordx4 v209, s[46:47]\n\t" \
    "s_add_u32 m0, s69, 44032\n\t" \
    "s_nop 0\n\t" \
    "global_load_lds_dwordx4 v210, s[46:47]\n\t" \
    "s_add_u32 m0, s69, 45056\n\t" \
    "s_nop 0\n\t" \
    "global_load_lds_dwordx4 v211, s[46:47]\n\t" \
    "s_waitcnt vmcnt(27) lgkmcnt(0)\n\t" \
    "s_barrier\n\t" \
    "ds_read_b64_tr_b16 a[48:49], v201 offset:32768\n\t" \
    "ds_read_b64_tr_b16 a[50:51], v201 offset:33792\n\t" \
    "ds_read_b64_tr_b16 a[52:53], v201 offset:40960\n\t" \
    "ds_read_b64_tr_b16 a[54:55], v201 offset:41984\n\t" \
    "ds_read_b64_tr_b16 a[120:121], v202 offset:32768\n\t" \
    "ds_read_b64_tr_b16 a[122:123], v202 offset:33792\n\t" \
    "ds_read_b64_tr_b16 a[124:125], v202 offset:40960\n\t" \
    "ds_read_b64_tr_b16 a[126:127], v202 offset:41984\n\t" \
    "s_waitcnt vmcnt(23) lgkmcnt(0)\n\t" \
    "s_barrier\n\t" \
    "ds_read_b64_tr_b16 a[56:57], v201 offset:49152\n\t" \
    "ds_read_b64_tr_b16 a[58:59], v201 offset:50176\n\t" \
    "ds_read_b64_tr_b16 a[60:61], v201 offset:57344\n\t" \
    "ds_read_b64_tr_b16 a[62:63], v201 offset:58368\n\t" \
    "ds_read_b64_tr_b16 a[128:129], v202 offset:49152\n\t" \
    "ds_read_b64_tr_b16 a[130:131], v202 offset:50176\n\t" \
    "ds_read_b64_tr_b16 a[132:133], v202 offset:57344\n\t" \
    "ds_read_b64_tr_b16 a[134:135], v202 offset:58368\n\t" \
    "s_add_u32 s46, s44, 73728\n\t" \
    "s_addc_u32 s47, s45, 0\n\t" \
    "s_mul_i32 s69, s82, 9\n\t" \
    "s_add_u32 m0, s69, 73728\n\t" \
    "s_nop 0\n\t" \
    "global_load_lds_dwordx4 v203, s[46:47]\n\t" \
    "s_add_u32 m0, s69, 74752\n\t" \
    "s_nop 0\n\t" \
    "global_load_lds_dwordx4 v204, s[46:47]\n\t" \
    "s_add_u32 m0, s69, 75776\n\t" \
    "s_nop 0\n\t" \
    "global_load_lds_dwordx4 v205, s[46:47]\n\t" \
    "s_add_u32 m0, s69, 76800\n\t" \
    "s_nop 0\n\t" \
    "global_load_lds_dwordx4 v206, s[46:47]\n\t" \
    "s_add_u32 m0, s69, 77824\n\t" \
    "s_nop 0\n\t" \
    "global_load_lds_dwordx4 v207, s[46:47]\n\t" \
    "s_add_u32 m0, s69, 78848\n\t" \
    "s_nop 0\n\t" \
    "global_load_lds_dwordx4 v208, s[46:47]\n\t" \
    "s_add_u32 m0, s69, 79872\n\t" \
    "s_nop 0\n\t" \
    "global_load_lds_dwordx4 v209, s[46:47]\n\t" \
    "s_add_u32 m0, s69, 80896\n\t" \
    "s_nop 0\n\t" \
    "global_load_lds_dwordx4 v210, s[46:47]\n\t" \
    "s_add_u32 m0, s69, 81920\n\t" \
    "s_nop 0\n\t" \
    "global_load_lds_dwordx4 v211, s[46:47]\n\t" \
    "v_mbcnt_lo_u32_b32 v140, -1, 0\n\t" \
    "v_mbcnt_hi_u32_b32 v140, -1, v140\n\t" \
    "v_lshlrev_b32 v140, 6, v140\n\t" \
    "v_add_u32 v140, 6225920, v140\n\t" \
    "v_mov_b32 v141, 6234112\n\t" \
    "global_load_dword v141, v141, s[36:37] sc1\n\t" \
    "global_load_dwordx2 v[142:143], v140, s[36:37] sc1\n\t" \
    "global_load_dword v144, v140, s[36:37] offset:2048 sc1\n\t" \
    "s_waitcnt vmcnt(31) lgkmcnt(0)\n\t" \
    "s_barrier\n\t" \
    "v_add_u32 v201, 0x10000, v201\n\t" \
    "v_add_u32 v202, 0x10000, v202\n\t" \
    "ds_read_b64_tr_b16 a[64:65], v201 offset:0\n\t" \
    "ds_read_b64_tr_b16 a[66:67], v201 offset:1024\n\t" \
    "ds_read_b64_tr_b16 a[68:69], v201 offset:8192\n\t" \
    "ds_read_b64_tr_b16 a[70:71], v201 offset:9216\n\t" \
    "ds_read_b64_tr_b16 a[136:137], v202 offset:0\n\t" \
    "ds_read_b64_tr_b16 a[138:139], v202 offset:1024\n\t" \
    "ds_read_b64_tr_b16 a[140:141], v202 offset:8192\n\t" \
    "ds_read_b64_tr_b16 a[142:143], v202 offset:9216\n\t" \
    "v_add_u32 v201, 0xfffe0000, v201\n\t" \
    "v_add_u32 v202, 0xfffe0000, v202\n\t" \
    "s_waitcnt lgkmcnt(0)\n\t" \
    "s_barrier\n\t" \
    "s_add_u32 s46, s44, 110592\n\t" \
    "s_addc_u32 s47, s45, 0\n\t" \
    "s_mul_i32 s69, s82, 9\n\t" \
    "s_add_u32 m0, s69, 110592\n\t" \
    "s_nop 0\n\t" \
    "global_load_lds_dwordx4 v203, s[46:47]\n\t" \
    "s_add_u32 m0, s69, 111616\n\t" \
    "s_nop 0\n\t" \
    "global_load_lds_dwordx4 v204, s[46:47]\n\t" \
    "s_add_u32 m0, s69, 112640\n\t" \
    "s_nop 0\n\t" \
    "global_load_lds_dwordx4 v205, s[46:47]\n\t" \
    "s_add_u32 m0, s69, 113664\n\t" \
    "s_nop 0\n\t" \
    "global_load_lds_dwordx4 v206, s[46:47]\n\t" \
    "s_add_u32 m0, s69, 114688\n\t" \
    "s_nop 0\n\t" \
    "global_load_lds_dwordx4 v207, s[46:47]\n\t" \
    "s_add_u32 m0, s69, 115712\n\t" \
    "s_nop 0\n\t" \
    "global_load_lds_dwordx4 v208, s[46:47]\n\t" \
    "s_add_u32 m0, s69, 116736\n\t" \
    "s_nop 0\n\t" \
    "global_load_lds_dwordx4 v209, s[46:47]\n\t" \
    "s_add_u32 m0, s69, 117760\n\t" \
    "s_nop 0\n\t" \
    "global_load_lds_dwordx4 v210, s[46:47]\n\t" \
    "s_add_u32 m0, s69, 118784\n\t" \
    "s_nop 0\n\t" \
    "global_load_lds_dwordx4 v211, s[46:47]\n\t" \
    "s_waitcnt vmcnt(21) lgkmcnt(0)\n\t" \
    "s_barrier\n\t" \
    "ds_read_b64_tr_b16 a[144:145], v212 offset:0\n\t" \
    "ds_read_b64_tr_b16 a[146:147], v212 offset:2304\n\t" \
    "ds_read_b64_tr_b16 a[148:149], v212 offset:18432\n\t" \
    "ds_read_b64_tr_b16 a[150:151], v212 offset:20736\n\t" \
    "s_waitcnt lgkmcnt(6)\n\t" \
    "ds_read_b64_tr_b16 a[176:177], v213 offset:0\n\t" \
    "ds_read_b64_tr_b16 a[178:179], v213 offset:2304\n\t" \
    "ds_read_b64_tr_b16 a[180:181], v213 offset:18432\n\t" \
    "ds_read_b64_tr_b16 a[182:183], v213 offset:20736\n\t" \
    "s_waitcnt lgkmcnt(6)\n\t" \
    "ds_read_b64_tr_b16 a[208:209], v212 offset:128\n\t" \
    "ds_read_b64_tr_b16 a[210:211], v212 offset:2432\n\t" \
    "ds_read_b64_tr_b16 a[212:213], v212 offset:18560\n\t" \
    "ds_read_b64_tr_b16 a[214:215], v212 offset:20864\n\t" \
    "s_waitcnt lgkmcnt(6)\n\t" \
    "ds_read_b64_tr_b16 a[240:241], v213 offset:128\n\t" \
    "ds_read_b64_tr_b16 a[242:243], v213 offset:2432\n\t" \
    "ds_read_b64_tr_b16 a[244:245], v213 offset:18560\n\t" \
    "ds_read_b64_tr_b16 a[246:247], v213 offset:20864\n\t" \
    "s_waitcnt lgkmcnt(6)\n\t" \
    "ds_read_b64_tr_b16 a[152:153], v214 offset:0\n\t" \
    "ds_read_b64_tr_b16 a[154:155], v214 offset:2304\n\t" \
    "ds_read_b64_tr_b16 a[156:157], v214 offset:18432\n\t" \
    "ds_read_b64_tr_b16 a[158:159], v214 offset:20736\n\t" \
    "s_waitcnt lgkmcnt(6)\n\t" \
    "ds_read_b64_tr_b16 a[184:185], v215 offset:0\n\t" \
    "ds_read_b64_tr_b16 a[186:187], v215 offset:2304\n\t" \
    "ds_read_b64_tr_b16 a[188:189], v215 offset:18432\n\t" \
    "ds_read_b64_tr_b16 a[190:191], v215 offset:20736\n\t" \
    "s_waitcnt lgkmcnt(6)\n\t" \
    "ds_read_b64_tr_b16 a[216:217], v214 offset:128\n\t" \
    "ds_read_b64_tr_b16 a[218:219], v214 offset:2432\n\t" \
    "ds_read_b64_tr_b16 a[220:221], v214 offset:18560\n\t" \
    "ds_read_b64_tr_b16 a[222:223], v214 offset:20864\n\t" \
    "s_waitcnt lgkmcnt(6)\n\t" \
    "ds_read_b64_tr_b16 a[248:249], v215 offset:128\n\t" \
    "ds_read_b64_tr_b16 a[250:251], v215 offset:2432\n\t" \
    "ds_read_b64_tr_b16 a[252:253], v215 offset:18560\n\t" \
    "ds_read_b64_tr_b16 a[254:255], v215 offset:20864\n\t" \
    "s_waitcnt lgkmcnt(6)\n\t" \
    "ds_read_b64_tr_b16 v[240:241], v24 offset:0\n\t" \
    "ds_read_b64_tr_b16 v[242:243], v24 offset:2304\n\t" \
    "ds_read_b64_tr_b16 v[244:245], v24 offset:18432\n\t" \
    "ds_read_b64_tr_b16 v[246:247], v24 offset:20736\n\t" \
    "v_add_u32 v212, 0x12000, v212\n\t" \
    "v_add_u32 v213, 0x12000, v213\n\t" \
    "v_add_u32 v214, 0x12000, v214\n\t" \
    "v_add_u32 v215, 0x12000, v215\n\t" \
    "v_add_u32 v24, 0x12000, v24\n\t" \
    "s_waitcnt vmcnt(9)\n\t" \
    "vv_C2_%=:\n\t" \
    "v_cmp_ne_u32 s[54:55], v142, v144\n\t" \
    "v_sub_u32 v142, v142, v141\n\t" \
    "v_cmp_ne_u32 vcc, 1, v142\n\t" \
    "s_nop 1\n\t" \
    "s_or_b64 vcc, vcc, s[54:55]\n\t" \
    "s_cbranch_scc0 vv_Cok3_%=\n\t" \
    "s_cmp_eq_u32 s68, 0\n\t" \
    "s_cbranch_scc1 vv_F5_%=\n\t" \
    "s_sub_u32 s68, s68, 1\n\t" \
    "s_sleep 6\n\t" \
    "s_branch vv_Cag4_%=\n\t" \
    "vv_F5_%=:\n\t" \
    "v_mov_b32 v144, 5767424\n\t" \
    "v_mov_b32 v145, 4\n\t" \
    "global_store_dword v144, v145, s[36:37] sc1\n\t" \
    "s_waitcnt vmcnt(0)\n\t" \
    "s_branch vv_Cok3_%=\n\t" \
    "vv_Cag4_%=:\n\t" \
    "global_load_dwordx2 v[142:143], v140, s[36:37] sc1\n\t" \
    "global_load_dword v144, v140, s[36:37] offset:2048 sc1\n\t" \
    "s_waitcnt vmcnt(0)\n\t" \
    "s_branch vv_C2_%=\n\t" \
    "vv_Cok3_%=:\n\t" \
    "v_readlane_b32 s69, v143, 2\n\t" \
    "v_readlane_b32 s70, v143, 3\n\t" \
    "s_nop 1\n\t" \
    "s_cmp_lt_u32 s70, s69\n\t" \
    "s_cselect_b32 s69, 0, 8192\n\t" \
    "s_add_u32 s100, s36, s69\n\t" \
    "s_addc_u32 s101, s37, 0\n\t" \
    "s_cmp_lg_u32 s66, 0\n\t" \
    "s_cbranch_scc1 vv_Ca6_%=\n\t" \
    "v_mov_b32 v140, 5768192\n\t" \
    "s_mov_b64 exec, 1\n\t" \
    "global_atomic_add v140, v142, s[36:37] sc1\n\t" \
    "s_mov_b64 exec, -1\n\t" \
    "vv_Ca6_%=:\n\t" \
    "v_mov_b32_dpp v140, v143 quad_perm:[1,0,3,2] row_mask:0xf bank_mask:0xf\n\t" \
    "v_cmp_lt_u32 s[44:45], v140, v143\n\t" \
    "v_min_u32 v140, v140, v143\n\t" \
    "v_and_b32 v141, 1, v140\n\t" \
    "v_cmp_ne_u32 vcc, 0, v141\n\t" \
    "s_nop 1\n\t" \
    "s_and_b32 s44, s44, 0x55555555\n\t" \
    "s_and_b32 s69, vcc_lo, 0xaaaaaaaa\n\t" \
    "s_or_b32 s44, s44, s69\n\t" \
    "s_and_b32 s45, s45, 0x55555555\n\t" \
    "s_and_b32 s69, vcc_hi, 0xaaaaaaaa\n\t" \
    "s_or_b32 s45, s45, s69\n\t" \
    "s_bitcmp1_b32 s44, 0\n\t" \
    "s_cselect_b32 s62, 8192, 0\n\t" \
    "s_bitcmp1_b32 s44, 2\n\t" \
    "s_cselect_b32 s63, 8192, 0\n\t" \
    "s_or_b32 s63, s63, 0x200000\n\t" \
    "v_cmp_lt_u32 vcc, 1320, v140\n\t" \
    "s_nop 1\n\t" \
    "s_bcnt1_i32_b64 s69, vcc\n\t" \
    "s_cmp_gt_u32 s69, 16\n\t" \
    "s_cselect_b32 s69, 1, 0\n\t" \
    "s_mul_i32 s70, s69, 160\n\t" \
    "s_add_u32 s86, s86, s70\n\t" \
    "s_mul_i32 s70, s69, 536\n\t" \
    "s_add_u32 s87, s87, s70\n\t" \
    "s_mul_i32 s70, s69, 256\n\t" \
    "s_add_u32 s88, s88, s70\n\t" \
    "s_waitcnt vmcnt(0) lgkmcnt(0)\n\t" \
    "s_barrier\n\t" \
    "ds_read_b64_tr_b16 a[160:161], v212 offset:0\n\t" \
    "ds_read_b64_tr_b16 a[162:163], v212 offset:2304\n\t" \
    "ds_read_b64_tr_b16 a[164:165], v212 offset:18432\n\t" \
    "ds_read_b64_tr_b16 a[166:167], v212 offset:20736\n\t" \
    "s_waitcnt lgkmcnt(6)\n\t" \
    "ds_read_b64_tr_b16 a[192:193], v213 offset:0\n\t" \
    "ds_read_b64_tr_b16 a[194:195], v213 offset:2304\n\t" \
    "ds_read_b64_tr_b16 a[196:197], v213 offset:18432\n\t" \
    "ds_read_b64_tr_b16 a[198:199], v213 offset:20736\n\t" \
    "s_waitcnt lgkmcnt(6)\n\t" \
    "ds_read_b64_tr_b16 a[224:225], v212 offset:128\n\t" \
    "ds_read_b64_tr_b16 a[226:227], v212 offset:2432\n\t" \
    "ds_read_b64_tr_b16 a[228:229], v212 offset:18560\n\t" \
    "ds_read_b64_tr_b16 a[230:231], v212 offset:20864\n\t" \
    "s_waitcnt lgkmcnt(6)\n\t" \
    "ds_read_b64_tr_b16 v[224:225], v213 offset:128\n\t" \
    "ds_read_b64_tr_b16 v[226:227], v213 offset:2432\n\t" \
    "ds_read_b64_tr_b16 v[228:229], v213 offset:18560\n\t" \
    "ds_read_b64_tr_b16 v[230:231], v213 offset:20864\n\t" \
    "s_waitcnt lgkmcnt(6)\n\t" \
    "ds_read_b64_tr_b16 a[168:169], v214 offset:0\n\t" \
    "ds_read_b64_tr_b16 a[170:171], v214 offset:2304\n\t" \
    "ds_read_b64_tr_b16 a[172:173], v214 offset:18432\n\t" \
    "ds_read_b64_tr_b16 a[174:175], v214 offset:20736\n\t" \
    "s_waitcnt lgkmcnt(6)\n\t" \
    "ds_read_b64_tr_b16 a[200:201], v215 offset:0\n\t" \
    "ds_read_b64_tr_b16 a[202:203], v215 offset:2304\n\t" \
    "ds_read_b64_tr_b16 a[204:205], v215 offset:18432\n\t" \
    "ds_read_b64_tr_b16 a[206:207], v215 offset:20736\n\t" \
    "s_waitcnt lgkmcnt(6)\n\t" \
    "ds_read_b64_tr_b16 a[232:233], v214 offset:128\n\t" \
    "ds_read_b64_tr_b16 a[234:235], v214 offset:2432\n\t" \
    "ds_read_b64_tr_b16 a[236:237], v214 offset:18560\n\t" \
    "ds_read_b64_tr_b16 a[238:239], v214 offset:20864\n\t" \
    "s_waitcnt lgkmcnt(6)\n\t" \
    "ds_read_b64_tr_b16 v[232:233], v215 offset:128\n\t" \
    "ds_read_b64_tr_b16 v[234:235], v215 offset:2432\n\t" \
    "ds_read_b64_tr_b16 v[236:237], v215 offset:18560\n\t" \
    "ds_read_b64_tr_b16 v[238:239], v215 offset:20864\n\t" \
    "s_waitcnt lgkmcnt(6)\n\t" \
    "ds_read_b64_tr_b16 v[248:249], v24 offset:0\n\t" \
    "ds_read_b64_tr_b16 v[250:251], v24 offset:2304\n\t" \
    "ds_read_b64_tr_b16 v[252:253], v24 offset:18432\n\t" \
    "ds_read_b64_tr_b16 v[254:255], v24 offset:20736\n\t" \
    "v_add_u32 v212, 0x12000, v212\n\t" \
    "v_add_u32 v213, 0x12000, v213\n\t" \
    "v_add_u32 v214, 0x12000, v214\n\t" \
    "v_add_u32 v215, 0x12000, v215\n\t" \
    "v_add_u32 v24, 0x12000, v24\n\t" \
    "s_waitcnt lgkmcnt(0)\n\t" \
    "s_barrier\n\t" \
    "s_cmp_lg_u32 s66, 3\n\t" \
    "s_cbranch_scc1 vv_na7_%=\n\t" \
    "v_mov_b32 v172, 5769216\n\t" \
    "v_mov_b32 v173, 1\n\t" \
    "s_mov_b64 exec, 1\n\t" \
    "global_atomic_add v174, v172, v173, s[36:37] sc0 sc1\n\t" \
    "s_mov_b64 exec, -1\n\t" \
    "v_mbcnt_lo_u32_b32 v172, -1, 0\n\t" \
    "v_and_b32 v173, 1, v172\n\t" \
    "v_lshlrev_b32 v173, 14, v173\n\t" \
    "v_bfe_u32 v175, v172, 1, 1\n\t" \
    "v_lshl_add_u32 v173, v175, 17, v173\n\t" \
    "v_bfe_u32 v175, v172, 2, 2\n\t" \
    "v_lshl_add_u32 v173, v175, 11, v173\n\t" \
    "v_bfe_u32 v175, v172, 4, 1\n\t" \
    "v_lshl_add_u32 v173, v175, 15, v173\n\t" \
    "v_add_u32 v173, s63, v173\n\t" \
    "s_waitcnt vmcnt(0)\n\t" \
    "v_readfirstlane_b32 s69, v174\n\t" \
    "s_nop 1\n\t" \
    "s_cmp_lg_u32 s69, 126\n\t" \
    "s_cbranch_scc1 vv_go8_%=\n\t" \
    "s_memrealtime s[56:57]\n\t" \
    "s_waitcnt lgkmcnt(0)\n\t" \
    "s_lshl_b32 s69, s56, 4\n\t" \
    "s_add_u32 s69, s69, 4160\n\t" \
    "s_add_u32 s70, s56, 24\n\t" \
    "s_lshr_b32 s70, s70, 4\n\t" \
    "v_lshrrev_b32 v175, 2, v172\n\t" \
    "v_add_u32 v175, s70, v175\n\t" \
    "v_and_b32 v175, 63, v175\n\t" \
    "v_lshlrev_b32 v175, 6, v175\n\t" \
    "v_and_b32 v174, 3, v172\n\t" \
    "v_lshl_add_u32 v175, v174, 12, v175\n\t" \
    "v_add_u32 v175, 5783552, v175\n\t" \
    "v_mov_b32 v172, s69\n\t" \
    "s_mov_b32 exec_hi, 0\n\t" \
    "global_store_dword v175, v172, s[36:37] sc1\n\t" \
    "global_store_dword v173, v172, s[36:37] sc1\n\t" \
    "s_mov_b32 exec_hi, -1\n\t" \
    "vv_go8_%=:\n\t" \
    "vv_na7_%=:\n\t" \
    "s_mov_b32 s79, s74\n\t" \
    "s_sub_u32 s80, s74, 32000\n\t" \
    "s_bitcmp0_b32 s44, 1\n\t" \
    "s_cselect_b32 s69, 16384, 0\n\t" \
    "v_xor_b32 v1, s69, v1\n\t" \
    "v_xor_b32 v2, s69, v2\n\t" \
    "v_xor_b32 v23, s69, v23\n\t" \
    "s_bitcmp1_b32 s44, 3\n\t" \
    "s_cselect_b32 s69, 16384, 0\n\t" \
    "v_xor_b32 v20, s69, v20\n\t" \
    "v_mov_b32 v72, 0\n\t" \
    "v_mov_b32 v73, 0\n\t" \
    "v_mov_b32 v74, 0\n\t" \
    "v_mov_b32 v75, 0\n\t" \
    "v_mov_b32 v76, 0\n\t" \
    "v_mov_b32 v77, 0\n\t" \
    "v_mov_b32 v78, 0\n\t" \
    "v_mov_b32 v79, 0\n\t" \
    "v_mov_b32 v108, 0\n\t" \
    "v_mov_b32 v109, 0\n\t" \
    "v_mov_b32 v110, 0\n\t" \
    "v_mov_b32 v111, 0\n\t" \
    "v_mov_b32 v112, 0\n\t" \
    "v_mov_b32 v113, 0\n\t" \
    "v_mov_b32 v114, 0\n\t" \
    "v_mov_b32 v115, 0\n\t" \
    "v_mov_b32 v116, 0\n\t" \
    "v_mov_b32 v117, 0\n\t" \
    "v_mov_b32 v118, 0\n\t" \
    "v_mov_b32 v119, 0\n\t" \
    "v_mov_b32 v120, 0\n\t" \
    "v_mov_b32 v121, 0\n\t" \
    "v_mov_b32 v122, 0\n\t" \
    "v_mov_b32 v123, 0\n\t" \
    "v_mov_b32 v124, 0\n\t" \
    "v_mov_b32 v125, 0\n\t" \
    "v_mov_b32 v126, 0\n\t" \
    "v_mov_b32 v127, 0\n\t" \
    "s_add_u32 s69, s65, 0\n\t" \
    "s_and_b32 s69, s69, 7\n\t" \
    "s_lshl_b32 s69, s69, 18\n\t" \
    "s_add_u32 s69, s69, s62\n\t" \
    "s_add_u32 s46, s36, s69\n\t" \
    "s_addc_u32 s47, s37, 0\n\t" \
    "s_bitcmp0_b32 s65, 0\n\t" \
    "s_cselect_b32 s69, 16384, 0\n\t" \
    "v_xor_b32 v1, s69, v1\n\t" \
    "v_xor_b32 v2, s69, v2\n\t" \
    "v_xor_b32 v23, s69, v23\n\t" \
    "s_add_u32 s70, s65, 0\n\t" \
    "s_mul_i32 s69, s70, 171\n\t" \
    "s_lshr_b32 s69, s69, 9\n\t" \
    "s_mul_i32 s71, s69, 3\n\t" \
    "s_sub_u32 s70, s70, s71\n\t" \
    "s_mul_i32 s70, s70, 589824\n\t" \
    "s_lshl1_add_u32 s71, s69, 4\n\t" \
    "s_lshr_b64 s[38:39], s[44:45], s71\n\t" \
    "s_lshl_b32 s71, s66, 1\n\t" \
    "s_xor_b32 s71, s71, s38\n\t" \
    "s_and_b32 s71, s71, 3\n\t" \
    "s_lshl_b32 s71, s71, 13\n\t" \
    "s_add_u32 s70, s70, s71\n\t" \
    "s_lshl_b32 s69, s69, 21\n\t" \
    "s_add_u32 s69, s69, 123731968\n\t" \
    "v_add_u32 v151, s70, v14\n\t" \
    "v_and_b32 v150, 0x80000, v151\n\t" \
    "v_lshrrev_b32 v150, 5, v150\n\t" \
    "v_xor_b32 v151, v151, v150\n\t" \
    "v_add_u32 v152, s70, v15\n\t" \
    "v_and_b32 v150, 0x80000, v152\n\t" \
    "v_lshrrev_b32 v150, 5, v150\n\t" \
    "v_xor_b32 v152, v152, v150\n\t" \
    "v_add_u32 v153, s70, v16\n\t" \
    "v_and_b32 v150, 0x80000, v153\n\t" \
    "v_lshrrev_b32 v150, 5, v150\n\t" \
    "v_xor_b32 v153, v153, v150\n\t" \
    "s_add_u32 s38, s36, s69\n\t" \
    "s_addc_u32 s39, s37, 0\n\t" \
    "s_and_b32 s69, s69, 0xffe00000\n\t" \
    "s_add_u32 s69, s69, 1807360\n\t" \
    "v_mov_b32 v17, s69\n\t" \
    "global_load_dword v17, v17, s[36:37]\n\t" \
    "s_and_b32 s69, s65, 7\n\t" \
    "s_lshl_b32 s69, s69, 18\n\t" \
    "s_add_u32 s69, s69, s63\n\t" \
    "s_add_u32 s52, s36, s69\n\t" \
    "s_addc_u32 s53, s37, 0\n\t" \
    "v_mov_b32 v100, 0\n\t" \
    "v_mov_b32 v101, 0\n\t" \
    "v_mov_b32 v102, 0\n\t" \
    "v_mov_b32 v103, 0\n\t" \
    "s_sub_u32 s70, s79, s74\n\t" \
    "s_max_i32 s70, s70, 0\n\t" \
    "s_min_i32 s70, s70, 16000\n\t" \
    "s_lshl_b32 s70, s70, 1\n\t" \
    "s_mul_hi_u32 s70, s70, s77\n\t" \
    "s_add_u32 s70, s70, s73\n\t" \
    "s_cmp_eq_u32 s66, 1\n\t" \
    "s_cbranch_scc1 vv_loop12_%=\n\t" \
    "s_cmp_eq_u32 s66, 2\n\t" \
    "s_cbranch_scc1 vv_loop13_%=\n\t" \
    "s_cmp_eq_u32 s66, 3\n\t" \
    "s_cbranch_scc1 vv_loop14_%=\n\t" \
    ".p2align 6\n\t" \
    "s_nop 0\n\t" \
    "s_nop 0\n\t" \
    "s_nop 0\n\t" \
    "s_nop 0\n\t" \
    "s_nop 0\n\t" \
    "s_nop 0\n\t" \
    "vv_loop11_%=:\n\t" \
    "s_getpc_b64 s[30:31]\n\t" \
    "vv_Wp17_%=:\n\t" \
    "s_add_u32 s30, s30, vv_We18_%=-vv_Wp17_%=\n\t" \
    "s_addc_u32 s31, s31, 0\n\t" \
    "s_sub_u32 s70, s70, -54\n\t" \
    "vv_W16_%=:\n\t" \
    "s_memtime s[56:57]\n\t" \
    "s_waitcnt lgkmcnt(0)\n\t" \
    "s_sub_u32 s69, s70, s56\n\t" \
    "s_cmp_gt_i32 s69, 80\n\t" \
    "s_cbranch_scc1 vv_W16_%=\n\t" \
    "s_max_i32 s69, s69, 0\n\t" \
    "s_and_b32 s69, s69, -4\n\t" \
    "s_sub_u32 s30, s30, s69\n\t" \
    "s_subb_u32 s31, s31, 0\n\t" \
    "s_setpc_b64 s[30:31]\n\t" \
    "s_nop 0\n\t" \
    "s_nop 0\n\t" \
    "s_nop 0\n\t" \
    "s_nop 0\n\t" \
    "s_nop 0\n\t" \
    "s_nop 0\n\t" \
    "s_nop 0\n\t" \
    "s_nop 0\n\t" \
    "s_nop 0\n\t" \
    "s_nop 0\n\t" \
    "s_nop 0\n\t" \
    "s_nop 0\n\t" \
    "s_nop 0\n\t" \
    "s_nop 0\n\t" \
    "s_nop 0\n\t" \
    "s_nop 0\n\t" \
    "s_nop 0\n\t" \
    "s_nop 0\n\t" \
    "s_nop 0\n\t" \
    "s_nop 0\n\t" \
    "vv_We18_%=:\n\t" \
    "vv_G19_%=:\n\t" \
    "global_load_dwordx4 v[36:39], v1, s[46:47] sc1\n\t" \
    "global_load_dword v141, v2, s[46:47] sc1\n\t" \
    "global_load_dwordx4 v[40:43], v23, s[46:47] sc1\n\t" \
    "s_waitcnt vmcnt(2)\n\t" \
    "vv_Garr24_%=:\n\t" \
    "ds_write_b128 v3, v[36:39]\n\t" \
    "v_smfmac_f32_16x16x64_bf16 v[72:75], v[36:39], a[0:7], v10\n\t" \
    "s_waitcnt vmcnt(1)\n\t" \
    "ds_write_b32 v22, v141 offset:19456\n\t" \
    "s_waitcnt vmcnt(0)\n\t" \
    "ds_write_b128 v3, v[40:43] offset:1024\n\t" \
    "v_max3_u32 v140, v36, v40, v141\n\t" \
    "v_cmp_eq_u32 vcc, -1, v140\n\t" \
    "s_cbranch_vccnz vv_Gbad20_%=\n\t" \
    "vv_Gc21_%=:\n\t" \
    "s_waitcnt lgkmcnt(0)\n\t" \
    "s_barrier\n\t" \
    "ds_read_b128 v[48:51], v4\n\t" \
    "ds_read_b128 v[52:55], v5\n\t" \
    "v_smfmac_f32_16x16x64_bf16 v[76:79], v[36:39], a[72:79], v10\n\t" \
    "ds_read_b128 v[56:59], v6\n\t" \
    "ds_read_b128 v[60:63], v7\n\t" \
    "v_smfmac_f32_16x16x64_bf16 v[72:75], v[40:43], a[8:15], v10\n\t" \
    "ds_read_b128 v[64:67], v8\n\t" \
    "v_smfmac_f32_16x16x64_bf16 v[76:79], v[40:43], a[80:87], v10\n\t" \
    "ds_read_b128 v[68:71], v9\n\t" \
    "s_waitcnt lgkmcnt(5)\n\t" \
    "v_smfmac_f32_16x16x64_bf16 v[72:75], v[48:51], a[16:23], v10\n\t" \
    "ds_read_b128 v[44:47], v0 offset:19456\n\t" \
    "s_waitcnt lgkmcnt(5)\n\t" \
    "v_smfmac_f32_16x16x64_bf16 v[72:75], v[52:55], a[24:31], v10\n\t" \
    "s_waitcnt lgkmcnt(4)\n\t" \
    "v_smfmac_f32_16x16x64_bf16 v[72:75], v[56:59], a[32:39], v10\n\t" \
    "s_waitcnt lgkmcnt(3)\n\t" \
    "v_smfmac_f32_16x16x64_bf16 v[72:75], v[60:63], a[40:47], v10\n\t" \
    "s_waitcnt lgkmcnt(2)\n\t" \
    "v_smfmac_f32_16x16x64_bf16 v[72:75], v[64:67], a[48:55], v10\n\t" \
    "s_waitcnt lgkmcnt(1)\n\t" \
    "v_smfmac_f32_16x16x64_bf16 v[72:75], v[68:71], a[56:63], v10\n\t" \
    "s_waitcnt lgkmcnt(0)\n\t" \
    "v_smfmac_f32_16x16x64_bf16 v[72:75], v[44:47], a[64:71], v10\n\t" \
    "s_nop 2\n\t" \
    "v_smfmac_f32_16x16x64_bf16 v[76:79], v[48:51], a[88:95], v10\n\t" \
    "s_nop 2\n\t" \
    "v_smfmac_f32_16x16x64_bf16 v[76:79], v[52:55], a[96:103], v10\n\t" \
    "v_add_f32 v72, v72, v74\n\t" \
    "v_smfmac_f32_16x16x64_bf16 v[76:79], v[56:59], a[104:111], v10\n\t" \
    "v_add_f32 v73, v73, v75\n\t" \
    "v_smfmac_f32_16x16x64_bf16 v[76:79], v[60:63], a[112:119], v10\n\t" \
    "v_cvt_pk_bf16_f32 v80, v72, v73\n\t" \
    "v_smfmac_f32_16x16x64_bf16 v[76:79], v[64:67], a[120:127], v10\n\t" \
    "ds_write_b32 v12, v80 offset:0\n\t" \
    "v_smfmac_f32_16x16x64_bf16 v[76:79], v[68:71], a[128:135], v10\n\t" \
    "v_smfmac_f32_16x16x64_bf16 v[76:79], v[44:47], a[136:143], v10\n\t" \
    "s_nop 6\n\t" \
    "v_cmp_u_f32 vcc, v72, v73\n\t" \
    "v_pk_add_f32 v[76:77], v[76:77], v[78:79]\n\t" \
    "v_cvt_pk_bf16_f32 v81, v76, v77\n\t" \
    "ds_write_b32 v12, v81 offset:256\n\t" \
    "s_cbranch_vccnz vv_Gslow22_%=\n\t" \
    "vv_Gback23_%=:\n\t" \
    "s_waitcnt lgkmcnt(0)\n\t" \
    "s_barrier\n\t" \
    "ds_read_b64_tr_b16 v[84:85], v13 offset:0\n\t" \
    "v_smfmac_f32_16x16x64_bf16 v[100:103], v[36:39], a[0:7], v10\n\t" \
    "ds_read_b64_tr_b16 v[86:87], v13 offset:64\n\t" \
    "ds_read_b64_tr_b16 v[92:93], v13 offset:1152\n\t" \
    "v_smfmac_f32_16x16x64_bf16 v[100:103], v[36:39], a[0:7], v10\n\t" \
    "ds_read_b64_tr_b16 v[94:95], v13 offset:1216\n\t" \
    "ds_read_b64_tr_b16 v[88:89], v25 offset:0\n\t" \
    "v_smfmac_f32_16x16x64_bf16 v[100:103], v[36:39], a[0:7], v10\n\t" \
    "ds_read_b64_tr_b16 v[90:91], v25 offset:64\n\t" \
    "s_waitcnt lgkmcnt(4)\n\t" \
    "v_smfmac_f32_16x16x64_bf16 v[124:127], v[84:87], v[240:247], v19\n\t" \
    "ds_read_b64_tr_b16 v[96:97], v25 offset:1152\n\t" \
    "ds_read_b64_tr_b16 v[98:99], v25 offset:1216\n\t" \
    "s_waitcnt lgkmcnt(4)\n\t" \
    "v_smfmac_f32_16x16x64_bf16 v[124:127], v[92:95], v[248:255], v19\n\t" \
    "s_nop 2\n\t" \
    "v_smfmac_f32_16x16x64_bf16 v[108:111], v[84:87], a[144:151], v19\n\t" \
    "s_nop 2\n\t" \
    "v_smfmac_f32_16x16x64_bf16 v[108:111], v[92:95], a[160:167], v19\n\t" \
    "s_nop 1\n\t" \
    "v_permlane32_swap_b32 v124, v126\n\t" \
    "v_smfmac_f32_16x16x64_bf16 v[112:115], v[84:87], a[176:183], v19\n\t" \
    "s_nop 1\n\t" \
    "v_permlane32_swap_b32 v125, v127\n\t" \
    "v_smfmac_f32_16x16x64_bf16 v[112:115], v[92:95], a[192:199], v19\n\t" \
    "s_nop 0\n\t" \
    "v_add_f32 v124, v124, v126\n\t" \
    "s_waitcnt lgkmcnt(2)\n\t" \
    "v_smfmac_f32_16x16x64_bf16 v[108:111], v[88:91], a[152:159], v19\n\t" \
    "s_nop 1\n\t" \
    "v_add_f32 v125, v125, v127\n\t" \
    "v_smfmac_f32_16x16x64_bf16 v[112:115], v[88:91], a[184:191], v19\n\t" \
    "s_nop 0\n\t" \
    "v_cvt_pk_bf16_f32 v136, v124, v125\n\t" \
    "s_waitcnt lgkmcnt(0)\n\t" \
    "v_smfmac_f32_16x16x64_bf16 v[108:111], v[96:99], a[168:175], v19\n\t" \
    "s_nop 1\n\t" \
    "v_cvt_pk_bf16_f32 v137, v124, v125\n\t" \
    "v_smfmac_f32_16x16x64_bf16 v[112:115], v[96:99], a[200:207], v19\n\t" \
    "s_nop 1\n\t" \
    "v_permlane32_swap_b32 v136, v137\n\t" \
    "v_smfmac_f32_16x16x64_bf16 v[116:119], v[84:87], a[208:215], v19\n\t" \
    "s_mov_b32 exec_hi, 0\n\t" \
    "global_store_dwordx2 v153, v[136:137], s[38:39] sc1\n\t" \
    "s_mov_b32 exec_hi, -1\n\t" \
    "v_cvt_pk_bf16_f32 v128, v108, v109\n\t" \
    "v_smfmac_f32_16x16x64_bf16 v[116:119], v[88:91], a[216:223], v19\n\t" \
    "s_nop 1\n\t" \
    "v_cvt_pk_bf16_f32 v129, v110, v111\n\t" \
    "v_smfmac_f32_16x16x64_bf16 v[116:119], v[92:95], a[224:231], v19\n\t" \
    "s_nop 1\n\t" \
    "v_cvt_pk_bf16_f32 v130, v112, v113\n\t" \
    "v_smfmac_f32_16x16x64_bf16 v[116:119], v[96:99], a[232:239], v19\n\t" \
    "s_nop 1\n\t" \
    "v_cvt_pk_bf16_f32 v131, v114, v115\n\t" \
    "v_smfmac_f32_16x16x64_bf16 v[120:123], v[84:87], a[240:247], v19\n\t" \
    "s_nop 1\n\t" \
    "v_permlane16_swap_b32 v128, v130\n\t" \
    "v_smfmac_f32_16x16x64_bf16 v[120:123], v[88:91], a[248:255], v19\n\t" \
    "s_nop 1\n\t" \
    "v_permlane16_swap_b32 v129, v131\n\t" \
    "v_smfmac_f32_16x16x64_bf16 v[120:123], v[92:95], v[224:231], v19\n\t" \
    "s_nop 0\n\t" \
    "global_store_dwordx4 v151, v[128:131], s[38:39] sc1\n\t" \
    "v_cvt_pk_bf16_f32 v132, v116, v117\n\t" \
    "v_smfmac_f32_16x16x64_bf16 v[120:123], v[96:99], v[232:239], v19\n\t" \
    "s_nop 1\n\t" \
    "v_cvt_pk_bf16_f32 v133, v118, v119\n\t" \
    "s_nop 4\n\t" \
    "v_cvt_pk_bf16_f32 v134, v120, v121\n\t" \
    "v_cvt_pk_bf16_f32 v135, v122, v123\n\t" \
    "s_nop 0\n\t" \
    "v_permlane16_swap_b32 v132, v134\n\t" \
    "v_permlane16_swap_b32 v133, v135\n\t" \
    "global_store_dwordx4 v152, v[132:135], s[38:39] sc1\n\t" \
    "s_cmp_eq_u32 s65, 63\n\t" \
    "s_cbranch_scc1 vv_end15_%=\n\t" \
    "s_cmp_lg_u32 s66, 0\n\t" \
    "s_cbranch_scc1 vv_SF25_%=\n\t" \
    "s_lshl_b32 s69, s67, 7\n\t" \
    "s_add_u32 s69, s69, 3182592\n\t" \
    "s_add_u32 s70, s65, -1\n\t" \
    "v_mov_b32 v142, s69\n\t" \
    "v_mov_b32 v143, s70\n\t" \
    "s_mov_b64 exec, 1\n\t" \
    "global_store_dword v142, v143, s[100:101] sc1\n\t" \
    "s_mov_b64 exec, -1\n\t" \
    "vv_SF25_%=:\n\t" \
    "s_add_u32 s65, s65, 1\n\t" \
    "v_mov_b32 v72, 0\n\t" \
    "v_mov_b32 v73, 0\n\t" \
    "v_mov_b32 v74, 0\n\t" \
    "v_mov_b32 v75, 0\n\t" \
    "v_mov_b32 v76, 0\n\t" \
    "v_mov_b32 v77, 0\n\t" \
    "v_mov_b32 v78, 0\n\t" \
    "v_mov_b32 v79, 0\n\t" \
    "v_mov_b32 v108, 0\n\t" \
    "v_mov_b32 v109, 0\n\t" \
    "v_mov_b32 v110, 0\n\t" \
    "v_mov_b32 v111, 0\n\t" \
    "v_mov_b32 v112, 0\n\t" \
    "v_mov_b32 v113, 0\n\t" \
    "v_mov_b32 v114, 0\n\t" \
    "v_mov_b32 v115, 0\n\t" \
    "v_mov_b32 v116, 0\n\t" \
    "v_mov_b32 v117, 0\n\t" \
    "v_mov_b32 v118, 0\n\t" \
    "v_mov_b32 v119, 0\n\t" \
    "v_mov_b32 v120, 0\n\t" \
    "v_mov_b32 v121, 0\n\t" \
    "v_mov_b32 v122, 0\n\t" \
    "v_mov_b32 v123, 0\n\t" \
    "v_mov_b32 v124, 0\n\t" \
    "v_mov_b32 v125, 0\n\t" \
    "v_mov_b32 v126, 0\n\t" \
    "v_mov_b32 v127, 0\n\t" \
    "s_add_u32 s69, s65, 0\n\t" \
    "s_and_b32 s69, s69, 7\n\t" \
    "s_lshl_b32 s69, s69, 18\n\t" \
    "s_add_u32 s69, s69, s62\n\t" \
    "s_add_u32 s46, s36, s69\n\t" \
    "s_addc_u32 s47, s37, 0\n\t" \
    "s_bitcmp0_b32 s65, 0\n\t" \
    "s_cselect_b32 s69, 16384, 0\n\t" \
    "v_xor_b32 v1, s69, v1\n\t" \
    "v_xor_b32 v2, s69, v2\n\t" \
    "v_xor_b32 v23, s69, v23\n\t" \
    "s_add_u32 s70, s65, 0\n\t" \
    "s_mul_i32 s69, s70, 171\n\t" \
    "s_lshr_b32 s69, s69, 9\n\t" \
    "s_mul_i32 s71, s69, 3\n\t" \
    "s_sub_u32 s70, s70, s71\n\t" \
    "s_mul_i32 s70, s70, 589824\n\t" \
    "s_lshl1_add_u32 s71, s69, 4\n\t" \
    "s_lshr_b64 s[38:39], s[44:45], s71\n\t" \
    "s_lshl_b32 s71, s66, 1\n\t" \
    "s_xor_b32 s71, s71, s38\n\t" \
    "s_and_b32 s71, s71, 3\n\t" \
    "s_lshl_b32 s71, s71, 13\n\t" \
    "s_add_u32 s70, s70, s71\n\t" \
    "s_lshl_b32 s69, s69, 21\n\t" \
    "s_add_u32 s69, s69, 123731968\n\t" \
    "v_add_u32 v151, s70, v14\n\t" \
    "v_and_b32 v150, 0x80000, v151\n\t" \
    "v_lshrrev_b32 v150, 5, v150\n\t" \
    "v_xor_b32 v151, v151, v150\n\t" \
    "v_add_u32 v152, s70, v15\n\t" \
    "v_and_b32 v150, 0x80000, v152\n\t" \
    "v_lshrrev_b32 v150, 5, v150\n\t" \
    "v_xor_b32 v152, v152, v150\n\t" \
    "v_add_u32 v153, s70, v16\n\t" \
    "v_and_b32 v150, 0x80000, v153\n\t" \
    "v_lshrrev_b32 v150, 5, v150\n\t" \
    "v_xor_b32 v153, v153, v150\n\t" \
    "s_add_u32 s38, s36, s69\n\t" \
    "s_addc_u32 s39, s37, 0\n\t" \
    "s_and_b32 s69, s69, 0xffe00000\n\t" \
    "s_add_u32 s69, s69, 1807360\n\t" \
    "v_mov_b32 v17, s69\n\t" \
    "global_load_dword v17, v17, s[36:37]\n\t" \
    "s_and_b32 s69, s65, 7\n\t" \
    "s_lshl_b32 s69, s69, 18\n\t" \
    "s_add_u32 s69, s69, s63\n\t" \
    "s_add_u32 s52, s36, s69\n\t" \
    "s_addc_u32 s53, s37, 0\n\t" \
    "s_barrier\n\t" \
    "ds_read_b32 v160, v11 offset:20480\n\t" \
    "s_waitcnt lgkmcnt(0)\n\t" \
    "v_readfirstlane_b32 s70, v160\n\t" \
    "s_branch vv_loop11_%=\n\t" \
    "vv_Gbad20_%=:\n\t" \
    "s_waitcnt vmcnt(0) lgkmcnt(0)\n\t" \
    "s_nop 7\n\t" \
    "v_mov_b32 v72, 0\n\t" \
    "v_mov_b32 v73, 0\n\t" \
    "v_mov_b32 v74, 0\n\t" \
    "v_mov_b32 v75, 0\n\t" \
    "v_mov_b32 v76, 0\n\t" \
    "v_mov_b32 v77, 0\n\t" \
    "v_mov_b32 v78, 0\n\t" \
    "v_mov_b32 v79, 0\n\t" \
    "s_cmp_eq_u32 s68, 0\n\t" \
    "s_cbranch_scc1 vv_F26_%=\n\t" \
    "s_sub_u32 s68, s68, 1\n\t" \
    "s_branch vv_G19_%=\n\t" \
    "vv_F26_%=:\n\t" \
    "v_mov_b32 v140, 5767424\n\t" \
    "v_mov_b32 v141, 1\n\t" \
    "global_store_dword v140, v141, s[36:37] sc1\n\t" \
    "s_waitcnt vmcnt(0)\n\t" \
    "s_branch vv_Gc21_%=\n\t" \
    "vv_Gslow22_%=:\n\t" \
    "v_max_u32 v140, v36, v37\n\t" \
    "v_max3_u32 v140, v140, v38, v39\n\t" \
    "v_max3_u32 v140, v140, v40, v41\n\t" \
    "v_max3_u32 v140, v140, v42, v43\n\t" \
    "v_max3_u32 v140, v140, v44, v45\n\t" \
    "v_max3_u32 v140, v140, v46, v47\n\t" \
    "v_max3_u32 v140, v140, v48, v49\n\t" \
    "v_max3_u32 v140, v140, v50, v51\n\t" \
    "v_max3_u32 v140, v140, v52, v53\n\t" \
    "v_max3_u32 v140, v140, v54, v55\n\t" \
    "v_max3_u32 v140, v140, v56, v57\n\t" \
    "v_max3_u32 v140, v140, v58, v59\n\t" \
    "v_max3_u32 v140, v140, v60, v61\n\t" \
    "v_max3_u32 v140, v140, v62, v63\n\t" \
    "v_max3_u32 v140, v140, v64, v65\n\t" \
    "v_max3_u32 v140, v140, v66, v67\n\t" \
    "v_max3_u32 v140, v140, v68, v69\n\t" \
    "v_max3_u32 v140, v140, v70, v71\n\t" \
    "v_cmp_eq_u32 vcc, -1, v140\n\t" \
    "s_cbranch_vccz vv_Gback23_%=\n\t" \
    "s_waitcnt lgkmcnt(0)\n\t" \
    "v_sub_u32 v140, v1, v3\n\t" \
    "s_nop 0\n\t" \
    "v_readfirstlane_b32 s69, v140\n\t" \
    "v_add_u32 v143, 36864, v0\n\t" \
    "s_nop 7\n\t" \
    "s_add_u32 s30, s46, s69\n\t" \
    "s_addc_u32 s31, s47, 0\n\t" \
    "vv_Gag27_%=:\n\t" \
    "global_load_dwordx4 v[36:39], v1, s[46:47] sc1\n\t" \
    "global_load_dwordx4 v[40:43], v23, s[46:47] sc1\n\t" \
    "global_load_dwordx4 v[44:47], v143, s[30:31] sc1\n\t" \
    "global_load_dwordx4 v[48:51], v4, s[30:31] sc1\n\t" \
    "global_load_dwordx4 v[52:55], v5, s[30:31] sc1\n\t" \
    "global_load_dwordx4 v[56:59], v6, s[30:31] sc1\n\t" \
    "global_load_dwordx4 v[60:63], v7, s[30:31] sc1\n\t" \
    "global_load_dwordx4 v[64:67], v8, s[30:31] sc1\n\t" \
    "global_load_dwordx4 v[68:71], v9, s[30:31] sc1\n\t" \
    "s_waitcnt vmcnt(0)\n\t" \
    "v_max_u32 v140, v36, v37\n\t" \
    "v_max3_u32 v140, v140, v38, v39\n\t" \
    "v_max3_u32 v140, v140, v40, v41\n\t" \
    "v_max3_u32 v140, v140, v42, v43\n\t" \
    "v_max3_u32 v140, v140, v44, v45\n\t" \
    "v_max3_u32 v140, v140, v46, v47\n\t" \
    "v_max3_u32 v140, v140, v48, v49\n\t" \
    "v_max3_u32 v140, v140, v50, v51\n\t" \
    "v_max3_u32 v140, v140, v52, v53\n\t" \
    "v_max3_u32 v140, v140, v54, v55\n\t" \
    "v_max3_u32 v140, v140, v56, v57\n\t" \
    "v_max3_u32 v140, v140, v58, v59\n\t" \
    "v_max3_u32 v140, v140, v60, v61\n\t" \
    "v_max3_u32 v140, v140, v62, v63\n\t" \
    "v_max3_u32 v140, v140, v64, v65\n\t" \
    "v_max3_u32 v140, v140, v66, v67\n\t" \
    "v_max3_u32 v140, v140, v68, v69\n\t" \
    "v_max3_u32 v140, v140, v70, v71\n\t" \
    "v_cmp_eq_u32 vcc, -1, v140\n\t" \
    "s_cbranch_vccz vv_Gredo28_%=\n\t" \
    "s_cmp_eq_u32 s68, 0\n\t" \
    "s_cbranch_scc1 vv_F29_%=\n\t" \
    "s_sub_u32 s68, s68, 1\n\t" \
    "s_sleep 1\n\t" \
    "s_branch vv_Gag27_%=\n\t" \
    "vv_F29_%=:\n\t" \
    "v_mov_b32 v140, 5767424\n\t" \
    "v_mov_b32 v141, 2\n\t" \
    "global_store_dword v140, v141, s[36:37] sc1\n\t" \
    "s_waitcnt vmcnt(0)\n\t" \
    "s_branch vv_Gredo28_%=\n\t" \
    "vv_Gredo28_%=:\n\t" \
    "v_mov_b32 v72, 0\n\t" \
    "v_mov_b32 v73, 0\n\t" \
    "v_mov_b32 v74, 0\n\t" \
    "v_mov_b32 v75, 0\n\t" \
    "v_mov_b32 v76, 0\n\t" \
    "v_mov_b32 v77, 0\n\t" \
    "v_mov_b32 v78, 0\n\t" \
    "v_mov_b32 v79, 0\n\t" \
    "v_smfmac_f32_16x16x64_bf16 v[72:75], v[36:39], a[0:7], v10\n\t" \
    "v_smfmac_f32_16x16x64_bf16 v[72:75], v[40:43], a[8:15], v10\n\t" \
    "v_smfmac_f32_16x16x64_bf16 v[72:75], v[48:51], a[16:23], v10\n\t" \
    "v_smfmac_f32_16x16x64_bf16 v[72:75], v[52:55], a[24:31], v10\n\t" \
    "v_smfmac_f32_16x16x64_bf16 v[72:75], v[56:59], a[32:39], v10\n\t" \
    "v_smfmac_f32_16x16x64_bf16 v[72:75], v[60:63], a[40:47], v10\n\t" \
    "v_smfmac_f32_16x16x64_bf16 v[72:75], v[64:67], a[48:55], v10\n\t" \
    "v_smfmac_f32_16x16x64_bf16 v[72:75], v[68:71], a[56:63], v10\n\t" \
    "v_smfmac_f32_16x16x64_bf16 v[72:75], v[44:47], a[64:71], v10\n\t" \
    "v_smfmac_f32_16x16x64_bf16 v[76:79], v[36:39], a[72:79], v10\n\t" \
    "v_smfmac_f32_16x16x64_bf16 v[76:79], v[40:43], a[80:87], v10\n\t" \
    "v_smfmac_f32_16x16x64_bf16 v[76:79], v[48:51], a[88:95], v10\n\t" \
    "v_smfmac_f32_16x16x64_bf16 v[76:79], v[52:55], a[96:103], v10\n\t" \
    "v_smfmac_f32_16x16x64_bf16 v[76:79], v[56:59], a[104:111], v10\n\t" \
    "v_smfmac_f32_16x16x64_bf16 v[76:79], v[60:63], a[112:119], v10\n\t" \
    "v_smfmac_f32_16x16x64_bf16 v[76:79], v[64:67], a[120:127], v10\n\t" \
    "v_smfmac_f32_16x16x64_bf16 v[76:79], v[68:71], a[128:135], v10\n\t" \
    "v_smfmac_f32_16x16x64_bf16 v[76:79], v[44:47], a[136:143], v10\n\t" \
    "v_add_f32 v72, v72, v74\n\t" \
    "v_add_f32 v73, v73, v75\n\t" \
    "v_cvt_pk_bf16_f32 v80, v72, v73\n\t" \
    "ds_write_b32 v12, v80 offset:0\n\t" \
    "s_nop 3\n\t" \
    "v_pk_add_f32 v[76:77], v[76:77], v[78:79]\n\t" \
    "v_cvt_pk_bf16_f32 v81, v76, v77\n\t" \
    "ds_write_b32 v12, v81 offset:256\n\t" \
    "s_branch vv_Gback23_%=\n\t" \
    "vv_end15_%=:\n\t" \
    "s_endpgm\n\t" \
    ".p2align 6\n\t" \
    "s_nop 0\n\t" \
    "s_nop 0\n\t" \
    "s_nop 0\n\t" \
    "s_nop 0\n\t" \
    "s_nop 0\n\t" \
    "s_nop 0\n\t" \
    "vv_loop12_%=:\n\t" \
    "s_getpc_b64 s[30:31]\n\t" \
    "vv_Wp32_%=:\n\t" \
    "s_add_u32 s30, s30, vv_We33_%=-vv_Wp32_%=\n\t" \
    "s_addc_u32 s31, s31, 0\n\t" \
    "s_sub_u32 s70, s70, -54\n\t" \
    "vv_W31_%=:\n\t" \
    "s_memtime s[56:57]\n\t" \
    "s_waitcnt lgkmcnt(0)\n\t" \
    "s_sub_u32 s69, s70, s56\n\t" \
    "s_cmp_gt_i32 s69, 80\n\t" \
    "s_cbranch_scc1 vv_W31_%=\n\t" \
    "s_max_i32 s69, s69, 0\n\t" \
    "s_and_b32 s69, s69, -4\n\t" \
    "s_sub_u32 s30, s30, s69\n\t" \
    "s_subb_u32 s31, s31, 0\n\t" \
    "s_setpc_b64 s[30:31]\n\t" \
    "s_nop 0\n\t" \
    "s_nop 0\n\t" \
    "s_nop 0\n\t" \
    "s_nop 0\n\t" \
    "s_nop 0\n\t" \
    "s_nop 0\n\t" \
    "s_nop 0\n\t" \
    "s_nop 0\n\t" \
    "s_nop 0\n\t" \
    "s_nop 0\n\t" \
    "s_nop 0\n\t" \
    "s_nop 0\n\t" \
    "s_nop 0\n\t" \
    "s_nop 0\n\t" \
    "s_nop 0\n\t" \
    "s_nop 0\n\t" \
    "s_nop 0\n\t" \
    "s_nop 0\n\t" \
    "s_nop 0\n\t" \
    "s_nop 0\n\t" \
    "vv_We33_%=:\n\t" \
    "vv_G34_%=:\n\t" \
    "global_load_dwordx4 v[36:39], v1, s[46:47] sc1\n\t" \
    "global_load_dword v141, v2, s[46:47] sc1\n\t" \
    "global_load_dwordx4 v[40:43], v23, s[46:47] sc1\n\t" \
    "s_waitcnt vmcnt(2)\n\t" \
    "vv_Garr39_%=:\n\t" \
    "ds_write_b128 v3, v[36:39]\n\t" \
    "v_smfmac_f32_16x16x64_bf16 v[72:75], v[36:39], a[16:23], v10\n\t" \
    "s_waitcnt vmcnt(1)\n\t" \
    "ds_write_b32 v22, v141 offset:19712\n\t" \
    "s_waitcnt vmcnt(0)\n\t" \
    "ds_write_b128 v3, v[40:43] offset:1024\n\t" \
    "v_max3_u32 v140, v36, v40, v141\n\t" \
    "v_cmp_eq_u32 vcc, -1, v140\n\t" \
    "s_cbranch_vccnz vv_Gbad35_%=\n\t" \
    "vv_Gc36_%=:\n\t" \
    "s_waitcnt lgkmcnt(0)\n\t" \
    "s_barrier\n\t" \
    "ds_read_b128 v[48:51], v4\n\t" \
    "ds_read_b128 v[52:55], v5\n\t" \
    "v_smfmac_f32_16x16x64_bf16 v[76:79], v[36:39], a[88:95], v10\n\t" \
    "ds_read_b128 v[56:59], v6\n\t" \
    "ds_read_b128 v[60:63], v7\n\t" \
    "v_smfmac_f32_16x16x64_bf16 v[72:75], v[40:43], a[24:31], v10\n\t" \
    "ds_read_b128 v[64:67], v8\n\t" \
    "v_smfmac_f32_16x16x64_bf16 v[76:79], v[40:43], a[96:103], v10\n\t" \
    "ds_read_b128 v[68:71], v9\n\t" \
    "s_waitcnt lgkmcnt(5)\n\t" \
    "v_smfmac_f32_16x16x64_bf16 v[72:75], v[48:51], a[32:39], v10\n\t" \
    "ds_read_b128 v[44:47], v0 offset:19456\n\t" \
    "s_waitcnt lgkmcnt(5)\n\t" \
    "v_smfmac_f32_16x16x64_bf16 v[72:75], v[52:55], a[40:47], v10\n\t" \
    "s_waitcnt lgkmcnt(4)\n\t" \
    "v_smfmac_f32_16x16x64_bf16 v[72:75], v[56:59], a[48:55], v10\n\t" \
    "s_waitcnt lgkmcnt(3)\n\t" \
    "v_smfmac_f32_16x16x64_bf16 v[72:75], v[60:63], a[56:63], v10\n\t" \
    "s_waitcnt lgkmcnt(2)\n\t" \
    "v_smfmac_f32_16x16x64_bf16 v[72:75], v[64:67], a[0:7], v10\n\t" \
    "s_waitcnt lgkmcnt(1)\n\t" \
    "v_smfmac_f32_16x16x64_bf16 v[72:75], v[68:71], a[8:15], v10\n\t" \
    "s_waitcnt lgkmcnt(0)\n\t" \
    "v_smfmac_f32_16x16x64_bf16 v[72:75], v[44:47], a[64:71], v10\n\t" \
    "s_nop 2\n\t" \
    "v_smfmac_f32_16x16x64_bf16 v[76:79], v[48:51], a[104:111], v10\n\t" \
    "s_nop 2\n\t" \
    "v_smfmac_f32_16x16x64_bf16 v[76:79], v[52:55], a[112:119], v10\n\t" \
    "v_add_f32 v72, v72, v74\n\t" \
    "v_smfmac_f32_16x16x64_bf16 v[76:79], v[56:59], a[120:127], v10\n\t" \
    "v_add_f32 v73, v73, v75\n\t" \
    "v_smfmac_f32_16x16x64_bf16 v[76:79], v[60:63], a[128:135], v10\n\t" \
    "v_cvt_pk_bf16_f32 v80, v72, v73\n\t" \
    "v_smfmac_f32_16x16x64_bf16 v[76:79], v[64:67], a[72:79], v10\n\t" \
    "ds_write_b32 v12, v80 offset:0\n\t" \
    "v_smfmac_f32_16x16x64_bf16 v[76:79], v[68:71], a[80:87], v10\n\t" \
    "v_smfmac_f32_16x16x64_bf16 v[76:79], v[44:47], a[136:143], v10\n\t" \
    "s_nop 6\n\t" \
    "v_cmp_u_f32 vcc, v72, v73\n\t" \
    "v_pk_add_f32 v[76:77], v[76:77], v[78:79]\n\t" \
    "v_cvt_pk_bf16_f32 v81, v76, v77\n\t" \
    "ds_write_b32 v12, v81 offset:256\n\t" \
    "s_cbranch_vccnz vv_Gslow37_%=\n\t" \
    "vv_Gback38_%=:\n\t" \
    "s_waitcnt lgkmcnt(0)\n\t" \
    "s_barrier\n\t" \
    "ds_read_b64_tr_b16 v[84:85], v13 offset:0\n\t" \
    "v_smfmac_f32_16x16x64_bf16 v[100:103], v[36:39], a[0:7], v10\n\t" \
    "ds_read_b64_tr_b16 v[86:87], v13 offset:64\n\t" \
    "ds_read_b64_tr_b16 v[92:93], v13 offset:1152\n\t" \
    "v_smfmac_f32_16x16x64_bf16 v[100:103], v[36:39], a[0:7], v10\n\t" \
    "ds_read_b64_tr_b16 v[94:95], v13 offset:1216\n\t" \
    "ds_read_b64_tr_b16 v[88:89], v25 offset:0\n\t" \
    "v_smfmac_f32_16x16x64_bf16 v[100:103], v[36:39], a[0:7], v10\n\t" \
    "ds_read_b64_tr_b16 v[90:91], v25 offset:64\n\t" \
    "s_waitcnt lgkmcnt(4)\n\t" \
    "v_smfmac_f32_16x16x64_bf16 v[124:127], v[84:87], v[240:247], v19\n\t" \
    "ds_read_b64_tr_b16 v[96:97], v25 offset:1152\n\t" \
    "ds_read_b64_tr_b16 v[98:99], v25 offset:1216\n\t" \
    "s_waitcnt lgkmcnt(4)\n\t" \
    "v_smfmac_f32_16x16x64_bf16 v[124:127], v[92:95], v[248:255], v19\n\t" \
    "s_nop 2\n\t" \
    "v_smfmac_f32_16x16x64_bf16 v[108:111], v[84:87], a[144:151], v19\n\t" \
    "s_nop 2\n\t" \
    "v_smfmac_f32_16x16x64_bf16 v[108:111], v[92:95], a[160:167], v19\n\t" \
    "s_nop 1\n\t" \
    "v_permlane32_swap_b32 v124, v126\n\t" \
    "v_smfmac_f32_16x16x64_bf16 v[112:115], v[84:87], a[176:183], v19\n\t" \
    "s_nop 1\n\t" \
    "v_permlane32_swap_b32 v125, v127\n\t" \
    "v_smfmac_f32_16x16x64_bf16 v[112:115], v[92:95], a[192:199], v19\n\t" \
    "s_nop 0\n\t" \
    "v_add_f32 v124, v124, v126\n\t" \
    "s_waitcnt lgkmcnt(2)\n\t" \
    "v_smfmac_f32_16x16x64_bf16 v[108:111], v[88:91], a[152:159], v19\n\t" \
    "s_nop 1\n\t" \
    "v_add_f32 v125, v125, v127\n\t" \
    "v_smfmac_f32_16x16x64_bf16 v[112:115], v[88:91], a[184:191], v19\n\t" \
    "s_nop 0\n\t" \
    "v_cvt_pk_bf16_f32 v136, v124, v125\n\t" \
    "s_waitcnt lgkmcnt(0)\n\t" \
    "v_smfmac_f32_16x16x64_bf16 v[108:111], v[96:99], a[168:175], v19\n\t" \
    "s_nop 1\n\t" \
    "v_cvt_pk_bf16_f32 v137, v124, v125\n\t" \
    "v_smfmac_f32_16x16x64_bf16 v[112:115], v[96:99], a[200:207], v19\n\t" \
    "s_nop 1\n\t" \
    "v_permlane32_swap_b32 v136, v137\n\t" \
    "v_smfmac_f32_16x16x64_bf16 v[116:119], v[84:87], a[208:215], v19\n\t" \
    "s_mov_b32 exec_hi, 0\n\t" \
    "global_store_dwordx2 v153, v[136:137], s[38:39] sc1\n\t" \
    "s_mov_b32 exec_hi, -1\n\t" \
    "v_cvt_pk_bf16_f32 v128, v108, v109\n\t" \
    "v_smfmac_f32_16x16x64_bf16 v[116:119], v[88:91], a[216:223], v19\n\t" \
    "s_nop 1\n\t" \
    "v_cvt_pk_bf16_f32 v129, v110, v111\n\t" \
    "v_smfmac_f32_16x16x64_bf16 v[116:119], v[92:95], a[224:231], v19\n\t" \
    "s_nop 1\n\t" \
    "v_cvt_pk_bf16_f32 v130, v112, v113\n\t" \
    "v_smfmac_f32_16x16x64_bf16 v[116:119], v[96:99], a[232:239], v19\n\t" \
    "s_nop 1\n\t" \
    "v_cvt_pk_bf16_f32 v131, v114, v115\n\t" \
    "v_smfmac_f32_16x16x64_bf16 v[120:123], v[84:87], a[240:247], v19\n\t" \
    "s_nop 1\n\t" \
    "v_permlane16_swap_b32 v128, v130\n\t" \
    "v_smfmac_f32_16x16x64_bf16 v[120:123], v[88:91], a[248:255], v19\n\t" \
    "s_nop 1\n\t" \
    "v_permlane16_swap_b32 v129, v131\n\t" \
    "v_smfmac_f32_16x16x64_bf16 v[120:123], v[92:95], v[224:231], v19\n\t" \
    "s_nop 0\n\t" \
    "global_store_dwordx4 v151, v[128:131], s[38:39] sc1\n\t" \
    "v_cvt_pk_bf16_f32 v132, v116, v117\n\t" \
    "v_smfmac_f32_16x16x64_bf16 v[120:123], v[96:99], v[232:239], v19\n\t" \
    "s_nop 1\n\t" \
    "v_cvt_pk_bf16_f32 v133, v118, v119\n\t" \
    "s_nop 4\n\t" \
    "v_cvt_pk_bf16_f32 v134, v120, v121\n\t" \
    "v_cvt_pk_bf16_f32 v135, v122, v123\n\t" \
    "s_nop 0\n\t" \
    "v_permlane16_swap_b32 v132, v134\n\t" \
    "v_permlane16_swap_b32 v133, v135\n\t" \
    "global_store_dwordx4 v152, v[132:135], s[38:39] sc1\n\t" \
    "s_cmp_eq_u32 s65, 63\n\t" \
    "s_cbranch_scc1 vv_end30_%=\n\t" \
    "s_add_u32 s65, s65, 1\n\t" \
    "v_mov_b32 v72, 0\n\t" \
    "v_mov_b32 v73, 0\n\t" \
    "v_mov_b32 v74, 0\n\t" \
    "v_mov_b32 v75, 0\n\t" \
    "v_mov_b32 v76, 0\n\t" \
    "v_mov_b32 v77, 0\n\t" \
    "v_mov_b32 v78, 0\n\t" \
    "v_mov_b32 v79, 0\n\t" \
    "v_mov_b32 v108, 0\n\t" \
    "v_mov_b32 v109, 0\n\t" \
    "v_mov_b32 v110, 0\n\t" \
    "v_mov_b32 v111, 0\n\t" \
    "v_mov_b32 v112, 0\n\t" \
    "v_mov_b32 v113, 0\n\t" \
    "v_mov_b32 v114, 0\n\t" \
    "v_mov_b32 v115, 0\n\t" \
    "v_mov_b32 v116, 0\n\t" \
    "v_mov_b32 v117, 0\n\t" \
    "v_mov_b32 v118, 0\n\t" \
    "v_mov_b32 v119, 0\n\t" \
    "v_mov_b32 v120, 0\n\t" \
    "v_mov_b32 v121, 0\n\t" \
    "v_mov_b32 v122, 0\n\t" \
    "v_mov_b32 v123, 0\n\t" \
    "v_mov_b32 v124, 0\n\t" \
    "v_mov_b32 v125, 0\n\t" \
    "v_mov_b32 v126, 0\n\t" \
    "v_mov_b32 v127, 0\n\t" \
    "s_add_u32 s69, s65, 0\n\t" \
    "s_and_b32 s69, s69, 7\n\t" \
    "s_lshl_b32 s69, s69, 18\n\t" \
    "s_add_u32 s69, s69, s62\n\t" \
    "s_add_u32 s46, s36, s69\n\t" \
    "s_addc_u32 s47, s37, 0\n\t" \
    "s_bitcmp0_b32 s65, 0\n\t" \
    "s_cselect_b32 s69, 16384, 0\n\t" \
    "v_xor_b32 v1, s69, v1\n\t" \
    "v_xor_b32 v2, s69, v2\n\t" \
    "v_xor_b32 v23, s69, v23\n\t" \
    "s_add_u32 s70, s65, 0\n\t" \
    "s_mul_i32 s69, s70, 171\n\t" \
    "s_lshr_b32 s69, s69, 9\n\t" \
    "s_mul_i32 s71, s69, 3\n\t" \
    "s_sub_u32 s70, s70, s71\n\t" \
    "s_mul_i32 s70, s70, 589824\n\t" \
    "s_lshl1_add_u32 s71, s69, 4\n\t" \
    "s_lshr_b64 s[38:39], s[44:45], s71\n\t" \
    "s_lshl_b32 s71, s66, 1\n\t" \
    "s_xor_b32 s71, s71, s38\n\t" \
    "s_and_b32 s71, s71, 3\n\t" \
    "s_lshl_b32 s71, s71, 13\n\t" \
    "s_add_u32 s70, s70, s71\n\t" \
    "s_lshl_b32 s69, s69, 21\n\t" \
    "s_add_u32 s69, s69, 123731968\n\t" \
    "v_add_u32 v151, s70, v14\n\t" \
    "v_and_b32 v150, 0x80000, v151\n\t" \
    "v_lshrrev_b32 v150, 5, v150\n\t" \
    "v_xor_b32 v151, v151, v150\n\t" \
    "v_add_u32 v152, s70, v15\n\t" \
    "v_and_b32 v150, 0x80000, v152\n\t" \
    "v_lshrrev_b32 v150, 5, v150\n\t" \
    "v_xor_b32 v152, v152, v150\n\t" \
    "v_add_u32 v153, s70, v16\n\t" \
    "v_and_b32 v150, 0x80000, v153\n\t" \
    "v_lshrrev_b32 v150, 5, v150\n\t" \
    "v_xor_b32 v153, v153, v150\n\t" \
    "s_add_u32 s38, s36, s69\n\t" \
    "s_addc_u32 s39, s37, 0\n\t" \
    "s_and_b32 s69, s69, 0xffe00000\n\t" \
    "s_add_u32 s69, s69, 1807360\n\t" \
    "v_mov_b32 v17, s69\n\t" \
    "global_load_dword v17, v17, s[36:37]\n\t" \
    "s_and_b32 s69, s65, 7\n\t" \
    "s_lshl_b32 s69, s69, 18\n\t" \
    "s_add_u32 s69, s69, s63\n\t" \
    "s_add_u32 s52, s36, s69\n\t" \
    "s_addc_u32 s53, s37, 0\n\t" \
    "s_barrier\n\t" \
    "ds_read_b32 v160, v11 offset:20480\n\t" \
    "s_waitcnt lgkmcnt(0)\n\t" \
    "v_readfirstlane_b32 s70, v160\n\t" \
    "s_branch vv_loop12_%=\n\t" \
    "vv_Gbad35_%=:\n\t" \
    "s_waitcnt vmcnt(0) lgkmcnt(0)\n\t" \
    "s_nop 7\n\t" \
    "v_mov_b32 v72, 0\n\t" \
    "v_mov_b32 v73, 0\n\t" \
    "v_mov_b32 v74, 0\n\t" \
    "v_mov_b32 v75, 0\n\t" \
    "v_mov_b32 v76, 0\n\t" \
    "v_mov_b32 v77, 0\n\t" \
    "v_mov_b32 v78, 0\n\t" \
    "v_mov_b32 v79, 0\n\t" \
    "s_cmp_eq_u32 s68, 0\n\t" \
    "s_cbranch_scc1 vv_F40_%=\n\t" \
    "s_sub_u32 s68, s68, 1\n\t" \
    "s_branch vv_G34_%=\n\t" \
    "vv_F40_%=:\n\t" \
    "v_mov_b32 v140, 5767424\n\t" \
    "v_mov_b32 v141, 1\n\t" \
    "global_store_dword v140, v141, s[36:37] sc1\n\t" \
    "s_waitcnt vmcnt(0)\n\t" \
    "s_branch vv_Gc36_%=\n\t" \
    "vv_Gslow37_%=:\n\t" \
    "v_max_u32 v140, v36, v37\n\t" \
    "v_max3_u32 v140, v140, v38, v39\n\t" \
    "v_max3_u32 v140, v140, v40, v41\n\t" \
    "v_max3_u32 v140, v140, v42, v43\n\t" \
    "v_max3_u32 v140, v140, v44, v45\n\t" \
    "v_max3_u32 v140, v140, v46, v47\n\t" \
    "v_max3_u32 v140, v140, v48, v49\n\t" \
    "v_max3_u32 v140, v140, v50, v51\n\t" \
    "v_max3_u32 v140, v140, v52, v53\n\t" \
    "v_max3_u32 v140, v140, v54, v55\n\t" \
    "v_max3_u32 v140, v140, v56, v57\n\t" \
    "v_max3_u32 v140, v140, v58, v59\n\t" \
    "v_max3_u32 v140, v140, v60, v61\n\t" \
    "v_max3_u32 v140, v140, v62, v63\n\t" \
    "v_max3_u32 v140, v140, v64, v65\n\t" \
    "v_max3_u32 v140, v140, v66, v67\n\t" \
    "v_max3_u32 v140, v140, v68, v69\n\t" \
    "v_max3_u32 v140, v140, v70, v71\n\t" \
    "v_cmp_eq_u32 vcc, -1, v140\n\t" \
    "s_cbranch_vccz vv_Gback38_%=\n\t" \
    "s_waitcnt lgkmcnt(0)\n\t" \
    "v_sub_u32 v140, v1, v3\n\t" \
    "s_nop 0\n\t" \
    "v_readfirstlane_b32 s69, v140\n\t" \
    "v_add_u32 v143, 36864, v0\n\t" \
    "s_nop 7\n\t" \
    "s_add_u32 s30, s46, s69\n\t" \
    "s_addc_u32 s31, s47, 0\n\t" \
    "vv_Gag41_%=:\n\t" \
    "global_load_dwordx4 v[36:39], v1, s[46:47] sc1\n\t" \
    "global_load_dwordx4 v[40:43], v23, s[46:47] sc1\n\t" \
    "global_load_dwordx4 v[44:47], v143, s[30:31] sc1\n\t" \
    "global_load_dwordx4 v[48:51], v4, s[30:31] sc1\n\t" \
    "global_load_dwordx4 v[52:55], v5, s[30:31] sc1\n\t" \
    "global_load_dwordx4 v[56:59], v6, s[30:31] sc1\n\t" \
    "global_load_dwordx4 v[60:63], v7, s[30:31] sc1\n\t" \
    "global_load_dwordx4 v[64:67], v8, s[30:31] sc1\n\t" \
    "global_load_dwordx4 v[68:71], v9, s[30:31] sc1\n\t" \
    "s_waitcnt vmcnt(0)\n\t" \
    "v_max_u32 v140, v36, v37\n\t" \
    "v_max3_u32 v140, v140, v38, v39\n\t" \
    "v_max3_u32 v140, v140, v40, v41\n\t" \
    "v_max3_u32 v140, v140, v42, v43\n\t" \
    "v_max3_u32 v140, v140, v44, v45\n\t" \
    "v_max3_u32 v140, v140, v46, v47\n\t" \
    "v_max3_u32 v140, v140, v48, v49\n\t" \
    "v_max3_u32 v140, v140, v50, v51\n\t" \
    "v_max3_u32 v140, v140, v52, v53\n\t" \
    "v_max3_u32 v140, v140, v54, v55\n\t" \
    "v_max3_u32 v140, v140, v56, v57\n\t" \
    "v_max3_u32 v140, v140, v58, v59\n\t" \
    "v_max3_u32 v140, v140, v60, v61\n\t" \
    "v_max3_u32 v140, v140, v62, v63\n\t" \
    "v_max3_u32 v140, v140, v64, v65\n\t" \
    "v_max3_u32 v140, v140, v66, v67\n\t" \
    "v_max3_u32 v140, v140, v68, v69\n\t" \
    "v_max3_u32 v140, v140, v70, v71\n\t" \
    "v_cmp_eq_u32 vcc, -1, v140\n\t" \
    "s_cbranch_vccz vv_Gredo42_%=\n\t" \
    "s_cmp_eq_u32 s68, 0\n\t" \
    "s_cbranch_scc1 vv_F43_%=\n\t" \
    "s_sub_u32 s68, s68, 1\n\t" \
    "s_sleep 1\n\t" \
    "s_branch vv_Gag41_%=\n\t" \
    "vv_F43_%=:\n\t" \
    "v_mov_b32 v140, 5767424\n\t" \
    "v_mov_b32 v141, 2\n\t" \
    "global_store_dword v140, v141, s[36:37] sc1\n\t" \
    "s_waitcnt vmcnt(0)\n\t" \
    "s_branch vv_Gredo42_%=\n\t" \
    "vv_Gredo42_%=:\n\t" \
    "v_mov_b32 v72, 0\n\t" \
    "v_mov_b32 v73, 0\n\t" \
    "v_mov_b32 v74, 0\n\t" \
    "v_mov_b32 v75, 0\n\t" \
    "v_mov_b32 v76, 0\n\t" \
    "v_mov_b32 v77, 0\n\t" \
    "v_mov_b32 v78, 0\n\t" \
    "v_mov_b32 v79, 0\n\t" \
    "v_smfmac_f32_16x16x64_bf16 v[72:75], v[36:39], a[16:23], v10\n\t" \
    "v_smfmac_f32_16x16x64_bf16 v[72:75], v[40:43], a[24:31], v10\n\t" \
    "v_smfmac_f32_16x16x64_bf16 v[72:75], v[48:51], a[32:39], v10\n\t" \
    "v_smfmac_f32_16x16x64_bf16 v[72:75], v[52:55], a[40:47], v10\n\t" \
    "v_smfmac_f32_16x16x64_bf16 v[72:75], v[56:59], a[48:55], v10\n\t" \
    "v_smfmac_f32_16x16x64_bf16 v[72:75], v[60:63], a[56:63], v10\n\t" \
    "v_smfmac_f32_16x16x64_bf16 v[72:75], v[64:67], a[0:7], v10\n\t" \
    "v_smfmac_f32_16x16x64_bf16 v[72:75], v[68:71], a[8:15], v10\n\t" \
    "v_smfmac_f32_16x16x64_bf16 v[72:75], v[44:47], a[64:71], v10\n\t" \
    "v_smfmac_f32_16x16x64_bf16 v[76:79], v[36:39], a[88:95], v10\n\t" \
    "v_smfmac_f32_16x16x64_bf16 v[76:79], v[40:43], a[96:103], v10\n\t" \
    "v_smfmac_f32_16x16x64_bf16 v[76:79], v[48:51], a[104:111], v10\n\t" \
    "v_smfmac_f32_16x16x64_bf16 v[76:79], v[52:55], a[112:119], v10\n\t" \
    "v_smfmac_f32_16x16x64_bf16 v[76:79], v[56:59], a[120:127], v10\n\t" \
    "v_smfmac_f32_16x16x64_bf16 v[76:79], v[60:63], a[128:135], v10\n\t" \
    "v_smfmac_f32_16x16x64_bf16 v[76:79], v[64:67], a[72:79], v10\n\t" \
    "v_smfmac_f32_16x16x64_bf16 v[76:79], v[68:71], a[80:87], v10\n\t" \
    "v_smfmac_f32_16x16x64_bf16 v[76:79], v[44:47], a[136:143], v10\n\t" \
    "v_add_f32 v72, v72, v74\n\t" \
    "v_add_f32 v73, v73, v75\n\t" \
    "v_cvt_pk_bf16_f32 v80, v72, v73\n\t" \
    "ds_write_b32 v12, v80 offset:0\n\t" \
    "s_nop 3\n\t" \
    "v_pk_add_f32 v[76:77], v[76:77], v[78:79]\n\t" \
    "v_cvt_pk_bf16_f32 v81, v76, v77\n\t" \
    "ds_write_b32 v12, v81 offset:256\n\t" \
    "s_branch vv_Gback38_%=\n\t" \
    "vv_end30_%=:\n\t" \
    "s_endpgm\n\t" \
    ".p2align 6\n\t" \
    "s_nop 0\n\t" \
    "s_nop 0\n\t" \
    "s_nop 0\n\t" \
    "s_nop 0\n\t" \
    "s_nop 0\n\t" \
    "s_nop 0\n\t" \
    "vv_loop13_%=:\n\t" \
    "s_getpc_b64 s[30:31]\n\t" \
    "vv_Wp46_%=:\n\t" \
    "s_add_u32 s30, s30, vv_We47_%=-vv_Wp46_%=\n\t" \
    "s_addc_u32 s31, s31, 0\n\t" \
    "s_sub_u32 s70, s70, -54\n\t" \
    "vv_W45_%=:\n\t" \
    "s_memtime s[56:57]\n\t" \
    "s_waitcnt lgkmcnt(0)\n\t" \
    "s_sub_u32 s69, s70, s56\n\t" \
    "s_cmp_gt_i32 s69, 80\n\t" \
    "s_cbranch_scc1 vv_W45_%=\n\t" \
    "s_max_i32 s69, s69, 0\n\t" \
    "s_and_b32 s69, s69, -4\n\t" \
    "s_sub_u32 s30, s30, s69\n\t" \
    "s_subb_u32 s31, s31, 0\n\t" \
    "s_setpc_b64 s[30:31]\n\t" \
    "s_nop 0\n\t" \
    "s_nop 0\n\t" \
    "s_nop 0\n\t" \
    "s_nop 0\n\t" \
    "s_nop 0\n\t" \
    "s_nop 0\n\t" \
    "s_nop 0\n\t" \
    "s_nop 0\n\t" \
    "s_nop 0\n\t" \
    "s_nop 0\n\t" \
    "s_nop 0\n\t" \
    "s_nop 0\n\t" \
    "s_nop 0\n\t" \
    "s_nop 0\n\t" \
    "s_nop 0\n\t" \
    "s_nop 0\n\t" \
    "s_nop 0\n\t" \
    "s_nop 0\n\t" \
    "s_nop 0\n\t" \
    "s_nop 0\n\t" \
    "vv_We47_%=:\n\t" \
    "vv_G48_%=:\n\t" \
    "global_load_dwordx4 v[36:39], v1, s[46:47] sc1\n\t" \
    "global_load_dword v141, v2, s[46:47] sc1\n\t" \
    "global_load_dwordx4 v[40:43], v23, s[46:47] sc1\n\t" \
    "s_waitcnt vmcnt(2)\n\t" \
    "vv_Garr53_%=:\n\t" \
    "ds_write_b128 v3, v[36:39]\n\t" \
    "v_smfmac_f32_16x16x64_bf16 v[72:75], v[36:39], a[32:39], v10\n\t" \
    "s_waitcnt vmcnt(1)\n\t" \
    "ds_write_b32 v22, v141 offset:19968\n\t" \
    "s_waitcnt vmcnt(0)\n\t" \
    "ds_write_b128 v3, v[40:43] offset:1024\n\t" \
    "v_max3_u32 v140, v36, v40, v141\n\t" \
    "v_cmp_eq_u32 vcc, -1, v140\n\t" \
    "s_cbranch_vccnz vv_Gbad49_%=\n\t" \
    "vv_Gc50_%=:\n\t" \
    "s_waitcnt lgkmcnt(0)\n\t" \
    "s_barrier\n\t" \
    "ds_read_b128 v[48:51], v4\n\t" \
    "ds_read_b128 v[52:55], v5\n\t" \
    "v_smfmac_f32_16x16x64_bf16 v[76:79], v[36:39], a[104:111], v10\n\t" \
    "ds_read_b128 v[56:59], v6\n\t" \
    "ds_read_b128 v[60:63], v7\n\t" \
    "v_smfmac_f32_16x16x64_bf16 v[72:75], v[40:43], a[40:47], v10\n\t" \
    "ds_read_b128 v[64:67], v8\n\t" \
    "v_smfmac_f32_16x16x64_bf16 v[76:79], v[40:43], a[112:119], v10\n\t" \
    "ds_read_b128 v[68:71], v9\n\t" \
    "s_waitcnt lgkmcnt(5)\n\t" \
    "v_smfmac_f32_16x16x64_bf16 v[72:75], v[48:51], a[48:55], v10\n\t" \
    "ds_read_b128 v[44:47], v0 offset:19456\n\t" \
    "s_waitcnt lgkmcnt(5)\n\t" \
    "v_smfmac_f32_16x16x64_bf16 v[72:75], v[52:55], a[56:63], v10\n\t" \
    "s_waitcnt lgkmcnt(4)\n\t" \
    "v_smfmac_f32_16x16x64_bf16 v[72:75], v[56:59], a[0:7], v10\n\t" \
    "s_waitcnt lgkmcnt(3)\n\t" \
    "v_smfmac_f32_16x16x64_bf16 v[72:75], v[60:63], a[8:15], v10\n\t" \
    "s_waitcnt lgkmcnt(2)\n\t" \
    "v_smfmac_f32_16x16x64_bf16 v[72:75], v[64:67], a[16:23], v10\n\t" \
    "s_waitcnt lgkmcnt(1)\n\t" \
    "v_smfmac_f32_16x16x64_bf16 v[72:75], v[68:71], a[24:31], v10\n\t" \
    "s_waitcnt lgkmcnt(0)\n\t" \
    "v_smfmac_f32_16x16x64_bf16 v[72:75], v[44:47], a[64:71], v10\n\t" \
    "s_nop 2\n\t" \
    "v_smfmac_f32_16x16x64_bf16 v[76:79], v[48:51], a[120:127], v10\n\t" \
    "s_nop 2\n\t" \
    "v_smfmac_f32_16x16x64_bf16 v[76:79], v[52:55], a[128:135], v10\n\t" \
    "v_add_f32 v72, v72, v74\n\t" \
    "v_smfmac_f32_16x16x64_bf16 v[76:79], v[56:59], a[72:79], v10\n\t" \
    "v_add_f32 v73, v73, v75\n\t" \
    "v_smfmac_f32_16x16x64_bf16 v[76:79], v[60:63], a[80:87], v10\n\t" \
    "v_cvt_pk_bf16_f32 v80, v72, v73\n\t" \
    "v_smfmac_f32_16x16x64_bf16 v[76:79], v[64:67], a[88:95], v10\n\t" \
    "ds_write_b32 v12, v80 offset:0\n\t" \
    "v_smfmac_f32_16x16x64_bf16 v[76:79], v[68:71], a[96:103], v10\n\t" \
    "v_smfmac_f32_16x16x64_bf16 v[76:79], v[44:47], a[136:143], v10\n\t" \
    "s_nop 6\n\t" \
    "v_cmp_u_f32 vcc, v72, v73\n\t" \
    "v_pk_add_f32 v[76:77], v[76:77], v[78:79]\n\t" \
    "v_cvt_pk_bf16_f32 v81, v76, v77\n\t" \
    "ds_write_b32 v12, v81 offset:256\n\t" \
    "s_cbranch_vccnz vv_Gslow51_%=\n\t" \
    "vv_Gback52_%=:\n\t" \
    "s_waitcnt lgkmcnt(0)\n\t" \
    "s_barrier\n\t" \
    "ds_read_b64_tr_b16 v[84:85], v13 offset:0\n\t" \
    "v_smfmac_f32_16x16x64_bf16 v[100:103], v[36:39], a[0:7], v10\n\t" \
    "ds_read_b64_tr_b16 v[86:87], v13 offset:64\n\t" \
    "ds_read_b64_tr_b16 v[92:93], v13 offset:1152\n\t" \
    "v_smfmac_f32_16x16x64_bf16 v[100:103], v[36:39], a[0:7], v10\n\t" \
    "ds_read_b64_tr_b16 v[94:95], v13 offset:1216\n\t" \
    "ds_read_b64_tr_b16 v[88:89], v25 offset:0\n\t" \
    "v_smfmac_f32_16x16x64_bf16 v[100:103], v[36:39], a[0:7], v10\n\t" \
    "ds_read_b64_tr_b16 v[90:91], v25 offset:64\n\t" \
    "s_waitcnt lgkmcnt(4)\n\t" \
    "v_smfmac_f32_16x16x64_bf16 v[124:127], v[84:87], v[240:247], v19\n\t" \
    "ds_read_b64_tr_b16 v[96:97], v25 offset:1152\n\t" \
    "ds_read_b64_tr_b16 v[98:99], v25 offset:1216\n\t" \
    "s_waitcnt lgkmcnt(4)\n\t" \
    "v_smfmac_f32_16x16x64_bf16 v[124:127], v[92:95], v[248:255], v19\n\t" \
    "s_nop 2\n\t" \
    "v_smfmac_f32_16x16x64_bf16 v[108:111], v[84:87], a[144:151], v19\n\t" \
    "s_nop 2\n\t" \
    "v_smfmac_f32_16x16x64_bf16 v[108:111], v[92:95], a[160:167], v19\n\t" \
    "s_nop 1\n\t" \
    "v_permlane32_swap_b32 v124, v126\n\t" \
    "v_smfmac_f32_16x16x64_bf16 v[112:115], v[84:87], a[176:183], v19\n\t" \
    "s_nop 1\n\t" \
    "v_permlane32_swap_b32 v125, v127\n\t" \
    "v_smfmac_f32_16x16x64_bf16 v[112:115], v[92:95], a[192:199], v19\n\t" \
    "s_nop 0\n\t" \
    "v_add_f32 v124, v124, v126\n\t" \
    "s_waitcnt lgkmcnt(2)\n\t" \
    "v_smfmac_f32_16x16x64_bf16 v[108:111], v[88:91], a[152:159], v19\n\t" \
    "s_nop 1\n\t" \
    "v_add_f32 v125, v125, v127\n\t" \
    "v_smfmac_f32_16x16x64_bf16 v[112:115], v[88:91], a[184:191], v19\n\t" \
    "s_nop 0\n\t" \
    "v_cvt_pk_bf16_f32 v136, v124, v125\n\t" \
    "s_waitcnt lgkmcnt(0)\n\t" \
    "v_smfmac_f32_16x16x64_bf16 v[108:111], v[96:99], a[168:175], v19\n\t" \
    "s_nop 1\n\t" \
    "v_cvt_pk_bf16_f32 v137, v124, v125\n\t" \
    "v_smfmac_f32_16x16x64_bf16 v[112:115], v[96:99], a[200:207], v19\n\t" \
    "s_nop 1\n\t" \
    "v_permlane32_swap_b32 v136, v137\n\t" \
    "v_smfmac_f32_16x16x64_bf16 v[116:119], v[84:87], a[208:215], v19\n\t" \
    "s_mov_b32 exec_hi, 0\n\t" \
    "global_store_dwordx2 v153, v[136:137], s[38:39] sc1\n\t" \
    "s_mov_b32 exec_hi, -1\n\t" \
    "v_cvt_pk_bf16_f32 v128, v108, v109\n\t" \
    "v_smfmac_f32_16x16x64_bf16 v[116:119], v[88:91], a[216:223], v19\n\t" \
    "s_nop 1\n\t" \
    "v_cvt_pk_bf16_f32 v129, v110, v111\n\t" \
    "v_smfmac_f32_16x16x64_bf16 v[116:119], v[92:95], a[224:231], v19\n\t" \
    "s_nop 1\n\t" \
    "v_cvt_pk_bf16_f32 v130, v112, v113\n\t" \
    "v_smfmac_f32_16x16x64_bf16 v[116:119], v[96:99], a[232:239], v19\n\t" \
    "s_nop 1\n\t" \
    "v_cvt_pk_bf16_f32 v131, v114, v115\n\t" \
    "v_smfmac_f32_16x16x64_bf16 v[120:123], v[84:87], a[240:247], v19\n\t" \
    "s_nop 1\n\t" \
    "v_permlane16_swap_b32 v128, v130\n\t" \
    "v_smfmac_f32_16x16x64_bf16 v[120:123], v[88:91], a[248:255], v19\n\t" \
    "s_nop 1\n\t" \
    "v_permlane16_swap_b32 v129, v131\n\t" \
    "v_smfmac_f32_16x16x64_bf16 v[120:123], v[92:95], v[224:231], v19\n\t" \
    "s_nop 0\n\t" \
    "global_store_dwordx4 v151, v[128:131], s[38:39] sc1\n\t" \
    "v_cvt_pk_bf16_f32 v132, v116, v117\n\t" \
    "v_smfmac_f32_16x16x64_bf16 v[120:123], v[96:99], v[232:239], v19\n\t" \
    "s_nop 1\n\t" \
    "v_cvt_pk_bf16_f32 v133, v118, v119\n\t" \
    "s_nop 4\n\t" \
    "v_cvt_pk_bf16_f32 v134, v120, v121\n\t" \
    "v_cvt_pk_bf16_f32 v135, v122, v123\n\t" \
    "s_nop 0\n\t" \
    "v_permlane16_swap_b32 v132, v134\n\t" \
    "v_permlane16_swap_b32 v133, v135\n\t" \
    "global_store_dwordx4 v152, v[132:135], s[38:39] sc1\n\t" \
    "s_cmp_eq_u32 s65, 63\n\t" \
    "s_cbranch_scc1 vv_end44_%=\n\t" \
    "s_add_u32 s65, s65, 1\n\t" \
    "v_mov_b32 v72, 0\n\t" \
    "v_mov_b32 v73, 0\n\t" \
    "v_mov_b32 v74, 0\n\t" \
    "v_mov_b32 v75, 0\n\t" \
    "v_mov_b32 v76, 0\n\t" \
    "v_mov_b32 v77, 0\n\t" \
    "v_mov_b32 v78, 0\n\t" \
    "v_mov_b32 v79, 0\n\t" \
    "v_mov_b32 v108, 0\n\t" \
    "v_mov_b32 v109, 0\n\t" \
    "v_mov_b32 v110, 0\n\t" \
    "v_mov_b32 v111, 0\n\t" \
    "v_mov_b32 v112, 0\n\t" \
    "v_mov_b32 v113, 0\n\t" \
    "v_mov_b32 v114, 0\n\t" \
    "v_mov_b32 v115, 0\n\t" \
    "v_mov_b32 v116, 0\n\t" \
    "v_mov_b32 v117, 0\n\t" \
    "v_mov_b32 v118, 0\n\t" \
    "v_mov_b32 v119, 0\n\t" \
    "v_mov_b32 v120, 0\n\t" \
    "v_mov_b32 v121, 0\n\t" \
    "v_mov_b32 v122, 0\n\t" \
    "v_mov_b32 v123, 0\n\t" \
    "v_mov_b32 v124, 0\n\t" \
    "v_mov_b32 v125, 0\n\t" \
    "v_mov_b32 v126, 0\n\t" \
    "v_mov_b32 v127, 0\n\t" \
    "s_add_u32 s69, s65, 0\n\t" \
    "s_and_b32 s69, s69, 7\n\t" \
    "s_lshl_b32 s69, s69, 18\n\t" \
    "s_add_u32 s69, s69, s62\n\t" \
    "s_add_u32 s46, s36, s69\n\t" \
    "s_addc_u32 s47, s37, 0\n\t" \
    "s_bitcmp0_b32 s65, 0\n\t" \
    "s_cselect_b32 s69, 16384, 0\n\t" \
    "v_xor_b32 v1, s69, v1\n\t" \
    "v_xor_b32 v2, s69, v2\n\t" \
    "v_xor_b32 v23, s69, v23\n\t" \
    "s_add_u32 s70, s65, 0\n\t" \
    "s_mul_i32 s69, s70, 171\n\t" \
    "s_lshr_b32 s69, s69, 9\n\t" \
    "s_mul_i32 s71, s69, 3\n\t" \
    "s_sub_u32 s70, s70, s71\n\t" \
    "s_mul_i32 s70, s70, 589824\n\t" \
    "s_lshl1_add_u32 s71, s69, 4\n\t" \
    "s_lshr_b64 s[38:39], s[44:45], s71\n\t" \
    "s_lshl_b32 s71, s66, 1\n\t" \
    "s_xor_b32 s71, s71, s38\n\t" \
    "s_and_b32 s71, s71, 3\n\t" \
    "s_lshl_b32 s71, s71, 13\n\t" \
    "s_add_u32 s70, s70, s71\n\t" \
    "s_lshl_b32 s69, s69, 21\n\t" \
    "s_add_u32 s69, s69, 123731968\n\t" \
    "v_add_u32 v151, s70, v14\n\t" \
    "v_and_b32 v150, 0x80000, v151\n\t" \
    "v_lshrrev_b32 v150, 5, v150\n\t" \
    "v_xor_b32 v151, v151, v150\n\t" \
    "v_add_u32 v152, s70, v15\n\t" \
    "v_and_b32 v150, 0x80000, v152\n\t" \
    "v_lshrrev_b32 v150, 5, v150\n\t" \
    "v_xor_b32 v152, v152, v150\n\t" \
    "v_add_u32 v153, s70, v16\n\t" \
    "v_and_b32 v150, 0x80000, v153\n\t" \
    "v_lshrrev_b32 v150, 5, v150\n\t" \
    "v_xor_b32 v153, v153, v150\n\t" \
    "s_add_u32 s38, s36, s69\n\t" \
    "s_addc_u32 s39, s37, 0\n\t" \
    "s_and_b32 s69, s69, 0xffe00000\n\t" \
    "s_add_u32 s69, s69, 1807360\n\t" \
    "v_mov_b32 v17, s69\n\t" \
    "global_load_dword v17, v17, s[36:37]\n\t" \
    "s_and_b32 s69, s65, 7\n\t" \
    "s_lshl_b32 s69, s69, 18\n\t" \
    "s_add_u32 s69, s69, s63\n\t" \
    "s_add_u32 s52, s36, s69\n\t" \
    "s_addc_u32 s53, s37, 0\n\t" \
    "s_barrier\n\t" \
    "ds_read_b32 v160, v11 offset:20480\n\t" \
    "s_waitcnt lgkmcnt(0)\n\t" \
    "v_readfirstlane_b32 s70, v160\n\t" \
    "s_branch vv_loop13_%=\n\t" \
    "vv_Gbad49_%=:\n\t" \
    "s_waitcnt vmcnt(0) lgkmcnt(0)\n\t" \
    "s_nop 7\n\t" \
    "v_mov_b32 v72, 0\n\t" \
    "v_mov_b32 v73, 0\n\t" \
    "v_mov_b32 v74, 0\n\t" \
    "v_mov_b32 v75, 0\n\t" \
    "v_mov_b32 v76, 0\n\t" \
    "v_mov_b32 v77, 0\n\t" \
    "v_mov_b32 v78, 0\n\t" \
    "v_mov_b32 v79, 0\n\t" \
    "s_cmp_eq_u32 s68, 0\n\t" \
    "s_cbranch_scc1 vv_F54_%=\n\t" \
    "s_sub_u32 s68, s68, 1\n\t" \
    "s_branch vv_G48_%=\n\t" \
    "vv_F54_%=:\n\t" \
    "v_mov_b32 v140, 5767424\n\t" \
    "v_mov_b32 v141, 1\n\t" \
    "global_store_dword v140, v141, s[36:37] sc1\n\t" \
    "s_waitcnt vmcnt(0)\n\t" \
    "s_branch vv_Gc50_%=\n\t" \
    "vv_Gslow51_%=:\n\t" \
    "v_max_u32 v140, v36, v37\n\t" \
    "v_max3_u32 v140, v140, v38, v39\n\t" \
    "v_max3_u32 v140, v140, v40, v41\n\t" \
    "v_max3_u32 v140, v140, v42, v43\n\t" \
    "v_max3_u32 v140, v140, v44, v45\n\t" \
    "v_max3_u32 v140, v140, v46, v47\n\t" \
    "v_max3_u32 v140, v140, v48, v49\n\t" \
    "v_max3_u32 v140, v140, v50, v51\n\t" \
    "v_max3_u32 v140, v140, v52, v53\n\t" \
    "v_max3_u32 v140, v140, v54, v55\n\t" \
    "v_max3_u32 v140, v140, v56, v57\n\t" \
    "v_max3_u32 v140, v140, v58, v59\n\t" \
    "v_max3_u32 v140, v140, v60, v61\n\t" \
    "v_max3_u32 v140, v140, v62, v63\n\t" \
    "v_max3_u32 v140, v140, v64, v65\n\t" \
    "v_max3_u32 v140, v140, v66, v67\n\t" \
    "v_max3_u32 v140, v140, v68, v69\n\t" \
    "v_max3_u32 v140, v140, v70, v71\n\t" \
    "v_cmp_eq_u32 vcc, -1, v140\n\t" \
    "s_cbranch_vccz vv_Gback52_%=\n\t" \
    "s_waitcnt lgkmcnt(0)\n\t" \
    "v_sub_u32 v140, v1, v3\n\t" \
    "s_nop 0\n\t" \
    "v_readfirstlane_b32 s69, v140\n\t" \
    "v_add_u32 v143, 36864, v0\n\t" \
    "s_nop 7\n\t" \
    "s_add_u32 s30, s46, s69\n\t" \
    "s_addc_u32 s31, s47, 0\n\t" \
    "vv_Gag55_%=:\n\t" \
    "global_load_dwordx4 v[36:39], v1, s[46:47] sc1\n\t" \
    "global_load_dwordx4 v[40:43], v23, s[46:47] sc1\n\t" \
    "global_load_dwordx4 v[44:47], v143, s[30:31] sc1\n\t" \
    "global_load_dwordx4 v[48:51], v4, s[30:31] sc1\n\t" \
    "global_load_dwordx4 v[52:55], v5, s[30:31] sc1\n\t" \
    "global_load_dwordx4 v[56:59], v6, s[30:31] sc1\n\t" \
    "global_load_dwordx4 v[60:63], v7, s[30:31] sc1\n\t" \
    "global_load_dwordx4 v[64:67], v8, s[30:31] sc1\n\t" \
    "global_load_dwordx4 v[68:71], v9, s[30:31] sc1\n\t" \
    "s_waitcnt vmcnt(0)\n\t" \
    "v_max_u32 v140, v36, v37\n\t" \
    "v_max3_u32 v140, v140, v38, v39\n\t" \
    "v_max3_u32 v140, v140, v40, v41\n\t" \
    "v_max3_u32 v140, v140, v42, v43\n\t" \
    "v_max3_u32 v140, v140, v44, v45\n\t" \
    "v_max3_u32 v140, v140, v46, v47\n\t" \
    "v_max3_u32 v140, v140, v48, v49\n\t" \
    "v_max3_u32 v140, v140, v50, v51\n\t" \
    "v_max3_u32 v140, v140, v52, v53\n\t" \
    "v_max3_u32 v140, v140, v54, v55\n\t" \
    "v_max3_u32 v140, v140, v56, v57\n\t" \
    "v_max3_u32 v140, v140, v58, v59\n\t" \
    "v_max3_u32 v140, v140, v60, v61\n\t" \
    "v_max3_u32 v140, v140, v62, v63\n\t" \
    "v_max3_u32 v140, v140, v64, v65\n\t" \
    "v_max3_u32 v140, v140, v66, v67\n\t" \
    "v_max3_u32 v140, v140, v68, v69\n\t" \
    "v_max3_u32 v140, v140, v70, v71\n\t" \
    "v_cmp_eq_u32 vcc, -1, v140\n\t" \
    "s_cbranch_vccz vv_Gredo56_%=\n\t" \
    "s_cmp_eq_u32 s68, 0\n\t" \
    "s_cbranch_scc1 vv_F57_%=\n\t" \
    "s_sub_u32 s68, s68, 1\n\t" \
    "s_sleep 1\n\t" \
    "s_branch vv_Gag55_%=\n\t" \
    "vv_F57_%=:\n\t" \
    "v_mov_b32 v140, 5767424\n\t" \
    "v_mov_b32 v141, 2\n\t" \
    "global_store_dword v140, v141, s[36:37] sc1\n\t" \
    "s_waitcnt vmcnt(0)\n\t" \
    "s_branch vv_Gredo56_%=\n\t" \
    "vv_Gredo56_%=:\n\t" \
    "v_mov_b32 v72, 0\n\t" \
    "v_mov_b32 v73, 0\n\t" \
    "v_mov_b32 v74, 0\n\t" \
    "v_mov_b32 v75, 0\n\t" \
    "v_mov_b32 v76, 0\n\t" \
    "v_mov_b32 v77, 0\n\t" \
    "v_mov_b32 v78, 0\n\t" \
    "v_mov_b32 v79, 0\n\t" \
    "v_smfmac_f32_16x16x64_bf16 v[72:75], v[36:39], a[32:39], v10\n\t" \
    "v_smfmac_f32_16x16x64_bf16 v[72:75], v[40:43], a[40:47], v10\n\t" \
    "v_smfmac_f32_16x16x64_bf16 v[72:75], v[48:51], a[48:55], v10\n\t" \
    "v_smfmac_f32_16x16x64_bf16 v[72:75], v[52:55], a[56:63], v10\n\t" \
    "v_smfmac_f32_16x16x64_bf16 v[72:75], v[56:59], a[0:7], v10\n\t" \
    "v_smfmac_f32_16x16x64_bf16 v[72:75], v[60:63], a[8:15], v10\n\t" \
    "v_smfmac_f32_16x16x64_bf16 v[72:75], v[64:67], a[16:23], v10\n\t" \
    "v_smfmac_f32_16x16x64_bf16 v[72:75], v[68:71], a[24:31], v10\n\t" \
    "v_smfmac_f32_16x16x64_bf16 v[72:75], v[44:47], a[64:71], v10\n\t" \
    "v_smfmac_f32_16x16x64_bf16 v[76:79], v[36:39], a[104:111], v10\n\t" \
    "v_smfmac_f32_16x16x64_bf16 v[76:79], v[40:43], a[112:119], v10\n\t" \
    "v_smfmac_f32_16x16x64_bf16 v[76:79], v[48:51], a[120:127], v10\n\t" \
    "v_smfmac_f32_16x16x64_bf16 v[76:79], v[52:55], a[128:135], v10\n\t" \
    "v_smfmac_f32_16x16x64_bf16 v[76:79], v[56:59], a[72:79], v10\n\t" \
    "v_smfmac_f32_16x16x64_bf16 v[76:79], v[60:63], a[80:87], v10\n\t" \
    "v_smfmac_f32_16x16x64_bf16 v[76:79], v[64:67], a[88:95], v10\n\t" \
    "v_smfmac_f32_16x16x64_bf16 v[76:79], v[68:71], a[96:103], v10\n\t" \
    "v_smfmac_f32_16x16x64_bf16 v[76:79], v[44:47], a[136:143], v10\n\t" \
    "v_add_f32 v72, v72, v74\n\t" \
    "v_add_f32 v73, v73, v75\n\t" \
    "v_cvt_pk_bf16_f32 v80, v72, v73\n\t" \
    "ds_write_b32 v12, v80 offset:0\n\t" \
    "s_nop 3\n\t" \
    "v_pk_add_f32 v[76:77], v[76:77], v[78:79]\n\t" \
    "v_cvt_pk_bf16_f32 v81, v76, v77\n\t" \
    "ds_write_b32 v12, v81 offset:256\n\t" \
    "s_branch vv_Gback52_%=\n\t" \
    "vv_end44_%=:\n\t" \
    "s_endpgm\n\t" \
    ".p2align 6\n\t" \
    "s_nop 0\n\t" \
    "s_nop 0\n\t" \
    "s_nop 0\n\t" \
    "s_nop 0\n\t" \
    "s_nop 0\n\t" \
    "s_nop 0\n\t" \
    "vv_loop14_%=:\n\t" \
    "s_getpc_b64 s[30:31]\n\t" \
    "vv_Wp60_%=:\n\t" \
    "s_add_u32 s30, s30, vv_We61_%=-vv_Wp60_%=\n\t" \
    "s_addc_u32 s31, s31, 0\n\t" \
    "s_sub_u32 s70, s70, -54\n\t" \
    "vv_W59_%=:\n\t" \
    "s_memtime s[56:57]\n\t" \
    "s_waitcnt lgkmcnt(0)\n\t" \
    "s_sub_u32 s69, s70, s56\n\t" \
    "s_cmp_gt_i32 s69, 80\n\t" \
    "s_cbranch_scc1 vv_W59_%=\n\t" \
    "s_max_i32 s69, s69, 0\n\t" \
    "s_and_b32 s69, s69, -4\n\t" \
    "s_sub_u32 s30, s30, s69\n\t" \
    "s_subb_u32 s31, s31, 0\n\t" \
    "s_setpc_b64 s[30:31]\n\t" \
    "s_nop 0\n\t" \
    "s_nop 0\n\t" \
    "s_nop 0\n\t" \
    "s_nop 0\n\t" \
    "s_nop 0\n\t" \
    "s_nop 0\n\t" \
    "s_nop 0\n\t" \
    "s_nop 0\n\t" \
    "s_nop 0\n\t" \
    "s_nop 0\n\t" \
    "s_nop 0\n\t" \
    "s_nop 0\n\t" \
    "s_nop 0\n\t" \
    "s_nop 0\n\t" \
    "s_nop 0\n\t" \
    "s_nop 0\n\t" \
    "s_nop 0\n\t" \
    "s_nop 0\n\t" \
    "s_nop 0\n\t" \
    "s_nop 0\n\t" \
    "vv_We61_%=:\n\t" \
    "vv_G62_%=:\n\t" \
    "global_load_dwordx4 v[36:39], v1, s[46:47] sc1\n\t" \
    "global_load_dword v141, v2, s[46:47] sc1\n\t" \
    "global_load_dwordx4 v[40:43], v23, s[46:47] sc1\n\t" \
    "s_waitcnt vmcnt(2)\n\t" \
    "vv_Garr67_%=:\n\t" \
    "ds_write_b128 v3, v[36:39]\n\t" \
    "v_smfmac_f32_16x16x64_bf16 v[72:75], v[36:39], a[48:55], v10\n\t" \
    "s_waitcnt vmcnt(1)\n\t" \
    "ds_write_b32 v22, v141 offset:20224\n\t" \
    "s_waitcnt vmcnt(0)\n\t" \
    "ds_write_b128 v3, v[40:43] offset:1024\n\t" \
    "v_max3_u32 v140, v36, v40, v141\n\t" \
    "v_cmp_eq_u32 vcc, -1, v140\n\t" \
    "s_cbranch_vccnz vv_Gbad63_%=\n\t" \
    "vv_Gc64_%=:\n\t" \
    "s_waitcnt lgkmcnt(0)\n\t" \
    "s_barrier\n\t" \
    "ds_read_b128 v[48:51], v4\n\t" \
    "ds_read_b128 v[52:55], v5\n\t" \
    "v_smfmac_f32_16x16x64_bf16 v[76:79], v[36:39], a[120:127], v10\n\t" \
    "ds_read_b128 v[56:59], v6\n\t" \
    "ds_read_b128 v[60:63], v7\n\t" \
    "v_smfmac_f32_16x16x64_bf16 v[72:75], v[40:43], a[56:63], v10\n\t" \
    "ds_read_b128 v[64:67], v8\n\t" \
    "v_smfmac_f32_16x16x64_bf16 v[76:79], v[40:43], a[128:135], v10\n\t" \
    "ds_read_b128 v[68:71], v9\n\t" \
    "s_waitcnt lgkmcnt(5)\n\t" \
    "v_smfmac_f32_16x16x64_bf16 v[72:75], v[48:51], a[0:7], v10\n\t" \
    "ds_read_b128 v[44:47], v0 offset:19456\n\t" \
    "s_waitcnt lgkmcnt(5)\n\t" \
    "v_smfmac_f32_16x16x64_bf16 v[72:75], v[52:55], a[8:15], v10\n\t" \
    "s_waitcnt lgkmcnt(4)\n\t" \
    "v_smfmac_f32_16x16x64_bf16 v[72:75], v[56:59], a[16:23], v10\n\t" \
    "s_waitcnt lgkmcnt(3)\n\t" \
    "v_smfmac_f32_16x16x64_bf16 v[72:75], v[60:63], a[24:31], v10\n\t" \
    "s_waitcnt lgkmcnt(2)\n\t" \
    "v_smfmac_f32_16x16x64_bf16 v[72:75], v[64:67], a[32:39], v10\n\t" \
    "s_waitcnt lgkmcnt(1)\n\t" \
    "v_smfmac_f32_16x16x64_bf16 v[72:75], v[68:71], a[40:47], v10\n\t" \
    "s_waitcnt lgkmcnt(0)\n\t" \
    "v_smfmac_f32_16x16x64_bf16 v[72:75], v[44:47], a[64:71], v10\n\t" \
    "s_nop 2\n\t" \
    "v_smfmac_f32_16x16x64_bf16 v[76:79], v[48:51], a[72:79], v10\n\t" \
    "s_nop 2\n\t" \
    "v_smfmac_f32_16x16x64_bf16 v[76:79], v[52:55], a[80:87], v10\n\t" \
    "v_add_f32 v72, v72, v74\n\t" \
    "v_smfmac_f32_16x16x64_bf16 v[76:79], v[56:59], a[88:95], v10\n\t" \
    "v_add_f32 v73, v73, v75\n\t" \
    "v_smfmac_f32_16x16x64_bf16 v[76:79], v[60:63], a[96:103], v10\n\t" \
    "v_cvt_pk_bf16_f32 v80, v72, v73\n\t" \
    "v_smfmac_f32_16x16x64_bf16 v[76:79], v[64:67], a[104:111], v10\n\t" \
    "ds_write_b32 v12, v80 offset:0\n\t" \
    "v_smfmac_f32_16x16x64_bf16 v[76:79], v[68:71], a[112:119], v10\n\t" \
    "v_smfmac_f32_16x16x64_bf16 v[76:79], v[44:47], a[136:143], v10\n\t" \
    "s_nop 6\n\t" \
    "v_cmp_u_f32 vcc, v72, v73\n\t" \
    "v_pk_add_f32 v[76:77], v[76:77], v[78:79]\n\t" \
    "v_cvt_pk_bf16_f32 v81, v76, v77\n\t" \
    "ds_write_b32 v12, v81 offset:256\n\t" \
    "s_cbranch_vccnz vv_Gslow65_%=\n\t" \
    "vv_Gback66_%=:\n\t" \
    "s_waitcnt lgkmcnt(0)\n\t" \
    "s_barrier\n\t" \
    "ds_read_b64_tr_b16 v[84:85], v13 offset:0\n\t" \
    "v_smfmac_f32_16x16x64_bf16 v[100:103], v[36:39], a[0:7], v10\n\t" \
    "ds_read_b64_tr_b16 v[86:87], v13 offset:64\n\t" \
    "ds_read_b64_tr_b16 v[92:93], v13 offset:1152\n\t" \
    "v_smfmac_f32_16x16x64_bf16 v[100:103], v[36:39], a[0:7], v10\n\t" \
    "ds_read_b64_tr_b16 v[94:95], v13 offset:1216\n\t" \
    "ds_read_b64_tr_b16 v[88:89], v25 offset:0\n\t" \
    "v_smfmac_f32_16x16x64_bf16 v[100:103], v[36:39], a[0:7], v10\n\t" \
    "ds_read_b64_tr_b16 v[90:91], v25 offset:64\n\t" \
    "s_waitcnt lgkmcnt(4)\n\t" \
    "v_smfmac_f32_16x16x64_bf16 v[124:127], v[84:87], v[240:247], v19\n\t" \
    "ds_read_b64_tr_b16 v[96:97], v25 offset:1152\n\t" \
    "ds_read_b64_tr_b16 v[98:99], v25 offset:1216\n\t" \
    "s_waitcnt lgkmcnt(4)\n\t" \
    "v_smfmac_f32_16x16x64_bf16 v[124:127], v[92:95], v[248:255], v19\n\t" \
    "s_nop 2\n\t" \
    "v_smfmac_f32_16x16x64_bf16 v[108:111], v[84:87], a[144:151], v19\n\t" \
    "s_nop 2\n\t" \
    "v_smfmac_f32_16x16x64_bf16 v[108:111], v[92:95], a[160:167], v19\n\t" \
    "s_nop 1\n\t" \
    "v_permlane32_swap_b32 v124, v126\n\t" \
    "v_smfmac_f32_16x16x64_bf16 v[112:115], v[84:87], a[176:183], v19\n\t" \
    "s_nop 1\n\t" \
    "v_permlane32_swap_b32 v125, v127\n\t" \
    "v_smfmac_f32_16x16x64_bf16 v[112:115], v[92:95], a[192:199], v19\n\t" \
    "s_nop 0\n\t" \
    "v_add_f32 v124, v124, v126\n\t" \
    "s_waitcnt lgkmcnt(2)\n\t" \
    "v_smfmac_f32_16x16x64_bf16 v[108:111], v[88:91], a[152:159], v19\n\t" \
    "s_nop 1\n\t" \
    "v_add_f32 v125, v125, v127\n\t" \
    "v_smfmac_f32_16x16x64_bf16 v[112:115], v[88:91], a[184:191], v19\n\t" \
    "s_nop 0\n\t" \
    "v_cvt_pk_bf16_f32 v136, v124, v125\n\t" \
    "s_waitcnt lgkmcnt(0)\n\t" \
    "v_smfmac_f32_16x16x64_bf16 v[108:111], v[96:99], a[168:175], v19\n\t" \
    "s_nop 1\n\t" \
    "v_cvt_pk_bf16_f32 v137, v124, v125\n\t" \
    "v_smfmac_f32_16x16x64_bf16 v[112:115], v[96:99], a[200:207], v19\n\t" \
    "s_nop 1\n\t" \
    "v_permlane32_swap_b32 v136, v137\n\t" \
    "v_smfmac_f32_16x16x64_bf16 v[116:119], v[84:87], a[208:215], v19\n\t" \
    "s_mov_b32 exec_hi, 0\n\t" \
    "global_store_dwordx2 v153, v[136:137], s[38:39] sc1\n\t" \
    "s_mov_b32 exec_hi, -1\n\t" \
    "v_cvt_pk_bf16_f32 v128, v108, v109\n\t" \
    "v_smfmac_f32_16x16x64_bf16 v[116:119], v[88:91], a[216:223], v19\n\t" \
    "s_nop 1\n\t" \
    "v_cvt_pk_bf16_f32 v129, v110, v111\n\t" \
    "v_smfmac_f32_16x16x64_bf16 v[116:119], v[92:95], a[224:231], v19\n\t" \
    "s_nop 1\n\t" \
    "v_cvt_pk_bf16_f32 v130, v112, v113\n\t" \
    "v_smfmac_f32_16x16x64_bf16 v[116:119], v[96:99], a[232:239], v19\n\t" \
    "s_nop 1\n\t" \
    "v_cvt_pk_bf16_f32 v131, v114, v115\n\t" \
    "v_smfmac_f32_16x16x64_bf16 v[120:123], v[84:87], a[240:247], v19\n\t" \
    "s_nop 1\n\t" \
    "v_permlane16_swap_b32 v128, v130\n\t" \
    "v_smfmac_f32_16x16x64_bf16 v[120:123], v[88:91], a[248:255], v19\n\t" \
    "s_nop 1\n\t" \
    "v_permlane16_swap_b32 v129, v131\n\t" \
    "v_smfmac_f32_16x16x64_bf16 v[120:123], v[92:95], v[224:231], v19\n\t" \
    "s_nop 0\n\t" \
    "global_store_dwordx4 v151, v[128:131], s[38:39] sc1\n\t" \
    "v_cvt_pk_bf16_f32 v132, v116, v117\n\t" \
    "v_smfmac_f32_16x16x64_bf16 v[120:123], v[96:99], v[232:239], v19\n\t" \
    "s_nop 1\n\t" \
    "v_cvt_pk_bf16_f32 v133, v118, v119\n\t" \
    "s_nop 4\n\t" \
    "v_cvt_pk_bf16_f32 v134, v120, v121\n\t" \
    "v_cvt_pk_bf16_f32 v135, v122, v123\n\t" \
    "s_nop 0\n\t" \
    "v_permlane16_swap_b32 v132, v134\n\t" \
    "v_permlane16_swap_b32 v133, v135\n\t" \
    "global_store_dwordx4 v152, v[132:135], s[38:39] sc1\n\t" \
    "global_load_dword v140, v20, s[52:53] sc1\n\t" \
    "s_cmp_eq_u32 s65, 63\n\t" \
    "s_cbranch_scc1 vv_end58_%=\n\t" \
    "s_cmp_lg_u32 s65, 0\n\t" \
    "s_cbranch_scc1 vv_ng68_%=\n\t" \
    "vv_ng68_%=:\n\t" \
    "s_waitcnt lgkmcnt(0)\n\t" \
    "s_mov_b32 s70, 116\n\t" \
    "vv_E72_%=:\n\t" \
    "s_memtime s[56:57]\n\t" \
    "s_memrealtime s[2:3]\n\t" \
    "s_nop 0\n\t" \
    "s_memrealtime s[4:5]\n\t" \
    "s_nop 0\n\t" \
    "s_memrealtime s[6:7]\n\t" \
    "s_nop 0\n\t" \
    "s_memrealtime s[8:9]\n\t" \
    "s_nop 0\n\t" \
    "s_memrealtime s[10:11]\n\t" \
    "s_nop 0\n\t" \
    "s_memrealtime s[12:13]\n\t" \
    "s_nop 0\n\t" \
    "s_memrealtime s[14:15]\n\t" \
    "s_nop 0\n\t" \
    "s_memrealtime s[16:17]\n\t" \
    "s_nop 0\n\t" \
    "s_memrealtime s[18:19]\n\t" \
    "s_nop 0\n\t" \
    "s_memrealtime s[20:21]\n\t" \
    "s_nop 0\n\t" \
    "s_memrealtime s[22:23]\n\t" \
    "s_nop 0\n\t" \
    "s_memrealtime s[24:25]\n\t" \
    "s_nop 0\n\t" \
    "s_memrealtime s[26:27]\n\t" \
    "s_nop 0\n\t" \
    "s_memrealtime s[28:29]\n\t" \
    "s_memtime s[54:55]\n\t" \
    "s_add_u32 s70, s70, 8\n\t" \
    "s_waitcnt lgkmcnt(0)\n\t" \
    "s_sub_u32 s69, s54, s56\n\t" \
    "s_cmp_gt_u32 s69, s70\n\t" \
    "s_cbranch_scc1 vv_E72_%=\n\t" \
    "s_add_u32 s2, s2, s4\n\t" \
    "s_add_u32 s6, s6, s8\n\t" \
    "s_add_u32 s10, s10, s12\n\t" \
    "s_add_u32 s14, s14, s16\n\t" \
    "s_add_u32 s18, s18, s20\n\t" \
    "s_add_u32 s22, s22, s24\n\t" \
    "s_add_u32 s2, s2, s6\n\t" \
    "s_add_u32 s10, s10, s14\n\t" \
    "s_add_u32 s18, s18, s22\n\t" \
    "s_add_u32 s2, s2, s26\n\t" \
    "s_add_u32 s10, s10, s18\n\t" \
    "s_add_u32 s2, s2, s10\n\t" \
    "s_mul_i32 s70, s28, 13\n\t" \
    "s_sub_u32 s70, s70, s2\n\t" \
    "s_min_u32 s70, s70, 52\n\t" \
    "s_mul_i32 s69, s69, s70\n\t" \
    "s_mul_hi_u32 s69, s69, 76695844\n\t" \
    "s_mov_b32 s75, s73\n\t" \
    "s_mov_b32 s76, s74\n\t" \
    "s_add_u32 s73, s56, s69\n\t" \
    "s_lshl_b32 s74, s28, 4\n\t" \
    "s_sub_u32 s69, s73, s75\n\t" \
    "s_sub_u32 s70, s74, s76\n\t" \
    "s_sub_u32 s71, s70, 1600\n\t" \
    "s_cmp_lt_u32 s71, 400000\n\t" \
    "s_cbranch_scc0 vv_K73_%=\n\t" \
    "v_cvt_f32_u32 v160, s69\n\t" \
    "v_cvt_f32_u32 v161, s70\n\t" \
    "v_rcp_f32 v162, v160\n\t" \
    "v_rcp_f32 v163, v161\n\t" \
    "v_mul_f32 v162, v162, v161\n\t" \
    "v_mul_f32 v163, v163, v160\n\t" \
    "v_mul_f32 v162, 0x4f800000, v162\n\t" \
    "v_mul_f32 v163, 0x4f000000, v163\n\t" \
    "v_cvt_u32_f32 v162, v162\n\t" \
    "v_cvt_u32_f32 v163, v163\n\t" \
    "v_readfirstlane_b32 s69, v162\n\t" \
    "v_readfirstlane_b32 s70, v163\n\t" \
    "s_nop 1\n\t" \
    "s_min_u32 s71, s65, 1\n\t" \
    "s_sub_u32 s69, s69, s78\n\t" \
    "s_ashr_i32 s69, s69, s71\n\t" \
    "s_add_u32 s78, s78, s69\n\t" \
    "s_sub_u32 s70, s70, s77\n\t" \
    "s_ashr_i32 s70, s70, s71\n\t" \
    "s_add_u32 s77, s77, s70\n\t" \
    "vv_K73_%=:\n\t" \
    "s_add_u32 s65, s65, 1\n\t" \
    "v_mov_b32 v72, 0\n\t" \
    "v_mov_b32 v73, 0\n\t" \
    "v_mov_b32 v74, 0\n\t" \
    "v_mov_b32 v75, 0\n\t" \
    "v_mov_b32 v76, 0\n\t" \
    "v_mov_b32 v77, 0\n\t" \
    "v_mov_b32 v78, 0\n\t" \
    "v_mov_b32 v79, 0\n\t" \
    "v_mov_b32 v108, 0\n\t" \
    "v_mov_b32 v109, 0\n\t" \
    "v_mov_b32 v110, 0\n\t" \
    "v_mov_b32 v111, 0\n\t" \
    "v_mov_b32 v112, 0\n\t" \
    "v_mov_b32 v113, 0\n\t" \
    "v_mov_b32 v114, 0\n\t" \
    "v_mov_b32 v115, 0\n\t" \
    "v_mov_b32 v116, 0\n\t" \
    "v_mov_b32 v117, 0\n\t" \
    "v_mov_b32 v118, 0\n\t" \
    "v_mov_b32 v119, 0\n\t" \
    "v_mov_b32 v120, 0\n\t" \
    "v_mov_b32 v121, 0\n\t" \
    "v_mov_b32 v122, 0\n\t" \
    "v_mov_b32 v123, 0\n\t" \
    "v_mov_b32 v124, 0\n\t" \
    "v_mov_b32 v125, 0\n\t" \
    "v_mov_b32 v126, 0\n\t" \
    "v_mov_b32 v127, 0\n\t" \
    "s_add_u32 s69, s65, 0\n\t" \
    "s_and_b32 s69, s69, 7\n\t" \
    "s_lshl_b32 s69, s69, 18\n\t" \
    "s_add_u32 s69, s69, s62\n\t" \
    "s_add_u32 s46, s36, s69\n\t" \
    "s_addc_u32 s47, s37, 0\n\t" \
    "s_bitcmp0_b32 s65, 0\n\t" \
    "s_cselect_b32 s69, 16384, 0\n\t" \
    "v_xor_b32 v1, s69, v1\n\t" \
    "v_xor_b32 v2, s69, v2\n\t" \
    "v_xor_b32 v23, s69, v23\n\t" \
    "s_add_u32 s70, s65, 0\n\t" \
    "s_mul_i32 s69, s70, 171\n\t" \
    "s_lshr_b32 s69, s69, 9\n\t" \
    "s_mul_i32 s71, s69, 3\n\t" \
    "s_sub_u32 s70, s70, s71\n\t" \
    "s_mul_i32 s70, s70, 589824\n\t" \
    "s_lshl1_add_u32 s71, s69, 4\n\t" \
    "s_lshr_b64 s[38:39], s[44:45], s71\n\t" \
    "s_lshl_b32 s71, s66, 1\n\t" \
    "s_xor_b32 s71, s71, s38\n\t" \
    "s_and_b32 s71, s71, 3\n\t" \
    "s_lshl_b32 s71, s71, 13\n\t" \
    "s_add_u32 s70, s70, s71\n\t" \
    "s_lshl_b32 s69, s69, 21\n\t" \
    "s_add_u32 s69, s69, 123731968\n\t" \
    "v_add_u32 v151, s70, v14\n\t" \
    "v_and_b32 v150, 0x80000, v151\n\t" \
    "v_lshrrev_b32 v150, 5, v150\n\t" \
    "v_xor_b32 v151, v151, v150\n\t" \
    "v_add_u32 v152, s70, v15\n\t" \
    "v_and_b32 v150, 0x80000, v152\n\t" \
    "v_lshrrev_b32 v150, 5, v150\n\t" \
    "v_xor_b32 v152, v152, v150\n\t" \
    "v_add_u32 v153, s70, v16\n\t" \
    "v_and_b32 v150, 0x80000, v153\n\t" \
    "v_lshrrev_b32 v150, 5, v150\n\t" \
    "v_xor_b32 v153, v153, v150\n\t" \
    "s_add_u32 s38, s36, s69\n\t" \
    "s_addc_u32 s39, s37, 0\n\t" \
    "s_and_b32 s69, s69, 0xffe00000\n\t" \
    "s_add_u32 s69, s69, 1807360\n\t" \
    "v_mov_b32 v17, s69\n\t" \
    "global_load_dword v17, v17, s[36:37]\n\t" \
    "s_waitcnt vmcnt(0)\n\t" \
    "v_cmp_eq_u32 vcc, -1, v140\n\t" \
    "s_cbranch_vccz vv_Hok70_%=\n\t" \
    "vv_H69_%=:\n\t" \
    "global_load_dword v140, v20, s[52:53] sc1\n\t" \
    "s_waitcnt vmcnt(0)\n\t" \
    "v_cmp_eq_u32 vcc, -1, v140\n\t" \
    "s_cbranch_vccz vv_Hc71_%=\n\t" \
    "s_cmp_eq_u32 s68, 0\n\t" \
    "s_cbranch_scc1 vv_F74_%=\n\t" \
    "s_sub_u32 s68, s68, 1\n\t" \
    "s_sleep 2\n\t" \
    "s_branch vv_H69_%=\n\t" \
    "vv_F74_%=:\n\t" \
    "v_mov_b32 v141, 5767424\n\t" \
    "v_mov_b32 v142, 6\n\t" \
    "global_store_dword v141, v142, s[36:37] sc1\n\t" \
    "s_waitcnt vmcnt(0)\n\t" \
    "s_branch vv_Hc71_%=\n\t" \
    "vv_Hc71_%=:\n\t" \
    "s_memtime s[56:57]\n\t" \
    "s_waitcnt lgkmcnt(0)\n\t" \
    "s_sub_u32 s69, s56, s73\n\t" \
    "s_cmp_lt_u32 s69, 3000\n\t" \
    "s_cbranch_scc1 vv_St75_%=\n\t" \
    "s_waitcnt lgkmcnt(0)\n\t" \
    "s_mov_b32 s70, 116\n\t" \
    "vv_E76_%=:\n\t" \
    "s_memtime s[56:57]\n\t" \
    "s_memrealtime s[2:3]\n\t" \
    "s_nop 0\n\t" \
    "s_memrealtime s[4:5]\n\t" \
    "s_nop 0\n\t" \
    "s_memrealtime s[6:7]\n\t" \
    "s_nop 0\n\t" \
    "s_memrealtime s[8:9]\n\t" \
    "s_nop 0\n\t" \
    "s_memrealtime s[10:11]\n\t" \
    "s_nop 0\n\t" \
    "s_memrealtime s[12:13]\n\t" \
    "s_nop 0\n\t" \
    "s_memrealtime s[14:15]\n\t" \
    "s_nop 0\n\t" \
    "s_memrealtime s[16:17]\n\t" \
    "s_nop 0\n\t" \
    "s_memrealtime s[18:19]\n\t" \
    "s_nop 0\n\t" \
    "s_memrealtime s[20:21]\n\t" \
    "s_nop 0\n\t" \
    "s_memrealtime s[22:23]\n\t" \
    "s_nop 0\n\t" \
    "s_memrealtime s[24:25]\n\t" \
    "s_nop 0\n\t" \
    "s_memrealtime s[26:27]\n\t" \
    "s_nop 0\n\t" \
    "s_memrealtime s[28:29]\n\t" \
    "s_memtime s[54:55]\n\t" \
    "s_add_u32 s70, s70, 8\n\t" \
    "s_waitcnt lgkmcnt(0)\n\t" \
    "s_sub_u32 s69, s54, s56\n\t" \
    "s_cmp_gt_u32 s69, s70\n\t" \
    "s_cbranch_scc1 vv_E76_%=\n\t" \
    "s_add_u32 s2, s2, s4\n\t" \
    "s_add_u32 s6, s6, s8\n\t" \
    "s_add_u32 s10, s10, s12\n\t" \
    "s_add_u32 s14, s14, s16\n\t" \
    "s_add_u32 s18, s18, s20\n\t" \
    "s_add_u32 s22, s22, s24\n\t" \
    "s_add_u32 s2, s2, s6\n\t" \
    "s_add_u32 s10, s10, s14\n\t" \
    "s_add_u32 s18, s18, s22\n\t" \
    "s_add_u32 s2, s2, s26\n\t" \
    "s_add_u32 s10, s10, s18\n\t" \
    "s_add_u32 s2, s2, s10\n\t" \
    "s_mul_i32 s70, s28, 13\n\t" \
    "s_sub_u32 s70, s70, s2\n\t" \
    "s_min_u32 s70, s70, 52\n\t" \
    "s_mul_i32 s69, s69, s70\n\t" \
    "s_mul_hi_u32 s69, s69, 76695844\n\t" \
    "s_mov_b32 s75, s73\n\t" \
    "s_mov_b32 s76, s74\n\t" \
    "s_add_u32 s73, s56, s69\n\t" \
    "s_lshl_b32 s74, s28, 4\n\t" \
    "vv_St75_%=:\n\t" \
    "vv_Hok70_%=:\n\t" \
    "v_max_u32_dpp v140, v140, v140 row_ror:1 row_mask:0xf bank_mask:0xf\n\t" \
    "s_nop 1\n\t" \
    "v_max_u32_dpp v140, v140, v140 row_ror:2 row_mask:0xf bank_mask:0xf\n\t" \
    "s_nop 1\n\t" \
    "v_max_u32_dpp v140, v140, v140 row_ror:4 row_mask:0xf bank_mask:0xf\n\t" \
    "s_nop 1\n\t" \
    "v_max_u32_dpp v140, v140, v140 row_ror:8 row_mask:0xf bank_mask:0xf\n\t" \
    "s_nop 0\n\t" \
    "v_readlane_b32 s69, v140, 0\n\t" \
    "v_readlane_b32 s71, v140, 16\n\t" \
    "s_nop 1\n\t" \
    "s_max_u32 s81, s69, s71\n\t" \
    "s_mul_hi_u32 s69, s85, s78\n\t" \
    "s_add_u32 s79, s81, s69\n\t" \
    "s_add_u32 s79, s79, s87\n\t" \
    "s_cmp_lt_u32 s65, 6\n\t" \
    "s_cselect_b32 s69, 48, 0\n\t" \
    "s_add_u32 s79, s79, s69\n\t" \
    "s_add_u32 s80, s79, s88\n\t" \
    "s_sub_u32 s70, s79, s74\n\t" \
    "s_max_i32 s70, s70, 0\n\t" \
    "s_min_i32 s70, s70, 16000\n\t" \
    "s_lshl_b32 s70, s70, 1\n\t" \
    "s_mul_hi_u32 s70, s70, s77\n\t" \
    "s_add_u32 s70, s70, s73\n\t" \
    "v_mov_b32 v160, s70\n\t" \
    "ds_write_b32 v11, v160 offset:20480\n\t" \
    "s_and_b32 s69, s65, 7\n\t" \
    "s_lshl_b32 s69, s69, 18\n\t" \
    "s_add_u32 s69, s69, s63\n\t" \
    "s_add_u32 s52, s36, s69\n\t" \
    "s_addc_u32 s53, s37, 0\n\t" \
    "s_waitcnt lgkmcnt(0)\n\t" \
    "s_barrier\n\t" \
    "s_branch vv_loop14_%=\n\t" \
    "vv_Gbad63_%=:\n\t" \
    "s_waitcnt vmcnt(0) lgkmcnt(0)\n\t" \
    "s_nop 7\n\t" \
    "v_mov_b32 v72, 0\n\t" \
    "v_mov_b32 v73, 0\n\t" \
    "v_mov_b32 v74, 0\n\t" \
    "v_mov_b32 v75, 0\n\t" \
    "v_mov_b32 v76, 0\n\t" \
    "v_mov_b32 v77, 0\n\t" \
    "v_mov_b32 v78, 0\n\t" \
    "v_mov_b32 v79, 0\n\t" \
    "s_cmp_eq_u32 s68, 0\n\t" \
    "s_cbranch_scc1 vv_F77_%=\n\t" \
    "s_sub_u32 s68, s68, 1\n\t" \
    "s_branch vv_G62_%=\n\t" \
    "vv_F77_%=:\n\t" \
    "v_mov_b32 v140, 5767424\n\t" \
    "v_mov_b32 v141, 1\n\t" \
    "global_store_dword v140, v141, s[36:37] sc1\n\t" \
    "s_waitcnt vmcnt(0)\n\t" \
    "s_branch vv_Gc64_%=\n\t" \
    "vv_Gslow65_%=:\n\t" \
    "v_max_u32 v140, v36, v37\n\t" \
    "v_max3_u32 v140, v140, v38, v39\n\t" \
    "v_max3_u32 v140, v140, v40, v41\n\t" \
    "v_max3_u32 v140, v140, v42, v43\n\t" \
    "v_max3_u32 v140, v140, v44, v45\n\t" \
    "v_max3_u32 v140, v140, v46, v47\n\t" \
    "v_max3_u32 v140, v140, v48, v49\n\t" \
    "v_max3_u32 v140, v140, v50, v51\n\t" \
    "v_max3_u32 v140, v140, v52, v53\n\t" \
    "v_max3_u32 v140, v140, v54, v55\n\t" \
    "v_max3_u32 v140, v140, v56, v57\n\t" \
    "v_max3_u32 v140, v140, v58, v59\n\t" \
    "v_max3_u32 v140, v140, v60, v61\n\t" \
    "v_max3_u32 v140, v140, v62, v63\n\t" \
    "v_max3_u32 v140, v140, v64, v65\n\t" \
    "v_max3_u32 v140, v140, v66, v67\n\t" \
    "v_max3_u32 v140, v140, v68, v69\n\t" \
    "v_max3_u32 v140, v140, v70, v71\n\t" \
    "v_cmp_eq_u32 vcc, -1, v140\n\t" \
    "s_cbranch_vccz vv_Gback66_%=\n\t" \
    "s_waitcnt lgkmcnt(0)\n\t" \
    "v_sub_u32 v140, v1, v3\n\t" \
    "s_nop 0\n\t" \
    "v_readfirstlane_b32 s69, v140\n\t" \
    "v_add_u32 v143, 36864, v0\n\t" \
    "s_nop 7\n\t" \
    "s_add_u32 s30, s46, s69\n\t" \
    "s_addc_u32 s31, s47, 0\n\t" \
    "vv_Gag78_%=:\n\t" \
    "global_load_dwordx4 v[36:39], v1, s[46:47] sc1\n\t" \
    "global_load_dwordx4 v[40:43], v23, s[46:47] sc1\n\t" \
    "global_load_dwordx4 v[44:47], v143, s[30:31] sc1\n\t" \
    "global_load_dwordx4 v[48:51], v4, s[30:31] sc1\n\t" \
    "global_load_dwordx4 v[52:55], v5, s[30:31] sc1\n\t" \
    "global_load_dwordx4 v[56:59], v6, s[30:31] sc1\n\t" \
    "global_load_dwordx4 v[60:63], v7, s[30:31] sc1\n\t" \
    "global_load_dwordx4 v[64:67], v8, s[30:31] sc1\n\t" \
    "global_load_dwordx4 v[68:71], v9, s[30:31] sc1\n\t" \
    "s_waitcnt vmcnt(0)\n\t" \
    "v_max_u32 v140, v36, v37\n\t" \
    "v_max3_u32 v140, v140, v38, v39\n\t" \
    "v_max3_u32 v140, v140, v40, v41\n\t" \
    "v_max3_u32 v140, v140, v42, v43\n\t" \
    "v_max3_u32 v140, v140, v44, v45\n\t" \
    "v_max3_u32 v140, v140, v46, v47\n\t" \
    "v_max3_u32 v140, v140, v48, v49\n\t" \
    "v_max3_u32 v140, v140, v50, v51\n\t" \
    "v_max3_u32 v140, v140, v52, v53\n\t" \
    "v_max3_u32 v140, v140, v54, v55\n\t" \
    "v_max3_u32 v140, v140, v56, v57\n\t" \
    "v_max3_u32 v140, v140, v58, v59\n\t" \
    "v_max3_u32 v140, v140, v60, v61\n\t" \
    "v_max3_u32 v140, v140, v62, v63\n\t" \
    "v_max3_u32 v140, v140, v64, v65\n\t" \
    "v_max3_u32 v140, v140, v66, v67\n\t" \
    "v_max3_u32 v140, v140, v68, v69\n\t" \
    "v_max3_u32 v140, v140, v70, v71\n\t" \
    "v_cmp_eq_u32 vcc, -1, v140\n\t" \
    "s_cbranch_vccz vv_Gredo79_%=\n\t" \
    "s_cmp_eq_u32 s68, 0\n\t" \
    "s_cbranch_scc1 vv_F80_%=\n\t" \
    "s_sub_u32 s68, s68, 1\n\t" \
    "s_sleep 1\n\t" \
    "s_branch vv_Gag78_%=\n\t" \
    "vv_F80_%=:\n\t" \
    "v_mov_b32 v140, 5767424\n\t" \
    "v_mov_b32 v141, 2\n\t" \
    "global_store_dword v140, v141, s[36:37] sc1\n\t" \
    "s_waitcnt vmcnt(0)\n\t" \
    "s_branch vv_Gredo79_%=\n\t" \
    "vv_Gredo79_%=:\n\t" \
    "v_mov_b32 v72, 0\n\t" \
    "v_mov_b32 v73, 0\n\t" \
    "v_mov_b32 v74, 0\n\t" \
    "v_mov_b32 v75, 0\n\t" \
    "v_mov_b32 v76, 0\n\t" \
    "v_mov_b32 v77, 0\n\t" \
    "v_mov_b32 v78, 0\n\t" \
    "v_mov_b32 v79, 0\n\t" \
    "v_smfmac_f32_16x16x64_bf16 v[72:75], v[36:39], a[48:55], v10\n\t" \
    "v_smfmac_f32_16x16x64_bf16 v[72:75], v[40:43], a[56:63], v10\n\t" \
    "v_smfmac_f32_16x16x64_bf16 v[72:75], v[48:51], a[0:7], v10\n\t" \
    "v_smfmac_f32_16x16x64_bf16 v[72:75], v[52:55], a[8:15], v10\n\t" \
    "v_smfmac_f32_16x16x64_bf16 v[72:75], v[56:59], a[16:23], v10\n\t" \
    "v_smfmac_f32_16x16x64_bf16 v[72:75], v[60:63], a[24:31], v10\n\t" \
    "v_smfmac_f32_16x16x64_bf16 v[72:75], v[64:67], a[32:39], v10\n\t" \
    "v_smfmac_f32_16x16x64_bf16 v[72:75], v[68:71], a[40:47], v10\n\t" \
    "v_smfmac_f32_16x16x64_bf16 v[72:75], v[44:47], a[64:71], v10\n\t" \
    "v_smfmac_f32_16x16x64_bf16 v[76:79], v[36:39], a[120:127], v10\n\t" \
    "v_smfmac_f32_16x16x64_bf16 v[76:79], v[40:43], a[128:135], v10\n\t" \
    "v_smfmac_f32_16x16x64_bf16 v[76:79], v[48:51], a[72:79], v10\n\t" \
    "v_smfmac_f32_16x16x64_bf16 v[76:79], v[52:55], a[80:87], v10\n\t" \
    "v_smfmac_f32_16x16x64_bf16 v[76:79], v[56:59], a[88:95], v10\n\t" \
    "v_smfmac_f32_16x16x64_bf16 v[76:79], v[60:63], a[96:103], v10\n\t" \
    "v_smfmac_f32_16x16x64_bf16 v[76:79], v[64:67], a[104:111], v10\n\t" \
    "v_smfmac_f32_16x16x64_bf16 v[76:79], v[68:71], a[112:119], v10\n\t" \
    "v_smfmac_f32_16x16x64_bf16 v[76:79], v[44:47], a[136:143], v10\n\t" \
    "v_add_f32 v72, v72, v74\n\t" \
    "v_add_f32 v73, v73, v75\n\t" \
    "v_cvt_pk_bf16_f32 v80, v72, v73\n\t" \
    "ds_write_b32 v12, v80 offset:0\n\t" \
    "s_nop 3\n\t" \
    "v_pk_add_f32 v[76:77], v[76:77], v[78:79]\n\t" \
    "v_cvt_pk_bf16_f32 v81, v76, v77\n\t" \
    "ds_write_b32 v12, v81 offset:256\n\t" \
    "s_branch vv_Gback66_%=\n\t" \
    "vv_end58_%=:\n\t" \
    "s_endpgm\n\t" \
    "vv_last_%=:\n\t" \
    ""
#define ASM_R \
    "s_mov_b64 s[36:37], %0\n\t" \
    "s_mov_b64 s[42:43], %1\n\t" \
    "s_mov_b64 s[44:45], %2\n\t" \
    "s_mov_b32 s66, %3\n\t" \
    "s_mov_b32 s67, %4\n\t" \
    "s_mov_b64 s[50:51], %5\n\t" \
    "s_mov_b32 s82, %7\n\t" \
    "s_load_dwordx8 s[84:91], %6, 0\n\t" \
    "v_mbcnt_lo_u32_b32 v223, -1, 0\n\t" \
    "v_mbcnt_hi_u32_b32 v223, -1, v223\n\t" \
    "s_getpc_b64 s[46:47]\n\t" \
    "rr_pc_%=:\n\t" \
    "s_lshl_b32 s69, s66, 13\n\t" \
    "v_lshl_add_u32 v222, v223, 7, s69\n\t" \
    "v_min_u32 v222, rr_last_%=-rr_pc_%=-4, v222\n\t" \
    "global_load_dword v222, v222, s[46:47]\n\t" \
    "v_lshlrev_b32 v223, 2, v223\n\t" \
    "v_add_u32 v223, s82, v223\n\t" \
    "ds_read_b32 v0, v223 offset:0\n\t" \
    "ds_read_b32 v1, v223 offset:256\n\t" \
    "ds_read_b32 v2, v223 offset:512\n\t" \
    "ds_read_b32 v3, v223 offset:768\n\t" \
    "ds_read_b32 v4, v223 offset:1024\n\t" \
    "ds_read_b32 v5, v223 offset:1280\n\t" \
    "ds_read_b32 v6, v223 offset:1536\n\t" \
    "ds_read_b32 v7, v223 offset:1792\n\t" \
    "ds_read_b32 v8, v223 offset:2048\n\t" \
    "ds_read_b32 v9, v223 offset:2304\n\t" \
    "ds_read_b32 v10, v223 offset:2560\n\t" \
    "ds_read_b32 v11, v223 offset:2816\n\t" \
    "s_waitcnt lgkmcnt(0)\n\t" \
    "ds_read_b32 v12, v223 offset:3072\n\t" \
    "ds_read_b32 v13, v223 offset:3328\n\t" \
    "ds_read_b32 v14, v223 offset:3584\n\t" \
    "ds_read_b32 v15, v223 offset:3840\n\t" \
    "ds_read_b32 v16, v223 offset:4096\n\t" \
    "ds_read_b32 v17, v223 offset:4352\n\t" \
    "ds_read_b32 v18, v223 offset:4608\n\t" \
    "ds_read_b32 v19, v223 offset:4864\n\t" \
    "ds_read_b32 v20, v223 offset:5120\n\t" \
    "ds_read_b32 v21, v223 offset:5376\n\t" \
    "ds_read_b32 v22, v223 offset:5632\n\t" \
    "ds_read_b32 v23, v223 offset:5888\n\t" \
    "s_waitcnt lgkmcnt(0)\n\t" \
    "ds_read_b32 v24, v223 offset:6144\n\t" \
    "ds_read_b32 v25, v223 offset:6400\n\t" \
    "ds_read_b32 v26, v223 offset:6656\n\t" \
    "ds_read_b32 v27, v223 offset:6912\n\t" \
    "ds_read_b32 v28, v223 offset:7168\n\t" \
    "ds_read_b32 v29, v223 offset:7424\n\t" \
    "ds_read_b32 v30, v223 offset:7680\n\t" \
    "ds_read_b32 v31, v223 offset:7936\n\t" \
    "ds_read_b32 v32, v223 offset:8192\n\t" \
    "ds_read_b32 v33, v223 offset:8448\n\t" \
    "ds_read_b32 v34, v223 offset:8704\n\t" \
    "ds_read_b32 v35, v223 offset:8960\n\t" \
    "s_waitcnt lgkmcnt(0)\n\t" \
    "ds_read_b32 v36, v223 offset:9216\n\t" \
    "ds_read_b32 v37, v223 offset:9472\n\t" \
    "ds_read_b32 v38, v223 offset:9728\n\t" \
    "ds_read_b32 v39, v223 offset:9984\n\t" \
    "ds_read_b32 v40, v223 offset:10240\n\t" \
    "ds_read_b32 v41, v223 offset:10496\n\t" \
    "ds_read_b32 v42, v223 offset:10752\n\t" \
    "ds_read_b32 v43, v223 offset:11008\n\t" \
    "ds_read_b32 v44, v223 offset:11264\n\t" \
    "ds_read_b32 v45, v223 offset:11520\n\t" \
    "ds_read_b32 v183, v223 offset:11776\n\t" \
    "ds_read_b32 v184, v223 offset:12032\n\t" \
    "s_waitcnt lgkmcnt(0)\n\t" \
    "ds_read_b32 v185, v223 offset:12288\n\t" \
    "ds_read_b32 v186, v223 offset:12544\n\t" \
    "ds_read_b32 v187, v223 offset:12800\n\t" \
    "ds_read_b32 v188, v223 offset:13056\n\t" \
    "s_waitcnt lgkmcnt(0)\n\t" \
    "s_mov_b32 s68, 262144\n\t" \
    "s_mov_b32 s72, 0\n\t" \
    "s_mov_b32 s83, -1\n\t" \
    "s_mov_b32 s65, 0\n\t" \
    "v_xor_b32 v180, 16384, v2\n\t" \
    "v_xor_b32 v181, 131072, v2\n\t" \
    "v_xor_b32 v182, 147456, v2\n\t" \
    "v_add_u32 v189, 4096, v7\n\t" \
    "v_add_u32 v190, 32768, v7\n\t" \
    "v_add_u32 v191, 36864, v7\n\t" \
    "s_mul_i32 s69, s67, 57\n\t" \
    "s_lshr_b32 s69, s69, 9\n\t" \
    "s_mul_i32 s69, s69, 9\n\t" \
    "s_sub_u32 s69, s67, s69\n\t" \
    "s_cmp_lt_u32 s69, 4\n\t" \
    "s_cselect_b32 s70, 21, 16\n\t" \
    "s_cmp_eq_u32 s69, 8\n\t" \
    "s_cselect_b32 s70, 26, s70\n\t" \
    "s_lshr_b32 s97, s91, s70\n\t" \
    "s_and_b32 s97, s97, 31\n\t" \
    "s_min_u32 s97, s97, 0\n\t" \
    "s_lshl_b32 s97, s97, 4\n\t" \
    "s_mov_b32 s77, 3019898880\n\t" \
    "s_mov_b32 s78, 3054198966\n\t" \
    "s_waitcnt lgkmcnt(0)\n\t" \
    "s_mov_b32 s70, 116\n\t" \
    "rr_E1_%=:\n\t" \
    "s_memtime s[56:57]\n\t" \
    "s_memrealtime s[2:3]\n\t" \
    "s_nop 0\n\t" \
    "s_memrealtime s[4:5]\n\t" \
    "s_nop 0\n\t" \
    "s_memrealtime s[6:7]\n\t" \
    "s_nop 0\n\t" \
    "s_memrealtime s[8:9]\n\t" \
    "s_nop 0\n\t" \
    "s_memrealtime s[10:11]\n\t" \
    "s_nop 0\n\t" \
    "s_memrealtime s[12:13]\n\t" \
    "s_nop 0\n\t" \
    "s_memrealtime s[14:15]\n\t" \
    "s_nop 0\n\t" \
    "s_memrealtime s[16:17]\n\t" \
    "s_nop 0\n\t" \
    "s_memrealtime s[18:19]\n\t" \
    "s_nop 0\n\t" \
    "s_memrealtime s[20:21]\n\t" \
    "s_nop 0\n\t" \
    "s_memrealtime s[22:23]\n\t" \
    "s_nop 0\n\t" \
    "s_memrealtime s[24:25]\n\t" \
    "s_nop 0\n\t" \
    "s_memrealtime s[26:27]\n\t" \
    "s_nop 0\n\t" \
    "s_memrealtime s[28:29]\n\t" \
    "s_memtime s[54:55]\n\t" \
    "s_add_u32 s70, s70, 8\n\t" \
    "s_waitcnt lgkmcnt(0)\n\t" \
    "s_sub_u32 s69, s54, s56\n\t" \
    "s_cmp_gt_u32 s69, s70\n\t" \
    "s_cbranch_scc1 rr_E1_%=\n\t" \
    "s_add_u32 s2, s2, s4\n\t" \
    "s_add_u32 s6, s6, s8\n\t" \
    "s_add_u32 s10, s10, s12\n\t" \
    "s_add_u32 s14, s14, s16\n\t" \
    "s_add_u32 s18, s18, s20\n\t" \
    "s_add_u32 s22, s22, s24\n\t" \
    "s_add_u32 s2, s2, s6\n\t" \
    "s_add_u32 s10, s10, s14\n\t" \
    "s_add_u32 s18, s18, s22\n\t" \
    "s_add_u32 s2, s2, s26\n\t" \
    "s_add_u32 s10, s10, s18\n\t" \
    "s_add_u32 s2, s2, s10\n\t" \
    "s_mul_i32 s70, s28, 13\n\t" \
    "s_sub_u32 s70, s70, s2\n\t" \
    "s_min_u32 s70, s70, 52\n\t" \
    "s_mul_i32 s69, s69, s70\n\t" \
    "s_mul_hi_u32 s69, s69, 76695844\n\t" \
    "s_mov_b32 s75, s73\n\t" \
    "s_mov_b32 s76, s74\n\t" \
    "s_add_u32 s73, s56, s69\n\t" \
    "s_lshl_b32 s74, s28, 4\n\t" \
    "v_mov_b32 v172, 1807360\n\t" \
    "v_mov_b32 v173, 3904512\n\t" \
    "global_load_dword v172, v172, s[36:37]\n\t" \
    "global_load_dword v173, v173, s[36:37]\n\t" \
    "s_mov_b32 s62, 0\n\t" \
    "s_mov_b32 s63, 0x200000\n\t" \
    "s_mov_b64 s[44:45], 0\n\t" \
    "v_mov_b32 v170, 0\n\t" \
    "s_mov_b32 s65, s66\n\t" \
    "s_mul_i32 s59, s67, 57\n\t" \
    "s_lshr_b32 s59, s59, 9\n\t" \
    "s_add_u32 s69, s59, 1\n\t" \
    "s_lshr_b32 s69, s69, 1\n\t" \
    "s_mul_i32 s69, s69, 9\n\t" \
    "s_sub_u32 s58, s67, s69\n\t" \
    "s_lshl_b32 s58, s58, 15\n\t" \
    "s_sub_u32 s80, s74, 32000\n\t" \
    "s_mov_b32 s64, 1\n\t" \
    "s_mov_b32 s65, 5\n\t" \
    "s_mov_b32 s83, 99\n\t" \
    "v_mov_b32 v164, 1809408\n\t" \
    "s_mov_b32 s69, 3906560\n\t" \
    "v_writelane_b32 v164, s69, 1\n\t" \
    "s_mov_b32 s69, 6238208\n\t" \
    "v_writelane_b32 v164, s69, 2\n\t" \
    "s_mov_b32 s69, 125541376\n\t" \
    "v_writelane_b32 v164, s69, 3\n\t" \
    "s_mov_b32 m0, 155648\n\t" \
    "s_nop 0\n\t" \
    "global_load_lds_dword v164, s[36:37]\n\t" \
    "s_lshl_b32 s70, s66, 8\n\t" \
    "s_mul_i32 s69, s67, 2097152\n\t" \
    "s_add_u32 s69, s69, 48235136\n\t" \
    "s_add_u32 s69, s69, s70\n\t" \
    "v_writelane_b32 v164, s69, 0\n\t" \
    "v_mov_b32 v165, 0\n\t" \
    "s_mov_b64 exec, 1\n\t" \
    "global_atomic_or v164, v165, s[36:37] sc1\n\t" \
    "s_mov_b64 exec, -1\n\t" \
    "s_add_u32 s70, s65, 59\n\t" \
    "s_mul_i32 s69, s70, 171\n\t" \
    "s_lshr_b32 s69, s69, 9\n\t" \
    "s_mul_i32 s71, s69, 3\n\t" \
    "s_sub_u32 s70, s70, s71\n\t" \
    "s_mul_i32 s70, s70, 589824\n\t" \
    "s_lshl1_add_u32 s71, s69, 4\n\t" \
    "s_lshr_b64 s[40:41], s[44:45], s71\n\t" \
    "s_lshl_b32 s71, s59, 1\n\t" \
    "s_xor_b32 s71, s71, s40\n\t" \
    "s_and_b32 s71, s71, 3\n\t" \
    "s_lshl_b32 s71, s71, 13\n\t" \
    "s_add_u32 s70, s70, s71\n\t" \
    "s_lshl_b32 s69, s69, 21\n\t" \
    "s_add_u32 s69, s69, 123731968\n\t" \
    "s_add_u32 s70, s70, s58\n\t" \
    "s_lshr_b32 s71, s70, 5\n\t" \
    "s_and_b32 s71, s71, 0x4000\n\t" \
    "s_xor_b32 s70, s70, s71\n\t" \
    "s_add_u32 s69, s69, s70\n\t" \
    "s_add_u32 s40, s36, s69\n\t" \
    "s_addc_u32 s41, s37, 0\n\t" \
    "v_mov_b32 v192, 0\n\t" \
    "s_mov_b64 exec, 1\n\t" \
    "s_branch rr_act3_%=\n\t" \
    "rr_g214_%=:\n\t" \
    "s_mov_b64 exec, -1\n\t" \
    "s_waitcnt vmcnt(0)\n\t" \
    "v_mbcnt_lo_u32_b32 v164, -1, 0\n\t" \
    "v_mbcnt_hi_u32_b32 v164, -1, v164\n\t" \
    "v_lshlrev_b32 v164, 6, v164\n\t" \
    "v_add_u32 v164, 6225920, v164\n\t" \
    "v_mov_b32 v165, 6234112\n\t" \
    "global_load_dword v165, v165, s[36:37] sc1\n\t" \
    "global_load_dwordx2 v[166:167], v164, s[36:37] sc1\n\t" \
    "global_load_dword v168, v164, s[36:37] offset:2048 sc1\n\t" \
    "s_waitcnt vmcnt(0)\n\t" \
    "rr_C15_%=:\n\t" \
    "v_cmp_ne_u32 s[54:55], v166, v168\n\t" \
    "v_sub_u32 v166, v166, v165\n\t" \
    "v_cmp_ne_u32 vcc, 1, v166\n\t" \
    "s_nop 1\n\t" \
    "s_or_b64 vcc, vcc, s[54:55]\n\t" \
    "s_cbranch_scc0 rr_Cok16_%=\n\t" \
    "s_cmp_eq_u32 s68, 0\n\t" \
    "s_cbranch_scc1 rr_F18_%=\n\t" \
    "s_sub_u32 s68, s68, 1\n\t" \
    "s_sleep 6\n\t" \
    "s_branch rr_Cag17_%=\n\t" \
    "rr_F18_%=:\n\t" \
    "v_mov_b32 v168, 5767424\n\t" \
    "v_mov_b32 v169, 4\n\t" \
    "global_store_dword v168, v169, s[36:37] sc1\n\t" \
    "s_waitcnt vmcnt(0)\n\t" \
    "s_branch rr_Cok16_%=\n\t" \
    "rr_Cag17_%=:\n\t" \
    "global_load_dwordx2 v[166:167], v164, s[36:37] sc1\n\t" \
    "global_load_dword v168, v164, s[36:37] offset:2048 sc1\n\t" \
    "s_waitcnt vmcnt(0)\n\t" \
    "s_branch rr_C15_%=\n\t" \
    "rr_Cok16_%=:\n\t" \
    "v_readlane_b32 s69, v167, 2\n\t" \
    "v_readlane_b32 s70, v167, 3\n\t" \
    "s_nop 1\n\t" \
    "s_cmp_lt_u32 s70, s69\n\t" \
    "s_cselect_b32 s69, 0, 8192\n\t" \
    "s_add_u32 s100, s36, s69\n\t" \
    "s_addc_u32 s101, s37, 0\n\t" \
    "s_cmp_lg_u32 s66, 0\n\t" \
    "s_cbranch_scc1 rr_Ca19_%=\n\t" \
    "v_mov_b32 v164, 5768192\n\t" \
    "s_mov_b64 exec, 1\n\t" \
    "global_atomic_add v164, v166, s[36:37] sc1\n\t" \
    "s_mov_b64 exec, -1\n\t" \
    "rr_Ca19_%=:\n\t" \
    "v_mov_b32_dpp v164, v167 quad_perm:[1,0,3,2] row_mask:0xf bank_mask:0xf\n\t" \
    "v_cmp_lt_u32 s[44:45], v164, v167\n\t" \
    "v_min_u32 v164, v164, v167\n\t" \
    "v_and_b32 v165, 1, v164\n\t" \
    "v_cmp_ne_u32 vcc, 0, v165\n\t" \
    "s_nop 1\n\t" \
    "s_and_b32 s44, s44, 0x55555555\n\t" \
    "s_and_b32 s69, vcc_lo, 0xaaaaaaaa\n\t" \
    "s_or_b32 s44, s44, s69\n\t" \
    "s_and_b32 s45, s45, 0x55555555\n\t" \
    "s_and_b32 s69, vcc_hi, 0xaaaaaaaa\n\t" \
    "s_or_b32 s45, s45, s69\n\t" \
    "s_bitcmp1_b32 s44, 0\n\t" \
    "s_cselect_b32 s62, 8192, 0\n\t" \
    "s_bitcmp1_b32 s44, 2\n\t" \
    "s_cselect_b32 s63, 8192, 0\n\t" \
    "s_or_b32 s63, s63, 0x200000\n\t" \
    "v_cmp_lt_u32 vcc, 1320, v164\n\t" \
    "s_nop 1\n\t" \
    "s_bcnt1_i32_b64 s69, vcc\n\t" \
    "s_cmp_gt_u32 s69, 16\n\t" \
    "s_cselect_b32 s69, 1, 0\n\t" \
    "s_mul_i32 s70, s69, 160\n\t" \
    "s_add_u32 s86, s86, s70\n\t" \
    "s_mul_i32 s70, s69, 536\n\t" \
    "s_add_u32 s87, s87, s70\n\t" \
    "s_mul_i32 s70, s69, 256\n\t" \
    "s_add_u32 s88, s88, s70\n\t" \
    "v_mov_b32 v172, -1\n\t" \
    "v_mov_b32 v173, -1\n\t" \
    "v_mov_b32 v174, -1\n\t" \
    "v_mov_b32 v175, -1\n\t" \
    "v_mov_b32 v176, -1\n\t" \
    "s_sub_u32 s69, s63, s62\n\t" \
    "s_sub_u32 s69, s69, 0x200000\n\t" \
    "v_bfe_u32 v164, v186, 21, 1\n\t" \
    "v_mad_i32_i24 v186, v164, s69, v186\n\t" \
    "v_bfe_u32 v164, v187, 21, 1\n\t" \
    "v_mad_i32_i24 v187, v164, s69, v187\n\t" \
    "v_bfe_u32 v164, v188, 21, 1\n\t" \
    "v_mad_i32_i24 v188, v164, s69, v188\n\t" \
    "s_bitcmp1_b32 s44, 1\n\t" \
    "s_cselect_b32 s69, 16384, 0\n\t" \
    "v_xor_b32 v10, s69, v10\n\t" \
    "v_xor_b32 v11, s69, v11\n\t" \
    "v_xor_b32 v12, s69, v12\n\t" \
    "v_xor_b32 v13, s69, v13\n\t" \
    "v_xor_b32 v14, s69, v14\n\t" \
    "v_xor_b32 v15, s69, v15\n\t" \
    "v_xor_b32 v16, s69, v16\n\t" \
    "v_xor_b32 v17, s69, v17\n\t" \
    "v_xor_b32 v18, s69, v18\n\t" \
    "v_xor_b32 v19, s69, v19\n\t" \
    "v_xor_b32 v20, s69, v20\n\t" \
    "v_xor_b32 v21, s69, v21\n\t" \
    "v_xor_b32 v22, s69, v22\n\t" \
    "v_xor_b32 v23, s69, v23\n\t" \
    "v_xor_b32 v24, s69, v24\n\t" \
    "v_xor_b32 v25, s69, v25\n\t" \
    "v_xor_b32 v42, s69, v42\n\t" \
    "v_xor_b32 v43, s69, v43\n\t" \
    "v_xor_b32 v44, s69, v44\n\t" \
    "v_xor_b32 v45, s69, v45\n\t" \
    "s_bitcmp1_b32 s44, 3\n\t" \
    "s_cselect_b32 s69, 16384, 0\n\t" \
    "v_xor_b32 v7, s69, v7\n\t" \
    "v_xor_b32 v189, s69, v189\n\t" \
    "v_xor_b32 v190, s69, v190\n\t" \
    "v_xor_b32 v191, s69, v191\n\t" \
    "s_mov_b32 s64, 0\n\t" \
    "s_mov_b32 s65, s66\n\t" \
    "s_mov_b32 s83, 1\n\t" \
    "s_mov_b32 s77, 3154116608\n\t" \
    "rr_pf9_%=:\n\t" \
    "s_cmp_gt_u32 s65, 8\n\t" \
    "s_cselect_b32 s83, 99, s83\n\t" \
    "v_cmp_gt_i32 vcc, s65, v176\n\t" \
    "s_nop 1\n\t" \
    "s_max_i32 s69, s83, s65\n\t" \
    "s_cmp_eq_u64 vcc, 0\n\t" \
    "s_cselect_b32 s83, s69, s83\n\t" \
    "s_lshl_b32 s69, s65, 11\n\t" \
    "s_add_u32 s69, s69, 5799936\n\t" \
    "v_and_b32 v169, 0x7c0, v7\n\t" \
    "v_add_u32 v169, s69, v169\n\t" \
    "s_cmp_lg_u32 s65, 2\n\t" \
    "s_cbranch_scc1 rr_n2s20_%=\n\t" \
    "s_sleep 10\n\t" \
    "rr_n2s20_%=:\n\t" \
    "rr_WF21_%=:\n\t" \
    "s_cmp_ge_i32 s83, s65\n\t" \
    "s_cbranch_scc1 rr_WFok22_%=\n\t" \
    "global_load_dword v164, v169, s[36:37] sc1\n\t" \
    "s_waitcnt vmcnt(0)\n\t" \
    "v_min_i32_dpp v164, v164, v164 row_ror:1 row_mask:0xf bank_mask:0xf\n\t" \
    "s_nop 1\n\t" \
    "v_min_i32_dpp v164, v164, v164 row_ror:2 row_mask:0xf bank_mask:0xf\n\t" \
    "s_nop 1\n\t" \
    "v_min_i32_dpp v164, v164, v164 row_ror:4 row_mask:0xf bank_mask:0xf\n\t" \
    "s_nop 1\n\t" \
    "v_min_i32_dpp v164, v164, v164 row_ror:8 row_mask:0xf bank_mask:0xf\n\t" \
    "s_nop 0\n\t" \
    "v_readlane_b32 s69, v164, 0\n\t" \
    "v_readlane_b32 s70, v164, 16\n\t" \
    "s_nop 1\n\t" \
    "s_min_i32 s83, s69, s70\n\t" \
    "s_cmp_ge_i32 s83, s65\n\t" \
    "s_cbranch_scc1 rr_WFok22_%=\n\t" \
    "s_cmp_eq_u32 s68, 0\n\t" \
    "s_cbranch_scc1 rr_F23_%=\n\t" \
    "s_sub_u32 s68, s68, 1\n\t" \
    "s_sleep 2\n\t" \
    "s_branch rr_WF21_%=\n\t" \
    "rr_F23_%=:\n\t" \
    "v_mov_b32 v164, 5767424\n\t" \
    "v_mov_b32 v165, 10\n\t" \
    "global_store_dword v164, v165, s[36:37] sc1\n\t" \
    "s_waitcnt vmcnt(0)\n\t" \
    "s_branch rr_WFok22_%=\n\t" \
    "rr_WFok22_%=:\n\t" \
    "s_mul_i32 s70, s67, 7\n\t" \
    "s_add_u32 s69, s65, 0\n\t" \
    "s_xor_b32 s69, s69, s70\n\t" \
    "s_and_b32 s69, s69, 63\n\t" \
    "s_lshl_b32 s69, s69, 15\n\t" \
    "s_mul_i32 s70, s67, 2097152\n\t" \
    "s_add_u32 s69, s69, s70\n\t" \
    "s_add_u32 s69, s69, 48234496\n\t" \
    "s_add_u32 s38, s36, s69\n\t" \
    "s_addc_u32 s39, s37, 0\n\t" \
    "s_add_u32 s70, s65, 0\n\t" \
    "s_mul_i32 s69, s70, 171\n\t" \
    "s_lshr_b32 s69, s69, 9\n\t" \
    "s_mul_i32 s71, s69, 3\n\t" \
    "s_sub_u32 s70, s70, s71\n\t" \
    "s_mul_i32 s70, s70, 589824\n\t" \
    "s_lshl1_add_u32 s71, s69, 4\n\t" \
    "s_lshr_b64 s[40:41], s[44:45], s71\n\t" \
    "s_lshl_b32 s71, s59, 1\n\t" \
    "s_xor_b32 s71, s71, s40\n\t" \
    "s_and_b32 s71, s71, 3\n\t" \
    "s_lshl_b32 s71, s71, 13\n\t" \
    "s_add_u32 s70, s70, s71\n\t" \
    "s_lshl_b32 s69, s69, 21\n\t" \
    "s_add_u32 s69, s69, 123731968\n\t" \
    "s_add_u32 s70, s70, s58\n\t" \
    "s_lshr_b32 s71, s70, 5\n\t" \
    "s_and_b32 s71, s71, 0x4000\n\t" \
    "s_xor_b32 s70, s70, s71\n\t" \
    "s_add_u32 s69, s69, s70\n\t" \
    "s_add_u32 s40, s36, s69\n\t" \
    "s_addc_u32 s41, s37, 0\n\t" \
    "s_and_b32 s69, s69, 0xffe00000\n\t" \
    "s_add_u32 s69, s69, 1807360\n\t" \
    "v_mov_b32 v168, s69\n\t" \
    "global_load_dword v168, v168, s[36:37]\n\t" \
    "s_cmp_gt_u32 s65, 61\n\t" \
    "s_cbranch_scc1 rr_idle2_%=\n\t" \
    "s_add_u32 s69, s65, 2\n\t" \
    "s_and_b32 s69, s69, 7\n\t" \
    "s_lshl_b32 s69, s69, 18\n\t" \
    "s_add_u32 s69, s69, s62\n\t" \
    "s_add_u32 s46, s36, s69\n\t" \
    "s_addc_u32 s47, s37, 0\n\t" \
    "global_store_dwordx4 v183, v[172:175], s[46:47] sc1\n\t" \
    "global_store_dwordx4 v184, v[172:175], s[46:47] sc1\n\t" \
    "global_store_dwordx4 v185, v[172:175], s[46:47] sc1\n\t" \
    "global_store_dwordx4 v186, v[172:175], s[46:47] sc1\n\t" \
    "global_store_dwordx4 v187, v[172:175], s[46:47] sc1\n\t" \
    "rr_idle2_%=:\n\t" \
    "v_mov_b32 v200, 0\n\t" \
    "s_cmp_lg_u32 s67, 35\n\t" \
    "s_cbranch_scc1 rr_cke24_%=\n\t" \
    "s_cmp_lt_u32 s65, 7\n\t" \
    "s_cbranch_scc1 rr_cke24_%=\n\t" \
    "s_lshl_b32 s69, s65, 7\n\t" \
    "s_add_u32 s69, s69, 5775360\n\t" \
    "v_mov_b32 v201, s69\n\t" \
    "global_load_dword v200, v201, s[36:37] sc1\n\t" \
    "rr_cke24_%=:\n\t" \
    "s_cmp_gt_u32 s65, 62\n\t" \
    "s_cbranch_scc1 rr_n225_%=\n\t" \
    "s_cmp_lt_u32 s65, 2\n\t" \
    "s_cbranch_scc1 rr_nw226_%=\n\t" \
    "s_mul_hi_u32 s69, s85, s78\n\t" \
    "s_add_u32 s79, s81, s69\n\t" \
    "s_add_u32 s79, s79, s87\n\t" \
    "s_cmp_lt_u32 s65, 7\n\t" \
    "s_cselect_b32 s69, 48, 0\n\t" \
    "s_add_u32 s79, s79, s69\n\t" \
    "s_add_u32 s80, s79, s88\n\t" \
    "s_cmp_gt_u32 s65, 4\n\t" \
    "s_cselect_b32 s71, 240, 64\n\t" \
    "s_add_u32 s71, s71, s79\n\t" \
    "s_sub_u32 s70, s71, s74\n\t" \
    "s_max_i32 s70, s70, 0\n\t" \
    "s_min_i32 s70, s70, 16000\n\t" \
    "s_lshl_b32 s70, s70, 1\n\t" \
    "s_mul_hi_u32 s70, s70, s77\n\t" \
    "s_add_u32 s70, s70, s73\n\t" \
    "rr_W27_%=:\n\t" \
    "s_memtime s[56:57]\n\t" \
    "s_waitcnt lgkmcnt(0)\n\t" \
    "s_sub_u32 s69, s70, s56\n\t" \
    "s_cmp_gt_i32 s69, 24\n\t" \
    "s_cbranch_scc1 rr_W27_%=\n\t" \
    "s_branch rr_g113_%=\n\t" \
    "rr_nw226_%=:\n\t" \
    "s_lshl_b32 s71, s59, 12\n\t" \
    "s_add_u32 s71, s71, 5783552\n\t" \
    "s_mov_b32 s96, 96\n\t" \
    "rr_g012_%=:\n\t" \
    "s_memrealtime s[54:55]\n\t" \
    "s_waitcnt lgkmcnt(0)\n\t" \
    "s_bfe_u32 s69, s54, 0x60004\n\t" \
    "s_lshl_b32 s69, s69, 6\n\t" \
    "s_add_u32 s69, s69, s71\n\t" \
    "v_mov_b32 v165, s69\n\t" \
    "global_load_dword v165, v165, s[36:37] sc1\n\t" \
    "s_sub_u32 s96, s96, 1\n\t" \
    "s_cbranch_scc1 rr_gs28_%=\n\t" \
    "s_waitcnt vmcnt(0)\n\t" \
    "v_readfirstlane_b32 s70, v165\n\t" \
    "s_sleep 1\n\t" \
    "s_cmp_eq_u32 s70, -1\n\t" \
    "s_cbranch_scc1 rr_g012_%=\n\t" \
    "s_mul_i32 s69, s66, 352\n\t" \
    "s_add_u32 s70, s70, s69\n\t" \
    "s_sub_u32 s70, s70, 2688\n\t" \
    "rr_gw29_%=:\n\t" \
    "s_memrealtime s[54:55]\n\t" \
    "s_waitcnt lgkmcnt(0)\n\t" \
    "s_lshl_b32 s69, s54, 4\n\t" \
    "s_sub_u32 s69, s70, s69\n\t" \
    "s_cmp_lt_u32 s69, 8000\n\t" \
    "s_cbranch_scc1 rr_gw29_%=\n\t" \
    "rr_gs28_%=:\n\t" \
    "s_waitcnt vmcnt(0)\n\t" \
    "s_mov_b32 s83, -1\n\t" \
    "rr_WF30_%=:\n\t" \
    "s_cmp_ge_i32 s83, s65\n\t" \
    "s_cbranch_scc1 rr_WFok31_%=\n\t" \
    "global_load_dword v164, v169, s[36:37] sc1\n\t" \
    "s_waitcnt vmcnt(0)\n\t" \
    "v_min_i32_dpp v164, v164, v164 row_ror:1 row_mask:0xf bank_mask:0xf\n\t" \
    "s_nop 1\n\t" \
    "v_min_i32_dpp v164, v164, v164 row_ror:2 row_mask:0xf bank_mask:0xf\n\t" \
    "s_nop 1\n\t" \
    "v_min_i32_dpp v164, v164, v164 row_ror:4 row_mask:0xf bank_mask:0xf\n\t" \
    "s_nop 1\n\t" \
    "v_min_i32_dpp v164, v164, v164 row_ror:8 row_mask:0xf bank_mask:0xf\n\t" \
    "s_nop 0\n\t" \
    "v_readlane_b32 s69, v164, 0\n\t" \
    "v_readlane_b32 s70, v164, 16\n\t" \
    "s_nop 1\n\t" \
    "s_min_i32 s83, s69, s70\n\t" \
    "s_cmp_ge_i32 s83, s65\n\t" \
    "s_cbranch_scc1 rr_WFok31_%=\n\t" \
    "s_cmp_eq_u32 s68, 0\n\t" \
    "s_cbranch_scc1 rr_F32_%=\n\t" \
    "s_sub_u32 s68, s68, 1\n\t" \
    "s_sleep 2\n\t" \
    "s_branch rr_WF30_%=\n\t" \
    "rr_F32_%=:\n\t" \
    "v_mov_b32 v164, 5767424\n\t" \
    "v_mov_b32 v165, 16\n\t" \
    "global_store_dword v164, v165, s[36:37] sc1\n\t" \
    "s_waitcnt vmcnt(0)\n\t" \
    "s_branch rr_WFok31_%=\n\t" \
    "rr_WFok31_%=:\n\t" \
    "rr_g113_%=:\n\t" \
    "global_load_dwordx4 a[0:3], v26, s[38:39] offset:0 sc1\n\t" \
    "global_load_dwordx4 a[4:7], v26, s[38:39] offset:1024 sc1\n\t" \
    "global_load_dwordx4 a[8:11], v27, s[38:39] offset:0 sc1\n\t" \
    "global_load_dwordx4 a[12:15], v27, s[38:39] offset:1024 sc1\n\t" \
    "global_load_dwordx4 a[16:19], v28, s[38:39] offset:0 sc1\n\t" \
    "global_load_dwordx4 a[20:23], v28, s[38:39] offset:1024 sc1\n\t" \
    "global_load_dwordx4 a[24:27], v29, s[38:39] offset:0 sc1\n\t" \
    "global_load_dwordx4 a[28:31], v29, s[38:39] offset:1024 sc1\n\t" \
    "global_load_dwordx4 a[32:35], v30, s[38:39] offset:0 sc1\n\t" \
    "global_load_dwordx4 a[36:39], v30, s[38:39] offset:1024 sc1\n\t" \
    "global_load_dwordx4 a[40:43], v31, s[38:39] offset:0 sc1\n\t" \
    "global_load_dwordx4 a[44:47], v31, s[38:39] offset:1024 sc1\n\t" \
    "global_load_dwordx4 a[48:51], v32, s[38:39] offset:0 sc1\n\t" \
    "global_load_dwordx4 a[52:55], v32, s[38:39] offset:1024 sc1\n\t" \
    "global_load_dwordx4 a[56:59], v33, s[38:39] offset:0 sc1\n\t" \
    "global_load_dwordx4 a[60:63], v33, s[38:39] offset:1024 sc1\n\t" \
    "global_load_dwordx4 a[64:67], v34, s[38:39] offset:0 sc1\n\t" \
    "global_load_dwordx4 a[68:71], v34, s[38:39] offset:1024 sc1\n\t" \
    "global_load_dwordx4 a[72:75], v35, s[38:39] offset:0 sc1\n\t" \
    "global_load_dwordx4 a[76:79], v35, s[38:39] offset:1024 sc1\n\t" \
    "global_load_dwordx4 a[80:83], v36, s[38:39] offset:0 sc1\n\t" \
    "global_load_dwordx4 a[84:87], v36, s[38:39] offset:1024 sc1\n\t" \
    "global_load_dwordx4 a[88:91], v37, s[38:39] offset:0 sc1\n\t" \
    "global_load_dwordx4 a[92:95], v37, s[38:39] offset:1024 sc1\n\t" \
    "global_load_dwordx4 a[96:99], v38, s[38:39] offset:0 sc1\n\t" \
    "global_load_dwordx4 a[100:103], v38, s[38:39] offset:1024 sc1\n\t" \
    "global_load_dwordx4 a[104:107], v39, s[38:39] offset:0 sc1\n\t" \
    "global_load_dwordx4 a[108:111], v39, s[38:39] offset:1024 sc1\n\t" \
    "global_load_dwordx4 a[112:115], v40, s[38:39] offset:0 sc1\n\t" \
    "global_load_dwordx4 a[116:119], v40, s[38:39] offset:1024 sc1\n\t" \
    "global_load_dwordx4 a[120:123], v41, s[38:39] offset:0 sc1\n\t" \
    "global_load_dwordx4 a[124:127], v41, s[38:39] offset:1024 sc1\n\t" \
    "rr_n225_%=:\n\t" \
    "s_cmp_eq_u32 s65, 0\n\t" \
    "s_cbranch_scc1 rr_act3_%=\n\t" \
    "s_waitcnt vmcnt(0)\n\t" \
    "rr_ckl34_%=:\n\t" \
    "v_cmp_eq_u32 vcc, -1, v200\n\t" \
    "s_cbranch_vccz rr_ck33_%=\n\t" \
    "global_load_dword v200, v201, s[36:37] sc1\n\t" \
    "s_waitcnt vmcnt(0)\n\t" \
    "s_cmp_eq_u32 s68, 0\n\t" \
    "s_cbranch_scc1 rr_F35_%=\n\t" \
    "s_sub_u32 s68, s68, 1\n\t" \
    "s_sleep 1\n\t" \
    "s_branch rr_ckl34_%=\n\t" \
    "rr_F35_%=:\n\t" \
    "v_mov_b32 v166, 5767424\n\t" \
    "v_mov_b32 v167, 15\n\t" \
    "global_store_dword v166, v167, s[36:37] sc1\n\t" \
    "s_waitcnt vmcnt(0)\n\t" \
    "s_branch rr_ck33_%=\n\t" \
    "rr_ck33_%=:\n\t" \
    "s_barrier\n\t" \
    "s_cmp_gt_u32 s65, 62\n\t" \
    "s_cbranch_scc1 rr_end4_%=\n\t" \
    "ds_read_b32 v164, v9\n\t" \
    "s_waitcnt lgkmcnt(0)\n\t" \
    "v_readfirstlane_b32 s81, v164\n\t" \
    "s_nop 1\n\t" \
    "s_cmp_lg_u32 s65, 1\n\t" \
    "s_cbranch_scc1 rr_n136_%=\n\t" \
    "s_waitcnt lgkmcnt(0)\n\t" \
    "s_mov_b32 s70, 116\n\t" \
    "rr_E37_%=:\n\t" \
    "s_memtime s[56:57]\n\t" \
    "s_memrealtime s[2:3]\n\t" \
    "s_nop 0\n\t" \
    "s_memrealtime s[4:5]\n\t" \
    "s_nop 0\n\t" \
    "s_memrealtime s[6:7]\n\t" \
    "s_nop 0\n\t" \
    "s_memrealtime s[8:9]\n\t" \
    "s_nop 0\n\t" \
    "s_memrealtime s[10:11]\n\t" \
    "s_nop 0\n\t" \
    "s_memrealtime s[12:13]\n\t" \
    "s_nop 0\n\t" \
    "s_memrealtime s[14:15]\n\t" \
    "s_nop 0\n\t" \
    "s_memrealtime s[16:17]\n\t" \
    "s_nop 0\n\t" \
    "s_memrealtime s[18:19]\n\t" \
    "s_nop 0\n\t" \
    "s_memrealtime s[20:21]\n\t" \
    "s_nop 0\n\t" \
    "s_memrealtime s[22:23]\n\t" \
    "s_nop 0\n\t" \
    "s_memrealtime s[24:25]\n\t" \
    "s_nop 0\n\t" \
    "s_memrealtime s[26:27]\n\t" \
    "s_nop 0\n\t" \
    "s_memrealtime s[28:29]\n\t" \
    "s_memtime s[54:55]\n\t" \
    "s_add_u32 s70, s70, 8\n\t" \
    "s_waitcnt lgkmcnt(0)\n\t" \
    "s_sub_u32 s69, s54, s56\n\t" \
    "s_cmp_gt_u32 s69, s70\n\t" \
    "s_cbranch_scc1 rr_E37_%=\n\t" \
    "s_add_u32 s2, s2, s4\n\t" \
    "s_add_u32 s6, s6, s8\n\t" \
    "s_add_u32 s10, s10, s12\n\t" \
    "s_add_u32 s14, s14, s16\n\t" \
    "s_add_u32 s18, s18, s20\n\t" \
    "s_add_u32 s22, s22, s24\n\t" \
    "s_add_u32 s2, s2, s6\n\t" \
    "s_add_u32 s10, s10, s14\n\t" \
    "s_add_u32 s18, s18, s22\n\t" \
    "s_add_u32 s2, s2, s26\n\t" \
    "s_add_u32 s10, s10, s18\n\t" \
    "s_add_u32 s2, s2, s10\n\t" \
    "s_mul_i32 s70, s28, 13\n\t" \
    "s_sub_u32 s70, s70, s2\n\t" \
    "s_min_u32 s70, s70, 52\n\t" \
    "s_mul_i32 s69, s69, s70\n\t" \
    "s_mul_hi_u32 s69, s69, 76695844\n\t" \
    "s_mov_b32 s75, s73\n\t" \
    "s_mov_b32 s76, s74\n\t" \
    "s_add_u32 s73, s56, s69\n\t" \
    "s_lshl_b32 s74, s28, 4\n\t" \
    "rr_n136_%=:\n\t" \
    "s_mul_hi_u32 s69, s85, s78\n\t" \
    "s_add_u32 s79, s81, s69\n\t" \
    "s_add_u32 s79, s79, s87\n\t" \
    "s_cmp_lt_u32 s65, 6\n\t" \
    "s_cselect_b32 s69, 48, 0\n\t" \
    "s_add_u32 s79, s79, s69\n\t" \
    "s_add_u32 s80, s79, s88\n\t" \
    "s_sub_u32 s70, s79, s74\n\t" \
    "s_max_i32 s70, s70, 0\n\t" \
    "s_min_i32 s70, s70, 16000\n\t" \
    "s_lshl_b32 s70, s70, 1\n\t" \
    "s_mul_hi_u32 s70, s70, s77\n\t" \
    "s_add_u32 s70, s70, s73\n\t" \
    "rr_W38_%=:\n\t" \
    "s_memtime s[56:57]\n\t" \
    "s_waitcnt lgkmcnt(0)\n\t" \
    "s_sub_u32 s69, s70, s56\n\t" \
    "s_cmp_gt_i32 s69, 24\n\t" \
    "s_cbranch_scc1 rr_W38_%=\n\t" \
    "rr_act3_%=:\n\t" \
    "s_add_u32 s69, s65, 0\n\t" \
    "s_and_b32 s69, s69, 7\n\t" \
    "s_lshl_b32 s69, s69, 18\n\t" \
    "s_add_u32 s69, s69, s62\n\t" \
    "s_add_u32 s46, s36, s69\n\t" \
    "s_addc_u32 s47, s37, 0\n\t" \
    "s_add_u32 s69, s65, 1\n\t" \
    "s_and_b32 s69, s69, 7\n\t" \
    "s_lshl_b32 s69, s69, 18\n\t" \
    "s_add_u32 s69, s69, s62\n\t" \
    "s_add_u32 s48, s36, s69\n\t" \
    "s_addc_u32 s49, s37, 0\n\t" \
    "s_and_b32 s69, s65, 7\n\t" \
    "s_lshl_b32 s69, s69, 18\n\t" \
    "s_add_u32 s69, s69, s63\n\t" \
    "s_add_u32 s52, s36, s69\n\t" \
    "s_addc_u32 s53, s37, 0\n\t" \
    "s_sub_u32 s69, s65, 2\n\t" \
    "s_cmp_ge_u32 s69, 5\n\t" \
    "s_cbranch_scc1 rr_nf39_%=\n\t" \
    "s_cmp_lg_u32 s64, 0\n\t" \
    "s_cbranch_scc1 rr_nf39_%=\n\t" \
    "v_add_u32 v169, 4096, v169\n\t" \
    "global_load_dword v176, v169, s[36:37] sc1\n\t" \
    "rr_nf39_%=:\n\t" \
    "s_waitcnt lgkmcnt(0)\n\t" \
    "s_mov_b32 s70, 116\n\t" \
    "rr_E40_%=:\n\t" \
    "s_memtime s[56:57]\n\t" \
    "s_memrealtime s[2:3]\n\t" \
    "s_nop 0\n\t" \
    "s_memrealtime s[4:5]\n\t" \
    "s_nop 0\n\t" \
    "s_memrealtime s[6:7]\n\t" \
    "s_nop 0\n\t" \
    "s_memrealtime s[8:9]\n\t" \
    "s_nop 0\n\t" \
    "s_memrealtime s[10:11]\n\t" \
    "s_nop 0\n\t" \
    "s_memrealtime s[12:13]\n\t" \
    "s_nop 0\n\t" \
    "s_memrealtime s[14:15]\n\t" \
    "s_nop 0\n\t" \
    "s_memrealtime s[16:17]\n\t" \
    "s_nop 0\n\t" \
    "s_memrealtime s[18:19]\n\t" \
    "s_nop 0\n\t" \
    "s_memrealtime s[20:21]\n\t" \
    "s_nop 0\n\t" \
    "s_memrealtime s[22:23]\n\t" \
    "s_nop 0\n\t" \
    "s_memrealtime s[24:25]\n\t" \
    "s_nop 0\n\t" \
    "s_memrealtime s[26:27]\n\t" \
    "s_nop 0\n\t" \
    "s_memrealtime s[28:29]\n\t" \
    "s_memtime s[54:55]\n\t" \
    "s_add_u32 s70, s70, 8\n\t" \
    "s_waitcnt lgkmcnt(0)\n\t" \
    "s_sub_u32 s69, s54, s56\n\t" \
    "s_cmp_gt_u32 s69, s70\n\t" \
    "s_cbranch_scc1 rr_E40_%=\n\t" \
    "s_add_u32 s2, s2, s4\n\t" \
    "s_add_u32 s6, s6, s8\n\t" \
    "s_add_u32 s10, s10, s12\n\t" \
    "s_add_u32 s14, s14, s16\n\t" \
    "s_add_u32 s18, s18, s20\n\t" \
    "s_add_u32 s22, s22, s24\n\t" \
    "s_add_u32 s2, s2, s6\n\t" \
    "s_add_u32 s10, s10, s14\n\t" \
    "s_add_u32 s18, s18, s22\n\t" \
    "s_add_u32 s2, s2, s26\n\t" \
    "s_add_u32 s10, s10, s18\n\t" \
    "s_add_u32 s2, s2, s10\n\t" \
    "s_mul_i32 s70, s28, 13\n\t" \
    "s_sub_u32 s70, s70, s2\n\t" \
    "s_min_u32 s70, s70, 52\n\t" \
    "s_mul_i32 s69, s69, s70\n\t" \
    "s_mul_hi_u32 s69, s69, 76695844\n\t" \
    "s_mov_b32 s75, s73\n\t" \
    "s_mov_b32 s76, s74\n\t" \
    "s_add_u32 s73, s56, s69\n\t" \
    "s_lshl_b32 s74, s28, 4\n\t" \
    "s_sub_u32 s69, s73, s75\n\t" \
    "s_sub_u32 s70, s74, s76\n\t" \
    "s_sub_u32 s71, s70, 1600\n\t" \
    "s_cmp_lt_u32 s71, 400000\n\t" \
    "s_cbranch_scc0 rr_K41_%=\n\t" \
    "v_cvt_f32_u32 v164, s69\n\t" \
    "v_cvt_f32_u32 v165, s70\n\t" \
    "v_rcp_f32 v166, v164\n\t" \
    "v_rcp_f32 v167, v165\n\t" \
    "v_mul_f32 v166, v166, v165\n\t" \
    "v_mul_f32 v167, v167, v164\n\t" \
    "v_mul_f32 v166, 0x4f800000, v166\n\t" \
    "v_mul_f32 v167, 0x4f000000, v167\n\t" \
    "v_cvt_u32_f32 v166, v166\n\t" \
    "v_cvt_u32_f32 v167, v167\n\t" \
    "v_readfirstlane_b32 s69, v166\n\t" \
    "v_readfirstlane_b32 s70, v167\n\t" \
    "s_nop 1\n\t" \
    "s_lshr_b32 s71, s65, 1\n\t" \
    "s_min_u32 s71, s71, 1\n\t" \
    "s_sub_u32 s69, s69, s78\n\t" \
    "s_ashr_i32 s69, s69, s71\n\t" \
    "s_add_u32 s78, s78, s69\n\t" \
    "s_sub_u32 s70, s70, s77\n\t" \
    "s_ashr_i32 s70, s70, s71\n\t" \
    "s_add_u32 s77, s77, s70\n\t" \
    "rr_K41_%=:\n\t" \
    "s_mov_b32 s91, s64\n\t" \
    "s_cmp_lg_u32 s64, 0\n\t" \
    "s_cselect_b32 s77, 0, s77\n\t" \
    "v_mov_b32 v160, 0\n\t" \
    "v_mov_b32 v161, 0\n\t" \
    "v_mov_b32 v162, 0\n\t" \
    "v_mov_b32 v163, 0\n\t" \
    ".p2align 6\n\t" \
    "s_nop 0\n\t" \
    "s_nop 0\n\t" \
    "s_sub_u32 s70, s80, s74\n\t" \
    "s_max_i32 s70, s70, 0\n\t" \
    "s_min_i32 s70, s70, 16000\n\t" \
    "s_lshl_b32 s70, s70, 1\n\t" \
    "s_mul_hi_u32 s70, s70, s77\n\t" \
    "s_add_u32 s70, s70, s73\n\t" \
    "s_getpc_b64 s[30:31]\n\t" \
    "rr_Wp44_%=:\n\t" \
    "s_add_u32 s30, s30, rr_We45_%=-rr_Wp44_%=\n\t" \
    "s_addc_u32 s31, s31, 0\n\t" \
    "s_sub_u32 s70, s70, 96\n\t" \
    "rr_W43_%=:\n\t" \
    "s_memtime s[56:57]\n\t" \
    "s_waitcnt lgkmcnt(0)\n\t" \
    "s_sub_u32 s69, s70, s56\n\t" \
    "s_cmp_gt_i32 s69, 80\n\t" \
    "s_cbranch_scc1 rr_W43_%=\n\t" \
    "s_max_i32 s69, s69, 0\n\t" \
    "s_and_b32 s69, s69, -4\n\t" \
    "s_sub_u32 s30, s30, s69\n\t" \
    "s_subb_u32 s31, s31, 0\n\t" \
    "s_setpc_b64 s[30:31]\n\t" \
    "s_nop 0\n\t" \
    "s_nop 0\n\t" \
    "s_nop 0\n\t" \
    "s_nop 0\n\t" \
    "s_nop 0\n\t" \
    "s_nop 0\n\t" \
    "s_nop 0\n\t" \
    "s_nop 0\n\t" \
    "s_nop 0\n\t" \
    "s_nop 0\n\t" \
    "s_nop 0\n\t" \
    "s_nop 0\n\t" \
    "s_nop 0\n\t" \
    "s_nop 0\n\t" \
    "s_nop 0\n\t" \
    "s_nop 0\n\t" \
    "s_nop 0\n\t" \
    "s_nop 0\n\t" \
    "s_nop 0\n\t" \
    "s_nop 0\n\t" \
    "rr_We45_%=:\n\t" \
    "global_load_dword v192, v7, s[52:53] offset:0 sc1\n\t" \
    "s_sub_u32 s71, s86, s74\n\t" \
    "s_lshl_b32 s71, s71, 2\n\t" \
    "s_lshr_b32 s33, s77, 1\n\t" \
    "s_sub_u32 s34, s73, 176\n\t" \
    "s_mov_b32 s96, 0\n\t" \
    "s_getpc_b64 s[30:31]\n\t" \
    "rr_rpc49_%=:\n\t" \
    "s_add_u32 s50, s30, rr_pgo52_%=-rr_rpc49_%=\n\t" \
    "s_addc_u32 s51, s31, 0\n\t" \
    "s_add_u32 s30, s30, rr_again5_%=-rr_rpc49_%=\n\t" \
    "s_addc_u32 s31, s31, 0\n\t" \
    "s_nop 7\n\t" \
    "s_nop 7\n\t" \
    "s_nop 7\n\t" \
    "s_nop 7\n\t" \
    "s_nop 3\n\t" \
    "global_load_dword v193, v7, s[52:53] offset:2048 sc1\n\t" \
    "s_nop 7\n\t" \
    "s_nop 7\n\t" \
    "s_nop 7\n\t" \
    "s_nop 7\n\t" \
    "s_nop 7\n\t" \
    "s_nop 7\n\t" \
    "global_load_dword v194, v189, s[52:53] offset:0 sc1\n\t" \
    "s_waitcnt vmcnt(2)\n\t" \
    "rr_hok46_%=:\n\t" \
    "s_memtime s[54:55]\n\t" \
    "v_max_u32_dpp v192, v192, v192 row_ror:1 row_mask:0xf bank_mask:0xf\n\t" \
    "s_nop 1\n\t" \
    "v_max_u32_dpp v192, v192, v192 row_ror:2 row_mask:0xf bank_mask:0xf\n\t" \
    "s_nop 1\n\t" \
    "v_max_u32_dpp v192, v192, v192 row_ror:4 row_mask:0xf bank_mask:0xf\n\t" \
    "s_nop 1\n\t" \
    "v_max_u32_dpp v192, v192, v192 row_ror:8 row_mask:0xf bank_mask:0xf\n\t" \
    "s_nop 0\n\t" \
    "v_readlane_b32 s69, v192, 0\n\t" \
    "v_readlane_b32 s70, v192, 16\n\t" \
    "s_waitcnt lgkmcnt(0)\n\t" \
    "rr_hmax55_%=:\n\t" \
    "s_sub_u32 s56, s34, s54\n\t" \
    "s_max_u32 s81, s69, s70\n\t" \
    "s_cmp_eq_u32 s81, -1\n\t" \
    "s_cbranch_scc1 rr_hlate47_%=\n\t" \
    "s_lshl2_add_u32 s70, s81, s71\n\t" \
    "s_mul_hi_i32 s70, s70, s33\n\t" \
    "s_add_u32 s69, s70, s56\n\t" \
    "s_max_i32 s69, s69, 0\n\t" \
    "s_cmp_gt_i32 s69, 80\n\t" \
    "s_cbranch_scc1 rr_rpoll50_%=\n\t" \
    "rr_rback51_%=:\n\t" \
    "s_and_b32 s69, s69, -4\n\t" \
    "s_sub_u32 s30, s30, s69\n\t" \
    "s_subb_u32 s31, s31, 0\n\t" \
    "s_setpc_b64 s[30:31]\n\t" \
    "rr_rpoll50_%=:\n\t" \
    "s_min_i32 s70, s70, 24000\n\t" \
    "s_add_u32 s70, s70, s73\n\t" \
    "s_sub_u32 s70, s70, 124\n\t" \
    "s_sub_u32 s70, s70, s96\n\t" \
    "rr_wl48_%=:\n\t" \
    "s_memtime s[56:57]\n\t" \
    "s_waitcnt lgkmcnt(0)\n\t" \
    "s_sub_u32 s69, s70, s56\n\t" \
    "s_cmp_gt_i32 s69, 80\n\t" \
    "s_cbranch_scc1 rr_wl48_%=\n\t" \
    "s_max_i32 s69, s69, 0\n\t" \
    "s_branch rr_rback51_%=\n\t" \
    "s_nop 0\n\t" \
    "s_nop 0\n\t" \
    "s_nop 0\n\t" \
    "s_nop 0\n\t" \
    "s_nop 0\n\t" \
    "s_nop 0\n\t" \
    "s_nop 0\n\t" \
    "s_nop 0\n\t" \
    "s_nop 0\n\t" \
    "s_nop 0\n\t" \
    "s_nop 0\n\t" \
    "s_nop 0\n\t" \
    "s_nop 0\n\t" \
    "s_nop 0\n\t" \
    "s_nop 0\n\t" \
    "s_nop 0\n\t" \
    "s_nop 0\n\t" \
    "s_nop 0\n\t" \
    "s_nop 0\n\t" \
    "s_nop 0\n\t" \
    "rr_again5_%=:\n\t" \
    "rr_pgo52_%=:\n\t" \
    "global_load_dwordx4 v[48:51], v10, s[46:47] sc1\n\t" \
    "global_load_dwordx4 v[52:55], v11, s[46:47] sc1\n\t" \
    "global_load_dwordx4 v[56:59], v12, s[46:47] sc1\n\t" \
    "global_load_dwordx4 v[60:63], v13, s[46:47] sc1\n\t" \
    "global_load_dwordx4 v[64:67], v14, s[46:47] sc1\n\t" \
    "global_load_dwordx4 v[68:71], v15, s[46:47] sc1\n\t" \
    "global_load_dwordx4 v[72:75], v16, s[46:47] sc1\n\t" \
    "global_load_dwordx4 v[76:79], v17, s[46:47] sc1\n\t" \
    "global_load_dwordx4 v[80:83], v18, s[46:47] sc1\n\t" \
    "global_load_dwordx4 v[84:87], v19, s[46:47] sc1\n\t" \
    "global_load_dwordx4 v[88:91], v20, s[46:47] sc1\n\t" \
    "global_load_dwordx4 v[92:95], v21, s[46:47] sc1\n\t" \
    "global_load_dwordx4 v[96:99], v22, s[46:47] sc1\n\t" \
    "global_load_dwordx4 v[100:103], v23, s[46:47] sc1\n\t" \
    "global_load_dwordx4 v[104:107], v24, s[46:47] sc1\n\t" \
    "global_load_dwordx4 v[108:111], v25, s[46:47] sc1\n\t" \
    "global_load_dwordx4 v[112:115], v42, s[46:47] sc1\n\t" \
    "global_load_dwordx4 v[116:119], v43, s[46:47] sc1\n\t" \
    "global_load_dwordx4 v[120:123], v44, s[46:47] sc1\n\t" \
    "global_load_dwordx4 v[124:127], v45, s[46:47] sc1\n\t" \
    "global_load_dwordx4 v[128:131], v3, s[40:41] offset:0 sc1\n\t" \
    "global_load_dwordx4 v[132:135], v3, s[40:41] offset:1024 sc1\n\t" \
    "global_load_dwordx4 v[136:139], v3, s[40:41] offset:2048 sc1\n\t" \
    "global_load_dwordx4 v[140:143], v3, s[40:41] offset:3072 sc1\n\t" \
    "global_load_dwordx4 v[144:147], v1, s[40:41] offset:0 sc1\n\t" \
    "global_load_dwordx4 v[148:151], v1, s[40:41] offset:1024 sc1\n\t" \
    "global_load_dwordx4 v[152:155], v1, s[40:41] offset:2048 sc1\n\t" \
    "global_load_dwordx4 v[156:159], v1, s[40:41] offset:3072 sc1\n\t" \
    "s_cmp_lg_u32 s91, 0\n\t" \
    "s_cbranch_scc1 rr_nob8_%=\n\t" \
    "rr_hbk56_%=:\n\t" \
    "v_mov_b32 v164, s81\n\t" \
    "ds_write_b32 v9, v164\n\t" \
    "s_waitcnt lgkmcnt(0)\n\t" \
    "s_barrier\n\t" \
    "rr_nob8_%=:\n\t" \
    "s_waitcnt vmcnt(27)\n\t" \
    "v_smfmac_f32_16x16x64_bf16 v[160:163], v[48:51], a[0:7], v4\n\t" \
    "s_waitcnt vmcnt(26)\n\t" \
    "v_smfmac_f32_16x16x64_bf16 v[160:163], v[52:55], a[8:15], v4\n\t" \
    "s_waitcnt vmcnt(25)\n\t" \
    "v_smfmac_f32_16x16x64_bf16 v[160:163], v[56:59], a[16:23], v4\n\t" \
    "s_waitcnt vmcnt(24)\n\t" \
    "v_smfmac_f32_16x16x64_bf16 v[160:163], v[60:63], a[24:31], v4\n\t" \
    "s_waitcnt vmcnt(23)\n\t" \
    "v_smfmac_f32_16x16x64_bf16 v[160:163], v[64:67], a[32:39], v4\n\t" \
    "s_waitcnt vmcnt(22)\n\t" \
    "v_smfmac_f32_16x16x64_bf16 v[160:163], v[68:71], a[40:47], v4\n\t" \
    "s_waitcnt vmcnt(21)\n\t" \
    "v_smfmac_f32_16x16x64_bf16 v[160:163], v[72:75], a[48:55], v4\n\t" \
    "s_waitcnt vmcnt(20)\n\t" \
    "v_smfmac_f32_16x16x64_bf16 v[160:163], v[76:79], a[56:63], v4\n\t" \
    "s_waitcnt vmcnt(19)\n\t" \
    "v_smfmac_f32_16x16x64_bf16 v[160:163], v[80:83], a[64:71], v4\n\t" \
    "s_waitcnt vmcnt(18)\n\t" \
    "v_smfmac_f32_16x16x64_bf16 v[160:163], v[84:87], a[72:79], v4\n\t" \
    "s_waitcnt vmcnt(17)\n\t" \
    "v_smfmac_f32_16x16x64_bf16 v[160:163], v[88:91], a[80:87], v4\n\t" \
    "s_waitcnt vmcnt(16)\n\t" \
    "v_smfmac_f32_16x16x64_bf16 v[160:163], v[92:95], a[88:95], v4\n\t" \
    "s_waitcnt vmcnt(15)\n\t" \
    "v_smfmac_f32_16x16x64_bf16 v[160:163], v[96:99], a[96:103], v4\n\t" \
    "s_waitcnt vmcnt(14)\n\t" \
    "v_smfmac_f32_16x16x64_bf16 v[160:163], v[100:103], a[104:111], v4\n\t" \
    "s_waitcnt vmcnt(13)\n\t" \
    "v_smfmac_f32_16x16x64_bf16 v[160:163], v[104:107], a[112:119], v4\n\t" \
    "s_waitcnt vmcnt(12)\n\t" \
    "v_smfmac_f32_16x16x64_bf16 v[160:163], v[108:111], a[120:127], v4\n\t" \
    "s_waitcnt vmcnt(6)\n\t" \
    "v_smfmac_f32_16x16x64_bf16 v[160:163], v[112:115], v[128:135], v4\n\t" \
    "s_waitcnt vmcnt(4)\n\t" \
    "v_smfmac_f32_16x16x64_bf16 v[160:163], v[116:119], v[136:143], v4\n\t" \
    "s_waitcnt vmcnt(2)\n\t" \
    "v_smfmac_f32_16x16x64_bf16 v[160:163], v[120:123], v[144:151], v4\n\t" \
    "s_waitcnt vmcnt(0)\n\t" \
    "v_smfmac_f32_16x16x64_bf16 v[160:163], v[124:127], v[152:159], v4\n\t" \
    "s_nop 7\n\t" \
    "v_pk_add_f32 v[160:161], v[160:161], v[162:163]\n\t" \
    "v_add_f32 v166, v160, v161\n\t" \
    "v_cvt_pk_bf16_f32 v164, v160, v161\n\t" \
    "s_nop 0\n\t" \
    "v_add_f32_dpp v166, v166, v166 quad_perm:[1,0,3,2] row_mask:0xf bank_mask:0xf\n\t" \
    "v_mov_b32_dpp v165, v164 quad_perm:[1,0,3,2] row_mask:0xf bank_mask:0xf\n\t" \
    "v_cmp_u_f32 vcc, v166, v166\n\t" \
    "v_perm_b32 v167, v165, v164, v5\n\t" \
    "s_nop 0\n\t" \
    "v_cndmask_b32 v164, v167, v172, vcc\n\t" \
    "rr_cont7_%=:\n\t" \
    "global_store_dword v180, v164, s[48:49] sc1\n\t" \
    "global_store_dword v181, v164, s[48:49] sc1\n\t" \
    "global_store_dword v182, v164, s[48:49] sc1\n\t" \
    "global_store_dword v2, v164, s[48:49] sc1\n\t" \
    "s_cmp_lg_u32 s64, 0\n\t" \
    "s_cbranch_scc1 rr_g214_%=\n\t" \
    "s_cbranch_vccnz rr_bad6_%=\n\t" \
    "s_cmp_eq_u32 s65, 62\n\t" \
    "s_cbranch_scc1 rr_end4_%=\n\t" \
    "s_add_u32 s65, s65, 2\n\t" \
    "v_xor_b32 v10, 0x4000, v10\n\t" \
    "v_xor_b32 v11, 0x4000, v11\n\t" \
    "v_xor_b32 v12, 0x4000, v12\n\t" \
    "v_xor_b32 v13, 0x4000, v13\n\t" \
    "v_xor_b32 v14, 0x4000, v14\n\t" \
    "v_xor_b32 v15, 0x4000, v15\n\t" \
    "v_xor_b32 v16, 0x4000, v16\n\t" \
    "v_xor_b32 v17, 0x4000, v17\n\t" \
    "v_xor_b32 v18, 0x4000, v18\n\t" \
    "v_xor_b32 v19, 0x4000, v19\n\t" \
    "v_xor_b32 v20, 0x4000, v20\n\t" \
    "v_xor_b32 v21, 0x4000, v21\n\t" \
    "v_xor_b32 v22, 0x4000, v22\n\t" \
    "v_xor_b32 v23, 0x4000, v23\n\t" \
    "v_xor_b32 v24, 0x4000, v24\n\t" \
    "v_xor_b32 v25, 0x4000, v25\n\t" \
    "v_xor_b32 v42, 0x4000, v42\n\t" \
    "v_xor_b32 v43, 0x4000, v43\n\t" \
    "v_xor_b32 v44, 0x4000, v44\n\t" \
    "v_xor_b32 v45, 0x4000, v45\n\t" \
    "v_xor_b32 v7, 0x4000, v7\n\t" \
    "v_xor_b32 v189, 0x4000, v189\n\t" \
    "v_xor_b32 v190, 0x4000, v190\n\t" \
    "v_xor_b32 v191, 0x4000, v191\n\t" \
    "s_branch rr_pf9_%=\n\t" \
    "rr_bad6_%=:\n\t" \
    "s_cmp_lg_u32 s64, 0\n\t" \
    "s_cbranch_scc1 rr_cont7_%=\n\t" \
    "s_waitcnt lgkmcnt(0)\n\t" \
    "v_max_u32 v165, v48, v49\n\t" \
    "v_max3_u32 v165, v165, v50, v51\n\t" \
    "v_max3_u32 v165, v165, v52, v53\n\t" \
    "v_max3_u32 v165, v165, v54, v55\n\t" \
    "v_max3_u32 v165, v165, v56, v57\n\t" \
    "v_max3_u32 v165, v165, v58, v59\n\t" \
    "v_max3_u32 v165, v165, v60, v61\n\t" \
    "v_max3_u32 v165, v165, v62, v63\n\t" \
    "v_max3_u32 v165, v165, v64, v65\n\t" \
    "v_max3_u32 v165, v165, v66, v67\n\t" \
    "v_max3_u32 v165, v165, v68, v69\n\t" \
    "v_max3_u32 v165, v165, v70, v71\n\t" \
    "v_max3_u32 v165, v165, v72, v73\n\t" \
    "v_max3_u32 v165, v165, v74, v75\n\t" \
    "v_max3_u32 v165, v165, v76, v77\n\t" \
    "v_max3_u32 v165, v165, v78, v79\n\t" \
    "v_max3_u32 v165, v165, v80, v81\n\t" \
    "v_max3_u32 v165, v165, v82, v83\n\t" \
    "v_max3_u32 v165, v165, v84, v85\n\t" \
    "v_max3_u32 v165, v165, v86, v87\n\t" \
    "v_max3_u32 v165, v165, v88, v89\n\t" \
    "v_max3_u32 v165, v165, v90, v91\n\t" \
    "v_max3_u32 v165, v165, v92, v93\n\t" \
    "v_max3_u32 v165, v165, v94, v95\n\t" \
    "v_max3_u32 v165, v165, v96, v97\n\t" \
    "v_max3_u32 v165, v165, v98, v99\n\t" \
    "v_max3_u32 v165, v165, v100, v101\n\t" \
    "v_max3_u32 v165, v165, v102, v103\n\t" \
    "v_max3_u32 v165, v165, v104, v105\n\t" \
    "v_max3_u32 v165, v165, v106, v107\n\t" \
    "v_max3_u32 v165, v165, v108, v109\n\t" \
    "v_max3_u32 v165, v165, v110, v111\n\t" \
    "v_max3_u32 v165, v165, v112, v113\n\t" \
    "v_max3_u32 v165, v165, v114, v115\n\t" \
    "v_max3_u32 v165, v165, v116, v117\n\t" \
    "v_max3_u32 v165, v165, v118, v119\n\t" \
    "v_max3_u32 v165, v165, v120, v121\n\t" \
    "v_max3_u32 v165, v165, v122, v123\n\t" \
    "v_max3_u32 v165, v165, v124, v125\n\t" \
    "v_max3_u32 v165, v165, v126, v127\n\t" \
    "v_max_u32 v166, v128, v129\n\t" \
    "v_max3_u32 v166, v166, v130, v131\n\t" \
    "v_max3_u32 v166, v166, v132, v133\n\t" \
    "v_max3_u32 v166, v166, v134, v135\n\t" \
    "v_max3_u32 v166, v166, v136, v137\n\t" \
    "v_max3_u32 v166, v166, v138, v139\n\t" \
    "v_max3_u32 v166, v166, v140, v141\n\t" \
    "v_max3_u32 v166, v166, v142, v143\n\t" \
    "v_max3_u32 v166, v166, v144, v145\n\t" \
    "v_max3_u32 v166, v166, v146, v147\n\t" \
    "v_max3_u32 v166, v166, v148, v149\n\t" \
    "v_max3_u32 v166, v166, v150, v151\n\t" \
    "v_max3_u32 v166, v166, v152, v153\n\t" \
    "v_max3_u32 v166, v166, v154, v155\n\t" \
    "v_max3_u32 v166, v166, v156, v157\n\t" \
    "v_max3_u32 v166, v166, v158, v159\n\t" \
    "v_max_u32 v165, v165, v166\n\t" \
    "v_mov_b32 v164, v167\n\t" \
    "v_cmp_eq_u32 vcc, -1, v165\n\t" \
    "s_cbranch_vccz rr_cont7_%=\n\t" \
    "s_mov_b64 vcc, 0\n\t" \
    "s_mov_b32 s91, 1\n\t" \
    "s_mov_b32 s96, 0\n\t" \
    "v_mov_b32 v160, 0\n\t" \
    "v_mov_b32 v161, 0\n\t" \
    "v_mov_b32 v162, 0\n\t" \
    "v_mov_b32 v163, 0\n\t" \
    "s_cmp_eq_u32 s68, 0\n\t" \
    "s_cbranch_scc1 rr_F59_%=\n\t" \
    "s_sub_u32 s68, s68, 1\n\t" \
    "s_branch rr_again5_%=\n\t" \
    "rr_F59_%=:\n\t" \
    "v_mov_b32 v166, 5767424\n\t" \
    "v_mov_b32 v167, 3\n\t" \
    "global_store_dword v166, v167, s[36:37] sc1\n\t" \
    "s_waitcnt vmcnt(0)\n\t" \
    "s_branch rr_cont7_%=\n\t" \
    "rr_hlate47_%=:\n\t" \
    "s_cmp_lg_u32 s64, 0\n\t" \
    "s_cbranch_scc1 rr_hpd58_%=\n\t" \
    "global_load_dword v195, v189, s[52:53] offset:2048 sc1\n\t" \
    "s_waitcnt vmcnt(2)\n\t" \
    "v_mov_b32 v192, v193\n\t" \
    "v_cmp_eq_u32 vcc, -1, v193\n\t" \
    "s_cbranch_vccz rr_hok46_%=\n\t" \
    "global_load_dword v196, v190, s[52:53] offset:0 sc1\n\t" \
    "s_waitcnt vmcnt(2)\n\t" \
    "v_mov_b32 v192, v194\n\t" \
    "v_cmp_eq_u32 vcc, -1, v194\n\t" \
    "s_cbranch_vccz rr_hok46_%=\n\t" \
    "global_load_dword v197, v190, s[52:53] offset:2048 sc1\n\t" \
    "s_waitcnt vmcnt(2)\n\t" \
    "v_mov_b32 v192, v195\n\t" \
    "v_cmp_eq_u32 vcc, -1, v195\n\t" \
    "s_cbranch_vccz rr_hok46_%=\n\t" \
    "s_waitcnt vmcnt(1)\n\t" \
    "v_mov_b32 v192, v196\n\t" \
    "v_cmp_eq_u32 vcc, -1, v196\n\t" \
    "s_cbranch_vccz rr_hok46_%=\n\t" \
    "s_waitcnt vmcnt(0)\n\t" \
    "v_mov_b32 v192, v197\n\t" \
    "v_cmp_eq_u32 vcc, -1, v197\n\t" \
    "s_cbranch_vccz rr_hok46_%=\n\t" \
    "rr_hp57_%=:\n\t" \
    "global_load_dword v192, v191, s[52:53] offset:0 sc1\n\t" \
    "s_waitcnt vmcnt(0)\n\t" \
    "v_cmp_eq_u32 vcc, -1, v192\n\t" \
    "s_cbranch_vccz rr_hpd58_%=\n\t" \
    "s_cmp_eq_u32 s68, 0\n\t" \
    "s_cbranch_scc1 rr_F60_%=\n\t" \
    "s_sub_u32 s68, s68, 1\n\t" \
    "s_sleep 1\n\t" \
    "s_branch rr_hp57_%=\n\t" \
    "rr_F60_%=:\n\t" \
    "v_mov_b32 v166, 5767424\n\t" \
    "v_mov_b32 v167, 7\n\t" \
    "global_store_dword v166, v167, s[36:37] sc1\n\t" \
    "s_waitcnt vmcnt(0)\n\t" \
    "s_branch rr_hpd58_%=\n\t" \
    "rr_hpd58_%=:\n\t" \
    "v_and_b32 v192, -2, v192\n\t" \
    "s_waitcnt lgkmcnt(0)\n\t" \
    "s_mov_b32 s70, 116\n\t" \
    "rr_E61_%=:\n\t" \
    "s_memtime s[56:57]\n\t" \
    "s_memrealtime s[2:3]\n\t" \
    "s_nop 0\n\t" \
    "s_memrealtime s[4:5]\n\t" \
    "s_nop 0\n\t" \
    "s_memrealtime s[6:7]\n\t" \
    "s_nop 0\n\t" \
    "s_memrealtime s[8:9]\n\t" \
    "s_nop 0\n\t" \
    "s_memrealtime s[10:11]\n\t" \
    "s_nop 0\n\t" \
    "s_memrealtime s[12:13]\n\t" \
    "s_nop 0\n\t" \
    "s_memrealtime s[14:15]\n\t" \
    "s_nop 0\n\t" \
    "s_memrealtime s[16:17]\n\t" \
    "s_nop 0\n\t" \
    "s_memrealtime s[18:19]\n\t" \
    "s_nop 0\n\t" \
    "s_memrealtime s[20:21]\n\t" \
    "s_nop 0\n\t" \
    "s_memrealtime s[22:23]\n\t" \
    "s_nop 0\n\t" \
    "s_memrealtime s[24:25]\n\t" \
    "s_nop 0\n\t" \
    "s_memrealtime s[26:27]\n\t" \
    "s_nop 0\n\t" \
    "s_memrealtime s[28:29]\n\t" \
    "s_memtime s[54:55]\n\t" \
    "s_add_u32 s70, s70, 8\n\t" \
    "s_waitcnt lgkmcnt(0)\n\t" \
    "s_sub_u32 s69, s54, s56\n\t" \
    "s_cmp_gt_u32 s69, s70\n\t" \
    "s_cbranch_scc1 rr_E61_%=\n\t" \
    "s_add_u32 s2, s2, s4\n\t" \
    "s_add_u32 s6, s6, s8\n\t" \
    "s_add_u32 s10, s10, s12\n\t" \
    "s_add_u32 s14, s14, s16\n\t" \
    "s_add_u32 s18, s18, s20\n\t" \
    "s_add_u32 s22, s22, s24\n\t" \
    "s_add_u32 s2, s2, s6\n\t" \
    "s_add_u32 s10, s10, s14\n\t" \
    "s_add_u32 s18, s18, s22\n\t" \
    "s_add_u32 s2, s2, s26\n\t" \
    "s_add_u32 s10, s10, s18\n\t" \
    "s_add_u32 s2, s2, s10\n\t" \
    "s_mul_i32 s70, s28, 13\n\t" \
    "s_sub_u32 s70, s70, s2\n\t" \
    "s_min_u32 s70, s70, 52\n\t" \
    "s_mul_i32 s69, s69, s70\n\t" \
    "s_mul_hi_u32 s69, s69, 76695844\n\t" \
    "s_mov_b32 s75, s73\n\t" \
    "s_mov_b32 s76, s74\n\t" \
    "s_add_u32 s73, s56, s69\n\t" \
    "s_lshl_b32 s74, s28, 4\n\t" \
    "s_sub_u32 s71, s86, s74\n\t" \
    "s_lshl_b32 s71, s71, 2\n\t" \
    "s_lshr_b32 s33, s77, 1\n\t" \
    "s_sub_u32 s34, s73, 176\n\t" \
    "s_branch rr_hok46_%=\n\t" \
    "rr_end4_%=:\n\t" \
    "s_endpgm\n\t" \
    "rr_last_%=:\n\t" \
    ""
#define ASM_H \
    "s_mov_b64 s[36:37], %0\n\t" \
    "s_mov_b64 s[42:43], %1\n\t" \
    "s_mov_b64 s[44:45], %2\n\t" \
    "s_mov_b32 s66, %3\n\t" \
    "s_mov_b32 s67, %4\n\t" \
    "s_mov_b64 s[50:51], %5\n\t" \
    "s_mov_b32 s82, %7\n\t" \
    "s_load_dwordx8 s[84:91], %6, 0\n\t" \
    "v_mbcnt_lo_u32_b32 v223, -1, 0\n\t" \
    "v_mbcnt_hi_u32_b32 v223, -1, v223\n\t" \
    "s_getpc_b64 s[46:47]\n\t" \
    "hh_pc_%=:\n\t" \
    "s_lshl_b32 s69, s66, 13\n\t" \
    "v_lshl_add_u32 v222, v223, 7, s69\n\t" \
    "v_min_u32 v222, hh_last_%=-hh_pc_%=-4, v222\n\t" \
    "global_load_dword v222, v222, s[46:47]\n\t" \
    "v_lshlrev_b32 v223, 2, v223\n\t" \
    "v_add_u32 v223, s82, v223\n\t" \
    "ds_read_b32 v0, v223 offset:0\n\t" \
    "ds_read_b32 v1, v223 offset:256\n\t" \
    "ds_read_b32 v2, v223 offset:512\n\t" \
    "ds_read_b32 v3, v223 offset:768\n\t" \
    "ds_read_b32 v4, v223 offset:1024\n\t" \
    "ds_read_b32 v5, v223 offset:1280\n\t" \
    "ds_read_b32 v6, v223 offset:1536\n\t" \
    "ds_read_b32 v7, v223 offset:1792\n\t" \
    "ds_read_b32 v8, v223 offset:2048\n\t" \
    "ds_read_b32 v9, v223 offset:2304\n\t" \
    "ds_read_b32 v10, v223 offset:2560\n\t" \
    "ds_read_b32 v11, v223 offset:2816\n\t" \
    "s_waitcnt lgkmcnt(0)\n\t" \
    "ds_read_b32 v12, v223 offset:3072\n\t" \
    "ds_read_b32 v13, v223 offset:3328\n\t" \
    "ds_read_b32 v14, v223 offset:3584\n\t" \
    "ds_read_b32 v15, v223 offset:3840\n\t" \
    "ds_read_b32 v16, v223 offset:4096\n\t" \
    "ds_read_b32 v17, v223 offset:4352\n\t" \
    "s_waitcnt lgkmcnt(0)\n\t" \
    "s_mov_b32 s68, 262144\n\t" \
    "s_mov_b32 s72, 0\n\t" \
    "s_mov_b32 s83, -1\n\t" \
    "s_mov_b32 s65, 0\n\t" \
    "s_mov_b32 s77, 3019898880\n\t" \
    "s_mov_b32 s78, 3054198966\n\t" \
    "s_waitcnt lgkmcnt(0)\n\t" \
    "s_mov_b32 s70, 116\n\t" \
    "hh_E1_%=:\n\t" \
    "s_memtime s[56:57]\n\t" \
    "s_memrealtime s[2:3]\n\t" \
    "s_nop 0\n\t" \
    "s_memrealtime s[4:5]\n\t" \
    "s_nop 0\n\t" \
    "s_memrealtime s[6:7]\n\t" \
    "s_nop 0\n\t" \
    "s_memrealtime s[8:9]\n\t" \
    "s_nop 0\n\t" \
    "s_memrealtime s[10:11]\n\t" \
    "s_nop 0\n\t" \
    "s_memrealtime s[12:13]\n\t" \
    "s_nop 0\n\t" \
    "s_memrealtime s[14:15]\n\t" \
    "s_nop 0\n\t" \
    "s_memrealtime s[16:17]\n\t" \
    "s_nop 0\n\t" \
    "s_memrealtime s[18:19]\n\t" \
    "s_nop 0\n\t" \
    "s_memrealtime s[20:21]\n\t" \
    "s_nop 0\n\t" \
    "s_memrealtime s[22:23]\n\t" \
    "s_nop 0\n\t" \
    "s_memrealtime s[24:25]\n\t" \
    "s_nop 0\n\t" \
    "s_memrealtime s[26:27]\n\t" \
    "s_nop 0\n\t" \
    "s_memrealtime s[28:29]\n\t" \
    "s_memtime s[54:55]\n\t" \
    "s_add_u32 s70, s70, 8\n\t" \
    "s_waitcnt lgkmcnt(0)\n\t" \
    "s_sub_u32 s69, s54, s56\n\t" \
    "s_cmp_gt_u32 s69, s70\n\t" \
    "s_cbranch_scc1 hh_E1_%=\n\t" \
    "s_add_u32 s2, s2, s4\n\t" \
    "s_add_u32 s6, s6, s8\n\t" \
    "s_add_u32 s10, s10, s12\n\t" \
    "s_add_u32 s14, s14, s16\n\t" \
    "s_add_u32 s18, s18, s20\n\t" \
    "s_add_u32 s22, s22, s24\n\t" \
    "s_add_u32 s2, s2, s6\n\t" \
    "s_add_u32 s10, s10, s14\n\t" \
    "s_add_u32 s18, s18, s22\n\t" \
    "s_add_u32 s2, s2, s26\n\t" \
    "s_add_u32 s10, s10, s18\n\t" \
    "s_add_u32 s2, s2, s10\n\t" \
    "s_mul_i32 s70, s28, 13\n\t" \
    "s_sub_u32 s70, s70, s2\n\t" \
    "s_min_u32 s70, s70, 52\n\t" \
    "s_mul_i32 s69, s69, s70\n\t" \
    "s_mul_hi_u32 s69, s69, 76695844\n\t" \
    "s_mov_b32 s75, s73\n\t" \
    "s_mov_b32 s76, s74\n\t" \
    "s_add_u32 s73, s56, s69\n\t" \
    "s_lshl_b32 s74, s28, 4\n\t" \
    "v_mbcnt_lo_u32_b32 v30, -1, 0\n\t" \
    "v_mbcnt_hi_u32_b32 v30, -1, v30\n\t" \
    "v_lshlrev_b32 v30, 6, v30\n\t" \
    "v_add_u32 v30, 6225920, v30\n\t" \
    "v_mov_b32 v31, 6234112\n\t" \
    "global_load_dword v31, v31, s[36:37] sc1\n\t" \
    "global_load_dwordx2 v[32:33], v30, s[36:37] sc1\n\t" \
    "global_load_dword v34, v30, s[36:37] offset:2048 sc1\n\t" \
    "s_waitcnt vmcnt(0)\n\t" \
    "hh_C2_%=:\n\t" \
    "v_cmp_ne_u32 s[54:55], v32, v34\n\t" \
    "v_sub_u32 v32, v32, v31\n\t" \
    "v_cmp_ne_u32 vcc, 1, v32\n\t" \
    "s_nop 1\n\t" \
    "s_or_b64 vcc, vcc, s[54:55]\n\t" \
    "s_branch hh_Cok3_%=\n\t" \
    "s_cmp_eq_u32 s68, 0\n\t" \
    "s_cbranch_scc1 hh_F5_%=\n\t" \
    "s_sub_u32 s68, s68, 1\n\t" \
    "s_sleep 6\n\t" \
    "s_branch hh_Cag4_%=\n\t" \
    "hh_F5_%=:\n\t" \
    "v_mov_b32 v34, 5767424\n\t" \
    "v_mov_b32 v35, 4\n\t" \
    "global_store_dword v34, v35, s[36:37] sc1\n\t" \
    "s_waitcnt vmcnt(0)\n\t" \
    "s_branch hh_Cok3_%=\n\t" \
    "hh_Cag4_%=:\n\t" \
    "global_load_dwordx2 v[32:33], v30, s[36:37] sc1\n\t" \
    "global_load_dword v34, v30, s[36:37] offset:2048 sc1\n\t" \
    "s_waitcnt vmcnt(0)\n\t" \
    "s_branch hh_C2_%=\n\t" \
    "hh_Cok3_%=:\n\t" \
    "v_readlane_b32 s69, v33, 2\n\t" \
    "v_readlane_b32 s70, v33, 3\n\t" \
    "s_nop 1\n\t" \
    "s_cmp_lt_u32 s70, s69\n\t" \
    "s_cselect_b32 s69, 0, 8192\n\t" \
    "s_add_u32 s100, s36, s69\n\t" \
    "s_addc_u32 s101, s37, 0\n\t" \
    "v_mov_b32_dpp v30, v33 quad_perm:[1,0,3,2] row_mask:0xf bank_mask:0xf\n\t" \
    "v_cmp_lt_u32 s[44:45], v30, v33\n\t" \
    "v_min_u32 v30, v30, v33\n\t" \
    "v_and_b32 v31, 1, v30\n\t" \
    "v_cmp_ne_u32 vcc, 0, v31\n\t" \
    "s_nop 1\n\t" \
    "s_and_b32 s44, s44, 0x55555555\n\t" \
    "s_and_b32 s69, vcc_lo, 0xaaaaaaaa\n\t" \
    "s_or_b32 s44, s44, s69\n\t" \
    "s_and_b32 s45, s45, 0x55555555\n\t" \
    "s_and_b32 s69, vcc_hi, 0xaaaaaaaa\n\t" \
    "s_or_b32 s45, s45, s69\n\t" \
    "s_bitcmp1_b32 s44, 0\n\t" \
    "s_cselect_b32 s62, 8192, 0\n\t" \
    "s_bitcmp1_b32 s44, 2\n\t" \
    "s_cselect_b32 s63, 8192, 0\n\t" \
    "s_or_b32 s63, s63, 0x200000\n\t" \
    "v_cmp_lt_u32 vcc, 1320, v30\n\t" \
    "s_nop 1\n\t" \
    "s_bcnt1_i32_b64 s69, vcc\n\t" \
    "s_cmp_gt_u32 s69, 16\n\t" \
    "s_cselect_b32 s69, 1, 0\n\t" \
    "s_mul_i32 s70, s69, 160\n\t" \
    "s_add_u32 s86, s86, s70\n\t" \
    "s_mul_i32 s70, s69, 536\n\t" \
    "s_add_u32 s87, s87, s70\n\t" \
    "s_mul_i32 s70, s69, 256\n\t" \
    "s_add_u32 s88, s88, s70\n\t" \
    "s_bitcmp1_b32 s44, 3\n\t" \
    "s_cselect_b32 s69, 16384, 0\n\t" \
    "v_xor_b32 v1, s69, v1\n\t" \
    "s_waitcnt lgkmcnt(0)\n\t" \
    "s_mov_b32 s70, 116\n\t" \
    "hh_E6_%=:\n\t" \
    "s_memtime s[56:57]\n\t" \
    "s_memrealtime s[2:3]\n\t" \
    "s_nop 0\n\t" \
    "s_memrealtime s[4:5]\n\t" \
    "s_nop 0\n\t" \
    "s_memrealtime s[6:7]\n\t" \
    "s_nop 0\n\t" \
    "s_memrealtime s[8:9]\n\t" \
    "s_nop 0\n\t" \
    "s_memrealtime s[10:11]\n\t" \
    "s_nop 0\n\t" \
    "s_memrealtime s[12:13]\n\t" \
    "s_nop 0\n\t" \
    "s_memrealtime s[14:15]\n\t" \
    "s_nop 0\n\t" \
    "s_memrealtime s[16:17]\n\t" \
    "s_nop 0\n\t" \
    "s_memrealtime s[18:19]\n\t" \
    "s_nop 0\n\t" \
    "s_memrealtime s[20:21]\n\t" \
    "s_nop 0\n\t" \
    "s_memrealtime s[22:23]\n\t" \
    "s_nop 0\n\t" \
    "s_memrealtime s[24:25]\n\t" \
    "s_nop 0\n\t" \
    "s_memrealtime s[26:27]\n\t" \
    "s_nop 0\n\t" \
    "s_memrealtime s[28:29]\n\t" \
    "s_memtime s[54:55]\n\t" \
    "s_add_u32 s70, s70, 8\n\t" \
    "s_waitcnt lgkmcnt(0)\n\t" \
    "s_sub_u32 s69, s54, s56\n\t" \
    "s_cmp_gt_u32 s69, s70\n\t" \
    "s_cbranch_scc1 hh_E6_%=\n\t" \
    "s_add_u32 s2, s2, s4\n\t" \
    "s_add_u32 s6, s6, s8\n\t" \
    "s_add_u32 s10, s10, s12\n\t" \
    "s_add_u32 s14, s14, s16\n\t" \
    "s_add_u32 s18, s18, s20\n\t" \
    "s_add_u32 s22, s22, s24\n\t" \
    "s_add_u32 s2, s2, s6\n\t" \
    "s_add_u32 s10, s10, s14\n\t" \
    "s_add_u32 s18, s18, s22\n\t" \
    "s_add_u32 s2, s2, s26\n\t" \
    "s_add_u32 s10, s10, s18\n\t" \
    "s_add_u32 s2, s2, s10\n\t" \
    "s_mul_i32 s70, s28, 13\n\t" \
    "s_sub_u32 s70, s70, s2\n\t" \
    "s_min_u32 s70, s70, 52\n\t" \
    "s_mul_i32 s69, s69, s70\n\t" \
    "s_mul_hi_u32 s69, s69, 76695844\n\t" \
    "s_mov_b32 s75, s73\n\t" \
    "s_mov_b32 s76, s74\n\t" \
    "s_add_u32 s73, s56, s69\n\t" \
    "s_lshl_b32 s74, s28, 4\n\t" \
    "s_sub_u32 s69, s73, s75\n\t" \
    "s_sub_u32 s70, s74, s76\n\t" \
    "s_sub_u32 s71, s70, 1600\n\t" \
    "s_cmp_lt_u32 s71, 400000\n\t" \
    "s_cbranch_scc0 hh_K7_%=\n\t" \
    "v_cvt_f32_u32 v30, s69\n\t" \
    "v_cvt_f32_u32 v31, s70\n\t" \
    "v_rcp_f32 v32, v30\n\t" \
    "v_rcp_f32 v33, v31\n\t" \
    "v_mul_f32 v32, v32, v31\n\t" \
    "v_mul_f32 v33, v33, v30\n\t" \
    "v_mul_f32 v32, 0x4f800000, v32\n\t" \
    "v_mul_f32 v33, 0x4f000000, v33\n\t" \
    "v_cvt_u32_f32 v32, v32\n\t" \
    "v_cvt_u32_f32 v33, v33\n\t" \
    "v_readfirstlane_b32 s69, v32\n\t" \
    "v_readfirstlane_b32 s70, v33\n\t" \
    "s_nop 1\n\t" \
    "s_mov_b32 s78, s69\n\t" \
    "s_mov_b32 s77, s70\n\t" \
    "hh_K7_%=:\n\t" \
    "s_sub_u32 s80, s74, 32000\n\t" \
    "v_mov_b32 v38, -1\n\t" \
    "v_mov_b32 v39, -1\n\t" \
    "s_sub_u32 s69, s100, s36\n\t" \
    "v_cmp_eq_u32 vcc, 0, v3\n\t" \
    "v_mov_b32 v30, s69\n\t" \
    "s_nop 0\n\t" \
    "v_cndmask_b32 v30, 0, v30, vcc\n\t" \
    "v_add_u32 v2, v2, v30\n\t" \
    "s_mov_b32 m0, 155648\n\t" \
    "s_mul_i32 s96, s67, 16\n\t" \
    "s_add_u32 s96, s96, 64\n\t" \
    "hh_loop8_%=:\n\t" \
    "s_and_b32 s69, s65, 7\n\t" \
    "s_lshl_b32 s69, s69, 18\n\t" \
    "s_add_u32 s69, s69, s63\n\t" \
    "s_add_u32 s52, s36, s69\n\t" \
    "s_addc_u32 s53, s37, 0\n\t" \
    "s_add_u32 s71, s80, 608\n\t" \
    "s_sub_u32 s70, s71, s74\n\t" \
    "s_max_i32 s70, s70, 0\n\t" \
    "s_min_i32 s70, s70, 16000\n\t" \
    "s_lshl_b32 s70, s70, 1\n\t" \
    "s_mul_hi_u32 s70, s70, s77\n\t" \
    "s_add_u32 s70, s70, s73\n\t" \
    "hh_W11_%=:\n\t" \
    "s_memtime s[56:57]\n\t" \
    "s_waitcnt lgkmcnt(0)\n\t" \
    "s_sub_u32 s69, s70, s56\n\t" \
    "s_cmp_gt_i32 s69, 24\n\t" \
    "s_cbranch_scc1 hh_W11_%=\n\t" \
    "global_load_dword v30, v1, s[52:53] sc1\n\t" \
    "s_cmp_lt_u32 s65, 7\n\t" \
    "s_cselect_b32 s69, -2, 63\n\t" \
    "v_mov_b32 v38, s69\n\t" \
    "s_sub_u32 s69, s65, 2\n\t" \
    "s_cmp_ge_u32 s69, 5\n\t" \
    "s_cbranch_scc1 hh_ne12_%=\n\t" \
    "v_mad_u32_u24 v39, s65, v3, v2\n\t" \
    "global_load_dword v38, v39, s[36:37] sc1\n\t" \
    "hh_ne12_%=:\n\t" \
    "s_waitcnt lgkmcnt(0)\n\t" \
    "s_mov_b32 s70, 116\n\t" \
    "hh_E16_%=:\n\t" \
    "s_memtime s[56:57]\n\t" \
    "s_memrealtime s[2:3]\n\t" \
    "s_nop 0\n\t" \
    "s_memrealtime s[4:5]\n\t" \
    "s_nop 0\n\t" \
    "s_memrealtime s[6:7]\n\t" \
    "s_nop 0\n\t" \
    "s_memrealtime s[8:9]\n\t" \
    "s_nop 0\n\t" \
    "s_memrealtime s[10:11]\n\t" \
    "s_nop 0\n\t" \
    "s_memrealtime s[12:13]\n\t" \
    "s_nop 0\n\t" \
    "s_memrealtime s[14:15]\n\t" \
    "s_nop 0\n\t" \
    "s_memrealtime s[16:17]\n\t" \
    "s_nop 0\n\t" \
    "s_memrealtime s[18:19]\n\t" \
    "s_nop 0\n\t" \
    "s_memrealtime s[20:21]\n\t" \
    "s_nop 0\n\t" \
    "s_memrealtime s[22:23]\n\t" \
    "s_nop 0\n\t" \
    "s_memrealtime s[24:25]\n\t" \
    "s_nop 0\n\t" \
    "s_memrealtime s[26:27]\n\t" \
    "s_nop 0\n\t" \
    "s_memrealtime s[28:29]\n\t" \
    "s_memtime s[54:55]\n\t" \
    "s_add_u32 s70, s70, 8\n\t" \
    "s_waitcnt lgkmcnt(0)\n\t" \
    "s_sub_u32 s69, s54, s56\n\t" \
    "s_cmp_gt_u32 s69, s70\n\t" \
    "s_cbranch_scc1 hh_E16_%=\n\t" \
    "s_add_u32 s2, s2, s4\n\t" \
    "s_add_u32 s6, s6, s8\n\t" \
    "s_add_u32 s10, s10, s12\n\t" \
    "s_add_u32 s14, s14, s16\n\t" \
    "s_add_u32 s18, s18, s20\n\t" \
    "s_add_u32 s22, s22, s24\n\t" \
    "s_add_u32 s2, s2, s6\n\t" \
    "s_add_u32 s10, s10, s14\n\t" \
    "s_add_u32 s18, s18, s22\n\t" \
    "s_add_u32 s2, s2, s26\n\t" \
    "s_add_u32 s10, s10, s18\n\t" \
    "s_add_u32 s2, s2, s10\n\t" \
    "s_mul_i32 s70, s28, 13\n\t" \
    "s_sub_u32 s70, s70, s2\n\t" \
    "s_min_u32 s70, s70, 52\n\t" \
    "s_mul_i32 s69, s69, s70\n\t" \
    "s_mul_hi_u32 s69, s69, 76695844\n\t" \
    "s_mov_b32 s75, s73\n\t" \
    "s_mov_b32 s76, s74\n\t" \
    "s_add_u32 s73, s56, s69\n\t" \
    "s_lshl_b32 s74, s28, 4\n\t" \
    "s_sub_u32 s69, s73, s75\n\t" \
    "s_sub_u32 s70, s74, s76\n\t" \
    "s_sub_u32 s71, s70, 1600\n\t" \
    "s_cmp_lt_u32 s71, 400000\n\t" \
    "s_cbranch_scc0 hh_K17_%=\n\t" \
    "v_cvt_f32_u32 v34, s69\n\t" \
    "v_cvt_f32_u32 v35, s70\n\t" \
    "v_rcp_f32 v36, v34\n\t" \
    "v_rcp_f32 v37, v35\n\t" \
    "v_mul_f32 v36, v36, v35\n\t" \
    "v_mul_f32 v37, v37, v34\n\t" \
    "v_mul_f32 v36, 0x4f800000, v36\n\t" \
    "v_mul_f32 v37, 0x4f000000, v37\n\t" \
    "v_cvt_u32_f32 v36, v36\n\t" \
    "v_cvt_u32_f32 v37, v37\n\t" \
    "v_readfirstlane_b32 s69, v36\n\t" \
    "v_readfirstlane_b32 s70, v37\n\t" \
    "s_nop 1\n\t" \
    "s_sub_u32 s69, s69, s78\n\t" \
    "s_ashr_i32 s69, s69, 1\n\t" \
    "s_add_u32 s78, s78, s69\n\t" \
    "s_sub_u32 s70, s70, s77\n\t" \
    "s_ashr_i32 s70, s70, 1\n\t" \
    "s_add_u32 s77, s77, s70\n\t" \
    "hh_K17_%=:\n\t" \
    "s_waitcnt vmcnt(0)\n\t" \
    "v_cmp_eq_u32 vcc, -1, v30\n\t" \
    "s_cbranch_vccz hh_Hok14_%=\n\t" \
    "hh_H13_%=:\n\t" \
    "global_load_dword v30, v1, s[52:53] sc1\n\t" \
    "s_waitcnt vmcnt(0)\n\t" \
    "v_cmp_eq_u32 vcc, -1, v30\n\t" \
    "s_cbranch_vccz hh_Hc15_%=\n\t" \
    "s_cmp_eq_u32 s68, 0\n\t" \
    "s_cbranch_scc1 hh_F18_%=\n\t" \
    "s_sub_u32 s68, s68, 1\n\t" \
    "s_sleep 2\n\t" \
    "s_branch hh_H13_%=\n\t" \
    "hh_F18_%=:\n\t" \
    "v_mov_b32 v31, 5767424\n\t" \
    "v_mov_b32 v32, 14\n\t" \
    "global_store_dword v31, v32, s[36:37] sc1\n\t" \
    "s_waitcnt vmcnt(0)\n\t" \
    "s_branch hh_Hc15_%=\n\t" \
    "hh_Hc15_%=:\n\t" \
    "s_waitcnt lgkmcnt(0)\n\t" \
    "s_mov_b32 s70, 116\n\t" \
    "hh_E19_%=:\n\t" \
    "s_memtime s[56:57]\n\t" \
    "s_memrealtime s[2:3]\n\t" \
    "s_nop 0\n\t" \
    "s_memrealtime s[4:5]\n\t" \
    "s_nop 0\n\t" \
    "s_memrealtime s[6:7]\n\t" \
    "s_nop 0\n\t" \
    "s_memrealtime s[8:9]\n\t" \
    "s_nop 0\n\t" \
    "s_memrealtime s[10:11]\n\t" \
    "s_nop 0\n\t" \
    "s_memrealtime s[12:13]\n\t" \
    "s_nop 0\n\t" \
    "s_memrealtime s[14:15]\n\t" \
    "s_nop 0\n\t" \
    "s_memrealtime s[16:17]\n\t" \
    "s_nop 0\n\t" \
    "s_memrealtime s[18:19]\n\t" \
    "s_nop 0\n\t" \
    "s_memrealtime s[20:21]\n\t" \
    "s_nop 0\n\t" \
    "s_memrealtime s[22:23]\n\t" \
    "s_nop 0\n\t" \
    "s_memrealtime s[24:25]\n\t" \
    "s_nop 0\n\t" \
    "s_memrealtime s[26:27]\n\t" \
    "s_nop 0\n\t" \
    "s_memrealtime s[28:29]\n\t" \
    "s_memtime s[54:55]\n\t" \
    "s_add_u32 s70, s70, 8\n\t" \
    "s_waitcnt lgkmcnt(0)\n\t" \
    "s_sub_u32 s69, s54, s56\n\t" \
    "s_cmp_gt_u32 s69, s70\n\t" \
    "s_cbranch_scc1 hh_E19_%=\n\t" \
    "s_add_u32 s2, s2, s4\n\t" \
    "s_add_u32 s6, s6, s8\n\t" \
    "s_add_u32 s10, s10, s12\n\t" \
    "s_add_u32 s14, s14, s16\n\t" \
    "s_add_u32 s18, s18, s20\n\t" \
    "s_add_u32 s22, s22, s24\n\t" \
    "s_add_u32 s2, s2, s6\n\t" \
    "s_add_u32 s10, s10, s14\n\t" \
    "s_add_u32 s18, s18, s22\n\t" \
    "s_add_u32 s2, s2, s26\n\t" \
    "s_add_u32 s10, s10, s18\n\t" \
    "s_add_u32 s2, s2, s10\n\t" \
    "s_mul_i32 s70, s28, 13\n\t" \
    "s_sub_u32 s70, s70, s2\n\t" \
    "s_min_u32 s70, s70, 52\n\t" \
    "s_mul_i32 s69, s69, s70\n\t" \
    "s_mul_hi_u32 s69, s69, 76695844\n\t" \
    "s_mov_b32 s75, s73\n\t" \
    "s_mov_b32 s76, s74\n\t" \
    "s_add_u32 s73, s56, s69\n\t" \
    "s_lshl_b32 s74, s28, 4\n\t" \
    "s_sub_u32 s69, s73, s75\n\t" \
    "s_sub_u32 s70, s74, s76\n\t" \
    "s_sub_u32 s71, s70, 1600\n\t" \
    "s_cmp_lt_u32 s71, 400000\n\t" \
    "s_cbranch_scc0 hh_K20_%=\n\t" \
    "v_cvt_f32_u32 v34, s69\n\t" \
    "v_cvt_f32_u32 v35, s70\n\t" \
    "v_rcp_f32 v36, v34\n\t" \
    "v_rcp_f32 v37, v35\n\t" \
    "v_mul_f32 v36, v36, v35\n\t" \
    "v_mul_f32 v37, v37, v34\n\t" \
    "v_mul_f32 v36, 0x4f800000, v36\n\t" \
    "v_mul_f32 v37, 0x4f000000, v37\n\t" \
    "v_cvt_u32_f32 v36, v36\n\t" \
    "v_cvt_u32_f32 v37, v37\n\t" \
    "v_readfirstlane_b32 s69, v36\n\t" \
    "v_readfirstlane_b32 s70, v37\n\t" \
    "s_nop 1\n\t" \
    "s_sub_u32 s69, s69, s78\n\t" \
    "s_ashr_i32 s69, s69, 1\n\t" \
    "s_add_u32 s78, s78, s69\n\t" \
    "s_sub_u32 s70, s70, s77\n\t" \
    "s_ashr_i32 s70, s70, 1\n\t" \
    "s_add_u32 s77, s77, s70\n\t" \
    "hh_K20_%=:\n\t" \
    "hh_Hok14_%=:\n\t" \
    "v_max_u32_dpp v30, v30, v30 row_ror:1 row_mask:0xf bank_mask:0xf\n\t" \
    "s_nop 1\n\t" \
    "v_max_u32_dpp v30, v30, v30 row_ror:2 row_mask:0xf bank_mask:0xf\n\t" \
    "s_nop 1\n\t" \
    "v_max_u32_dpp v30, v30, v30 row_ror:4 row_mask:0xf bank_mask:0xf\n\t" \
    "s_nop 1\n\t" \
    "v_max_u32_dpp v30, v30, v30 row_ror:8 row_mask:0xf bank_mask:0xf\n\t" \
    "s_nop 0\n\t" \
    "v_readlane_b32 s69, v30, 0\n\t" \
    "v_readlane_b32 s71, v30, 16\n\t" \
    "s_nop 1\n\t" \
    "s_max_u32 s81, s69, s71\n\t" \
    "s_mul_hi_u32 s69, s85, s78\n\t" \
    "s_add_u32 s79, s81, s69\n\t" \
    "s_add_u32 s79, s79, s87\n\t" \
    "s_cmp_lt_u32 s65, 5\n\t" \
    "s_cselect_b32 s69, 48, 0\n\t" \
    "s_add_u32 s79, s79, s69\n\t" \
    "s_add_u32 s80, s79, s88\n\t" \
    "s_add_u32 s65, s65, 1\n\t" \
    "s_cmp_gt_u32 s65, 62\n\t" \
    "s_cbranch_scc1 hh_end10_%=\n\t" \
    "v_lshrrev_b32 v39, 11, v3\n\t" \
    "v_sub_u32 v38, v38, v39\n\t" \
    "s_min_u32 s69, s65, 32\n\t" \
    "v_cmp_ge_i32 vcc, s69, v38\n\t" \
    "s_cbranch_vccnz hh_loop8_%=\n\t" \
    "v_lshrrev_b32 v20, 15, v11\n\t" \
    "v_add3_u32 v20, v20, s65, 1\n\t" \
    "v_min_u32 v20, 63, v20\n\t" \
    "v_mul_u32_u24 v20, v20, v11\n\t" \
    "v_xor_b32 v20, v20, v4\n\t" \
    "v_lshrrev_b32 v21, 15, v12\n\t" \
    "v_add3_u32 v21, v21, s65, 1\n\t" \
    "v_min_u32 v21, 63, v21\n\t" \
    "v_mul_u32_u24 v21, v21, v12\n\t" \
    "v_xor_b32 v21, v21, v5\n\t" \
    "v_lshrrev_b32 v22, 15, v13\n\t" \
    "v_add3_u32 v22, v22, s65, 1\n\t" \
    "v_min_u32 v22, 63, v22\n\t" \
    "v_mul_u32_u24 v22, v22, v13\n\t" \
    "v_xor_b32 v22, v22, v6\n\t" \
    "v_lshrrev_b32 v23, 15, v14\n\t" \
    "v_add3_u32 v23, v23, s65, 1\n\t" \
    "v_min_u32 v23, 63, v23\n\t" \
    "v_mul_u32_u24 v23, v23, v14\n\t" \
    "v_xor_b32 v23, v23, v7\n\t" \
    "v_lshrrev_b32 v24, 15, v15\n\t" \
    "v_add3_u32 v24, v24, s65, 1\n\t" \
    "v_min_u32 v24, 63, v24\n\t" \
    "v_mul_u32_u24 v24, v24, v15\n\t" \
    "v_xor_b32 v24, v24, v8\n\t" \
    "v_lshrrev_b32 v25, 15, v16\n\t" \
    "v_add3_u32 v25, v25, s65, 1\n\t" \
    "v_min_u32 v25, 63, v25\n\t" \
    "v_mul_u32_u24 v25, v25, v16\n\t" \
    "v_xor_b32 v25, v25, v9\n\t" \
    "v_lshrrev_b32 v26, 15, v17\n\t" \
    "v_add3_u32 v26, v26, s65, 1\n\t" \
    "v_min_u32 v26, 63, v26\n\t" \
    "v_mul_u32_u24 v26, v26, v17\n\t" \
    "v_xor_b32 v26, v26, v10\n\t" \
    "s_add_u32 s71, s79, s96\n\t" \
    "s_sub_u32 s70, s71, s74\n\t" \
    "s_max_i32 s70, s70, 0\n\t" \
    "s_min_i32 s70, s70, 16000\n\t" \
    "s_lshl_b32 s70, s70, 1\n\t" \
    "s_mul_hi_u32 s70, s70, s77\n\t" \
    "s_add_u32 s70, s70, s73\n\t" \
    "s_getpc_b64 s[30:31]\n\t" \
    "hh_Wp23_%=:\n\t" \
    "s_add_u32 s30, s30, hh_We24_%=-hh_Wp23_%=\n\t" \
    "s_addc_u32 s31, s31, 0\n\t" \
    "s_sub_u32 s70, s70, 96\n\t" \
    "hh_W22_%=:\n\t" \
    "s_memtime s[56:57]\n\t" \
    "s_waitcnt lgkmcnt(0)\n\t" \
    "s_sub_u32 s69, s70, s56\n\t" \
    "s_cmp_gt_i32 s69, 80\n\t" \
    "s_cbranch_scc1 hh_W22_%=\n\t" \
    "s_max_i32 s69, s69, 0\n\t" \
    "s_and_b32 s69, s69, -4\n\t" \
    "s_sub_u32 s30, s30, s69\n\t" \
    "s_subb_u32 s31, s31, 0\n\t" \
    "s_setpc_b64 s[30:31]\n\t" \
    "s_nop 0\n\t" \
    "s_nop 0\n\t" \
    "s_nop 0\n\t" \
    "s_nop 0\n\t" \
    "s_nop 0\n\t" \
    "s_nop 0\n\t" \
    "s_nop 0\n\t" \
    "s_nop 0\n\t" \
    "s_nop 0\n\t" \
    "s_nop 0\n\t" \
    "s_nop 0\n\t" \
    "s_nop 0\n\t" \
    "s_nop 0\n\t" \
    "s_nop 0\n\t" \
    "s_nop 0\n\t" \
    "s_nop 0\n\t" \
    "s_nop 0\n\t" \
    "s_nop 0\n\t" \
    "s_nop 0\n\t" \
    "s_nop 0\n\t" \
    "hh_We24_%=:\n\t" \
    "global_load_lds_dword v26, s[36:37] sc1\n\t" \
    "s_sleep 2\n\t" \
    "s_nop 5\n\t" \
    "global_load_lds_dword v25, s[36:37] sc1\n\t" \
    "s_sleep 2\n\t" \
    "s_nop 5\n\t" \
    "global_load_lds_dword v24, s[36:37] sc1\n\t" \
    "s_sleep 2\n\t" \
    "s_nop 5\n\t" \
    "global_load_lds_dword v23, s[36:37] sc1\n\t" \
    "s_sleep 2\n\t" \
    "s_nop 5\n\t" \
    "global_load_lds_dword v22, s[36:37] sc1\n\t" \
    "s_sleep 2\n\t" \
    "s_nop 5\n\t" \
    "global_load_lds_dword v21, s[36:37] sc1\n\t" \
    "s_sleep 2\n\t" \
    "s_nop 5\n\t" \
    "global_load_lds_dword v20, s[36:37] sc1\n\t" \
    "s_branch hh_loop8_%=\n\t" \
    "hh_end10_%=:\n\t" \
    "s_waitcnt vmcnt(0)\n\t" \
    "s_endpgm\n\t" \
    "hh_last_%=:\n\t" \
    ""
#define ASM_GX \
    "s_load_dwordx2 s[42:43], %3, 48\n\t" \
    "s_load_dwordx2 s[44:45], %3, 40\n\t" \
    "s_load_dwordx2 s[36:37], %3, 0x58\n\t" \
    "s_load_dwordx2 s[50:51], %3, 0x48\n\t" \
    "s_mov_b32 s66, %4\n\t" \
    "s_mov_b32 s67, %5\n\t" \
    "s_mov_b32 s68, 262144\n\t" \
    "s_mov_b32 s72, 0\n\t" \
    "s_mov_b32 s83, -1\n\t" \
    "s_mov_b32 s65, 0\n\t" \
    "s_lshl_b32 s82, s66, 10\n\t" \
    "s_waitcnt lgkmcnt(0)\n\t" \
    "s_mul_i32 s89, s66, 9216\n\t" \
    "s_mov_b32 s90, 0\n\t" \
    "s_add_u32 s46, s42, 2097152\n\t" \
    "s_addc_u32 s47, s43, 0\n\t" \
    "s_add_u32 m0, s82, 65536\n\t" \
    "s_nop 0\n\t" \
    "global_load_lds_dwordx4 v200, s[46:47]\n\t" \
    "s_add_u32 s46, s42, 2228224\n\t" \
    "s_addc_u32 s47, s43, 0\n\t" \
    "s_add_u32 m0, s82, 69632\n\t" \
    "s_nop 0\n\t" \
    "global_load_lds_dwordx4 v200, s[46:47]\n\t" \
    "s_add_u32 s46, s42, 2359296\n\t" \
    "s_addc_u32 s47, s43, 0\n\t" \
    "s_add_u32 m0, s82, 73728\n\t" \
    "s_nop 0\n\t" \
    "global_load_lds_dwordx4 v200, s[46:47]\n\t" \
    "s_add_u32 s46, s42, 2490368\n\t" \
    "s_addc_u32 s47, s43, 0\n\t" \
    "s_add_u32 m0, s82, 77824\n\t" \
    "s_nop 0\n\t" \
    "global_load_lds_dwordx4 v200, s[46:47]\n\t" \
    "s_add_u32 s46, s42, 2621440\n\t" \
    "s_addc_u32 s47, s43, 0\n\t" \
    "s_add_u32 m0, s82, 81920\n\t" \
    "s_nop 0\n\t" \
    "global_load_lds_dwordx4 v200, s[46:47]\n\t" \
    "s_add_u32 s46, s42, 2752512\n\t" \
    "s_addc_u32 s47, s43, 0\n\t" \
    "s_add_u32 m0, s82, 86016\n\t" \
    "s_nop 0\n\t" \
    "global_load_lds_dwordx4 v200, s[46:47]\n\t" \
    "s_add_u32 s46, s42, 2883584\n\t" \
    "s_addc_u32 s47, s43, 0\n\t" \
    "s_add_u32 m0, s82, 90112\n\t" \
    "s_nop 0\n\t" \
    "global_load_lds_dwordx4 v200, s[46:47]\n\t" \
    "s_add_u32 s46, s42, 3014656\n\t" \
    "s_addc_u32 s47, s43, 0\n\t" \
    "s_add_u32 m0, s82, 94208\n\t" \
    "s_nop 0\n\t" \
    "global_load_lds_dwordx4 v200, s[46:47]\n\t" \
    "s_add_u32 s46, s42, 3145728\n\t" \
    "s_addc_u32 s47, s43, 0\n\t" \
    "s_add_u32 m0, s82, 98304\n\t" \
    "s_nop 0\n\t" \
    "global_load_lds_dwordx4 v200, s[46:47]\n\t" \
    "s_add_u32 s46, s42, 3276800\n\t" \
    "s_addc_u32 s47, s43, 0\n\t" \
    "s_add_u32 m0, s82, 102400\n\t" \
    "s_nop 0\n\t" \
    "global_load_lds_dwordx4 v200, s[46:47]\n\t" \
    "s_add_u32 s46, s42, 3407872\n\t" \
    "s_addc_u32 s47, s43, 0\n\t" \
    "s_add_u32 m0, s82, 106496\n\t" \
    "s_nop 0\n\t" \
    "global_load_lds_dwordx4 v200, s[46:47]\n\t" \
    "s_add_u32 s46, s42, 3538944\n\t" \
    "s_addc_u32 s47, s43, 0\n\t" \
    "s_add_u32 m0, s82, 110592\n\t" \
    "s_nop 0\n\t" \
    "global_load_lds_dwordx4 v200, s[46:47]\n\t" \
    "s_add_u32 s46, s42, 3670016\n\t" \
    "s_addc_u32 s47, s43, 0\n\t" \
    "s_add_u32 m0, s82, 114688\n\t" \
    "s_nop 0\n\t" \
    "global_load_lds_dwordx4 v200, s[46:47]\n\t" \
    "s_add_u32 s46, s42, 3801088\n\t" \
    "s_addc_u32 s47, s43, 0\n\t" \
    "s_add_u32 m0, s82, 118784\n\t" \
    "s_nop 0\n\t" \
    "global_load_lds_dwordx4 v200, s[46:47]\n\t" \
    "s_add_u32 s46, s42, 3932160\n\t" \
    "s_addc_u32 s47, s43, 0\n\t" \
    "s_add_u32 m0, s82, 122880\n\t" \
    "s_nop 0\n\t" \
    "global_load_lds_dwordx4 v200, s[46:47]\n\t" \
    "s_add_u32 s46, s42, 4063232\n\t" \
    "s_addc_u32 s47, s43, 0\n\t" \
    "s_add_u32 m0, s82, 126976\n\t" \
    "s_nop 0\n\t" \
    "global_load_lds_dwordx4 v200, s[46:47]\n\t" \
    "s_add_u32 s46, s42, 4194304\n\t" \
    "s_addc_u32 s47, s43, 0\n\t" \
    "s_add_u32 m0, s82, 131072\n\t" \
    "s_nop 0\n\t" \
    "global_load_lds_dwordx4 v200, s[46:47]\n\t" \
    "s_add_u32 s46, s42, 4325376\n\t" \
    "s_addc_u32 s47, s43, 0\n\t" \
    "s_add_u32 m0, s82, 135168\n\t" \
    "s_nop 0\n\t" \
    "global_load_lds_dwordx4 v200, s[46:47]\n\t" \
    "s_add_u32 s46, s42, 4456448\n\t" \
    "s_addc_u32 s47, s43, 0\n\t" \
    "s_add_u32 m0, s82, 139264\n\t" \
    "s_nop 0\n\t" \
    "global_load_lds_dwordx4 v200, s[46:47]\n\t" \
    "s_add_u32 s46, s42, 4587520\n\t" \
    "s_addc_u32 s47, s43, 0\n\t" \
    "s_add_u32 m0, s82, 143360\n\t" \
    "s_nop 0\n\t" \
    "global_load_lds_dwordx4 v200, s[46:47]\n\t" \
    "global_load_dwordx4 v[120:123], v203, s[50:51]\n\t" \
    "global_load_dwordx4 v[124:127], v204, s[50:51]\n\t" \
    "global_load_dwordx4 v[128:131], v205, s[50:51]\n\t" \
    "global_load_dwordx4 v[132:135], v206, s[50:51]\n\t" \
    "global_load_dwordx4 v[136:139], v207, s[50:51]\n\t" \
    "global_load_dwordx4 v[140:143], v208, s[50:51]\n\t" \
    "global_load_dwordx4 v[144:147], v209, s[50:51]\n\t" \
    "global_load_dwordx4 v[148:151], v210, s[50:51]\n\t" \
    "global_load_dwordx4 v[152:155], v211, s[50:51]\n\t" \
    "s_add_u32 s52, s50, 36864\n\t" \
    "s_addc_u32 s53, s51, 0\n\t" \
    "global_load_dwordx4 v[72:75], v203, s[52:53]\n\t" \
    "global_load_dwordx4 v[76:79], v204, s[52:53]\n\t" \
    "global_load_dwordx4 v[80:83], v205, s[52:53]\n\t" \
    "global_load_dwordx4 v[84:87], v206, s[52:53]\n\t" \
    "global_load_dwordx4 v[88:91], v207, s[52:53]\n\t" \
    "global_load_dwordx4 v[92:95], v208, s[52:53]\n\t" \
    "global_load_dwordx4 v[96:99], v209, s[52:53]\n\t" \
    "global_load_dwordx4 v[100:103], v210, s[52:53]\n\t" \
    "global_load_dwordx4 v[104:107], v211, s[52:53]\n\t" \
    "s_waitcnt vmcnt(50) lgkmcnt(0)\n\t" \
    "s_barrier\n\t" \
    "ds_read_b64_tr_b16 a[144:145], v201 offset:0\n\t" \
    "ds_read_b64_tr_b16 a[146:147], v201 offset:1024\n\t" \
    "ds_read_b64_tr_b16 a[148:149], v201 offset:8192\n\t" \
    "ds_read_b64_tr_b16 a[150:151], v201 offset:9216\n\t" \
    "ds_read_b64_tr_b16 a[216:217], v202 offset:0\n\t" \
    "ds_read_b64_tr_b16 a[218:219], v202 offset:1024\n\t" \
    "ds_read_b64_tr_b16 a[220:221], v202 offset:8192\n\t" \
    "ds_read_b64_tr_b16 a[222:223], v202 offset:9216\n\t" \
    "s_waitcnt vmcnt(46) lgkmcnt(0)\n\t" \
    "s_barrier\n\t" \
    "ds_read_b64_tr_b16 a[152:153], v201 offset:16384\n\t" \
    "ds_read_b64_tr_b16 a[154:155], v201 offset:17408\n\t" \
    "ds_read_b64_tr_b16 a[156:157], v201 offset:24576\n\t" \
    "ds_read_b64_tr_b16 a[158:159], v201 offset:25600\n\t" \
    "ds_read_b64_tr_b16 a[224:225], v202 offset:16384\n\t" \
    "ds_read_b64_tr_b16 a[226:227], v202 offset:17408\n\t" \
    "ds_read_b64_tr_b16 a[228:229], v202 offset:24576\n\t" \
    "ds_read_b64_tr_b16 a[230:231], v202 offset:25600\n\t" \
    "v_mov_b32 v156, 3906560\n\t" \
    "s_mov_b32 s69, 6238208\n\t" \
    "v_writelane_b32 v156, s69, 1\n\t" \
    "s_mov_b32 s69, 175112192\n\t" \
    "v_writelane_b32 v156, s69, 2\n\t" \
    "s_mov_b32 m0, 155648\n\t" \
    "s_nop 0\n\t" \
    "global_load_lds_dword v156, s[36:37]\n\t" \
    "s_lshl_b32 s70, s66, 8\n\t" \
    "s_mul_i32 s69, s67, 524288\n\t" \
    "s_add_u32 s69, s69, 23068672\n\t" \
    "s_add_u32 s69, s69, s70\n\t" \
    "v_writelane_b32 v156, s69, 0\n\t" \
    "s_mul_i32 s69, s67, 524288\n\t" \
    "s_add_u32 s69, s69, 6291456\n\t" \
    "s_add_u32 s69, s69, s70\n\t" \
    "v_writelane_b32 v156, s69, 1\n\t" \
    "v_mov_b32 v157, 0\n\t" \
    "s_mov_b64 exec, 3\n\t" \
    "global_atomic_or v156, v157, s[36:37] sc1\n\t" \
    "s_mov_b64 exec, -1\n\t" \
    "s_waitcnt vmcnt(44) lgkmcnt(0)\n\t" \
    "s_barrier\n\t" \
    "ds_read_b64_tr_b16 a[160:161], v201 offset:32768\n\t" \
    "ds_read_b64_tr_b16 a[162:163], v201 offset:33792\n\t" \
    "ds_read_b64_tr_b16 a[164:165], v201 offset:40960\n\t" \
    "ds_read_b64_tr_b16 a[166:167], v201 offset:41984\n\t" \
    "ds_read_b64_tr_b16 a[232:233], v202 offset:32768\n\t" \
    "ds_read_b64_tr_b16 a[234:235], v202 offset:33792\n\t" \
    "ds_read_b64_tr_b16 a[236:237], v202 offset:40960\n\t" \
    "ds_read_b64_tr_b16 a[238:239], v202 offset:41984\n\t" \
    "s_waitcnt vmcnt(40) lgkmcnt(0)\n\t" \
    "s_barrier\n\t" \
    "ds_read_b64_tr_b16 a[168:169], v201 offset:49152\n\t" \
    "ds_read_b64_tr_b16 a[170:171], v201 offset:50176\n\t" \
    "ds_read_b64_tr_b16 a[172:173], v201 offset:57344\n\t" \
    "ds_read_b64_tr_b16 a[174:175], v201 offset:58368\n\t" \
    "ds_read_b64_tr_b16 a[240:241], v202 offset:49152\n\t" \
    "ds_read_b64_tr_b16 a[242:243], v202 offset:50176\n\t" \
    "ds_read_b64_tr_b16 a[244:245], v202 offset:57344\n\t" \
    "ds_read_b64_tr_b16 a[246:247], v202 offset:58368\n\t" \
    "v_mbcnt_lo_u32_b32 v164, -1, 0\n\t" \
    "v_mbcnt_hi_u32_b32 v164, -1, v164\n\t" \
    "v_lshlrev_b32 v164, 6, v164\n\t" \
    "v_add_u32 v164, 6225920, v164\n\t" \
    "v_mov_b32 v165, 6234112\n\t" \
    "global_load_dword v165, v165, s[36:37] sc1\n\t" \
    "global_load_dwordx2 v[166:167], v164, s[36:37] sc1\n\t" \
    "global_load_dword v168, v164, s[36:37] offset:2048 sc1\n\t" \
    "s_waitcnt vmcnt(39) lgkmcnt(0)\n\t" \
    "s_barrier\n\t" \
    "v_add_u32 v201, 0x10000, v201\n\t" \
    "v_add_u32 v202, 0x10000, v202\n\t" \
    "ds_read_b64_tr_b16 a[176:177], v201 offset:0\n\t" \
    "ds_read_b64_tr_b16 a[178:179], v201 offset:1024\n\t" \
    "ds_read_b64_tr_b16 a[180:181], v201 offset:8192\n\t" \
    "ds_read_b64_tr_b16 a[182:183], v201 offset:9216\n\t" \
    "ds_read_b64_tr_b16 a[248:249], v202 offset:0\n\t" \
    "ds_read_b64_tr_b16 a[250:251], v202 offset:1024\n\t" \
    "ds_read_b64_tr_b16 a[252:253], v202 offset:8192\n\t" \
    "ds_read_b64_tr_b16 a[254:255], v202 offset:9216\n\t" \
    "s_waitcnt vmcnt(35) lgkmcnt(0)\n\t" \
    "s_barrier\n\t" \
    "ds_read_b64_tr_b16 a[184:185], v201 offset:16384\n\t" \
    "ds_read_b64_tr_b16 a[186:187], v201 offset:17408\n\t" \
    "ds_read_b64_tr_b16 a[188:189], v201 offset:24576\n\t" \
    "ds_read_b64_tr_b16 a[190:191], v201 offset:25600\n\t" \
    "ds_read_b64_tr_b16 v[224:225], v202 offset:16384\n\t" \
    "ds_read_b64_tr_b16 v[226:227], v202 offset:17408\n\t" \
    "ds_read_b64_tr_b16 v[228:229], v202 offset:24576\n\t" \
    "ds_read_b64_tr_b16 v[230:231], v202 offset:25600\n\t" \
    "s_waitcnt vmcnt(31) lgkmcnt(0)\n\t" \
    "s_barrier\n\t" \
    "ds_read_b64_tr_b16 a[192:193], v201 offset:32768\n\t" \
    "ds_read_b64_tr_b16 a[194:195], v201 offset:33792\n\t" \
    "ds_read_b64_tr_b16 a[196:197], v201 offset:40960\n\t" \
    "ds_read_b64_tr_b16 a[198:199], v201 offset:41984\n\t" \
    "ds_read_b64_tr_b16 v[232:233], v202 offset:32768\n\t" \
    "ds_read_b64_tr_b16 v[234:235], v202 offset:33792\n\t" \
    "ds_read_b64_tr_b16 v[236:237], v202 offset:40960\n\t" \
    "ds_read_b64_tr_b16 v[238:239], v202 offset:41984\n\t" \
    "s_waitcnt vmcnt(27) lgkmcnt(0)\n\t" \
    "s_barrier\n\t" \
    "ds_read_b64_tr_b16 a[200:201], v201 offset:49152\n\t" \
    "ds_read_b64_tr_b16 a[202:203], v201 offset:50176\n\t" \
    "ds_read_b64_tr_b16 a[204:205], v201 offset:57344\n\t" \
    "ds_read_b64_tr_b16 a[206:207], v201 offset:58368\n\t" \
    "ds_read_b64_tr_b16 v[240:241], v202 offset:49152\n\t" \
    "ds_read_b64_tr_b16 v[242:243], v202 offset:50176\n\t" \
    "ds_read_b64_tr_b16 v[244:245], v202 offset:57344\n\t" \
    "ds_read_b64_tr_b16 v[246:247], v202 offset:58368\n\t" \
    "s_waitcnt vmcnt(23) lgkmcnt(0)\n\t" \
    "s_barrier\n\t" \
    "v_add_u32 v201, 0x10000, v201\n\t" \
    "v_add_u32 v202, 0x10000, v202\n\t" \
    "ds_read_b64_tr_b16 a[208:209], v201 offset:0\n\t" \
    "ds_read_b64_tr_b16 a[210:211], v201 offset:1024\n\t" \
    "ds_read_b64_tr_b16 a[212:213], v201 offset:8192\n\t" \
    "ds_read_b64_tr_b16 a[214:215], v201 offset:9216\n\t" \
    "ds_read_b64_tr_b16 v[248:249], v202 offset:0\n\t" \
    "ds_read_b64_tr_b16 v[250:251], v202 offset:1024\n\t" \
    "ds_read_b64_tr_b16 v[252:253], v202 offset:8192\n\t" \
    "ds_read_b64_tr_b16 v[254:255], v202 offset:9216\n\t" \
    "v_add_u32 v201, 0xfffe0000, v201\n\t" \
    "v_add_u32 v202, 0xfffe0000, v202\n\t" \
    "s_waitcnt lgkmcnt(0)\n\t" \
    "s_barrier\n\t" \
    "s_waitcnt vmcnt(0)\n\t" \
    "ds_write_b128 v3, v[120:123] offset:0\n\t" \
    "ds_write_b128 v3, v[124:127] offset:1024\n\t" \
    "ds_write_b128 v3, v[128:131] offset:2048\n\t" \
    "ds_write_b128 v3, v[132:135] offset:3072\n\t" \
    "ds_write_b128 v3, v[136:139] offset:4096\n\t" \
    "ds_write_b128 v3, v[140:143] offset:5120\n\t" \
    "ds_write_b128 v3, v[144:147] offset:6144\n\t" \
    "ds_write_b128 v3, v[148:151] offset:7168\n\t" \
    "ds_write_b128 v3, v[152:155] offset:8192\n\t" \
    "ds_write_b128 v3, v[72:75] offset:36864\n\t" \
    "ds_write_b128 v3, v[76:79] offset:37888\n\t" \
    "ds_write_b128 v3, v[80:83] offset:38912\n\t" \
    "ds_write_b128 v3, v[84:87] offset:39936\n\t" \
    "ds_write_b128 v3, v[88:91] offset:40960\n\t" \
    "ds_write_b128 v3, v[92:95] offset:41984\n\t" \
    "ds_write_b128 v3, v[96:99] offset:43008\n\t" \
    "ds_write_b128 v3, v[100:103] offset:44032\n\t" \
    "ds_write_b128 v3, v[104:107] offset:45056\n\t" \
    "gx_C1_%=:\n\t" \
    "v_cmp_ne_u32 s[54:55], v166, v168\n\t" \
    "v_sub_u32 v166, v166, v165\n\t" \
    "v_cmp_ne_u32 vcc, 1, v166\n\t" \
    "s_nop 1\n\t" \
    "s_or_b64 vcc, vcc, s[54:55]\n\t" \
    "s_cbranch_scc0 gx_Cok2_%=\n\t" \
    "s_cmp_eq_u32 s68, 0\n\t" \
    "s_cbranch_scc1 gx_F4_%=\n\t" \
    "s_sub_u32 s68, s68, 1\n\t" \
    "s_sleep 6\n\t" \
    "s_branch gx_Cag3_%=\n\t" \
    "gx_F4_%=:\n\t" \
    "v_mov_b32 v168, 5767424\n\t" \
    "v_mov_b32 v169, 4\n\t" \
    "global_store_dword v168, v169, s[36:37] sc1\n\t" \
    "s_waitcnt vmcnt(0)\n\t" \
    "s_branch gx_Cok2_%=\n\t" \
    "gx_Cag3_%=:\n\t" \
    "global_load_dwordx2 v[166:167], v164, s[36:37] sc1\n\t" \
    "global_load_dword v168, v164, s[36:37] offset:2048 sc1\n\t" \
    "s_waitcnt vmcnt(0)\n\t" \
    "s_branch gx_C1_%=\n\t" \
    "gx_Cok2_%=:\n\t" \
    "v_readlane_b32 s69, v167, 2\n\t" \
    "v_readlane_b32 s70, v167, 3\n\t" \
    "s_nop 1\n\t" \
    "s_cmp_lt_u32 s70, s69\n\t" \
    "s_cselect_b32 s69, 0, 8192\n\t" \
    "s_add_u32 s100, s36, s69\n\t" \
    "s_addc_u32 s101, s37, 0\n\t" \
    "s_cmp_lg_u32 s66, 0\n\t" \
    "s_cbranch_scc1 gx_Ca5_%=\n\t" \
    "v_mov_b32 v164, 5768192\n\t" \
    "s_mov_b64 exec, 1\n\t" \
    "global_atomic_add v164, v166, s[36:37] sc1\n\t" \
    "s_mov_b64 exec, -1\n\t" \
    "gx_Ca5_%=:\n\t" \
    "s_waitcnt lgkmcnt(0)\n\t" \
    "s_barrier\n\t" \
    "ds_read_b128 v[24:27], v1 offset:0\n\t" \
    "ds_read_b128 v[28:31], v1 offset:18432\n\t" \
    "ds_read_b128 v[32:35], v2 offset:0\n\t" \
    "ds_read_b128 v[36:39], v2 offset:18432\n\t" \
    "ds_read_b128 v[40:43], v1 offset:128\n\t" \
    "ds_read_b128 v[44:47], v1 offset:18560\n\t" \
    "s_add_u32 s91, s89, 0x12000\n\t" \
    "s_add_u32 s85, s91, s89\n\t" \
    "s_add_u32 s52, s50, 73728\n\t" \
    "s_addc_u32 s53, s51, 0\n\t" \
    "s_mul_i32 s69, s67, 8192\n\t" \
    "s_add_u32 s69, s69, 175112192\n\t" \
    "s_add_u32 s42, s36, s69\n\t" \
    "s_addc_u32 s43, s37, 0\n\t" \
    "s_mov_b64 s[40:41], s[42:43]\n\t" \
    "gx_l16_%=:\n\t" \
    "s_waitcnt lgkmcnt(5)\n\t" \
    "v_mfma_f32_16x16x32_bf16 v[88:91], a[144:147], v[24:27], 0\n\t" \
    "ds_read_b128 v[48:51], v2 offset:128\n\t" \
    "s_waitcnt lgkmcnt(5)\n\t" \
    "v_mfma_f32_16x16x32_bf16 v[92:95], a[144:147], v[28:31], 0\n\t" \
    "ds_read_b128 v[52:55], v2 offset:18560\n\t" \
    "v_mfma_f32_16x16x32_bf16 v[96:99], a[216:219], v[24:27], 0\n\t" \
    "s_add_u32 m0, s91, 0\n\t" \
    "v_mfma_f32_16x16x32_bf16 v[100:103], a[216:219], v[28:31], 0\n\t" \
    "global_load_lds_dwordx4 v203, s[52:53]\n\t" \
    "s_waitcnt lgkmcnt(5)\n\t" \
    "v_mfma_f32_16x16x32_bf16 v[88:91], a[148:151], v[32:35], v[88:91]\n\t" \
    "ds_read_b128 v[56:59], v1 offset:256\n\t" \
    "s_waitcnt lgkmcnt(5)\n\t" \
    "v_mfma_f32_16x16x32_bf16 v[92:95], a[148:151], v[36:39], v[92:95]\n\t" \
    "ds_read_b128 v[60:63], v1 offset:18688\n\t" \
    "v_mfma_f32_16x16x32_bf16 v[96:99], a[220:223], v[32:35], v[96:99]\n\t" \
    "s_add_u32 m0, s91, 1024\n\t" \
    "v_mfma_f32_16x16x32_bf16 v[100:103], a[220:223], v[36:39], v[100:103]\n\t" \
    "global_load_lds_dwordx4 v204, s[52:53]\n\t" \
    "s_waitcnt lgkmcnt(5)\n\t" \
    "v_mfma_f32_16x16x32_bf16 v[88:91], a[152:155], v[40:43], v[88:91]\n\t" \
    "ds_read_b128 v[64:67], v2 offset:256\n\t" \
    "s_waitcnt lgkmcnt(5)\n\t" \
    "v_mfma_f32_16x16x32_bf16 v[92:95], a[152:155], v[44:47], v[92:95]\n\t" \
    "ds_read_b128 v[68:71], v2 offset:18688\n\t" \
    "v_cvt_pk_bf16_f32 v104, v72, v73\n\t" \
    "v_mfma_f32_16x16x32_bf16 v[96:99], a[224:227], v[40:43], v[96:99]\n\t" \
    "s_add_u32 m0, s91, 2048\n\t" \
    "v_cvt_pk_bf16_f32 v105, v74, v75\n\t" \
    "v_mfma_f32_16x16x32_bf16 v[100:103], a[224:227], v[44:47], v[100:103]\n\t" \
    "global_load_lds_dwordx4 v205, s[52:53]\n\t" \
    "s_waitcnt lgkmcnt(5)\n\t" \
    "v_mfma_f32_16x16x32_bf16 v[88:91], a[156:159], v[48:51], v[88:91]\n\t" \
    "ds_read_b128 v[24:27], v1 offset:384\n\t" \
    "s_waitcnt lgkmcnt(5)\n\t" \
    "v_mfma_f32_16x16x32_bf16 v[92:95], a[156:159], v[52:55], v[92:95]\n\t" \
    "ds_read_b128 v[28:31], v1 offset:18816\n\t" \
    "v_cvt_pk_bf16_f32 v106, v80, v81\n\t" \
    "v_mfma_f32_16x16x32_bf16 v[96:99], a[228:231], v[48:51], v[96:99]\n\t" \
    "s_add_u32 m0, s91, 3072\n\t" \
    "v_cvt_pk_bf16_f32 v107, v82, v83\n\t" \
    "v_mfma_f32_16x16x32_bf16 v[100:103], a[228:231], v[52:55], v[100:103]\n\t" \
    "global_load_lds_dwordx4 v206, s[52:53]\n\t" \
    "s_waitcnt lgkmcnt(5)\n\t" \
    "v_mfma_f32_16x16x32_bf16 v[88:91], a[160:163], v[56:59], v[88:91]\n\t" \
    "ds_read_b128 v[32:35], v2 offset:384\n\t" \
    "s_waitcnt lgkmcnt(5)\n\t" \
    "v_mfma_f32_16x16x32_bf16 v[92:95], a[160:163], v[60:63], v[92:95]\n\t" \
    "ds_read_b128 v[36:39], v2 offset:18816\n\t" \
    "global_store_dwordx4 v8, v[104:107], s[40:41] offset:0 sc1\n\t" \
    "v_mfma_f32_16x16x32_bf16 v[96:99], a[232:235], v[56:59], v[96:99]\n\t" \
    "s_add_u32 m0, s91, 4096\n\t" \
    "v_cvt_pk_bf16_f32 v108, v76, v77\n\t" \
    "v_mfma_f32_16x16x32_bf16 v[100:103], a[232:235], v[60:63], v[100:103]\n\t" \
    "global_load_lds_dwordx4 v207, s[52:53]\n\t" \
    "s_waitcnt lgkmcnt(5)\n\t" \
    "v_mfma_f32_16x16x32_bf16 v[88:91], a[164:167], v[64:67], v[88:91]\n\t" \
    "ds_read_b128 v[40:43], v1 offset:512\n\t" \
    "s_waitcnt lgkmcnt(5)\n\t" \
    "v_mfma_f32_16x16x32_bf16 v[92:95], a[164:167], v[68:71], v[92:95]\n\t" \
    "ds_read_b128 v[44:47], v1 offset:18944\n\t" \
    "v_cvt_pk_bf16_f32 v109, v78, v79\n\t" \
    "v_mfma_f32_16x16x32_bf16 v[96:99], a[236:239], v[64:67], v[96:99]\n\t" \
    "s_add_u32 m0, s91, 5120\n\t" \
    "v_cvt_pk_bf16_f32 v110, v84, v85\n\t" \
    "v_mfma_f32_16x16x32_bf16 v[100:103], a[236:239], v[68:71], v[100:103]\n\t" \
    "global_load_lds_dwordx4 v208, s[52:53]\n\t" \
    "s_waitcnt lgkmcnt(5)\n\t" \
    "v_mfma_f32_16x16x32_bf16 v[88:91], a[168:171], v[24:27], v[88:91]\n\t" \
    "ds_read_b128 v[48:51], v2 offset:512\n\t" \
    "s_waitcnt lgkmcnt(5)\n\t" \
    "v_mfma_f32_16x16x32_bf16 v[92:95], a[168:171], v[28:31], v[92:95]\n\t" \
    "ds_read_b128 v[52:55], v2 offset:18944\n\t" \
    "v_cvt_pk_bf16_f32 v111, v86, v87\n\t" \
    "v_mfma_f32_16x16x32_bf16 v[96:99], a[240:243], v[24:27], v[96:99]\n\t" \
    "s_add_u32 m0, s91, 6144\n\t" \
    "global_store_dwordx4 v8, v[108:111], s[40:41] offset:1024 sc1\n\t" \
    "v_mfma_f32_16x16x32_bf16 v[100:103], a[240:243], v[28:31], v[100:103]\n\t" \
    "global_load_lds_dwordx4 v209, s[52:53]\n\t" \
    "s_waitcnt lgkmcnt(5)\n\t" \
    "v_mfma_f32_16x16x32_bf16 v[88:91], a[172:175], v[32:35], v[88:91]\n\t" \
    "ds_read_b128 v[56:59], v1 offset:640\n\t" \
    "s_waitcnt lgkmcnt(5)\n\t" \
    "v_mfma_f32_16x16x32_bf16 v[92:95], a[172:175], v[36:39], v[92:95]\n\t" \
    "ds_read_b128 v[60:63], v1 offset:19072\n\t" \
    "v_mfma_f32_16x16x32_bf16 v[96:99], a[244:247], v[32:35], v[96:99]\n\t" \
    "s_add_u32 m0, s91, 7168\n\t" \
    "v_mfma_f32_16x16x32_bf16 v[100:103], a[244:247], v[36:39], v[100:103]\n\t" \
    "global_load_lds_dwordx4 v210, s[52:53]\n\t" \
    "s_waitcnt lgkmcnt(5)\n\t" \
    "v_mfma_f32_16x16x32_bf16 v[88:91], a[176:179], v[40:43], v[88:91]\n\t" \
    "ds_read_b128 v[64:67], v2 offset:640\n\t" \
    "s_waitcnt lgkmcnt(5)\n\t" \
    "v_mfma_f32_16x16x32_bf16 v[92:95], a[176:179], v[44:47], v[92:95]\n\t" \
    "ds_read_b128 v[68:71], v2 offset:19072\n\t" \
    "v_mfma_f32_16x16x32_bf16 v[96:99], a[248:251], v[40:43], v[96:99]\n\t" \
    "s_add_u32 m0, s91, 8192\n\t" \
    "v_mfma_f32_16x16x32_bf16 v[100:103], a[248:251], v[44:47], v[100:103]\n\t" \
    "global_load_lds_dwordx4 v211, s[52:53]\n\t" \
    "s_waitcnt lgkmcnt(5)\n\t" \
    "v_mfma_f32_16x16x32_bf16 v[88:91], a[180:183], v[48:51], v[88:91]\n\t" \
    "ds_read_b128 v[24:27], v1 offset:768\n\t" \
    "s_waitcnt lgkmcnt(5)\n\t" \
    "v_mfma_f32_16x16x32_bf16 v[92:95], a[180:183], v[52:55], v[92:95]\n\t" \
    "ds_read_b128 v[28:31], v1 offset:19200\n\t" \
    "v_mfma_f32_16x16x32_bf16 v[96:99], a[252:255], v[48:51], v[96:99]\n\t" \
    "v_mfma_f32_16x16x32_bf16 v[100:103], a[252:255], v[52:55], v[100:103]\n\t" \
    "s_waitcnt lgkmcnt(5)\n\t" \
    "v_mfma_f32_16x16x32_bf16 v[88:91], a[184:187], v[56:59], v[88:91]\n\t" \
    "ds_read_b128 v[32:35], v2 offset:768\n\t" \
    "s_waitcnt lgkmcnt(5)\n\t" \
    "v_mfma_f32_16x16x32_bf16 v[92:95], a[184:187], v[60:63], v[92:95]\n\t" \
    "ds_read_b128 v[36:39], v2 offset:19200\n\t" \
    "v_mfma_f32_16x16x32_bf16 v[96:99], v[224:227], v[56:59], v[96:99]\n\t" \
    "s_waitcnt vmcnt(11)\n\t" \
    "s_barrier\n\t" \
    "v_mfma_f32_16x16x32_bf16 v[100:103], v[224:227], v[60:63], v[100:103]\n\t" \
    "s_cmp_lg_u32 s66, 0\n\t" \
    "s_cbranch_scc1 gx_SF8_%=\n\t" \
    "s_lshl_b32 s69, s67, 7\n\t" \
    "s_add_u32 s69, s69, 3149824\n\t" \
    "s_add_u32 s70, s65, -2\n\t" \
    "v_mov_b32 v156, s69\n\t" \
    "v_mov_b32 v157, s70\n\t" \
    "s_mov_b64 exec, 1\n\t" \
    "global_store_dword v156, v157, s[100:101] sc1\n\t" \
    "s_mov_b64 exec, -1\n\t" \
    "gx_SF8_%=:\n\t" \
    "s_waitcnt lgkmcnt(5)\n\t" \
    "v_mfma_f32_16x16x32_bf16 v[88:91], a[188:191], v[64:67], v[88:91]\n\t" \
    "ds_read_b128 v[40:43], v1 offset:896\n\t" \
    "s_waitcnt lgkmcnt(5)\n\t" \
    "v_mfma_f32_16x16x32_bf16 v[92:95], a[188:191], v[68:71], v[92:95]\n\t" \
    "ds_read_b128 v[44:47], v1 offset:19328\n\t" \
    "v_mfma_f32_16x16x32_bf16 v[96:99], v[228:231], v[64:67], v[96:99]\n\t" \
    "v_add_u32 v22, 0x9000, v1\n\t" \
    "v_add_u32 v23, 0x9000, v2\n\t" \
    "v_mfma_f32_16x16x32_bf16 v[100:103], v[228:231], v[68:71], v[100:103]\n\t" \
    "s_add_u32 s48, s52, 36864\n\t" \
    "s_addc_u32 s49, s53, 0\n\t" \
    "s_waitcnt lgkmcnt(5)\n\t" \
    "v_mfma_f32_16x16x32_bf16 v[88:91], a[192:195], v[24:27], v[88:91]\n\t" \
    "ds_read_b128 v[48:51], v2 offset:896\n\t" \
    "s_waitcnt lgkmcnt(5)\n\t" \
    "v_mfma_f32_16x16x32_bf16 v[92:95], a[192:195], v[28:31], v[92:95]\n\t" \
    "ds_read_b128 v[52:55], v2 offset:19328\n\t" \
    "v_mfma_f32_16x16x32_bf16 v[96:99], v[232:235], v[24:27], v[96:99]\n\t" \
    "s_mul_i32 s70, s67, 7\n\t" \
    "s_add_u32 s69, s65, 0\n\t" \
    "v_mfma_f32_16x16x32_bf16 v[100:103], v[232:235], v[28:31], v[100:103]\n\t" \
    "s_xor_b32 s69, s69, s70\n\t" \
    "s_and_b32 s69, s69, 63\n\t" \
    "s_waitcnt lgkmcnt(5)\n\t" \
    "v_mfma_f32_16x16x32_bf16 v[88:91], a[196:199], v[32:35], v[88:91]\n\t" \
    "ds_read_b128 v[56:59], v1 offset:1024\n\t" \
    "s_waitcnt lgkmcnt(5)\n\t" \
    "v_mfma_f32_16x16x32_bf16 v[92:95], a[196:199], v[36:39], v[92:95]\n\t" \
    "ds_read_b128 v[60:63], v1 offset:19456\n\t" \
    "v_mfma_f32_16x16x32_bf16 v[96:99], v[236:239], v[32:35], v[96:99]\n\t" \
    "s_lshl_b32 s69, s69, 13\n\t" \
    "s_mul_i32 s70, s67, 524288\n\t" \
    "v_mfma_f32_16x16x32_bf16 v[100:103], v[236:239], v[36:39], v[100:103]\n\t" \
    "s_add_u32 s69, s69, s70\n\t" \
    "s_add_u32 s69, s69, 23068672\n\t" \
    "s_waitcnt lgkmcnt(5)\n\t" \
    "v_mfma_f32_16x16x32_bf16 v[88:91], a[200:203], v[40:43], v[88:91]\n\t" \
    "ds_read_b128 v[64:67], v2 offset:1024\n\t" \
    "s_waitcnt lgkmcnt(5)\n\t" \
    "v_mfma_f32_16x16x32_bf16 v[92:95], a[200:203], v[44:47], v[92:95]\n\t" \
    "ds_read_b128 v[68:71], v2 offset:19456\n\t" \
    "v_mfma_f32_16x16x32_bf16 v[96:99], v[240:243], v[40:43], v[96:99]\n\t" \
    "s_add_u32 s38, s36, s69\n\t" \
    "s_addc_u32 s39, s37, 0\n\t" \
    "v_mfma_f32_16x16x32_bf16 v[100:103], v[240:243], v[44:47], v[100:103]\n\t" \
    "s_waitcnt lgkmcnt(5)\n\t" \
    "v_mfma_f32_16x16x32_bf16 v[88:91], a[204:207], v[48:51], v[88:91]\n\t" \
    "ds_read_b128 v[24:27], v22 offset:0\n\t" \
    "s_waitcnt lgkmcnt(5)\n\t" \
    "v_mfma_f32_16x16x32_bf16 v[92:95], a[204:207], v[52:55], v[92:95]\n\t" \
    "ds_read_b128 v[28:31], v22 offset:18432\n\t" \
    "v_mfma_f32_16x16x32_bf16 v[96:99], v[244:247], v[48:51], v[96:99]\n\t" \
    "v_mfma_f32_16x16x32_bf16 v[100:103], v[244:247], v[52:55], v[100:103]\n\t" \
    "s_waitcnt lgkmcnt(5)\n\t" \
    "v_mfma_f32_16x16x32_bf16 v[88:91], a[208:211], v[56:59], v[88:91]\n\t" \
    "ds_read_b128 v[32:35], v23 offset:0\n\t" \
    "s_waitcnt lgkmcnt(5)\n\t" \
    "v_mfma_f32_16x16x32_bf16 v[92:95], a[208:211], v[60:63], v[92:95]\n\t" \
    "ds_read_b128 v[36:39], v23 offset:18432\n\t" \
    "v_mfma_f32_16x16x32_bf16 v[96:99], v[248:251], v[56:59], v[96:99]\n\t" \
    "v_mfma_f32_16x16x32_bf16 v[100:103], v[248:251], v[60:63], v[100:103]\n\t" \
    "s_waitcnt lgkmcnt(5)\n\t" \
    "v_mfma_f32_16x16x32_bf16 v[88:91], a[212:215], v[64:67], v[88:91]\n\t" \
    "ds_read_b128 v[40:43], v22 offset:128\n\t" \
    "s_waitcnt lgkmcnt(5)\n\t" \
    "v_mfma_f32_16x16x32_bf16 v[92:95], a[212:215], v[68:71], v[92:95]\n\t" \
    "ds_read_b128 v[44:47], v22 offset:18560\n\t" \
    "v_mfma_f32_16x16x32_bf16 v[96:99], v[252:255], v[64:67], v[96:99]\n\t" \
    "v_mfma_f32_16x16x32_bf16 v[100:103], v[252:255], v[68:71], v[100:103]\n\t" \
    "s_cmp_lg_u32 s65, 0\n\t" \
    "s_cbranch_scc1 gx_nf09_%=\n\t" \
    "v_cvt_pk_bf16_f32 v104, v88, v89\n\t" \
    "v_cvt_pk_bf16_f32 v105, v90, v91\n\t" \
    "s_nop 2\n\t" \
    "v_cvt_pk_bf16_f32 v106, v96, v97\n\t" \
    "v_cvt_pk_bf16_f32 v107, v98, v99\n\t" \
    "global_store_dwordx4 v8, v[104:107], s[38:39] offset:0 sc1\n\t" \
    "v_cvt_pk_bf16_f32 v108, v92, v93\n\t" \
    "v_cvt_pk_bf16_f32 v109, v94, v95\n\t" \
    "v_cvt_pk_bf16_f32 v110, v100, v101\n\t" \
    "v_cvt_pk_bf16_f32 v111, v102, v103\n\t" \
    "global_store_dwordx4 v8, v[108:111], s[38:39] offset:1024 sc1\n\t" \
    "s_waitcnt vmcnt(0)\n\t" \
    "s_barrier\n\t" \
    "s_cmp_lg_u32 s66, 0\n\t" \
    "s_cbranch_scc1 gx_SF10_%=\n\t" \
    "s_lshl_b32 s69, s67, 7\n\t" \
    "s_add_u32 s69, s69, 3149824\n\t" \
    "s_add_u32 s70, s65, 0\n\t" \
    "v_mov_b32 v156, s69\n\t" \
    "v_mov_b32 v157, s70\n\t" \
    "s_mov_b64 exec, 1\n\t" \
    "global_store_dword v156, v157, s[100:101] sc1\n\t" \
    "s_mov_b64 exec, -1\n\t" \
    "gx_SF10_%=:\n\t" \
    "gx_nf09_%=:\n\t" \
    "s_waitcnt lgkmcnt(5)\n\t" \
    "v_mfma_f32_16x16x32_bf16 v[72:75], a[144:147], v[24:27], 0\n\t" \
    "ds_read_b128 v[48:51], v23 offset:128\n\t" \
    "s_waitcnt lgkmcnt(5)\n\t" \
    "v_mfma_f32_16x16x32_bf16 v[76:79], a[144:147], v[28:31], 0\n\t" \
    "ds_read_b128 v[52:55], v23 offset:18560\n\t" \
    "v_mfma_f32_16x16x32_bf16 v[80:83], a[216:219], v[24:27], 0\n\t" \
    "s_add_u32 m0, s91, 36864\n\t" \
    "v_mfma_f32_16x16x32_bf16 v[84:87], a[216:219], v[28:31], 0\n\t" \
    "global_load_lds_dwordx4 v203, s[48:49]\n\t" \
    "s_waitcnt lgkmcnt(5)\n\t" \
    "v_mfma_f32_16x16x32_bf16 v[72:75], a[148:151], v[32:35], v[72:75]\n\t" \
    "ds_read_b128 v[56:59], v22 offset:256\n\t" \
    "s_waitcnt lgkmcnt(5)\n\t" \
    "v_mfma_f32_16x16x32_bf16 v[76:79], a[148:151], v[36:39], v[76:79]\n\t" \
    "ds_read_b128 v[60:63], v22 offset:18688\n\t" \
    "v_mfma_f32_16x16x32_bf16 v[80:83], a[220:223], v[32:35], v[80:83]\n\t" \
    "s_add_u32 m0, s91, 37888\n\t" \
    "v_mfma_f32_16x16x32_bf16 v[84:87], a[220:223], v[36:39], v[84:87]\n\t" \
    "global_load_lds_dwordx4 v204, s[48:49]\n\t" \
    "s_waitcnt lgkmcnt(5)\n\t" \
    "v_mfma_f32_16x16x32_bf16 v[72:75], a[152:155], v[40:43], v[72:75]\n\t" \
    "ds_read_b128 v[64:67], v23 offset:256\n\t" \
    "s_waitcnt lgkmcnt(5)\n\t" \
    "v_mfma_f32_16x16x32_bf16 v[76:79], a[152:155], v[44:47], v[76:79]\n\t" \
    "ds_read_b128 v[68:71], v23 offset:18688\n\t" \
    "v_cvt_pk_bf16_f32 v104, v88, v89\n\t" \
    "v_mfma_f32_16x16x32_bf16 v[80:83], a[224:227], v[40:43], v[80:83]\n\t" \
    "s_add_u32 m0, s91, 38912\n\t" \
    "v_cvt_pk_bf16_f32 v105, v90, v91\n\t" \
    "v_mfma_f32_16x16x32_bf16 v[84:87], a[224:227], v[44:47], v[84:87]\n\t" \
    "global_load_lds_dwordx4 v205, s[48:49]\n\t" \
    "s_waitcnt lgkmcnt(5)\n\t" \
    "v_mfma_f32_16x16x32_bf16 v[72:75], a[156:159], v[48:51], v[72:75]\n\t" \
    "ds_read_b128 v[24:27], v22 offset:384\n\t" \
    "s_waitcnt lgkmcnt(5)\n\t" \
    "v_mfma_f32_16x16x32_bf16 v[76:79], a[156:159], v[52:55], v[76:79]\n\t" \
    "ds_read_b128 v[28:31], v22 offset:18816\n\t" \
    "v_cvt_pk_bf16_f32 v106, v96, v97\n\t" \
    "v_mfma_f32_16x16x32_bf16 v[80:83], a[228:231], v[48:51], v[80:83]\n\t" \
    "s_add_u32 m0, s91, 39936\n\t" \
    "v_cvt_pk_bf16_f32 v107, v98, v99\n\t" \
    "v_mfma_f32_16x16x32_bf16 v[84:87], a[228:231], v[52:55], v[84:87]\n\t" \
    "global_load_lds_dwordx4 v206, s[48:49]\n\t" \
    "s_waitcnt lgkmcnt(5)\n\t" \
    "v_mfma_f32_16x16x32_bf16 v[72:75], a[160:163], v[56:59], v[72:75]\n\t" \
    "ds_read_b128 v[32:35], v23 offset:384\n\t" \
    "s_waitcnt lgkmcnt(5)\n\t" \
    "v_mfma_f32_16x16x32_bf16 v[76:79], a[160:163], v[60:63], v[76:79]\n\t" \
    "ds_read_b128 v[36:39], v23 offset:18816\n\t" \
    "global_store_dwordx4 v8, v[104:107], s[38:39] offset:0 sc1\n\t" \
    "v_mfma_f32_16x16x32_bf16 v[80:83], a[232:235], v[56:59], v[80:83]\n\t" \
    "s_add_u32 m0, s91, 40960\n\t" \
    "v_cvt_pk_bf16_f32 v108, v92, v93\n\t" \
    "v_mfma_f32_16x16x32_bf16 v[84:87], a[232:235], v[60:63], v[84:87]\n\t" \
    "global_load_lds_dwordx4 v207, s[48:49]\n\t" \
    "s_waitcnt lgkmcnt(5)\n\t" \
    "v_mfma_f32_16x16x32_bf16 v[72:75], a[164:167], v[64:67], v[72:75]\n\t" \
    "ds_read_b128 v[40:43], v22 offset:512\n\t" \
    "s_waitcnt lgkmcnt(5)\n\t" \
    "v_mfma_f32_16x16x32_bf16 v[76:79], a[164:167], v[68:71], v[76:79]\n\t" \
    "ds_read_b128 v[44:47], v22 offset:18944\n\t" \
    "v_cvt_pk_bf16_f32 v109, v94, v95\n\t" \
    "v_mfma_f32_16x16x32_bf16 v[80:83], a[236:239], v[64:67], v[80:83]\n\t" \
    "s_add_u32 m0, s91, 41984\n\t" \
    "v_cvt_pk_bf16_f32 v110, v100, v101\n\t" \
    "v_mfma_f32_16x16x32_bf16 v[84:87], a[236:239], v[68:71], v[84:87]\n\t" \
    "global_load_lds_dwordx4 v208, s[48:49]\n\t" \
    "s_waitcnt lgkmcnt(5)\n\t" \
    "v_mfma_f32_16x16x32_bf16 v[72:75], a[168:171], v[24:27], v[72:75]\n\t" \
    "ds_read_b128 v[48:51], v23 offset:512\n\t" \
    "s_waitcnt lgkmcnt(5)\n\t" \
    "v_mfma_f32_16x16x32_bf16 v[76:79], a[168:171], v[28:31], v[76:79]\n\t" \
    "ds_read_b128 v[52:55], v23 offset:18944\n\t" \
    "v_cvt_pk_bf16_f32 v111, v102, v103\n\t" \
    "v_mfma_f32_16x16x32_bf16 v[80:83], a[240:243], v[24:27], v[80:83]\n\t" \
    "s_add_u32 m0, s91, 43008\n\t" \
    "global_store_dwordx4 v8, v[108:111], s[38:39] offset:1024 sc1\n\t" \
    "v_mfma_f32_16x16x32_bf16 v[84:87], a[240:243], v[28:31], v[84:87]\n\t" \
    "global_load_lds_dwordx4 v209, s[48:49]\n\t" \
    "s_waitcnt lgkmcnt(5)\n\t" \
    "v_mfma_f32_16x16x32_bf16 v[72:75], a[172:175], v[32:35], v[72:75]\n\t" \
    "ds_read_b128 v[56:59], v22 offset:640\n\t" \
    "s_waitcnt lgkmcnt(5)\n\t" \
    "v_mfma_f32_16x16x32_bf16 v[76:79], a[172:175], v[36:39], v[76:79]\n\t" \
    "ds_read_b128 v[60:63], v22 offset:19072\n\t" \
    "v_mfma_f32_16x16x32_bf16 v[80:83], a[244:247], v[32:35], v[80:83]\n\t" \
    "s_add_u32 m0, s91, 44032\n\t" \
    "v_mfma_f32_16x16x32_bf16 v[84:87], a[244:247], v[36:39], v[84:87]\n\t" \
    "global_load_lds_dwordx4 v210, s[48:49]\n\t" \
    "s_waitcnt lgkmcnt(5)\n\t" \
    "v_mfma_f32_16x16x32_bf16 v[72:75], a[176:179], v[40:43], v[72:75]\n\t" \
    "ds_read_b128 v[64:67], v23 offset:640\n\t" \
    "s_waitcnt lgkmcnt(5)\n\t" \
    "v_mfma_f32_16x16x32_bf16 v[76:79], a[176:179], v[44:47], v[76:79]\n\t" \
    "ds_read_b128 v[68:71], v23 offset:19072\n\t" \
    "v_mfma_f32_16x16x32_bf16 v[80:83], a[248:251], v[40:43], v[80:83]\n\t" \
    "s_add_u32 m0, s91, 45056\n\t" \
    "v_mfma_f32_16x16x32_bf16 v[84:87], a[248:251], v[44:47], v[84:87]\n\t" \
    "global_load_lds_dwordx4 v211, s[48:49]\n\t" \
    "s_waitcnt lgkmcnt(5)\n\t" \
    "v_mfma_f32_16x16x32_bf16 v[72:75], a[180:183], v[48:51], v[72:75]\n\t" \
    "ds_read_b128 v[24:27], v22 offset:768\n\t" \
    "s_waitcnt lgkmcnt(5)\n\t" \
    "v_mfma_f32_16x16x32_bf16 v[76:79], a[180:183], v[52:55], v[76:79]\n\t" \
    "ds_read_b128 v[28:31], v22 offset:19200\n\t" \
    "v_mfma_f32_16x16x32_bf16 v[80:83], a[252:255], v[48:51], v[80:83]\n\t" \
    "v_mfma_f32_16x16x32_bf16 v[84:87], a[252:255], v[52:55], v[84:87]\n\t" \
    "s_waitcnt lgkmcnt(5)\n\t" \
    "v_mfma_f32_16x16x32_bf16 v[72:75], a[184:187], v[56:59], v[72:75]\n\t" \
    "ds_read_b128 v[32:35], v23 offset:768\n\t" \
    "s_waitcnt lgkmcnt(5)\n\t" \
    "v_mfma_f32_16x16x32_bf16 v[76:79], a[184:187], v[60:63], v[76:79]\n\t" \
    "ds_read_b128 v[36:39], v23 offset:19200\n\t" \
    "v_mfma_f32_16x16x32_bf16 v[80:83], v[224:227], v[56:59], v[80:83]\n\t" \
    "s_waitcnt vmcnt(11)\n\t" \
    "s_barrier\n\t" \
    "v_mfma_f32_16x16x32_bf16 v[84:87], v[224:227], v[60:63], v[84:87]\n\t" \
    "s_max_u32 s71, s65, 1\n\t" \
    "s_cmp_lg_u32 s66, 0\n\t" \
    "s_cbranch_scc1 gx_SF11_%=\n\t" \
    "s_lshl_b32 s69, s67, 7\n\t" \
    "s_add_u32 s69, s69, 3149824\n\t" \
    "s_add_u32 s70, s71, -1\n\t" \
    "v_mov_b32 v156, s69\n\t" \
    "v_mov_b32 v157, s70\n\t" \
    "s_mov_b64 exec, 1\n\t" \
    "global_store_dword v156, v157, s[100:101] sc1\n\t" \
    "s_mov_b64 exec, -1\n\t" \
    "gx_SF11_%=:\n\t" \
    "s_waitcnt lgkmcnt(5)\n\t" \
    "v_mfma_f32_16x16x32_bf16 v[72:75], a[188:191], v[64:67], v[72:75]\n\t" \
    "ds_read_b128 v[40:43], v22 offset:896\n\t" \
    "s_waitcnt lgkmcnt(5)\n\t" \
    "v_mfma_f32_16x16x32_bf16 v[76:79], a[188:191], v[68:71], v[76:79]\n\t" \
    "ds_read_b128 v[44:47], v22 offset:19328\n\t" \
    "v_mfma_f32_16x16x32_bf16 v[80:83], v[228:231], v[64:67], v[80:83]\n\t" \
    "s_xor_b32 s90, s90, 0x12000\n\t" \
    "v_add_u32 v1, s90, v0\n\t" \
    "v_mfma_f32_16x16x32_bf16 v[84:87], v[228:231], v[68:71], v[84:87]\n\t" \
    "v_xor_b32 v2, 64, v1\n\t" \
    "s_add_u32 s52, s52, 73728\n\t" \
    "s_waitcnt lgkmcnt(5)\n\t" \
    "v_mfma_f32_16x16x32_bf16 v[72:75], a[192:195], v[24:27], v[72:75]\n\t" \
    "ds_read_b128 v[48:51], v23 offset:896\n\t" \
    "s_waitcnt lgkmcnt(5)\n\t" \
    "v_mfma_f32_16x16x32_bf16 v[76:79], a[192:195], v[28:31], v[76:79]\n\t" \
    "ds_read_b128 v[52:55], v23 offset:19328\n\t" \
    "v_mfma_f32_16x16x32_bf16 v[80:83], v[232:235], v[24:27], v[80:83]\n\t" \
    "s_addc_u32 s53, s53, 0\n\t" \
    "s_mul_i32 s70, s67, 7\n\t" \
    "v_mfma_f32_16x16x32_bf16 v[84:87], v[232:235], v[28:31], v[84:87]\n\t" \
    "s_add_u32 s69, s65, 1\n\t" \
    "s_xor_b32 s69, s69, s70\n\t" \
    "s_waitcnt lgkmcnt(5)\n\t" \
    "v_mfma_f32_16x16x32_bf16 v[72:75], a[196:199], v[32:35], v[72:75]\n\t" \
    "ds_read_b128 v[56:59], v22 offset:1024\n\t" \
    "s_waitcnt lgkmcnt(5)\n\t" \
    "v_mfma_f32_16x16x32_bf16 v[76:79], a[196:199], v[36:39], v[76:79]\n\t" \
    "ds_read_b128 v[60:63], v22 offset:19456\n\t" \
    "v_mfma_f32_16x16x32_bf16 v[80:83], v[236:239], v[32:35], v[80:83]\n\t" \
    "s_and_b32 s69, s69, 63\n\t" \
    "s_lshl_b32 s69, s69, 13\n\t" \
    "v_mfma_f32_16x16x32_bf16 v[84:87], v[236:239], v[36:39], v[84:87]\n\t" \
    "s_mul_i32 s70, s67, 524288\n\t" \
    "s_add_u32 s69, s69, s70\n\t" \
    "s_waitcnt lgkmcnt(5)\n\t" \
    "v_mfma_f32_16x16x32_bf16 v[72:75], a[200:203], v[40:43], v[72:75]\n\t" \
    "ds_read_b128 v[64:67], v23 offset:1024\n\t" \
    "s_waitcnt lgkmcnt(5)\n\t" \
    "v_mfma_f32_16x16x32_bf16 v[76:79], a[200:203], v[44:47], v[76:79]\n\t" \
    "ds_read_b128 v[68:71], v23 offset:19456\n\t" \
    "v_mfma_f32_16x16x32_bf16 v[80:83], v[240:243], v[40:43], v[80:83]\n\t" \
    "s_add_u32 s69, s69, 23068672\n\t" \
    "s_add_u32 s40, s36, s69\n\t" \
    "v_mfma_f32_16x16x32_bf16 v[84:87], v[240:243], v[44:47], v[84:87]\n\t" \
    "s_addc_u32 s41, s37, 0\n\t" \
    "s_sub_u32 s91, s85, s91\n\t" \
    "s_waitcnt lgkmcnt(5)\n\t" \
    "v_mfma_f32_16x16x32_bf16 v[72:75], a[204:207], v[48:51], v[72:75]\n\t" \
    "ds_read_b128 v[24:27], v1 offset:0\n\t" \
    "s_waitcnt lgkmcnt(5)\n\t" \
    "v_mfma_f32_16x16x32_bf16 v[76:79], a[204:207], v[52:55], v[76:79]\n\t" \
    "ds_read_b128 v[28:31], v1 offset:18432\n\t" \
    "v_mfma_f32_16x16x32_bf16 v[80:83], v[244:247], v[48:51], v[80:83]\n\t" \
    "v_mfma_f32_16x16x32_bf16 v[84:87], v[244:247], v[52:55], v[84:87]\n\t" \
    "s_waitcnt lgkmcnt(5)\n\t" \
    "v_mfma_f32_16x16x32_bf16 v[72:75], a[208:211], v[56:59], v[72:75]\n\t" \
    "ds_read_b128 v[32:35], v2 offset:0\n\t" \
    "s_waitcnt lgkmcnt(5)\n\t" \
    "v_mfma_f32_16x16x32_bf16 v[76:79], a[208:211], v[60:63], v[76:79]\n\t" \
    "ds_read_b128 v[36:39], v2 offset:18432\n\t" \
    "v_mfma_f32_16x16x32_bf16 v[80:83], v[248:251], v[56:59], v[80:83]\n\t" \
    "v_mfma_f32_16x16x32_bf16 v[84:87], v[248:251], v[60:63], v[84:87]\n\t" \
    "s_waitcnt lgkmcnt(5)\n\t" \
    "v_mfma_f32_16x16x32_bf16 v[72:75], a[212:215], v[64:67], v[72:75]\n\t" \
    "ds_read_b128 v[40:43], v1 offset:128\n\t" \
    "s_waitcnt lgkmcnt(5)\n\t" \
    "v_mfma_f32_16x16x32_bf16 v[76:79], a[212:215], v[68:71], v[76:79]\n\t" \
    "ds_read_b128 v[44:47], v1 offset:18560\n\t" \
    "v_mfma_f32_16x16x32_bf16 v[80:83], v[252:255], v[64:67], v[80:83]\n\t" \
    "v_mfma_f32_16x16x32_bf16 v[84:87], v[252:255], v[68:71], v[84:87]\n\t" \
    "s_lshl_b32 s69, s65, 6\n\t" \
    "s_lshl_b32 s70, s66, 5\n\t" \
    "s_add_u32 s69, s69, s70\n\t" \
    "v_lshrrev_b32 v156, 3, v6\n\t" \
    "v_add_u32 v156, s69, v156\n\t" \
    "v_min_u32 v156, 575, v156\n\t" \
    "v_and_b32 v157, 4, v6\n\t" \
    "v_lshlrev_b32 v157, 5, v157\n\t" \
    "v_lshl_add_u32 v156, v156, 13, v157\n\t" \
    "s_lshl_b32 s69, s67, 8\n\t" \
    "v_add_u32 v156, s69, v156\n\t" \
    "s_mov_b32 m0, 155648\n\t" \
    "s_nop 0\n\t" \
    "global_load_lds_dword v156, s[44:45]\n\t" \
    "s_add_u32 s65, s65, 2\n\t" \
    "s_cmp_lt_u32 s65, 8\n\t" \
    "s_cbranch_scc1 gx_l16_%=\n\t" \
    "v_cvt_pk_bf16_f32 v104, v72, v73\n\t" \
    "v_cvt_pk_bf16_f32 v105, v74, v75\n\t" \
    "v_cvt_pk_bf16_f32 v106, v80, v81\n\t" \
    "v_cvt_pk_bf16_f32 v107, v82, v83\n\t" \
    "global_store_dwordx4 v8, v[104:107], s[40:41] offset:0 sc1\n\t" \
    "v_cvt_pk_bf16_f32 v108, v76, v77\n\t" \
    "v_cvt_pk_bf16_f32 v109, v78, v79\n\t" \
    "v_cvt_pk_bf16_f32 v110, v84, v85\n\t" \
    "v_cvt_pk_bf16_f32 v111, v86, v87\n\t" \
    "global_store_dwordx4 v8, v[108:111], s[40:41] offset:1024 sc1\n\t" \
    "s_waitcnt vmcnt(0) lgkmcnt(0)\n\t" \
    "s_barrier\n\t" \
    "s_cmp_lg_u32 s66, 0\n\t" \
    "s_cbranch_scc1 gx_SF12_%=\n\t" \
    "s_lshl_b32 s69, s67, 7\n\t" \
    "s_add_u32 s69, s69, 3149824\n\t" \
    "s_add_u32 s70, s65, -1\n\t" \
    "v_mov_b32 v156, s69\n\t" \
    "v_mov_b32 v157, s70\n\t" \
    "s_mov_b64 exec, 1\n\t" \
    "global_store_dword v156, v157, s[100:101] sc1\n\t" \
    "s_mov_b64 exec, -1\n\t" \
    "gx_SF12_%=:\n\t" \
    "s_add_u32 s46, s44, 0\n\t" \
    "s_addc_u32 s47, s45, 0\n\t" \
    "s_add_u32 m0, s82, 0\n\t" \
    "s_nop 0\n\t" \
    "global_load_lds_dwordx4 v200, s[46:47]\n\t" \
    "s_add_u32 s46, s44, 131072\n\t" \
    "s_addc_u32 s47, s45, 0\n\t" \
    "s_add_u32 m0, s82, 4096\n\t" \
    "s_nop 0\n\t" \
    "global_load_lds_dwordx4 v200, s[46:47]\n\t" \
    "s_add_u32 s46, s44, 262144\n\t" \
    "s_addc_u32 s47, s45, 0\n\t" \
    "s_add_u32 m0, s82, 8192\n\t" \
    "s_nop 0\n\t" \
    "global_load_lds_dwordx4 v200, s[46:47]\n\t" \
    "s_add_u32 s46, s44, 393216\n\t" \
    "s_addc_u32 s47, s45, 0\n\t" \
    "s_add_u32 m0, s82, 12288\n\t" \
    "s_nop 0\n\t" \
    "global_load_lds_dwordx4 v200, s[46:47]\n\t" \
    "s_add_u32 s46, s44, 524288\n\t" \
    "s_addc_u32 s47, s45, 0\n\t" \
    "s_add_u32 m0, s82, 16384\n\t" \
    "s_nop 0\n\t" \
    "global_load_lds_dwordx4 v200, s[46:47]\n\t" \
    "s_add_u32 s46, s44, 655360\n\t" \
    "s_addc_u32 s47, s45, 0\n\t" \
    "s_add_u32 m0, s82, 20480\n\t" \
    "s_nop 0\n\t" \
    "global_load_lds_dwordx4 v200, s[46:47]\n\t" \
    "s_add_u32 s46, s44, 786432\n\t" \
    "s_addc_u32 s47, s45, 0\n\t" \
    "s_add_u32 m0, s82, 24576\n\t" \
    "s_nop 0\n\t" \
    "global_load_lds_dwordx4 v200, s[46:47]\n\t" \
    "s_add_u32 s46, s44, 917504\n\t" \
    "s_addc_u32 s47, s45, 0\n\t" \
    "s_add_u32 m0, s82, 28672\n\t" \
    "s_nop 0\n\t" \
    "global_load_lds_dwordx4 v200, s[46:47]\n\t" \
    "s_add_u32 s46, s44, 1048576\n\t" \
    "s_addc_u32 s47, s45, 0\n\t" \
    "s_add_u32 m0, s82, 32768\n\t" \
    "s_nop 0\n\t" \
    "global_load_lds_dwordx4 v200, s[46:47]\n\t" \
    "s_add_u32 s46, s44, 1179648\n\t" \
    "s_addc_u32 s47, s45, 0\n\t" \
    "s_add_u32 m0, s82, 36864\n\t" \
    "s_nop 0\n\t" \
    "global_load_lds_dwordx4 v200, s[46:47]\n\t" \
    "s_add_u32 s46, s44, 1310720\n\t" \
    "s_addc_u32 s47, s45, 0\n\t" \
    "s_add_u32 m0, s82, 40960\n\t" \
    "s_nop 0\n\t" \
    "global_load_lds_dwordx4 v200, s[46:47]\n\t" \
    "s_add_u32 s46, s44, 1441792\n\t" \
    "s_addc_u32 s47, s45, 0\n\t" \
    "s_add_u32 m0, s82, 45056\n\t" \
    "s_nop 0\n\t" \
    "global_load_lds_dwordx4 v200, s[46:47]\n\t" \
    "s_add_u32 s46, s44, 1572864\n\t" \
    "s_addc_u32 s47, s45, 0\n\t" \
    "s_add_u32 m0, s82, 49152\n\t" \
    "s_nop 0\n\t" \
    "global_load_lds_dwordx4 v200, s[46:47]\n\t" \
    "s_add_u32 s46, s44, 1703936\n\t" \
    "s_addc_u32 s47, s45, 0\n\t" \
    "s_add_u32 m0, s82, 53248\n\t" \
    "s_nop 0\n\t" \
    "global_load_lds_dwordx4 v200, s[46:47]\n\t" \
    "s_add_u32 s46, s44, 1835008\n\t" \
    "s_addc_u32 s47, s45, 0\n\t" \
    "s_add_u32 m0, s82, 57344\n\t" \
    "s_nop 0\n\t" \
    "global_load_lds_dwordx4 v200, s[46:47]\n\t" \
    "s_add_u32 s46, s44, 1966080\n\t" \
    "s_addc_u32 s47, s45, 0\n\t" \
    "s_add_u32 m0, s82, 61440\n\t" \
    "s_nop 0\n\t" \
    "global_load_lds_dwordx4 v200, s[46:47]\n\t" \
    "s_add_u32 s46, s44, 2097152\n\t" \
    "s_addc_u32 s47, s45, 0\n\t" \
    "s_add_u32 m0, s82, 65536\n\t" \
    "s_nop 0\n\t" \
    "global_load_lds_dwordx4 v200, s[46:47]\n\t" \
    "s_add_u32 s46, s44, 2228224\n\t" \
    "s_addc_u32 s47, s45, 0\n\t" \
    "s_add_u32 m0, s82, 69632\n\t" \
    "s_nop 0\n\t" \
    "global_load_lds_dwordx4 v200, s[46:47]\n\t" \
    "s_add_u32 s46, s44, 2359296\n\t" \
    "s_addc_u32 s47, s45, 0\n\t" \
    "s_add_u32 m0, s82, 73728\n\t" \
    "s_nop 0\n\t" \
    "global_load_lds_dwordx4 v200, s[46:47]\n\t" \
    "s_add_u32 s46, s44, 2490368\n\t" \
    "s_addc_u32 s47, s45, 0\n\t" \
    "s_add_u32 m0, s82, 77824\n\t" \
    "s_nop 0\n\t" \
    "global_load_lds_dwordx4 v200, s[46:47]\n\t" \
    "s_add_u32 s46, s44, 2621440\n\t" \
    "s_addc_u32 s47, s45, 0\n\t" \
    "s_add_u32 m0, s82, 81920\n\t" \
    "s_nop 0\n\t" \
    "global_load_lds_dwordx4 v200, s[46:47]\n\t" \
    "s_add_u32 s46, s44, 2752512\n\t" \
    "s_addc_u32 s47, s45, 0\n\t" \
    "s_add_u32 m0, s82, 86016\n\t" \
    "s_nop 0\n\t" \
    "global_load_lds_dwordx4 v200, s[46:47]\n\t" \
    "s_add_u32 s46, s44, 2883584\n\t" \
    "s_addc_u32 s47, s45, 0\n\t" \
    "s_add_u32 m0, s82, 90112\n\t" \
    "s_nop 0\n\t" \
    "global_load_lds_dwordx4 v200, s[46:47]\n\t" \
    "s_add_u32 s46, s44, 3014656\n\t" \
    "s_addc_u32 s47, s45, 0\n\t" \
    "s_add_u32 m0, s82, 94208\n\t" \
    "s_nop 0\n\t" \
    "global_load_lds_dwordx4 v200, s[46:47]\n\t" \
    "s_add_u32 s46, s44, 3145728\n\t" \
    "s_addc_u32 s47, s45, 0\n\t" \
    "s_add_u32 m0, s82, 98304\n\t" \
    "s_nop 0\n\t" \
    "global_load_lds_dwordx4 v200, s[46:47]\n\t" \
    "s_add_u32 s46, s44, 3276800\n\t" \
    "s_addc_u32 s47, s45, 0\n\t" \
    "s_add_u32 m0, s82, 102400\n\t" \
    "s_nop 0\n\t" \
    "global_load_lds_dwordx4 v200, s[46:47]\n\t" \
    "s_add_u32 s46, s44, 3407872\n\t" \
    "s_addc_u32 s47, s45, 0\n\t" \
    "s_add_u32 m0, s82, 106496\n\t" \
    "s_nop 0\n\t" \
    "global_load_lds_dwordx4 v200, s[46:47]\n\t" \
    "s_add_u32 s46, s44, 3538944\n\t" \
    "s_addc_u32 s47, s45, 0\n\t" \
    "s_add_u32 m0, s82, 110592\n\t" \
    "s_nop 0\n\t" \
    "global_load_lds_dwordx4 v200, s[46:47]\n\t" \
    "s_add_u32 s46, s44, 3670016\n\t" \
    "s_addc_u32 s47, s45, 0\n\t" \
    "s_add_u32 m0, s82, 114688\n\t" \
    "s_nop 0\n\t" \
    "global_load_lds_dwordx4 v200, s[46:47]\n\t" \
    "s_add_u32 s46, s44, 3801088\n\t" \
    "s_addc_u32 s47, s45, 0\n\t" \
    "s_add_u32 m0, s82, 118784\n\t" \
    "s_nop 0\n\t" \
    "global_load_lds_dwordx4 v200, s[46:47]\n\t" \
    "s_add_u32 s46, s44, 3932160\n\t" \
    "s_addc_u32 s47, s45, 0\n\t" \
    "s_add_u32 m0, s82, 122880\n\t" \
    "s_nop 0\n\t" \
    "global_load_lds_dwordx4 v200, s[46:47]\n\t" \
    "s_add_u32 s46, s44, 4063232\n\t" \
    "s_addc_u32 s47, s45, 0\n\t" \
    "s_add_u32 m0, s82, 126976\n\t" \
    "s_nop 0\n\t" \
    "global_load_lds_dwordx4 v200, s[46:47]\n\t" \
    "s_add_u32 s46, s44, 4194304\n\t" \
    "s_addc_u32 s47, s45, 0\n\t" \
    "s_add_u32 m0, s82, 131072\n\t" \
    "s_nop 0\n\t" \
    "global_load_lds_dwordx4 v200, s[46:47]\n\t" \
    "s_add_u32 s46, s44, 4325376\n\t" \
    "s_addc_u32 s47, s45, 0\n\t" \
    "s_add_u32 m0, s82, 135168\n\t" \
    "s_nop 0\n\t" \
    "global_load_lds_dwordx4 v200, s[46:47]\n\t" \
    "s_add_u32 s46, s44, 4456448\n\t" \
    "s_addc_u32 s47, s45, 0\n\t" \
    "s_add_u32 m0, s82, 139264\n\t" \
    "s_nop 0\n\t" \
    "global_load_lds_dwordx4 v200, s[46:47]\n\t" \
    "s_add_u32 s46, s44, 4587520\n\t" \
    "s_addc_u32 s47, s45, 0\n\t" \
    "s_add_u32 m0, s82, 143360\n\t" \
    "s_nop 0\n\t" \
    "global_load_lds_dwordx4 v200, s[46:47]\n\t" \
    "global_load_dwordx4 v[120:123], v203, s[50:51]\n\t" \
    "global_load_dwordx4 v[124:127], v204, s[50:51]\n\t" \
    "global_load_dwordx4 v[128:131], v205, s[50:51]\n\t" \
    "global_load_dwordx4 v[132:135], v206, s[50:51]\n\t" \
    "global_load_dwordx4 v[136:139], v207, s[50:51]\n\t" \
    "global_load_dwordx4 v[140:143], v208, s[50:51]\n\t" \
    "global_load_dwordx4 v[144:147], v209, s[50:51]\n\t" \
    "global_load_dwordx4 v[148:151], v210, s[50:51]\n\t" \
    "global_load_dwordx4 v[152:155], v211, s[50:51]\n\t" \
    "s_add_u32 s52, s50, 294912\n\t" \
    "s_addc_u32 s53, s51, 0\n\t" \
    "global_load_dwordx4 v[72:75], v203, s[52:53]\n\t" \
    "global_load_dwordx4 v[76:79], v204, s[52:53]\n\t" \
    "global_load_dwordx4 v[80:83], v205, s[52:53]\n\t" \
    "global_load_dwordx4 v[84:87], v206, s[52:53]\n\t" \
    "global_load_dwordx4 v[88:91], v207, s[52:53]\n\t" \
    "global_load_dwordx4 v[92:95], v208, s[52:53]\n\t" \
    "global_load_dwordx4 v[96:99], v209, s[52:53]\n\t" \
    "global_load_dwordx4 v[100:103], v210, s[52:53]\n\t" \
    "global_load_dwordx4 v[104:107], v211, s[52:53]\n\t" \
    "s_waitcnt vmcnt(50) lgkmcnt(0)\n\t" \
    "s_barrier\n\t" \
    "ds_read_b64_tr_b16 a[0:1], v201 offset:0\n\t" \
    "ds_read_b64_tr_b16 a[2:3], v201 offset:1024\n\t" \
    "ds_read_b64_tr_b16 a[4:5], v201 offset:8192\n\t" \
    "ds_read_b64_tr_b16 a[6:7], v201 offset:9216\n\t" \
    "ds_read_b64_tr_b16 a[72:73], v202 offset:0\n\t" \
    "ds_read_b64_tr_b16 a[74:75], v202 offset:1024\n\t" \
    "ds_read_b64_tr_b16 a[76:77], v202 offset:8192\n\t" \
    "ds_read_b64_tr_b16 a[78:79], v202 offset:9216\n\t" \
    "s_waitcnt vmcnt(46) lgkmcnt(0)\n\t" \
    "s_barrier\n\t" \
    "ds_read_b64_tr_b16 a[8:9], v201 offset:16384\n\t" \
    "ds_read_b64_tr_b16 a[10:11], v201 offset:17408\n\t" \
    "ds_read_b64_tr_b16 a[12:13], v201 offset:24576\n\t" \
    "ds_read_b64_tr_b16 a[14:15], v201 offset:25600\n\t" \
    "ds_read_b64_tr_b16 a[80:81], v202 offset:16384\n\t" \
    "ds_read_b64_tr_b16 a[82:83], v202 offset:17408\n\t" \
    "ds_read_b64_tr_b16 a[84:85], v202 offset:24576\n\t" \
    "ds_read_b64_tr_b16 a[86:87], v202 offset:25600\n\t" \
    "s_waitcnt vmcnt(42) lgkmcnt(0)\n\t" \
    "s_barrier\n\t" \
    "ds_read_b64_tr_b16 a[16:17], v201 offset:32768\n\t" \
    "ds_read_b64_tr_b16 a[18:19], v201 offset:33792\n\t" \
    "ds_read_b64_tr_b16 a[20:21], v201 offset:40960\n\t" \
    "ds_read_b64_tr_b16 a[22:23], v201 offset:41984\n\t" \
    "ds_read_b64_tr_b16 a[88:89], v202 offset:32768\n\t" \
    "ds_read_b64_tr_b16 a[90:91], v202 offset:33792\n\t" \
    "ds_read_b64_tr_b16 a[92:93], v202 offset:40960\n\t" \
    "ds_read_b64_tr_b16 a[94:95], v202 offset:41984\n\t" \
    "s_waitcnt vmcnt(38) lgkmcnt(0)\n\t" \
    "s_barrier\n\t" \
    "ds_read_b64_tr_b16 a[24:25], v201 offset:49152\n\t" \
    "ds_read_b64_tr_b16 a[26:27], v201 offset:50176\n\t" \
    "ds_read_b64_tr_b16 a[28:29], v201 offset:57344\n\t" \
    "ds_read_b64_tr_b16 a[30:31], v201 offset:58368\n\t" \
    "ds_read_b64_tr_b16 a[96:97], v202 offset:49152\n\t" \
    "ds_read_b64_tr_b16 a[98:99], v202 offset:50176\n\t" \
    "ds_read_b64_tr_b16 a[100:101], v202 offset:57344\n\t" \
    "ds_read_b64_tr_b16 a[102:103], v202 offset:58368\n\t" \
    "s_waitcnt vmcnt(34) lgkmcnt(0)\n\t" \
    "s_barrier\n\t" \
    "v_add_u32 v201, 0x10000, v201\n\t" \
    "v_add_u32 v202, 0x10000, v202\n\t" \
    "ds_read_b64_tr_b16 a[32:33], v201 offset:0\n\t" \
    "ds_read_b64_tr_b16 a[34:35], v201 offset:1024\n\t" \
    "ds_read_b64_tr_b16 a[36:37], v201 offset:8192\n\t" \
    "ds_read_b64_tr_b16 a[38:39], v201 offset:9216\n\t" \
    "ds_read_b64_tr_b16 a[104:105], v202 offset:0\n\t" \
    "ds_read_b64_tr_b16 a[106:107], v202 offset:1024\n\t" \
    "ds_read_b64_tr_b16 a[108:109], v202 offset:8192\n\t" \
    "ds_read_b64_tr_b16 a[110:111], v202 offset:9216\n\t" \
    "s_waitcnt vmcnt(30) lgkmcnt(0)\n\t" \
    "s_barrier\n\t" \
    "ds_read_b64_tr_b16 a[40:41], v201 offset:16384\n\t" \
    "ds_read_b64_tr_b16 a[42:43], v201 offset:17408\n\t" \
    "ds_read_b64_tr_b16 a[44:45], v201 offset:24576\n\t" \
    "ds_read_b64_tr_b16 a[46:47], v201 offset:25600\n\t" \
    "ds_read_b64_tr_b16 a[112:113], v202 offset:16384\n\t" \
    "ds_read_b64_tr_b16 a[114:115], v202 offset:17408\n\t" \
    "ds_read_b64_tr_b16 a[116:117], v202 offset:24576\n\t" \
    "ds_read_b64_tr_b16 a[118:119], v202 offset:25600\n\t" \
    "s_waitcnt vmcnt(26) lgkmcnt(0)\n\t" \
    "s_barrier\n\t" \
    "ds_read_b64_tr_b16 a[48:49], v201 offset:32768\n\t" \
    "ds_read_b64_tr_b16 a[50:51], v201 offset:33792\n\t" \
    "ds_read_b64_tr_b16 a[52:53], v201 offset:40960\n\t" \
    "ds_read_b64_tr_b16 a[54:55], v201 offset:41984\n\t" \
    "ds_read_b64_tr_b16 a[120:121], v202 offset:32768\n\t" \
    "ds_read_b64_tr_b16 a[122:123], v202 offset:33792\n\t" \
    "ds_read_b64_tr_b16 a[124:125], v202 offset:40960\n\t" \
    "ds_read_b64_tr_b16 a[126:127], v202 offset:41984\n\t" \
    "s_waitcnt vmcnt(22) lgkmcnt(0)\n\t" \
    "s_barrier\n\t" \
    "ds_read_b64_tr_b16 a[56:57], v201 offset:49152\n\t" \
    "ds_read_b64_tr_b16 a[58:59], v201 offset:50176\n\t" \
    "ds_read_b64_tr_b16 a[60:61], v201 offset:57344\n\t" \
    "ds_read_b64_tr_b16 a[62:63], v201 offset:58368\n\t" \
    "ds_read_b64_tr_b16 a[128:129], v202 offset:49152\n\t" \
    "ds_read_b64_tr_b16 a[130:131], v202 offset:50176\n\t" \
    "ds_read_b64_tr_b16 a[132:133], v202 offset:57344\n\t" \
    "ds_read_b64_tr_b16 a[134:135], v202 offset:58368\n\t" \
    "s_waitcnt vmcnt(18) lgkmcnt(0)\n\t" \
    "s_barrier\n\t" \
    "v_add_u32 v201, 0x10000, v201\n\t" \
    "v_add_u32 v202, 0x10000, v202\n\t" \
    "ds_read_b64_tr_b16 a[64:65], v201 offset:0\n\t" \
    "ds_read_b64_tr_b16 a[66:67], v201 offset:1024\n\t" \
    "ds_read_b64_tr_b16 a[68:69], v201 offset:8192\n\t" \
    "ds_read_b64_tr_b16 a[70:71], v201 offset:9216\n\t" \
    "ds_read_b64_tr_b16 a[136:137], v202 offset:0\n\t" \
    "ds_read_b64_tr_b16 a[138:139], v202 offset:1024\n\t" \
    "ds_read_b64_tr_b16 a[140:141], v202 offset:8192\n\t" \
    "ds_read_b64_tr_b16 a[142:143], v202 offset:9216\n\t" \
    "v_add_u32 v201, 0xfffe0000, v201\n\t" \
    "v_add_u32 v202, 0xfffe0000, v202\n\t" \
    "s_waitcnt lgkmcnt(0)\n\t" \
    "s_barrier\n\t" \
    "s_waitcnt vmcnt(0)\n\t" \
    "ds_write_b128 v3, v[120:123] offset:0\n\t" \
    "ds_write_b128 v3, v[124:127] offset:1024\n\t" \
    "ds_write_b128 v3, v[128:131] offset:2048\n\t" \
    "ds_write_b128 v3, v[132:135] offset:3072\n\t" \
    "ds_write_b128 v3, v[136:139] offset:4096\n\t" \
    "ds_write_b128 v3, v[140:143] offset:5120\n\t" \
    "ds_write_b128 v3, v[144:147] offset:6144\n\t" \
    "ds_write_b128 v3, v[148:151] offset:7168\n\t" \
    "ds_write_b128 v3, v[152:155] offset:8192\n\t" \
    "ds_write_b128 v3, v[72:75] offset:36864\n\t" \
    "ds_write_b128 v3, v[76:79] offset:37888\n\t" \
    "ds_write_b128 v3, v[80:83] offset:38912\n\t" \
    "ds_write_b128 v3, v[84:87] offset:39936\n\t" \
    "ds_write_b128 v3, v[88:91] offset:40960\n\t" \
    "ds_write_b128 v3, v[92:95] offset:41984\n\t" \
    "ds_write_b128 v3, v[96:99] offset:43008\n\t" \
    "ds_write_b128 v3, v[100:103] offset:44032\n\t" \
    "ds_write_b128 v3, v[104:107] offset:45056\n\t" \
    "s_mov_b32 s65, 0\n\t" \
    "v_mov_b32 v1, v0\n\t" \
    "v_xor_b32 v2, 64, v0\n\t" \
    "s_mov_b32 s90, 0\n\t" \
    "s_add_u32 s91, s89, 0x12000\n\t" \
    "s_add_u32 s85, s91, s89\n\t" \
    "s_add_u32 s52, s50, 36864\n\t" \
    "s_addc_u32 s53, s51, 0\n\t" \
    "s_mul_i32 s69, s67, 8192\n\t" \
    "s_add_u32 s69, s69, 175112192\n\t" \
    "s_add_u32 s42, s36, s69\n\t" \
    "s_addc_u32 s43, s37, 0\n\t" \
    "s_mov_b64 s[40:41], s[42:43]\n\t" \
    "s_waitcnt lgkmcnt(0)\n\t" \
    "s_barrier\n\t" \
    "ds_read_b128 v[24:27], v1 offset:0\n\t" \
    "ds_read_b128 v[28:31], v1 offset:18432\n\t" \
    "ds_read_b128 v[32:35], v2 offset:0\n\t" \
    "ds_read_b128 v[36:39], v2 offset:18432\n\t" \
    "ds_read_b128 v[40:43], v1 offset:128\n\t" \
    "ds_read_b128 v[44:47], v1 offset:18560\n\t" \
    ".p2align 6\n\t" \
    "s_nop 0\n\t" \
    "s_nop 0\n\t" \
    "gx_l27_%=:\n\t" \
    "s_waitcnt lgkmcnt(5)\n\t" \
    "v_mfma_f32_16x16x32_bf16 v[88:91], a[0:3], v[24:27], 0\n\t" \
    "ds_read_b128 v[48:51], v2 offset:128\n\t" \
    "s_waitcnt lgkmcnt(5)\n\t" \
    "v_mfma_f32_16x16x32_bf16 v[92:95], a[0:3], v[28:31], 0\n\t" \
    "ds_read_b128 v[52:55], v2 offset:18560\n\t" \
    "v_mfma_f32_16x16x32_bf16 v[96:99], a[72:75], v[24:27], 0\n\t" \
    "s_add_u32 m0, s91, 0\n\t" \
    "v_mfma_f32_16x16x32_bf16 v[100:103], a[72:75], v[28:31], 0\n\t" \
    "global_load_lds_dwordx4 v203, s[52:53]\n\t" \
    "s_waitcnt lgkmcnt(5)\n\t" \
    "v_mfma_f32_16x16x32_bf16 v[88:91], a[4:7], v[32:35], v[88:91]\n\t" \
    "ds_read_b128 v[56:59], v1 offset:256\n\t" \
    "s_waitcnt lgkmcnt(5)\n\t" \
    "v_mfma_f32_16x16x32_bf16 v[92:95], a[4:7], v[36:39], v[92:95]\n\t" \
    "ds_read_b128 v[60:63], v1 offset:18688\n\t" \
    "v_mfma_f32_16x16x32_bf16 v[96:99], a[76:79], v[32:35], v[96:99]\n\t" \
    "s_add_u32 m0, s91, 1024\n\t" \
    "v_mfma_f32_16x16x32_bf16 v[100:103], a[76:79], v[36:39], v[100:103]\n\t" \
    "global_load_lds_dwordx4 v204, s[52:53]\n\t" \
    "s_waitcnt lgkmcnt(5)\n\t" \
    "v_mfma_f32_16x16x32_bf16 v[88:91], a[8:11], v[40:43], v[88:91]\n\t" \
    "ds_read_b128 v[64:67], v2 offset:256\n\t" \
    "s_waitcnt lgkmcnt(5)\n\t" \
    "v_mfma_f32_16x16x32_bf16 v[92:95], a[8:11], v[44:47], v[92:95]\n\t" \
    "ds_read_b128 v[68:71], v2 offset:18688\n\t" \
    "v_cvt_pk_bf16_f32 v104, v72, v73\n\t" \
    "v_mfma_f32_16x16x32_bf16 v[96:99], a[80:83], v[40:43], v[96:99]\n\t" \
    "s_add_u32 m0, s91, 2048\n\t" \
    "v_cvt_pk_bf16_f32 v105, v74, v75\n\t" \
    "v_mfma_f32_16x16x32_bf16 v[100:103], a[80:83], v[44:47], v[100:103]\n\t" \
    "global_load_lds_dwordx4 v205, s[52:53]\n\t" \
    "s_waitcnt lgkmcnt(5)\n\t" \
    "v_mfma_f32_16x16x32_bf16 v[88:91], a[12:15], v[48:51], v[88:91]\n\t" \
    "ds_read_b128 v[24:27], v1 offset:384\n\t" \
    "s_waitcnt lgkmcnt(5)\n\t" \
    "v_mfma_f32_16x16x32_bf16 v[92:95], a[12:15], v[52:55], v[92:95]\n\t" \
    "ds_read_b128 v[28:31], v1 offset:18816\n\t" \
    "v_cvt_pk_bf16_f32 v106, v80, v81\n\t" \
    "v_mfma_f32_16x16x32_bf16 v[96:99], a[84:87], v[48:51], v[96:99]\n\t" \
    "s_add_u32 m0, s91, 3072\n\t" \
    "v_cvt_pk_bf16_f32 v107, v82, v83\n\t" \
    "v_mfma_f32_16x16x32_bf16 v[100:103], a[84:87], v[52:55], v[100:103]\n\t" \
    "global_load_lds_dwordx4 v206, s[52:53]\n\t" \
    "s_waitcnt lgkmcnt(5)\n\t" \
    "v_mfma_f32_16x16x32_bf16 v[88:91], a[16:19], v[56:59], v[88:91]\n\t" \
    "ds_read_b128 v[32:35], v2 offset:384\n\t" \
    "s_waitcnt lgkmcnt(5)\n\t" \
    "v_mfma_f32_16x16x32_bf16 v[92:95], a[16:19], v[60:63], v[92:95]\n\t" \
    "ds_read_b128 v[36:39], v2 offset:18816\n\t" \
    "global_store_dwordx4 v8, v[104:107], s[40:41] offset:0 sc1\n\t" \
    "v_mfma_f32_16x16x32_bf16 v[96:99], a[88:91], v[56:59], v[96:99]\n\t" \
    "s_add_u32 m0, s91, 4096\n\t" \
    "v_cvt_pk_bf16_f32 v108, v76, v77\n\t" \
    "v_mfma_f32_16x16x32_bf16 v[100:103], a[88:91], v[60:63], v[100:103]\n\t" \
    "global_load_lds_dwordx4 v207, s[52:53]\n\t" \
    "s_waitcnt lgkmcnt(5)\n\t" \
    "v_mfma_f32_16x16x32_bf16 v[88:91], a[20:23], v[64:67], v[88:91]\n\t" \
    "ds_read_b128 v[40:43], v1 offset:512\n\t" \
    "s_waitcnt lgkmcnt(5)\n\t" \
    "v_mfma_f32_16x16x32_bf16 v[92:95], a[20:23], v[68:71], v[92:95]\n\t" \
    "ds_read_b128 v[44:47], v1 offset:18944\n\t" \
    "v_cvt_pk_bf16_f32 v109, v78, v79\n\t" \
    "v_mfma_f32_16x16x32_bf16 v[96:99], a[92:95], v[64:67], v[96:99]\n\t" \
    "s_add_u32 m0, s91, 5120\n\t" \
    "v_cvt_pk_bf16_f32 v110, v84, v85\n\t" \
    "v_mfma_f32_16x16x32_bf16 v[100:103], a[92:95], v[68:71], v[100:103]\n\t" \
    "global_load_lds_dwordx4 v208, s[52:53]\n\t" \
    "s_waitcnt lgkmcnt(5)\n\t" \
    "v_mfma_f32_16x16x32_bf16 v[88:91], a[24:27], v[24:27], v[88:91]\n\t" \
    "ds_read_b128 v[48:51], v2 offset:512\n\t" \
    "s_waitcnt lgkmcnt(5)\n\t" \
    "v_mfma_f32_16x16x32_bf16 v[92:95], a[24:27], v[28:31], v[92:95]\n\t" \
    "ds_read_b128 v[52:55], v2 offset:18944\n\t" \
    "v_cvt_pk_bf16_f32 v111, v86, v87\n\t" \
    "v_mfma_f32_16x16x32_bf16 v[96:99], a[96:99], v[24:27], v[96:99]\n\t" \
    "s_add_u32 m0, s91, 6144\n\t" \
    "global_store_dwordx4 v8, v[108:111], s[40:41] offset:1024 sc1\n\t" \
    "v_mfma_f32_16x16x32_bf16 v[100:103], a[96:99], v[28:31], v[100:103]\n\t" \
    "global_load_lds_dwordx4 v209, s[52:53]\n\t" \
    "s_waitcnt lgkmcnt(5)\n\t" \
    "v_mfma_f32_16x16x32_bf16 v[88:91], a[28:31], v[32:35], v[88:91]\n\t" \
    "ds_read_b128 v[56:59], v1 offset:640\n\t" \
    "s_waitcnt lgkmcnt(5)\n\t" \
    "v_mfma_f32_16x16x32_bf16 v[92:95], a[28:31], v[36:39], v[92:95]\n\t" \
    "ds_read_b128 v[60:63], v1 offset:19072\n\t" \
    "v_mfma_f32_16x16x32_bf16 v[96:99], a[100:103], v[32:35], v[96:99]\n\t" \
    "s_add_u32 m0, s91, 7168\n\t" \
    "v_mfma_f32_16x16x32_bf16 v[100:103], a[100:103], v[36:39], v[100:103]\n\t" \
    "global_load_lds_dwordx4 v210, s[52:53]\n\t" \
    "s_waitcnt lgkmcnt(5)\n\t" \
    "v_mfma_f32_16x16x32_bf16 v[88:91], a[32:35], v[40:43], v[88:91]\n\t" \
    "ds_read_b128 v[64:67], v2 offset:640\n\t" \
    "s_waitcnt lgkmcnt(5)\n\t" \
    "v_mfma_f32_16x16x32_bf16 v[92:95], a[32:35], v[44:47], v[92:95]\n\t" \
    "ds_read_b128 v[68:71], v2 offset:19072\n\t" \
    "v_mfma_f32_16x16x32_bf16 v[96:99], a[104:107], v[40:43], v[96:99]\n\t" \
    "s_add_u32 m0, s91, 8192\n\t" \
    "v_mfma_f32_16x16x32_bf16 v[100:103], a[104:107], v[44:47], v[100:103]\n\t" \
    "global_load_lds_dwordx4 v211, s[52:53]\n\t" \
    "s_waitcnt lgkmcnt(5)\n\t" \
    "v_mfma_f32_16x16x32_bf16 v[88:91], a[36:39], v[48:51], v[88:91]\n\t" \
    "ds_read_b128 v[24:27], v1 offset:768\n\t" \
    "s_waitcnt lgkmcnt(5)\n\t" \
    "v_mfma_f32_16x16x32_bf16 v[92:95], a[36:39], v[52:55], v[92:95]\n\t" \
    "ds_read_b128 v[28:31], v1 offset:19200\n\t" \
    "v_mfma_f32_16x16x32_bf16 v[96:99], a[108:111], v[48:51], v[96:99]\n\t" \
    "v_mfma_f32_16x16x32_bf16 v[100:103], a[108:111], v[52:55], v[100:103]\n\t" \
    "s_waitcnt lgkmcnt(5)\n\t" \
    "v_mfma_f32_16x16x32_bf16 v[88:91], a[40:43], v[56:59], v[88:91]\n\t" \
    "ds_read_b128 v[32:35], v2 offset:768\n\t" \
    "s_waitcnt lgkmcnt(5)\n\t" \
    "v_mfma_f32_16x16x32_bf16 v[92:95], a[40:43], v[60:63], v[92:95]\n\t" \
    "ds_read_b128 v[36:39], v2 offset:19200\n\t" \
    "v_mfma_f32_16x16x32_bf16 v[96:99], a[112:115], v[56:59], v[96:99]\n\t" \
    "s_waitcnt vmcnt(11)\n\t" \
    "s_barrier\n\t" \
    "v_mfma_f32_16x16x32_bf16 v[100:103], a[112:115], v[60:63], v[100:103]\n\t" \
    "s_cmp_lg_u32 s66, 0\n\t" \
    "s_cbranch_scc1 gx_SF13_%=\n\t" \
    "s_lshl_b32 s69, s67, 7\n\t" \
    "s_add_u32 s69, s69, 3211264\n\t" \
    "s_add_u32 s70, s65, -1\n\t" \
    "v_mov_b32 v156, s69\n\t" \
    "v_mov_b32 v157, s70\n\t" \
    "s_mov_b64 exec, 1\n\t" \
    "global_store_dword v156, v157, s[100:101] sc1\n\t" \
    "s_mov_b64 exec, -1\n\t" \
    "gx_SF13_%=:\n\t" \
    "s_waitcnt lgkmcnt(5)\n\t" \
    "v_mfma_f32_16x16x32_bf16 v[88:91], a[44:47], v[64:67], v[88:91]\n\t" \
    "ds_read_b128 v[40:43], v1 offset:896\n\t" \
    "s_waitcnt lgkmcnt(5)\n\t" \
    "v_mfma_f32_16x16x32_bf16 v[92:95], a[44:47], v[68:71], v[92:95]\n\t" \
    "ds_read_b128 v[44:47], v1 offset:19328\n\t" \
    "v_mfma_f32_16x16x32_bf16 v[96:99], a[116:119], v[64:67], v[96:99]\n\t" \
    "v_add_u32 v22, 0x9000, v1\n\t" \
    "v_add_u32 v23, 0x9000, v2\n\t" \
    "v_mfma_f32_16x16x32_bf16 v[100:103], a[116:119], v[68:71], v[100:103]\n\t" \
    "s_cmp_lt_u32 s65, 55\n\t" \
    "s_cselect_b32 s84, 9, 2\n\t" \
    "s_waitcnt lgkmcnt(5)\n\t" \
    "v_mfma_f32_16x16x32_bf16 v[88:91], a[48:51], v[24:27], v[88:91]\n\t" \
    "ds_read_b128 v[48:51], v2 offset:896\n\t" \
    "s_waitcnt lgkmcnt(5)\n\t" \
    "v_mfma_f32_16x16x32_bf16 v[92:95], a[48:51], v[28:31], v[92:95]\n\t" \
    "ds_read_b128 v[52:55], v2 offset:19328\n\t" \
    "v_mfma_f32_16x16x32_bf16 v[96:99], a[120:123], v[24:27], v[96:99]\n\t" \
    "s_add_u32 s84, s65, s84\n\t" \
    "s_min_u32 s69, s84, 63\n\t" \
    "v_mfma_f32_16x16x32_bf16 v[100:103], a[120:123], v[28:31], v[100:103]\n\t" \
    "s_mul_i32 s69, s69, 36864\n\t" \
    "s_add_u32 s48, s50, s69\n\t" \
    "s_waitcnt lgkmcnt(5)\n\t" \
    "v_mfma_f32_16x16x32_bf16 v[88:91], a[52:55], v[32:35], v[88:91]\n\t" \
    "ds_read_b128 v[56:59], v1 offset:1024\n\t" \
    "s_waitcnt lgkmcnt(5)\n\t" \
    "v_mfma_f32_16x16x32_bf16 v[92:95], a[52:55], v[36:39], v[92:95]\n\t" \
    "ds_read_b128 v[60:63], v1 offset:19456\n\t" \
    "v_mfma_f32_16x16x32_bf16 v[96:99], a[124:127], v[32:35], v[96:99]\n\t" \
    "s_addc_u32 s49, s51, 0\n\t" \
    "s_mul_i32 s70, s67, 7\n\t" \
    "v_mfma_f32_16x16x32_bf16 v[100:103], a[124:127], v[36:39], v[100:103]\n\t" \
    "s_add_u32 s69, s65, 0\n\t" \
    "s_xor_b32 s69, s69, s70\n\t" \
    "s_waitcnt lgkmcnt(5)\n\t" \
    "v_mfma_f32_16x16x32_bf16 v[88:91], a[56:59], v[40:43], v[88:91]\n\t" \
    "ds_read_b128 v[64:67], v2 offset:1024\n\t" \
    "s_waitcnt lgkmcnt(5)\n\t" \
    "v_mfma_f32_16x16x32_bf16 v[92:95], a[56:59], v[44:47], v[92:95]\n\t" \
    "ds_read_b128 v[68:71], v2 offset:19456\n\t" \
    "v_mfma_f32_16x16x32_bf16 v[96:99], a[128:131], v[40:43], v[96:99]\n\t" \
    "s_and_b32 s69, s69, 63\n\t" \
    "s_lshl_b32 s69, s69, 13\n\t" \
    "v_mfma_f32_16x16x32_bf16 v[100:103], a[128:131], v[44:47], v[100:103]\n\t" \
    "s_mul_i32 s70, s67, 524288\n\t" \
    "s_add_u32 s69, s69, s70\n\t" \
    "s_waitcnt lgkmcnt(5)\n\t" \
    "v_mfma_f32_16x16x32_bf16 v[88:91], a[60:63], v[48:51], v[88:91]\n\t" \
    "ds_read_b128 v[24:27], v22 offset:0\n\t" \
    "s_waitcnt lgkmcnt(5)\n\t" \
    "v_mfma_f32_16x16x32_bf16 v[92:95], a[60:63], v[52:55], v[92:95]\n\t" \
    "ds_read_b128 v[28:31], v22 offset:18432\n\t" \
    "v_mfma_f32_16x16x32_bf16 v[96:99], a[132:135], v[48:51], v[96:99]\n\t" \
    "s_add_u32 s69, s69, 6291456\n\t" \
    "s_add_u32 s38, s36, s69\n\t" \
    "v_mfma_f32_16x16x32_bf16 v[100:103], a[132:135], v[52:55], v[100:103]\n\t" \
    "s_addc_u32 s39, s37, 0\n\t" \
    "s_waitcnt lgkmcnt(5)\n\t" \
    "v_mfma_f32_16x16x32_bf16 v[88:91], a[64:67], v[56:59], v[88:91]\n\t" \
    "ds_read_b128 v[32:35], v23 offset:0\n\t" \
    "s_waitcnt lgkmcnt(5)\n\t" \
    "v_mfma_f32_16x16x32_bf16 v[92:95], a[64:67], v[60:63], v[92:95]\n\t" \
    "ds_read_b128 v[36:39], v23 offset:18432\n\t" \
    "v_mfma_f32_16x16x32_bf16 v[96:99], a[136:139], v[56:59], v[96:99]\n\t" \
    "v_mfma_f32_16x16x32_bf16 v[100:103], a[136:139], v[60:63], v[100:103]\n\t" \
    "s_waitcnt lgkmcnt(5)\n\t" \
    "v_mfma_f32_16x16x32_bf16 v[88:91], a[68:71], v[64:67], v[88:91]\n\t" \
    "ds_read_b128 v[40:43], v22 offset:128\n\t" \
    "s_waitcnt lgkmcnt(5)\n\t" \
    "v_mfma_f32_16x16x32_bf16 v[92:95], a[68:71], v[68:71], v[92:95]\n\t" \
    "ds_read_b128 v[44:47], v22 offset:18560\n\t" \
    "v_mfma_f32_16x16x32_bf16 v[96:99], a[140:143], v[64:67], v[96:99]\n\t" \
    "v_mfma_f32_16x16x32_bf16 v[100:103], a[140:143], v[68:71], v[100:103]\n\t" \
    "s_waitcnt lgkmcnt(5)\n\t" \
    "v_mfma_f32_16x16x32_bf16 v[72:75], a[144:147], v[24:27], 0\n\t" \
    "ds_read_b128 v[48:51], v23 offset:128\n\t" \
    "s_waitcnt lgkmcnt(5)\n\t" \
    "v_mfma_f32_16x16x32_bf16 v[76:79], a[144:147], v[28:31], 0\n\t" \
    "ds_read_b128 v[52:55], v23 offset:18560\n\t" \
    "v_mfma_f32_16x16x32_bf16 v[80:83], a[216:219], v[24:27], 0\n\t" \
    "s_add_u32 m0, s91, 36864\n\t" \
    "v_mfma_f32_16x16x32_bf16 v[84:87], a[216:219], v[28:31], 0\n\t" \
    "global_load_lds_dwordx4 v203, s[48:49]\n\t" \
    "s_waitcnt lgkmcnt(5)\n\t" \
    "v_mfma_f32_16x16x32_bf16 v[72:75], a[148:151], v[32:35], v[72:75]\n\t" \
    "ds_read_b128 v[56:59], v22 offset:256\n\t" \
    "s_waitcnt lgkmcnt(5)\n\t" \
    "v_mfma_f32_16x16x32_bf16 v[76:79], a[148:151], v[36:39], v[76:79]\n\t" \
    "ds_read_b128 v[60:63], v22 offset:18688\n\t" \
    "v_mfma_f32_16x16x32_bf16 v[80:83], a[220:223], v[32:35], v[80:83]\n\t" \
    "s_add_u32 m0, s91, 37888\n\t" \
    "v_mfma_f32_16x16x32_bf16 v[84:87], a[220:223], v[36:39], v[84:87]\n\t" \
    "global_load_lds_dwordx4 v204, s[48:49]\n\t" \
    "s_waitcnt lgkmcnt(5)\n\t" \
    "v_mfma_f32_16x16x32_bf16 v[72:75], a[152:155], v[40:43], v[72:75]\n\t" \
    "ds_read_b128 v[64:67], v23 offset:256\n\t" \
    "s_waitcnt lgkmcnt(5)\n\t" \
    "v_mfma_f32_16x16x32_bf16 v[76:79], a[152:155], v[44:47], v[76:79]\n\t" \
    "ds_read_b128 v[68:71], v23 offset:18688\n\t" \
    "v_cvt_pk_bf16_f32 v104, v88, v89\n\t" \
    "v_mfma_f32_16x16x32_bf16 v[80:83], a[224:227], v[40:43], v[80:83]\n\t" \
    "s_add_u32 m0, s91, 38912\n\t" \
    "v_cvt_pk_bf16_f32 v105, v90, v91\n\t" \
    "v_mfma_f32_16x16x32_bf16 v[84:87], a[224:227], v[44:47], v[84:87]\n\t" \
    "global_load_lds_dwordx4 v205, s[48:49]\n\t" \
    "s_waitcnt lgkmcnt(5)\n\t" \
    "v_mfma_f32_16x16x32_bf16 v[72:75], a[156:159], v[48:51], v[72:75]\n\t" \
    "ds_read_b128 v[24:27], v22 offset:384\n\t" \
    "s_waitcnt lgkmcnt(5)\n\t" \
    "v_mfma_f32_16x16x32_bf16 v[76:79], a[156:159], v[52:55], v[76:79]\n\t" \
    "ds_read_b128 v[28:31], v22 offset:18816\n\t" \
    "v_cvt_pk_bf16_f32 v106, v96, v97\n\t" \
    "v_mfma_f32_16x16x32_bf16 v[80:83], a[228:231], v[48:51], v[80:83]\n\t" \
    "s_add_u32 m0, s91, 39936\n\t" \
    "v_cvt_pk_bf16_f32 v107, v98, v99\n\t" \
    "v_mfma_f32_16x16x32_bf16 v[84:87], a[228:231], v[52:55], v[84:87]\n\t" \
    "global_load_lds_dwordx4 v206, s[48:49]\n\t" \
    "s_waitcnt lgkmcnt(5)\n\t" \
    "v_mfma_f32_16x16x32_bf16 v[72:75], a[160:163], v[56:59], v[72:75]\n\t" \
    "ds_read_b128 v[32:35], v23 offset:384\n\t" \
    "s_waitcnt lgkmcnt(5)\n\t" \
    "v_mfma_f32_16x16x32_bf16 v[76:79], a[160:163], v[60:63], v[76:79]\n\t" \
    "ds_read_b128 v[36:39], v23 offset:18816\n\t" \
    "global_store_dwordx4 v8, v[104:107], s[38:39] offset:0 sc1\n\t" \
    "v_mfma_f32_16x16x32_bf16 v[80:83], a[232:235], v[56:59], v[80:83]\n\t" \
    "s_add_u32 m0, s91, 40960\n\t" \
    "v_cvt_pk_bf16_f32 v108, v92, v93\n\t" \
    "v_mfma_f32_16x16x32_bf16 v[84:87], a[232:235], v[60:63], v[84:87]\n\t" \
    "global_load_lds_dwordx4 v207, s[48:49]\n\t" \
    "s_waitcnt lgkmcnt(5)\n\t" \
    "v_mfma_f32_16x16x32_bf16 v[72:75], a[164:167], v[64:67], v[72:75]\n\t" \
    "ds_read_b128 v[40:43], v22 offset:512\n\t" \
    "s_waitcnt lgkmcnt(5)\n\t" \
    "v_mfma_f32_16x16x32_bf16 v[76:79], a[164:167], v[68:71], v[76:79]\n\t" \
    "ds_read_b128 v[44:47], v22 offset:18944\n\t" \
    "v_cvt_pk_bf16_f32 v109, v94, v95\n\t" \
    "v_mfma_f32_16x16x32_bf16 v[80:83], a[236:239], v[64:67], v[80:83]\n\t" \
    "s_add_u32 m0, s91, 41984\n\t" \
    "v_cvt_pk_bf16_f32 v110, v100, v101\n\t" \
    "v_mfma_f32_16x16x32_bf16 v[84:87], a[236:239], v[68:71], v[84:87]\n\t" \
    "global_load_lds_dwordx4 v208, s[48:49]\n\t" \
    "s_waitcnt lgkmcnt(5)\n\t" \
    "v_mfma_f32_16x16x32_bf16 v[72:75], a[168:171], v[24:27], v[72:75]\n\t" \
    "ds_read_b128 v[48:51], v23 offset:512\n\t" \
    "s_waitcnt lgkmcnt(5)\n\t" \
    "v_mfma_f32_16x16x32_bf16 v[76:79], a[168:171], v[28:31], v[76:79]\n\t" \
    "ds_read_b128 v[52:55], v23 offset:18944\n\t" \
    "v_cvt_pk_bf16_f32 v111, v102, v103\n\t" \
    "v_mfma_f32_16x16x32_bf16 v[80:83], a[240:243], v[24:27], v[80:83]\n\t" \
    "s_add_u32 m0, s91, 43008\n\t" \
    "global_store_dwordx4 v8, v[108:111], s[38:39] offset:1024 sc1\n\t" \
    "v_mfma_f32_16x16x32_bf16 v[84:87], a[240:243], v[28:31], v[84:87]\n\t" \
    "global_load_lds_dwordx4 v209, s[48:49]\n\t" \
    "s_waitcnt lgkmcnt(5)\n\t" \
    "v_mfma_f32_16x16x32_bf16 v[72:75], a[172:175], v[32:35], v[72:75]\n\t" \
    "ds_read_b128 v[56:59], v22 offset:640\n\t" \
    "s_waitcnt lgkmcnt(5)\n\t" \
    "v_mfma_f32_16x16x32_bf16 v[76:79], a[172:175], v[36:39], v[76:79]\n\t" \
    "ds_read_b128 v[60:63], v22 offset:19072\n\t" \
    "v_mfma_f32_16x16x32_bf16 v[80:83], a[244:247], v[32:35], v[80:83]\n\t" \
    "s_add_u32 m0, s91, 44032\n\t" \
    "v_mfma_f32_16x16x32_bf16 v[84:87], a[244:247], v[36:39], v[84:87]\n\t" \
    "global_load_lds_dwordx4 v210, s[48:49]\n\t" \
    "s_waitcnt lgkmcnt(5)\n\t" \
    "v_mfma_f32_16x16x32_bf16 v[72:75], a[176:179], v[40:43], v[72:75]\n\t" \
    "ds_read_b128 v[64:67], v23 offset:640\n\t" \
    "s_waitcnt lgkmcnt(5)\n\t" \
    "v_mfma_f32_16x16x32_bf16 v[76:79], a[176:179], v[44:47], v[76:79]\n\t" \
    "ds_read_b128 v[68:71], v23 offset:19072\n\t" \
    "v_mfma_f32_16x16x32_bf16 v[80:83], a[248:251], v[40:43], v[80:83]\n\t" \
    "s_add_u32 m0, s91, 45056\n\t" \
    "v_mfma_f32_16x16x32_bf16 v[84:87], a[248:251], v[44:47], v[84:87]\n\t" \
    "global_load_lds_dwordx4 v211, s[48:49]\n\t" \
    "s_waitcnt lgkmcnt(5)\n\t" \
    "v_mfma_f32_16x16x32_bf16 v[72:75], a[180:183], v[48:51], v[72:75]\n\t" \
    "ds_read_b128 v[24:27], v22 offset:768\n\t" \
    "s_waitcnt lgkmcnt(5)\n\t" \
    "v_mfma_f32_16x16x32_bf16 v[76:79], a[180:183], v[52:55], v[76:79]\n\t" \
    "ds_read_b128 v[28:31], v22 offset:19200\n\t" \
    "v_mfma_f32_16x16x32_bf16 v[80:83], a[252:255], v[48:51], v[80:83]\n\t" \
    "v_mfma_f32_16x16x32_bf16 v[84:87], a[252:255], v[52:55], v[84:87]\n\t" \
    "s_waitcnt lgkmcnt(5)\n\t" \
    "v_mfma_f32_16x16x32_bf16 v[72:75], a[184:187], v[56:59], v[72:75]\n\t" \
    "ds_read_b128 v[32:35], v23 offset:768\n\t" \
    "s_waitcnt lgkmcnt(5)\n\t" \
    "v_mfma_f32_16x16x32_bf16 v[76:79], a[184:187], v[60:63], v[76:79]\n\t" \
    "ds_read_b128 v[36:39], v23 offset:19200\n\t" \
    "v_mfma_f32_16x16x32_bf16 v[80:83], v[224:227], v[56:59], v[80:83]\n\t" \
    "s_waitcnt vmcnt(11)\n\t" \
    "s_barrier\n\t" \
    "v_mfma_f32_16x16x32_bf16 v[84:87], v[224:227], v[60:63], v[84:87]\n\t" \
    "s_add_u32 s71, s65, 7\n\t" \
    "s_cmp_lg_u32 s66, 0\n\t" \
    "s_cbranch_scc1 gx_SF14_%=\n\t" \
    "s_lshl_b32 s69, s67, 7\n\t" \
    "s_add_u32 s69, s69, 3149824\n\t" \
    "s_add_u32 s70, s71, 0\n\t" \
    "v_mov_b32 v156, s69\n\t" \
    "v_mov_b32 v157, s70\n\t" \
    "s_mov_b64 exec, 1\n\t" \
    "global_store_dword v156, v157, s[100:101] sc1\n\t" \
    "s_mov_b64 exec, -1\n\t" \
    "gx_SF14_%=:\n\t" \
    "s_waitcnt lgkmcnt(5)\n\t" \
    "v_mfma_f32_16x16x32_bf16 v[72:75], a[188:191], v[64:67], v[72:75]\n\t" \
    "ds_read_b128 v[40:43], v22 offset:896\n\t" \
    "s_waitcnt lgkmcnt(5)\n\t" \
    "v_mfma_f32_16x16x32_bf16 v[76:79], a[188:191], v[68:71], v[76:79]\n\t" \
    "ds_read_b128 v[44:47], v22 offset:19328\n\t" \
    "v_mfma_f32_16x16x32_bf16 v[80:83], v[228:231], v[64:67], v[80:83]\n\t" \
    "s_xor_b32 s90, s90, 0x12000\n\t" \
    "v_add_u32 v1, s90, v0\n\t" \
    "v_mfma_f32_16x16x32_bf16 v[84:87], v[228:231], v[68:71], v[84:87]\n\t" \
    "v_xor_b32 v2, 64, v1\n\t" \
    "s_add_u32 s84, s65, 2\n\t" \
    "s_waitcnt lgkmcnt(5)\n\t" \
    "v_mfma_f32_16x16x32_bf16 v[72:75], a[192:195], v[24:27], v[72:75]\n\t" \
    "ds_read_b128 v[48:51], v23 offset:896\n\t" \
    "s_waitcnt lgkmcnt(5)\n\t" \
    "v_mfma_f32_16x16x32_bf16 v[76:79], a[192:195], v[28:31], v[76:79]\n\t" \
    "ds_read_b128 v[52:55], v23 offset:19328\n\t" \
    "v_mfma_f32_16x16x32_bf16 v[80:83], v[232:235], v[24:27], v[80:83]\n\t" \
    "s_min_u32 s69, s84, 63\n\t" \
    "s_mul_i32 s69, s69, 36864\n\t" \
    "v_mfma_f32_16x16x32_bf16 v[84:87], v[232:235], v[28:31], v[84:87]\n\t" \
    "s_add_u32 s52, s50, s69\n\t" \
    "s_addc_u32 s53, s51, 0\n\t" \
    "s_waitcnt lgkmcnt(5)\n\t" \
    "v_mfma_f32_16x16x32_bf16 v[72:75], a[196:199], v[32:35], v[72:75]\n\t" \
    "ds_read_b128 v[56:59], v22 offset:1024\n\t" \
    "s_waitcnt lgkmcnt(5)\n\t" \
    "v_mfma_f32_16x16x32_bf16 v[76:79], a[196:199], v[36:39], v[76:79]\n\t" \
    "ds_read_b128 v[60:63], v22 offset:19456\n\t" \
    "v_mfma_f32_16x16x32_bf16 v[80:83], v[236:239], v[32:35], v[80:83]\n\t" \
    "s_mul_i32 s70, s67, 7\n\t" \
    "s_add_u32 s69, s65, 8\n\t" \
    "v_mfma_f32_16x16x32_bf16 v[84:87], v[236:239], v[36:39], v[84:87]\n\t" \
    "s_xor_b32 s69, s69, s70\n\t" \
    "s_and_b32 s69, s69, 63\n\t" \
    "s_waitcnt lgkmcnt(5)\n\t" \
    "v_mfma_f32_16x16x32_bf16 v[72:75], a[200:203], v[40:43], v[72:75]\n\t" \
    "ds_read_b128 v[64:67], v23 offset:1024\n\t" \
    "s_waitcnt lgkmcnt(5)\n\t" \
    "v_mfma_f32_16x16x32_bf16 v[76:79], a[200:203], v[44:47], v[76:79]\n\t" \
    "ds_read_b128 v[68:71], v23 offset:19456\n\t" \
    "v_mfma_f32_16x16x32_bf16 v[80:83], v[240:243], v[40:43], v[80:83]\n\t" \
    "s_lshl_b32 s69, s69, 13\n\t" \
    "s_mul_i32 s70, s67, 524288\n\t" \
    "v_mfma_f32_16x16x32_bf16 v[84:87], v[240:243], v[44:47], v[84:87]\n\t" \
    "s_add_u32 s69, s69, s70\n\t" \
    "s_add_u32 s69, s69, 23068672\n\t" \
    "s_waitcnt lgkmcnt(5)\n\t" \
    "v_mfma_f32_16x16x32_bf16 v[72:75], a[204:207], v[48:51], v[72:75]\n\t" \
    "ds_read_b128 v[24:27], v1 offset:0\n\t" \
    "s_waitcnt lgkmcnt(5)\n\t" \
    "v_mfma_f32_16x16x32_bf16 v[76:79], a[204:207], v[52:55], v[76:79]\n\t" \
    "ds_read_b128 v[28:31], v1 offset:18432\n\t" \
    "v_mfma_f32_16x16x32_bf16 v[80:83], v[244:247], v[48:51], v[80:83]\n\t" \
    "s_add_u32 s40, s36, s69\n\t" \
    "s_addc_u32 s41, s37, 0\n\t" \
    "v_mfma_f32_16x16x32_bf16 v[84:87], v[244:247], v[52:55], v[84:87]\n\t" \
    "s_sub_u32 s91, s85, s91\n\t" \
    "s_add_u32 s65, s65, 1\n\t" \
    "s_waitcnt lgkmcnt(5)\n\t" \
    "v_mfma_f32_16x16x32_bf16 v[72:75], a[208:211], v[56:59], v[72:75]\n\t" \
    "ds_read_b128 v[32:35], v2 offset:0\n\t" \
    "s_waitcnt lgkmcnt(5)\n\t" \
    "v_mfma_f32_16x16x32_bf16 v[76:79], a[208:211], v[60:63], v[76:79]\n\t" \
    "ds_read_b128 v[36:39], v2 offset:18432\n\t" \
    "v_mfma_f32_16x16x32_bf16 v[80:83], v[248:251], v[56:59], v[80:83]\n\t" \
    "v_mfma_f32_16x16x32_bf16 v[84:87], v[248:251], v[60:63], v[84:87]\n\t" \
    "s_waitcnt lgkmcnt(5)\n\t" \
    "v_mfma_f32_16x16x32_bf16 v[72:75], a[212:215], v[64:67], v[72:75]\n\t" \
    "ds_read_b128 v[40:43], v1 offset:128\n\t" \
    "s_waitcnt lgkmcnt(5)\n\t" \
    "v_mfma_f32_16x16x32_bf16 v[76:79], a[212:215], v[68:71], v[76:79]\n\t" \
    "ds_read_b128 v[44:47], v1 offset:18560\n\t" \
    "v_mfma_f32_16x16x32_bf16 v[80:83], v[252:255], v[64:67], v[80:83]\n\t" \
    "v_mfma_f32_16x16x32_bf16 v[84:87], v[252:255], v[68:71], v[84:87]\n\t" \
    "s_cmp_lt_u32 s65, 56\n\t" \
    "s_cbranch_scc1 gx_l27_%=\n\t" \
    "v_cvt_pk_bf16_f32 v104, v72, v73\n\t" \
    "v_cvt_pk_bf16_f32 v105, v74, v75\n\t" \
    "s_nop 2\n\t" \
    "v_cvt_pk_bf16_f32 v106, v80, v81\n\t" \
    "v_cvt_pk_bf16_f32 v107, v82, v83\n\t" \
    "global_store_dwordx4 v8, v[104:107], s[40:41] offset:0 sc1\n\t" \
    "v_cvt_pk_bf16_f32 v108, v76, v77\n\t" \
    "v_cvt_pk_bf16_f32 v109, v78, v79\n\t" \
    "v_cvt_pk_bf16_f32 v110, v84, v85\n\t" \
    "v_cvt_pk_bf16_f32 v111, v86, v87\n\t" \
    "global_store_dwordx4 v8, v[108:111], s[40:41] offset:1024 sc1\n\t" \
    "gx_l315_%=:\n\t" \
    "s_add_u32 s84, s65, 2\n\t" \
    "s_min_u32 s69, s84, 63\n\t" \
    "s_mul_i32 s69, s69, 36864\n\t" \
    "s_add_u32 s52, s50, s69\n\t" \
    "s_addc_u32 s53, s51, 0\n\t" \
    "s_add_u32 s90, s90, 36864\n\t" \
    "s_cmp_eq_u32 s90, 147456\n\t" \
    "s_cselect_b32 s90, 0, s90\n\t" \
    "s_add_u32 s91, s90, 36864\n\t" \
    "s_cmp_eq_u32 s91, 147456\n\t" \
    "s_cselect_b32 s91, 0, s91\n\t" \
    "s_add_u32 s91, s91, s89\n\t" \
    "v_add_u32 v22, s90, v0\n\t" \
    "v_xor_b32 v23, 64, v22\n\t" \
    "s_mul_i32 s70, s67, 7\n\t" \
    "s_add_u32 s69, s65, 0\n\t" \
    "s_xor_b32 s69, s69, s70\n\t" \
    "s_and_b32 s69, s69, 63\n\t" \
    "s_lshl_b32 s69, s69, 13\n\t" \
    "s_mul_i32 s70, s67, 524288\n\t" \
    "s_add_u32 s69, s69, s70\n\t" \
    "s_add_u32 s69, s69, 6291456\n\t" \
    "s_add_u32 s38, s36, s69\n\t" \
    "s_addc_u32 s39, s37, 0\n\t" \
    "s_waitcnt lgkmcnt(5)\n\t" \
    "v_mfma_f32_16x16x32_bf16 v[88:91], a[0:3], v[24:27], 0\n\t" \
    "ds_read_b128 v[48:51], v2 offset:128\n\t" \
    "s_waitcnt lgkmcnt(5)\n\t" \
    "v_mfma_f32_16x16x32_bf16 v[92:95], a[0:3], v[28:31], 0\n\t" \
    "ds_read_b128 v[52:55], v2 offset:18560\n\t" \
    "v_mfma_f32_16x16x32_bf16 v[96:99], a[72:75], v[24:27], 0\n\t" \
    "s_add_u32 m0, s91, 0\n\t" \
    "v_mfma_f32_16x16x32_bf16 v[100:103], a[72:75], v[28:31], 0\n\t" \
    "global_load_lds_dwordx4 v203, s[52:53]\n\t" \
    "s_waitcnt lgkmcnt(5)\n\t" \
    "v_mfma_f32_16x16x32_bf16 v[88:91], a[4:7], v[32:35], v[88:91]\n\t" \
    "ds_read_b128 v[56:59], v1 offset:256\n\t" \
    "s_waitcnt lgkmcnt(5)\n\t" \
    "v_mfma_f32_16x16x32_bf16 v[92:95], a[4:7], v[36:39], v[92:95]\n\t" \
    "ds_read_b128 v[60:63], v1 offset:18688\n\t" \
    "v_mfma_f32_16x16x32_bf16 v[96:99], a[76:79], v[32:35], v[96:99]\n\t" \
    "s_add_u32 m0, s91, 1024\n\t" \
    "v_mfma_f32_16x16x32_bf16 v[100:103], a[76:79], v[36:39], v[100:103]\n\t" \
    "global_load_lds_dwordx4 v204, s[52:53]\n\t" \
    "s_waitcnt lgkmcnt(5)\n\t" \
    "v_mfma_f32_16x16x32_bf16 v[88:91], a[8:11], v[40:43], v[88:91]\n\t" \
    "ds_read_b128 v[64:67], v2 offset:256\n\t" \
    "s_waitcnt lgkmcnt(5)\n\t" \
    "v_mfma_f32_16x16x32_bf16 v[92:95], a[8:11], v[44:47], v[92:95]\n\t" \
    "ds_read_b128 v[68:71], v2 offset:18688\n\t" \
    "v_mfma_f32_16x16x32_bf16 v[96:99], a[80:83], v[40:43], v[96:99]\n\t" \
    "s_add_u32 m0, s91, 2048\n\t" \
    "v_mfma_f32_16x16x32_bf16 v[100:103], a[80:83], v[44:47], v[100:103]\n\t" \
    "global_load_lds_dwordx4 v205, s[52:53]\n\t" \
    "s_waitcnt lgkmcnt(5)\n\t" \
    "v_mfma_f32_16x16x32_bf16 v[88:91], a[12:15], v[48:51], v[88:91]\n\t" \
    "ds_read_b128 v[24:27], v1 offset:384\n\t" \
    "s_waitcnt lgkmcnt(5)\n\t" \
    "v_mfma_f32_16x16x32_bf16 v[92:95], a[12:15], v[52:55], v[92:95]\n\t" \
    "ds_read_b128 v[28:31], v1 offset:18816\n\t" \
    "v_mfma_f32_16x16x32_bf16 v[96:99], a[84:87], v[48:51], v[96:99]\n\t" \
    "s_add_u32 m0, s91, 3072\n\t" \
    "v_mfma_f32_16x16x32_bf16 v[100:103], a[84:87], v[52:55], v[100:103]\n\t" \
    "global_load_lds_dwordx4 v206, s[52:53]\n\t" \
    "s_waitcnt lgkmcnt(5)\n\t" \
    "v_mfma_f32_16x16x32_bf16 v[88:91], a[16:19], v[56:59], v[88:91]\n\t" \
    "ds_read_b128 v[32:35], v2 offset:384\n\t" \
    "s_waitcnt lgkmcnt(5)\n\t" \
    "v_mfma_f32_16x16x32_bf16 v[92:95], a[16:19], v[60:63], v[92:95]\n\t" \
    "ds_read_b128 v[36:39], v2 offset:18816\n\t" \
    "v_mfma_f32_16x16x32_bf16 v[96:99], a[88:91], v[56:59], v[96:99]\n\t" \
    "s_add_u32 m0, s91, 4096\n\t" \
    "v_mfma_f32_16x16x32_bf16 v[100:103], a[88:91], v[60:63], v[100:103]\n\t" \
    "global_load_lds_dwordx4 v207, s[52:53]\n\t" \
    "s_waitcnt lgkmcnt(5)\n\t" \
    "v_mfma_f32_16x16x32_bf16 v[88:91], a[20:23], v[64:67], v[88:91]\n\t" \
    "ds_read_b128 v[40:43], v1 offset:512\n\t" \
    "s_waitcnt lgkmcnt(5)\n\t" \
    "v_mfma_f32_16x16x32_bf16 v[92:95], a[20:23], v[68:71], v[92:95]\n\t" \
    "ds_read_b128 v[44:47], v1 offset:18944\n\t" \
    "v_mfma_f32_16x16x32_bf16 v[96:99], a[92:95], v[64:67], v[96:99]\n\t" \
    "s_add_u32 m0, s91, 5120\n\t" \
    "v_mfma_f32_16x16x32_bf16 v[100:103], a[92:95], v[68:71], v[100:103]\n\t" \
    "global_load_lds_dwordx4 v208, s[52:53]\n\t" \
    "s_waitcnt lgkmcnt(5)\n\t" \
    "v_mfma_f32_16x16x32_bf16 v[88:91], a[24:27], v[24:27], v[88:91]\n\t" \
    "ds_read_b128 v[48:51], v2 offset:512\n\t" \
    "s_waitcnt lgkmcnt(5)\n\t" \
    "v_mfma_f32_16x16x32_bf16 v[92:95], a[24:27], v[28:31], v[92:95]\n\t" \
    "ds_read_b128 v[52:55], v2 offset:18944\n\t" \
    "v_mfma_f32_16x16x32_bf16 v[96:99], a[96:99], v[24:27], v[96:99]\n\t" \
    "s_add_u32 m0, s91, 6144\n\t" \
    "v_mfma_f32_16x16x32_bf16 v[100:103], a[96:99], v[28:31], v[100:103]\n\t" \
    "global_load_lds_dwordx4 v209, s[52:53]\n\t" \
    "s_waitcnt lgkmcnt(5)\n\t" \
    "v_mfma_f32_16x16x32_bf16 v[88:91], a[28:31], v[32:35], v[88:91]\n\t" \
    "ds_read_b128 v[56:59], v1 offset:640\n\t" \
    "s_waitcnt lgkmcnt(5)\n\t" \
    "v_mfma_f32_16x16x32_bf16 v[92:95], a[28:31], v[36:39], v[92:95]\n\t" \
    "ds_read_b128 v[60:63], v1 offset:19072\n\t" \
    "v_mfma_f32_16x16x32_bf16 v[96:99], a[100:103], v[32:35], v[96:99]\n\t" \
    "s_add_u32 m0, s91, 7168\n\t" \
    "v_mfma_f32_16x16x32_bf16 v[100:103], a[100:103], v[36:39], v[100:103]\n\t" \
    "global_load_lds_dwordx4 v210, s[52:53]\n\t" \
    "s_waitcnt lgkmcnt(5)\n\t" \
    "v_mfma_f32_16x16x32_bf16 v[88:91], a[32:35], v[40:43], v[88:91]\n\t" \
    "ds_read_b128 v[64:67], v2 offset:640\n\t" \
    "s_waitcnt lgkmcnt(5)\n\t" \
    "v_mfma_f32_16x16x32_bf16 v[92:95], a[32:35], v[44:47], v[92:95]\n\t" \
    "ds_read_b128 v[68:71], v2 offset:19072\n\t" \
    "v_mfma_f32_16x16x32_bf16 v[96:99], a[104:107], v[40:43], v[96:99]\n\t" \
    "s_add_u32 m0, s91, 8192\n\t" \
    "v_mfma_f32_16x16x32_bf16 v[100:103], a[104:107], v[44:47], v[100:103]\n\t" \
    "global_load_lds_dwordx4 v211, s[52:53]\n\t" \
    "s_waitcnt lgkmcnt(5)\n\t" \
    "v_mfma_f32_16x16x32_bf16 v[88:91], a[36:39], v[48:51], v[88:91]\n\t" \
    "ds_read_b128 v[24:27], v1 offset:768\n\t" \
    "s_waitcnt lgkmcnt(5)\n\t" \
    "v_mfma_f32_16x16x32_bf16 v[92:95], a[36:39], v[52:55], v[92:95]\n\t" \
    "ds_read_b128 v[28:31], v1 offset:19200\n\t" \
    "v_mfma_f32_16x16x32_bf16 v[96:99], a[108:111], v[48:51], v[96:99]\n\t" \
    "v_mfma_f32_16x16x32_bf16 v[100:103], a[108:111], v[52:55], v[100:103]\n\t" \
    "s_waitcnt lgkmcnt(5)\n\t" \
    "v_mfma_f32_16x16x32_bf16 v[88:91], a[40:43], v[56:59], v[88:91]\n\t" \
    "ds_read_b128 v[32:35], v2 offset:768\n\t" \
    "s_waitcnt lgkmcnt(5)\n\t" \
    "v_mfma_f32_16x16x32_bf16 v[92:95], a[40:43], v[60:63], v[92:95]\n\t" \
    "ds_read_b128 v[36:39], v2 offset:19200\n\t" \
    "v_mfma_f32_16x16x32_bf16 v[96:99], a[112:115], v[56:59], v[96:99]\n\t" \
    "s_waitcnt vmcnt(9)\n\t" \
    "s_barrier\n\t" \
    "v_mfma_f32_16x16x32_bf16 v[100:103], a[112:115], v[60:63], v[100:103]\n\t" \
    "s_cmp_lg_u32 s66, 0\n\t" \
    "s_cbranch_scc1 gx_SF16_%=\n\t" \
    "s_lshl_b32 s69, s67, 7\n\t" \
    "s_add_u32 s69, s69, 3211264\n\t" \
    "s_add_u32 s70, s65, -1\n\t" \
    "v_mov_b32 v156, s69\n\t" \
    "v_mov_b32 v157, s70\n\t" \
    "s_mov_b64 exec, 1\n\t" \
    "global_store_dword v156, v157, s[100:101] sc1\n\t" \
    "s_mov_b64 exec, -1\n\t" \
    "gx_SF16_%=:\n\t" \
    "s_cmp_lg_u32 s66, 1\n\t" \
    "s_cbranch_scc1 gx_SF17_%=\n\t" \
    "s_lshl_b32 s69, s67, 7\n\t" \
    "s_add_u32 s69, s69, 3149824\n\t" \
    "s_add_u32 s70, s65, 40\n\t" \
    "v_mov_b32 v156, s69\n\t" \
    "v_mov_b32 v157, s70\n\t" \
    "s_mov_b64 exec, 1\n\t" \
    "global_store_dword v156, v157, s[100:101] sc1\n\t" \
    "s_mov_b64 exec, -1\n\t" \
    "gx_SF17_%=:\n\t" \
    "s_waitcnt lgkmcnt(5)\n\t" \
    "v_mfma_f32_16x16x32_bf16 v[88:91], a[44:47], v[64:67], v[88:91]\n\t" \
    "ds_read_b128 v[40:43], v1 offset:896\n\t" \
    "s_waitcnt lgkmcnt(5)\n\t" \
    "v_mfma_f32_16x16x32_bf16 v[92:95], a[44:47], v[68:71], v[92:95]\n\t" \
    "ds_read_b128 v[44:47], v1 offset:19328\n\t" \
    "v_mfma_f32_16x16x32_bf16 v[96:99], a[116:119], v[64:67], v[96:99]\n\t" \
    "v_mfma_f32_16x16x32_bf16 v[100:103], a[116:119], v[68:71], v[100:103]\n\t" \
    "s_waitcnt lgkmcnt(5)\n\t" \
    "v_mfma_f32_16x16x32_bf16 v[88:91], a[48:51], v[24:27], v[88:91]\n\t" \
    "ds_read_b128 v[48:51], v2 offset:896\n\t" \
    "s_waitcnt lgkmcnt(5)\n\t" \
    "v_mfma_f32_16x16x32_bf16 v[92:95], a[48:51], v[28:31], v[92:95]\n\t" \
    "ds_read_b128 v[52:55], v2 offset:19328\n\t" \
    "v_mfma_f32_16x16x32_bf16 v[96:99], a[120:123], v[24:27], v[96:99]\n\t" \
    "v_mfma_f32_16x16x32_bf16 v[100:103], a[120:123], v[28:31], v[100:103]\n\t" \
    "s_waitcnt lgkmcnt(5)\n\t" \
    "v_mfma_f32_16x16x32_bf16 v[88:91], a[52:55], v[32:35], v[88:91]\n\t" \
    "ds_read_b128 v[56:59], v1 offset:1024\n\t" \
    "s_waitcnt lgkmcnt(5)\n\t" \
    "v_mfma_f32_16x16x32_bf16 v[92:95], a[52:55], v[36:39], v[92:95]\n\t" \
    "ds_read_b128 v[60:63], v1 offset:19456\n\t" \
    "v_mfma_f32_16x16x32_bf16 v[96:99], a[124:127], v[32:35], v[96:99]\n\t" \
    "v_mfma_f32_16x16x32_bf16 v[100:103], a[124:127], v[36:39], v[100:103]\n\t" \
    "s_waitcnt lgkmcnt(5)\n\t" \
    "v_mfma_f32_16x16x32_bf16 v[88:91], a[56:59], v[40:43], v[88:91]\n\t" \
    "ds_read_b128 v[64:67], v2 offset:1024\n\t" \
    "s_waitcnt lgkmcnt(5)\n\t" \
    "v_mfma_f32_16x16x32_bf16 v[92:95], a[56:59], v[44:47], v[92:95]\n\t" \
    "ds_read_b128 v[68:71], v2 offset:19456\n\t" \
    "v_mfma_f32_16x16x32_bf16 v[96:99], a[128:131], v[40:43], v[96:99]\n\t" \
    "v_mfma_f32_16x16x32_bf16 v[100:103], a[128:131], v[44:47], v[100:103]\n\t" \
    "s_waitcnt lgkmcnt(5)\n\t" \
    "v_mfma_f32_16x16x32_bf16 v[88:91], a[60:63], v[48:51], v[88:91]\n\t" \
    "ds_read_b128 v[24:27], v22 offset:0\n\t" \
    "s_waitcnt lgkmcnt(5)\n\t" \
    "v_mfma_f32_16x16x32_bf16 v[92:95], a[60:63], v[52:55], v[92:95]\n\t" \
    "ds_read_b128 v[28:31], v22 offset:18432\n\t" \
    "v_mfma_f32_16x16x32_bf16 v[96:99], a[132:135], v[48:51], v[96:99]\n\t" \
    "v_mfma_f32_16x16x32_bf16 v[100:103], a[132:135], v[52:55], v[100:103]\n\t" \
    "s_waitcnt lgkmcnt(5)\n\t" \
    "v_mfma_f32_16x16x32_bf16 v[88:91], a[64:67], v[56:59], v[88:91]\n\t" \
    "ds_read_b128 v[32:35], v23 offset:0\n\t" \
    "s_waitcnt lgkmcnt(5)\n\t" \
    "v_mfma_f32_16x16x32_bf16 v[92:95], a[64:67], v[60:63], v[92:95]\n\t" \
    "ds_read_b128 v[36:39], v23 offset:18432\n\t" \
    "v_mfma_f32_16x16x32_bf16 v[96:99], a[136:139], v[56:59], v[96:99]\n\t" \
    "v_mfma_f32_16x16x32_bf16 v[100:103], a[136:139], v[60:63], v[100:103]\n\t" \
    "s_waitcnt lgkmcnt(5)\n\t" \
    "v_mfma_f32_16x16x32_bf16 v[88:91], a[68:71], v[64:67], v[88:91]\n\t" \
    "ds_read_b128 v[40:43], v22 offset:128\n\t" \
    "s_waitcnt lgkmcnt(5)\n\t" \
    "v_mfma_f32_16x16x32_bf16 v[92:95], a[68:71], v[68:71], v[92:95]\n\t" \
    "ds_read_b128 v[44:47], v22 offset:18560\n\t" \
    "v_mfma_f32_16x16x32_bf16 v[96:99], a[140:143], v[64:67], v[96:99]\n\t" \
    "v_mfma_f32_16x16x32_bf16 v[100:103], a[140:143], v[68:71], v[100:103]\n\t" \
    "v_mov_b32 v1, v22\n\t" \
    "v_mov_b32 v2, v23\n\t" \
    "v_cvt_pk_bf16_f32 v104, v88, v89\n\t" \
    "v_cvt_pk_bf16_f32 v105, v90, v91\n\t" \
    "s_nop 2\n\t" \
    "v_cvt_pk_bf16_f32 v106, v96, v97\n\t" \
    "v_cvt_pk_bf16_f32 v107, v98, v99\n\t" \
    "global_store_dwordx4 v8, v[104:107], s[38:39] offset:0 sc1\n\t" \
    "v_cvt_pk_bf16_f32 v108, v92, v93\n\t" \
    "v_cvt_pk_bf16_f32 v109, v94, v95\n\t" \
    "v_cvt_pk_bf16_f32 v110, v100, v101\n\t" \
    "v_cvt_pk_bf16_f32 v111, v102, v103\n\t" \
    "global_store_dwordx4 v8, v[108:111], s[38:39] offset:1024 sc1\n\t" \
    "s_add_u32 s65, s65, 1\n\t" \
    "s_cmp_lt_u32 s65, 64\n\t" \
    "s_cbranch_scc1 gx_l315_%=\n\t" \
    "s_waitcnt vmcnt(0) lgkmcnt(0)\n\t" \
    "s_barrier\n\t" \
    "s_cmp_lg_u32 s66, 0\n\t" \
    "s_cbranch_scc1 gx_SF18_%=\n\t" \
    "s_lshl_b32 s69, s67, 7\n\t" \
    "s_add_u32 s69, s69, 3211264\n\t" \
    "s_add_u32 s70, s65, -1\n\t" \
    "v_mov_b32 v156, s69\n\t" \
    "v_mov_b32 v157, s70\n\t" \
    "s_mov_b64 exec, 1\n\t" \
    "global_store_dword v156, v157, s[100:101] sc1\n\t" \
    "s_mov_b64 exec, -1\n\t" \
    "gx_SF18_%=:\n\t" \
    "gx_last_%=:\n\t" \
    ""
#define ASM_GV \
    "s_load_dwordx2 s[42:43], %3, 56\n\t" \
    "s_load_dwordx2 s[44:45], %3, 64\n\t" \
    "s_load_dwordx2 s[36:37], %3, 0x58\n\t" \
    "s_load_dwordx2 s[50:51], %3, 0x48\n\t" \
    "s_mov_b32 s66, %4\n\t" \
    "s_mov_b32 s67, %5\n\t" \
    "s_mov_b32 s68, 262144\n\t" \
    "s_mov_b32 s72, 0\n\t" \
    "s_mov_b32 s83, -1\n\t" \
    "s_mov_b32 s65, 0\n\t" \
    "s_lshl_b32 s82, s66, 10\n\t" \
    "s_waitcnt lgkmcnt(0)\n\t" \
    "s_lshr_b32 s65, s67, 5\n\t" \
    "s_cselect_b32 s88, 24, 64\n\t" \
    "s_cmp_lt_u32 s65, 24\n\t" \
    "s_addc_u32 s84, s65, 1\n\t" \
    "s_cmp_lt_u32 s84, 24\n\t" \
    "s_addc_u32 s85, s84, 1\n\t" \
    "s_mov_b32 s86, -1\n\t" \
    "s_mov_b32 s87, -1\n\t" \
    "s_add_u32 s38, s36, 48234496\n\t" \
    "s_addc_u32 s39, s37, 0\n\t" \
    "s_and_b32 s69, s67, 31\n\t" \
    "s_mul_i32 s69, s69, 147456\n\t" \
    "s_add_u32 s44, s44, s69\n\t" \
    "s_addc_u32 s45, s45, 0\n\t" \
    "s_mul_i32 s89, s66, 9216\n\t" \
    "s_mov_b32 s90, 0\n\t" \
    "v_mov_b32 v196, -1\n\t" \
    "v_mov_b32 v197, -1\n\t" \
    "v_mov_b32 v198, -1\n\t" \
    "v_mov_b32 v199, -1\n\t" \
    "s_add_u32 s46, s42, 2097152\n\t" \
    "s_addc_u32 s47, s43, 0\n\t" \
    "s_add_u32 m0, s82, 65536\n\t" \
    "s_nop 0\n\t" \
    "global_load_lds_dwordx4 v200, s[46:47]\n\t" \
    "s_add_u32 s46, s42, 2228224\n\t" \
    "s_addc_u32 s47, s43, 0\n\t" \
    "s_add_u32 m0, s82, 69632\n\t" \
    "s_nop 0\n\t" \
    "global_load_lds_dwordx4 v200, s[46:47]\n\t" \
    "s_add_u32 s46, s42, 2359296\n\t" \
    "s_addc_u32 s47, s43, 0\n\t" \
    "s_add_u32 m0, s82, 73728\n\t" \
    "s_nop 0\n\t" \
    "global_load_lds_dwordx4 v200, s[46:47]\n\t" \
    "s_add_u32 s46, s42, 2490368\n\t" \
    "s_addc_u32 s47, s43, 0\n\t" \
    "s_add_u32 m0, s82, 77824\n\t" \
    "s_nop 0\n\t" \
    "global_load_lds_dwordx4 v200, s[46:47]\n\t" \
    "s_add_u32 s46, s42, 2621440\n\t" \
    "s_addc_u32 s47, s43, 0\n\t" \
    "s_add_u32 m0, s82, 81920\n\t" \
    "s_nop 0\n\t" \
    "global_load_lds_dwordx4 v200, s[46:47]\n\t" \
    "s_add_u32 s46, s42, 2752512\n\t" \
    "s_addc_u32 s47, s43, 0\n\t" \
    "s_add_u32 m0, s82, 86016\n\t" \
    "s_nop 0\n\t" \
    "global_load_lds_dwordx4 v200, s[46:47]\n\t" \
    "s_add_u32 s46, s42, 2883584\n\t" \
    "s_addc_u32 s47, s43, 0\n\t" \
    "s_add_u32 m0, s82, 90112\n\t" \
    "s_nop 0\n\t" \
    "global_load_lds_dwordx4 v200, s[46:47]\n\t" \
    "s_add_u32 s46, s42, 3014656\n\t" \
    "s_addc_u32 s47, s43, 0\n\t" \
    "s_add_u32 m0, s82, 94208\n\t" \
    "s_nop 0\n\t" \
    "global_load_lds_dwordx4 v200, s[46:47]\n\t" \
    "s_add_u32 s46, s42, 3145728\n\t" \
    "s_addc_u32 s47, s43, 0\n\t" \
    "s_add_u32 m0, s82, 98304\n\t" \
    "s_nop 0\n\t" \
    "global_load_lds_dwordx4 v200, s[46:47]\n\t" \
    "s_add_u32 s46, s42, 3276800\n\t" \
    "s_addc_u32 s47, s43, 0\n\t" \
    "s_add_u32 m0, s82, 102400\n\t" \
    "s_nop 0\n\t" \
    "global_load_lds_dwordx4 v200, s[46:47]\n\t" \
    "s_add_u32 s46, s42, 3407872\n\t" \
    "s_addc_u32 s47, s43, 0\n\t" \
    "s_add_u32 m0, s82, 106496\n\t" \
    "s_nop 0\n\t" \
    "global_load_lds_dwordx4 v200, s[46:47]\n\t" \
    "s_add_u32 s46, s42, 3538944\n\t" \
    "s_addc_u32 s47, s43, 0\n\t" \
    "s_add_u32 m0, s82, 110592\n\t" \
    "s_nop 0\n\t" \
    "global_load_lds_dwordx4 v200, s[46:47]\n\t" \
    "s_add_u32 s46, s42, 3670016\n\t" \
    "s_addc_u32 s47, s43, 0\n\t" \
    "s_add_u32 m0, s82, 114688\n\t" \
    "s_nop 0\n\t" \
    "global_load_lds_dwordx4 v200, s[46:47]\n\t" \
    "s_add_u32 s46, s42, 3801088\n\t" \
    "s_addc_u32 s47, s43, 0\n\t" \
    "s_add_u32 m0, s82, 118784\n\t" \
    "s_nop 0\n\t" \
    "global_load_lds_dwordx4 v200, s[46:47]\n\t" \
    "s_add_u32 s46, s42, 3932160\n\t" \
    "s_addc_u32 s47, s43, 0\n\t" \
    "s_add_u32 m0, s82, 122880\n\t" \
    "s_nop 0\n\t" \
    "global_load_lds_dwordx4 v200, s[46:47]\n\t" \
    "s_add_u32 s46, s42, 4063232\n\t" \
    "s_addc_u32 s47, s43, 0\n\t" \
    "s_add_u32 m0, s82, 126976\n\t" \
    "s_nop 0\n\t" \
    "global_load_lds_dwordx4 v200, s[46:47]\n\t" \
    "s_add_u32 s46, s42, 4194304\n\t" \
    "s_addc_u32 s47, s43, 0\n\t" \
    "s_add_u32 m0, s82, 131072\n\t" \
    "s_nop 0\n\t" \
    "global_load_lds_dwordx4 v200, s[46:47]\n\t" \
    "s_add_u32 s46, s42, 4325376\n\t" \
    "s_addc_u32 s47, s43, 0\n\t" \
    "s_add_u32 m0, s82, 135168\n\t" \
    "s_nop 0\n\t" \
    "global_load_lds_dwordx4 v200, s[46:47]\n\t" \
    "s_add_u32 s46, s42, 4456448\n\t" \
    "s_addc_u32 s47, s43, 0\n\t" \
    "s_add_u32 m0, s82, 139264\n\t" \
    "s_nop 0\n\t" \
    "global_load_lds_dwordx4 v200, s[46:47]\n\t" \
    "s_add_u32 s46, s42, 4587520\n\t" \
    "s_addc_u32 s47, s43, 0\n\t" \
    "s_add_u32 m0, s82, 143360\n\t" \
    "s_nop 0\n\t" \
    "global_load_lds_dwordx4 v200, s[46:47]\n\t" \
    "s_waitcnt vmcnt(32) lgkmcnt(0)\n\t" \
    "s_barrier\n\t" \
    "ds_read_b64_tr_b16 a[0:1], v201 offset:0\n\t" \
    "ds_read_b64_tr_b16 a[2:3], v201 offset:1024\n\t" \
    "ds_read_b64_tr_b16 a[4:5], v201 offset:8192\n\t" \
    "ds_read_b64_tr_b16 a[6:7], v201 offset:9216\n\t" \
    "ds_read_b64_tr_b16 a[72:73], v202 offset:0\n\t" \
    "ds_read_b64_tr_b16 a[74:75], v202 offset:1024\n\t" \
    "ds_read_b64_tr_b16 a[76:77], v202 offset:8192\n\t" \
    "ds_read_b64_tr_b16 a[78:79], v202 offset:9216\n\t" \
    "s_waitcnt vmcnt(28) lgkmcnt(0)\n\t" \
    "s_barrier\n\t" \
    "ds_read_b64_tr_b16 a[8:9], v201 offset:16384\n\t" \
    "ds_read_b64_tr_b16 a[10:11], v201 offset:17408\n\t" \
    "ds_read_b64_tr_b16 a[12:13], v201 offset:24576\n\t" \
    "ds_read_b64_tr_b16 a[14:15], v201 offset:25600\n\t" \
    "ds_read_b64_tr_b16 a[80:81], v202 offset:16384\n\t" \
    "ds_read_b64_tr_b16 a[82:83], v202 offset:17408\n\t" \
    "ds_read_b64_tr_b16 a[84:85], v202 offset:24576\n\t" \
    "ds_read_b64_tr_b16 a[86:87], v202 offset:25600\n\t" \
    "v_mov_b32 v156, 1809408\n\t" \
    "s_mov_b32 s69, 3906560\n\t" \
    "v_writelane_b32 v156, s69, 1\n\t" \
    "s_mov_b32 s69, 6238208\n\t" \
    "v_writelane_b32 v156, s69, 2\n\t" \
    "s_mov_b32 s69, 125541376\n\t" \
    "v_writelane_b32 v156, s69, 3\n\t" \
    "s_mov_b32 s69, 169581568\n\t" \
    "v_writelane_b32 v156, s69, 4\n\t" \
    "s_mov_b32 m0, 155648\n\t" \
    "s_nop 0\n\t" \
    "global_load_lds_dword v156, s[36:37]\n\t" \
    "v_readfirstlane_b32 s69, v8\n\t" \
    "s_nop 3\n\t" \
    "v_writelane_b32 v156, s69, 0\n\t" \
    "v_readfirstlane_b32 s69, v9\n\t" \
    "s_nop 3\n\t" \
    "v_writelane_b32 v156, s69, 1\n\t" \
    "v_readfirstlane_b32 s69, v10\n\t" \
    "s_nop 3\n\t" \
    "v_writelane_b32 v156, s69, 2\n\t" \
    "v_readfirstlane_b32 s69, v11\n\t" \
    "s_nop 3\n\t" \
    "v_writelane_b32 v156, s69, 3\n\t" \
    "v_readfirstlane_b32 s69, v12\n\t" \
    "s_nop 3\n\t" \
    "v_writelane_b32 v156, s69, 4\n\t" \
    "v_readfirstlane_b32 s69, v13\n\t" \
    "s_nop 3\n\t" \
    "v_writelane_b32 v156, s69, 5\n\t" \
    "v_readfirstlane_b32 s69, v14\n\t" \
    "s_nop 3\n\t" \
    "v_writelane_b32 v156, s69, 6\n\t" \
    "v_readfirstlane_b32 s69, v15\n\t" \
    "s_nop 3\n\t" \
    "v_writelane_b32 v156, s69, 7\n\t" \
    "v_readfirstlane_b32 s69, v16\n\t" \
    "s_nop 3\n\t" \
    "v_writelane_b32 v156, s69, 8\n\t" \
    "v_xor_b32 v156, 2064384, v156\n\t" \
    "s_lshl_b32 s69, s65, 8\n\t" \
    "v_add_u32 v156, s69, v156\n\t" \
    "v_mov_b32 v157, 0\n\t" \
    "s_mov_b64 exec, 511\n\t" \
    "global_atomic_or v156, v157, s[38:39] sc1\n\t" \
    "s_mov_b64 exec, -1\n\t" \
    "s_waitcnt vmcnt(26) lgkmcnt(0)\n\t" \
    "s_barrier\n\t" \
    "ds_read_b64_tr_b16 a[16:17], v201 offset:32768\n\t" \
    "ds_read_b64_tr_b16 a[18:19], v201 offset:33792\n\t" \
    "ds_read_b64_tr_b16 a[20:21], v201 offset:40960\n\t" \
    "ds_read_b64_tr_b16 a[22:23], v201 offset:41984\n\t" \
    "ds_read_b64_tr_b16 a[88:89], v202 offset:32768\n\t" \
    "ds_read_b64_tr_b16 a[90:91], v202 offset:33792\n\t" \
    "ds_read_b64_tr_b16 a[92:93], v202 offset:40960\n\t" \
    "ds_read_b64_tr_b16 a[94:95], v202 offset:41984\n\t" \
    "s_waitcnt vmcnt(22) lgkmcnt(0)\n\t" \
    "s_barrier\n\t" \
    "ds_read_b64_tr_b16 a[24:25], v201 offset:49152\n\t" \
    "ds_read_b64_tr_b16 a[26:27], v201 offset:50176\n\t" \
    "ds_read_b64_tr_b16 a[28:29], v201 offset:57344\n\t" \
    "ds_read_b64_tr_b16 a[30:31], v201 offset:58368\n\t" \
    "ds_read_b64_tr_b16 a[96:97], v202 offset:49152\n\t" \
    "ds_read_b64_tr_b16 a[98:99], v202 offset:50176\n\t" \
    "ds_read_b64_tr_b16 a[100:101], v202 offset:57344\n\t" \
    "ds_read_b64_tr_b16 a[102:103], v202 offset:58368\n\t" \
    "s_add_u32 s46, s44, 0\n\t" \
    "s_addc_u32 s47, s45, 0\n\t" \
    "s_mul_i32 s69, s82, 9\n\t" \
    "s_add_u32 m0, s69, 0\n\t" \
    "s_nop 0\n\t" \
    "global_load_lds_dwordx4 v203, s[46:47]\n\t" \
    "s_add_u32 m0, s69, 1024\n\t" \
    "s_nop 0\n\t" \
    "global_load_lds_dwordx4 v204, s[46:47]\n\t" \
    "s_add_u32 m0, s69, 2048\n\t" \
    "s_nop 0\n\t" \
    "global_load_lds_dwordx4 v205, s[46:47]\n\t" \
    "s_add_u32 m0, s69, 3072\n\t" \
    "s_nop 0\n\t" \
    "global_load_lds_dwordx4 v206, s[46:47]\n\t" \
    "s_add_u32 m0, s69, 4096\n\t" \
    "s_nop 0\n\t" \
    "global_load_lds_dwordx4 v207, s[46:47]\n\t" \
    "s_add_u32 m0, s69, 5120\n\t" \
    "s_nop 0\n\t" \
    "global_load_lds_dwordx4 v208, s[46:47]\n\t" \
    "s_add_u32 m0, s69, 6144\n\t" \
    "s_nop 0\n\t" \
    "global_load_lds_dwordx4 v209, s[46:47]\n\t" \
    "s_add_u32 m0, s69, 7168\n\t" \
    "s_nop 0\n\t" \
    "global_load_lds_dwordx4 v210, s[46:47]\n\t" \
    "s_add_u32 m0, s69, 8192\n\t" \
    "s_nop 0\n\t" \
    "global_load_lds_dwordx4 v211, s[46:47]\n\t" \
    "s_min_u32 s69, s65, 63\n\t" \
    "s_mul_i32 s69, s69, 36864\n\t" \
    "s_add_u32 s52, s50, s69\n\t" \
    "s_addc_u32 s53, s51, 0\n\t" \
    "global_load_dwordx4 v[120:123], v203, s[52:53]\n\t" \
    "global_load_dwordx4 v[124:127], v204, s[52:53]\n\t" \
    "global_load_dwordx4 v[128:131], v205, s[52:53]\n\t" \
    "global_load_dwordx4 v[132:135], v206, s[52:53]\n\t" \
    "global_load_dwordx4 v[136:139], v207, s[52:53]\n\t" \
    "global_load_dwordx4 v[140:143], v208, s[52:53]\n\t" \
    "global_load_dwordx4 v[144:147], v209, s[52:53]\n\t" \
    "global_load_dwordx4 v[148:151], v210, s[52:53]\n\t" \
    "global_load_dwordx4 v[152:155], v211, s[52:53]\n\t" \
    "s_waitcnt vmcnt(36) lgkmcnt(0)\n\t" \
    "s_barrier\n\t" \
    "v_add_u32 v201, 0x10000, v201\n\t" \
    "v_add_u32 v202, 0x10000, v202\n\t" \
    "ds_read_b64_tr_b16 a[32:33], v201 offset:0\n\t" \
    "ds_read_b64_tr_b16 a[34:35], v201 offset:1024\n\t" \
    "ds_read_b64_tr_b16 a[36:37], v201 offset:8192\n\t" \
    "ds_read_b64_tr_b16 a[38:39], v201 offset:9216\n\t" \
    "ds_read_b64_tr_b16 a[104:105], v202 offset:0\n\t" \
    "ds_read_b64_tr_b16 a[106:107], v202 offset:1024\n\t" \
    "ds_read_b64_tr_b16 a[108:109], v202 offset:8192\n\t" \
    "ds_read_b64_tr_b16 a[110:111], v202 offset:9216\n\t" \
    "s_waitcnt vmcnt(32) lgkmcnt(0)\n\t" \
    "s_barrier\n\t" \
    "ds_read_b64_tr_b16 a[40:41], v201 offset:16384\n\t" \
    "ds_read_b64_tr_b16 a[42:43], v201 offset:17408\n\t" \
    "ds_read_b64_tr_b16 a[44:45], v201 offset:24576\n\t" \
    "ds_read_b64_tr_b16 a[46:47], v201 offset:25600\n\t" \
    "ds_read_b64_tr_b16 a[112:113], v202 offset:16384\n\t" \
    "ds_read_b64_tr_b16 a[114:115], v202 offset:17408\n\t" \
    "ds_read_b64_tr_b16 a[116:117], v202 offset:24576\n\t" \
    "ds_read_b64_tr_b16 a[118:119], v202 offset:25600\n\t" \
    "s_add_u32 s46, s44, 36864\n\t" \
    "s_addc_u32 s47, s45, 0\n\t" \
    "s_mul_i32 s69, s82, 9\n\t" \
    "s_add_u32 m0, s69, 36864\n\t" \
    "s_nop 0\n\t" \
    "global_load_lds_dwordx4 v203, s[46:47]\n\t" \
    "s_add_u32 m0, s69, 37888\n\t" \
    "s_nop 0\n\t" \
    "global_load_lds_dwordx4 v204, s[46:47]\n\t" \
    "s_add_u32 m0, s69, 38912\n\t" \
    "s_nop 0\n\t" \
    "global_load_lds_dwordx4 v205, s[46:47]\n\t" \
    "s_add_u32 m0, s69, 39936\n\t" \
    "s_nop 0\n\t" \
    "global_load_lds_dwordx4 v206, s[46:47]\n\t" \
    "s_add_u32 m0, s69, 40960\n\t" \
    "s_nop 0\n\t" \
    "global_load_lds_dwordx4 v207, s[46:47]\n\t" \
    "s_add_u32 m0, s69, 41984\n\t" \
    "s_nop 0\n\t" \
    "global_load_lds_dwordx4 v208, s[46:47]\n\t" \
    "s_add_u32 m0, s69, 43008\n\t" \
    "s_nop 0\n\t" \
    "global_load_lds_dwordx4 v209, s[46:47]\n\t" \
    "s_add_u32 m0, s69, 44032\n\t" \
    "s_nop 0\n\t" \
    "global_load_lds_dwordx4 v210, s[46:47]\n\t" \
    "s_add_u32 m0, s69, 45056\n\t" \
    "s_nop 0\n\t" \
    "global_load_lds_dwordx4 v211, s[46:47]\n\t" \
    "s_waitcnt vmcnt(37) lgkmcnt(0)\n\t" \
    "s_barrier\n\t" \
    "ds_read_b64_tr_b16 a[48:49], v201 offset:32768\n\t" \
    "ds_read_b64_tr_b16 a[50:51], v201 offset:33792\n\t" \
    "ds_read_b64_tr_b16 a[52:53], v201 offset:40960\n\t" \
    "ds_read_b64_tr_b16 a[54:55], v201 offset:41984\n\t" \
    "ds_read_b64_tr_b16 a[120:121], v202 offset:32768\n\t" \
    "ds_read_b64_tr_b16 a[122:123], v202 offset:33792\n\t" \
    "ds_read_b64_tr_b16 a[124:125], v202 offset:40960\n\t" \
    "ds_read_b64_tr_b16 a[126:127], v202 offset:41984\n\t" \
    "s_waitcnt vmcnt(33) lgkmcnt(0)\n\t" \
    "s_barrier\n\t" \
    "ds_read_b64_tr_b16 a[56:57], v201 offset:49152\n\t" \
    "ds_read_b64_tr_b16 a[58:59], v201 offset:50176\n\t" \
    "ds_read_b64_tr_b16 a[60:61], v201 offset:57344\n\t" \
    "ds_read_b64_tr_b16 a[62:63], v201 offset:58368\n\t" \
    "ds_read_b64_tr_b16 a[128:129], v202 offset:49152\n\t" \
    "ds_read_b64_tr_b16 a[130:131], v202 offset:50176\n\t" \
    "ds_read_b64_tr_b16 a[132:133], v202 offset:57344\n\t" \
    "ds_read_b64_tr_b16 a[134:135], v202 offset:58368\n\t" \
    "s_add_u32 s46, s44, 73728\n\t" \
    "s_addc_u32 s47, s45, 0\n\t" \
    "s_mul_i32 s69, s82, 9\n\t" \
    "s_add_u32 m0, s69, 73728\n\t" \
    "s_nop 0\n\t" \
    "global_load_lds_dwordx4 v203, s[46:47]\n\t" \
    "s_add_u32 m0, s69, 74752\n\t" \
    "s_nop 0\n\t" \
    "global_load_lds_dwordx4 v204, s[46:47]\n\t" \
    "s_add_u32 m0, s69, 75776\n\t" \
    "s_nop 0\n\t" \
    "global_load_lds_dwordx4 v205, s[46:47]\n\t" \
    "s_add_u32 m0, s69, 76800\n\t" \
    "s_nop 0\n\t" \
    "global_load_lds_dwordx4 v206, s[46:47]\n\t" \
    "s_add_u32 m0, s69, 77824\n\t" \
    "s_nop 0\n\t" \
    "global_load_lds_dwordx4 v207, s[46:47]\n\t" \
    "s_add_u32 m0, s69, 78848\n\t" \
    "s_nop 0\n\t" \
    "global_load_lds_dwordx4 v208, s[46:47]\n\t" \
    "s_add_u32 m0, s69, 79872\n\t" \
    "s_nop 0\n\t" \
    "global_load_lds_dwordx4 v209, s[46:47]\n\t" \
    "s_add_u32 m0, s69, 80896\n\t" \
    "s_nop 0\n\t" \
    "global_load_lds_dwordx4 v210, s[46:47]\n\t" \
    "s_add_u32 m0, s69, 81920\n\t" \
    "s_nop 0\n\t" \
    "global_load_lds_dwordx4 v211, s[46:47]\n\t" \
    "v_mbcnt_lo_u32_b32 v164, -1, 0\n\t" \
    "v_mbcnt_hi_u32_b32 v164, -1, v164\n\t" \
    "v_lshlrev_b32 v164, 6, v164\n\t" \
    "v_add_u32 v164, 6225920, v164\n\t" \
    "v_mov_b32 v165, 6234112\n\t" \
    "global_load_dword v165, v165, s[36:37] sc1\n\t" \
    "global_load_dwordx2 v[166:167], v164, s[36:37] sc1\n\t" \
    "global_load_dword v168, v164, s[36:37] offset:2048 sc1\n\t" \
    "s_waitcnt vmcnt(41) lgkmcnt(0)\n\t" \
    "s_barrier\n\t" \
    "v_add_u32 v201, 0x10000, v201\n\t" \
    "v_add_u32 v202, 0x10000, v202\n\t" \
    "ds_read_b64_tr_b16 a[64:65], v201 offset:0\n\t" \
    "ds_read_b64_tr_b16 a[66:67], v201 offset:1024\n\t" \
    "ds_read_b64_tr_b16 a[68:69], v201 offset:8192\n\t" \
    "ds_read_b64_tr_b16 a[70:71], v201 offset:9216\n\t" \
    "ds_read_b64_tr_b16 a[136:137], v202 offset:0\n\t" \
    "ds_read_b64_tr_b16 a[138:139], v202 offset:1024\n\t" \
    "ds_read_b64_tr_b16 a[140:141], v202 offset:8192\n\t" \
    "ds_read_b64_tr_b16 a[142:143], v202 offset:9216\n\t" \
    "v_add_u32 v201, 0xfffe0000, v201\n\t" \
    "v_add_u32 v202, 0xfffe0000, v202\n\t" \
    "gv_jn2_%=:\n\t" \
    "s_waitcnt lgkmcnt(0)\n\t" \
    "s_barrier\n\t" \
    "s_add_u32 s46, s44, 110592\n\t" \
    "s_addc_u32 s47, s45, 0\n\t" \
    "s_mul_i32 s69, s82, 9\n\t" \
    "s_add_u32 m0, s69, 110592\n\t" \
    "s_nop 0\n\t" \
    "global_load_lds_dwordx4 v203, s[46:47]\n\t" \
    "s_add_u32 m0, s69, 111616\n\t" \
    "s_nop 0\n\t" \
    "global_load_lds_dwordx4 v204, s[46:47]\n\t" \
    "s_add_u32 m0, s69, 112640\n\t" \
    "s_nop 0\n\t" \
    "global_load_lds_dwordx4 v205, s[46:47]\n\t" \
    "s_add_u32 m0, s69, 113664\n\t" \
    "s_nop 0\n\t" \
    "global_load_lds_dwordx4 v206, s[46:47]\n\t" \
    "s_add_u32 m0, s69, 114688\n\t" \
    "s_nop 0\n\t" \
    "global_load_lds_dwordx4 v207, s[46:47]\n\t" \
    "s_add_u32 m0, s69, 115712\n\t" \
    "s_nop 0\n\t" \
    "global_load_lds_dwordx4 v208, s[46:47]\n\t" \
    "s_add_u32 m0, s69, 116736\n\t" \
    "s_nop 0\n\t" \
    "global_load_lds_dwordx4 v209, s[46:47]\n\t" \
    "s_add_u32 m0, s69, 117760\n\t" \
    "s_nop 0\n\t" \
    "global_load_lds_dwordx4 v210, s[46:47]\n\t" \
    "s_add_u32 m0, s69, 118784\n\t" \
    "s_nop 0\n\t" \
    "global_load_lds_dwordx4 v211, s[46:47]\n\t" \
    "s_waitcnt vmcnt(39) lgkmcnt(0)\n\t" \
    "s_barrier\n\t" \
    "ds_read_b64_tr_b16 a[144:145], v212 offset:0\n\t" \
    "ds_read_b64_tr_b16 a[146:147], v212 offset:18432\n\t" \
    "ds_read_b64_tr_b16 a[160:161], v213 offset:0\n\t" \
    "ds_read_b64_tr_b16 a[162:163], v213 offset:18432\n\t" \
    "ds_read_b64_tr_b16 a[176:177], v214 offset:0\n\t" \
    "ds_read_b64_tr_b16 a[178:179], v214 offset:18432\n\t" \
    "s_waitcnt lgkmcnt(6)\n\t" \
    "ds_read_b64_tr_b16 a[192:193], v215 offset:0\n\t" \
    "ds_read_b64_tr_b16 a[194:195], v215 offset:18432\n\t" \
    "ds_read_b64_tr_b16 a[208:209], v212 offset:128\n\t" \
    "ds_read_b64_tr_b16 a[210:211], v212 offset:18560\n\t" \
    "ds_read_b64_tr_b16 a[224:225], v213 offset:128\n\t" \
    "ds_read_b64_tr_b16 a[226:227], v213 offset:18560\n\t" \
    "s_waitcnt lgkmcnt(6)\n\t" \
    "ds_read_b64_tr_b16 a[240:241], v214 offset:128\n\t" \
    "ds_read_b64_tr_b16 a[242:243], v214 offset:18560\n\t" \
    "ds_read_b64_tr_b16 v[224:225], v215 offset:128\n\t" \
    "ds_read_b64_tr_b16 v[226:227], v215 offset:18560\n\t" \
    "ds_read_b64_tr_b16 v[240:241], v212 offset:256\n\t" \
    "ds_read_b64_tr_b16 v[242:243], v212 offset:18688\n\t" \
    "s_waitcnt lgkmcnt(6)\n\t" \
    "v_add_u32 v212, 0x9000, v212\n\t" \
    "v_add_u32 v213, 0x9000, v213\n\t" \
    "v_add_u32 v214, 0x9000, v214\n\t" \
    "v_add_u32 v215, 0x9000, v215\n\t" \
    "s_waitcnt vmcnt(21) lgkmcnt(0)\n\t" \
    "s_barrier\n\t" \
    "ds_read_b64_tr_b16 a[148:149], v212 offset:0\n\t" \
    "ds_read_b64_tr_b16 a[150:151], v212 offset:18432\n\t" \
    "ds_read_b64_tr_b16 a[164:165], v213 offset:0\n\t" \
    "ds_read_b64_tr_b16 a[166:167], v213 offset:18432\n\t" \
    "ds_read_b64_tr_b16 a[180:181], v214 offset:0\n\t" \
    "ds_read_b64_tr_b16 a[182:183], v214 offset:18432\n\t" \
    "s_waitcnt lgkmcnt(6)\n\t" \
    "ds_read_b64_tr_b16 a[196:197], v215 offset:0\n\t" \
    "ds_read_b64_tr_b16 a[198:199], v215 offset:18432\n\t" \
    "ds_read_b64_tr_b16 a[212:213], v212 offset:128\n\t" \
    "ds_read_b64_tr_b16 a[214:215], v212 offset:18560\n\t" \
    "ds_read_b64_tr_b16 a[228:229], v213 offset:128\n\t" \
    "ds_read_b64_tr_b16 a[230:231], v213 offset:18560\n\t" \
    "s_waitcnt lgkmcnt(6)\n\t" \
    "ds_read_b64_tr_b16 a[244:245], v214 offset:128\n\t" \
    "ds_read_b64_tr_b16 a[246:247], v214 offset:18560\n\t" \
    "ds_read_b64_tr_b16 v[228:229], v215 offset:128\n\t" \
    "ds_read_b64_tr_b16 v[230:231], v215 offset:18560\n\t" \
    "ds_read_b64_tr_b16 v[244:245], v212 offset:256\n\t" \
    "ds_read_b64_tr_b16 v[246:247], v212 offset:18688\n\t" \
    "s_waitcnt lgkmcnt(6)\n\t" \
    "v_add_u32 v212, 0x9000, v212\n\t" \
    "v_add_u32 v213, 0x9000, v213\n\t" \
    "v_add_u32 v214, 0x9000, v214\n\t" \
    "v_add_u32 v215, 0x9000, v215\n\t" \
    "ds_write_b128 v3, v[120:123] offset:0\n\t" \
    "ds_write_b128 v3, v[124:127] offset:1024\n\t" \
    "ds_write_b128 v3, v[128:131] offset:2048\n\t" \
    "ds_write_b128 v3, v[132:135] offset:3072\n\t" \
    "ds_write_b128 v3, v[136:139] offset:4096\n\t" \
    "ds_write_b128 v3, v[140:143] offset:5120\n\t" \
    "ds_write_b128 v3, v[144:147] offset:6144\n\t" \
    "ds_write_b128 v3, v[148:151] offset:7168\n\t" \
    "ds_write_b128 v3, v[152:155] offset:8192\n\t" \
    "s_waitcnt vmcnt(12) lgkmcnt(0)\n\t" \
    "s_barrier\n\t" \
    "ds_read_b64_tr_b16 a[152:153], v212 offset:0\n\t" \
    "ds_read_b64_tr_b16 a[154:155], v212 offset:18432\n\t" \
    "ds_read_b64_tr_b16 a[168:169], v213 offset:0\n\t" \
    "ds_read_b64_tr_b16 a[170:171], v213 offset:18432\n\t" \
    "ds_read_b64_tr_b16 a[184:185], v214 offset:0\n\t" \
    "ds_read_b64_tr_b16 a[186:187], v214 offset:18432\n\t" \
    "s_waitcnt lgkmcnt(6)\n\t" \
    "ds_read_b64_tr_b16 a[200:201], v215 offset:0\n\t" \
    "ds_read_b64_tr_b16 a[202:203], v215 offset:18432\n\t" \
    "ds_read_b64_tr_b16 a[216:217], v212 offset:128\n\t" \
    "ds_read_b64_tr_b16 a[218:219], v212 offset:18560\n\t" \
    "ds_read_b64_tr_b16 a[232:233], v213 offset:128\n\t" \
    "ds_read_b64_tr_b16 a[234:235], v213 offset:18560\n\t" \
    "s_waitcnt lgkmcnt(6)\n\t" \
    "ds_read_b64_tr_b16 a[248:249], v214 offset:128\n\t" \
    "ds_read_b64_tr_b16 a[250:251], v214 offset:18560\n\t" \
    "ds_read_b64_tr_b16 v[232:233], v215 offset:128\n\t" \
    "ds_read_b64_tr_b16 v[234:235], v215 offset:18560\n\t" \
    "ds_read_b64_tr_b16 v[248:249], v212 offset:256\n\t" \
    "ds_read_b64_tr_b16 v[250:251], v212 offset:18688\n\t" \
    "s_waitcnt lgkmcnt(6)\n\t" \
    "v_add_u32 v212, 0x9000, v212\n\t" \
    "v_add_u32 v213, 0x9000, v213\n\t" \
    "v_add_u32 v214, 0x9000, v214\n\t" \
    "v_add_u32 v215, 0x9000, v215\n\t" \
    "s_min_u32 s69, s84, 63\n\t" \
    "s_mul_i32 s69, s69, 36864\n\t" \
    "s_add_u32 s52, s50, s69\n\t" \
    "s_addc_u32 s53, s51, 0\n\t" \
    "s_add_u32 m0, s89, 36864\n\t" \
    "s_nop 0\n\t" \
    "global_load_lds_dwordx4 v203, s[52:53]\n\t" \
    "s_add_u32 m0, s89, 37888\n\t" \
    "s_nop 0\n\t" \
    "global_load_lds_dwordx4 v204, s[52:53]\n\t" \
    "s_add_u32 m0, s89, 38912\n\t" \
    "s_nop 0\n\t" \
    "global_load_lds_dwordx4 v205, s[52:53]\n\t" \
    "s_add_u32 m0, s89, 39936\n\t" \
    "s_nop 0\n\t" \
    "global_load_lds_dwordx4 v206, s[52:53]\n\t" \
    "s_add_u32 m0, s89, 40960\n\t" \
    "s_nop 0\n\t" \
    "global_load_lds_dwordx4 v207, s[52:53]\n\t" \
    "s_add_u32 m0, s89, 41984\n\t" \
    "s_nop 0\n\t" \
    "global_load_lds_dwordx4 v208, s[52:53]\n\t" \
    "s_add_u32 m0, s89, 43008\n\t" \
    "s_nop 0\n\t" \
    "global_load_lds_dwordx4 v209, s[52:53]\n\t" \
    "s_add_u32 m0, s89, 44032\n\t" \
    "s_nop 0\n\t" \
    "global_load_lds_dwordx4 v210, s[52:53]\n\t" \
    "s_add_u32 m0, s89, 45056\n\t" \
    "s_nop 0\n\t" \
    "global_load_lds_dwordx4 v211, s[52:53]\n\t" \
    "s_waitcnt vmcnt(18)\n\t" \
    "gv_C3_%=:\n\t" \
    "v_cmp_ne_u32 s[54:55], v166, v168\n\t" \
    "v_sub_u32 v166, v166, v165\n\t" \
    "v_cmp_ne_u32 vcc, 1, v166\n\t" \
    "s_nop 1\n\t" \
    "s_or_b64 vcc, vcc, s[54:55]\n\t" \
    "s_cbranch_scc0 gv_Cok4_%=\n\t" \
    "s_cmp_eq_u32 s68, 0\n\t" \
    "s_cbranch_scc1 gv_F6_%=\n\t" \
    "s_sub_u32 s68, s68, 1\n\t" \
    "s_sleep 6\n\t" \
    "s_branch gv_Cag5_%=\n\t" \
    "gv_F6_%=:\n\t" \
    "v_mov_b32 v168, 5767424\n\t" \
    "v_mov_b32 v169, 4\n\t" \
    "global_store_dword v168, v169, s[36:37] sc1\n\t" \
    "s_waitcnt vmcnt(0)\n\t" \
    "s_branch gv_Cok4_%=\n\t" \
    "gv_Cag5_%=:\n\t" \
    "global_load_dwordx2 v[166:167], v164, s[36:37] sc1\n\t" \
    "global_load_dword v168, v164, s[36:37] offset:2048 sc1\n\t" \
    "s_waitcnt vmcnt(0)\n\t" \
    "s_branch gv_C3_%=\n\t" \
    "gv_Cok4_%=:\n\t" \
    "v_readlane_b32 s69, v167, 2\n\t" \
    "v_readlane_b32 s70, v167, 3\n\t" \
    "s_nop 1\n\t" \
    "s_cmp_lt_u32 s70, s69\n\t" \
    "s_cselect_b32 s69, 0, 8192\n\t" \
    "s_add_u32 s100, s36, s69\n\t" \
    "s_addc_u32 s101, s37, 0\n\t" \
    "s_cmp_lg_u32 s66, 0\n\t" \
    "s_cbranch_scc1 gv_Ca7_%=\n\t" \
    "v_mov_b32 v164, 5768192\n\t" \
    "s_mov_b64 exec, 1\n\t" \
    "global_atomic_add v164, v166, s[36:37] sc1\n\t" \
    "s_mov_b64 exec, -1\n\t" \
    "gv_Ca7_%=:\n\t" \
    "v_mov_b32_dpp v164, v167 quad_perm:[1,0,3,2] row_mask:0xf bank_mask:0xf\n\t" \
    "v_cmp_lt_u32 s[44:45], v164, v167\n\t" \
    "v_min_u32 v164, v164, v167\n\t" \
    "v_and_b32 v165, 1, v164\n\t" \
    "v_cmp_ne_u32 vcc, 0, v165\n\t" \
    "s_nop 1\n\t" \
    "s_and_b32 s44, s44, 0x55555555\n\t" \
    "s_and_b32 s69, vcc_lo, 0xaaaaaaaa\n\t" \
    "s_or_b32 s44, s44, s69\n\t" \
    "s_and_b32 s45, s45, 0x55555555\n\t" \
    "s_and_b32 s69, vcc_hi, 0xaaaaaaaa\n\t" \
    "s_or_b32 s45, s45, s69\n\t" \
    "s_waitcnt lgkmcnt(0)\n\t" \
    "s_barrier\n\t" \
    "ds_read_b128 v[24:27], v1 offset:0\n\t" \
    "ds_read_b128 v[28:31], v1 offset:18432\n\t" \
    "ds_read_b128 v[32:35], v2 offset:0\n\t" \
    "ds_read_b128 v[36:39], v2 offset:18432\n\t" \
    "ds_read_b128 v[40:43], v1 offset:128\n\t" \
    "ds_read_b128 v[44:47], v1 offset:18560\n\t" \
    "s_mov_b64 vcc, 0\n\t" \
    "s_min_u32 s69, s85, 63\n\t" \
    "s_mul_i32 s69, s69, 36864\n\t" \
    "s_add_u32 s52, s50, s69\n\t" \
    "s_addc_u32 s53, s51, 0\n\t" \
    "s_add_u32 s90, s90, 36864\n\t" \
    "s_cmp_eq_u32 s90, 110592\n\t" \
    "s_cselect_b32 s90, 0, s90\n\t" \
    "s_add_u32 s91, s90, 36864\n\t" \
    "s_cmp_eq_u32 s91, 110592\n\t" \
    "s_cselect_b32 s91, 0, s91\n\t" \
    "s_add_u32 s91, s91, s89\n\t" \
    ".p2align 6\n\t" \
    "s_nop 0\n\t" \
    "s_nop 0\n\t" \
    "gv_loop8_%=:\n\t" \
    "s_waitcnt lgkmcnt(5)\n\t" \
    "v_mfma_f32_16x16x32_bf16 v[72:75], a[0:3], v[24:27], 0\n\t" \
    "ds_read_b128 v[48:51], v2 offset:128\n\t" \
    "s_lshl_b32 s40, s65, 15\n\t" \
    "s_cselect_b32 s70, s65, 64\n\t" \
    "s_waitcnt lgkmcnt(5)\n\t" \
    "v_mfma_f32_16x16x32_bf16 v[76:79], a[0:3], v[28:31], 0\n\t" \
    "ds_read_b128 v[52:55], v2 offset:18560\n\t" \
    "s_mul_i32 s69, s70, 171\n\t" \
    "v_mfma_f32_16x16x32_bf16 v[80:83], a[72:75], v[24:27], 0\n\t" \
    "s_add_u32 m0, s91, 0\n\t" \
    "s_lshr_b32 s69, s69, 9\n\t" \
    "v_mfma_f32_16x16x32_bf16 v[84:87], a[72:75], v[28:31], 0\n\t" \
    "global_load_lds_dwordx4 v203, s[52:53]\n\t" \
    "s_waitcnt lgkmcnt(5)\n\t" \
    "v_mfma_f32_16x16x32_bf16 v[72:75], a[4:7], v[32:35], v[72:75]\n\t" \
    "ds_read_b128 v[56:59], v1 offset:256\n\t" \
    "s_mul_i32 s71, s69, 3\n\t" \
    "s_waitcnt lgkmcnt(5)\n\t" \
    "v_mfma_f32_16x16x32_bf16 v[76:79], a[4:7], v[36:39], v[76:79]\n\t" \
    "ds_read_b128 v[60:63], v1 offset:18688\n\t" \
    "s_sub_u32 s70, s70, s71\n\t" \
    "v_mfma_f32_16x16x32_bf16 v[80:83], a[76:79], v[32:35], v[80:83]\n\t" \
    "s_add_u32 m0, s91, 1024\n\t" \
    "s_mul_i32 s70, s70, 589824\n\t" \
    "v_mfma_f32_16x16x32_bf16 v[84:87], a[76:79], v[36:39], v[84:87]\n\t" \
    "global_load_lds_dwordx4 v204, s[52:53]\n\t" \
    "s_waitcnt lgkmcnt(5)\n\t" \
    "v_mfma_f32_16x16x32_bf16 v[72:75], a[8:11], v[40:43], v[72:75]\n\t" \
    "ds_read_b128 v[64:67], v2 offset:256\n\t" \
    "s_lshl1_add_u32 s71, s69, 4\n\t" \
    "s_waitcnt lgkmcnt(5)\n\t" \
    "v_mfma_f32_16x16x32_bf16 v[76:79], a[8:11], v[44:47], v[76:79]\n\t" \
    "ds_read_b128 v[68:71], v2 offset:18688\n\t" \
    "s_lshr_b64 s[48:49], s[44:45], s71\n\t" \
    "v_mfma_f32_16x16x32_bf16 v[80:83], a[80:83], v[40:43], v[80:83]\n\t" \
    "s_add_u32 m0, s91, 2048\n\t" \
    "s_lshl_b32 s71, s66, 1\n\t" \
    "v_mfma_f32_16x16x32_bf16 v[84:87], a[80:83], v[44:47], v[84:87]\n\t" \
    "global_load_lds_dwordx4 v205, s[52:53]\n\t" \
    "s_waitcnt lgkmcnt(5)\n\t" \
    "v_mfma_f32_16x16x32_bf16 v[72:75], a[12:15], v[48:51], v[72:75]\n\t" \
    "ds_read_b128 v[24:27], v1 offset:384\n\t" \
    "s_xor_b32 s71, s71, s48\n\t" \
    "s_waitcnt lgkmcnt(5)\n\t" \
    "v_mfma_f32_16x16x32_bf16 v[76:79], a[12:15], v[52:55], v[76:79]\n\t" \
    "ds_read_b128 v[28:31], v1 offset:18816\n\t" \
    "s_and_b32 s71, s71, 3\n\t" \
    "v_mfma_f32_16x16x32_bf16 v[80:83], a[84:87], v[48:51], v[80:83]\n\t" \
    "s_add_u32 m0, s91, 3072\n\t" \
    "s_lshl_b32 s71, s71, 13\n\t" \
    "v_mfma_f32_16x16x32_bf16 v[84:87], a[84:87], v[52:55], v[84:87]\n\t" \
    "global_load_lds_dwordx4 v206, s[52:53]\n\t" \
    "s_waitcnt lgkmcnt(5)\n\t" \
    "v_mfma_f32_16x16x32_bf16 v[72:75], a[16:19], v[56:59], v[72:75]\n\t" \
    "ds_read_b128 v[32:35], v2 offset:384\n\t" \
    "s_add_u32 s70, s70, s71\n\t" \
    "s_waitcnt lgkmcnt(5)\n\t" \
    "v_mfma_f32_16x16x32_bf16 v[76:79], a[16:19], v[60:63], v[76:79]\n\t" \
    "ds_read_b128 v[36:39], v2 offset:18816\n\t" \
    "s_lshl_b32 s69, s69, 21\n\t" \
    "v_mfma_f32_16x16x32_bf16 v[80:83], a[88:91], v[56:59], v[80:83]\n\t" \
    "s_add_u32 m0, s91, 4096\n\t" \
    "s_add_u32 s69, s69, 123731968\n\t" \
    "v_mfma_f32_16x16x32_bf16 v[84:87], a[88:91], v[60:63], v[84:87]\n\t" \
    "global_load_lds_dwordx4 v207, s[52:53]\n\t" \
    "s_waitcnt lgkmcnt(5)\n\t" \
    "v_mfma_f32_16x16x32_bf16 v[72:75], a[20:23], v[64:67], v[72:75]\n\t" \
    "ds_read_b128 v[40:43], v1 offset:512\n\t" \
    "v_add_u32 v153, s70, v19\n\t" \
    "s_waitcnt lgkmcnt(5)\n\t" \
    "v_mfma_f32_16x16x32_bf16 v[76:79], a[20:23], v[68:71], v[76:79]\n\t" \
    "ds_read_b128 v[44:47], v1 offset:18944\n\t" \
    "v_and_b32 v152, 0x80000, v153\n\t" \
    "v_mfma_f32_16x16x32_bf16 v[80:83], a[92:95], v[64:67], v[80:83]\n\t" \
    "s_add_u32 m0, s91, 5120\n\t" \
    "v_lshrrev_b32 v152, 5, v152\n\t" \
    "v_mfma_f32_16x16x32_bf16 v[84:87], a[92:95], v[68:71], v[84:87]\n\t" \
    "global_load_lds_dwordx4 v208, s[52:53]\n\t" \
    "s_waitcnt lgkmcnt(5)\n\t" \
    "v_mfma_f32_16x16x32_bf16 v[72:75], a[24:27], v[24:27], v[72:75]\n\t" \
    "ds_read_b128 v[48:51], v2 offset:512\n\t" \
    "v_xor_b32 v153, v153, v152\n\t" \
    "s_waitcnt lgkmcnt(5)\n\t" \
    "v_mfma_f32_16x16x32_bf16 v[76:79], a[24:27], v[28:31], v[76:79]\n\t" \
    "ds_read_b128 v[52:55], v2 offset:18944\n\t" \
    "v_add_u32 v154, s70, v20\n\t" \
    "v_mfma_f32_16x16x32_bf16 v[80:83], a[96:99], v[24:27], v[80:83]\n\t" \
    "s_add_u32 m0, s91, 6144\n\t" \
    "v_and_b32 v152, 0x80000, v154\n\t" \
    "v_mfma_f32_16x16x32_bf16 v[84:87], a[96:99], v[28:31], v[84:87]\n\t" \
    "global_load_lds_dwordx4 v209, s[52:53]\n\t" \
    "s_waitcnt lgkmcnt(5)\n\t" \
    "v_mfma_f32_16x16x32_bf16 v[72:75], a[28:31], v[32:35], v[72:75]\n\t" \
    "ds_read_b128 v[56:59], v1 offset:640\n\t" \
    "v_lshrrev_b32 v152, 5, v152\n\t" \
    "s_waitcnt lgkmcnt(5)\n\t" \
    "v_mfma_f32_16x16x32_bf16 v[76:79], a[28:31], v[36:39], v[76:79]\n\t" \
    "ds_read_b128 v[60:63], v1 offset:19072\n\t" \
    "v_xor_b32 v154, v154, v152\n\t" \
    "v_mfma_f32_16x16x32_bf16 v[80:83], a[100:103], v[32:35], v[80:83]\n\t" \
    "s_add_u32 m0, s91, 7168\n\t" \
    "v_add_u32 v155, s70, v21\n\t" \
    "v_mfma_f32_16x16x32_bf16 v[84:87], a[100:103], v[36:39], v[84:87]\n\t" \
    "global_load_lds_dwordx4 v210, s[52:53]\n\t" \
    "s_waitcnt lgkmcnt(5)\n\t" \
    "v_mfma_f32_16x16x32_bf16 v[72:75], a[32:35], v[40:43], v[72:75]\n\t" \
    "ds_read_b128 v[64:67], v2 offset:640\n\t" \
    "v_and_b32 v152, 0x80000, v155\n\t" \
    "s_waitcnt lgkmcnt(5)\n\t" \
    "v_mfma_f32_16x16x32_bf16 v[76:79], a[32:35], v[44:47], v[76:79]\n\t" \
    "ds_read_b128 v[68:71], v2 offset:19072\n\t" \
    "v_lshrrev_b32 v152, 5, v152\n\t" \
    "v_mfma_f32_16x16x32_bf16 v[80:83], a[104:107], v[40:43], v[80:83]\n\t" \
    "s_add_u32 m0, s91, 8192\n\t" \
    "v_xor_b32 v155, v155, v152\n\t" \
    "v_mfma_f32_16x16x32_bf16 v[84:87], a[104:107], v[44:47], v[84:87]\n\t" \
    "global_load_lds_dwordx4 v211, s[52:53]\n\t" \
    "s_waitcnt lgkmcnt(5)\n\t" \
    "v_mfma_f32_16x16x32_bf16 v[72:75], a[36:39], v[48:51], v[72:75]\n\t" \
    "ds_read_b128 v[24:27], v1 offset:768\n\t" \
    "s_add_u32 s48, s36, s69\n\t" \
    "s_addc_u32 s49, s37, 0\n\t" \
    "s_waitcnt lgkmcnt(5)\n\t" \
    "v_mfma_f32_16x16x32_bf16 v[76:79], a[36:39], v[52:55], v[76:79]\n\t" \
    "ds_read_b128 v[28:31], v1 offset:19200\n\t" \
    "v_add_u32 v22, s90, v0\n\t" \
    "v_mfma_f32_16x16x32_bf16 v[80:83], a[108:111], v[48:51], v[80:83]\n\t" \
    "v_xor_b32 v23, 64, v22\n\t" \
    "v_mfma_f32_16x16x32_bf16 v[84:87], a[108:111], v[52:55], v[84:87]\n\t" \
    "s_waitcnt lgkmcnt(5)\n\t" \
    "v_mfma_f32_16x16x32_bf16 v[72:75], a[40:43], v[56:59], v[72:75]\n\t" \
    "ds_read_b128 v[32:35], v2 offset:768\n\t" \
    "s_waitcnt lgkmcnt(5)\n\t" \
    "v_mfma_f32_16x16x32_bf16 v[76:79], a[40:43], v[60:63], v[76:79]\n\t" \
    "ds_read_b128 v[36:39], v2 offset:19200\n\t" \
    "v_mfma_f32_16x16x32_bf16 v[80:83], a[112:115], v[56:59], v[80:83]\n\t" \
    "v_mfma_f32_16x16x32_bf16 v[84:87], a[112:115], v[60:63], v[84:87]\n\t" \
    "s_waitcnt lgkmcnt(5)\n\t" \
    "v_mfma_f32_16x16x32_bf16 v[72:75], a[44:47], v[64:67], v[72:75]\n\t" \
    "ds_read_b128 v[40:43], v1 offset:896\n\t" \
    "s_waitcnt lgkmcnt(5)\n\t" \
    "v_mfma_f32_16x16x32_bf16 v[76:79], a[44:47], v[68:71], v[76:79]\n\t" \
    "ds_read_b128 v[44:47], v1 offset:19328\n\t" \
    "v_mfma_f32_16x16x32_bf16 v[80:83], a[116:119], v[64:67], v[80:83]\n\t" \
    "v_mfma_f32_16x16x32_bf16 v[84:87], a[116:119], v[68:71], v[84:87]\n\t" \
    "global_store_dwordx4 v153, v[196:199], s[48:49] sc1\n\t" \
    "s_waitcnt lgkmcnt(5)\n\t" \
    "v_mfma_f32_16x16x32_bf16 v[72:75], a[48:51], v[24:27], v[72:75]\n\t" \
    "ds_read_b128 v[48:51], v2 offset:896\n\t" \
    "s_waitcnt lgkmcnt(5)\n\t" \
    "v_mfma_f32_16x16x32_bf16 v[76:79], a[48:51], v[28:31], v[76:79]\n\t" \
    "ds_read_b128 v[52:55], v2 offset:19328\n\t" \
    "v_mfma_f32_16x16x32_bf16 v[80:83], a[120:123], v[24:27], v[80:83]\n\t" \
    "v_mfma_f32_16x16x32_bf16 v[84:87], a[120:123], v[28:31], v[84:87]\n\t" \
    "s_waitcnt lgkmcnt(5)\n\t" \
    "v_mfma_f32_16x16x32_bf16 v[72:75], a[52:55], v[32:35], v[72:75]\n\t" \
    "ds_read_b128 v[56:59], v1 offset:1024\n\t" \
    "s_waitcnt lgkmcnt(5)\n\t" \
    "v_mfma_f32_16x16x32_bf16 v[76:79], a[52:55], v[36:39], v[76:79]\n\t" \
    "ds_read_b128 v[60:63], v1 offset:19456\n\t" \
    "v_mfma_f32_16x16x32_bf16 v[80:83], a[124:127], v[32:35], v[80:83]\n\t" \
    "v_mfma_f32_16x16x32_bf16 v[84:87], a[124:127], v[36:39], v[84:87]\n\t" \
    "s_waitcnt lgkmcnt(5)\n\t" \
    "v_mfma_f32_16x16x32_bf16 v[72:75], a[56:59], v[40:43], v[72:75]\n\t" \
    "ds_read_b128 v[64:67], v2 offset:1024\n\t" \
    "s_waitcnt lgkmcnt(5)\n\t" \
    "v_mfma_f32_16x16x32_bf16 v[76:79], a[56:59], v[44:47], v[76:79]\n\t" \
    "ds_read_b128 v[68:71], v2 offset:19456\n\t" \
    "v_mfma_f32_16x16x32_bf16 v[80:83], a[128:131], v[40:43], v[80:83]\n\t" \
    "v_mfma_f32_16x16x32_bf16 v[84:87], a[128:131], v[44:47], v[84:87]\n\t" \
    "global_store_dwordx4 v154, v[196:199], s[48:49] sc1\n\t" \
    "s_waitcnt lgkmcnt(5)\n\t" \
    "v_mfma_f32_16x16x32_bf16 v[72:75], a[60:63], v[48:51], v[72:75]\n\t" \
    "s_waitcnt lgkmcnt(4)\n\t" \
    "v_mfma_f32_16x16x32_bf16 v[76:79], a[60:63], v[52:55], v[76:79]\n\t" \
    "v_mfma_f32_16x16x32_bf16 v[80:83], a[132:135], v[48:51], v[80:83]\n\t" \
    "v_mfma_f32_16x16x32_bf16 v[84:87], a[132:135], v[52:55], v[84:87]\n\t" \
    "s_waitcnt lgkmcnt(3)\n\t" \
    "v_mfma_f32_16x16x32_bf16 v[72:75], a[64:67], v[56:59], v[72:75]\n\t" \
    "s_waitcnt lgkmcnt(2)\n\t" \
    "v_mfma_f32_16x16x32_bf16 v[76:79], a[64:67], v[60:63], v[76:79]\n\t" \
    "v_mfma_f32_16x16x32_bf16 v[80:83], a[136:139], v[56:59], v[80:83]\n\t" \
    "v_mfma_f32_16x16x32_bf16 v[84:87], a[136:139], v[60:63], v[84:87]\n\t" \
    "s_waitcnt lgkmcnt(1)\n\t" \
    "v_mfma_f32_16x16x32_bf16 v[72:75], a[68:71], v[64:67], v[72:75]\n\t" \
    "s_waitcnt lgkmcnt(0)\n\t" \
    "v_mfma_f32_16x16x32_bf16 v[76:79], a[68:71], v[68:71], v[76:79]\n\t" \
    "v_mfma_f32_16x16x32_bf16 v[80:83], a[140:143], v[64:67], v[80:83]\n\t" \
    "v_mfma_f32_16x16x32_bf16 v[84:87], a[140:143], v[68:71], v[84:87]\n\t" \
    "global_store_dword v155, v196, s[48:49] sc1\n\t" \
    "v_mov_b32 v1, v22\n\t" \
    "v_mov_b32 v2, v23\n\t" \
    "s_nop 0\n\t" \
    "v_cvt_pk_bf16_f32 v104, v72, v73\n\t" \
    "v_cvt_pk_bf16_f32 v105, v74, v75\n\t" \
    "s_nop 0\n\t" \
    "v_cvt_pk_bf16_f32 v106, v80, v81\n\t" \
    "v_cvt_pk_bf16_f32 v107, v82, v83\n\t" \
    "ds_write_b128 v4, v[104:107] offset:0\n\t" \
    "v_cvt_pk_bf16_f32 v108, v76, v77\n\t" \
    "v_cvt_pk_bf16_f32 v109, v78, v79\n\t" \
    "v_cvt_pk_bf16_f32 v110, v84, v85\n\t" \
    "v_cvt_pk_bf16_f32 v111, v86, v87\n\t" \
    "ds_write_b128 v4, v[108:111] offset:1024\n\t" \
    "s_cmp_lt_i32 s86, 16\n\t" \
    "s_cbranch_scc1 gv_rest10_%=\n\t" \
    "gv_back11_%=:\n\t" \
    "s_waitcnt vmcnt(21) lgkmcnt(0)\n\t" \
    "s_barrier\n\t" \
    "ds_read_b128 v[164:167], v5 offset:0\n\t" \
    "ds_read_b128 v[168:171], v5 offset:1024\n\t" \
    "ds_read_b128 v[172:175], v5 offset:2048\n\t" \
    "ds_read_b128 v[176:179], v5 offset:3072\n\t" \
    "ds_read_b128 v[180:183], v5 offset:4096\n\t" \
    "ds_read_b128 v[184:187], v5 offset:5120\n\t" \
    "ds_read_b128 v[188:191], v5 offset:6144\n\t" \
    "ds_read_b128 v[192:195], v5 offset:7168\n\t" \
    "s_cmp_lt_i32 s86, 16\n\t" \
    "s_cselect_b32 s71, s86, s87\n\t" \
    "s_cmp_lg_u32 s66, 0\n\t" \
    "s_cbranch_scc1 gv_SF17_%=\n\t" \
    "s_lshl_b32 s69, s67, 7\n\t" \
    "s_add_u32 s69, s69, 3162112\n\t" \
    "s_add_u32 s70, s71, 0\n\t" \
    "v_mov_b32 v156, s69\n\t" \
    "v_mov_b32 v157, s70\n\t" \
    "s_mov_b64 exec, 1\n\t" \
    "global_store_dword v156, v157, s[100:101] sc1\n\t" \
    "s_mov_b64 exec, -1\n\t" \
    "gv_SF17_%=:\n\t" \
    "s_cmp_ge_u32 s86, 10\n\t" \
    "s_cbranch_scc1 gv_ef18_%=\n\t" \
    "s_cmp_lg_u32 s66, 2\n\t" \
    "s_cbranch_scc1 gv_ef18_%=\n\t" \
    "s_cmp_lt_u32 s86, 4\n\t" \
    "s_cbranch_scc1 gv_ef18_%=\n\t" \
    "s_lshr_b32 s69, s86, 1\n\t" \
    "s_lshl_b32 s69, s69, 6\n\t" \
    "s_add_u32 s69, s69, s67\n\t" \
    "s_lshl_b32 s69, s69, 6\n\t" \
    "s_add_u32 s69, s69, 5799936\n\t" \
    "v_mov_b32 v156, s69\n\t" \
    "v_mov_b32 v157, s86\n\t" \
    "global_store_dword v156, v157, s[36:37] sc1\n\t" \
    "gv_ef18_%=:\n\t" \
    "v_xor_b32 v4, 0x2000, v4\n\t" \
    "v_xor_b32 v5, 0x2000, v5\n\t" \
    "s_waitcnt lgkmcnt(7)\n\t" \
    "v_mfma_f32_16x16x32_bf16 v[72:75], v[164:167], a[144:147], 0\n\t" \
    "s_waitcnt lgkmcnt(6)\n\t" \
    "v_mfma_f32_16x16x32_bf16 v[76:79], v[168:171], a[144:147], 0\n\t" \
    "s_waitcnt lgkmcnt(5)\n\t" \
    "v_mfma_f32_16x16x32_bf16 v[72:75], v[172:175], a[148:151], v[72:75]\n\t" \
    "s_mov_b32 s87, s86\n\t" \
    "s_mov_b32 s86, s65\n\t" \
    "s_waitcnt lgkmcnt(4)\n\t" \
    "v_mfma_f32_16x16x32_bf16 v[76:79], v[176:179], a[148:151], v[76:79]\n\t" \
    "s_mov_b32 s65, s84\n\t" \
    "s_mov_b32 s84, s85\n\t" \
    "s_waitcnt lgkmcnt(3)\n\t" \
    "v_mfma_f32_16x16x32_bf16 v[72:75], v[180:183], a[152:155], v[72:75]\n\t" \
    "s_cmp_lt_u32 s84, 24\n\t" \
    "s_addc_u32 s85, s84, 1\n\t" \
    "s_waitcnt lgkmcnt(2)\n\t" \
    "v_mfma_f32_16x16x32_bf16 v[76:79], v[184:187], a[152:155], v[76:79]\n\t" \
    "s_min_u32 s69, s85, 63\n\t" \
    "s_mul_i32 s69, s69, 36864\n\t" \
    "s_waitcnt lgkmcnt(1)\n\t" \
    "v_mfma_f32_16x16x32_bf16 v[72:75], v[188:191], a[156:159], v[72:75]\n\t" \
    "s_add_u32 s52, s50, s69\n\t" \
    "s_addc_u32 s53, s51, 0\n\t" \
    "s_waitcnt lgkmcnt(0)\n\t" \
    "v_mfma_f32_16x16x32_bf16 v[76:79], v[192:195], a[156:159], v[76:79]\n\t" \
    "s_add_u32 s90, s90, 36864\n\t" \
    "s_cmp_eq_u32 s90, 110592\n\t" \
    "s_cselect_b32 s90, 0, s90\n\t" \
    "v_mfma_f32_16x16x32_bf16 v[80:83], v[164:167], a[160:163], 0\n\t" \
    "s_add_u32 s91, s90, 36864\n\t" \
    "s_cmp_eq_u32 s91, 110592\n\t" \
    "s_cselect_b32 s91, 0, s91\n\t" \
    "v_mfma_f32_16x16x32_bf16 v[84:87], v[168:171], a[160:163], 0\n\t" \
    "s_add_u32 s91, s91, s89\n\t" \
    "v_mfma_f32_16x16x32_bf16 v[80:83], v[172:175], a[164:167], v[80:83]\n\t" \
    "v_mfma_f32_16x16x32_bf16 v[84:87], v[176:179], a[164:167], v[84:87]\n\t" \
    "v_mfma_f32_16x16x32_bf16 v[80:83], v[180:183], a[168:171], v[80:83]\n\t" \
    "v_mfma_f32_16x16x32_bf16 v[84:87], v[184:187], a[168:171], v[84:87]\n\t" \
    "v_cvt_pk_bf16_f32 v104, v72, v73\n\t" \
    "v_mfma_f32_16x16x32_bf16 v[80:83], v[188:191], a[172:175], v[80:83]\n\t" \
    "v_cvt_pk_bf16_f32 v105, v74, v75\n\t" \
    "v_mfma_f32_16x16x32_bf16 v[84:87], v[192:195], a[172:175], v[84:87]\n\t" \
    "v_cvt_pk_bf16_f32 v106, v76, v77\n\t" \
    "v_mfma_f32_16x16x32_bf16 v[88:91], v[164:167], a[176:179], 0\n\t" \
    "v_cvt_pk_bf16_f32 v107, v78, v79\n\t" \
    "v_mfma_f32_16x16x32_bf16 v[92:95], v[168:171], a[176:179], 0\n\t" \
    "v_xor_b32 v18, s40, v8\n\t" \
    "v_mfma_f32_16x16x32_bf16 v[88:91], v[172:175], a[180:183], v[88:91]\n\t" \
    "global_store_dwordx4 v18, v[104:107], s[38:39] sc1\n\t" \
    "v_mfma_f32_16x16x32_bf16 v[92:95], v[176:179], a[180:183], v[92:95]\n\t" \
    "v_mfma_f32_16x16x32_bf16 v[88:91], v[180:183], a[184:187], v[88:91]\n\t" \
    "v_mfma_f32_16x16x32_bf16 v[92:95], v[184:187], a[184:187], v[92:95]\n\t" \
    "v_cvt_pk_bf16_f32 v108, v80, v81\n\t" \
    "v_mfma_f32_16x16x32_bf16 v[88:91], v[188:191], a[188:191], v[88:91]\n\t" \
    "v_cvt_pk_bf16_f32 v109, v82, v83\n\t" \
    "v_mfma_f32_16x16x32_bf16 v[92:95], v[192:195], a[188:191], v[92:95]\n\t" \
    "v_cvt_pk_bf16_f32 v110, v84, v85\n\t" \
    "v_mfma_f32_16x16x32_bf16 v[72:75], v[164:167], a[192:195], 0\n\t" \
    "v_cvt_pk_bf16_f32 v111, v86, v87\n\t" \
    "v_mfma_f32_16x16x32_bf16 v[76:79], v[168:171], a[192:195], 0\n\t" \
    "v_xor_b32 v18, s40, v9\n\t" \
    "v_mfma_f32_16x16x32_bf16 v[72:75], v[172:175], a[196:199], v[72:75]\n\t" \
    "global_store_dwordx4 v18, v[108:111], s[38:39] sc1\n\t" \
    "v_mfma_f32_16x16x32_bf16 v[76:79], v[176:179], a[196:199], v[76:79]\n\t" \
    "v_mfma_f32_16x16x32_bf16 v[72:75], v[180:183], a[200:203], v[72:75]\n\t" \
    "v_mfma_f32_16x16x32_bf16 v[76:79], v[184:187], a[200:203], v[76:79]\n\t" \
    "v_cvt_pk_bf16_f32 v104, v88, v89\n\t" \
    "v_mfma_f32_16x16x32_bf16 v[72:75], v[188:191], a[204:207], v[72:75]\n\t" \
    "v_cvt_pk_bf16_f32 v105, v90, v91\n\t" \
    "v_mfma_f32_16x16x32_bf16 v[76:79], v[192:195], a[204:207], v[76:79]\n\t" \
    "v_cvt_pk_bf16_f32 v106, v92, v93\n\t" \
    "v_mfma_f32_16x16x32_bf16 v[80:83], v[164:167], a[208:211], 0\n\t" \
    "v_cvt_pk_bf16_f32 v107, v94, v95\n\t" \
    "v_mfma_f32_16x16x32_bf16 v[84:87], v[168:171], a[208:211], 0\n\t" \
    "v_xor_b32 v18, s40, v10\n\t" \
    "v_mfma_f32_16x16x32_bf16 v[80:83], v[172:175], a[212:215], v[80:83]\n\t" \
    "global_store_dwordx4 v18, v[104:107], s[38:39] sc1\n\t" \
    "v_mfma_f32_16x16x32_bf16 v[84:87], v[176:179], a[212:215], v[84:87]\n\t" \
    "v_mfma_f32_16x16x32_bf16 v[80:83], v[180:183], a[216:219], v[80:83]\n\t" \
    "v_mfma_f32_16x16x32_bf16 v[84:87], v[184:187], a[216:219], v[84:87]\n\t" \
    "v_cvt_pk_bf16_f32 v108, v72, v73\n\t" \
    "v_mfma_f32_16x16x32_bf16 v[80:83], v[188:191], a[220:223], v[80:83]\n\t" \
    "v_cvt_pk_bf16_f32 v109, v74, v75\n\t" \
    "v_mfma_f32_16x16x32_bf16 v[84:87], v[192:195], a[220:223], v[84:87]\n\t" \
    "v_cvt_pk_bf16_f32 v110, v76, v77\n\t" \
    "v_mfma_f32_16x16x32_bf16 v[88:91], v[164:167], a[224:227], 0\n\t" \
    "v_cvt_pk_bf16_f32 v111, v78, v79\n\t" \
    "v_mfma_f32_16x16x32_bf16 v[92:95], v[168:171], a[224:227], 0\n\t" \
    "v_xor_b32 v18, s40, v11\n\t" \
    "v_mfma_f32_16x16x32_bf16 v[88:91], v[172:175], a[228:231], v[88:91]\n\t" \
    "global_store_dwordx4 v18, v[108:111], s[38:39] sc1\n\t" \
    "v_mfma_f32_16x16x32_bf16 v[92:95], v[176:179], a[228:231], v[92:95]\n\t" \
    "v_mfma_f32_16x16x32_bf16 v[88:91], v[180:183], a[232:235], v[88:91]\n\t" \
    "v_mfma_f32_16x16x32_bf16 v[92:95], v[184:187], a[232:235], v[92:95]\n\t" \
    "v_cvt_pk_bf16_f32 v104, v80, v81\n\t" \
    "v_mfma_f32_16x16x32_bf16 v[88:91], v[188:191], a[236:239], v[88:91]\n\t" \
    "v_cvt_pk_bf16_f32 v105, v82, v83\n\t" \
    "v_mfma_f32_16x16x32_bf16 v[92:95], v[192:195], a[236:239], v[92:95]\n\t" \
    "v_cvt_pk_bf16_f32 v106, v84, v85\n\t" \
    "v_mfma_f32_16x16x32_bf16 v[72:75], v[164:167], a[240:243], 0\n\t" \
    "v_cvt_pk_bf16_f32 v107, v86, v87\n\t" \
    "v_mfma_f32_16x16x32_bf16 v[76:79], v[168:171], a[240:243], 0\n\t" \
    "v_xor_b32 v18, s40, v12\n\t" \
    "v_mfma_f32_16x16x32_bf16 v[72:75], v[172:175], a[244:247], v[72:75]\n\t" \
    "global_store_dwordx4 v18, v[104:107], s[38:39] sc1\n\t" \
    "v_mfma_f32_16x16x32_bf16 v[76:79], v[176:179], a[244:247], v[76:79]\n\t" \
    "s_cbranch_vccnz gv_gol12_%=\n\t" \
    "gv_gob13_%=:\n\t" \
    "v_mfma_f32_16x16x32_bf16 v[72:75], v[180:183], a[248:251], v[72:75]\n\t" \
    "v_mfma_f32_16x16x32_bf16 v[76:79], v[184:187], a[248:251], v[76:79]\n\t" \
    "v_cvt_pk_bf16_f32 v108, v88, v89\n\t" \
    "v_mfma_f32_16x16x32_bf16 v[72:75], v[188:191], a[252:255], v[72:75]\n\t" \
    "v_cvt_pk_bf16_f32 v109, v90, v91\n\t" \
    "v_mfma_f32_16x16x32_bf16 v[76:79], v[192:195], a[252:255], v[76:79]\n\t" \
    "v_cvt_pk_bf16_f32 v110, v92, v93\n\t" \
    "v_mfma_f32_16x16x32_bf16 v[80:83], v[164:167], v[224:227], 0\n\t" \
    "v_cvt_pk_bf16_f32 v111, v94, v95\n\t" \
    "v_mfma_f32_16x16x32_bf16 v[84:87], v[168:171], v[224:227], 0\n\t" \
    "v_xor_b32 v18, s40, v13\n\t" \
    "v_mfma_f32_16x16x32_bf16 v[80:83], v[172:175], v[228:231], v[80:83]\n\t" \
    "global_store_dwordx4 v18, v[108:111], s[38:39] sc1\n\t" \
    "v_mfma_f32_16x16x32_bf16 v[84:87], v[176:179], v[228:231], v[84:87]\n\t" \
    "v_mfma_f32_16x16x32_bf16 v[80:83], v[180:183], v[232:235], v[80:83]\n\t" \
    "v_mfma_f32_16x16x32_bf16 v[84:87], v[184:187], v[232:235], v[84:87]\n\t" \
    "v_cvt_pk_bf16_f32 v104, v72, v73\n\t" \
    "v_mfma_f32_16x16x32_bf16 v[80:83], v[188:191], v[236:239], v[80:83]\n\t" \
    "v_cvt_pk_bf16_f32 v105, v74, v75\n\t" \
    "v_mfma_f32_16x16x32_bf16 v[84:87], v[192:195], v[236:239], v[84:87]\n\t" \
    "v_cvt_pk_bf16_f32 v106, v76, v77\n\t" \
    "v_mfma_f32_16x16x32_bf16 v[88:91], v[164:167], v[240:243], 0\n\t" \
    "v_cvt_pk_bf16_f32 v107, v78, v79\n\t" \
    "ds_read_b128 v[24:27], v22 offset:0\n\t" \
    "v_mfma_f32_16x16x32_bf16 v[92:95], v[168:171], v[240:243], 0\n\t" \
    "v_xor_b32 v18, s40, v14\n\t" \
    "ds_read_b128 v[28:31], v22 offset:18432\n\t" \
    "v_mfma_f32_16x16x32_bf16 v[88:91], v[172:175], v[244:247], v[88:91]\n\t" \
    "global_store_dwordx4 v18, v[104:107], s[38:39] sc1\n\t" \
    "ds_read_b128 v[32:35], v23 offset:0\n\t" \
    "v_mfma_f32_16x16x32_bf16 v[92:95], v[176:179], v[244:247], v[92:95]\n\t" \
    "ds_read_b128 v[36:39], v23 offset:18432\n\t" \
    "v_mfma_f32_16x16x32_bf16 v[88:91], v[180:183], v[248:251], v[88:91]\n\t" \
    "ds_read_b128 v[40:43], v22 offset:128\n\t" \
    "v_mfma_f32_16x16x32_bf16 v[92:95], v[184:187], v[248:251], v[92:95]\n\t" \
    "v_cvt_pk_bf16_f32 v108, v80, v81\n\t" \
    "ds_read_b128 v[44:47], v22 offset:18560\n\t" \
    "v_mfma_f32_16x16x32_bf16 v[88:91], v[188:191], v[252:255], v[88:91]\n\t" \
    "v_cvt_pk_bf16_f32 v109, v82, v83\n\t" \
    "v_mfma_f32_16x16x32_bf16 v[92:95], v[192:195], v[252:255], v[92:95]\n\t" \
    "v_cvt_pk_bf16_f32 v110, v84, v85\n\t" \
    "v_cvt_pk_bf16_f32 v111, v86, v87\n\t" \
    "v_xor_b32 v18, s40, v15\n\t" \
    "global_store_dwordx4 v18, v[108:111], s[38:39] sc1\n\t" \
    "s_nop 1\n\t" \
    "v_cvt_pk_bf16_f32 v104, v88, v89\n\t" \
    "v_cvt_pk_bf16_f32 v105, v90, v91\n\t" \
    "v_cvt_pk_bf16_f32 v106, v92, v93\n\t" \
    "v_cvt_pk_bf16_f32 v107, v94, v95\n\t" \
    "v_xor_b32 v18, s40, v16\n\t" \
    "global_store_dwordx4 v18, v[104:107], s[38:39] sc1\n\t" \
    "s_cmp_ge_u32 s86, 4\n\t" \
    "s_cbranch_scc1 gv_nofast9_%=\n\t" \
    "s_waitcnt vmcnt(0)\n\t" \
    "s_barrier\n\t" \
    "s_cmp_lg_u32 s66, 0\n\t" \
    "s_cbranch_scc1 gv_SF19_%=\n\t" \
    "s_lshl_b32 s69, s67, 7\n\t" \
    "s_add_u32 s69, s69, 3162112\n\t" \
    "s_add_u32 s70, s86, 0\n\t" \
    "v_mov_b32 v156, s69\n\t" \
    "v_mov_b32 v157, s70\n\t" \
    "s_mov_b64 exec, 1\n\t" \
    "global_store_dword v156, v157, s[100:101] sc1\n\t" \
    "s_mov_b64 exec, -1\n\t" \
    "gv_SF19_%=:\n\t" \
    "s_cmp_ge_u32 s86, 10\n\t" \
    "s_cbranch_scc1 gv_ef20_%=\n\t" \
    "s_cmp_lg_u32 s66, 2\n\t" \
    "s_cbranch_scc1 gv_ef20_%=\n\t" \
    "s_lshr_b32 s69, s86, 1\n\t" \
    "s_lshl_b32 s69, s69, 6\n\t" \
    "s_add_u32 s69, s69, s67\n\t" \
    "s_lshl_b32 s69, s69, 6\n\t" \
    "s_add_u32 s69, s69, 5799936\n\t" \
    "v_mov_b32 v156, s69\n\t" \
    "v_mov_b32 v157, s86\n\t" \
    "global_store_dword v156, v157, s[36:37] sc1\n\t" \
    "gv_ef20_%=:\n\t" \
    "gv_nofast9_%=:\n\t" \
    "s_cmp_lt_u32 s65, s88\n\t" \
    "s_cbranch_scc1 gv_loop8_%=\n\t" \
    "s_waitcnt vmcnt(0) lgkmcnt(0)\n\t" \
    "s_barrier\n\t" \
    "s_mov_b32 s65, 99\n\t" \
    "s_cmp_lg_u32 s66, 0\n\t" \
    "s_cbranch_scc1 gv_SF21_%=\n\t" \
    "s_lshl_b32 s69, s67, 7\n\t" \
    "s_add_u32 s69, s69, 3162112\n\t" \
    "s_add_u32 s70, s65, 0\n\t" \
    "v_mov_b32 v156, s69\n\t" \
    "v_mov_b32 v157, s70\n\t" \
    "s_mov_b64 exec, 1\n\t" \
    "global_store_dword v156, v157, s[100:101] sc1\n\t" \
    "s_mov_b64 exec, -1\n\t" \
    "gv_SF21_%=:\n\t" \
    "s_branch gv_end22_%=\n\t" \
    "gv_gol12_%=:\n\t" \
    "s_mov_b64 vcc, 0\n\t" \
    "v_mbcnt_lo_u32_b32 v120, -1, 0\n\t" \
    "v_and_b32 v121, 1, v120\n\t" \
    "v_lshlrev_b32 v121, 14, v121\n\t" \
    "v_bfe_u32 v123, v120, 1, 1\n\t" \
    "v_lshl_add_u32 v121, v123, 17, v121\n\t" \
    "v_bfe_u32 v123, v120, 2, 2\n\t" \
    "v_lshl_add_u32 v121, v123, 11, v121\n\t" \
    "v_bfe_u32 v123, v120, 4, 1\n\t" \
    "v_lshl_add_u32 v121, v123, 15, v121\n\t" \
    "s_bitcmp1_b32 s44, 2\n\t" \
    "s_cselect_b32 s63, 8192, 0\n\t" \
    "s_or_b32 s63, s63, 0x200000\n\t" \
    "v_add_u32 v121, s63, v121\n\t" \
    "s_waitcnt vmcnt(5)\n\t" \
    "v_readfirstlane_b32 s69, v122\n\t" \
    "s_nop 1\n\t" \
    "s_cmp_lg_u32 s69, 126\n\t" \
    "s_cbranch_scc1 gv_go23_%=\n\t" \
    "s_memrealtime s[56:57]\n\t" \
    "s_waitcnt lgkmcnt(0)\n\t" \
    "s_lshl_b32 s69, s56, 4\n\t" \
    "s_add_u32 s69, s69, 4160\n\t" \
    "s_add_u32 s70, s56, 24\n\t" \
    "s_lshr_b32 s70, s70, 4\n\t" \
    "v_lshrrev_b32 v123, 2, v120\n\t" \
    "v_add_u32 v123, s70, v123\n\t" \
    "v_and_b32 v123, 63, v123\n\t" \
    "v_lshlrev_b32 v123, 6, v123\n\t" \
    "v_and_b32 v122, 3, v120\n\t" \
    "v_lshl_add_u32 v123, v122, 12, v123\n\t" \
    "v_add_u32 v123, 5783552, v123\n\t" \
    "v_mov_b32 v120, s69\n\t" \
    "s_mov_b32 exec_hi, 0\n\t" \
    "global_store_dword v123, v120, s[36:37] sc1\n\t" \
    "global_store_dword v121, v120, s[36:37] sc1\n\t" \
    "s_mov_b32 exec_hi, -1\n\t" \
    "gv_go23_%=:\n\t" \
    "s_branch gv_gob13_%=\n\t" \
    "gv_rest10_%=:\n\t" \
    "s_waitcnt vmcnt(12) lgkmcnt(0)\n\t" \
    "s_cmp_ge_i32 s86, 0\n\t" \
    "s_cbranch_scc1 gv_back11_%=\n\t" \
    "s_barrier\n\t" \
    "s_cmp_lg_u32 s66, 1\n\t" \
    "s_cbranch_scc1 gv_nw124_%=\n\t" \
    "v_mov_b32 v120, 5769216\n\t" \
    "v_mov_b32 v121, 1\n\t" \
    "s_mov_b64 exec, 1\n\t" \
    "global_atomic_add v122, v120, v121, s[36:37] sc0 sc1\n\t" \
    "s_mov_b64 exec, -1\n\t" \
    "s_mov_b64 vcc, -1\n\t" \
    "gv_nw124_%=:\n\t" \
    "ds_read_b64_tr_b16 a[156:157], v212 offset:0\n\t" \
    "ds_read_b64_tr_b16 a[158:159], v212 offset:18432\n\t" \
    "ds_read_b64_tr_b16 a[172:173], v213 offset:0\n\t" \
    "ds_read_b64_tr_b16 a[174:175], v213 offset:18432\n\t" \
    "ds_read_b64_tr_b16 a[188:189], v214 offset:0\n\t" \
    "ds_read_b64_tr_b16 a[190:191], v214 offset:18432\n\t" \
    "s_waitcnt lgkmcnt(6)\n\t" \
    "ds_read_b64_tr_b16 a[204:205], v215 offset:0\n\t" \
    "ds_read_b64_tr_b16 a[206:207], v215 offset:18432\n\t" \
    "ds_read_b64_tr_b16 a[220:221], v212 offset:128\n\t" \
    "ds_read_b64_tr_b16 a[222:223], v212 offset:18560\n\t" \
    "ds_read_b64_tr_b16 a[236:237], v213 offset:128\n\t" \
    "ds_read_b64_tr_b16 a[238:239], v213 offset:18560\n\t" \
    "s_waitcnt lgkmcnt(6)\n\t" \
    "ds_read_b64_tr_b16 a[252:253], v214 offset:128\n\t" \
    "ds_read_b64_tr_b16 a[254:255], v214 offset:18560\n\t" \
    "ds_read_b64_tr_b16 v[236:237], v215 offset:128\n\t" \
    "ds_read_b64_tr_b16 v[238:239], v215 offset:18560\n\t" \
    "ds_read_b64_tr_b16 v[252:253], v212 offset:256\n\t" \
    "ds_read_b64_tr_b16 v[254:255], v212 offset:18688\n\t" \
    "s_waitcnt lgkmcnt(6)\n\t" \
    "v_add_u32 v212, 0x9000, v212\n\t" \
    "v_add_u32 v213, 0x9000, v213\n\t" \
    "v_add_u32 v214, 0x9000, v214\n\t" \
    "v_add_u32 v215, 0x9000, v215\n\t" \
    "s_branch gv_back11_%=\n\t" \
    "gv_end22_%=:\n\t" \
    "gv_last_%=:\n\t" \
    ""
#define ASM_CV \
    "v_mov_b32 v0, %0\n\t" \
    "v_mov_b32 v2, %2\n\t" \
    "v_subrev_u32 v1, 768, %1\n\t" \
    "v_mov_b32 v4, 0\n\t" \
    "v_mov_b32 v5, 0\n\t" \
    "v_mov_b32 v6, 0\n\t" \
    "v_mov_b32 v7, 0\n\t" \
    "v_mov_b32 v8, 0\n\t" \
    "v_mov_b32 v9, 0\n\t" \
    "v_mov_b32 v10, 0\n\t" \
    "v_mov_b32 v11, 0\n\t" \
    "v_mov_b32 v12, 0\n\t" \
    "v_mov_b32 v13, 0\n\t" \
    "v_mov_b32 v14, 0\n\t" \
    "v_mov_b32 v15, 0\n\t" \
    "v_mov_b32 v16, 0\n\t" \
    "v_mov_b32 v17, 0\n\t" \
    "v_mov_b32 v18, 0\n\t" \
    "v_mov_b32 v19, 0\n\t" \
    "v_mov_b32 v20, 0\n\t" \
    "v_mov_b32 v21, 0\n\t" \
    "v_mov_b32 v22, 0\n\t" \
    "v_mov_b32 v23, 0\n\t" \
    "v_mov_b32 v24, 0\n\t" \
    "v_mov_b32 v25, 0\n\t" \
    "v_mov_b32 v26, 0\n\t" \
    "v_mov_b32 v27, 0\n\t" \
    "v_mov_b32 v28, 0\n\t" \
    "v_mov_b32 v29, 0\n\t" \
    "v_mov_b32 v30, 0\n\t" \
    "v_mov_b32 v31, 0\n\t" \
    "v_mov_b32 v32, 0\n\t" \
    "v_mov_b32 v33, 0\n\t" \
    "v_mov_b32 v34, 0\n\t" \
    "v_mov_b32 v35, 0\n\t" \
    "s_waitcnt lgkmcnt(0)\n\t" \
    "ds_read_b64 v[36:37], v0 offset:0\n\t" \
    "ds_read_b64 v[38:39], v0 offset:8\n\t" \
    "ds_read_b64 v[40:41], v0 offset:32\n\t" \
    "ds_read_b64 v[42:43], v0 offset:40\n\t" \
    "ds_read_b128 v[44:47], v1 offset:768\n\t" \
    "ds_read_b128 v[48:51], v1 offset:2304\n\t" \
    "ds_read_b128 v[52:55], v1 offset:3840\n\t" \
    "ds_read_b128 v[56:59], v1 offset:5376\n\t" \
    "s_mov_b32 s69, 8\n\t" \
    "cv_c1_%=:\n\t" \
    "s_waitcnt lgkmcnt(3)\n\t" \
    "v_mfma_f32_16x16x32_bf16 v[4:7], v[36:39], v[44:47], v[4:7]\n\t" \
    "ds_read_b64 v[76:77], v0 offset:64\n\t" \
    "ds_read_b64 v[78:79], v0 offset:72\n\t" \
    "ds_read_b64 v[80:81], v0 offset:96\n\t" \
    "v_mfma_f32_16x16x32_bf16 v[20:23], v[40:43], v[44:47], v[20:23]\n\t" \
    "ds_read_b64 v[82:83], v0 offset:104\n\t" \
    "ds_read_b128 v[84:87], v1 offset:672\n\t" \
    "ds_read_b128 v[88:91], v1 offset:2208\n\t" \
    "s_waitcnt lgkmcnt(8)\n\t" \
    "v_mfma_f32_16x16x32_bf16 v[8:11], v[36:39], v[48:51], v[8:11]\n\t" \
    "ds_read_b128 v[92:95], v1 offset:3744\n\t" \
    "ds_read_b128 v[96:99], v1 offset:5280\n\t" \
    "v_mfma_f32_16x16x32_bf16 v[24:27], v[40:43], v[48:51], v[24:27]\n\t" \
    "s_waitcnt lgkmcnt(9)\n\t" \
    "v_mfma_f32_16x16x32_bf16 v[12:15], v[36:39], v[52:55], v[12:15]\n\t" \
    "v_mfma_f32_16x16x32_bf16 v[28:31], v[40:43], v[52:55], v[28:31]\n\t" \
    "s_waitcnt lgkmcnt(8)\n\t" \
    "v_mfma_f32_16x16x32_bf16 v[16:19], v[36:39], v[56:59], v[16:19]\n\t" \
    "v_mfma_f32_16x16x32_bf16 v[32:35], v[40:43], v[56:59], v[32:35]\n\t" \
    "s_waitcnt lgkmcnt(3)\n\t" \
    "v_mfma_f32_16x16x32_bf16 v[4:7], v[76:79], v[84:87], v[4:7]\n\t" \
    "ds_read_b64 v[36:37], v0 offset:128\n\t" \
    "ds_read_b64 v[38:39], v0 offset:136\n\t" \
    "ds_read_b64 v[40:41], v0 offset:160\n\t" \
    "v_mfma_f32_16x16x32_bf16 v[20:23], v[80:83], v[84:87], v[20:23]\n\t" \
    "ds_read_b64 v[42:43], v0 offset:168\n\t" \
    "ds_read_b128 v[44:47], v1 offset:576\n\t" \
    "ds_read_b128 v[48:51], v1 offset:2112\n\t" \
    "s_waitcnt lgkmcnt(8)\n\t" \
    "v_mfma_f32_16x16x32_bf16 v[8:11], v[76:79], v[88:91], v[8:11]\n\t" \
    "ds_read_b128 v[52:55], v1 offset:3648\n\t" \
    "ds_read_b128 v[56:59], v1 offset:5184\n\t" \
    "v_mfma_f32_16x16x32_bf16 v[24:27], v[80:83], v[88:91], v[24:27]\n\t" \
    "s_waitcnt lgkmcnt(9)\n\t" \
    "v_mfma_f32_16x16x32_bf16 v[12:15], v[76:79], v[92:95], v[12:15]\n\t" \
    "v_mfma_f32_16x16x32_bf16 v[28:31], v[80:83], v[92:95], v[28:31]\n\t" \
    "s_waitcnt lgkmcnt(8)\n\t" \
    "v_mfma_f32_16x16x32_bf16 v[16:19], v[76:79], v[96:99], v[16:19]\n\t" \
    "v_mfma_f32_16x16x32_bf16 v[32:35], v[80:83], v[96:99], v[32:35]\n\t" \
    "v_add_u32 v0, 128, v0\n\t" \
    "v_subrev_u32 v1, 192, v1\n\t" \
    "s_sub_u32 s69, s69, 1\n\t" \
    "s_cmp_lg_u32 s69, 0\n\t" \
    "s_cbranch_scc1 cv_c1_%=\n\t" \
    "s_waitcnt lgkmcnt(0)\n\t" \
    "ds_read_b64 v[36:37], v0 offset:0\n\t" \
    "ds_read_b64 v[38:39], v0 offset:8\n\t" \
    "ds_read_b64 v[40:41], v0 offset:32\n\t" \
    "ds_read_b64 v[42:43], v0 offset:40\n\t" \
    "ds_read_b128 v[48:51], v1 offset:2304\n\t" \
    "ds_read_b128 v[52:55], v1 offset:3840\n\t" \
    "ds_read_b128 v[56:59], v1 offset:5376\n\t" \
    "ds_read_b64 v[76:77], v0 offset:64\n\t" \
    "ds_read_b64 v[78:79], v0 offset:72\n\t" \
    "ds_read_b64 v[80:81], v0 offset:96\n\t" \
    "ds_read_b64 v[82:83], v0 offset:104\n\t" \
    "ds_read_b128 v[88:91], v1 offset:2208\n\t" \
    "ds_read_b128 v[92:95], v1 offset:3744\n\t" \
    "ds_read_b128 v[96:99], v1 offset:5280\n\t" \
    "s_mov_b32 s69, 4\n\t" \
    "cv_c2_%=:\n\t" \
    "s_waitcnt lgkmcnt(9)\n\t" \
    "v_mfma_f32_16x16x32_bf16 v[8:11], v[36:39], v[48:51], v[8:11]\n\t" \
    "ds_read_b64 v[116:117], v0 offset:128\n\t" \
    "ds_read_b64 v[118:119], v0 offset:136\n\t" \
    "ds_read_b64 v[120:121], v0 offset:160\n\t" \
    "v_mfma_f32_16x16x32_bf16 v[24:27], v[40:43], v[48:51], v[24:27]\n\t" \
    "ds_read_b64 v[122:123], v0 offset:168\n\t" \
    "ds_read_b128 v[128:131], v1 offset:2112\n\t" \
    "ds_read_b128 v[132:135], v1 offset:3648\n\t" \
    "s_waitcnt lgkmcnt(14)\n\t" \
    "v_mfma_f32_16x16x32_bf16 v[12:15], v[36:39], v[52:55], v[12:15]\n\t" \
    "ds_read_b128 v[136:139], v1 offset:5184\n\t" \
    "v_mfma_f32_16x16x32_bf16 v[28:31], v[40:43], v[52:55], v[28:31]\n\t" \
    "s_waitcnt lgkmcnt(14)\n\t" \
    "v_mfma_f32_16x16x32_bf16 v[16:19], v[36:39], v[56:59], v[16:19]\n\t" \
    "ds_read_b64 v[156:157], v0 offset:192\n\t" \
    "v_mfma_f32_16x16x32_bf16 v[32:35], v[40:43], v[56:59], v[32:35]\n\t" \
    "s_waitcnt lgkmcnt(10)\n\t" \
    "v_mfma_f32_16x16x32_bf16 v[8:11], v[76:79], v[88:91], v[8:11]\n\t" \
    "ds_read_b64 v[158:159], v0 offset:200\n\t" \
    "ds_read_b64 v[160:161], v0 offset:224\n\t" \
    "ds_read_b64 v[162:163], v0 offset:232\n\t" \
    "v_mfma_f32_16x16x32_bf16 v[24:27], v[80:83], v[88:91], v[24:27]\n\t" \
    "ds_read_b128 v[168:171], v1 offset:2016\n\t" \
    "ds_read_b128 v[172:175], v1 offset:3552\n\t" \
    "s_waitcnt lgkmcnt(14)\n\t" \
    "v_mfma_f32_16x16x32_bf16 v[12:15], v[76:79], v[92:95], v[12:15]\n\t" \
    "ds_read_b128 v[176:179], v1 offset:5088\n\t" \
    "v_mfma_f32_16x16x32_bf16 v[28:31], v[80:83], v[92:95], v[28:31]\n\t" \
    "s_waitcnt lgkmcnt(14)\n\t" \
    "v_mfma_f32_16x16x32_bf16 v[16:19], v[76:79], v[96:99], v[16:19]\n\t" \
    "v_mfma_f32_16x16x32_bf16 v[32:35], v[80:83], v[96:99], v[32:35]\n\t" \
    "s_waitcnt lgkmcnt(9)\n\t" \
    "v_mfma_f32_16x16x32_bf16 v[8:11], v[116:119], v[128:131], v[8:11]\n\t" \
    "ds_read_b64 v[36:37], v0 offset:256\n\t" \
    "ds_read_b64 v[38:39], v0 offset:264\n\t" \
    "ds_read_b64 v[40:41], v0 offset:288\n\t" \
    "v_mfma_f32_16x16x32_bf16 v[24:27], v[120:123], v[128:131], v[24:27]\n\t" \
    "ds_read_b64 v[42:43], v0 offset:296\n\t" \
    "ds_read_b128 v[48:51], v1 offset:1920\n\t" \
    "ds_read_b128 v[52:55], v1 offset:3456\n\t" \
    "s_waitcnt lgkmcnt(14)\n\t" \
    "v_mfma_f32_16x16x32_bf16 v[12:15], v[116:119], v[132:135], v[12:15]\n\t" \
    "ds_read_b128 v[56:59], v1 offset:4992\n\t" \
    "v_mfma_f32_16x16x32_bf16 v[28:31], v[120:123], v[132:135], v[28:31]\n\t" \
    "s_waitcnt lgkmcnt(14)\n\t" \
    "v_mfma_f32_16x16x32_bf16 v[16:19], v[116:119], v[136:139], v[16:19]\n\t" \
    "ds_read_b64 v[76:77], v0 offset:320\n\t" \
    "v_mfma_f32_16x16x32_bf16 v[32:35], v[120:123], v[136:139], v[32:35]\n\t" \
    "s_waitcnt lgkmcnt(10)\n\t" \
    "v_mfma_f32_16x16x32_bf16 v[8:11], v[156:159], v[168:171], v[8:11]\n\t" \
    "ds_read_b64 v[78:79], v0 offset:328\n\t" \
    "ds_read_b64 v[80:81], v0 offset:352\n\t" \
    "ds_read_b64 v[82:83], v0 offset:360\n\t" \
    "v_mfma_f32_16x16x32_bf16 v[24:27], v[160:163], v[168:171], v[24:27]\n\t" \
    "ds_read_b128 v[88:91], v1 offset:1824\n\t" \
    "ds_read_b128 v[92:95], v1 offset:3360\n\t" \
    "s_waitcnt lgkmcnt(14)\n\t" \
    "v_mfma_f32_16x16x32_bf16 v[12:15], v[156:159], v[172:175], v[12:15]\n\t" \
    "ds_read_b128 v[96:99], v1 offset:4896\n\t" \
    "v_mfma_f32_16x16x32_bf16 v[28:31], v[160:163], v[172:175], v[28:31]\n\t" \
    "s_waitcnt lgkmcnt(14)\n\t" \
    "v_mfma_f32_16x16x32_bf16 v[16:19], v[156:159], v[176:179], v[16:19]\n\t" \
    "v_mfma_f32_16x16x32_bf16 v[32:35], v[160:163], v[176:179], v[32:35]\n\t" \
    "v_add_u32 v0, 256, v0\n\t" \
    "v_subrev_u32 v1, 384, v1\n\t" \
    "s_sub_u32 s69, s69, 1\n\t" \
    "s_cmp_lg_u32 s69, 0\n\t" \
    "s_cbranch_scc1 cv_c2_%=\n\t" \
    "s_waitcnt lgkmcnt(0)\n\t" \
    "s_mov_b32 s69, 4\n\t" \
    "cv_c3_%=:\n\t" \
    "s_waitcnt lgkmcnt(7)\n\t" \
    "v_mfma_f32_16x16x32_bf16 v[12:15], v[36:39], v[52:55], v[12:15]\n\t" \
    "ds_read_b64 v[116:117], v0 offset:128\n\t" \
    "ds_read_b64 v[118:119], v0 offset:136\n\t" \
    "ds_read_b64 v[120:121], v0 offset:160\n\t" \
    "v_mfma_f32_16x16x32_bf16 v[28:31], v[40:43], v[52:55], v[28:31]\n\t" \
    "ds_read_b64 v[122:123], v0 offset:168\n\t" \
    "ds_read_b128 v[132:135], v1 offset:3648\n\t" \
    "ds_read_b128 v[136:139], v1 offset:5184\n\t" \
    "s_waitcnt lgkmcnt(12)\n\t" \
    "v_mfma_f32_16x16x32_bf16 v[16:19], v[36:39], v[56:59], v[16:19]\n\t" \
    "ds_read_b64 v[156:157], v0 offset:192\n\t" \
    "ds_read_b64 v[158:159], v0 offset:200\n\t" \
    "ds_read_b64 v[160:161], v0 offset:224\n\t" \
    "v_mfma_f32_16x16x32_bf16 v[32:35], v[40:43], v[56:59], v[32:35]\n\t" \
    "s_waitcnt lgkmcnt(10)\n\t" \
    "v_mfma_f32_16x16x32_bf16 v[12:15], v[76:79], v[92:95], v[12:15]\n\t" \
    "ds_read_b64 v[162:163], v0 offset:232\n\t" \
    "ds_read_b128 v[172:175], v1 offset:3552\n\t" \
    "ds_read_b128 v[176:179], v1 offset:5088\n\t" \
    "v_mfma_f32_16x16x32_bf16 v[28:31], v[80:83], v[92:95], v[28:31]\n\t" \
    "s_waitcnt lgkmcnt(12)\n\t" \
    "v_mfma_f32_16x16x32_bf16 v[16:19], v[76:79], v[96:99], v[16:19]\n\t" \
    "v_mfma_f32_16x16x32_bf16 v[32:35], v[80:83], v[96:99], v[32:35]\n\t" \
    "s_waitcnt lgkmcnt(7)\n\t" \
    "v_mfma_f32_16x16x32_bf16 v[12:15], v[116:119], v[132:135], v[12:15]\n\t" \
    "ds_read_b64 v[36:37], v0 offset:256\n\t" \
    "ds_read_b64 v[38:39], v0 offset:264\n\t" \
    "ds_read_b64 v[40:41], v0 offset:288\n\t" \
    "v_mfma_f32_16x16x32_bf16 v[28:31], v[120:123], v[132:135], v[28:31]\n\t" \
    "ds_read_b64 v[42:43], v0 offset:296\n\t" \
    "ds_read_b128 v[52:55], v1 offset:3456\n\t" \
    "ds_read_b128 v[56:59], v1 offset:4992\n\t" \
    "s_waitcnt lgkmcnt(12)\n\t" \
    "v_mfma_f32_16x16x32_bf16 v[16:19], v[116:119], v[136:139], v[16:19]\n\t" \
    "ds_read_b64 v[76:77], v0 offset:320\n\t" \
    "ds_read_b64 v[78:79], v0 offset:328\n\t" \
    "ds_read_b64 v[80:81], v0 offset:352\n\t" \
    "v_mfma_f32_16x16x32_bf16 v[32:35], v[120:123], v[136:139], v[32:35]\n\t" \
    "s_waitcnt lgkmcnt(10)\n\t" \
    "v_mfma_f32_16x16x32_bf16 v[12:15], v[156:159], v[172:175], v[12:15]\n\t" \
    "ds_read_b64 v[82:83], v0 offset:360\n\t" \
    "ds_read_b128 v[92:95], v1 offset:3360\n\t" \
    "ds_read_b128 v[96:99], v1 offset:4896\n\t" \
    "v_mfma_f32_16x16x32_bf16 v[28:31], v[160:163], v[172:175], v[28:31]\n\t" \
    "s_waitcnt lgkmcnt(12)\n\t" \
    "v_mfma_f32_16x16x32_bf16 v[16:19], v[156:159], v[176:179], v[16:19]\n\t" \
    "v_mfma_f32_16x16x32_bf16 v[32:35], v[160:163], v[176:179], v[32:35]\n\t" \
    "v_add_u32 v0, 256, v0\n\t" \
    "v_subrev_u32 v1, 384, v1\n\t" \
    "s_sub_u32 s69, s69, 1\n\t" \
    "s_cmp_lg_u32 s69, 0\n\t" \
    "s_cbranch_scc1 cv_c3_%=\n\t" \
    "s_waitcnt lgkmcnt(0)\n\t" \
    "s_mov_b32 s69, 4\n\t" \
    "cv_c4_%=:\n\t" \
    "s_waitcnt lgkmcnt(5)\n\t" \
    "v_mfma_f32_16x16x32_bf16 v[16:19], v[36:39], v[56:59], v[16:19]\n\t" \
    "ds_read_b64 v[116:117], v0 offset:128\n\t" \
    "ds_read_b64 v[118:119], v0 offset:136\n\t" \
    "ds_read_b64 v[120:121], v0 offset:160\n\t" \
    "v_mfma_f32_16x16x32_bf16 v[32:35], v[40:43], v[56:59], v[32:35]\n\t" \
    "ds_read_b64 v[122:123], v0 offset:168\n\t" \
    "ds_read_b128 v[136:139], v1 offset:5184\n\t" \
    "ds_read_b64 v[156:157], v0 offset:192\n\t" \
    "s_waitcnt lgkmcnt(6)\n\t" \
    "v_mfma_f32_16x16x32_bf16 v[16:19], v[76:79], v[96:99], v[16:19]\n\t" \
    "ds_read_b64 v[158:159], v0 offset:200\n\t" \
    "ds_read_b64 v[160:161], v0 offset:224\n\t" \
    "ds_read_b64 v[162:163], v0 offset:232\n\t" \
    "v_mfma_f32_16x16x32_bf16 v[32:35], v[80:83], v[96:99], v[32:35]\n\t" \
    "ds_read_b128 v[176:179], v1 offset:5088\n\t" \
    "s_waitcnt lgkmcnt(5)\n\t" \
    "v_mfma_f32_16x16x32_bf16 v[16:19], v[116:119], v[136:139], v[16:19]\n\t" \
    "ds_read_b64 v[36:37], v0 offset:256\n\t" \
    "ds_read_b64 v[38:39], v0 offset:264\n\t" \
    "ds_read_b64 v[40:41], v0 offset:288\n\t" \
    "v_mfma_f32_16x16x32_bf16 v[32:35], v[120:123], v[136:139], v[32:35]\n\t" \
    "ds_read_b64 v[42:43], v0 offset:296\n\t" \
    "ds_read_b128 v[56:59], v1 offset:4992\n\t" \
    "ds_read_b64 v[76:77], v0 offset:320\n\t" \
    "s_waitcnt lgkmcnt(6)\n\t" \
    "v_mfma_f32_16x16x32_bf16 v[16:19], v[156:159], v[176:179], v[16:19]\n\t" \
    "ds_read_b64 v[78:79], v0 offset:328\n\t" \
    "ds_read_b64 v[80:81], v0 offset:352\n\t" \
    "ds_read_b64 v[82:83], v0 offset:360\n\t" \
    "v_mfma_f32_16x16x32_bf16 v[32:35], v[160:163], v[176:179], v[32:35]\n\t" \
    "ds_read_b128 v[96:99], v1 offset:4896\n\t" \
    "v_add_u32 v0, 256, v0\n\t" \
    "v_subrev_u32 v1, 384, v1\n\t" \
    "s_sub_u32 s69, s69, 1\n\t" \
    "s_cmp_lg_u32 s69, 0\n\t" \
    "s_cbranch_scc1 cv_c4_%=\n\t" \
    "s_waitcnt lgkmcnt(0)\n\t" \
    "s_nop 7\n\t" \
    "ds_write_b128 v2, v[4:7] offset:0\n\t" \
    "ds_write_b128 v2, v[8:11] offset:1024\n\t" \
    "ds_write_b128 v2, v[12:15] offset:2048\n\t" \
    "ds_write_b128 v2, v[16:19] offset:3072\n\t" \
    "ds_write_b128 v2, v[20:23] offset:4096\n\t" \
    "ds_write_b128 v2, v[24:27] offset:5120\n\t" \
    "ds_write_b128 v2, v[28:31] offset:6144\n\t" \
    "ds_write_b128 v2, v[32:35] offset:7168\n\t" \
    "s_waitcnt lgkmcnt(0)\n\t" \
    "cv_last_%=:\n\t" \
    ""
#define ASM_EA \
    "s_bitcmp1_b32 %1, 2\n\t" \
    "s_cselect_b32 s40, 0x30, 0x28\n\t" \
    "s_cmp_lt_u32 %1, 64\n\t" \
    "s_cselect_b32 s40, s40, 0x38\n\t" \
    "s_load_dwordx2 s[42:43], %0, s40\n\t" \
    "s_load_dword s48, %0, 0x40\n\t" \
    "s_getpc_b64 s[44:45]\n\t" \
    "ea_pc_%=:\n\t" \
    "s_bfe_u32 s40, %1, 0x20003\n\t" \
    "s_lshl_b32 s40, s40, 15\n\t" \
    "v_lshl_add_u32 v1, %2, 7, s40\n\t" \
    "v_min_u32 v1, code_end_marker-ea_pc_%=, v1\n\t" \
    "s_mov_b32 m0, 155648\n\t" \
    "v_readfirstlane_b32 s41, %2\n\t" \
    "global_load_lds_dword v1, s[44:45]\n\t" \
    "s_bitcmp1_b32 %1, 2\n\t" \
    "s_cselect_b32 s40, 24, 16\n\t" \
    "s_lshr_b32 s47, %1, 3\n\t" \
    "s_cmp_ge_u32 s47, s40\n\t" \
    "s_cbranch_scc1 ea_done1_%=\n\t" \
    "s_mov_b32 s46, pad_qk-ea_pc_%=\n\t" \
    "s_mov_b32 s40, pad_vv-ea_pc_%=\n\t" \
    "s_bitcmp1_b32 %1, 2\n\t" \
    "s_cselect_b32 s46, pad_gx-ea_pc_%=, s46\n\t" \
    "s_cselect_b32 s40, pad_gv-ea_pc_%=, s40\n\t" \
    "s_cmp_lt_u32 %1, 64\n\t" \
    "s_cselect_b32 s40, s46, s40\n\t" \
    "s_add_u32 s44, s44, s40\n\t" \
    "s_addc_u32 s45, s45, 0\n\t" \
    "s_swappc_b64 s[46:47], s[44:45]\n\t" \
    "s_lshl_b32 m0, s41, 4\n\t" \
    "s_and_b32 s40, %1, 3\n\t" \
    "s_lshl_b32 s40, s40, 11\n\t" \
    "s_bfe_u32 s46, %1, 0x30003\n\t" \
    "s_lshl_b32 s46, s46, 8\n\t" \
    "s_add_u32 s40, s40, s46\n\t" \
    "s_and_b32 s41, s41, 0x80\n\t" \
    "s_xor_b32 s40, s40, s41\n\t" \
    "v_lshrrev_b32 v1, 3, %2\n\t" \
    "v_and_b32 v1, 6, v1\n\t" \
    "v_xor_b32 v1, v1, %2\n\t" \
    "v_and_b32 v1, 15, v1\n\t" \
    "v_lshlrev_b32 v1, 4, v1\n\t" \
    "v_xor_b32 v1, s40, v1\n\t" \
    "v_lshrrev_b32 v2, 4, %2\n\t" \
    "v_lshl_add_u32 v1, v2, 13, v1\n\t" \
    "s_mov_b32 s40, 16\n\t" \
    "s_waitcnt lgkmcnt(0)\n\t" \
    "ea_lp2_%=:\n\t" \
    "global_load_lds_dwordx4 v1, s[42:43]\n\t" \
    "v_add_u32 v1, 0x20000, v1\n\t" \
    "s_add_u32 m0, m0, 4096\n\t" \
    "s_sub_u32 s40, s40, 1\n\t" \
    "s_cmp_lg_u32 s40, 0\n\t" \
    "s_cbranch_scc1 ea_lp2_%=\n\t" \
    "ea_done1_%=:\n\t" \
    "s_waitcnt lgkmcnt(0)\n\t" \
    "ea_last_%=:\n\t" \
    ""
#define ASM_CLOB "v0", "v1", "v2", "v3", "v4", "v5", "v6", "v7", "v8", "v9", "v10", "v11", "v12", "v13", "v14", "v15", "v16", "v17", "v18", "v19", "v20", "v21", "v22", "v23", "v24", "v25", "v26", "v27", "v28", "v29", "v30", "v31", "v32", "v33", "v34", "v35", "v36", "v37", "v38", "v39", "v40", "v41", "v42", "v43", "v44", "v45", "v46", "v47", "v48", "v49", "v50", "v51", "v52", "v53", "v54", "v55", "v56", "v57", "v58", "v59", "v60", "v61", "v62", "v63", "v64", "v65", "v66", "v67", "v68", "v69", "v70", "v71", "v72", "v73", "v74", "v75", "v76", "v77", "v78", "v79", "v80", "v81", "v82", "v83", "v84", "v85", "v86", "v87", "v88", "v89", "v90", "v91", "v92", "v93", "v94", "v95", "v96", "v97", "v98", "v99", "v100", "v101", "v102", "v103", "v104", "v105", "v106", "v107", "v108", "v109", "v110", "v111", "v112", "v113", "v114", "v115", "v116", "v117", "v118", "v119", "v120", "v121", "v122", "v123", "v124", "v125", "v126", "v127", "v128", "v129", "v130", "v131", "v132", "v133", "v134", "v135", "v136", "v137", "v138", "v139", "v140", "v141", "v142", "v143", "v144", "v145", "v146", "v147", "v148", "v149", "v150", "v151", "v152", "v153", "v154", "v155", "v156", "v157", "v158", "v159", "v160", "v161", "v162", "v163", "v164", "v165", "v166", "v167", "v168", "v169", "v170", "v171", "v172", "v173", "v174", "v175", "v176", "v177", "v178", "v179", "v180", "v181", "v182", "v183", "v184", "v185", "v186", "v187", "v188", "v189", "v190", "v191", "v192", "v193", "v194", "v195", "v196", "v197", "v198", "v199", "v200", "v201", "v202", "v203", "v204", "v205", "v206", "v207", "v208", "v209", "v210", "v211", "v212", "v213", "v214", "v215", "v222", "v223", "v224", "v225", "v226", "v227", "v228", "v229", "v230", "v231", "v232", "v233", "v234", "v235", "v236", "v237", "v238", "v239", "v240", "v241", "v242", "v243", "v244", "v245", "v246", "v247", "v248", "v249", "v250", "v251", "v252", "v253", "v254", "v255", "a0", "a1", "a2", "a3", "a4", "a5", "a6", "a7", "a8", "a9", "a10", "a11", "a12", "a13", "a14", "a15", "a16", "a17", "a18", "a19", "a20", "a21", "a22", "a23", "a24", "a25", "a26", "a27", "a28", "a29", "a30", "a31", "a32", "a33", "a34", "a35", "a36", "a37", "a38", "a39", "a40", "a41", "a42", "a43", "a44", "a45", "a46", "a47", "a48", "a49", "a50", "a51", "a52", "a53", "a54", "a55", "a56", "a57", "a58", "a59", "a60", "a61", "a62", "a63", "a64", "a65", "a66", "a67", "a68", "a69", "a70", "a71", "a72", "a73", "a74", "a75", "a76", "a77", "a78", "a79", "a80", "a81", "a82", "a83", "a84", "a85", "a86", "a87", "a88", "a89", "a90", "a91", "a92", "a93", "a94", "a95", "a96", "a97", "a98", "a99", "a100", "a101", "a102", "a103", "a104", "a105", "a106", "a107", "a108", "a109", "a110", "a111", "a112", "a113", "a114", "a115", "a116", "a117", "a118", "a119", "a120", "a121", "a122", "a123", "a124", "a125", "a126", "a127", "a128", "a129", "a130", "a131", "a132", "a133", "a134", "a135", "a136", "a137", "a138", "a139", "a140", "a141", "a142", "a143", "a144", "a145", "a146", "a147", "a148", "a149", "a150", "a151", "a152", "a153", "a154", "a155", "a156", "a157", "a158", "a159", "a160", "a161", "a162", "a163", "a164", "a165", "a166", "a167", "a168", "a169", "a170", "a171", "a172", "a173", "a174", "a175", "a176", "a177", "a178", "a179", "a180", "a181", "a182", "a183", "a184", "a185", "a186", "a187", "a188", "a189", "a190", "a191", "a192", "a193", "a194", "a195", "a196", "a197", "a198", "a199", "a200", "a201", "a202", "a203", "a204", "a205", "a206", "a207", "a208", "a209", "a210", "a211", "a212", "a213", "a214", "a215", "a216", "a217", "a218", "a219", "a220", "a221", "a222", "a223", "a224", "a225", "a226", "a227", "a228", "a229", "a230", "a231", "a232", "a233", "a234", "a235", "a236", "a237", "a238", "a239", "a240", "a241", "a242", "a243", "a244", "a245", "a246", "a247", "a248", "a249", "a250", "a251", "a252", "a253", "a254", "a255", "s33", "s34", "s35", "s36", "s37", "s38", "s39", "s40", "s41", "s42", "s43", "s44", "s45", "s46", "s47", "s48", "s49", "s50", "s51", "s52", "s53", "s54", "s55", "s56", "s57", "s58", "s59", "s60", "s61", "s62", "s63", "s64", "s65", "s66", "s67", "s68", "s69", "s70", "s71", "s72", "s73", "s74", "s75", "s76", "s77", "s78", "s79", "s80", "s81", "s82", "s83", "s84", "s85", "s86", "s87", "s88", "s89", "s90", "s91", "s92", "s93", "s94", "s95", "s96", "s97", "s98", "s99", "s100", "s101", "vcc", "scc", "memory"
#define ASM_CLOBW "v32", "v33", "v34", "v35", "v36", "v37", "v38", "v39", "v40", "v41", "v42", "v43", "v44", "v45", "v46", "v47", "v48", "v49", "v50", "v51", "v52", "v53", "v54", "v55", "v56", "v57", "v58", "v59", "v60", "v61", "v62", "v63", "v64", "v65", "v66", "v67", "v68", "v69", "v70", "v71", "v72", "v73", "v74", "v75", "v76", "v77", "v78", "v79", "v80", "v81", "v82", "v83", "v84", "v85", "v86", "v87", "v88", "v89", "v90", "v91", "v92", "v93", "v94", "v95", "v96", "v97", "v98", "v99", "v100", "v101", "v102", "v103", "v104", "v105", "v106", "v107", "v108", "v109", "v110", "v111", "v112", "v113", "v114", "v115", "v116", "v117", "v118", "v119", "v120", "v121", "v122", "v123", "v124", "v125", "v126", "v127", "v128", "v129", "v130", "v131", "v132", "v133", "v134", "v135", "v136", "v137", "v138", "v139", "v140", "v141", "v142", "v143", "v144", "v145", "v146", "v147", "v148", "v149", "v150", "v151", "v152", "v153", "v154", "v155", "v156", "v157", "v158", "v159", "v160", "v161", "v162", "v163", "v164", "v165", "v166", "v167", "v168", "v169", "v170", "v171", "v172", "v173", "v174", "v175", "v176", "v177", "v178", "v179", "v180", "v181", "v182", "v183", "v184", "v185", "v186", "v187", "v188", "v189", "v190", "v191", "v192", "v193", "v194", "v195", "v196", "v197", "v198", "v199", "v222", "v223", "v224", "v225", "v226", "v227", "v228", "v229", "v230", "v231", "v232", "v233", "v234", "v235", "v236", "v237", "v238", "v239", "v240", "v241", "v242", "v243", "v244", "v245", "v246", "v247", "v248", "v249", "v250", "v251", "v252", "v253", "v254", "v255", "a0", "a1", "a2", "a3", "a4", "a5", "a6", "a7", "a8", "a9", "a10", "a11", "a12", "a13", "a14", "a15", "a16", "a17", "a18", "a19", "a20", "a21", "a22", "a23", "a24", "a25", "a26", "a27", "a28", "a29", "a30", "a31", "a32", "a33", "a34", "a35", "a36", "a37", "a38", "a39", "a40", "a41", "a42", "a43", "a44", "a45", "a46", "a47", "a48", "a49", "a50", "a51", "a52", "a53", "a54", "a55", "a56", "a57", "a58", "a59", "a60", "a61", "a62", "a63", "a64", "a65", "a66", "a67", "a68", "a69", "a70", "a71", "a72", "a73", "a74", "a75", "a76", "a77", "a78", "a79", "a80", "a81", "a82", "a83", "a84", "a85", "a86", "a87", "a88", "a89", "a90", "a91", "a92", "a93", "a94", "a95", "a96", "a97", "a98", "a99", "a100", "a101", "a102", "a103", "a104", "a105", "a106", "a107", "a108", "a109", "a110", "a111", "a112", "a113", "a114", "a115", "a116", "a117", "a118", "a119", "a120", "a121", "a122", "a123", "a124", "a125", "a126", "a127", "a128", "a129", "a130", "a131", "a132", "a133", "a134", "a135", "a136", "a137", "a138", "a139", "a140", "a141", "a142", "a143", "a144", "a145", "a146", "a147", "a148", "a149", "a150", "a151", "a152", "a153", "a154", "a155", "a156", "a157", "a158", "a159", "a160", "a161", "a162", "a163", "a164", "a165", "a166", "a167", "a168", "a169", "a170", "a171", "a172", "a173", "a174", "a175", "a176", "a177", "a178", "a179", "a180", "a181", "a182", "a183", "a184", "a185", "a186", "a187", "a188", "a189", "a190", "a191", "a192", "a193", "a194", "a195", "a196", "a197", "a198", "a199", "a200", "a201", "a202", "a203", "a204", "a205", "a206", "a207", "a208", "a209", "a210", "a211", "a212", "a213", "a214", "a215", "a216", "a217", "a218", "a219", "a220", "a221", "a222", "a223", "a224", "a225", "a226", "a227", "a228", "a229", "a230", "a231", "a232", "a233", "a234", "a235", "a236", "a237", "a238", "a239", "a240", "a241", "a242", "a243", "a244", "a245", "a246", "a247", "a248", "a249", "a250", "a251", "a252", "a253", "a254", "a255", "s33", "s34", "s35", "s36", "s37", "s38", "s39", "s40", "s41", "s42", "s43", "s44", "s45", "s46", "s47", "s48", "s49", "s50", "s51", "s52", "s53", "s54", "s55", "s56", "s57", "s58", "s59", "s60", "s61", "s62", "s63", "s64", "s65", "s66", "s67", "s68", "s69", "s70", "s71", "s72", "s73", "s74", "s75", "s76", "s77", "s78", "s79", "s80", "s81", "s82", "s83", "s84", "s85", "s86", "s87", "s88", "s89", "s90", "s91", "s92", "s93", "s94", "s95", "s96", "s97", "s98", "s99", "s100", "s101", "vcc", "scc", "memory"
#define CV_CLOB "v0", "v1", "v2", "v3", "v4", "v5", "v6", "v7", "v8", "v9", "v10", "v11", "v12", "v13", "v14", "v15", "v16", "v17", "v18", "v19", "v20", "v21", "v22", "v23", "v24", "v25", "v26", "v27", "v28", "v29", "v30", "v31", "v32", "v33", "v34", "v35", "v36", "v37", "v38", "v39", "v40", "v41", "v42", "v43", "v44", "v45", "v46", "v47", "v48", "v49", "v50", "v51", "v52", "v53", "v54", "v55", "v56", "v57", "v58", "v59", "v60", "v61", "v62", "v63", "v64", "v65", "v66", "v67", "v68", "v69", "v70", "v71", "v72", "v73", "v74", "v75", "v76", "v77", "v78", "v79", "v80", "v81", "v82", "v83", "v84", "v85", "v86", "v87", "v88", "v89", "v90", "v91", "v92", "v93", "v94", "v95", "v96", "v97", "v98", "v99", "v100", "v101", "v102", "v103", "v104", "v105", "v106", "v107", "v108", "v109", "v110", "v111", "v112", "v113", "v114", "v115", "v116", "v117", "v118", "v119", "v120", "v121", "v122", "v123", "v124", "v125", "v126", "v127", "v128", "v129", "v130", "v131", "v132", "v133", "v134", "v135", "v136", "v137", "v138", "v139", "v140", "v141", "v142", "v143", "v144", "v145", "v146", "v147", "v148", "v149", "v150", "v151", "v152", "v153", "v154", "v155", "v156", "v157", "v158", "v159", "v160", "v161", "v162", "v163", "v164", "v165", "v166", "v167", "v168", "v169", "v170", "v171", "v172", "v173", "v174", "v175", "v176", "v177", "v178", "v179", "v180", "v181", "v182", "v183", "v184", "v185", "v186", "v187", "v188", "v189", "v190", "v191", "v192", "v193", "v194", "v195", "s69", "scc", "memory"
static constexpr unsigned G_STEP = 0x10000;
static constexpr unsigned G_WS_CTL = 0x300000;
static constexpr unsigned G_WS_FIX = 0x580000;
static constexpr unsigned G_WS_DONE = 0x5f0000;
static constexpr unsigned G_WS_EP = 0x5f2000;
static constexpr unsigned G_WS_STAMPS = 0xc000000;
static constexpr unsigned G_C_TASK = 0x200;
static constexpr unsigned G_C_GX = 0x1000;
static constexpr unsigned G_C_GV = 0x4000;
static constexpr unsigned G_C_KM = 0x8000;
static constexpr unsigned G_C_UM = 0x9000;
static constexpr unsigned G_C_PC = 0xc000;
static constexpr unsigned G_C_YC = 0xd000;
static constexpr unsigned G_C_GQ = 0x10000;
static constexpr unsigned G_C_FX = 0x280000;
static constexpr unsigned G_C_ERR = 0x280100;
static constexpr unsigned G_C_ARR = 0x280400;
static constexpr unsigned G_C_GO = 0x280800;
static constexpr unsigned G_C_UT = 0x281000;
static constexpr unsigned G_C_RDY = 0x282000;
static constexpr unsigned G_C_GT = 0x284000;
static constexpr unsigned G_C_KG = 0x290000;
static constexpr unsigned G_C_EF = 0x288000;
static constexpr unsigned G_C_RW = 0x280000;
static constexpr unsigned G_WS_H = 0x0;
static constexpr unsigned G_HS8 = 0x80000;
static constexpr unsigned G_HS4 = 0x40000;
static constexpr unsigned G_UXS = 0x200000;
static constexpr unsigned G_UMS = 0x90000;
static constexpr unsigned G_PROBE = 0x1b9400;
static constexpr unsigned G_D_QX = 0x600000;
static constexpr unsigned G_D_KX = 0x1600000;
static constexpr unsigned G_D_KM = 0x2600000;
static constexpr unsigned G_D_UX = 0x2e00000;
static constexpr unsigned G_D_UM = 0x7600000;
static constexpr unsigned G_D_PX = 0xa200000;
static constexpr unsigned G_D_PM = 0xa600000;
static constexpr unsigned G_D_KT = 0xa700000;
static constexpr unsigned G_D_UT = 0xac00000;
static constexpr unsigned G_D_YB = 0xaf00000;
static constexpr unsigned G_D_END = 0xb200000;
static constexpr unsigned G_SKM = 0x7;
static constexpr unsigned G_S1 = 0x18;
static constexpr unsigned G_G2 = 0x0;
static constexpr unsigned G_S0 = 0x7;
static constexpr unsigned G_RSW = 0x0;
static constexpr unsigned G_GVN = 0x2;
static constexpr unsigned G_STG = 0x9000;
static constexpr unsigned G_KROW = 0x30;
static constexpr unsigned G_NHL = 0x1;
#define FLG(x_) (kDefault[5])
#define FLH(x_) (kDefault[7])
static constexpr int kDefault[8] = {PRM_DEFAULT};
#define MFMA32(a_, b_, c_) __builtin_amdgcn_mfma_f32_16x16x32_bf16(__builtin_bit_cast(bf16x8, a_), __builtin_bit_cast(bf16x8, b_), c_, 0, 0, 0)
#define MFMA16(a_, b_, c_) __builtin_amdgcn_mfma_f32_16x16x16bf16_1k(__builtin_bit_cast(s16x4, a_), __builtin_bit_cast(s16x4, b_), c_, 0, 0, 0)
#define WLD8(d_, voff_, base_, off_) asm volatile("s_nop 4\n\tglobal_load_dwordx2 %0, %1, %2 offset:%3 sc1" : "=v"(d_) : "v"(voff_), "s"(base_), "i"(off_) : "memory")
#define WLD16(d_, voff_, base_, off_) asm volatile("s_nop 4\n\tglobal_load_dwordx4 %0, %1, %2 offset:%3 sc1" : "=v"(d_) : "v"(voff_), "s"(base_), "i"(off_) : "memory")
#define WST16(voff_, d_, base_, off_) asm volatile("s_nop 4\n\tglobal_store_dwordx4 %0, %1, %2 offset:%3 sc1\n\ts_nop 1" :: "v"(voff_), "v"(d_), "s"(base_), "i"(off_) : "memory")
#define OMOD ""
#define OST8(p_, d_) asm volatile("global_store_dwordx2 %0, %1, off " OMOD :: "v"(p_), "v"(d_) : "memory")
#define OST16(p_, d_) asm volatile("global_store_dwordx4 %0, %1, off " OMOD "\n\ts_nop 1" :: "v"(p_), "v"(d_) : "memory")
#define OWT 0
#define OWM "sc1"
#define OST16W(p_, d_) asm volatile("global_store_dwordx4 %0, %1, off " OWM "\n\ts_nop 1" :: "v"(p_), "v"(d_) : "memory")
#define WLD16X4(d0, d1, d2, d3, voff_, base_) asm volatile("s_nop 4\n\tglobal_load_dwordx4 %0, %4, %5 sc1\n\tglobal_load_dwordx4 %1, %4, %5 offset:1024 sc1\n\t" \
    "global_load_dwordx4 %2, %4, %5 offset:2048 sc1\n\tglobal_load_dwordx4 %3, %4, %5 offset:3072 sc1" \
    : "=&v"(d0), "=&v"(d1), "=&v"(d2), "=&v"(d3) : "v"(voff_), "s"(base_) : "memory")

__device__ __forceinline__ u32x4 pack8(const f32x4& a, const f32x4& b) {
    return u32x4{pk2bf(a[0], a[1]), pk2bf(a[2], a[3]), pk2bf(b[0], b[1]), pk2bf(b[2], b[3])};
}
template <class T> __device__ __forceinline__ T* uni(T* q) {
    const u64 v = (u64)q;
    unsigned lo = __builtin_amdgcn_readfirstlane((unsigned)v), hi = __builtin_amdgcn_readfirstlane((unsigned)(v >> 32));
    asm volatile("s_nop 4" : "+s"(lo), "+s"(hi));
    return (T*)(((u64)hi << 32) | lo);
}
#define WC(p_) ((char*)(p_).X)
template <bool FAST = false>
__device__ __forceinline__ void wait_word(const MainP& p, const char* f, int want, unsigned code) {
    unsigned tries = 0;
#pragma unroll 1
    while (__any((int)__hip_atomic_load((const gu32*)f, RLX_AGENT) < want)) {
        if (++tries > kWaitCap) { __hip_atomic_store((gu32*)(p.ws + G_WS_CTL + G_C_ERR), code, RLX_AGENT); break; }
        if ((tries & 1023u) == 0u && __hip_atomic_load((const gu32*)(p.ws + G_WS_CTL + G_C_ERR), RLX_AGENT) != 0xffffffffu) break;
        if (FAST) __builtin_amdgcn_s_sleep(1); else __builtin_amdgcn_s_sleep(10);
    }
}

__device__ __forceinline__ int skew(int s, int id) { return (s ^ (G_SKM * id)) & 63; }
__device__ __forceinline__ unsigned um_base(int s, unsigned nm) { return G_D_UM + (s / 3) * 0x200000 + (s % 3) * G_UMS + ((nm >> (2 + s / 3)) & 1) * 8192; }
typedef GAS unsigned long long gu64;
#define IST(k_, slot_)
__device__ __forceinline__ void init_done(char* ws, int slot, unsigned val) {
    const unsigned e = __hip_atomic_load((const gu32*)(ws + G_WS_EP), RLX_AGENT);
    __hip_atomic_store((gu64*)(ws + G_WS_DONE + slot * 64), ((u64)val << 32) | (e + 1u), RLX_AGENT);
}
__device__ __forceinline__ void init_wait(char* ws, int w, int lane) {
    if (w == 0) {
        const unsigned e = __hip_atomic_load((const gu32*)(ws + G_WS_EP), RLX_AGENT);
        for (unsigned tries = 0; tries < kWaitCap; ++tries) {
            const unsigned d0 = __hip_atomic_load((const gu32*)(ws + G_WS_DONE + lane * 64), RLX_AGENT), d1 = __hip_atomic_load((const gu32*)(ws + G_WS_DONE + 2048 + lane * 64), RLX_AGENT);
            if (!__any((d0 != e + 1u) | (d1 != e + 1u))) break;
            __builtin_amdgcn_s_sleep(8);
        }
    }
    __syncthreads();
    if (w == 0 && lane == 0) __hip_atomic_fetch_add((gu32*)(ws + G_WS_CTL + G_C_ARR), 1u, RLX_AGENT);
}
__device__ __forceinline__ void fill_block(char* ws, int b, int tid, const u32x4& v0, const u32x4& v1) {
    const unsigned o = b * 8192 + tid * 16, o2 = o + 4096;
    asm volatile("global_store_dwordx4 %0, %1, %2 sc1\n\ts_nop 1" :: "v"(o), "v"(v0), "s"(ws) : "memory");
    if (b >= 256 || (b & 12) != 12 || tid < 64)
        asm volatile("global_store_dwordx4 %0, %1, %2 sc1\n\ts_nop 1" :: "v"(o2), "v"(v1), "s"(ws) : "memory");
}
__device__ __forceinline__ void init_fill(char* ws, const u16* mem0, int f, int tid) {
    const u32x4 sent = {0xffffffffu, 0xffffffffu, 0xffffffffu, 0xffffffffu};
    u32x4 v0 = sent, v1 = sent;
    if (f < 32 && (f & 8)) {
#pragma unroll
        for (int k = 0; k < 2; ++k) {
            const int q = tid + 256 * k + ((f & 4) ? 256 : 0), m = q & 15;
            if (!(f & 4) || (q >= 512 && q < 576)) {
                const unsigned* src = (const unsigned*)(mem0 + (2 * (m >> 2) + (m & 1)) * DM + 16 * (q >> 4) + (m & 2));
                const u32x4 o = {min(src[0], 0xfffffffeu), min(src[2], 0xfffffffeu), min(src[4], 0xfffffffeu), min(src[6], 0xfffffffeu)};
                if (k) v1 = o; else v0 = o;
            }
        }
    }
#pragma unroll 1
    for (int q = f; q < 185; q += 48) {
        if (q >= 136 && q < 152 && (tid & 127)) { v0 = u32x4{0u, 0u, 0u, 0u}; v1 = v0; }
        fill_block(ws, q < 64 ? q : q < 136 ? q - 64 + G_D_UM / 8192 : q < 168 ? 256 + ((q - 136) >> 3) * 16 + (q & 7) : q < 178 ? q - 168 + G_WS_CTL / 8192 : q - 178 + G_WS_FIX / 8192, tid, v0, v1);
        v0 = sent; v1 = sent;
    }
    const unsigned e = __hip_atomic_load((const gu32*)(ws + G_WS_EP), RLX_AGENT);
    asm volatile("s_waitcnt vmcnt(0)" :: "v"(e) : "memory");
    __syncthreads();
    if (tid == 0) __hip_atomic_store((gu64*)(ws + G_WS_DONE + (48 + f) * 64), (u64)(e + 1u), RLX_AGENT);
    if (tid == 0) IST(0, 48 + f);
}
__device__ __forceinline__ void init_cal(char* ws, int id, int odd, int tid, int n) {
    if (tid < 64) {
        const int pg = id >> 1;
        char* a = ws + (pg < 2 ? pg * 0x200000u : G_D_UM + (pg - 2) * 0x200000u) + G_PROBE + (id & 1) * 8192;
        const u32x4 z = {0u, 0u, 0u, 0u};
        asm volatile("global_store_dwordx4 %0, %2, off sc1\n\tglobal_store_dwordx4 %1, %2, off sc1\n\ts_waitcnt vmcnt(0)" :: "v"(a + tid * 16), "v"(a + 16384 + tid * 16), "v"(z) : "memory");
        if (tid == 0) {
            unsigned b0 = 0xffffu, b1 = 0xffffu;
            const unsigned ts1 = (unsigned)__builtin_amdgcn_s_memrealtime() << 4;
#pragma unroll 1
            for (int it = 0; it < n; ++it) {
                const unsigned c0 = (unsigned)__builtin_amdgcn_s_memtime();
                const unsigned v = __hip_atomic_load((const gu32*)(a + (it & 1) * 16384 + (it >> 1) * 128), RLX_AGENT);
                asm volatile("s_waitcnt vmcnt(0)" :: "v"(v) : "memory");
                const unsigned t = (unsigned)__builtin_amdgcn_s_memtime() - c0;
                if (it & 1) b1 = min(b1, t); else b0 = min(b0, t);
            }
            init_done(ws, id, 2 * min(b0, b1) + ((b1 < b0 ? 1 : 0) ^ odd));
            IST(0, id);
        }
    }
    __syncthreads();
}
__device__ __forceinline__ void near_mask(char* ws, int lane, int& nm, int& nc) {
    const gu32* q = (const gu32*)(ws + G_WS_DONE + (lane & 31) * 128 + 4);
    const unsigned v0 = __hip_atomic_load(q, RLX_AGENT), v1 = __hip_atomic_load(q + 16, RLX_AGENT);
    nm = (int)__ballot(v1 < v0);
    nc = (int)__ballot(min(v0, v1) & 1u);
}

#define TST(slot_, i_)
#define PIN8(a) asm volatile("" : "+v"(a[0]), "+v"(a[1]), "+v"(a[2]), "+v"(a[3]), "+v"(a[4]), "+v"(a[5]), "+v"(a[6]), "+v"(a[7]))
struct AOps { u32x4 q[4], k[8]; int fl; };
struct APtr { const char* q; const char* k; const char* m; unsigned voff, vkm, voffm, vfl; };
__device__ __forceinline__ void attn_batch(const MainP& p, AOps& o, const APtr& a) {
    const char* q2 = a.q + 4096; const char* k2 = a.k + 4096;
    WLD16(o.q[0], a.voff, a.q, 0); WLD16(o.q[1], a.voff, a.q, 2048); WLD16(o.q[2], a.voff, q2, 0); WLD16(o.q[3], a.voff, q2, 2048);
    WLD16X4(o.k[0], o.k[1], o.k[2], o.k[3], a.voff, a.k); WLD16X4(o.k[4], o.k[5], o.k[6], o.k[7], a.voff, k2);
    asm volatile("global_load_dword %0, %1, %2 sc1" : "=v"(o.fl) : "v"(a.vfl), "s"(WC(p)) : "memory");
}
__device__ __forceinline__ void attn_pin(AOps& o) {
    asm volatile("" : "+v"(o.fl), "+v"(o.q[0]), "+v"(o.q[1]), "+v"(o.q[2]), "+v"(o.q[3])); PIN8(o.k);
}
static constexpr int NFINE = 6;
static constexpr int NG1 = 64 - NFINE;
__device__ __forceinline__ void score_round4(float* r, const f32x4& x) {
    asm volatile("v_cvt_pk_bf16_f32 %0, 0, %4\n\tv_cvt_pk_bf16_f32 %1, 0, %5\n\tv_cvt_pk_bf16_f32 %2, 0, %6\n\tv_cvt_pk_bf16_f32 %3, 0, %7\n\t"
                 "v_mul_f32 %0, 0x3db504f3, %0\n\tv_mul_f32 %1, 0x3db504f3, %1\n\tv_mul_f32 %2, 0x3db504f3, %2\n\tv_mul_f32 %3, 0x3db504f3, %3\n\t"
                 "v_cvt_pk_bf16_f32 %0, 0, %0\n\tv_cvt_pk_bf16_f32 %1, 0, %1\n\tv_cvt_pk_bf16_f32 %2, 0, %2\n\tv_cvt_pk_bf16_f32 %3, 0, %3"
                 : "=&v"(r[0]), "=&v"(r[1]), "=&v"(r[2]), "=&v"(r[3]) : "v"(x[0]), "v"(x[1]), "v"(x[2]), "v"(x[3]));
}
__device__ __forceinline__ float vmax3(float a, float b, float c) { float r; asm("v_max3_f32 %0, %1, %2, %3" : "=v"(r) : "v"(a), "v"(b), "v"(c)); return r; }
template <int N>
__device__ __forceinline__ void attn_soft(float (&q)[12], u32x2& Pm, u32x4& Px) {
    float m = vmax3(q[0], q[1], q[2]);
#pragma unroll
    for (int i = 3; i < N; i += 2) m = vmax3(m, q[i], q[i + 1 < N ? i + 1 : i]);
    { auto a = __builtin_amdgcn_permlane16_swap(__float_as_uint(m), __float_as_uint(m), false, false); m = vmax3(m, __uint_as_float(a[0]), __uint_as_float(a[1])); }
    { auto a = __builtin_amdgcn_permlane32_swap(__float_as_uint(m), __float_as_uint(m), false, false); m = vmax3(m, __uint_as_float(a[0]), __uint_as_float(a[1])); }
    const float ml = -m * 1.4426950408889634f;
#pragma unroll
    for (int i = 0; i < N; ++i) q[i] = __builtin_amdgcn_exp2f(fmaf(q[i], 1.4426950408889634f, ml));
    float sum = 0.5f * ((q[0] + q[1]) + (q[2] + q[3])) + ((q[4] + q[5]) + (q[6] + q[7]));
    if (N > 8) sum += (q[8] + q[9]) + (q[10] + q[11]);
    sum = rowsum4(sum);
    const float inv = __builtin_amdgcn_rcpf(sum);
    Pm = u32x2{pk2bf(q[0] * inv, q[1] * inv), pk2bf(q[2] * inv, q[3] * inv)};
    Px = u32x4{pk2bf(q[4] * inv, q[5] * inv), pk2bf(q[6] * inv, q[7] * inv), N > 8 ? pk2bf(q[8] * inv, q[9] * inv) : 0u, N > 8 ? pk2bf(q[10] * inv, q[11] * inv) : 0u};
}
#define NASPLIT 12
#define KTOUCH 0
#define HSL 1
#define NAG 2
static constexpr int NA = NASPLIT;
__device__ __forceinline__ int attn_next(int s) { return s < NA ? s + 2 : s + NAG; }
__device__ __forceinline__ unsigned attn_flag(int s, int hq) { return G_WS_CTL + G_C_PC + (s < NA ? s & 1 : (s - NA) % NAG) * 1024 + hq * 128; }
struct BOps { u32x4 q[8], k[8]; };
#define WLD16X4A(d0, d1, d2, d3, voff_, base_) asm volatile("s_nop 4\n\tglobal_load_dwordx4 %0, %4, %5 sc1\n\tglobal_load_dwordx4 %1, %4, %5 offset:1024 sc1\n\t" \
    "global_load_dwordx4 %2, %4, %5 offset:2048 sc1\n\tglobal_load_dwordx4 %3, %4, %5 offset:3072 sc1" \
    : "=&a"(d0), "=&a"(d1), "=&a"(d2), "=&a"(d3) : "v"(voff_), "s"(base_) : "memory")
#define PIN8A(a) asm volatile("" : "+a"(a[0]), "+a"(a[1]), "+a"(a[2]), "+a"(a[3]), "+a"(a[4]), "+a"(a[5]), "+a"(a[6]), "+a"(a[7]))
#define MFMA_AV0(acc, a_, b_) asm volatile("v_mfma_f32_16x16x32_bf16 %0, %1, %2, 0" : "=&v"(acc) : "a"(a_), "v"(b_))
#define MFMA_AV(acc, a_, b_) asm volatile("v_mfma_f32_16x16x32_bf16 %0, %1, %2, %0" : "+v"(acc) : "a"(a_), "v"(b_))
__device__ __forceinline__ void attn_batch8(const MainP& p, BOps& o, const APtr& a) {
    const char* q2 = a.q + 4096; const char* k2 = a.k + 4096;
    WLD16X4(o.q[0], o.q[1], o.q[2], o.q[3], a.voff, a.q); WLD16X4(o.q[4], o.q[5], o.q[6], o.q[7], a.voff, q2);
    WLD16X4A(o.k[0], o.k[1], o.k[2], o.k[3], a.voff, a.k); WLD16X4A(o.k[4], o.k[5], o.k[6], o.k[7], a.voff, k2);
}
__device__ __forceinline__ void attn_step(const MainP& p, BOps& o, BOps& n1, BOps& n2, APtr& a, int& kg, int& kk, int s, int h, int w, int lane) {
    const int g = lane >> 4, c = lane & 15;
    const float scale = 0.08838834764831845f;
    TST(s * 64 + h * 2, 0);
    const int s2 = min(attn_next(attn_next(s)), NSEG - 1);
    if (kg < s2 || kk < s) {
        VMC0();
        if (s < 50) wait_word(p, WC(p) + a.vfl, (lane & 1) ? s : s2, 13u); else wait_word<true>(p, WC(p) + a.vfl, (lane & 1) ? s : s2, 13u);
    }
    TST(s * 64 + h * 2, 1);
    a.q = p.ws + G_D_QX + (size_t)h * G_HS8 + skew(s2, h) * 8192; a.k = a.q + (G_D_KX - G_D_QX); a.m = p.ws + G_D_KM + (size_t)h * G_HS4 + skew(s, h) * 4096;
    __builtin_amdgcn_sched_barrier(0);
    u32x4 km[4];
    int fl;
    WLD16X4A(km[0], km[1], km[2], km[3], a.vkm, a.m);
    asm volatile("global_load_dword %0, %1, %2 sc1" : "=v"(fl) : "v"(a.vfl), "s"(WC(p)) : "memory");
    attn_batch8(p, n2, a);
    f32x4 y[5];
#pragma unroll
    for (int wc = 0; wc < 4; ++wc)
#pragma unroll
        for (int i = 0; i < 3; ++i) { if (wc == 0) MFMA_AV0(y[i], o.k[i >> 1], o.q[(i + 1) >> 1]); else MFMA_AV(y[i], o.k[wc * 2 + (i >> 1)], o.q[wc * 2 + ((i + 1) >> 1)]); }
    asm volatile("s_nop 7" : "+v"(y[0]), "+v"(y[1]), "+v"(y[2]));
    float q0[12], q1[12];
    score_round4(q0 + 4, y[0]); score_round4(q1 + 4, y[1]); score_round4(q1 + 8, y[2]);
#pragma unroll
    for (int i = 0; i < 4; ++i) {
        const bool vis = 4 * g + i <= c;
        q0[4 + i] = vis ? q0[4 + i] : -INFINITY; q1[8 + i] = vis ? q1[8 + i] : -INFINITY;
    }
    __builtin_amdgcn_sched_barrier(0);
    asm volatile("s_waitcnt vmcnt(16)\n\ts_barrier" : "+a"(km[0]), "+a"(km[1]), "+a"(km[2]), "+a"(km[3]), "+v"(fl) :: "memory");
    PIN8(n1.q); PIN8A(n1.k);
    { const unsigned fo = attn_flag(s, h >> 2); const int fv = s - 1;
      asm volatile("s_cmp_lg_u32 %3, 0\n\ts_cbranch_scc1 1f\n\tglobal_store_dword %0, %1, %2 sc1\n1:" :: "v"(fo), "v"(fv), "s"(WC(p)), "s"(w) : "scc", "memory"); }
#pragma unroll
    for (int wc = 0; wc < 4; ++wc)
#pragma unroll
        for (int i = 0; i < 2; ++i) { if (wc == 0) MFMA_AV0(y[3 + i], km[0], o.q[i]); else MFMA_AV(y[3 + i], km[wc], o.q[wc * 2 + i]); }
    asm volatile("s_nop 7" : "+v"(y[3]), "+v"(y[4]));
    kg = __builtin_amdgcn_readlane(fl, 0); kk = __builtin_amdgcn_readlane(fl, 1);
    score_round4(q0, y[3]); score_round4(q1, y[4]);
    u32x2 Pm0, Pm1; u32x4 Px0, Px1;
    attn_soft<8>(q0, Pm0, Px0); attn_soft<12>(q1, Pm1, Px1);
    char* pxb = p.ws + G_D_PX + (size_t)(s * 32 + h) * 2048;
    char* pmb = p.ws + G_D_PM + (size_t)s * 16384 + h * 256; char* pm2 = pmb + 8192;
    WST16(a.voff, Px0, pxb, 0); WST16(a.voff, Px1, pxb, 1024);
    asm volatile("global_store_dwordx2 %0, %1, %3 sc1\n\tglobal_store_dwordx2 %0, %2, %4 sc1" :: "v"(a.voffm), "v"(Pm0), "v"(Pm1), "s"(pmb), "s"(pm2) : "memory");
    { const unsigned fo = attn_flag(s, h >> 2);
      asm volatile("s_cmp_lt_u32 %4, 46\n\ts_cbranch_scc1 1f\n\ts_waitcnt vmcnt(0)\n\ts_barrier\n\ts_cmp_lg_u32 %3, 0\n\ts_cbranch_scc1 1f\n\tglobal_store_dword %0, %1, %2 sc1\n1:"
                   :: "v"(fo), "v"(s), "s"(WC(p)), "s"(w), "s"(s) : "scc", "memory"); }
    TST(s * 64 + h * 2, 2);
}
__device__ __forceinline__ void attn_role(const MainP& p, int s, int h, int w, int lane) {
    APtr a;
    a.q = p.ws + G_D_QX + (size_t)h * G_HS8 + skew(s, h) * 8192; a.k = a.q + (G_D_KX - G_D_QX);
    a.voff = lane * 16; a.vkm = (lane & 55) * 16; a.voffm = (lane & 15) * 16 + ((lane >> 4) & 1) * 8;
    a.vfl = G_WS_CTL + ((lane & 1) ? G_C_KM : G_C_GQ) + h * 128;
    BOps o0, o1, o2;
    wait_word(p, WC(p) + G_WS_CTL + G_C_GQ + h * 128, attn_next(s), 10u);
    attn_batch8(p, o0, a);
    a.q = p.ws + G_D_QX + (size_t)h * G_HS8 + skew(attn_next(s), h) * 8192; a.k = a.q + (G_D_KX - G_D_QX);
    attn_batch8(p, o1, a);
    VMC0();
    PIN8(o0.q); PIN8A(o0.k); PIN8(o1.q); PIN8A(o1.k);
    int kg = -1, kk = -1;
    static_assert(NA % 2 == 0, "ranges");
#pragma unroll 1
    for (;;) {
        attn_step(p, o0, o1, o2, a, kg, kk, s, h, w, lane);
        if ((s = attn_next(s)) >= NG1) break;
        attn_step(p, o1, o2, o0, a, kg, kk, s, h, w, lane);
        if ((s = attn_next(s)) >= NG1) break;
        attn_step(p, o2, o0, o1, a, kg, kk, s, h, w, lane);
        if ((s = attn_next(s)) >= NG1) break;
    }
    VMC0();
}
__device__ __forceinline__ unsigned umax4(const u32x4& v) { return max(max(v[0], v[1]), max(v[2], v[3])); }
__device__ __forceinline__ bool give_up(const MainP& p, unsigned& tries, unsigned code) {
    if (++tries <= kWaitCap / 4) return false;
    __hip_atomic_store((gu32*)(p.ws + G_WS_CTL + G_C_ERR), code, RLX_AGENT);
    return true;
}
__device__ __forceinline__ void task_attn_tail(const MainP& p, int s, int h, int w, int lane) {
    const int g = lane >> 4, c = lane & 15, qt = w & 1;
    const float scale = 0.08838834764831845f;
    APtr a;
    a.q = p.ws + G_D_QX + (size_t)h * G_HS8 + skew(s, h) * 8192 + qt * 1024; a.k = p.ws + G_D_KX + (size_t)h * G_HS8 + skew(s, h) * 8192; a.m = p.ws + G_D_KM + (size_t)h * G_HS4 + skew(s, h) * 4096;
    a.voff = lane * 16; a.vkm = (lane & 55) * 16; a.voffm = c * 16 + (g & 1) * 8; a.vfl = G_WS_CTL;
    AOps o;
    wait_word(p, WC(p) + G_WS_CTL + G_C_GQ + h * 128, s, 14u);
    attn_batch(p, o, a);
    VMC0();
    attn_pin(o);
    float q[12];
    u32x4 km[4];
#pragma unroll
    for (int kt = 1; kt >= -1; --kt) {
        if (kt < 0) {
            TST(s * 64 + h * 2 + qt, 0);
            wait_word(p, WC(p) + G_WS_CTL + G_C_KM + h * 128, s - 2, 15u);
            if (FLG(p.prm[5]) & 2) wait_word<true>(p, WC(p) + G_WS_CTL + G_C_KM + h * 128, s - 1, 15u);
            for (int i = 0; i < ((FLH(p.prm[7]) >> 14) & 15); ++i) __builtin_amdgcn_s_sleep(1);
            for (unsigned tries = 0;;) {
                WLD16X4(km[0], km[1], km[2], km[3], a.vkm, a.m);
                asm volatile("s_waitcnt vmcnt(0)" : "+v"(km[0]), "+v"(km[1]), "+v"(km[2]), "+v"(km[3]) :: "memory");
                if (!__any(max(max(umax4(km[0]), umax4(km[1])), max(umax4(km[2]), umax4(km[3]))) == 0xffffffffu) || give_up(p, tries, 16u)) break;
            }
            TST(s * 64 + h * 2 + qt, 1);
        }
        f32x4 y = {0.f, 0.f, 0.f, 0.f};
#pragma unroll
        for (int wc = 0; wc < 4; ++wc) y = MFMA32(kt < 0 ? km[wc] : o.k[wc * 2 + (kt < 0 ? 0 : kt)], o.q[wc], y);
#pragma unroll
        for (int i = 0; i < 4; ++i) q[4 + 4 * kt + i] = (16 * kt + 4 * g + i <= 16 * qt + c) ? bfr(bfr(y[i]) * scale) : -INFINITY;
    }
    u32x2 Pm; u32x4 Px;
    attn_soft<12>(q, Pm, Px);
    char* pxb = p.ws + G_D_PX + (size_t)(s * 32 + h) * 2048 + qt * 1024;
    char* pmb = p.ws + G_D_PM + (size_t)s * 16384 + h * 256 + qt * 8192;
    WST16(a.voff, Px, pxb, 0);
    asm volatile("global_store_dwordx2 %0, %1, %2 sc1" :: "v"(a.voffm), "v"(Pm), "s"(pmb) : "memory");
    TST(s * 64 + h * 2 + qt, 2);
}

#define RLD(d_, voff_, base_, off_) asm volatile("global_load_dwordx4 %0, %1, %2 offset:%3 sc1" : "=v"(d_) : "v"(voff_), "s"(base_), "i"(off_) : "memory")
#define RLDA(d_, voff_, base_, off_) asm volatile("global_load_dwordx4 %0, %1, %2 offset:%3 sc1" : "=a"(d_) : "v"(voff_), "s"(base_), "i"(off_) : "memory")
#define MFMA_ACC_A(acc, a_, b_) asm volatile("v_mfma_f32_16x16x32_bf16 %0, %1, %2, %0" : "+v"(acc) : "a"(a_), "v"(b_))
#define VMW(n_) asm volatile("s_waitcnt vmcnt(%0)" :: "i"(n_) : "memory")
template <int NT, int DEPTH, bool TAIL, int NM = 2>
__device__ __forceinline__ void task_out(const MainP& p, char* smem, int s, int ct0, int w, int lane, int m0 = 0) {
    const int g = lane >> 4, c = lane & 15;
    const unsigned voff = lane * 16;
    f32x4 acc[NT][2];
#pragma unroll
    for (int t = 0; t < NT; ++t) { acc[t][0] = f32x4{0.f, 0.f, 0.f, 0.f}; acc[t][1] = acc[t][0]; asm volatile("" : "+v"(acc[t][0]), "+v"(acc[t][1])); }
    const char* ub[NT];
#pragma unroll
    for (int t = 0; t < NT; ++t) ub[t] = uni(p.ws + G_D_UX + (size_t)(ct0 + t) * G_UXS + skew(s, ct0 + t) * 32768 + w * 8192);
    const char* pb = uni(p.ws + G_D_PX + (size_t)(s * 32 + w * 8) * 2048 + m0 * 1024);
    const char* um = uni(p.ws + um_base(s, p.prm[0]));
    const int cbit = (p.prm[1] >> (2 + s / 3)) & 1;
    unsigned vu[NT];
#pragma unroll
    for (int t = 0; t < NT; ++t) { const int j = ct0 + t, xd = j / 9; const int pl = (j - 9 * ((xd + 1) >> 1)) * 32768; vu[t] = pl + ((xd ^ cbit ^ ((pl + (s % 3) * G_UMS) >> 19)) & 1) * 16384 + w * 2048 + voff; asm volatile("" : "+v"(vu[t])); }
    const char* pm = uni(p.ws + G_D_PM + (size_t)s * 16384 + w * 2048 + m0 * 8192);
    constexpr int NR = DEPTH + 1 > 10 ? 10 : DEPTH + 1;
    u32x4 a[NR][NT], b[10][2];
    unsigned vb = voff, va = voff;
    TST(4096 + (s * 36 + ct0) * 4 + w, 0);
    const int f0 = (int)__hip_atomic_load((const gu32*)(WC(p) + G_WS_CTL + (lane < 8 ? G_C_UM + (w * 8 + lane) * 128 : lane < 16 ? G_C_GV + ((s < (int)G_S1 ? s & 1 : 0) * 32 + w * 8 + lane - 8) * 128
                                                                                                                                : attn_flag(s, w * 2 + (lane & 1)) - G_WS_CTL)), RLX_AGENT);
    const bool early = !TAIL && !__any(f0 < s);
    if (!early) wait_word(p, WC(p) + G_WS_CTL + (lane < 8 ? G_C_GV + ((s < (int)G_S1 ? s & 1 : 0) * 32 + w * 8 + lane) * 128 : G_C_UM + w * 8 * 128), lane < 8 ? s : s - 3, 21u);
#define ALOAD(q) do { if ((q) < 8) { _Pragma("unroll") for (int t = 0; t < NT; ++t) RLDA(a[(q) % NR][t], va, ub[t], 0);  \
                                     asm volatile("v_add_u32 %0, 0x400, %0" : "+v"(va)); }                                            \
                      else { _Pragma("unroll") for (int t = 0; t < NT; ++t) RLDA(a[(q) % NR][t], vu[t], um, ((q) & 1) * 1024); } } while (0)
    constexpr int D0 = DEPTH < 8 ? DEPTH : 8;
    asm volatile("s_nop 4");
#pragma unroll
    for (int q = 0; q < D0; ++q) ALOAD(q);
    asm volatile("s_cmp_lg_u32 %0, 0\n\ts_cbranch_scc1 1f\n\ts_waitcnt vmcnt(0)\n1:\n\ts_waitcnt vmcnt(30)" :: "s"(__builtin_amdgcn_readfirstlane((int)early)) : "scc", "memory");
    unsigned tries = 0;
    if (TAIL) {
        const char* sp = pm + (lane & 7) * 256 + ((lane >> 3) & 1) * 8192;
        const int dl = (FLG(p.prm[5]) >> 26) & 31;
        if (dl) {
            wait_word<true>(p, WC(p) + G_WS_CTL + G_C_KM + ((ct0 >> 1) + w) * 128, s - 1, 25u);
            for (int i = 0; i < dl + (s < 63 ? 5 : 0); ++i) __builtin_amdgcn_s_sleep(2);
        } else
        while (__any(__hip_atomic_load((const gu32*)sp, RLX_AGENT) == 0xffffffffu) && !give_up(p, tries, 23u)) __builtin_amdgcn_s_sleep(1);
    } else if (!early) wait_word(p, WC(p) + G_WS_CTL + (lane < 8 ? G_C_UM + (w * 8 + lane) * 128 : attn_flag(s, w * 2 + (lane & 1)) - G_WS_CTL), s, 22u);
    if (!early) TST(4096 + (s * 36 + ct0) * 4 + w, 1);
    for (;;) {
    asm volatile("s_nop 4");
#pragma unroll
    for (int q = 0; q < 8; ++q) { RLD(b[q][0], vb, pb, 0); if (NM == 2) RLD(b[q][1], vb, pb, 1024); asm volatile("v_add_u32 %0, 0x800, %0" : "+v"(vb)); }
#pragma unroll
    for (int q = 8; q < 10; ++q) { RLD(b[q][0], voff, pm, (q & 1) * 1024); if (NM == 2) RLD(b[q][1], voff, pm + 8192, (q & 1) * 1024); }
#pragma unroll
    for (int q = D0; q < NR; ++q) ALOAD(q);
#pragma unroll
    for (int q = 0; q < 10; ++q) {
        const int issuedA = (NR + q < 10 ? NR + q : 10) - D0;
        if (q < D0) VMW(NM * (9 - q) + issuedA * NT); else VMW((issuedA - (q - D0 + 1)) * NT);
#pragma unroll
        for (int t = 0; t < NT; ++t) { MFMA_ACC_A(acc[t][0], a[q % NR][t], b[q][0]); if (NM == 2) MFMA_ACC_A(acc[t][1], a[q % NR][t], b[q][1]); }
        if (NR + q < 10) ALOAD(NR + q);
    }
    asm volatile("s_nop 7" ::: "memory");
#pragma unroll
    for (int t = 0; t < NT; ++t) asm volatile("" : "+v"(acc[t][0]), "+v"(acc[t][1]));
    if (!TAIL) break;
    f32x4 chk = acc[0][0];
#pragma unroll
    for (int t = 0; t < NT; ++t) chk += acc[t][1] + acc[t][0];
    const float ck = (chk[0] + chk[1]) + (chk[2] + chk[3]);
    if (!__any(ck != ck)) break;
    unsigned mx = 0;
#pragma unroll
    for (int q = 0; q < 10; ++q) { asm volatile("" : "+v"(b[q][0])); mx = max(mx, umax4(b[q][0])); if (NM == 2) { asm volatile("" : "+v"(b[q][1])); mx = max(mx, umax4(b[q][1])); } }
#pragma unroll
    for (int t = 0; t < NT; ++t) { asm volatile("" : "+a"(a[8 % NR][t]), "+a"(a[9 % NR][t])); mx = max(mx, max(umax4(a[8 % NR][t]), umax4(a[9 % NR][t]))); }
    if (!__any(mx == 0xffffffffu) || give_up(p, tries, 24u)) break;
    vb = voff;
#pragma unroll
    for (int t = 0; t < NT; ++t) { acc[t][0] = f32x4{0.f, 0.f, 0.f, 0.f}; acc[t][1] = acc[t][0]; asm volatile("" : "+v"(acc[t][0]), "+v"(acc[t][1])); }
    }
    TST(4096 + (s * 36 + ct0) * 4 + w, 2);
    f32x4* red = (f32x4*)smem;
#pragma unroll
    for (int t = 0; t < NT; ++t) { red[((w * NT + t) * 2 + 0) * 64 + lane] = acc[t][0]; if (NM == 2) red[((w * NT + t) * 2 + 1) * 64 + lane] = acc[t][1]; }
    __syncthreads();
    const int row = lane >> 2, pc = lane & 3;
#pragma unroll
    for (int it = 0; it < NT / 2 * NM; ++it)
        if ((it & 3) == w) {
            const int m = it % NM, t = 2 * (it / NM) + (pc >> 1);
            const f32x4* r0 = red + (t * 2 + m) * 64 + (pc & 1) * 32 + row;
            const f32x4 v0 = (r0[0] + r0[NT * 128]) + (r0[NT * 256] + r0[NT * 384]), v1 = (r0[16] + r0[NT * 128 + 16]) + (r0[NT * 256 + 16] + r0[NT * 384 + 16]);
            u16* o = p.out + (size_t)(32 * s + 16 * (m + m0) + row) * OUTW + 16 * ct0 + 32 * (it / NM) + 8 * pc;
            if (TAIL || s >= OWT) OST16W(o, pack8(v0, v1)); else OST16(o, pack8(v0, v1));
        }
}

__device__ __forceinline__ int index_of_pos(int pv) { return min(min(pv, 128 + (pv >> 1)), min(256 + (pv >> 2), 384 + (pv >> 3))); }
static constexpr int KROW = G_KROW;
static constexpr int KARR = 80 * KROW;
static constexpr int UCS = 2208;
struct BCh {
    u16 uc[4][UCS];
    f32x4 cst[NST];
    unsigned Kd[648];
    u16 Kh[KARR];
    u16 Kl[KARR];
};
static_assert(4 * sizeof(BCh) <= LDS_TOTAL - 32, "LDS");
__device__ __forceinline__ unsigned prm(unsigned hi, unsigned lo, unsigned sel) { return __builtin_amdgcn_perm(hi, lo, sel); }
#define BST(i) asm volatile("" ::: "memory")
#define UT_LOAD(p_) __builtin_nontemporal_load(p_)
template <class F>
__device__ __forceinline__ void task_ut(const MainP& p, char* smem, int rb, int w, int lane, F more) {
    const int tid = w * 64 + lane, g = lane >> 4, c = lane & 15;
    { char* ws = p.ws; if (tid == 0) IST(4, rb); }
    const u32x4* src = (const u32x4*)(p.U + (size_t)rb * 64 * DM);
    u32x4 v[18];
#pragma unroll
    for (int i = 0; i < 18; ++i) v[i] = UT_LOAD(src + tid + 256 * i);
#pragma unroll
    for (int i = 0; i < 18; ++i) { const int ch = tid + 256 * i, row = ch / 72; *(u32x4*)(smem + row * 1184 + (ch - 72 * row) * 16) = v[i]; }
    __syncthreads();
    { char* ws = p.ws; if (tid == 0) IST(3, rb); }
    const unsigned ad = (unsigned)(uintptr_t)smem + (16 * g + (c >> 2)) * 1184 + (w * 144 + 4 * (c & 3)) * 2;
    char* tw = smem + 77824 + w * 20736;
#pragma unroll
    for (int i = 0; i < 9; ++i) {
        u32x2 x0, x1, x2, x3;
        asm volatile("ds_read_b64_tr_b16 %0, %4 offset:%5\n\tds_read_b64_tr_b16 %1, %4 offset:%6\n\tds_read_b64_tr_b16 %2, %4 offset:%7\n\tds_read_b64_tr_b16 %3, %4 offset:%8\n\ts_waitcnt lgkmcnt(0)"
                     : "=&v"(x0), "=&v"(x1), "=&v"(x2), "=&v"(x3) : "v"(ad), "i"(i * 32), "i"(4 * 1184 + i * 32), "i"(8 * 1184 + i * 32), "i"(12 * 1184 + i * 32) : "memory");
        u32x4* o = (u32x4*)(tw + (16 * i + c) * 144 + g * 32);
        o[0] = u32x4{x0[0], x0[1], x1[0], x1[1]}; o[1] = u32x4{x2[0], x2[1], x3[0], x3[1]};
    }
    asm volatile("" ::: "memory");
    const unsigned vo = (w * 144 + (lane >> 3)) * (SL * 2) + rb * 128 + (lane & 7) * 16;
    const char* dst = p.ws + G_D_UT;
#pragma unroll
    for (int i = 0; i < 18; ++i) {
        const u32x4 a = *(const u32x4*)(tw + (8 * i + (lane >> 3)) * 144 + (lane & 7) * 16);
        const unsigned vi = vo + i * 8 * (SL * 2);
        WST16(vi, a, dst, 0);
    }
    VMC0();
    init_wait(p.ws, w, lane);
    if (tid == 0) __hip_atomic_store((gu32*)(p.ws + G_WS_CTL + G_C_UT + rb * 128), 1u, RLX_AGENT);
    { char* ws = p.ws; if (tid == 0) IST(2, rb); }
    more();
    VMC0();
    __syncthreads();
    if (tid == 0) __hip_atomic_store((gu32*)(p.ws + G_WS_CTL + G_C_UT + rb * 128), 2u, RLX_AGENT);
}
struct KPar { u16 ldt, dsk, pa[2][4]; };
__device__ __forceinline__ KPar kgen_par(const MainP& p, int d, int lane) {
    KPar k;
    k.ldt = p.logdt[d]; k.dsk = p.Dskip[d];
#pragma unroll
    for (int q = 0; q < 2; ++q) { const int i = d * NST + lane + 64 * q; k.pa[q][0] = p.logA[i]; k.pa[q][1] = p.Aim[i]; k.pa[q][2] = p.Cre[i]; k.pa[q][3] = p.Cim[i]; }
    return k;
}
__device__ __forceinline__ void task_s4d(const MainP& p, BCh& L, int d, int lane, bool first, int& nb, KPar& kp, int& pend) {
    const int g = lane >> 4, c = lane & 15;
    BST(0);
    const float dsk = bf2f(kp.dsk);
    const auto& pa = kp.pa;
    const float dt = bfr(expf(bf2f(kp.ldt)));
#pragma unroll
    for (int q = 0; q < 2; ++q) {
        const float ar = -bfr(expf(bf2f(pa[q][0])));
        const float cr = bf2f(pa[q][2]) * dt, ci = bf2f(pa[q][3]) * dt;
        L.cst[lane + 64 * q] = f32x4{ar * dt * 1.4426950408889634f, bf2f(pa[q][1]) * dt * 0.15915494309189535f,
                                     __log2f(hypotf(cr, ci)), atan2f(ci, cr) * 0.15915494309189535f};
    }
    const char* ut = uni(p.ws + G_D_UT + (size_t)d * 4096);
    const unsigned voff = lane * 16;
    u32x4 uu[4];
    unsigned tk = nb >= 0 ? 1u : 0u;
    __builtin_amdgcn_sched_barrier(0);
    asm volatile("s_mov_b64 exec, 1\n\tglobal_atomic_add %0, %1, %0, %2 sc0\n\ts_mov_b64 exec, -1" : "+v"(tk) : "v"(G_WS_CTL + G_C_TASK + 128), "s"(WC(p)) : "memory");
    const char* ue = uni(first ? (const char*)p.logA : ut);
    WLD16X4A(uu[0], uu[1], uu[2], uu[3], voff, ue);
    BST(1);
    {
        f32x4 acc[3];
#pragma unroll
        for (int v = 0; v < 3; ++v) acc[v] = f32x4{0.f, 0.f, 0.f, 0.f};
        auto base_of = [](int v, int r) { return v == 0 ? (r & 7) + 128 * (r >> 3) : v == 1 ? (r < 8 ? 256 + 2 * (r & 3) + 128 * (r >> 2) : 512 + 4 * (r & 1) + 128 * ((r - 8) >> 1))
                                                                                           : 1024 + 128 * min(r, 8); };
        const float b0 = (float)base_of(0, c), b1 = (float)base_of(1, c), b2 = (float)base_of(2, c), off = (float)(8 * c);
#define KG_POLAR(h_, l_, lg_, f_, a_, b_) do { const float m_ = __builtin_amdgcn_exp2f(fmaf(cc[0], b_, lg_)), p_ = __builtin_amdgcn_fractf(fmaf(a_, b_, f_)); \
            const float re_ = m_ * __builtin_amdgcn_cosf(p_), im_ = m_ * __builtin_amdgcn_sinf(p_);                                                            \
            h_ = pk2bf(re_, im_); l_ = pk2bf(re_ - lo2f(h_), im_ - hi2f(h_)); } while (0)
#pragma unroll 1
        for (int t = 0; t < NST / 16; ++t) {
            u32x4 fh, fl, ah[3], al[3];
#pragma unroll
            for (int j = 0; j < 4; ++j) {
                const f32x4 cc = L.cst[16 * t + 4 * g + j];
                KG_POLAR(fh[j], fl[j], 0.f, 0.f, -cc[1], off);
                KG_POLAR(ah[0][j], al[0][j], cc[2], cc[3], cc[1], b0); KG_POLAR(ah[1][j], al[1][j], cc[2], cc[3], cc[1], b1); KG_POLAR(ah[2][j], al[2][j], cc[2], cc[3], cc[1], b2);
            }
#pragma unroll
            for (int v = 0; v < 3; ++v) { acc[v] = MFMA32(al[v], fh, acc[v]); acc[v] = MFMA32(ah[v], fl, acc[v]); acc[v] = MFMA32(ah[v], fh, acc[v]); }
        }
        BST(2);
        auto split = [](float k) { const unsigned hi = pk2bf(k, 0.f) & 0xffffu; return hi | (pk2bf(0.f, k - __uint_as_float(hi << 16)) & 0xffff0000u); };
#pragma unroll
        for (int v = 0; v < 3; ++v)
#pragma unroll
            for (int i = 0; i < 4; ++i) {
                const int pos = base_of(v, 4 * g + i) + 8 * c;
                if (pos <= 2048) L.Kd[index_of_pos(pos)] = split(2.0f * acc[v][i]);
            }
    }
    __builtin_amdgcn_sched_barrier(0);
    asm volatile("s_waitcnt vmcnt(0)" : "+v"(tk), "+a"(uu[0]), "+a"(uu[1]), "+a"(uu[2]), "+a"(uu[3]) :: "memory");
    if (first) {
        wait_word(p, p.ws + G_WS_CTL + G_C_UT + (lane & 31) * 128, 1, 30u);
        WLD16X4A(uu[0], uu[1], uu[2], uu[3], voff, ut);
        asm volatile("s_waitcnt vmcnt(0)" : "+a"(uu[0]), "+a"(uu[1]), "+a"(uu[2]), "+a"(uu[3]) :: "memory");
    }
    if (pend >= 0 && lane == 0) __hip_atomic_fetch_add((gu32*)(WC(p) + G_WS_CTL + G_C_YC + pend * 128), 1u, RLX_AGENT);
    nb = __builtin_amdgcn_readfirstlane(tk) + 1;
    const u32x4 z4 = {0u, 0u, 0u, 0u};
    *(u32x4*)&L.Kh[8 * lane] = z4; *(u32x4*)&L.Kl[8 * lane] = z4;
    *(u32x4*)&L.Kh[512 + 8 * (lane & 31)] = z4; *(u32x4*)&L.Kl[512 + 8 * (lane & 31)] = z4;
    *(u32x4*)&L.uc[0][64 + SL] = z4;
    *(u32x4*)&L.uc[(lane >> 3) & 3][8 * (lane & 7)] = z4;
    asm volatile("" ::: "memory");
#pragma unroll
    for (int q = 0; q < 4; ++q) {
        const int e = lane + 64 * q, at = (16 + (e >> 2)) * KROW + 24 - 8 * (e & 3);
        unsigned k[8];
        const unsigned* kd = L.Kd + index_of_pos(8 * e);
        if (q == 0) {
            const bool two = lane >= 32;
#pragma unroll
            for (int r = 0; r < 8; ++r) k[7 - r] = kd[two ? (0x43222100 >> (4 * r)) & 7 : r];
        } else if (q == 1) {
            const unsigned k0 = kd[0], k1 = kd[1], k2 = kd[2];
            k[7] = k[6] = k[5] = k0; k[4] = k[3] = k[2] = k1; k[1] = k[0] = k2;
        } else {
            const unsigned k0 = kd[0], k1 = kd[1];
            k[7] = k[6] = k[5] = k[4] = k0; k[3] = (e & 1) ? k1 : k0; k[2] = k[1] = k[0] = k1;
        }
        u32x4 kh, kl;
#pragma unroll
        for (int j = 0; j < 4; ++j) { kh[j] = prm(k[2 * j + 1], k[2 * j], 0x05040100u); kl[j] = prm(k[2 * j + 1], k[2 * j], 0x07060302u); }
        *(u32x4*)&L.Kh[at] = kh;
        if (G_NHL == 2) *(u32x4*)&L.Kl[at] = kl;
    }
    BST(3);
#pragma unroll
    for (int q = 0; q < 4; ++q) *(u32x4*)&L.uc[0][64 + 8 * (lane + 64 * q)] = uu[q];
    asm volatile("" ::: "memory");
#pragma unroll
    for (int q = 0; q < 4; ++q) {
        const int e = lane + 64 * q;
        const int ue = 56 + 8 * e;
        const u32x4 a = *(const u32x4*)&L.uc[0][ue];
        const u32x2 b = *(const u32x2*)&L.uc[0][ue + 8];
        const unsigned s0 = __builtin_amdgcn_alignbit(a[1], a[0], 16), s1 = __builtin_amdgcn_alignbit(a[2], a[1], 16), s2 = __builtin_amdgcn_alignbit(a[3], a[2], 16),
                       s3 = __builtin_amdgcn_alignbit(b[0], a[3], 16), s4 = __builtin_amdgcn_alignbit(b[1], b[0], 16);
        *(u32x4*)&L.uc[1][ue] = u32x4{s0, s1, s2, s3};
        *(u32x4*)&L.uc[2][ue] = u32x4{a[1], a[2], a[3], b[0]};
        *(u32x4*)&L.uc[3][ue] = u32x4{s1, s2, s3, s4};
    }
    if (lane == 0) {
        const u32x4 a = *(const u32x4*)&L.uc[0][56 + SL];
        const unsigned s0 = __builtin_amdgcn_alignbit(a[1], a[0], 16), s1 = __builtin_amdgcn_alignbit(a[2], a[1], 16), s2 = __builtin_amdgcn_alignbit(a[3], a[2], 16), s3 = a[3] >> 16;
        *(u32x4*)&L.uc[1][56 + SL] = u32x4{s0, s1, s2, s3};
        *(u32x4*)&L.uc[2][56 + SL] = u32x4{a[1], a[2], a[3], 0u};
        *(u32x4*)&L.uc[3][56 + SL] = u32x4{s1, s2, s3, 0u};
    }
    kp = kgen_par(p, min(nb, DM - 1), lane);
    BST(4);
    {
        const int o = 33 + c + 8 * g;
        const unsigned ua = (unsigned)(uintptr_t)&L.uc[o & 3][o & ~3], ka = (unsigned)(uintptr_t)&L.Kh[(16 + c) * KROW + 8 * g];
        const f32x4* res = (const f32x4*)L.Kh + lane;
        asm volatile(ASM_CV :: "v"(ua), "v"(ka), "v"((unsigned)(uintptr_t)res) : CV_CLOB);
        asm volatile("" :: "v"(kp.ldt), "v"(kp.dsk), "v"(kp.pa[0][0]), "v"(kp.pa[0][1]), "v"(kp.pa[0][2]), "v"(kp.pa[0][3]), "v"(kp.pa[1][0]), "v"(kp.pa[1][1]), "v"(kp.pa[1][2]), "v"(kp.pa[1][3]));
        BST(5);
#pragma unroll
        for (int par = 0; par < 2; ++par)
#pragma unroll
            for (int t = 0; t < 4; ++t) {
                const int n = 16 * (par + 2 * (16 * t + c)) + 4 * g;
                const u32x2 uu = *(const u32x2*)&L.uc[0][64 + n];
                const f32x4 y4 = res[(par * 4 + t) * 64];
                const float r0 = bfr(y4[0]) + bfr(lo2f(uu[0]) * dsk), r1 = bfr(y4[1]) + bfr(hi2f(uu[0]) * dsk);
                const float r2 = bfr(y4[2]) + bfr(lo2f(uu[1]) * dsk), r3 = bfr(y4[3]) + bfr(hi2f(uu[1]) * dsk);
                *(u32x2*)&L.uc[1][n] = u32x2{pk2bf(r0, r1), pk2bf(r2, r3)};
            }
    }
    BST(6);
    char* yb = uni(p.ws + G_D_YB + (size_t)d * 4096);
#pragma unroll
    for (int q = 0; q < 4; ++q) { const u32x4 o = *(const u32x4*)&L.uc[1][8 * (lane + 64 * q)]; WST16(voff, o, yb, q * 1024); }
    pend = d >> 6;
    BST(7);
}
__device__ __forceinline__ void s4d_wave(const MainP& p, char* smem, int w, int lane, int quota) {
    int nb = 0, pend = -1;
    if (lane == 0) nb = (int)(__hip_atomic_fetch_add((gu32*)(WC(p) + G_WS_CTL + G_C_TASK + 128), 1u, RLX_AGENT) + 1u);
    nb = __builtin_amdgcn_readfirstlane(nb);
    KPar kp = kgen_par(p, min(nb, DM - 1), lane);
#pragma unroll 1
    for (int n = 0; n < quota; ++n) {
        const int d = nb;
        if (d >= DM) break;
        nb = n + 1 < quota ? 0 : -1;
        task_s4d(p, reinterpret_cast<BCh*>(smem)[w], d, lane, n == 0, nb, kp, pend);
    }
    VMC0();
    if (pend >= 0 && lane == 0) __hip_atomic_fetch_add((gu32*)(WC(p) + G_WS_CTL + G_C_YC + pend * 128), 1u, RLX_AGENT);
}
__device__ __forceinline__ void watch_role(const MainP& p, int lane) {
    const char* c = WC(p) + G_WS_CTL;
#pragma unroll 1
    for (int s = G_S0; s < NSEG; ++s) {
        const int n = min(s + 3, NSEG - 1);
        wait_word(p, c + G_C_GV + lane * 128, n - (n < (int)G_S1 ? (n ^ (lane >> 5)) & 1 : 0), 60u);
        wait_word(p, (lane < 32 ? c + G_C_GX : p.ws + G_WS_CTL + G_C_UT - 4096) + lane * 128, lane < 32 ? min(s + 1 + KTOUCH, NSEG - 1) : s < 40 ? -1 : 2, 61u);
        if (lane == 0) __hip_atomic_store((gu32*)(p.ws + G_WS_CTL + G_C_RDY + s * 128), 1u, RLX_AGENT);
    }
}
__device__ __forceinline__ void task_merge(const MainP& p, char* smem, int gr, int i, int w, int lane) {
    const int g = lane >> 4, c = lane & 15;
    wait_word(p, WC(p) + G_WS_CTL + G_C_YC + gr * 128, 63, 31u);
    const char* sb = uni(p.ws + G_D_YB + (size_t)gr * 262144 + i * 1024 + w * 256);
    const unsigned vt = g * 4096 + c * 16;
    u32x4 z[16];
#pragma unroll
    for (int j = 0; j < 16; ++j) { const char* sj = sb + j * 16384; WLD16(z[j], vt, sj, 0); }
    asm volatile("s_waitcnt vmcnt(0)" : "+v"(z[0]), "+v"(z[1]), "+v"(z[2]), "+v"(z[3]), "+v"(z[4]), "+v"(z[5]), "+v"(z[6]), "+v"(z[7]),
                 "+v"(z[8]), "+v"(z[9]), "+v"(z[10]), "+v"(z[11]), "+v"(z[12]), "+v"(z[13]), "+v"(z[14]), "+v"(z[15]) :: "memory");
    char* tw = smem + w * 17408;
#pragma unroll
    for (int j = 0; j < 16; ++j) *(u32x4*)(tw + (4 * j + g) * 272 + c * 16) = z[j];
    const unsigned ad = (unsigned)(uintptr_t)tw + (8 * g + (c >> 2)) * 272 + (c & 3) * 8;
    u16* orow = p.out + (size_t)(512 * i + 128 * w + c) * OUTW + DM + 64 * gr + 8 * g;
#pragma unroll
    for (int k = 0; k < 8; ++k) {
        u32x2 x0, x1, x2, x3;
        asm volatile("ds_read_b64_tr_b16 %0, %4 offset:%5\n\tds_read_b64_tr_b16 %1, %4 offset:%6\n\tds_read_b64_tr_b16 %2, %4 offset:%7\n\tds_read_b64_tr_b16 %3, %4 offset:%8\n\ts_waitcnt lgkmcnt(0)"
                     : "=&v"(x0), "=&v"(x1), "=&v"(x2), "=&v"(x3) : "v"(ad), "i"(k * 32), "i"(4 * 272 + k * 32), "i"(32 * 272 + k * 32), "i"(36 * 272 + k * 32) : "memory");
        OST16(orow + (size_t)16 * k * OUTW, (u32x4{x0[0], x0[1], x1[0], x1[1]}));
        OST16(orow + (size_t)16 * k * OUTW + 32, (u32x4{x2[0], x2[1], x3[0], x3[1]}));
    }
}

static constexpr int Q1 = NG1 * 6 + 36, Q2 = Q1 + (NFINE - 2) * 25;
static constexpr int NTASK = Q2 + 34 + 52;
__host__ __device__ __forceinline__ void task_of(int u, int m0, int& sg, int& r) {
    if (u < 6 * m0) { sg = u / 6; r = u % 6; }
    else if (u < 6 * m0 + 108) { sg = m0 + (u - 6 * m0) / 9; r = (u - 6 * m0) % 9; }
    else if (u < Q1) { sg = (u - 36) / 6; r = (u - 36) % 6; }
    else if (u < Q2) { sg = NG1 + (u - Q1) / 25; r = (u - Q1) % 25; }
    else { sg = u < Q2 + 34 ? 62 : 63; r = u < Q2 + 34 ? u - Q2 : u - Q2 - 34; }
}
__device__ __forceinline__ void worker_body(const MainP& p, char* smem, const int w, const int lane) {
    const int tid = w * 64 + lane;
    int* slot = (int*)(smem + LDS_TOTAL - 16);
    int nst = 0, nxt = -1;
    gu32* qa = (gu32*)(WC(p) + G_WS_CTL + G_C_TASK);
    const int m0 = (FLH(p.prm[7]) >> 4) & 63;
    for (;;) {
        if (tid == 0) {
            *slot = nxt >= 0 ? nxt : (int)(__hip_atomic_fetch_add(qa, 1u, RLX_AGENT) + 1u);
            nxt = -1;
        }
        __syncthreads();
        const int u = __builtin_amdgcn_readfirstlane(*slot);
        if (u >= NTASK) break;
        if (tid == 0 && u < Q1 - 140) nxt = (int)(__hip_atomic_fetch_add(qa, 1u, RLX_AGENT) + 1u);
        const bool last2 = u >= Q2;
        int sg, r;
        task_of(u, m0, sg, r);
        if (u < Q1) {
            if (r < 6) task_out<6, 6, false>(p, smem, sg, 6 * r, w, lane);
            else { const int m = 3 * (sg - m0) + r - 6; task_merge(p, smem, m >> 2, m & 3, w, lane); }
        }
        else if (r < 16) task_attn_tail(p, sg, 2 * r + (w >> 1), w, lane);
        else if (sg == 63) task_out<2, 9, true, 1>(p, smem, sg, (r - 16) & ~1, w, lane, r & 1);
        else if (last2) task_out<2, 9, true>(p, smem, sg, 2 * (r - 16), w, lane);
        else task_out<4, 9, true>(p, smem, sg, 4 * (r - 16), w, lane);
        if (last2) break;
        __syncthreads();
    }
}

__host__ __device__ constexpr unsigned phys(unsigned x) { return (x >> 13) * 32768 + (x & 8191); }
__host__ __device__ constexpr unsigned rep(int x) { return G_RSW ? (x >> 1) * 16384 + (x & 1) * 131072 : (x & 1) * 16384 + (x >> 1) * 131072; }
__host__ __device__ constexpr unsigned aMB(int f, int b) { return phys(f < 8 ? 16384 + f * 1024 + b : 28672 + b); }
__host__ __device__ constexpr unsigned aPX(int pr, int b) { return phys(pr * 1024 + b); }
__host__ __device__ constexpr unsigned aPM(int u, int b) { return phys(24576 + u * 1024 + b); }
static constexpr int L_KI = 8192, L_QI = 16384, L_VI = 147456;

__device__ __forceinline__ int lane_id() { int l; asm volatile("v_mbcnt_lo_u32_b32 %0, -1, 0\n\tv_mbcnt_hi_u32_b32 %0, -1, %0" : "=v"(l)); return l; }
typedef const __attribute__((address_space(4))) MainP* KArg;

__global__ __launch_bounds__(256, 1) void main_kernel(MainP p_) {
    __shared__ __attribute__((aligned(16))) char smem[LDS_TOTAL];
    KArg ka = (KArg)__builtin_amdgcn_kernarg_segment_ptr();
    static_assert(offsetof(MainP, W) == 0x28, "argument layout");
    asm volatile(ASM_EA :: "s"(ka), "s"(blockIdx.x), "v"(threadIdx.x) : "v1", "v2", "s40", "s41", "s42", "s43", "s44", "s45", "s46", "s47", "s48", "s49", "scc", "memory");
    asm volatile("" : "+s"(ka));
    const int b = blockIdx.x, x = b & 7, ix = b >> 3;
    const int w = __builtin_amdgcn_readfirstlane(threadIdx.x >> 6);
    const bool isr = x < 4 && ix >= 16;
    constexpr int NRF = 8 + 8 * G_GVN;
    if (__builtin_expect(ix < (x < 4 ? 16 : NRF), 1)) {
        const int h = (x & 3) * 8 + (ix & 7), id = h + (ix >> 4) * 32;
        const unsigned stb = 0;
#define UNR _Pragma("unroll")
#define CW(k, val) do { if ((k) < 16) ca[(k) & 15] = (unsigned)(val); else if ((k) < 32) cb[(k) & 15] = (unsigned)(val); else cl[((k) - 40) & 15] = (unsigned)(val); } while (0)
#define ROLEW(A_) asm volatile(A_ : "+{v[0:15]}"(ca), "+{v[16:31]}"(cb), "+{v[200:215]}"(cl) : "s"(ka), "s"(w), "s"(id), "s"(stb) : ASM_CLOBW)
#define PAD(n_) asm volatile("s_branch skip_" n_ "\npad_" n_ ":\n\ts_setpc_b64 s[46:47]\nskip_" n_ ":")
#define CW_ALL const int lane = lane_id(), g = lane >> 4, c = lane & 15, q = c >> 2; u32x16 ca = {}, cb = {}, cl = {};                                      \
        CW(0, lane * 16); CW(40, (4 * w + g) * 8192 + h * 256 + (c ^ (2 * g + 8 * (w >> 1))) * 16);                                                          \
        UNR for (int k = 0; k < 2; ++k) CW(41 + k, (8 * g + q) * 256 + ((4 * w + 2 * k + ((c >> 1) & 1)) ^ (2 * q + 8 * (g & 1))) * 16 + (c & 1) * 8)
#define CW_SRC(b_, key_) UNR for (int k = 0; k < 9; ++k) { const int pp = 64 * k + lane, r = pp / 72, pc = pp - 72 * r; CW(43 + k, (b_) + (8 * w + r) * 1152 + (pc ^ (key_)) * 16); }
#define UNIT(u_, key_) (((u_) >> 2) * 128 + (((u_) & 3) ^ (key_)) * 32 + (c & 3) * 8)
#define CW_NEAR CW(1, rep(x) + aMB(2 * w, lane * 16)); CW(2, rep(x) + aMB(8, w * 256 + lane * 4)); CW(3, 2 * w * 1024 + lane * 16);                            \
        UNR for (int i = 0; i < 6; ++i) CW(4 + i, ((2 * w + 2 + i) & 7) * 1024 + lane * 16);                                                                 \
        CW(10, (c & 2) ? 0xEEEE : 0x4444);                                                                                                                   \
        CW(20, rep(x) + 38912 + (lane & 31) * 64); CW(21, lane < 32 ? rep(lane & 3) + phys((lane >> 2) * 2048) + h * 64 : 65536 + h * 128);          \
        CW(22, lane * 4); CW(23, rep(x) + aMB(2 * w + 1, lane * 16))
#define CW_FAR CW(0, c * 1152 + (g ^ (c >> 1)) * 16); CW(1, ca[0]); CW(2, ca[0] ^ 64); CW(3, w * 9216 + lane * 16); CW_SRC(0, (r >> 1) + 4 * (w & 1))                                   \
        CW(4, L_VI + w * 2048 + lane * 16); CW(5, L_VI + lane * 16); CW(6, lane * 4)
        if (x >= 4 && ix >= 8) {
            PAD("gv"); CW_ALL; CW_FAR;
            UNR for (int t = 0; t < 9; ++t) CW(8 + t, (9 * w + t) * G_UXS + skew(0, 9 * w + t) * 32768 + h * 1024 + lane * 16);
            UNR for (int k = 0; k < 2; ++k) CW(19 + k, (9 * (w >> 1) + 4 * k + g) * 32768 + h * 256 + c * 16);
            CW(21, (9 * (w >> 1) + 8) * 32768 + h * 256 + lane * 4);
            UNR for (int k = 0; k < 4; ++k) CW(52 + k, (4 * g + q) * 1152 + ((9 * w + k) >> 2) * 128 + ((2 * ((9 * w + k) & 3) + ((c >> 1) & 1)) ^ (2 * g + (q >> 1))) * 16 + (c & 1) * 8);
            ROLEW(ASM_GV);
        } else if (x >= 4) {
            PAD("gx"); CW_ALL; CW_FAR;
            CW(8, w * 2048 + lane * 16);
            ROLEW(ASM_GX);
        } else if (ix < 8) {
            PAD("qk"); CW_ALL; CW_NEAR;
            CW(12, L_QI + (16 * w + (c & 13)) * 32 + 8 * g + 4 * ((c >> 1) & 1));
            CW(13, L_QI + (8 * g + q) * 32 + (c & 3) * 8);
            CW(14, G_WS_CTL + G_C_KM + h * 128); CW(15, lane * 16 + 4096);
            CW(16, L_KI + (32 * w + c) * 16 + 4 * g);
            CW(17, L_KI + (4 * g + q) * 16 + (c & 1) * 8);
            const int mm = 8 * (w & 1) + 4 * (g >> 1) + (g & 1);
            CW(18, rep(x ^ 3) + aPX(h >> 1, (h & 1) * 512 + (w & 1) * 256 + g * 64 + (c >> 3) * 32 + ((c >> 1) & 1) * 16 + (2 * (q & 1) + (c & 1)) * 4));
            CW(19, rep((c & 1) + 2 * (c >> 3)) + aPM(h >> 3, ((h >> 1) & 3) * 256 + (mm + 2 * ((c >> 1) & 1)) * 16 + (h & 1) * 8 + (q & 1) * 4));
            CW(24, G_WS_CTL + G_C_GX + h * 128);
            ROLEW(ASM_QK);
        } else {
            PAD("vv"); CW_ALL; CW_NEAR; CW_SRC(h * 147456, r & 4);
            const int tq = 9 * (w >> 1) + 2 * (g & 1) + (g >> 1);
            CW(14, tq * 32768 + h * 256 + c * 16);
            CW(15, (tq + 4) * 32768 + h * 256 + c * 16);
            CW(16, (9 * (w >> 1) + 8) * 32768 + h * 256 + c * 16 + (g & 1) * 8);
            CW(19, (c & 8) ? 0xEEEE : 0x4444);
            CW(12, L_QI + w * 576 + c * 16 + 4 * g);
            CW(13, L_QI + (8 * g + q) * 16 + (c & 1) * 8 + ((c >> 1) & 1) * 576);
            CW(25, L_QI + (8 * g + q) * 16 + (c & 1) * 8 + (1 - ((c >> 1) & 1)) * 576);
            UNR for (int k = 0; k < 4; ++k) CW(52 + k, ((k >> 1) ^ (q >> 1)) * 36864 + (4 * g + (q & 1)) * 1152 + UNIT(9 * w + 2 * (k & 1) + (q >> 1), 2 * (g & 1)));
            CW(24, (q >> 1) * 36864 + (4 * g + (q & 1)) * 1152 + UNIT(9 * w + 8, 2 * (g & 1)));
            ROLEW(ASM_V);
        }
    } else {
    if (isr && (ix < 25 || ix >= 29)) {
        const int id = x * 12 + (ix < 25 ? ix - 13 : ix - 29);
        init_cal(ka->ws, id, (x ^ (FLG(ka->prm[5]) >> 3)) & 1, threadIdx.x, (FLG(ka->prm[5]) >> 16) & 15);
        if (FLG(ka->prm[5]) & 4) {
            const unsigned o = (id * 384 + threadIdx.x) * 128, o2 = o + (threadIdx.x & 128 ? 16384 : 32768);
            const char* a = (const char*)ka->X + o; const char* b2 = (const char*)ka->U + o;
            const char* c2 = (const char*)(w < 2 ? ka->X : ka->U) + o2;
            const char* ps = (const char*)(w == 0 ? ka->logA : w == 1 ? ka->Aim : w == 2 ? ka->Cre : ka->Cim) + id * 3072 + min((int)(threadIdx.x & 63), 23) * 128;
            asm volatile("s_mov_b32 m0, %4\n\ts_nop 0\n\tglobal_load_lds_dword %0, off\n\tglobal_load_lds_dword %1, off\n\tglobal_load_lds_dword %2, off\n\tglobal_load_lds_dword %3, off"
                         :: "v"(a), "v"(c2), "v"(b2), "v"(ps), "s"(163072) : "memory");
        }
    }
    if (x >= 4 ? ix >= 24 : (ix >= 25 && ix < 29)) init_fill(ka->ws, ka->mem0, x >= 4 ? (ix - 24) * 4 + (x & 3) : 32 + (ix - 25) * 4 + x, threadIdx.x);
    if (x >= 4 && !(FLG(ka->prm[5]) & 1)) {
        const char* src = (const char*)ka->W[3] + (size_t)((x & 3) * 8 + (ix & 7)) * 147456 + threadIdx.x * 128;
#pragma unroll
        for (int i = 0; i < 5; ++i)
            if (i < 4 || w < 2) asm volatile("s_mov_b32 m0, %1\n\ts_nop 0\n\tglobal_load_lds_dword %0, off" :: "v"(src + i * 32768), "s"(155648) : "memory");
    }
    if (isr && ix >= 25) {
        for (int i = 0; i < (ix < 29 ? 3 : 0) + HSL; ++i) __builtin_amdgcn_s_sleep(40);
        init_wait(ka->ws, w, threadIdx.x & 63);
    }
    asm volatile("" : "+s"(ka));
    if (x < 4 && (ix < 25 || w == 0)) {
        const int lane = threadIdx.x & 63, g = lane >> 4, c = lane & 15;
        const unsigned lb = w * G_STG;
        unsigned* ct = (unsigned*)(smem + lb) + lane;
        asm volatile("" :: "v"(ct) : "memory");
#define CTW(k, val) ct[(k) * 64] = (unsigned)(val)
        const u64 ws = (u64)ka->ws, prm = (u64)ka;
        const u64 stb = 0;
        CTW(0, lane * 16);
        const int j = x * 9 + ix - 16;
        if (ix >= 25) {
            CTW(1, rep(x) + 38912 + (lane & 31) * 64);
            CTW(2, G_WS_CTL + (lane < 32 || !KTOUCH ? G_C_EF + 3 * 2048 + (lane & 31) * 64 : G_C_GX + (8 * x + (lane & 7)) * 128)); CTW(3, lane < 32 || !KTOUCH ? 2048 : 0);
            for (int k = 0; k < 7; ++k) {
                const int q = min(ix - 25 + 7 * k, KTOUCH ? 43 : 35), id = q < 36 ? 9 * x + (q >> 2) : 8 * x + q - 36;
                CTW(4 + k, (q < 36 ? G_D_UX + id * G_UXS + skew(0, id) * 32768 + (q & 3) * 8192 : G_D_KX + id * G_HS8 + skew(0, id) * 8192) + lane * 128);
                CTW(11 + k, q < 36 ? 32768 : 8192);
            }
        } else {
            const int rot = (FLG(ka->prm[5]) & 16) ? 0 : (x ^ 3) * 4;
            if (w >= 2) {
                return;
            }
            const int us = (FLH(ka->prm[7]) & 1) ? (ix + (x >> 1)) & 1 : 0;
            CTW(1, lane * 16 + 4096 * (1 - us)); CTW(3, lane * 16 + 4096 * us);
            CTW(2, rep(x) + aMB(j >> 2, (j & 3) * 256 + (4 * g + 2 * ((c >> 1) & 1) + (c & 1)) * 16 + (c >> 2) * 4));
            for (int k = 0; k < 6; ++k) { const int q = 9 * k + ix - 16; CTW(46 + k, rep(x) + lane * 16 + (q < 29 ? phys(q * 1024) : 0x200000 + phys((q < 45 ? q - 29 : q < 53 ? q - 21 : 20) * 1024))); }
            CTW(4, (c & 2) ? 0xEEEE : 0x4444); CTW(5, (c & 1) ? 0x03020706u : 0x05040100u);
            CTW(6, G_WS_CTL + G_C_GV + ((w & (G_GVN - 1)) * 32 + ((lane & 31) ^ (2 * rot))) * 128); CTW(7, rep(x) + (lane & 31) * 64); CTW(8, lane * 4); CTW(9, 131072);
            for (int k = 0; k < 16; ++k) { const int i = k ^ rot; CTW(10 + k, rep(x) + aPX(i, (g >> 1) * 512 + (2 * (c >> 2) + (c & 1)) * 64 + (g & 1) * 32 + ((c >> 1) & 1) * 16)); CTW(26 + k, i * 2048 + lane * 16); }
            for (int k = 0; k < 4; ++k) CTW(42 + k, rep(x) + aPM(k ^ (2 * us), lane * 16));
        }
        const int id = ix >= 25 ? ix - 25 : j;
#define ROLE(A_) asm volatile(A_ :: "s"(ws), "s"(ws), "s"(ws), "s"(w), "s"(id), "s"(ws), "s"(prm), "s"(lb), "s"(stb) : ASM_CLOB)
        if (ix >= 25) ROLE(ASM_H);
        else ROLE(ASM_R);
    }
    else if (x >= 4 && ix >= 24) {
        if (ix >= 25) for (int i = 0; i < ((FLG(ka->prm[5]) >> 5) & 7); ++i) __builtin_amdgcn_s_sleep(36);
        const char* xs = (const char*)ka->X + (size_t)((ix - 24) * 4 + (x & 3)) * 73728 + threadIdx.x * 128;
        const int f = (ix - 24) * 4 + (x & 3);
        const u16* ps = (w == 0 ? ka->logA : w == 1 ? ka->Aim : w == 2 ? ka->Cre : ka->Cim) + f * 2304 + min((int)(threadIdx.x & 63), 35) * 64;
        if (ix < 25 || !(FLG(ka->prm[5]) & 4))
        asm volatile("s_mov_b32 m0, %4\n\ts_nop 0\n\tglobal_load_lds_dword %0, off\n\tglobal_load_lds_dword %1, off\n\tglobal_load_lds_dword %2, off\n\tglobal_load_lds_dword %3, off"
                     :: "v"(xs), "v"(xs + 32768), "v"(xs + 65536 - w * 8192), "v"(ps), "s"(163072) : "memory");
        if (ix < 25) for (int i = 0; i < ((FLG(ka->prm[5]) >> 5) & 7); ++i) __builtin_amdgcn_s_sleep(36);
    }
    }
    asm volatile("" : "+s"(ka));
    MainP p;
    p.prm[5] = ka->prm[5]; p.prm[7] = ka->prm[7];
    p.U = ka->U; p.ws = ka->ws; p.logA = ka->logA; p.Aim = ka->Aim; p.Cre = ka->Cre; p.Cim = ka->Cim; p.logdt = ka->logdt; p.Dskip = ka->Dskip; p.out = ka->out;
    if (x >= 4 && ix >= 24) {
        const u32x4 sent = {0xffffffffu, 0xffffffffu, 0xffffffffu, 0xffffffffu};
        const int tid = w * 64 + lane_id(), h = (ix - 24) * 4 + (x & 3);
        task_ut(p, smem, h, w, lane_id(), [&] {
#pragma unroll 1
            for (int sg = NG1; sg < 64; ++sg) {
                const unsigned ok = G_D_KM + h * G_HS4 + skew(sg, h) * 4096 + tid * 16;
                const unsigned op = tid < 128 ? G_D_PX + (sg * 32 + h) * 2048 + tid * 16 : G_D_PM + sg * 16384 + ((tid >> 4) & 1) * 8192 + h * 256 + (tid & 15) * 16;
                asm volatile("global_store_dwordx4 %0, %2, %3 sc1\n\tglobal_store_dwordx4 %1, %2, %3 sc1\n\ts_nop 1" :: "v"(ok), "v"(op), "v"(sent), "s"(p.ws) : "memory");
            }
        });
    }
    near_mask(p.ws, lane_id(), p.prm[0], p.prm[1]);
    p.X = (const u16*)(p.ws + ((p.prm[0] & 2) ? 0 : 8192));
    {
        const int nq = x < 4 ? (w + ((FLH(p.prm[7]) >> 1) & 3) > 3 ? 0 : 1000) : ix < 8 ? (FLG(p.prm[5]) >> 8) & 3 : ix < 16 ? (FLG(p.prm[5]) >> 10) & 3 : (ix & 2) && ((31 - ix) >> 2) < NAG ? 0 : ix < 24 ? (ix < 16 + ((FLH(p.prm[7]) >> 10) & 15) ? (FLG(p.prm[5]) >> 12) & 3 : 0) : (FLG(p.prm[5]) >> 14) & 3;
        if (b == 239 && w == 0) watch_role(p, lane_id());
        else if (nq || b == 239) {
            for (int i = 0; i < (int)(((ix & 7) * 4 + w) * ((FLG(p.prm[5]) >> 23) & 7)); ++i) __builtin_amdgcn_s_sleep(3);
            s4d_wave(p, smem, w, lane_id(), nq == 3 || b == 239 ? 1000 : nq);
        }
        if (x < 4) return;
        __syncthreads();
    }
    if ((ix & 18) == 18 && ((31 - ix) >> 2) < NAG) { attn_role(p, ((31 - ix) >> 2) + (ix < 24 ? NA : 0), (x & 3) * 8 + (ix & 1) * 4 + w, w, lane_id());
        if (b == 255) {
            wait_word(p, p.ws + G_WS_CTL + G_C_ARR, 255, 50u);
            if (w == 0 && lane_id() == 0) __hip_atomic_fetch_add((gu32*)(p.ws + G_WS_EP), 1u, RLX_AGENT);
        }
        __syncthreads();
    }
    worker_body(p, smem, w, lane_id());
}

extern "C" __global__ void code_end_marker() {}

static constexpr int g_prm[8] = {PRM_DEFAULT};

extern "C" void kernel_launch(void* const* d_in, const int* in_sizes, int n_in, void* d_out, int out_size,
                              void* d_ws, size_t ws_size, hipStream_t stream) {
    char* ws = (char*)(((uintptr_t)d_ws + 0x1FFFFF) & ~(uintptr_t)0x1FFFFF);
    MainP p;
    for (int i = 0; i < 8; ++i) p.prm[i] = g_prm[i];
    p.mem0 = (const u16*)d_in[1];
    for (int i = 0; i < 4; ++i) p.W[i] = (const u16*)d_in[2 + i];
    p.X = (const u16*)d_in[0]; p.U = (const u16*)d_in[7]; p.ws = ws;
    p.logA = (const u16*)d_in[8]; p.Aim = (const u16*)d_in[9]; p.Cre = (const u16*)d_in[10]; p.Cim = (const u16*)d_in[11];
    p.logdt = (const u16*)d_in[12]; p.Dskip = (const u16*)d_in[13]; p.out = (u16*)d_out;
    main_kernel<<<256, 256, 0, stream>>>(p);
}
```
